```python
import torch

def _softmax(x, axis=-1):
    return torch.softmax(x, dim=axis)
def _gelu(x):
    return torch.nn.functional.gelu(x, approximate="tanh")
def _silu(x):
    return torch.nn.functional.silu(x)
def _sigmoid(x):
    return torch.sigmoid(x)
def _layernorm(x, axis=-1, eps=1e-5):
    m = x.mean(dim=axis, keepdim=True)
    v = x.var(dim=axis, keepdim=True, unbiased=False)
    return (x - m) / torch.sqrt(v + eps)
def _scatter_add(tgt, idx, src, axis=0):
    return tgt.clone().index_add_(axis, idx, src)
def _segment_sum(src, seg, n):
    out = torch.zeros((n,) + src.shape[1:], dtype=src.dtype)
    return out.index_add_(0, seg, src)
def _pad(x, pw):
    flat = tuple(v for pair in reversed(pw) for v in pair)
    return torch.nn.functional.pad(x, flat)

kernel_name = 'interleave_layers__sliding_local_dilated__ssm_conv_s4d__prompt'

def setup_inputs():

    _w = (lambda a: (lambda t: t.to(torch.bfloat16) if t.is_floating_point() else t)(torch.as_tensor(a)))
    def _A_setup():
        B, S, D = 1, 2048, 640
        return {
            "x": _w((lambda *s: torch.randn(*s, dtype=torch.bfloat16) * 0.05)(B, S, D)),
            "wqkv": _w((lambda *s: torch.randn(*s, dtype=torch.bfloat16) * 0.05)(D, 3 * D)),
            "wo": _w((lambda *s: torch.randn(*s, dtype=torch.bfloat16) * 0.05)(D, D)),
        }
    _A_in = _A_setup()

    def _B_setup():
        Bsz, L, D, N = 1, 2048, 640, 192
        return {
            "u": _w((lambda *s: torch.randn(*s, dtype=torch.bfloat16) * 0.05)(Bsz, L, D)),
            "log_A_real": _w((lambda *s: torch.randn(*s, dtype=torch.bfloat16) * 3.2)(D, N) * 0.5),
            "A_imag": _w((lambda *s: torch.randn(*s, dtype=torch.bfloat16) * 0.1)(D, N) * 3.14),
            "C_real": _w((lambda *s: torch.randn(*s, dtype=torch.bfloat16) * 0.4)(D, N) / N ** 0.5),
            "C_imag": _w((lambda *s: torch.randn(*s, dtype=torch.bfloat16) * 0.4)(D, N) / N ** 0.5),
            "log_dt": _w((lambda *s: torch.randn(*s, dtype=torch.bfloat16) * 0.1)(D) * 0.5 - 4.0),
            "D_skip": _w((lambda *s: torch.randn(*s, dtype=torch.bfloat16) * 0.1)(D)),
        }
    _B_in = _B_setup()

    return {
        "A_x": _A_in["x"],
        "A_wqkv": _A_in["wqkv"],
        "A_wo": _A_in["wo"],
        "B_u": _B_in["u"],
        "B_log_A_real": _B_in["log_A_real"],
        "B_A_imag": _B_in["A_imag"],
        "B_C_real": _B_in["C_real"],
        "B_C_imag": _B_in["C_imag"],
        "B_log_dt": _B_in["log_dt"],
        "B_D_skip": _B_in["D_skip"],
    }

def reference(A_x, A_wqkv, A_wo, B_u, B_log_A_real, B_A_imag, B_C_real, B_C_imag, B_log_dt, B_D_skip):
    def _A_ref(x, wqkv, wo):
        B, S, D = x.shape
        H = 32; Dh = D // H
        W, dil = 8, 2
        qkv = (lambda x, *d: x.permute(*d))((x @ wqkv).reshape(B, S, 3, H, Dh), 2, 0, 3, 1, 4)
        q, k, v = qkv[0], qkv[1], qkv[2]
        scale = Dh ** -0.5
        pos = torch.arange(S)
        off = torch.arange(W) * dil
        idx = pos[:, None] - off[None, :]
        mask = idx >= 0
        idx_c = torch.where(mask, idx, 0).reshape(-1)
        kg = k[:, :, idx_c, :].reshape(B, H, S, W, Dh)
        vg = v[:, :, idx_c, :].reshape(B, H, S, W, Dh)
        logits = torch.einsum("bhsd,bhswd->bhsw", q, kg) * scale
        logits = torch.where(mask[None, None, :, :], logits, -1e9)
        attn = _softmax(logits, axis=-1)
        o = torch.einsum("bhsw,bhswd->bhsd", attn, vg)
        return (lambda x, *d: x.permute(*d))(o, 0, 2, 1, 3).reshape(B, S, D) @ wo

    def _B_ref(u, log_A_real, A_imag, C_real, C_imag, log_dt, D_skip):
        L = u.shape[1]
        dt = torch.exp(log_dt)
        A = -torch.exp(log_A_real) + 1j * A_imag
        C = (C_real + 1j * C_imag) * dt[:, None]
        pos = (lambda x, dt: x.to(dt))(torch.arange(L), log_dt.dtype)
        P = torch.exp((A * dt[:, None])[:, :, None] * pos[None, None, :])
        K = 2.0 * torch.einsum("dn,dnl->dl", C, P).real
        L2 = 2 * L
        u_t = (lambda x, *d: x.permute(*d))(u, 0, 2, 1)
        Kf = (lambda x, *a, **k: torch.fft.rfft(x.float(), *a, **k))(K, L2)
        Uf = (lambda x, *a, **k: torch.fft.rfft(x.float(), *a, **k))(u_t, L2)
        y = (lambda x, *a, **k: torch.fft.irfft(x, *a, **k).to(torch.bfloat16))(Uf * Kf[None, :, :], L2)[:, :, :L]
        y = (lambda x, *d: x.permute(*d))(y, 0, 2, 1)
        return y + u * D_skip[None, None, :]

    x = A_x
    x = x + _A_ref(_layernorm(x, axis=-1), A_wqkv, A_wo)
    x = x + _B_ref(_layernorm(x, axis=-1), B_log_A_real, B_A_imag, B_C_real, B_C_imag, B_log_dt, B_D_skip)
    x = x + _A_ref(_layernorm(x, axis=-1), A_wqkv, A_wo)
    x = x + _B_ref(_layernorm(x, axis=-1), B_log_A_real, B_A_imag, B_C_real, B_C_imag, B_log_dt, B_D_skip)
    return x
```

```cpp
#define OSLEEP 6
#define OSLEEP1 0
#define XSLEEP 8
#if defined(__HIP_DEVICE_COMPILE__) && !defined(ALLOW_PK)
#pragma clang attribute push(__attribute__((target("no-packed-fp32-ops"))), apply_to = function)
#endif
#include <hip/hip_runtime.h>
#include <hip/hip_bf16.h>
#include <stdint.h>

#define BIDX ((int)__builtin_amdgcn_workgroup_id_x())
#define TIDX ((int)__builtin_amdgcn_workitem_id_x())
typedef unsigned short u16;
typedef unsigned int u32;
typedef unsigned long long u64;
typedef __attribute__((ext_vector_type(8))) short bf16x8;
typedef __attribute__((ext_vector_type(4))) short s16x4;
typedef __attribute__((ext_vector_type(4))) float f32x4;
typedef __attribute__((ext_vector_type(2))) float f32x2;
typedef __attribute__((ext_vector_type(2))) __bf16 bf2v;
typedef __attribute__((address_space(3))) s16x4* lds_s16x4_p;
typedef __attribute__((ext_vector_type(8))) u32 u32x8;
typedef __attribute__((ext_vector_type(4))) u32 u32x4;
typedef __attribute__((ext_vector_type(2))) u32 u32x2;
typedef __attribute__((address_space(1))) u32 gu32;
typedef __attribute__((address_space(1))) u64 gu64;

#define S_ 2048
#define XP 2112
#define D_ 640
#define NST 192

#define WS_WQ  0
#define WS_WOT (WS_WQ + 1920 * 640 * 2)
#define WS_CW  (WS_WOT + 640 * 640 * 2)
#define WS_KP  (WS_CW + 4 * 2048 * 4)
#define WS_IM0 (WS_KP + 640 * 768 * 4)
#define WS_IM1 (WS_IM0 + 32 * 102400)
#define HO_ST (2048 * 640 * 2)
#define HO_X1 (HO_ST + 2048 * 128)
#define HO_FS (HO_X1 + 640 * XP * 2)
#define HO_BYTES (256 * 1392 * 16)
#define WS_O   (WS_IM1 + 32 * 102400)
#define WS_FIN (WS_O + 2 * HO_BYTES)
#define WS_FLG (WS_FIN + 2048 * 640 * 2)
#define FL_W 64
#define FL_M (FL_W + 480 * 64)
#define FL_M2 (FL_M + 384 * 64)
#define FL_END (FL_M2 + 256 * 8)
#define WS_END (WS_FLG + FL_END)

#define RETRY(i, n)
#define ST_DECL
#define ST(i)
#define ST_FLUSH(kid)

__device__ __forceinline__ float bf2f(u16 x) { return __uint_as_float(((u32)x) << 16); }
__device__ __forceinline__ u32 pack2(float a, float b) {
  f32x2 v = {a, b};
  bf2v r = __builtin_convertvector(v, bf2v);
  return __builtin_bit_cast(u32, r);
}
__device__ __forceinline__ float lo_f(u32 w) { return __uint_as_float(w << 16); }
__device__ __forceinline__ float hi_f(u32 w) { return __uint_as_float(w & 0xffff0000u); }
__device__ __forceinline__ float rbf(float f) { return lo_f(pack2(f, f)); }
__device__ __forceinline__ bf16x8 as_frag(uint4 v) {
  union { uint4 u; bf16x8 f; } c; c.u = v; return c.f;
}
__device__ __forceinline__ float2 cmul(float2 a, float2 b) {
  return make_float2(a.x * b.x - a.y * b.y, a.x * b.y + a.y * b.x);
}
__device__ __forceinline__ float2 cexp_scaled(float zr, float zi, float e) {
  float ar = zr * e, ai = zi * e;
  float m = __expf(ar);
  const float INV2PI_HI = 0.15915493667125702f;
  const float INV2PI_LO = 6.4206382432985265e-09f;
  float t = ai * INV2PI_HI;
  float err = fmaf(ai, INV2PI_HI, -t);
  float r = t - rintf(t);
  r = r + (err + ai * INV2PI_LO);
  float s = __builtin_amdgcn_sinf(r), c = __builtin_amdgcn_cosf(r);
  return make_float2(m * c, m * s);
}
__device__ __forceinline__ void cexp2(f32x2 zr, f32x2 zi, float e, f32x2& re, f32x2& im) {
  f32x2 ar = zr * e, ai = zi * e;
  f32x2 m = {__expf(ar.x), __expf(ar.y)};
  const float INV2PI_HI = 0.15915493667125702f;
  const float INV2PI_LO = 6.4206382432985265e-09f;
  f32x2 t = ai * INV2PI_HI;
  f32x2 err = {fmaf(ai.x, INV2PI_HI, -t.x), fmaf(ai.y, INV2PI_HI, -t.y)};
  f32x2 r = t - (f32x2){rintf(t.x), rintf(t.y)};
  r = r + (err + ai * INV2PI_LO);
  re = m * (f32x2){__builtin_amdgcn_cosf(r.x), __builtin_amdgcn_cosf(r.y)};
  im = m * (f32x2){__builtin_amdgcn_sinf(r.x), __builtin_amdgcn_sinf(r.y)};
}
__device__ __forceinline__ float dpp_ror8(float x) {
  return __builtin_bit_cast(float, __builtin_amdgcn_update_dpp(0, __builtin_bit_cast(int, x), 0x128, 0xf, 0xf, false));
}
__device__ __forceinline__ float BCAST0(float x) {
  return __builtin_bit_cast(float, __builtin_amdgcn_update_dpp(0, __builtin_bit_cast(int, x), 0x150, 0xf, 0xf, false));
}
__device__ __forceinline__ float rowsum16(float x) {
  x += __builtin_bit_cast(float, __builtin_amdgcn_update_dpp(0, __builtin_bit_cast(int, x), 0xB1, 0xf, 0xf, false));
  x += __builtin_bit_cast(float, __builtin_amdgcn_update_dpp(0, __builtin_bit_cast(int, x), 0x4E, 0xf, 0xf, false));
  x += __builtin_bit_cast(float, __builtin_amdgcn_update_dpp(0, __builtin_bit_cast(int, x), 0x141, 0xf, 0xf, false));
  x += __builtin_bit_cast(float, __builtin_amdgcn_update_dpp(0, __builtin_bit_cast(int, x), 0x140, 0xf, 0xf, false));
  return x;
}
__device__ __forceinline__ float dpp_xor1(float x) {
  return __builtin_bit_cast(float, __builtin_amdgcn_update_dpp(0, __builtin_bit_cast(int, x), 0xB1, 0xf, 0xf, false));
}
__device__ __forceinline__ bf16x8 tr_frag(const u16* p0, const u16* p1) {
  s16x4 a = __builtin_amdgcn_ds_read_tr16_b64_v4i16((lds_s16x4_p)p0);
  s16x4 b = __builtin_amdgcn_ds_read_tr16_b64_v4i16((lds_s16x4_p)p1);
  return __builtin_shufflevector(a, b, 0, 1, 2, 3, 4, 5, 6, 7);
}

#define X1_P 248
__device__ __forceinline__ float dot2bf(u32 a, u32 b, float c) {
  return __builtin_amdgcn_fdot2_f32_bf16(__builtin_bit_cast(bf2v, a), __builtin_bit_cast(bf2v, b), c, false);
}
__device__ __forceinline__ float xsum16_32(float x) {
  auto s = __builtin_amdgcn_permlane16_swap(__float_as_uint(x), __float_as_uint(x), false, false);
  x = __uint_as_float(s[0]) + __uint_as_float(s[1]);
  auto t = __builtin_amdgcn_permlane32_swap(__float_as_uint(x), __float_as_uint(x), false, false);
  return __uint_as_float(t[0]) + __uint_as_float(t[1]);
}
__device__ __forceinline__ float xmax16_32(float x) {
  auto s = __builtin_amdgcn_permlane16_swap(__float_as_uint(x), __float_as_uint(x), false, false);
  x = fmaxf(__uint_as_float(s[0]), __uint_as_float(s[1]));
  auto t = __builtin_amdgcn_permlane32_swap(__float_as_uint(x), __float_as_uint(x), false, false);
  return fmaxf(__uint_as_float(t[0]), __uint_as_float(t[1]));
}

#define WT " sc0 sc1"
template <class T> __device__ __forceinline__ void st16(void* p, T v) {
  asm volatile("global_store_dwordx4 %0, %1, off" WT "\n\ts_nop 1" :: "v"(p), "v"(v) : "memory");
}
template <class T> __device__ __forceinline__ void st8(void* p, T v) {
  asm volatile("global_store_dwordx2 %0, %1, off" WT :: "v"(p), "v"(v) : "memory");
}

typedef __attribute__((ext_vector_type(8))) _Float16 f16x8;
typedef __attribute__((ext_vector_type(2))) _Float16 f16x2;
__device__ __forceinline__ u32 packh(float a, float b) {
  f32x2 v = {a, b};
  return __builtin_bit_cast(u32, __builtin_convertvector(v, f16x2));
}
__device__ __forceinline__ float lo_h(u32 w) { return (float)__builtin_bit_cast(f16x2, w).x; }
__device__ __forceinline__ float hi_h(u32 w) { return (float)__builtin_bit_cast(f16x2, w).y; }
__device__ __forceinline__ f16x8 as_hfrag(u32 a, u32 b, u32 c, u32 d) { return __builtin_bit_cast(f16x8, (u32x4){a, b, c, d}); }
__device__ __forceinline__ float wave_max(float x) {
  x = fmaxf(x, __builtin_bit_cast(float, __builtin_amdgcn_update_dpp(0, __builtin_bit_cast(int, x), 0xB1, 0xf, 0xf, false)));
  x = fmaxf(x, __builtin_bit_cast(float, __builtin_amdgcn_update_dpp(0, __builtin_bit_cast(int, x), 0x4E, 0xf, 0xf, false)));
  x = fmaxf(x, __builtin_bit_cast(float, __builtin_amdgcn_update_dpp(0, __builtin_bit_cast(int, x), 0x141, 0xf, 0xf, false)));
  x = fmaxf(x, __builtin_bit_cast(float, __builtin_amdgcn_update_dpp(0, __builtin_bit_cast(int, x), 0x140, 0xf, 0xf, false)));
  return xmax16_32(x);
}
template <int T> __device__ __forceinline__ void pow2_scale(float m, float& s, float& inv) {
  int e = (int)(__float_as_uint(m) >> 23);
  e = min(max(e, T + 2), 250);
  s = __uint_as_float((u32)(254 + T - e) << 23);
  inv = __uint_as_float((u32)(e - T) << 23);
}
__device__ __forceinline__ void cexpr(float zl, float e, float zh, float ep, float& re, float& im) {
  const float m = __builtin_amdgcn_exp2f(zl * e);
  const float r = zh * ep;
  re = m * __builtin_amdgcn_cosf(r);
  im = m * __builtin_amdgcn_sinf(r);
}
struct KgenArgs { const u16 *logA, *Aim, *Cre, *Cim, *logdt, *Dskip; };
#define LDSA(p) ((u32)(size_t)(__attribute__((address_space(3))) void*)(p))
#define PIN(x) asm volatile("" : "+v"(x))
template <int OFF> __device__ __forceinline__ void lds_inc(u32 a, u32 one) {
  asm volatile("s_mov_b64 exec, 1\n\tds_add_u32 %0, %1 offset:%2\n\ts_mov_b64 exec, -1" :: "v"(a), "v"(one), "i"(OFF) : "memory");
}
template <int OFF, int SLEEP> __device__ __forceinline__ void lds_poll(u32 a, int target) {
  u32 v; int s, cnt;
  asm volatile(
      "s_mov_b32 %2, 0x100000\n\t"
      "1:\n\t"
      "ds_read_b32 %0, %3 offset:%5\n\t"
      "s_waitcnt lgkmcnt(0)\n\t"
      "v_readfirstlane_b32 %1, %0\n\t"
      "s_nop 0\n\t"
      "s_cmp_ge_i32 %1, %4\n\t"
      "s_cbranch_scc1 2f\n\t"
      "s_sleep %6\n\t"
      "s_sub_u32 %2, %2, 1\n\t"
      "s_cbranch_scc0 1b\n\t"
      "2:\n\t"
      : "=&v"(v), "=&s"(s), "=&s"(cnt) : "v"(a), "s"(target), "i"(OFF), "i"(SLEEP) : "memory", "scc");
}
template <int OFF, int SLEEP> __device__ __forceinline__ void lds_poll2(u32 a, int target, u32& v) {
  int s, cnt;
  asm volatile(
      "v_readfirstlane_b32 %1, %0\n\t"
      "s_mov_b32 %2, 0x100000\n\t"
      "s_cmp_ge_i32 %1, %4\n\t"
      "s_cbranch_scc1 2f\n\t"
      "1:\n\t"
      "ds_read_b32 %0, %3 offset:%5\n\t"
      "s_waitcnt lgkmcnt(0)\n\t"
      "v_readfirstlane_b32 %1, %0\n\t"
      "s_nop 0\n\t"
      "s_cmp_ge_i32 %1, %4\n\t"
      "s_cbranch_scc1 2f\n\t"
      "s_sleep %6\n\t"
      "s_sub_u32 %2, %2, 1\n\t"
      "s_cbranch_scc0 1b\n\t"
      "2:\n\t"
      : "+v"(v), "=&s"(s), "=&s"(cnt) : "v"(a), "s"(target), "i"(OFF), "i"(SLEEP) : "memory", "scc");
}
#define SY_PART 0
#define SY_FREE 4
#define SY_WO 8
#define SY_FIN 24
#define SY_X1 32
#define SY_FS 36
#define SY_WI 40
#define SY_KP 44
#define SY_MK 48
#define SY_RV 52
#define SY_TI 56
#define SY_PG 64
#define SY_X 128
#define SY_STAT 168
#define SY_W 172
#define SY_RED 192
#define SY_Z 224
template <int SLEEP> __device__ __forceinline__ void pg_poll(u32 a, u32 target) {
  int cnt = 1 << 20;
  _Pragma("unroll 1")
  while (true) {
    u32x4 v;
    asm volatile("ds_read_b128 %0, %1 offset:%2\n\ts_waitcnt lgkmcnt(0)" : "=v"(v) : "v"(a), "i"(SY_PG) : "memory");
    if ((u32)__builtin_amdgcn_readfirstlane(min(min(v[0], v[1]), min(v[2], v[3]))) >= target || --cnt < 0) break;
    if (SLEEP) __builtin_amdgcn_s_sleep(SLEEP);
  }
}
__device__ __forceinline__ void kgen_units(int lane, const float* Kd, int nk, f32x4 (&acc)[3]) {
  const int i = lane & 15, kg = lane >> 4;
  const float eE = 8.f * (float)i;
  const int r_ = i & 3;
  const float e0 = (float)((i < 8 ? 2 * r_ : r_ == 0 ? 512 : r_ == 1 ? 516 : 512 * r_) + 32 * (i & 4));
  const float e0p = i < 8 ? e0 : 0.25f * e0;
  typedef __attribute__((address_space(3))) const f32x4* lf4;
  typedef __attribute__((address_space(3))) const u32x4* lu4;
  u32 pp = LDSA(Kd) + 16 * kg, pc = pp + (i < 8 ? 0 : 256);
#pragma unroll 1
  for (int ks = 0; ks < nk; ++ks) {
    PIN(pp); PIN(pc);
    const f32x4 zl = *(lf4)(size_t)pp, zs = ((lf4)(size_t)pc)[16], zh = ((lf4)(size_t)pp)[16];
    const f32x4 lc = ((lf4)(size_t)pp)[48], ph = ((lf4)(size_t)pp)[64];
    const u32x4 ma[2] = {((lu4)(size_t)pc)[80], ((lu4)(size_t)pp)[112]};
    pp += 64; pc += 64;
    float r[4], q[4], gr[4], gi[4];
#pragma unroll
    for (int e = 0; e < 4; ++e) cexpr(zl[e], e0, zs[e], e0p, gr[e], gi[e]);
#pragma unroll
    for (int e = 0; e < 4; ++e) {
      const float m = __builtin_amdgcn_exp2f(fmaf(zl[e], eE, lc[e])), th = fmaf(zh[e], -eE, ph[e]);
      r[e] = m * __builtin_amdgcn_cosf(th);
      q[e] = m * __builtin_amdgcn_sinf(th);
    }
    const f16x8 eh = as_hfrag(packh(r[0], r[1]), packh(r[2], r[3]), packh(q[0], q[1]), packh(q[2], q[3]));
    const u32 g[4] = {packh(gr[0], gr[1]), packh(gr[2], gr[3]), packh(gi[0], gi[1]), packh(gi[2], gi[3])};
    acc[0] = __builtin_amdgcn_mfma_f32_16x16x32_f16(as_hfrag(g[0], g[1], g[2], g[3]), eh, acc[0], 0, 0, 0);
#pragma unroll
    for (int m = 0; m < 2; ++m) {
      u32 d[4];
#pragma unroll
      for (int e = 0; e < 2; ++e) {
        const f16x2 xr = __builtin_bit_cast(f16x2, g[e]), xi = __builtin_bit_cast(f16x2, g[2 + e]);
        const u32 ur = ma[m][e], ui = ma[m][2 + e];
        const f16x2 mr = __builtin_bit_cast(f16x2, ur), mi = __builtin_bit_cast(f16x2, ui);
        d[e] = __builtin_bit_cast(u32, __builtin_elementwise_fma(xr, mr, -(xi * mi)));
        d[2 + e] = __builtin_bit_cast(u32, __builtin_elementwise_fma(xi, mr, xr * mi));
      }
      acc[m + 1] = __builtin_amdgcn_mfma_f32_16x16x32_f16(as_hfrag(d[0], d[1], d[2], d[3]), eh, acc[m + 1], 0, 0, 0);
    }
  }
}
template <int NQ> struct KgenIn { u16 la[NQ], ai[NQ], cr[NQ], ci[NQ], ld, ds; };
template <int NQ, int MODE>
__device__ __forceinline__ void kgen_load(int c, int ks0, int nk, int lane, const KgenArgs& ka, KgenIn<NQ>& in) {
  in.ld = ka.logdt[c];
#pragma unroll
  for (int q = 0; q < NQ; ++q) {
    const int n = 64 * q + lane, gi = c * NST + 16 * ks0 + (n < 16 * nk ? n : 0);
    in.la[q] = ka.logA[gi]; in.ai[q] = ka.Aim[gi]; in.cr[q] = ka.Cre[gi]; in.ci[q] = ka.Cim[gi];
  }
  if (MODE == 1) in.ds = ka.Dskip[c];
}
#define ATC0 1.591508832e-01f
#define ATC1 -5.292989150e-02f
#define ATC2 3.075435195e-02f
#define ATC3 -1.841891266e-02f
#define ATC4 8.268183977e-03f
#define ATC5 -1.824840286e-03f
#define KU 4
__device__ __forceinline__ float kgen_wave(int lane, const KgenIn<1>& in, float* Kd, f32x4 (&acc)[3]) {
#pragma unroll
  for (int m = 0; m < 3; ++m) acc[m] = (f32x4){0.f, 0.f, 0.f, 0.f};
  float cs, csi;
  const u32 za[1] = {in.la[0] | ((u32)in.ai[0] << 16)}, ca[1] = {in.cr[0] | ((u32)in.ci[0] << 16)};
  const float dt = rbf(__expf(bf2f(in.ld)));
  pow2_scale<10>(wave_max(fmaxf(fabsf(bf2f(in.cr[0])), fabsf(bf2f(in.ci[0])))), cs, csi);
  {
    const float zl = (-rbf(__expf(lo_f(za[0]))) * dt) * 1.4426950408889634f, zi = hi_f(za[0]) * dt;
    const float zh = zi * 0.15915494309189535f;
    const float z1 = zh - rintf(zh);
    float ar, ai, br, bi;
    cexpr(zl, 1.0f, z1, 1.0f, ar, ai);
    cexpr(zl, 256.0f, 256.0f * zh - rintf(256.0f * zh), 1.0f, br, bi);
    Kd[lane] = zl;
    Kd[64 + lane] = z1;
    Kd[128 + lane] = 4.0f * zh - rintf(4.0f * zh);
    const float cr = lo_f(ca[0]) * cs, ci = hi_f(ca[0]) * cs, ax = fabsf(cr), ay = fabsf(ci);
    const float tq = fminf(ax, ay) * __builtin_amdgcn_rcpf(fmaxf(fmaxf(ax, ay), 1e-30f)), t2 = tq * tq;
    float an = tq * fmaf(t2, fmaf(t2, fmaf(t2, fmaf(t2, fmaf(t2, ATC5, ATC4), ATC3), ATC2), ATC1), ATC0);
    an = ay > ax ? 0.25f - an : an;
    an = cr < 0.f ? 0.5f - an : an;
    Kd[192 + lane] = 0.5f * __builtin_amdgcn_logf(fmaf(cr, cr, ci * ci));
    Kd[256 + lane] = ci < 0.f ? an : -an;
    u16* kh = (u16*)(Kd + 320) + 8 * (lane >> 2) + (lane & 3);
    const u32 h0 = packh(ar, ai), h1 = packh(br * br - bi * bi, 2.0f * br * bi), h2 = packh(br, bi);
    kh[0] = (u16)h0; kh[4] = (u16)(h0 >> 16);
    kh[128] = (u16)h1; kh[132] = (u16)(h1 >> 16);
    kh[256] = (u16)h2; kh[260] = (u16)(h2 >> 16);
    asm volatile("s_waitcnt lgkmcnt(0)" ::: "memory");
    kgen_units(lane, Kd, KU, acc);
    asm volatile("" ::: "memory");
  }
#pragma unroll
  for (int m = 0; m < 3; ++m) acc[m] *= csi;
  return dt;
}
__device__ __forceinline__ void kgen_fin(int lane, float dt, float dsk, float* Kd, f32x4 (&acc)[3]) {
  const int i = lane & 15, kg = lane >> 4;
  float km = 0.f, ks_, ksi;
#pragma unroll
  for (int m = 0; m < 3; ++m)
#pragma unroll
    for (int r = 0; r < 4; ++r) km = fmaxf(km, fabsf(acc[m][r]));
  pow2_scale<14>(wave_max(km), ks_, ksi);
#pragma unroll
  for (int m = 0; m < 3; ++m) acc[m] *= ks_;
  u16* kq = (u16*)Kd;
  const u32 t0 = packh(acc[2][0], acc[2][1]), t1 = packh(acc[2][2], acc[2][3]);
  if (kg < 2) {
    *(u32x4*)&kq[128 * kg + 8 * i] = (u32x4){packh(acc[0][0], acc[1][0]), packh(acc[0][1], acc[1][1]), packh(acc[0][2], acc[1][2]), packh(acc[0][3], acc[1][3])};
    *(u32x2*)&kq[256 + 64 * kg + 4 * i] = (u32x2){t0, t1};
  } else {
    const u32 s1 = packh(acc[0][2], acc[0][3]);
    *(u32*)&kq[320 + 32 * kg + 2 * i] = packh(acc[0][0], acc[0][1]);
    *(u32*)&kq[384 + 32 * kg + 2 * i] = t0;
    u16* kr = kq + 480 + lane;
    kr[0] = (u16)s1; kr[64] = (u16)(s1 >> 16); kr[32] = (u16)t1; kr[96] = (u16)(t1 >> 16);
    if (lane == 32) kq[640] = (u16)packh(acc[1][3], 0.f);
  }
  if (lane == 0) { Kd[384] = (dt * (2.0f / 256.0f)) * ksi; Kd[385] = dsk * (1.0f / 256.0f); }
}

#define A_R1 105600
#define A_R2 (A_R1 + 80 * X1_P * 2)
#define SY_OFF (A_R2 + 1280)
#define CW_OFF (SY_OFF + 512)
#define KQ_OFF (CW_OFF + 1024)
#define BND_OFF (KQ_OFF + 3 * 3072)
#define WO1_OFF 52800
#define A_LDS (BND_OFF + 2048)
#define VMCNT(n) asm volatile("s_waitcnt vmcnt(" #n ")" ::: "memory")
template <int N> __device__ __forceinline__ void vmwait() { asm volatile("s_waitcnt vmcnt(%0)" :: "i"(N) : "memory"); }
__device__ __forceinline__ void vmwaitn(int n) {
  switch (n) {
    case 0: VMCNT(0); break; case 5: VMCNT(5); break; case 10: VMCNT(10); break; case 15: VMCNT(15); break; default: VMCNT(20); break;
  }
}
__device__ __forceinline__ u32 umax3(u32 a, u32 b, u32 c) { return max(max(a, b), c); }
#define XDEP 2
#define RS0 4
#define RS1 5
#define SSLEEP 12
#define PG1B 0
#define PG1 2
#define PG2 2
#define WOMODE 2
#define PG1P 3
#define XPOLL 1
#define XEARLY 1
#define WPW 3
#define WSLEEP0 0
#define GCAP (1 << 16)
#define X1T_OFF 130176
#define MARK16 ((u32x4){0xffffffffu, 0xffffffffu, 0xffffffffu, 0xffffffffu})
#define WSLEEP 2
#define IMPOS 2
#define XMPOS 1
#define IMSL 0
#define MSLEEP 32
#define STX(i)
#define STY(i) ST(i)
#define LANE(x) int x; asm volatile("v_mbcnt_lo_u32_b32 %0, -1, 0\n\tv_mbcnt_hi_u32_b32 %0, -1, %0" : "=v"(x))
#define NST0 7
#define NST1 2
#define PF 2
#define PTX 1
#define PTW 1
#define PTA 2
#define WOAT 28
#define FPOS 2
#define PFK 0
#define WARM 0
#define WOSL 0
#define EXP 0
#define XSSL0 24
#define XE 2
#define MSL0 1
#define MSL1 2
#define MSL2 2
#define P0E 2
#define P1F 1
#define MKW 4
#define MKAT 12
#define DQ 5
#define XSAT 16
#define CWAT 16
#define OSLEEP2 0
#define CWW 8
#define PFS 3
#define PTA1 2
#define APS 1
#define OSLEEP3 8
#define PFD 3
#define XSSL 0
__device__ __forceinline__ f32x4 ln_fin(float s, float ss) {
  const float mean = s * (1.0f / 640.0f);
  const float var = fmaxf(ss * (1.0f / 640.0f) - mean * mean, 0.f);
  const float m = -rbf(mean), r = __builtin_amdgcn_rcpf(rbf(__builtin_amdgcn_sqrtf(rbf(rbf(var) + 1e-5f))));
  return (f32x4){m, m, r, r};
}
template <int layer>
__device__ __forceinline__ void a_phase(unsigned char* smem, const int b, const int t, const u16* xt, const u16* wqkv, const u16* wo, unsigned char* ws,
                                        u16* obuf, u16* xtout, float2* stats, u32* fstat, const KgenArgs& ka, u32x4& sink, u32& tg0, u32& tg1) {
  u16* wq = (u16*)(ws + WS_WQ);
  u16* wot = (u16*)(ws + WS_WOT);
  const __amdgpu_buffer_rsrc_t frs = __builtin_amdgcn_make_buffer_rsrc(ws + WS_FLG, 0, FL_END, 0x00020000);
  const int kid = layer ? 2 : 1;
  u16* At = (u16*)smem;
  u16* St = (u16*)(smem + A_R1);
  const int xcd = b & 7, idx = b >> 3;
  const int rb = 8 * (xcd >> 1) + (idx & 7), hg = 4 * (xcd & 1) + (idx >> 3), s0 = rb * 64;
  const int rot = hg + (hg >> 2);
  const int lane = t & 63, w = __builtin_amdgcn_readfirstlane(t >> 6);
  const int l15 = lane & 15, g = lane >> 4;
  u32 sya = LDSA(smem) + SY_OFF + 256 * layer, one = 1u;
  const bf16x8 onesf = as_frag(make_uint4(0x3f803f80u, 0x3f803f80u, 0x3f803f80u, 0x3f803f80u));
  u32x2 tag;
  ST_DECL

  const __amdgpu_buffer_rsrc_t xsrs = __builtin_amdgcn_make_buffer_rsrc(ws + WS_IM0 + layer * 262144, 0, 262144, 0x00020000);
  if (!layer) {
#define XDMA0(P) { int pc = (P) + rot; pc -= pc >= 10 ? 10 : 0; \
      const u16* sp = xt + (size_t)(s0 - 16 + (lane >> 3)) * D_ + 8 * ((lane & 7) ^ ((lane >> 3) & 6)) + pc * 64; \
      _Pragma("unroll") for (int i = 0; i < 10; ++i) \
        __builtin_amdgcn_global_load_lds((const u32*)(sp + 8 * i * D_ + (i < 2 && !rb ? 16 * D_ : 0)), (u32*)(At + ((P) * 10 + i) * 512), 16, 0, 0); }
    if (__builtin_expect(w < 2, 1)) {
      const int r6 = (lane * 205) >> 11, c10 = lane - 10 * r6;
      const int q = (lane >> 2) & 3, p4 = lane & 3;
      __builtin_amdgcn_s_setprio(3);
      const bool tw = b >= 16;
      const int j = 2 * b - 32 + w, jq = j / 3, part = j - 3 * jq, jk = jq >> 3, jh = jq & 7;
      if (__builtin_expect(tw, 1)) {
        const u16* src = wqkv + (size_t)(32 * jk + r6) * 1920 + part * 640 + jh * 80 + 8 * c10;
        u16* tl = St + 13952 + 2560 * w;
        if (lane < 60) {
#pragma unroll
          for (int i = 0; i < 5; ++i) __builtin_amdgcn_global_load_lds((const u32*)(src + i * 11520), (u32*)(tl + i * 480), 16, 0, 0);
          if (lane < 20) __builtin_amdgcn_global_load_lds((const u32*)(src + 5 * 11520), (u32*)(tl + 5 * 480), 16, 0, 0);
        }
      }
      asm volatile("buffer_load_dwordx2 %0, off, %1, 0 sc1" : "=v"(tag) : "s"(frs) : "memory");
      __builtin_amdgcn_s_barrier();
      const int pb = l15 + (part ? 40 * part - 40 : 80), pd = part ? 120 : 0;
      int vo[5], co[2];
#pragma unroll
      for (int ct = 0; ct < 5; ++ct) {
        const int col = pb + 16 * ct + (16 * ct + l15 >= 40 ? pd : 0);
        vo[ct] = (int)((0xeda6273b51c9840ull >> (4 * (col >> 4))) & 15) * 1024 + (g * 16 + (col & 15)) * 16;
        PIN(vo[ct]);
      }
      co[0] = (pb + 16 * g + (lane >= 40 ? pd : 0)) * 4; co[1] = g ? -1 : (pb + 64 + pd) * 4;
      u32 ta = LDSA(St + 13952 + 2560 * w) + (8 * g + q) * 160 + 8 * p4;
      PIN(ta); PIN(co[0]); PIN(co[1]);
      asm volatile("s_waitcnt vmcnt(0)" : "+v"(tag), "+v"(sink) :: "memory");
      tag[0] += 1u;
      if (__builtin_expect(tw, 1)) {
        int kp = jk - 2 * (jh + (jh >> 2));
        kp += kp < 0 ? 20 : 0;
        const __amdgpu_buffer_rsrc_t trs = __builtin_amdgcn_make_buffer_rsrc((void*)(wq + (size_t)jh * 153600 + kp * 7680), 0, 15360, 0x00020000);
        const __amdgpu_buffer_rsrc_t crs = __builtin_amdgcn_make_buffer_rsrc(ws + WS_CW + (jh * 20 + jk) * 1024, 0, 1024, 0x00020000);
        s16x4 fl[5], fh[5];
        asm volatile(
            "ds_read_b64_tr_b16 %0, %10\n\t"
            "ds_read_b64_tr_b16 %5, %10 offset:640\n\t"
            "ds_read_b64_tr_b16 %1, %10 offset:32\n\t"
            "ds_read_b64_tr_b16 %6, %10 offset:672\n\t"
            "ds_read_b64_tr_b16 %2, %10 offset:64\n\t"
            "ds_read_b64_tr_b16 %7, %10 offset:704\n\t"
            "ds_read_b64_tr_b16 %3, %10 offset:96\n\t"
            "ds_read_b64_tr_b16 %8, %10 offset:736\n\t"
            "ds_read_b64_tr_b16 %4, %10 offset:128\n\t"
            "ds_read_b64_tr_b16 %9, %10 offset:768\n\t"
            "s_waitcnt lgkmcnt(0)"
            : "=&v"(fl[0]), "=&v"(fl[1]), "=&v"(fl[2]), "=&v"(fl[3]), "=&v"(fl[4]),
              "=&v"(fh[0]), "=&v"(fh[1]), "=&v"(fh[2]), "=&v"(fh[3]), "=&v"(fh[4])
            : "v"(ta) : "memory");
        ST(1);
        float cs[5];
#pragma unroll
        for (int ct = 0; ct < 5; ++ct) {
          const bf16x8 fr = __builtin_shufflevector(fl[ct], fh[ct], 0, 1, 2, 3, 4, 5, 6, 7);
          asm volatile("buffer_store_dwordx4 %0, %1, %2, 0 offen sc1\n\ts_nop 1" :: "v"(fr), "v"(vo[ct]), "s"(trs) : "memory");
          cs[ct] = __builtin_amdgcn_mfma_f32_16x16x32_bf16(onesf, fr, (f32x4){0.f, 0.f, 0.f, 0.f}, 0, 0, 0)[0];
        }
        const float c03 = g < 2 ? (g ? cs[1] : cs[0]) : (g == 2 ? cs[2] : cs[3]), c4 = g ? cs[0] : cs[4];
        asm volatile("buffer_store_dword %0, %1, %2, 0 offen sc1\n\tbuffer_store_dword %3, %4, %2, 0 offen sc1" :: "v"(c03), "v"(co[0]), "s"(crs), "v"(c4), "v"(co[1]) : "memory");
      }
      lds_inc<SY_TI>(sya, one);
      tg0 = __builtin_amdgcn_readfirstlane(tag[0]); tg1 = __builtin_amdgcn_readfirstlane(tag[1]);
      ST(3);
      XDMA0(2 + w)
      if (tw) {
        VMCNT(10);
        if (lane == 0) asm volatile("buffer_store_dwordx2 %0, off, %1, %2 sc1" :: "v"(tag), "s"(frs), "s"(FL_W + (jh * 60 + jk * 3 + part) * 64) : "memory");
      }
      ST(7);
      XDMA0(5 + w)
      XDMA0(8 + w)
      if (w == 0) {
        __builtin_amdgcn_s_sleep(WSLEEP0);
        int spins = 0;
        _Pragma("unroll 1")
        while (true) {
          asm volatile("" ::: "memory");
          const u32x2 f = __builtin_amdgcn_raw_buffer_load_b64(frs, min(lane, 59) * 64, FL_W + hg * 3840, 16);
          if (__builtin_amdgcn_ballot_w64(f[0] != tg0 || f[1] != tg1) == 0 || ++spins > GCAP) break;
          __builtin_amdgcn_s_sleep(WSLEEP);
        }
        RETRY(0, spins);
        lds_inc<SY_W>(sya, one);
        VMCNT(0);
        lds_inc<SY_X>(sya, one);
      } else {
        const __amdgpu_buffer_rsrc_t mrs = __builtin_amdgcn_make_buffer_rsrc(obuf, 0, HO_BYTES, 0x00020000);
        _Pragma("unroll 1")
        for (int i = 0; i < 11; ++i) {
          __builtin_amdgcn_raw_buffer_store_b128(MARK16, mrs, lane * 16, i < 10 ? (rb * 8 + hg) * 10240 + i * 1024 : HO_ST + rb * 8192 + hg * 1024, 17);
          __builtin_amdgcn_s_sleep(MSL0);
        }
        VMCNT(11);
        lds_inc<SY_X>(sya, one);
        ST(6);
        VMCNT(0);
        if (lane == 0) asm volatile("buffer_store_dwordx2 %0, off, %1, %2 sc1" :: "v"(tag), "s"(frs), "s"(FL_M + b * 64) : "memory");
      }
      ST(9);
    } else if (w == 2) {
      const int r6 = (lane * 205) >> 11, c10 = lane - 10 * r6, q = (lane >> 2) & 3, p4 = lane & 3;
      const bool to = b < 128;
      __builtin_amdgcn_s_setprio(3);
      asm volatile("buffer_load_dwordx2 %0, off, %1, 0 sc1" : "=v"(tag) : "s"(frs) : "memory");
      XDMA0(0)
      __builtin_amdgcn_s_barrier();
      XDMA0(1)
      if (to) {
        const u16* src = wo + (size_t)(80 * (b >> 4) + 40 * (b & 1) + r6) * D_ + ((b >> 1) & 7) * 80 + 8 * c10;
        u16* tl = (u16*)(smem + KQ_OFF);
        if (lane < 60) {
#pragma unroll
          for (int i = 0; i < 6; ++i) __builtin_amdgcn_global_load_lds((const u32*)(src + i * 6 * D_), (u32*)(tl + i * 480), 16, 0, 0);
          if (lane < 40) __builtin_amdgcn_global_load_lds((const u32*)(src + 36 * D_), (u32*)(tl + 6 * 480), 16, 0, 0);
        }
      }
      asm volatile("s_waitcnt vmcnt(0)" : "+v"(tag) :: "memory");
      lds_inc<SY_X + 4>(sya, one);
      tag[0] += 1u;
      tg0 = __builtin_amdgcn_readfirstlane(tag[0]); tg1 = __builtin_amdgcn_readfirstlane(tag[1]);
      ST(1);
      lds_poll<SY_TI, 1>(sya, 2);
      if (to) {
        const int s = b >> 4;
        const __amdgpu_buffer_rsrc_t trs = __builtin_amdgcn_make_buffer_rsrc((void*)(wot + (size_t)((b >> 1) & 7) * 51200 + ((b & 1) * 2 + (s >> 2)) * 12800), 0, 25600, 0x00020000);
        const u32 t0 = LDSA(smem) + KQ_OFF + (4 * g + q) * 160 + 8 * p4, t1 = LDSA(smem) + KQ_OFF + (16 + q) * 160 + 8 * p4;
        const int o0 = lane * 16, o1 = g ? -1 : lane * 16;
#pragma unroll
        for (int nt = 0; nt < 5; ++nt) {
          s16x4 f[4];
          const int np = (s < 4 ? (nt + 2) % 5 : nt) * 1024;
          asm volatile("ds_read_b64_tr_b16 %0, %4 offset:%6\n\tds_read_b64_tr_b16 %1, %4 offset:%6+3200\n\tds_read_b64_tr_b16 %2, %5 offset:%6\n\tds_read_b64_tr_b16 %3, %5 offset:%6+3200\n\ts_waitcnt lgkmcnt(0)"
                       : "=&v"(f[0]), "=&v"(f[1]), "=&v"(f[2]), "=&v"(f[3]) : "v"(t0), "v"(t1), "i"(32 * nt) : "memory");
          asm volatile("buffer_store_dwordx4 %0, %2, %4, %5 offen sc1\n\tbuffer_store_dwordx4 %1, %3, %4, %6 offen sc1\n\ts_nop 1"
                       :: "v"(__builtin_shufflevector(f[0], f[1], 0, 1, 2, 3, 4, 5, 6, 7)), "v"(__builtin_shufflevector(f[2], f[3], 0, 1, 2, 3, 4, 5, 6, 7)),
                          "v"(o0), "v"(o1), "s"(trs), "s"((s & 3) * 5120 + np), "s"(20480 + (s & 3) * 256 + np) : "memory");
        }
      }
      ST(3);
      XDMA0(4)
      XDMA0(7)
      {
        const u32 xa = LDSA(At) + (2 + (l15 >> 3)) * 1024 + (l15 & 7) * 128 + ((g ^ (l15 & 6)) << 4);
        const int xso = g == (l15 >> 2) ? (rb * 8 + hg) * 1024 + l15 * 16 : -4096;
        u32x4 sv[4];
#pragma unroll
        for (int mt = 0; mt < 4; ++mt) {
          bf16x8 fv[4];
          asm volatile("ds_read_b128 %0, %4 offset:%6\n\tds_read_b128 %1, %5 offset:%6\n\tds_read_b128 %2, %4 offset:%6+10240\n\tds_read_b128 %3, %5 offset:%6+10240\n\ts_waitcnt lgkmcnt(0)"
                       : "=&v"(fv[0]), "=&v"(fv[1]), "=&v"(fv[2]), "=&v"(fv[3]) : "v"(xa), "v"(xa ^ 64), "i"(2048 * mt) : "memory");
          f32x4 s1 = {0.f, 0.f, 0.f, 0.f}, s2 = s1;
#pragma unroll
          for (int i = 0; i < 4; ++i) {
            if (i < 2 || (hg & 3) == 3) {
              s1 = __builtin_amdgcn_mfma_f32_16x16x32_bf16(onesf, fv[i], s1, 0, 0, 0);
              s2 = __builtin_amdgcn_mfma_f32_16x16x32_bf16(fv[i], fv[i], s2, 0, 0, 0);
            }
          }
          const float d2 = (l15 & 2) ? ((l15 & 1) ? s2[3] : s2[2]) : ((l15 & 1) ? s2[1] : s2[0]);
          sv[mt] = (u32x4){__float_as_uint(s1[0] + 0.f), tg0, __float_as_uint(d2), tg0 + tg1};
          PIN(sv[mt]);
        }
        ST(6);
        __builtin_amdgcn_s_waitcnt(0x0070);
        lds_inc<SY_X>(sya, one);
#pragma unroll
        for (int mt = 0; mt < 4; ++mt) asm volatile("buffer_store_dwordx4 %0, %1, %2, 0 offen offset:%3 sc1" :: "v"(sv[mt]), "v"(xso), "s"(xsrs), "i"(256 * mt) : "memory");
        if (to && lane == 0) asm volatile("buffer_store_dwordx2 %0, off, %1, %2 sc1" :: "v"(tag), "s"(frs), "s"(FL_M + (256 + b) * 64) : "memory");
        asm volatile("s_nop 1");
      }
      ST(9);
    } else {
      const int ar = w - 3, ci = ar % 3;
      KgenIn<1> kin;
      tag = __builtin_amdgcn_raw_buffer_load_b64(frs, 0, 0, 16);
      kgen_load<1, 1>(5 * (b >> 1) + (ci == 2 ? 2 : 3 * (b & 1) + ci), 4 * (ar / 3), 4, lane, ka, kin);
      asm volatile("" ::: "memory");
      if (w == 4) asm volatile("ds_write_b64 %0, %1\n\ts_waitcnt lgkmcnt(0)" :: "v"(sya + 8 * lane), "v"((u32x2){0u, 0u}) : "memory");
      __builtin_amdgcn_s_barrier();
      if (w == 8) {
        bf16x8 be, bo;
#pragma unroll
        for (int e = 0; e < 8; ++e) {
          const int d = 8 * g + e - l15;
          be[e] = (d >= 0 && d <= 8) ? (short)0x3c00 : (short)0;
          bo[e] = (d >= 1 && d <= 7) ? (short)0x3c00 : (short)0;
        }
        asm volatile("ds_write_b128 %0, %1\n\tds_write_b128 %0, %2 offset:1024" :: "v"(LDSA(smem) + BND_OFF + 16 * lane), "v"(be), "v"(bo) : "memory");
      }
      if (w == 3) __builtin_amdgcn_s_setprio(2); else __builtin_amdgcn_s_setprio(1);
      tg0 = __builtin_amdgcn_readfirstlane(tag[0]) + 1u; tg1 = __builtin_amdgcn_readfirstlane(tag[1]);
      f32x4 kacc[3];
      f32x4* ka_ = (f32x4*)(St + ar * 1536);
      const float dt = kgen_wave(lane, kin, (float*)ka_, kacc);
      if (w == 3) ST(6); else ST(4);
      if (w < 9) {
#pragma unroll
        for (int m = 0; m < 3; ++m) ka_[64 * m + lane] = kacc[m];
        lds_inc<SY_KP>(sya, one);
      } else {
        const f32x4* pa = (const f32x4*)St + ci * 192 + lane;
        lds_poll<SY_KP, 2>(sya, 6);
#pragma unroll
        for (int m = 0; m < 3; ++m) { kacc[m] += pa[64 * m] + pa[576 + 64 * m]; PIN(kacc[m]); }
        lds_inc<SY_STAT>(sya, one);
        lds_poll<SY_X, 2>(sya, 3);
        kgen_fin(lane, dt, bf2f(kin.ds), (float*)(smem + KQ_OFF + ci * 3072), kacc);
      }
      __builtin_amdgcn_s_waitcnt(0x0070);
    }
  }
  if (w < 4) {
    {
    LANE(lane);
    const int l15 = lane & 15, g = lane >> 4;
    const __amdgpu_buffer_rsrc_t wrsrc = __builtin_amdgcn_make_buffer_rsrc((void*)(wq + (size_t)hg * 153600), 0, 307200, 0x00020000);
    const int tb = w == 1 ? 1 : 0;
    u32 wa = (w == 3 ? 11 : 4 * w) * 1024 + lane * 16;
    u32 xa = LDSA(At) + (layer ? (4 * g + ((lane >> 2) & 3)) * 32 + 8 * (lane & 3) : (l15 >> 3) * 1024 + (l15 & 7) * 128 + ((g ^ (l15 & 6)) << 4)), xb = xa ^ 64;
    u32 sy = sya + SY_X, pga = sya + SY_PG + 4 * w, cwa = LDSA(smem) + CW_OFF + tb * 64 + g * 16, mra = LDSA(smem) + A_R2 + l15 * 16;
    u32 sta = LDSA(St) + (l15 * X1_P + tb * 16 + 4 * g) * 2, pv;
    int so, s0_, s1_;
    __builtin_amdgcn_s_setprio(PF);
    if (!layer) {
      asm volatile(
        "s_cmp_lt_u32 %[w], 2\ns_cbranch_scc0 .LB%=\ns_mov_b32 %[s1], 0x100000\n.LA0v%=:\nds_read_b32 %[pv], %[sy] offset:0\ns_waitcnt lgkmcnt(0)\nv_readfirstlane_b32 %[s0], %[pv]\ns_nop 0\n"
        "s_cmp_ge_i32 %[s0], 3\ns_cbranch_scc1 .LA0h%=\ns_sleep 1\ns_sub_u32 %[s1], %[s1], 1\ns_cbranch_scc0 .LA0v%=\n.LA0h%=:\ns_mov_b32 %[s1], 0x100000\n.LA0w%=:\n"
        "ds_read_b32 %[pv], %[sy] offset:%[ow]\ns_waitcnt lgkmcnt(0)\nv_readfirstlane_b32 %[s0], %[pv]\ns_nop 0\ns_cmp_ge_i32 %[s0], 1\ns_cbranch_scc1 .LA0g%=\ns_sleep 1\ns_sub_u32 %[s1], %[s1], 1\n"
        "s_cbranch_scc0 .LA0w%=\n.LA0g%=:\ns_mov_b32 %[so], 0\nbuffer_load_dwordx4 v[32:35], %[wa], %[rs], %[so] offen offset:0 sc1\n"
        "buffer_load_dwordx4 v[36:39], %[wa], %[rs], %[so] offen offset:1024 sc1\nbuffer_load_dwordx4 v[40:43], %[wa], %[rs], %[so] offen offset:2048 sc1\n"
        "buffer_load_dwordx4 v[44:47], %[wa], %[rs], %[so] offen offset:3072 sc1\ns_mov_b32 %[so], 15360\nbuffer_load_dwordx4 v[48:51], %[wa], %[rs], %[so] offen offset:0 sc1\n"
        "buffer_load_dwordx4 v[52:55], %[wa], %[rs], %[so] offen offset:1024 sc1\nbuffer_load_dwordx4 v[56:59], %[wa], %[rs], %[so] offen offset:2048 sc1\n"
        "buffer_load_dwordx4 v[60:63], %[wa], %[rs], %[so] offen offset:3072 sc1\ns_mov_b32 %[so], 30720\nbuffer_load_dwordx4 v[64:67], %[wa], %[rs], %[so] offen offset:0 sc1\n"
        "buffer_load_dwordx4 v[68:71], %[wa], %[rs], %[so] offen offset:1024 sc1\nbuffer_load_dwordx4 v[72:75], %[wa], %[rs], %[so] offen offset:2048 sc1\n"
        "buffer_load_dwordx4 v[76:79], %[wa], %[rs], %[so] offen offset:3072 sc1\ns_mov_b32 %[so], 46080\nbuffer_load_dwordx4 v[80:83], %[wa], %[rs], %[so] offen offset:0 sc1\n"
        "buffer_load_dwordx4 v[84:87], %[wa], %[rs], %[so] offen offset:1024 sc1\nbuffer_load_dwordx4 v[88:91], %[wa], %[rs], %[so] offen offset:2048 sc1\n"
        "buffer_load_dwordx4 v[92:95], %[wa], %[rs], %[so] offen offset:3072 sc1\nds_read_b128 v[16:19], %[xa] offset:2048\nds_read_b128 v[20:23], %[xa] offset:4096\n"
        "ds_read_b128 v[12:15], %[xa] offset:0\nds_read_b128 v[24:27], %[xa] offset:6144\ns_waitcnt vmcnt(12) lgkmcnt(3)\nv_mfma_f32_16x16x32_bf16 v[100:103], v[32:35], v[16:19], 0\n"
        "ds_read_b128 v[28:31], %[xa] offset:8192\nv_mfma_f32_16x16x32_bf16 v[116:119], v[36:39], v[16:19], 0\nv_mfma_f32_16x16x32_bf16 v[136:139], v[40:43], v[16:19], 0\n"
        "v_mfma_f32_16x16x32_bf16 v[152:155], v[44:47], v[16:19], 0\nds_read_b128 v[16:19], %[xb] offset:2048\ns_waitcnt lgkmcnt(4)\nv_mfma_f32_16x16x32_bf16 v[104:107], v[32:35], v[20:23], 0\n"
        "v_mfma_f32_16x16x32_bf16 v[120:123], v[36:39], v[20:23], 0\nv_mfma_f32_16x16x32_bf16 v[140:143], v[40:43], v[20:23], 0\nv_mfma_f32_16x16x32_bf16 v[156:159], v[44:47], v[20:23], 0\n"
        "ds_read_b128 v[20:23], %[xb] offset:4096\ns_waitcnt lgkmcnt(4)\nv_mfma_f32_16x16x32_bf16 v[96:99], v[32:35], v[12:15], 0\nv_mfma_f32_16x16x32_bf16 v[132:135], v[40:43], v[12:15], 0\n"
        "ds_read_b128 v[12:15], %[xb] offset:0\ns_waitcnt lgkmcnt(4)\nv_mfma_f32_16x16x32_bf16 v[108:111], v[32:35], v[24:27], 0\nv_mfma_f32_16x16x32_bf16 v[124:127], v[36:39], v[24:27], 0\n"
        "v_mfma_f32_16x16x32_bf16 v[144:147], v[40:43], v[24:27], 0\nv_mfma_f32_16x16x32_bf16 v[160:163], v[44:47], v[24:27], 0\nds_read_b128 v[24:27], %[xb] offset:6144\ns_waitcnt lgkmcnt(4)\n"
        "v_mfma_f32_16x16x32_bf16 v[112:115], v[32:35], v[28:31], 0\nv_mfma_f32_16x16x32_bf16 v[128:131], v[36:39], v[28:31], 0\nv_mfma_f32_16x16x32_bf16 v[148:151], v[40:43], v[28:31], 0\n"
        "v_mfma_f32_16x16x32_bf16 v[164:167], v[44:47], v[28:31], 0\n.LA0l%=:\ns_waitcnt vmcnt(8) lgkmcnt(3)\nv_mfma_f32_16x16x32_bf16 v[100:103], v[48:51], v[16:19], v[100:103]\n"
        "s_addk_i32 %[so], 15360\nds_read_b128 v[28:31], %[xb] offset:8192\nv_mfma_f32_16x16x32_bf16 v[116:119], v[52:55], v[16:19], v[116:119]\n"
        "v_mfma_f32_16x16x32_bf16 v[136:139], v[56:59], v[16:19], v[136:139]\nbuffer_load_dwordx4 v[32:35], %[wa], %[rs], %[so] offen offset:0 sc1\n"
        "v_mfma_f32_16x16x32_bf16 v[152:155], v[60:63], v[16:19], v[152:155]\nds_read_b128 v[16:19], %[xa] offset:12288\ns_waitcnt lgkmcnt(4)\n"
        "v_mfma_f32_16x16x32_bf16 v[104:107], v[48:51], v[20:23], v[104:107]\nv_mfma_f32_16x16x32_bf16 v[120:123], v[52:55], v[20:23], v[120:123]\n"
        "buffer_load_dwordx4 v[36:39], %[wa], %[rs], %[so] offen offset:1024 sc1\nv_mfma_f32_16x16x32_bf16 v[140:143], v[56:59], v[20:23], v[140:143]\n"
        "v_mfma_f32_16x16x32_bf16 v[156:159], v[60:63], v[20:23], v[156:159]\nds_read_b128 v[20:23], %[xa] offset:14336\ns_waitcnt lgkmcnt(4)\n"
        "v_mfma_f32_16x16x32_bf16 v[96:99], v[48:51], v[12:15], v[96:99]\nv_mfma_f32_16x16x32_bf16 v[132:135], v[56:59], v[12:15], v[132:135]\nds_read_b128 v[12:15], %[xa] offset:10240\n"
        "s_waitcnt lgkmcnt(4)\nv_mfma_f32_16x16x32_bf16 v[108:111], v[48:51], v[24:27], v[108:111]\nbuffer_load_dwordx4 v[40:43], %[wa], %[rs], %[so] offen offset:2048 sc1\n"
        "v_mfma_f32_16x16x32_bf16 v[124:127], v[52:55], v[24:27], v[124:127]\nv_mfma_f32_16x16x32_bf16 v[144:147], v[56:59], v[24:27], v[144:147]\n"
        "buffer_load_dwordx4 v[44:47], %[wa], %[rs], %[so] offen offset:3072 sc1\nv_mfma_f32_16x16x32_bf16 v[160:163], v[60:63], v[24:27], v[160:163]\nds_read_b128 v[24:27], %[xa] offset:16384\n"
        "s_waitcnt lgkmcnt(4)\nv_mfma_f32_16x16x32_bf16 v[112:115], v[48:51], v[28:31], v[112:115]\nv_mfma_f32_16x16x32_bf16 v[128:131], v[52:55], v[28:31], v[128:131]\nv_add_u32 %[sy], 4, %[sy]\n"
        "v_mfma_f32_16x16x32_bf16 v[148:151], v[56:59], v[28:31], v[148:151]\nds_write_b32 %[pg], %[sy]\nv_mfma_f32_16x16x32_bf16 v[164:167], v[60:63], v[28:31], v[164:167]\n"
        "s_waitcnt vmcnt(8) lgkmcnt(4)\nv_mfma_f32_16x16x32_bf16 v[100:103], v[64:67], v[16:19], v[100:103]\ns_addk_i32 %[so], 15360\nds_read_b128 v[28:31], %[xa] offset:18432\n"
        "v_mfma_f32_16x16x32_bf16 v[116:119], v[68:71], v[16:19], v[116:119]\nv_mfma_f32_16x16x32_bf16 v[136:139], v[72:75], v[16:19], v[136:139]\n"
        "buffer_load_dwordx4 v[48:51], %[wa], %[rs], %[so] offen offset:0 sc1\nv_mfma_f32_16x16x32_bf16 v[152:155], v[76:79], v[16:19], v[152:155]\nds_read_b128 v[16:19], %[xb] offset:12288\n"
        "s_waitcnt lgkmcnt(5)\nv_mfma_f32_16x16x32_bf16 v[104:107], v[64:67], v[20:23], v[104:107]\nv_mfma_f32_16x16x32_bf16 v[120:123], v[68:71], v[20:23], v[120:123]\n"
        "buffer_load_dwordx4 v[52:55], %[wa], %[rs], %[so] offen offset:1024 sc1\nv_mfma_f32_16x16x32_bf16 v[140:143], v[72:75], v[20:23], v[140:143]\n"
        "v_mfma_f32_16x16x32_bf16 v[156:159], v[76:79], v[20:23], v[156:159]\nds_read_b128 v[20:23], %[xb] offset:14336\ns_waitcnt lgkmcnt(5)\n"
        "v_mfma_f32_16x16x32_bf16 v[96:99], v[64:67], v[12:15], v[96:99]\nv_mfma_f32_16x16x32_bf16 v[132:135], v[72:75], v[12:15], v[132:135]\nds_read_b128 v[12:15], %[xb] offset:10240\n"
        "s_waitcnt lgkmcnt(5)\nv_mfma_f32_16x16x32_bf16 v[108:111], v[64:67], v[24:27], v[108:111]\nbuffer_load_dwordx4 v[56:59], %[wa], %[rs], %[so] offen offset:2048 sc1\n"
        "v_mfma_f32_16x16x32_bf16 v[124:127], v[68:71], v[24:27], v[124:127]\nv_mfma_f32_16x16x32_bf16 v[144:147], v[72:75], v[24:27], v[144:147]\n"
        "buffer_load_dwordx4 v[60:63], %[wa], %[rs], %[so] offen offset:3072 sc1\nv_mfma_f32_16x16x32_bf16 v[160:163], v[76:79], v[24:27], v[160:163]\nds_read_b128 v[24:27], %[xb] offset:16384\n"
        "s_waitcnt lgkmcnt(4)\nv_mfma_f32_16x16x32_bf16 v[112:115], v[64:67], v[28:31], v[112:115]\nv_mfma_f32_16x16x32_bf16 v[128:131], v[68:71], v[28:31], v[128:131]\n"
        "v_mfma_f32_16x16x32_bf16 v[148:151], v[72:75], v[28:31], v[148:151]\nv_mfma_f32_16x16x32_bf16 v[164:167], v[76:79], v[28:31], v[164:167]\ns_waitcnt vmcnt(8) lgkmcnt(3)\n"
        "v_mfma_f32_16x16x32_bf16 v[100:103], v[80:83], v[16:19], v[100:103]\ns_addk_i32 %[so], 15360\nds_read_b128 v[28:31], %[xb] offset:18432\n"
        "v_mfma_f32_16x16x32_bf16 v[116:119], v[84:87], v[16:19], v[116:119]\nv_mfma_f32_16x16x32_bf16 v[136:139], v[88:91], v[16:19], v[136:139]\n"
        "buffer_load_dwordx4 v[64:67], %[wa], %[rs], %[so] offen offset:0 sc1\nv_mfma_f32_16x16x32_bf16 v[152:155], v[92:95], v[16:19], v[152:155]\nds_read_b128 v[16:19], %[xa] offset:22528\n"
        "s_waitcnt lgkmcnt(4)\nv_mfma_f32_16x16x32_bf16 v[104:107], v[80:83], v[20:23], v[104:107]\nv_mfma_f32_16x16x32_bf16 v[120:123], v[84:87], v[20:23], v[120:123]\n"
        "buffer_load_dwordx4 v[68:71], %[wa], %[rs], %[so] offen offset:1024 sc1\nv_mfma_f32_16x16x32_bf16 v[140:143], v[88:91], v[20:23], v[140:143]\n"
        "v_mfma_f32_16x16x32_bf16 v[156:159], v[92:95], v[20:23], v[156:159]\nds_read_b128 v[20:23], %[xa] offset:24576\ns_waitcnt lgkmcnt(4)\n"
        "v_mfma_f32_16x16x32_bf16 v[96:99], v[80:83], v[12:15], v[96:99]\nv_mfma_f32_16x16x32_bf16 v[132:135], v[88:91], v[12:15], v[132:135]\nds_read_b128 v[12:15], %[xa] offset:20480\n"
        "s_waitcnt lgkmcnt(4)\nv_mfma_f32_16x16x32_bf16 v[108:111], v[80:83], v[24:27], v[108:111]\nbuffer_load_dwordx4 v[72:75], %[wa], %[rs], %[so] offen offset:2048 sc1\n"
        "v_mfma_f32_16x16x32_bf16 v[124:127], v[84:87], v[24:27], v[124:127]\nv_mfma_f32_16x16x32_bf16 v[144:147], v[88:91], v[24:27], v[144:147]\n"
        "buffer_load_dwordx4 v[76:79], %[wa], %[rs], %[so] offen offset:3072 sc1\nv_mfma_f32_16x16x32_bf16 v[160:163], v[92:95], v[24:27], v[160:163]\nds_read_b128 v[24:27], %[xa] offset:26624\n"
        "s_waitcnt lgkmcnt(4)\nv_mfma_f32_16x16x32_bf16 v[112:115], v[80:83], v[28:31], v[112:115]\nv_mfma_f32_16x16x32_bf16 v[128:131], v[84:87], v[28:31], v[128:131]\nv_add_u32 %[sy], 4, %[sy]\n"
        "v_mfma_f32_16x16x32_bf16 v[148:151], v[88:91], v[28:31], v[148:151]\nds_write_b32 %[pg], %[sy]\nv_mfma_f32_16x16x32_bf16 v[164:167], v[92:95], v[28:31], v[164:167]\n"
        "s_waitcnt vmcnt(8) lgkmcnt(4)\nv_mfma_f32_16x16x32_bf16 v[100:103], v[32:35], v[16:19], v[100:103]\ns_addk_i32 %[so], 15360\nds_read_b128 v[28:31], %[xa] offset:28672\n"
        "v_mfma_f32_16x16x32_bf16 v[116:119], v[36:39], v[16:19], v[116:119]\nv_mfma_f32_16x16x32_bf16 v[136:139], v[40:43], v[16:19], v[136:139]\n"
        "buffer_load_dwordx4 v[80:83], %[wa], %[rs], %[so] offen offset:0 sc1\nv_mfma_f32_16x16x32_bf16 v[152:155], v[44:47], v[16:19], v[152:155]\nds_read_b128 v[16:19], %[xb] offset:22528\n"
        "s_waitcnt lgkmcnt(5)\nv_mfma_f32_16x16x32_bf16 v[104:107], v[32:35], v[20:23], v[104:107]\nv_mfma_f32_16x16x32_bf16 v[120:123], v[36:39], v[20:23], v[120:123]\n"
        "buffer_load_dwordx4 v[84:87], %[wa], %[rs], %[so] offen offset:1024 sc1\nv_mfma_f32_16x16x32_bf16 v[140:143], v[40:43], v[20:23], v[140:143]\n"
        "v_mfma_f32_16x16x32_bf16 v[156:159], v[44:47], v[20:23], v[156:159]\nds_read_b128 v[20:23], %[xb] offset:24576\ns_waitcnt lgkmcnt(5)\n"
        "v_mfma_f32_16x16x32_bf16 v[96:99], v[32:35], v[12:15], v[96:99]\nv_mfma_f32_16x16x32_bf16 v[132:135], v[40:43], v[12:15], v[132:135]\nds_read_b128 v[12:15], %[xb] offset:20480\n"
        "s_waitcnt lgkmcnt(5)\nv_mfma_f32_16x16x32_bf16 v[108:111], v[32:35], v[24:27], v[108:111]\nbuffer_load_dwordx4 v[88:91], %[wa], %[rs], %[so] offen offset:2048 sc1\n"
        "v_mfma_f32_16x16x32_bf16 v[124:127], v[36:39], v[24:27], v[124:127]\nv_mfma_f32_16x16x32_bf16 v[144:147], v[40:43], v[24:27], v[144:147]\n"
        "buffer_load_dwordx4 v[92:95], %[wa], %[rs], %[so] offen offset:3072 sc1\nv_mfma_f32_16x16x32_bf16 v[160:163], v[44:47], v[24:27], v[160:163]\nds_read_b128 v[24:27], %[xb] offset:26624\n"
        "s_waitcnt lgkmcnt(4)\nv_mfma_f32_16x16x32_bf16 v[112:115], v[32:35], v[28:31], v[112:115]\nv_mfma_f32_16x16x32_bf16 v[128:131], v[36:39], v[28:31], v[128:131]\n"
        "v_mfma_f32_16x16x32_bf16 v[148:151], v[40:43], v[28:31], v[148:151]\ns_cmp_lg_u32 %[so], 291840\nv_mfma_f32_16x16x32_bf16 v[164:167], v[44:47], v[28:31], v[164:167]\n"
        "v_add_u32 %[xa], 20480, %[xa]\nv_add_u32 %[xb], 20480, %[xb]\ns_cbranch_scc1 .LA0l%=\ns_waitcnt vmcnt(8) lgkmcnt(3)\nv_mfma_f32_16x16x32_bf16 v[100:103], v[48:51], v[16:19], v[100:103]\n"
        "ds_read_b128 v[28:31], %[xb] offset:8192\nv_mfma_f32_16x16x32_bf16 v[116:119], v[52:55], v[16:19], v[116:119]\nv_mfma_f32_16x16x32_bf16 v[136:139], v[56:59], v[16:19], v[136:139]\n"
        "v_mfma_f32_16x16x32_bf16 v[152:155], v[60:63], v[16:19], v[152:155]\nds_read_b128 v[16:19], %[xa] offset:12288\ns_waitcnt lgkmcnt(4)\n"
        "v_mfma_f32_16x16x32_bf16 v[104:107], v[48:51], v[20:23], v[104:107]\nv_mfma_f32_16x16x32_bf16 v[120:123], v[52:55], v[20:23], v[120:123]\n"
        "v_mfma_f32_16x16x32_bf16 v[140:143], v[56:59], v[20:23], v[140:143]\nv_mfma_f32_16x16x32_bf16 v[156:159], v[60:63], v[20:23], v[156:159]\nds_read_b128 v[20:23], %[xa] offset:14336\n"
        "s_waitcnt lgkmcnt(4)\nv_mfma_f32_16x16x32_bf16 v[96:99], v[48:51], v[12:15], v[96:99]\nv_mfma_f32_16x16x32_bf16 v[132:135], v[56:59], v[12:15], v[132:135]\n"
        "ds_read_b128 v[12:15], %[xa] offset:10240\ns_waitcnt lgkmcnt(4)\nv_mfma_f32_16x16x32_bf16 v[108:111], v[48:51], v[24:27], v[108:111]\n"
        "v_mfma_f32_16x16x32_bf16 v[124:127], v[52:55], v[24:27], v[124:127]\nv_mfma_f32_16x16x32_bf16 v[144:147], v[56:59], v[24:27], v[144:147]\n"
        "v_mfma_f32_16x16x32_bf16 v[160:163], v[60:63], v[24:27], v[160:163]\nds_read_b128 v[24:27], %[xa] offset:16384\ns_waitcnt lgkmcnt(4)\n"
        "v_mfma_f32_16x16x32_bf16 v[112:115], v[48:51], v[28:31], v[112:115]\nv_mfma_f32_16x16x32_bf16 v[128:131], v[52:55], v[28:31], v[128:131]\nv_add_u32 %[sy], 4, %[sy]\n"
        "v_mfma_f32_16x16x32_bf16 v[148:151], v[56:59], v[28:31], v[148:151]\nv_mfma_f32_16x16x32_bf16 v[164:167], v[60:63], v[28:31], v[164:167]\ns_waitcnt vmcnt(4) lgkmcnt(3)\n"
        "v_mfma_f32_16x16x32_bf16 v[100:103], v[64:67], v[16:19], v[100:103]\nds_read_b128 v[28:31], %[xa] offset:18432\nv_mfma_f32_16x16x32_bf16 v[116:119], v[68:71], v[16:19], v[116:119]\n"
        "v_mfma_f32_16x16x32_bf16 v[136:139], v[72:75], v[16:19], v[136:139]\nds_read_b32 %[pv], %[sy] offset:4\nv_mfma_f32_16x16x32_bf16 v[152:155], v[76:79], v[16:19], v[152:155]\n"
        "ds_read_b128 v[16:19], %[xb] offset:12288\ns_waitcnt lgkmcnt(5)\nv_mfma_f32_16x16x32_bf16 v[104:107], v[64:67], v[20:23], v[104:107]\n"
        "v_mfma_f32_16x16x32_bf16 v[120:123], v[68:71], v[20:23], v[120:123]\nv_mfma_f32_16x16x32_bf16 v[140:143], v[72:75], v[20:23], v[140:143]\n"
        "v_mfma_f32_16x16x32_bf16 v[156:159], v[76:79], v[20:23], v[156:159]\nds_read_b128 v[20:23], %[xb] offset:14336\ns_waitcnt lgkmcnt(5)\n"
        "v_mfma_f32_16x16x32_bf16 v[96:99], v[64:67], v[12:15], v[96:99]\nv_mfma_f32_16x16x32_bf16 v[132:135], v[72:75], v[12:15], v[132:135]\nds_read_b128 v[12:15], %[xb] offset:10240\n"
        "s_waitcnt lgkmcnt(5)\nv_mfma_f32_16x16x32_bf16 v[108:111], v[64:67], v[24:27], v[108:111]\nv_mfma_f32_16x16x32_bf16 v[124:127], v[68:71], v[24:27], v[124:127]\n"
        "v_mfma_f32_16x16x32_bf16 v[144:147], v[72:75], v[24:27], v[144:147]\nv_mfma_f32_16x16x32_bf16 v[160:163], v[76:79], v[24:27], v[160:163]\nds_read_b128 v[24:27], %[xb] offset:16384\n"
        "s_waitcnt lgkmcnt(5)\nv_mfma_f32_16x16x32_bf16 v[112:115], v[64:67], v[28:31], v[112:115]\nv_mfma_f32_16x16x32_bf16 v[128:131], v[68:71], v[28:31], v[128:131]\n"
        "v_mfma_f32_16x16x32_bf16 v[148:151], v[72:75], v[28:31], v[148:151]\nv_mfma_f32_16x16x32_bf16 v[164:167], v[76:79], v[28:31], v[164:167]\ns_waitcnt vmcnt(0) lgkmcnt(3)\n"
        "v_mfma_f32_16x16x32_bf16 v[100:103], v[80:83], v[16:19], v[100:103]\nv_readfirstlane_b32 %[s0], %[pv]\nds_read_b128 v[28:31], %[xb] offset:18432\n"
        "v_mfma_f32_16x16x32_bf16 v[116:119], v[84:87], v[16:19], v[116:119]\nv_mfma_f32_16x16x32_bf16 v[136:139], v[88:91], v[16:19], v[136:139]\ns_cmp_ge_i32 %[s0], %[nst]\n"
        "v_mfma_f32_16x16x32_bf16 v[152:155], v[92:95], v[16:19], v[152:155]\ns_cbranch_scc0 .LA0t%=\n.LA0u%=:\ns_waitcnt lgkmcnt(3)\n"
        "v_mfma_f32_16x16x32_bf16 v[104:107], v[80:83], v[20:23], v[104:107]\nv_add_u32 %[sy], 4, %[sy]\nds_read_b128 v[16:19], %[mra] offset:256\n"
        "v_mfma_f32_16x16x32_bf16 v[120:123], v[84:87], v[20:23], v[120:123]\nv_mfma_f32_16x16x32_bf16 v[140:143], v[88:91], v[20:23], v[140:143]\n"
        "v_mfma_f32_16x16x32_bf16 v[156:159], v[92:95], v[20:23], v[156:159]\nds_read_b128 v[32:35], %[cwa] offset:0\ns_waitcnt lgkmcnt(4)\n"
        "v_mfma_f32_16x16x32_bf16 v[96:99], v[80:83], v[12:15], v[96:99]\nds_read_b128 v[36:39], %[cwa] offset:320\nds_read_b128 v[20:23], %[mra] offset:512\n"
        "v_mfma_f32_16x16x32_bf16 v[132:135], v[88:91], v[12:15], v[132:135]\nds_read_b128 v[40:43], %[cwa] offset:640\ns_waitcnt lgkmcnt(6)\n"
        "v_mfma_f32_16x16x32_bf16 v[108:111], v[80:83], v[24:27], v[108:111]\nds_read_b128 v[44:47], %[cwa] offset:512\nds_read_b128 v[12:15], %[mra] offset:0\n"
        "v_mfma_f32_16x16x32_bf16 v[124:127], v[84:87], v[24:27], v[124:127]\nv_add_u32 %[sy], 4, %[sy]\nv_mfma_f32_16x16x32_bf16 v[144:147], v[88:91], v[24:27], v[144:147]\n"
        "v_mfma_f32_16x16x32_bf16 v[160:163], v[92:95], v[24:27], v[160:163]\nds_read_b128 v[24:27], %[mra] offset:768\ns_waitcnt lgkmcnt(8)\n"
        "v_mfma_f32_16x16x32_bf16 v[112:115], v[80:83], v[28:31], v[112:115]\nv_mfma_f32_16x16x32_bf16 v[128:131], v[84:87], v[28:31], v[128:131]\n"
        "v_mfma_f32_16x16x32_bf16 v[148:151], v[88:91], v[28:31], v[148:151]\nv_mfma_f32_16x16x32_bf16 v[164:167], v[92:95], v[28:31], v[164:167]\nds_read_b128 v[28:31], %[mra] offset:1024\n"
        "s_setprio %[pfs]\ns_waitcnt lgkmcnt(1)\n.long 0xd3b04064, 0x1d922120\n.long 0xd3b04066, 0x1d9a2122\n.long 0xd3b04074, 0x1dd22124\n.long 0xd3b04076, 0x1dda2126\n.long 0xd3b14064, 0x18022564\n"
        ".long 0xd3b14066, 0x18022566\n.long 0xd3b04068, 0x1da22920\n.long 0xd3b0406a, 0x1daa2922\n.long 0xd3b14074, 0x18022574\n.long 0xd3b14076, 0x18022576\nv_cvt_pk_bf16_f32 v100, v100, v101\n"
        "v_cvt_pk_bf16_f32 v101, v102, v103\n.long 0xd3b04078, 0x1de22924\n.long 0xd3b0407a, 0x1dea2926\n.long 0xd3b14068, 0x18022d68\n.long 0xd3b1406a, 0x18022d6a\nv_cvt_pk_bf16_f32 v116, v116, v117\n"
        "v_cvt_pk_bf16_f32 v117, v118, v119\nds_write_b64 %[sta], v[100:101] offset:7936\n.long 0xd3b04060, 0x1d821920\n.long 0xd3b04062, 0x1d8a1922\n.long 0xd3b14078, 0x18022d78\n"
        ".long 0xd3b1407a, 0x18022d7a\nv_cvt_pk_bf16_f32 v104, v104, v105\nv_cvt_pk_bf16_f32 v105, v106, v107\nds_write_b64 %[sta], v[116:117] offset:8096\n.long 0xd3b0406c, 0x1db23120\n"
        ".long 0xd3b0406e, 0x1dba3122\n.long 0xd3b14060, 0x18021d60\n.long 0xd3b14062, 0x18021d62\nv_cvt_pk_bf16_f32 v120, v120, v121\nv_cvt_pk_bf16_f32 v121, v122, v123\n"
        "ds_write_b64 %[sta], v[104:105] offset:15872\n.long 0xd3b0407c, 0x1df23124\n.long 0xd3b0407e, 0x1dfa3126\n.long 0xd3b1406c, 0x1802356c\n.long 0xd3b1406e, 0x1802356e\n"
        "v_cvt_pk_bf16_f32 v96, v96, v97\nv_cvt_pk_bf16_f32 v97, v98, v99\nds_write_b64 %[sta], v[120:121] offset:16032\ns_waitcnt lgkmcnt(4)\n.long 0xd3b04070, 0x1dc23920\n"
        ".long 0xd3b04072, 0x1dca3922\n.long 0xd3b1407c, 0x1802357c\n.long 0xd3b1407e, 0x1802357e\nv_cvt_pk_bf16_f32 v108, v108, v109\nv_cvt_pk_bf16_f32 v109, v110, v111\n"
        "ds_write_b64 %[sta], v[96:97] offset:0\n.long 0xd3b04080, 0x1e023924\n.long 0xd3b04082, 0x1e0a3926\n.long 0xd3b14070, 0x18023d70\n.long 0xd3b14072, 0x18023d72\n"
        "v_cvt_pk_bf16_f32 v124, v124, v125\nv_cvt_pk_bf16_f32 v125, v126, v127\nds_write_b64 %[sta], v[108:109] offset:23808\n.long 0xd3b14080, 0x18023d80\n.long 0xd3b14082, 0x18023d82\n"
        "v_cvt_pk_bf16_f32 v112, v112, v113\nv_cvt_pk_bf16_f32 v113, v114, v115\nds_write_b64 %[sta], v[124:125] offset:23968\nv_cvt_pk_bf16_f32 v128, v128, v129\nv_cvt_pk_bf16_f32 v129, v130, v131\n"
        "ds_write_b64 %[sta], v[112:113] offset:31744\nds_write_b64 %[sta], v[128:129] offset:31904\nds_write_b32 %[pg], %[sy]\nv_add_u32 %[sy], 4, %[sy]\n.long 0xd3b04088, 0x1e222128\n"
        ".long 0xd3b0408a, 0x1e2a212a\n.long 0xd3b04098, 0x1e62212c\n.long 0xd3b0409a, 0x1e6a212e\n.long 0xd3b14088, 0x18022588\n.long 0xd3b1408a, 0x1802258a\n.long 0xd3b0408c, 0x1e322928\n"
        ".long 0xd3b0408e, 0x1e3a292a\n.long 0xd3b14098, 0x18022598\n.long 0xd3b1409a, 0x1802259a\nv_cvt_pk_bf16_f32 v136, v136, v137\nv_cvt_pk_bf16_f32 v137, v138, v139\n.long 0xd3b0409c, 0x1e72292c\n"
        ".long 0xd3b0409e, 0x1e7a292e\n.long 0xd3b1408c, 0x18022d8c\n.long 0xd3b1408e, 0x18022d8e\nv_cvt_pk_bf16_f32 v152, v152, v153\nv_cvt_pk_bf16_f32 v153, v154, v155\n"
        "ds_write_b64 %[sta], v[136:137] offset:8256\n.long 0xd3b04084, 0x1e121928\n.long 0xd3b04086, 0x1e1a192a\n.long 0xd3b1409c, 0x18022d9c\n.long 0xd3b1409e, 0x18022d9e\n"
        "v_cvt_pk_bf16_f32 v140, v140, v141\nv_cvt_pk_bf16_f32 v141, v142, v143\nds_write_b64 %[sta], v[152:153] offset:8192\n.long 0xd3b04090, 0x1e423128\n.long 0xd3b04092, 0x1e4a312a\n"
        ".long 0xd3b14084, 0x18021d84\n.long 0xd3b14086, 0x18021d86\nv_cvt_pk_bf16_f32 v156, v156, v157\nv_cvt_pk_bf16_f32 v157, v158, v159\nds_write_b64 %[sta], v[140:141] offset:16192\n"
        ".long 0xd3b040a0, 0x1e82312c\n.long 0xd3b040a2, 0x1e8a312e\n.long 0xd3b14090, 0x18023590\n.long 0xd3b14092, 0x18023592\nv_cvt_pk_bf16_f32 v132, v132, v133\nv_cvt_pk_bf16_f32 v133, v134, v135\n"
        "ds_write_b64 %[sta], v[156:157] offset:16128\n.long 0xd3b04094, 0x1e523928\n.long 0xd3b04096, 0x1e5a392a\n.long 0xd3b140a0, 0x180235a0\n.long 0xd3b140a2, 0x180235a2\n"
        "v_cvt_pk_bf16_f32 v144, v144, v145\nv_cvt_pk_bf16_f32 v145, v146, v147\nds_write_b64 %[sta], v[132:133] offset:320\n.long 0xd3b040a4, 0x1e92392c\n.long 0xd3b040a6, 0x1e9a392e\n"
        ".long 0xd3b14094, 0x18023d94\n.long 0xd3b14096, 0x18023d96\nv_cvt_pk_bf16_f32 v160, v160, v161\nv_cvt_pk_bf16_f32 v161, v162, v163\nds_write_b64 %[sta], v[144:145] offset:24128\n"
        ".long 0xd3b140a4, 0x18023da4\n.long 0xd3b140a6, 0x18023da6\nv_cvt_pk_bf16_f32 v148, v148, v149\nv_cvt_pk_bf16_f32 v149, v150, v151\nds_write_b64 %[sta], v[160:161] offset:24064\n"
        "v_cvt_pk_bf16_f32 v164, v164, v165\nv_cvt_pk_bf16_f32 v165, v166, v167\nds_write_b64 %[sta], v[148:149] offset:32064\nds_write_b64 %[sta], v[164:165] offset:32000\nds_write_b32 %[pg], %[sy]\n"
        "s_branch .LE%=\n.LA0t%=:\ns_mov_b32 %[s1], 0x100000\n.LA0q%=:\nds_read_b32 %[pv], %[sy] offset:4\ns_waitcnt lgkmcnt(0)\nv_readfirstlane_b32 %[s0], %[pv]\ns_nop 0\ns_cmp_ge_i32 %[s0], %[nst]\n"
        "s_cbranch_scc1 .LA0u%=\ns_sleep 1\ns_sub_u32 %[s1], %[s1], 1\ns_cbranch_scc0 .LA0q%=\ns_branch .LA0u%=\n.LB%=:\ns_cmp_eq_u32 %[w], 2\ns_cbranch_scc0 .LC%=\ns_mov_b32 %[s1], 0x100000\n"
        ".LB0v%=:\nds_read_b32 %[pv], %[sy] offset:0\ns_waitcnt lgkmcnt(0)\nv_readfirstlane_b32 %[s0], %[pv]\ns_nop 0\ns_cmp_ge_i32 %[s0], 3\ns_cbranch_scc1 .LB0h%=\ns_sleep 1\n"
        "s_sub_u32 %[s1], %[s1], 1\ns_cbranch_scc0 .LB0v%=\n.LB0h%=:\ns_mov_b32 %[s1], 0x100000\n.LB0w%=:\nds_read_b32 %[pv], %[sy] offset:%[ow]\ns_waitcnt lgkmcnt(0)\n"
        "v_readfirstlane_b32 %[s0], %[pv]\ns_nop 0\ns_cmp_ge_i32 %[s0], 1\ns_cbranch_scc1 .LB0g%=\ns_sleep 1\ns_sub_u32 %[s1], %[s1], 1\ns_cbranch_scc0 .LB0w%=\n.LB0g%=:\ns_mov_b32 %[so], 0\n"
        "buffer_load_dwordx4 v[32:35], %[wa], %[rs], %[so] offen offset:3072 sc1\nbuffer_load_dwordx4 v[36:39], %[wa], %[rs], %[so] offen offset:0 sc1\n"
        "buffer_load_dwordx4 v[40:43], %[wa], %[rs], %[so] offen offset:1024 sc1\nbuffer_load_dwordx4 v[44:47], %[wa], %[rs], %[so] offen offset:2048 sc1\ns_mov_b32 %[so], 15360\n"
        "buffer_load_dwordx4 v[48:51], %[wa], %[rs], %[so] offen offset:3072 sc1\nbuffer_load_dwordx4 v[52:55], %[wa], %[rs], %[so] offen offset:0 sc1\n"
        "buffer_load_dwordx4 v[56:59], %[wa], %[rs], %[so] offen offset:1024 sc1\nbuffer_load_dwordx4 v[60:63], %[wa], %[rs], %[so] offen offset:2048 sc1\ns_mov_b32 %[so], 30720\n"
        "buffer_load_dwordx4 v[64:67], %[wa], %[rs], %[so] offen offset:3072 sc1\nbuffer_load_dwordx4 v[68:71], %[wa], %[rs], %[so] offen offset:0 sc1\n"
        "buffer_load_dwordx4 v[72:75], %[wa], %[rs], %[so] offen offset:1024 sc1\nbuffer_load_dwordx4 v[76:79], %[wa], %[rs], %[so] offen offset:2048 sc1\ns_mov_b32 %[so], 46080\n"
        "buffer_load_dwordx4 v[80:83], %[wa], %[rs], %[so] offen offset:3072 sc1\nbuffer_load_dwordx4 v[84:87], %[wa], %[rs], %[so] offen offset:0 sc1\n"
        "buffer_load_dwordx4 v[88:91], %[wa], %[rs], %[so] offen offset:1024 sc1\nbuffer_load_dwordx4 v[92:95], %[wa], %[rs], %[so] offen offset:2048 sc1\nds_read_b128 v[16:19], %[xa] offset:2048\n"
        "ds_read_b128 v[20:23], %[xa] offset:4096\nds_read_b128 v[12:15], %[xa] offset:0\nds_read_b128 v[24:27], %[xa] offset:6144\ns_waitcnt vmcnt(12) lgkmcnt(3)\n"
        "v_mfma_f32_16x16x32_bf16 v[96:99], v[32:35], v[16:19], 0\nds_read_b128 v[28:31], %[xa] offset:8192\nv_mfma_f32_16x16x32_bf16 v[108:111], v[36:39], v[16:19], 0\n"
        "v_mfma_f32_16x16x32_bf16 v[128:131], v[40:43], v[16:19], 0\nv_mfma_f32_16x16x32_bf16 v[148:151], v[44:47], v[16:19], 0\nds_read_b128 v[16:19], %[xb] offset:2048\ns_waitcnt lgkmcnt(4)\n"
        "v_mfma_f32_16x16x32_bf16 v[100:103], v[32:35], v[20:23], 0\nv_mfma_f32_16x16x32_bf16 v[112:115], v[36:39], v[20:23], 0\nv_mfma_f32_16x16x32_bf16 v[132:135], v[40:43], v[20:23], 0\n"
        "v_mfma_f32_16x16x32_bf16 v[152:155], v[44:47], v[20:23], 0\nds_read_b128 v[20:23], %[xb] offset:4096\ns_waitcnt lgkmcnt(4)\nv_mfma_f32_16x16x32_bf16 v[104:107], v[36:39], v[12:15], 0\n"
        "v_mfma_f32_16x16x32_bf16 v[124:127], v[40:43], v[12:15], 0\nv_mfma_f32_16x16x32_bf16 v[144:147], v[44:47], v[12:15], 0\nds_read_b128 v[12:15], %[xb] offset:0\ns_waitcnt lgkmcnt(4)\n"
        "v_mfma_f32_16x16x32_bf16 v[116:119], v[36:39], v[24:27], 0\nv_mfma_f32_16x16x32_bf16 v[136:139], v[40:43], v[24:27], 0\nv_mfma_f32_16x16x32_bf16 v[156:159], v[44:47], v[24:27], 0\n"
        "ds_read_b128 v[24:27], %[xb] offset:6144\ns_waitcnt lgkmcnt(4)\nv_mfma_f32_16x16x32_bf16 v[120:123], v[36:39], v[28:31], 0\nv_mfma_f32_16x16x32_bf16 v[140:143], v[40:43], v[28:31], 0\n"
        "v_mfma_f32_16x16x32_bf16 v[160:163], v[44:47], v[28:31], 0\n.LB0l%=:\ns_waitcnt vmcnt(8) lgkmcnt(3)\nv_mfma_f32_16x16x32_bf16 v[96:99], v[48:51], v[16:19], v[96:99]\ns_addk_i32 %[so], 15360\n"
        "ds_read_b128 v[28:31], %[xb] offset:8192\nv_mfma_f32_16x16x32_bf16 v[108:111], v[52:55], v[16:19], v[108:111]\nv_mfma_f32_16x16x32_bf16 v[128:131], v[56:59], v[16:19], v[128:131]\n"
        "buffer_load_dwordx4 v[32:35], %[wa], %[rs], %[so] offen offset:3072 sc1\nv_mfma_f32_16x16x32_bf16 v[148:151], v[60:63], v[16:19], v[148:151]\nds_read_b128 v[16:19], %[xa] offset:12288\n"
        "s_waitcnt lgkmcnt(4)\nv_mfma_f32_16x16x32_bf16 v[100:103], v[48:51], v[20:23], v[100:103]\nv_mfma_f32_16x16x32_bf16 v[112:115], v[52:55], v[20:23], v[112:115]\n"
        "buffer_load_dwordx4 v[36:39], %[wa], %[rs], %[so] offen offset:0 sc1\nv_mfma_f32_16x16x32_bf16 v[132:135], v[56:59], v[20:23], v[132:135]\n"
        "v_mfma_f32_16x16x32_bf16 v[152:155], v[60:63], v[20:23], v[152:155]\nds_read_b128 v[20:23], %[xa] offset:14336\ns_waitcnt lgkmcnt(4)\n"
        "v_mfma_f32_16x16x32_bf16 v[104:107], v[52:55], v[12:15], v[104:107]\nv_mfma_f32_16x16x32_bf16 v[124:127], v[56:59], v[12:15], v[124:127]\n"
        "buffer_load_dwordx4 v[40:43], %[wa], %[rs], %[so] offen offset:1024 sc1\nv_mfma_f32_16x16x32_bf16 v[144:147], v[60:63], v[12:15], v[144:147]\nds_read_b128 v[12:15], %[xa] offset:10240\n"
        "s_waitcnt lgkmcnt(4)\nv_mfma_f32_16x16x32_bf16 v[116:119], v[52:55], v[24:27], v[116:119]\nv_mfma_f32_16x16x32_bf16 v[136:139], v[56:59], v[24:27], v[136:139]\n"
        "buffer_load_dwordx4 v[44:47], %[wa], %[rs], %[so] offen offset:2048 sc1\nv_mfma_f32_16x16x32_bf16 v[156:159], v[60:63], v[24:27], v[156:159]\nds_read_b128 v[24:27], %[xa] offset:16384\n"
        "s_waitcnt lgkmcnt(4)\nv_mfma_f32_16x16x32_bf16 v[120:123], v[52:55], v[28:31], v[120:123]\nv_add_u32 %[sy], 4, %[sy]\nv_mfma_f32_16x16x32_bf16 v[140:143], v[56:59], v[28:31], v[140:143]\n"
        "ds_write_b32 %[pg], %[sy]\nv_mfma_f32_16x16x32_bf16 v[160:163], v[60:63], v[28:31], v[160:163]\ns_waitcnt vmcnt(8) lgkmcnt(4)\nv_mfma_f32_16x16x32_bf16 v[96:99], v[64:67], v[16:19], v[96:99]\n"
        "s_addk_i32 %[so], 15360\nds_read_b128 v[28:31], %[xa] offset:18432\nv_mfma_f32_16x16x32_bf16 v[108:111], v[68:71], v[16:19], v[108:111]\n"
        "v_mfma_f32_16x16x32_bf16 v[128:131], v[72:75], v[16:19], v[128:131]\nbuffer_load_dwordx4 v[48:51], %[wa], %[rs], %[so] offen offset:3072 sc1\n"
        "v_mfma_f32_16x16x32_bf16 v[148:151], v[76:79], v[16:19], v[148:151]\nds_read_b128 v[16:19], %[xb] offset:12288\ns_waitcnt lgkmcnt(5)\n"
        "v_mfma_f32_16x16x32_bf16 v[100:103], v[64:67], v[20:23], v[100:103]\nv_mfma_f32_16x16x32_bf16 v[112:115], v[68:71], v[20:23], v[112:115]\n"
        "buffer_load_dwordx4 v[52:55], %[wa], %[rs], %[so] offen offset:0 sc1\nv_mfma_f32_16x16x32_bf16 v[132:135], v[72:75], v[20:23], v[132:135]\n"
        "v_mfma_f32_16x16x32_bf16 v[152:155], v[76:79], v[20:23], v[152:155]\nds_read_b128 v[20:23], %[xb] offset:14336\ns_waitcnt lgkmcnt(5)\n"
        "v_mfma_f32_16x16x32_bf16 v[104:107], v[68:71], v[12:15], v[104:107]\nv_mfma_f32_16x16x32_bf16 v[124:127], v[72:75], v[12:15], v[124:127]\n"
        "buffer_load_dwordx4 v[56:59], %[wa], %[rs], %[so] offen offset:1024 sc1\nv_mfma_f32_16x16x32_bf16 v[144:147], v[76:79], v[12:15], v[144:147]\nds_read_b128 v[12:15], %[xb] offset:10240\n"
        "s_waitcnt lgkmcnt(5)\nv_mfma_f32_16x16x32_bf16 v[116:119], v[68:71], v[24:27], v[116:119]\nv_mfma_f32_16x16x32_bf16 v[136:139], v[72:75], v[24:27], v[136:139]\n"
        "buffer_load_dwordx4 v[60:63], %[wa], %[rs], %[so] offen offset:2048 sc1\nv_mfma_f32_16x16x32_bf16 v[156:159], v[76:79], v[24:27], v[156:159]\nds_read_b128 v[24:27], %[xb] offset:16384\n"
        "s_waitcnt lgkmcnt(4)\nv_mfma_f32_16x16x32_bf16 v[120:123], v[68:71], v[28:31], v[120:123]\nv_mfma_f32_16x16x32_bf16 v[140:143], v[72:75], v[28:31], v[140:143]\n"
        "v_mfma_f32_16x16x32_bf16 v[160:163], v[76:79], v[28:31], v[160:163]\ns_waitcnt vmcnt(8) lgkmcnt(3)\nv_mfma_f32_16x16x32_bf16 v[96:99], v[80:83], v[16:19], v[96:99]\ns_addk_i32 %[so], 15360\n"
        "ds_read_b128 v[28:31], %[xb] offset:18432\nv_mfma_f32_16x16x32_bf16 v[108:111], v[84:87], v[16:19], v[108:111]\nv_mfma_f32_16x16x32_bf16 v[128:131], v[88:91], v[16:19], v[128:131]\n"
        "buffer_load_dwordx4 v[64:67], %[wa], %[rs], %[so] offen offset:3072 sc1\nv_mfma_f32_16x16x32_bf16 v[148:151], v[92:95], v[16:19], v[148:151]\nds_read_b128 v[16:19], %[xa] offset:22528\n"
        "s_waitcnt lgkmcnt(4)\nv_mfma_f32_16x16x32_bf16 v[100:103], v[80:83], v[20:23], v[100:103]\nv_mfma_f32_16x16x32_bf16 v[112:115], v[84:87], v[20:23], v[112:115]\n"
        "buffer_load_dwordx4 v[68:71], %[wa], %[rs], %[so] offen offset:0 sc1\nv_mfma_f32_16x16x32_bf16 v[132:135], v[88:91], v[20:23], v[132:135]\n"
        "v_mfma_f32_16x16x32_bf16 v[152:155], v[92:95], v[20:23], v[152:155]\nds_read_b128 v[20:23], %[xa] offset:24576\ns_waitcnt lgkmcnt(4)\n"
        "v_mfma_f32_16x16x32_bf16 v[104:107], v[84:87], v[12:15], v[104:107]\nv_mfma_f32_16x16x32_bf16 v[124:127], v[88:91], v[12:15], v[124:127]\n"
        "buffer_load_dwordx4 v[72:75], %[wa], %[rs], %[so] offen offset:1024 sc1\nv_mfma_f32_16x16x32_bf16 v[144:147], v[92:95], v[12:15], v[144:147]\nds_read_b128 v[12:15], %[xa] offset:20480\n"
        "s_waitcnt lgkmcnt(4)\nv_mfma_f32_16x16x32_bf16 v[116:119], v[84:87], v[24:27], v[116:119]\nv_mfma_f32_16x16x32_bf16 v[136:139], v[88:91], v[24:27], v[136:139]\n"
        "buffer_load_dwordx4 v[76:79], %[wa], %[rs], %[so] offen offset:2048 sc1\nv_mfma_f32_16x16x32_bf16 v[156:159], v[92:95], v[24:27], v[156:159]\nds_read_b128 v[24:27], %[xa] offset:26624\n"
        "s_waitcnt lgkmcnt(4)\nv_mfma_f32_16x16x32_bf16 v[120:123], v[84:87], v[28:31], v[120:123]\nv_add_u32 %[sy], 4, %[sy]\nv_mfma_f32_16x16x32_bf16 v[140:143], v[88:91], v[28:31], v[140:143]\n"
        "ds_write_b32 %[pg], %[sy]\nv_mfma_f32_16x16x32_bf16 v[160:163], v[92:95], v[28:31], v[160:163]\ns_waitcnt vmcnt(8) lgkmcnt(4)\nv_mfma_f32_16x16x32_bf16 v[96:99], v[32:35], v[16:19], v[96:99]\n"
        "s_addk_i32 %[so], 15360\nds_read_b128 v[28:31], %[xa] offset:28672\nv_mfma_f32_16x16x32_bf16 v[108:111], v[36:39], v[16:19], v[108:111]\n"
        "v_mfma_f32_16x16x32_bf16 v[128:131], v[40:43], v[16:19], v[128:131]\nbuffer_load_dwordx4 v[80:83], %[wa], %[rs], %[so] offen offset:3072 sc1\n"
        "v_mfma_f32_16x16x32_bf16 v[148:151], v[44:47], v[16:19], v[148:151]\nds_read_b128 v[16:19], %[xb] offset:22528\ns_waitcnt lgkmcnt(5)\n"
        "v_mfma_f32_16x16x32_bf16 v[100:103], v[32:35], v[20:23], v[100:103]\nv_mfma_f32_16x16x32_bf16 v[112:115], v[36:39], v[20:23], v[112:115]\n"
        "buffer_load_dwordx4 v[84:87], %[wa], %[rs], %[so] offen offset:0 sc1\nv_mfma_f32_16x16x32_bf16 v[132:135], v[40:43], v[20:23], v[132:135]\n"
        "v_mfma_f32_16x16x32_bf16 v[152:155], v[44:47], v[20:23], v[152:155]\nds_read_b128 v[20:23], %[xb] offset:24576\ns_waitcnt lgkmcnt(5)\n"
        "v_mfma_f32_16x16x32_bf16 v[104:107], v[36:39], v[12:15], v[104:107]\nv_mfma_f32_16x16x32_bf16 v[124:127], v[40:43], v[12:15], v[124:127]\n"
        "buffer_load_dwordx4 v[88:91], %[wa], %[rs], %[so] offen offset:1024 sc1\nv_mfma_f32_16x16x32_bf16 v[144:147], v[44:47], v[12:15], v[144:147]\nds_read_b128 v[12:15], %[xb] offset:20480\n"
        "s_waitcnt lgkmcnt(5)\nv_mfma_f32_16x16x32_bf16 v[116:119], v[36:39], v[24:27], v[116:119]\nv_mfma_f32_16x16x32_bf16 v[136:139], v[40:43], v[24:27], v[136:139]\n"
        "buffer_load_dwordx4 v[92:95], %[wa], %[rs], %[so] offen offset:2048 sc1\nv_mfma_f32_16x16x32_bf16 v[156:159], v[44:47], v[24:27], v[156:159]\nds_read_b128 v[24:27], %[xb] offset:26624\n"
        "s_waitcnt lgkmcnt(4)\nv_mfma_f32_16x16x32_bf16 v[120:123], v[36:39], v[28:31], v[120:123]\nv_mfma_f32_16x16x32_bf16 v[140:143], v[40:43], v[28:31], v[140:143]\ns_cmp_lg_u32 %[so], 291840\n"
        "v_mfma_f32_16x16x32_bf16 v[160:163], v[44:47], v[28:31], v[160:163]\nv_add_u32 %[xa], 20480, %[xa]\nv_add_u32 %[xb], 20480, %[xb]\ns_cbranch_scc1 .LB0l%=\ns_waitcnt vmcnt(8) lgkmcnt(3)\n"
        "v_mfma_f32_16x16x32_bf16 v[96:99], v[48:51], v[16:19], v[96:99]\nds_read_b128 v[28:31], %[xb] offset:8192\nv_mfma_f32_16x16x32_bf16 v[108:111], v[52:55], v[16:19], v[108:111]\n"
        "v_mfma_f32_16x16x32_bf16 v[128:131], v[56:59], v[16:19], v[128:131]\nv_mfma_f32_16x16x32_bf16 v[148:151], v[60:63], v[16:19], v[148:151]\nds_read_b128 v[16:19], %[xa] offset:12288\n"
        "s_waitcnt lgkmcnt(4)\nv_mfma_f32_16x16x32_bf16 v[100:103], v[48:51], v[20:23], v[100:103]\nv_mfma_f32_16x16x32_bf16 v[112:115], v[52:55], v[20:23], v[112:115]\n"
        "v_mfma_f32_16x16x32_bf16 v[132:135], v[56:59], v[20:23], v[132:135]\nv_mfma_f32_16x16x32_bf16 v[152:155], v[60:63], v[20:23], v[152:155]\nds_read_b128 v[20:23], %[xa] offset:14336\n"
        "s_waitcnt lgkmcnt(4)\nv_mfma_f32_16x16x32_bf16 v[104:107], v[52:55], v[12:15], v[104:107]\nv_mfma_f32_16x16x32_bf16 v[124:127], v[56:59], v[12:15], v[124:127]\n"
        "v_mfma_f32_16x16x32_bf16 v[144:147], v[60:63], v[12:15], v[144:147]\nds_read_b128 v[12:15], %[xa] offset:10240\ns_waitcnt lgkmcnt(4)\n"
        "v_mfma_f32_16x16x32_bf16 v[116:119], v[52:55], v[24:27], v[116:119]\nv_mfma_f32_16x16x32_bf16 v[136:139], v[56:59], v[24:27], v[136:139]\n"
        "v_mfma_f32_16x16x32_bf16 v[156:159], v[60:63], v[24:27], v[156:159]\nds_read_b128 v[24:27], %[xa] offset:16384\ns_waitcnt lgkmcnt(4)\n"
        "v_mfma_f32_16x16x32_bf16 v[120:123], v[52:55], v[28:31], v[120:123]\nv_add_u32 %[sy], 4, %[sy]\nv_mfma_f32_16x16x32_bf16 v[140:143], v[56:59], v[28:31], v[140:143]\n"
        "v_mfma_f32_16x16x32_bf16 v[160:163], v[60:63], v[28:31], v[160:163]\ns_waitcnt vmcnt(4) lgkmcnt(3)\nv_mfma_f32_16x16x32_bf16 v[96:99], v[64:67], v[16:19], v[96:99]\n"
        "ds_read_b128 v[28:31], %[xa] offset:18432\nv_mfma_f32_16x16x32_bf16 v[108:111], v[68:71], v[16:19], v[108:111]\nv_mfma_f32_16x16x32_bf16 v[128:131], v[72:75], v[16:19], v[128:131]\n"
        "ds_read_b32 %[pv], %[sy] offset:4\nv_mfma_f32_16x16x32_bf16 v[148:151], v[76:79], v[16:19], v[148:151]\nds_read_b128 v[16:19], %[xb] offset:12288\ns_waitcnt lgkmcnt(5)\n"
        "v_mfma_f32_16x16x32_bf16 v[100:103], v[64:67], v[20:23], v[100:103]\nv_mfma_f32_16x16x32_bf16 v[112:115], v[68:71], v[20:23], v[112:115]\n"
        "v_mfma_f32_16x16x32_bf16 v[132:135], v[72:75], v[20:23], v[132:135]\nv_mfma_f32_16x16x32_bf16 v[152:155], v[76:79], v[20:23], v[152:155]\nds_read_b128 v[20:23], %[xb] offset:14336\n"
        "s_waitcnt lgkmcnt(5)\nv_mfma_f32_16x16x32_bf16 v[104:107], v[68:71], v[12:15], v[104:107]\nv_mfma_f32_16x16x32_bf16 v[124:127], v[72:75], v[12:15], v[124:127]\n"
        "v_mfma_f32_16x16x32_bf16 v[144:147], v[76:79], v[12:15], v[144:147]\nds_read_b128 v[12:15], %[xb] offset:10240\ns_waitcnt lgkmcnt(5)\n"
        "v_mfma_f32_16x16x32_bf16 v[116:119], v[68:71], v[24:27], v[116:119]\nv_mfma_f32_16x16x32_bf16 v[136:139], v[72:75], v[24:27], v[136:139]\n"
        "v_mfma_f32_16x16x32_bf16 v[156:159], v[76:79], v[24:27], v[156:159]\nds_read_b128 v[24:27], %[xb] offset:16384\ns_waitcnt lgkmcnt(5)\n"
        "v_mfma_f32_16x16x32_bf16 v[120:123], v[68:71], v[28:31], v[120:123]\nv_mfma_f32_16x16x32_bf16 v[140:143], v[72:75], v[28:31], v[140:143]\n"
        "v_mfma_f32_16x16x32_bf16 v[160:163], v[76:79], v[28:31], v[160:163]\ns_waitcnt vmcnt(0) lgkmcnt(3)\nv_mfma_f32_16x16x32_bf16 v[96:99], v[80:83], v[16:19], v[96:99]\n"
        "v_readfirstlane_b32 %[s0], %[pv]\nds_read_b128 v[28:31], %[xb] offset:18432\nv_mfma_f32_16x16x32_bf16 v[108:111], v[84:87], v[16:19], v[108:111]\n"
        "v_mfma_f32_16x16x32_bf16 v[128:131], v[88:91], v[16:19], v[128:131]\ns_cmp_ge_i32 %[s0], %[nst]\nv_mfma_f32_16x16x32_bf16 v[148:151], v[92:95], v[16:19], v[148:151]\ns_cbranch_scc0 .LB0t%=\n"
        ".LB0u%=:\ns_waitcnt lgkmcnt(3)\nv_mfma_f32_16x16x32_bf16 v[100:103], v[80:83], v[20:23], v[100:103]\nv_add_u32 %[sy], 4, %[sy]\nds_read_b128 v[16:19], %[mra] offset:256\n"
        "v_mfma_f32_16x16x32_bf16 v[112:115], v[84:87], v[20:23], v[112:115]\nv_mfma_f32_16x16x32_bf16 v[132:135], v[88:91], v[20:23], v[132:135]\n"
        "v_mfma_f32_16x16x32_bf16 v[152:155], v[92:95], v[20:23], v[152:155]\nds_read_b128 v[32:35], %[cwa] offset:448\ns_waitcnt lgkmcnt(4)\n"
        "v_mfma_f32_16x16x32_bf16 v[104:107], v[84:87], v[12:15], v[104:107]\nds_read_b128 v[36:39], %[cwa] offset:128\nds_read_b128 v[20:23], %[mra] offset:512\n"
        "v_mfma_f32_16x16x32_bf16 v[124:127], v[88:91], v[12:15], v[124:127]\nds_read_b128 v[40:43], %[cwa] offset:192\nv_mfma_f32_16x16x32_bf16 v[144:147], v[92:95], v[12:15], v[144:147]\n"
        "ds_read_b128 v[44:47], %[cwa] offset:768\ns_waitcnt lgkmcnt(7)\nv_mfma_f32_16x16x32_bf16 v[116:119], v[84:87], v[24:27], v[116:119]\nv_add_u32 %[sy], 4, %[sy]\n"
        "ds_read_b128 v[12:15], %[mra] offset:0\nv_mfma_f32_16x16x32_bf16 v[136:139], v[88:91], v[24:27], v[136:139]\nv_mfma_f32_16x16x32_bf16 v[156:159], v[92:95], v[24:27], v[156:159]\n"
        "ds_read_b128 v[24:27], %[mra] offset:768\ns_waitcnt lgkmcnt(8)\nv_mfma_f32_16x16x32_bf16 v[120:123], v[84:87], v[28:31], v[120:123]\n"
        "v_mfma_f32_16x16x32_bf16 v[140:143], v[88:91], v[28:31], v[140:143]\nv_mfma_f32_16x16x32_bf16 v[160:163], v[92:95], v[28:31], v[160:163]\nds_read_b128 v[28:31], %[mra] offset:1024\n"
        "s_setprio %[pfs]\ns_waitcnt lgkmcnt(1)\n.long 0xd3b04060, 0x1d822120\n.long 0xd3b04062, 0x1d8a2122\n.long 0xd3b0406c, 0x1db22124\n.long 0xd3b0406e, 0x1dba2126\n.long 0xd3b14060, 0x18022560\n"
        ".long 0xd3b14062, 0x18022562\n.long 0xd3b04080, 0x1e022128\n.long 0xd3b04082, 0x1e0a212a\n.long 0xd3b1406c, 0x1802256c\n.long 0xd3b1406e, 0x1802256e\nv_cvt_pk_bf16_f32 v96, v96, v97\n"
        "v_cvt_pk_bf16_f32 v97, v98, v99\n.long 0xd3b04064, 0x1d922920\n.long 0xd3b04066, 0x1d9a2922\n.long 0xd3b14080, 0x18022580\n.long 0xd3b14082, 0x18022582\nv_cvt_pk_bf16_f32 v108, v108, v109\n"
        "v_cvt_pk_bf16_f32 v109, v110, v111\nds_write_b64 %[sta], v[96:97] offset:8160\n.long 0xd3b04070, 0x1dc22924\n.long 0xd3b04072, 0x1dca2926\n.long 0xd3b14064, 0x18022d64\n"
        ".long 0xd3b14066, 0x18022d66\nv_cvt_pk_bf16_f32 v128, v128, v129\nv_cvt_pk_bf16_f32 v129, v130, v131\nds_write_b64 %[sta], v[108:109] offset:8000\n.long 0xd3b04084, 0x1e122928\n"
        ".long 0xd3b04086, 0x1e1a292a\n.long 0xd3b14070, 0x18022d70\n.long 0xd3b14072, 0x18022d72\nv_cvt_pk_bf16_f32 v100, v100, v101\nv_cvt_pk_bf16_f32 v101, v102, v103\n"
        "ds_write_b64 %[sta], v[128:129] offset:8032\n.long 0xd3b04068, 0x1da21924\n.long 0xd3b0406a, 0x1daa1926\n.long 0xd3b14084, 0x18022d84\n.long 0xd3b14086, 0x18022d86\n"
        "v_cvt_pk_bf16_f32 v112, v112, v113\nv_cvt_pk_bf16_f32 v113, v114, v115\nds_write_b64 %[sta], v[100:101] offset:16096\n.long 0xd3b0407c, 0x1df21928\n.long 0xd3b0407e, 0x1dfa192a\n"
        ".long 0xd3b14068, 0x18021d68\n.long 0xd3b1406a, 0x18021d6a\nv_cvt_pk_bf16_f32 v132, v132, v133\nv_cvt_pk_bf16_f32 v133, v134, v135\nds_write_b64 %[sta], v[112:113] offset:15936\n"
        ".long 0xd3b04074, 0x1dd23124\n.long 0xd3b04076, 0x1dda3126\n.long 0xd3b1407c, 0x18021d7c\n.long 0xd3b1407e, 0x18021d7e\nv_cvt_pk_bf16_f32 v104, v104, v105\nv_cvt_pk_bf16_f32 v105, v106, v107\n"
        "ds_write_b64 %[sta], v[132:133] offset:15968\n.long 0xd3b04088, 0x1e223128\n.long 0xd3b0408a, 0x1e2a312a\n.long 0xd3b14074, 0x18023574\n.long 0xd3b14076, 0x18023576\n"
        "v_cvt_pk_bf16_f32 v124, v124, v125\nv_cvt_pk_bf16_f32 v125, v126, v127\nds_write_b64 %[sta], v[104:105] offset:64\ns_waitcnt lgkmcnt(7)\n.long 0xd3b04078, 0x1de23924\n"
        ".long 0xd3b0407a, 0x1dea3926\n.long 0xd3b14088, 0x18023588\n.long 0xd3b1408a, 0x1802358a\nv_cvt_pk_bf16_f32 v116, v116, v117\nv_cvt_pk_bf16_f32 v117, v118, v119\n"
        "ds_write_b64 %[sta], v[124:125] offset:96\n.long 0xd3b0408c, 0x1e323928\n.long 0xd3b0408e, 0x1e3a392a\n.long 0xd3b14078, 0x18023d78\n.long 0xd3b1407a, 0x18023d7a\n"
        "v_cvt_pk_bf16_f32 v136, v136, v137\nv_cvt_pk_bf16_f32 v137, v138, v139\nds_write_b64 %[sta], v[116:117] offset:23872\n.long 0xd3b1408c, 0x18023d8c\n.long 0xd3b1408e, 0x18023d8e\n"
        "v_cvt_pk_bf16_f32 v120, v120, v121\nv_cvt_pk_bf16_f32 v121, v122, v123\nds_write_b64 %[sta], v[136:137] offset:23904\nv_cvt_pk_bf16_f32 v140, v140, v141\nv_cvt_pk_bf16_f32 v141, v142, v143\n"
        "ds_write_b64 %[sta], v[120:121] offset:31808\nds_write_b64 %[sta], v[140:141] offset:31840\nds_write_b32 %[pg], %[sy]\nv_add_u32 %[sy], 4, %[sy]\n.long 0xd3b04094, 0x1e52212c\n"
        ".long 0xd3b04096, 0x1e5a212e\n.long 0xd3b04098, 0x1e62292c\n.long 0xd3b0409a, 0x1e6a292e\n.long 0xd3b14094, 0x18022594\n.long 0xd3b14096, 0x18022596\n.long 0xd3b04090, 0x1e42192c\n"
        ".long 0xd3b04092, 0x1e4a192e\n.long 0xd3b14098, 0x18022d98\n.long 0xd3b1409a, 0x18022d9a\nv_cvt_pk_bf16_f32 v148, v148, v149\nv_cvt_pk_bf16_f32 v149, v150, v151\n.long 0xd3b0409c, 0x1e72312c\n"
        ".long 0xd3b0409e, 0x1e7a312e\n.long 0xd3b14090, 0x18021d90\n.long 0xd3b14092, 0x18021d92\nv_cvt_pk_bf16_f32 v152, v152, v153\nv_cvt_pk_bf16_f32 v153, v154, v155\n"
        "ds_write_b64 %[sta], v[148:149] offset:8320\n.long 0xd3b040a0, 0x1e82392c\n.long 0xd3b040a2, 0x1e8a392e\n.long 0xd3b1409c, 0x1802359c\n.long 0xd3b1409e, 0x1802359e\n"
        "v_cvt_pk_bf16_f32 v144, v144, v145\nv_cvt_pk_bf16_f32 v145, v146, v147\nds_write_b64 %[sta], v[152:153] offset:16256\n.long 0xd3b140a0, 0x18023da0\n.long 0xd3b140a2, 0x18023da2\n"
        "v_cvt_pk_bf16_f32 v156, v156, v157\nv_cvt_pk_bf16_f32 v157, v158, v159\nds_write_b64 %[sta], v[144:145] offset:384\nv_cvt_pk_bf16_f32 v160, v160, v161\nv_cvt_pk_bf16_f32 v161, v162, v163\n"
        "ds_write_b64 %[sta], v[156:157] offset:24192\nds_write_b64 %[sta], v[160:161] offset:32128\nds_write_b32 %[pg], %[sy]\ns_branch .LE%=\n.LB0t%=:\ns_mov_b32 %[s1], 0x100000\n.LB0q%=:\n"
        "ds_read_b32 %[pv], %[sy] offset:4\ns_waitcnt lgkmcnt(0)\nv_readfirstlane_b32 %[s0], %[pv]\ns_nop 0\ns_cmp_ge_i32 %[s0], %[nst]\ns_cbranch_scc1 .LB0u%=\ns_sleep 1\ns_sub_u32 %[s1], %[s1], 1\n"
        "s_cbranch_scc0 .LB0q%=\ns_branch .LB0u%=\n.LC%=:\ns_mov_b32 %[s1], 0x100000\n.LC0v%=:\nds_read_b32 %[pv], %[sy] offset:0\ns_waitcnt lgkmcnt(0)\nv_readfirstlane_b32 %[s0], %[pv]\ns_nop 0\n"
        "s_cmp_ge_i32 %[s0], 3\ns_cbranch_scc1 .LC0h%=\ns_sleep 1\ns_sub_u32 %[s1], %[s1], 1\ns_cbranch_scc0 .LC0v%=\n.LC0h%=:\ns_mov_b32 %[s1], 0x100000\n.LC0w%=:\n"
        "ds_read_b32 %[pv], %[sy] offset:%[ow]\ns_waitcnt lgkmcnt(0)\nv_readfirstlane_b32 %[s0], %[pv]\ns_nop 0\ns_cmp_ge_i32 %[s0], 1\ns_cbranch_scc1 .LC0g%=\ns_sleep 1\ns_sub_u32 %[s1], %[s1], 1\n"
        "s_cbranch_scc0 .LC0w%=\n.LC0g%=:\ns_mov_b32 %[so], 0\nbuffer_load_dwordx4 v[32:35], %[wa], %[rs], %[so] offen offset:0 sc1\n"
        "buffer_load_dwordx4 v[36:39], %[wa], %[rs], %[so] offen offset:1024 sc1\nbuffer_load_dwordx4 v[40:43], %[wa], %[rs], %[so] offen offset:2048 sc1\n"
        "buffer_load_dwordx4 v[44:47], %[wa], %[rs], %[so] offen offset:3072 sc1\ns_mov_b32 %[so], 15360\nbuffer_load_dwordx4 v[48:51], %[wa], %[rs], %[so] offen offset:0 sc1\n"
        "buffer_load_dwordx4 v[52:55], %[wa], %[rs], %[so] offen offset:1024 sc1\nbuffer_load_dwordx4 v[56:59], %[wa], %[rs], %[so] offen offset:2048 sc1\n"
        "buffer_load_dwordx4 v[60:63], %[wa], %[rs], %[so] offen offset:3072 sc1\ns_mov_b32 %[so], 30720\nbuffer_load_dwordx4 v[64:67], %[wa], %[rs], %[so] offen offset:0 sc1\n"
        "buffer_load_dwordx4 v[68:71], %[wa], %[rs], %[so] offen offset:1024 sc1\nbuffer_load_dwordx4 v[72:75], %[wa], %[rs], %[so] offen offset:2048 sc1\n"
        "buffer_load_dwordx4 v[76:79], %[wa], %[rs], %[so] offen offset:3072 sc1\ns_mov_b32 %[so], 46080\nbuffer_load_dwordx4 v[80:83], %[wa], %[rs], %[so] offen offset:0 sc1\n"
        "buffer_load_dwordx4 v[84:87], %[wa], %[rs], %[so] offen offset:1024 sc1\nbuffer_load_dwordx4 v[88:91], %[wa], %[rs], %[so] offen offset:2048 sc1\n"
        "buffer_load_dwordx4 v[92:95], %[wa], %[rs], %[so] offen offset:3072 sc1\nds_read_b128 v[24:27], %[xa] offset:6144\nds_read_b128 v[28:31], %[xa] offset:8192\n"
        "ds_read_b128 v[12:15], %[xa] offset:0\nds_read_b128 v[16:19], %[xa] offset:2048\ns_waitcnt vmcnt(12) lgkmcnt(3)\nv_mfma_f32_16x16x32_bf16 v[96:99], v[32:35], v[24:27], 0\n"
        "ds_read_b128 v[20:23], %[xa] offset:4096\nv_mfma_f32_16x16x32_bf16 v[116:119], v[36:39], v[24:27], 0\nv_mfma_f32_16x16x32_bf16 v[136:139], v[40:43], v[24:27], 0\n"
        "v_mfma_f32_16x16x32_bf16 v[156:159], v[44:47], v[24:27], 0\nds_read_b128 v[24:27], %[xb] offset:6144\ns_waitcnt lgkmcnt(4)\nv_mfma_f32_16x16x32_bf16 v[100:103], v[32:35], v[28:31], 0\n"
        "v_mfma_f32_16x16x32_bf16 v[120:123], v[36:39], v[28:31], 0\nv_mfma_f32_16x16x32_bf16 v[140:143], v[40:43], v[28:31], 0\nv_mfma_f32_16x16x32_bf16 v[160:163], v[44:47], v[28:31], 0\n"
        "ds_read_b128 v[28:31], %[xb] offset:8192\ns_waitcnt lgkmcnt(4)\nv_mfma_f32_16x16x32_bf16 v[104:107], v[36:39], v[12:15], 0\nv_mfma_f32_16x16x32_bf16 v[124:127], v[40:43], v[12:15], 0\n"
        "v_mfma_f32_16x16x32_bf16 v[144:147], v[44:47], v[12:15], 0\nds_read_b128 v[12:15], %[xb] offset:0\ns_waitcnt lgkmcnt(4)\nv_mfma_f32_16x16x32_bf16 v[108:111], v[36:39], v[16:19], 0\n"
        "v_mfma_f32_16x16x32_bf16 v[128:131], v[40:43], v[16:19], 0\nv_mfma_f32_16x16x32_bf16 v[148:151], v[44:47], v[16:19], 0\nds_read_b128 v[16:19], %[xb] offset:2048\ns_waitcnt lgkmcnt(4)\n"
        "v_mfma_f32_16x16x32_bf16 v[112:115], v[36:39], v[20:23], 0\nv_mfma_f32_16x16x32_bf16 v[132:135], v[40:43], v[20:23], 0\nv_mfma_f32_16x16x32_bf16 v[152:155], v[44:47], v[20:23], 0\n.LC0l%=:\n"
        "s_waitcnt vmcnt(8) lgkmcnt(3)\nv_mfma_f32_16x16x32_bf16 v[96:99], v[48:51], v[24:27], v[96:99]\ns_addk_i32 %[so], 15360\nds_read_b128 v[20:23], %[xb] offset:4096\n"
        "v_mfma_f32_16x16x32_bf16 v[116:119], v[52:55], v[24:27], v[116:119]\nv_mfma_f32_16x16x32_bf16 v[136:139], v[56:59], v[24:27], v[136:139]\n"
        "buffer_load_dwordx4 v[32:35], %[wa], %[rs], %[so] offen offset:0 sc1\nv_mfma_f32_16x16x32_bf16 v[156:159], v[60:63], v[24:27], v[156:159]\nds_read_b128 v[24:27], %[xa] offset:16384\n"
        "s_waitcnt lgkmcnt(4)\nv_mfma_f32_16x16x32_bf16 v[100:103], v[48:51], v[28:31], v[100:103]\nv_mfma_f32_16x16x32_bf16 v[120:123], v[52:55], v[28:31], v[120:123]\n"
        "buffer_load_dwordx4 v[36:39], %[wa], %[rs], %[so] offen offset:1024 sc1\nv_mfma_f32_16x16x32_bf16 v[140:143], v[56:59], v[28:31], v[140:143]\n"
        "v_mfma_f32_16x16x32_bf16 v[160:163], v[60:63], v[28:31], v[160:163]\nds_read_b128 v[28:31], %[xa] offset:18432\ns_waitcnt lgkmcnt(4)\n"
        "v_mfma_f32_16x16x32_bf16 v[104:107], v[52:55], v[12:15], v[104:107]\nv_mfma_f32_16x16x32_bf16 v[124:127], v[56:59], v[12:15], v[124:127]\n"
        "buffer_load_dwordx4 v[40:43], %[wa], %[rs], %[so] offen offset:2048 sc1\nv_mfma_f32_16x16x32_bf16 v[144:147], v[60:63], v[12:15], v[144:147]\nds_read_b128 v[12:15], %[xa] offset:10240\n"
        "s_waitcnt lgkmcnt(4)\nv_mfma_f32_16x16x32_bf16 v[108:111], v[52:55], v[16:19], v[108:111]\nv_mfma_f32_16x16x32_bf16 v[128:131], v[56:59], v[16:19], v[128:131]\n"
        "buffer_load_dwordx4 v[44:47], %[wa], %[rs], %[so] offen offset:3072 sc1\nv_mfma_f32_16x16x32_bf16 v[148:151], v[60:63], v[16:19], v[148:151]\nds_read_b128 v[16:19], %[xa] offset:12288\n"
        "s_waitcnt lgkmcnt(4)\nv_mfma_f32_16x16x32_bf16 v[112:115], v[52:55], v[20:23], v[112:115]\nv_add_u32 %[sy], 4, %[sy]\nv_mfma_f32_16x16x32_bf16 v[132:135], v[56:59], v[20:23], v[132:135]\n"
        "ds_write_b32 %[pg], %[sy]\nv_mfma_f32_16x16x32_bf16 v[152:155], v[60:63], v[20:23], v[152:155]\ns_waitcnt vmcnt(8) lgkmcnt(4)\nv_mfma_f32_16x16x32_bf16 v[96:99], v[64:67], v[24:27], v[96:99]\n"
        "s_addk_i32 %[so], 15360\nds_read_b128 v[20:23], %[xa] offset:14336\nv_mfma_f32_16x16x32_bf16 v[116:119], v[68:71], v[24:27], v[116:119]\n"
        "v_mfma_f32_16x16x32_bf16 v[136:139], v[72:75], v[24:27], v[136:139]\nbuffer_load_dwordx4 v[48:51], %[wa], %[rs], %[so] offen offset:0 sc1\n"
        "v_mfma_f32_16x16x32_bf16 v[156:159], v[76:79], v[24:27], v[156:159]\nds_read_b128 v[24:27], %[xb] offset:16384\ns_waitcnt lgkmcnt(5)\n"
        "v_mfma_f32_16x16x32_bf16 v[100:103], v[64:67], v[28:31], v[100:103]\nv_mfma_f32_16x16x32_bf16 v[120:123], v[68:71], v[28:31], v[120:123]\n"
        "buffer_load_dwordx4 v[52:55], %[wa], %[rs], %[so] offen offset:1024 sc1\nv_mfma_f32_16x16x32_bf16 v[140:143], v[72:75], v[28:31], v[140:143]\n"
        "v_mfma_f32_16x16x32_bf16 v[160:163], v[76:79], v[28:31], v[160:163]\nds_read_b128 v[28:31], %[xb] offset:18432\ns_waitcnt lgkmcnt(5)\n"
        "v_mfma_f32_16x16x32_bf16 v[104:107], v[68:71], v[12:15], v[104:107]\nv_mfma_f32_16x16x32_bf16 v[124:127], v[72:75], v[12:15], v[124:127]\n"
        "buffer_load_dwordx4 v[56:59], %[wa], %[rs], %[so] offen offset:2048 sc1\nv_mfma_f32_16x16x32_bf16 v[144:147], v[76:79], v[12:15], v[144:147]\nds_read_b128 v[12:15], %[xb] offset:10240\n"
        "s_waitcnt lgkmcnt(5)\nv_mfma_f32_16x16x32_bf16 v[108:111], v[68:71], v[16:19], v[108:111]\nv_mfma_f32_16x16x32_bf16 v[128:131], v[72:75], v[16:19], v[128:131]\n"
        "buffer_load_dwordx4 v[60:63], %[wa], %[rs], %[so] offen offset:3072 sc1\nv_mfma_f32_16x16x32_bf16 v[148:151], v[76:79], v[16:19], v[148:151]\nds_read_b128 v[16:19], %[xb] offset:12288\n"
        "s_waitcnt lgkmcnt(4)\nv_mfma_f32_16x16x32_bf16 v[112:115], v[68:71], v[20:23], v[112:115]\nv_mfma_f32_16x16x32_bf16 v[132:135], v[72:75], v[20:23], v[132:135]\n"
        "v_mfma_f32_16x16x32_bf16 v[152:155], v[76:79], v[20:23], v[152:155]\ns_waitcnt vmcnt(8) lgkmcnt(3)\nv_mfma_f32_16x16x32_bf16 v[96:99], v[80:83], v[24:27], v[96:99]\ns_addk_i32 %[so], 15360\n"
        "ds_read_b128 v[20:23], %[xb] offset:14336\nv_mfma_f32_16x16x32_bf16 v[116:119], v[84:87], v[24:27], v[116:119]\nv_mfma_f32_16x16x32_bf16 v[136:139], v[88:91], v[24:27], v[136:139]\n"
        "buffer_load_dwordx4 v[64:67], %[wa], %[rs], %[so] offen offset:0 sc1\nv_mfma_f32_16x16x32_bf16 v[156:159], v[92:95], v[24:27], v[156:159]\nds_read_b128 v[24:27], %[xa] offset:26624\n"
        "s_waitcnt lgkmcnt(4)\nv_mfma_f32_16x16x32_bf16 v[100:103], v[80:83], v[28:31], v[100:103]\nv_mfma_f32_16x16x32_bf16 v[120:123], v[84:87], v[28:31], v[120:123]\n"
        "buffer_load_dwordx4 v[68:71], %[wa], %[rs], %[so] offen offset:1024 sc1\nv_mfma_f32_16x16x32_bf16 v[140:143], v[88:91], v[28:31], v[140:143]\n"
        "v_mfma_f32_16x16x32_bf16 v[160:163], v[92:95], v[28:31], v[160:163]\nds_read_b128 v[28:31], %[xa] offset:28672\ns_waitcnt lgkmcnt(4)\n"
        "v_mfma_f32_16x16x32_bf16 v[104:107], v[84:87], v[12:15], v[104:107]\nv_mfma_f32_16x16x32_bf16 v[124:127], v[88:91], v[12:15], v[124:127]\n"
        "buffer_load_dwordx4 v[72:75], %[wa], %[rs], %[so] offen offset:2048 sc1\nv_mfma_f32_16x16x32_bf16 v[144:147], v[92:95], v[12:15], v[144:147]\nds_read_b128 v[12:15], %[xa] offset:20480\n"
        "s_waitcnt lgkmcnt(4)\nv_mfma_f32_16x16x32_bf16 v[108:111], v[84:87], v[16:19], v[108:111]\nv_mfma_f32_16x16x32_bf16 v[128:131], v[88:91], v[16:19], v[128:131]\n"
        "buffer_load_dwordx4 v[76:79], %[wa], %[rs], %[so] offen offset:3072 sc1\nv_mfma_f32_16x16x32_bf16 v[148:151], v[92:95], v[16:19], v[148:151]\nds_read_b128 v[16:19], %[xa] offset:22528\n"
        "s_waitcnt lgkmcnt(4)\nv_mfma_f32_16x16x32_bf16 v[112:115], v[84:87], v[20:23], v[112:115]\nv_add_u32 %[sy], 4, %[sy]\nv_mfma_f32_16x16x32_bf16 v[132:135], v[88:91], v[20:23], v[132:135]\n"
        "ds_write_b32 %[pg], %[sy]\nv_mfma_f32_16x16x32_bf16 v[152:155], v[92:95], v[20:23], v[152:155]\ns_waitcnt vmcnt(8) lgkmcnt(4)\nv_mfma_f32_16x16x32_bf16 v[96:99], v[32:35], v[24:27], v[96:99]\n"
        "s_addk_i32 %[so], 15360\nds_read_b128 v[20:23], %[xa] offset:24576\nv_mfma_f32_16x16x32_bf16 v[116:119], v[36:39], v[24:27], v[116:119]\n"
        "v_mfma_f32_16x16x32_bf16 v[136:139], v[40:43], v[24:27], v[136:139]\nbuffer_load_dwordx4 v[80:83], %[wa], %[rs], %[so] offen offset:0 sc1\n"
        "v_mfma_f32_16x16x32_bf16 v[156:159], v[44:47], v[24:27], v[156:159]\nds_read_b128 v[24:27], %[xb] offset:26624\ns_waitcnt lgkmcnt(5)\n"
        "v_mfma_f32_16x16x32_bf16 v[100:103], v[32:35], v[28:31], v[100:103]\nv_mfma_f32_16x16x32_bf16 v[120:123], v[36:39], v[28:31], v[120:123]\n"
        "buffer_load_dwordx4 v[84:87], %[wa], %[rs], %[so] offen offset:1024 sc1\nv_mfma_f32_16x16x32_bf16 v[140:143], v[40:43], v[28:31], v[140:143]\n"
        "v_mfma_f32_16x16x32_bf16 v[160:163], v[44:47], v[28:31], v[160:163]\nds_read_b128 v[28:31], %[xb] offset:28672\ns_waitcnt lgkmcnt(5)\n"
        "v_mfma_f32_16x16x32_bf16 v[104:107], v[36:39], v[12:15], v[104:107]\nv_mfma_f32_16x16x32_bf16 v[124:127], v[40:43], v[12:15], v[124:127]\n"
        "buffer_load_dwordx4 v[88:91], %[wa], %[rs], %[so] offen offset:2048 sc1\nv_mfma_f32_16x16x32_bf16 v[144:147], v[44:47], v[12:15], v[144:147]\nds_read_b128 v[12:15], %[xb] offset:20480\n"
        "s_waitcnt lgkmcnt(5)\nv_mfma_f32_16x16x32_bf16 v[108:111], v[36:39], v[16:19], v[108:111]\nv_mfma_f32_16x16x32_bf16 v[128:131], v[40:43], v[16:19], v[128:131]\n"
        "buffer_load_dwordx4 v[92:95], %[wa], %[rs], %[so] offen offset:3072 sc1\nv_mfma_f32_16x16x32_bf16 v[148:151], v[44:47], v[16:19], v[148:151]\nds_read_b128 v[16:19], %[xb] offset:22528\n"
        "s_waitcnt lgkmcnt(4)\nv_mfma_f32_16x16x32_bf16 v[112:115], v[36:39], v[20:23], v[112:115]\nv_mfma_f32_16x16x32_bf16 v[132:135], v[40:43], v[20:23], v[132:135]\ns_cmp_lg_u32 %[so], 291840\n"
        "v_mfma_f32_16x16x32_bf16 v[152:155], v[44:47], v[20:23], v[152:155]\nv_add_u32 %[xa], 20480, %[xa]\nv_add_u32 %[xb], 20480, %[xb]\ns_cbranch_scc1 .LC0l%=\ns_waitcnt vmcnt(8) lgkmcnt(3)\n"
        "v_mfma_f32_16x16x32_bf16 v[96:99], v[48:51], v[24:27], v[96:99]\nds_read_b128 v[20:23], %[xb] offset:4096\nv_mfma_f32_16x16x32_bf16 v[116:119], v[52:55], v[24:27], v[116:119]\n"
        "v_mfma_f32_16x16x32_bf16 v[136:139], v[56:59], v[24:27], v[136:139]\nv_mfma_f32_16x16x32_bf16 v[156:159], v[60:63], v[24:27], v[156:159]\nds_read_b128 v[24:27], %[xa] offset:16384\n"
        "s_waitcnt lgkmcnt(4)\nv_mfma_f32_16x16x32_bf16 v[100:103], v[48:51], v[28:31], v[100:103]\nv_mfma_f32_16x16x32_bf16 v[120:123], v[52:55], v[28:31], v[120:123]\n"
        "v_mfma_f32_16x16x32_bf16 v[140:143], v[56:59], v[28:31], v[140:143]\nv_mfma_f32_16x16x32_bf16 v[160:163], v[60:63], v[28:31], v[160:163]\nds_read_b128 v[28:31], %[xa] offset:18432\n"
        "s_waitcnt lgkmcnt(4)\nv_mfma_f32_16x16x32_bf16 v[104:107], v[52:55], v[12:15], v[104:107]\nv_mfma_f32_16x16x32_bf16 v[124:127], v[56:59], v[12:15], v[124:127]\n"
        "v_mfma_f32_16x16x32_bf16 v[144:147], v[60:63], v[12:15], v[144:147]\nds_read_b128 v[12:15], %[xa] offset:10240\ns_waitcnt lgkmcnt(4)\n"
        "v_mfma_f32_16x16x32_bf16 v[108:111], v[52:55], v[16:19], v[108:111]\nv_mfma_f32_16x16x32_bf16 v[128:131], v[56:59], v[16:19], v[128:131]\n"
        "v_mfma_f32_16x16x32_bf16 v[148:151], v[60:63], v[16:19], v[148:151]\nds_read_b128 v[16:19], %[xa] offset:12288\ns_waitcnt lgkmcnt(4)\n"
        "v_mfma_f32_16x16x32_bf16 v[112:115], v[52:55], v[20:23], v[112:115]\nv_add_u32 %[sy], 4, %[sy]\nv_mfma_f32_16x16x32_bf16 v[132:135], v[56:59], v[20:23], v[132:135]\n"
        "v_mfma_f32_16x16x32_bf16 v[152:155], v[60:63], v[20:23], v[152:155]\ns_waitcnt vmcnt(4) lgkmcnt(3)\nv_mfma_f32_16x16x32_bf16 v[96:99], v[64:67], v[24:27], v[96:99]\n"
        "ds_read_b128 v[20:23], %[xa] offset:14336\nv_mfma_f32_16x16x32_bf16 v[116:119], v[68:71], v[24:27], v[116:119]\nv_mfma_f32_16x16x32_bf16 v[136:139], v[72:75], v[24:27], v[136:139]\n"
        "ds_read_b32 %[pv], %[sy] offset:4\nv_mfma_f32_16x16x32_bf16 v[156:159], v[76:79], v[24:27], v[156:159]\nds_read_b128 v[24:27], %[xb] offset:16384\ns_waitcnt lgkmcnt(5)\n"
        "v_mfma_f32_16x16x32_bf16 v[100:103], v[64:67], v[28:31], v[100:103]\nv_mfma_f32_16x16x32_bf16 v[120:123], v[68:71], v[28:31], v[120:123]\n"
        "v_mfma_f32_16x16x32_bf16 v[140:143], v[72:75], v[28:31], v[140:143]\nv_mfma_f32_16x16x32_bf16 v[160:163], v[76:79], v[28:31], v[160:163]\nds_read_b128 v[28:31], %[xb] offset:18432\n"
        "s_waitcnt lgkmcnt(5)\nv_mfma_f32_16x16x32_bf16 v[104:107], v[68:71], v[12:15], v[104:107]\nv_mfma_f32_16x16x32_bf16 v[124:127], v[72:75], v[12:15], v[124:127]\n"
        "v_mfma_f32_16x16x32_bf16 v[144:147], v[76:79], v[12:15], v[144:147]\nds_read_b128 v[12:15], %[xb] offset:10240\ns_waitcnt lgkmcnt(5)\n"
        "v_mfma_f32_16x16x32_bf16 v[108:111], v[68:71], v[16:19], v[108:111]\nv_mfma_f32_16x16x32_bf16 v[128:131], v[72:75], v[16:19], v[128:131]\n"
        "v_mfma_f32_16x16x32_bf16 v[148:151], v[76:79], v[16:19], v[148:151]\nds_read_b128 v[16:19], %[xb] offset:12288\ns_waitcnt lgkmcnt(5)\n"
        "v_mfma_f32_16x16x32_bf16 v[112:115], v[68:71], v[20:23], v[112:115]\nv_mfma_f32_16x16x32_bf16 v[132:135], v[72:75], v[20:23], v[132:135]\n"
        "v_mfma_f32_16x16x32_bf16 v[152:155], v[76:79], v[20:23], v[152:155]\ns_waitcnt vmcnt(0) lgkmcnt(3)\nv_mfma_f32_16x16x32_bf16 v[96:99], v[80:83], v[24:27], v[96:99]\n"
        "v_readfirstlane_b32 %[s0], %[pv]\nds_read_b128 v[20:23], %[xb] offset:14336\nv_mfma_f32_16x16x32_bf16 v[116:119], v[84:87], v[24:27], v[116:119]\n"
        "v_mfma_f32_16x16x32_bf16 v[136:139], v[88:91], v[24:27], v[136:139]\ns_cmp_ge_i32 %[s0], %[nst]\nv_mfma_f32_16x16x32_bf16 v[156:159], v[92:95], v[24:27], v[156:159]\ns_cbranch_scc0 .LC0t%=\n"
        ".LC0u%=:\ns_waitcnt lgkmcnt(3)\nv_mfma_f32_16x16x32_bf16 v[100:103], v[80:83], v[28:31], v[100:103]\nv_add_u32 %[sy], 4, %[sy]\nds_read_b128 v[24:27], %[mra] offset:768\n"
        "v_mfma_f32_16x16x32_bf16 v[120:123], v[84:87], v[28:31], v[120:123]\nv_mfma_f32_16x16x32_bf16 v[140:143], v[88:91], v[28:31], v[140:143]\n"
        "v_mfma_f32_16x16x32_bf16 v[160:163], v[92:95], v[28:31], v[160:163]\nds_read_b128 v[32:35], %[cwa] offset:448\ns_waitcnt lgkmcnt(4)\n"
        "v_mfma_f32_16x16x32_bf16 v[104:107], v[84:87], v[12:15], v[104:107]\nds_read_b128 v[36:39], %[cwa] offset:256\nds_read_b128 v[28:31], %[mra] offset:1024\n"
        "v_mfma_f32_16x16x32_bf16 v[124:127], v[88:91], v[12:15], v[124:127]\nds_read_b128 v[40:43], %[cwa] offset:832\nv_mfma_f32_16x16x32_bf16 v[144:147], v[92:95], v[12:15], v[144:147]\n"
        "ds_read_b128 v[44:47], %[cwa] offset:896\ns_waitcnt lgkmcnt(7)\nv_mfma_f32_16x16x32_bf16 v[108:111], v[84:87], v[16:19], v[108:111]\nv_add_u32 %[sy], 4, %[sy]\n"
        "ds_read_b128 v[12:15], %[mra] offset:0\nv_mfma_f32_16x16x32_bf16 v[128:131], v[88:91], v[16:19], v[128:131]\nv_mfma_f32_16x16x32_bf16 v[148:151], v[92:95], v[16:19], v[148:151]\n"
        "ds_read_b128 v[16:19], %[mra] offset:256\ns_waitcnt lgkmcnt(8)\nv_mfma_f32_16x16x32_bf16 v[112:115], v[84:87], v[20:23], v[112:115]\n"
        "v_mfma_f32_16x16x32_bf16 v[132:135], v[88:91], v[20:23], v[132:135]\nv_mfma_f32_16x16x32_bf16 v[152:155], v[92:95], v[20:23], v[152:155]\nds_read_b128 v[20:23], %[mra] offset:512\n"
        "s_setprio %[pfs]\ns_waitcnt lgkmcnt(1)\n.long 0xd3b04060, 0x1d823120\n.long 0xd3b04062, 0x1d8a3122\n.long 0xd3b04074, 0x1dd23124\n.long 0xd3b04076, 0x1dda3126\n.long 0xd3b14060, 0x18023560\n"
        ".long 0xd3b14062, 0x18023562\n.long 0xd3b04064, 0x1d923920\n.long 0xd3b04066, 0x1d9a3922\n.long 0xd3b14074, 0x18023574\n.long 0xd3b14076, 0x18023576\nv_cvt_pk_bf16_f32 v96, v96, v97\n"
        "v_cvt_pk_bf16_f32 v97, v98, v99\n.long 0xd3b04078, 0x1de23924\n.long 0xd3b0407a, 0x1dea3926\n.long 0xd3b14064, 0x18023d64\n.long 0xd3b14066, 0x18023d66\nv_cvt_pk_bf16_f32 v116, v116, v117\n"
        "v_cvt_pk_bf16_f32 v117, v118, v119\nds_write_b64 %[sta], v[96:97] offset:24032\n.long 0xd3b04068, 0x1da21924\n.long 0xd3b0406a, 0x1daa1926\n.long 0xd3b14078, 0x18023d78\n"
        ".long 0xd3b1407a, 0x18023d7a\nv_cvt_pk_bf16_f32 v100, v100, v101\nv_cvt_pk_bf16_f32 v101, v102, v103\nds_write_b64 %[sta], v[116:117] offset:23936\n.long 0xd3b0406c, 0x1db22124\n"
        ".long 0xd3b0406e, 0x1dba2126\n.long 0xd3b14068, 0x18021d68\n.long 0xd3b1406a, 0x18021d6a\nv_cvt_pk_bf16_f32 v120, v120, v121\nv_cvt_pk_bf16_f32 v121, v122, v123\n"
        "ds_write_b64 %[sta], v[100:101] offset:31968\ns_waitcnt lgkmcnt(3)\n.long 0xd3b04070, 0x1dc22924\n.long 0xd3b04072, 0x1dca2926\n.long 0xd3b1406c, 0x1802256c\n.long 0xd3b1406e, 0x1802256e\n"
        "v_cvt_pk_bf16_f32 v104, v104, v105\nv_cvt_pk_bf16_f32 v105, v106, v107\nds_write_b64 %[sta], v[120:121] offset:31872\n.long 0xd3b14070, 0x18022d70\n.long 0xd3b14072, 0x18022d72\n"
        "v_cvt_pk_bf16_f32 v108, v108, v109\nv_cvt_pk_bf16_f32 v109, v110, v111\nds_write_b64 %[sta], v[104:105] offset:128\nv_cvt_pk_bf16_f32 v112, v112, v113\nv_cvt_pk_bf16_f32 v113, v114, v115\n"
        "ds_write_b64 %[sta], v[108:109] offset:8064\nds_write_b64 %[sta], v[112:113] offset:16000\nds_write_b32 %[pg], %[sy]\nv_add_u32 %[sy], 4, %[sy]\n.long 0xd3b04088, 0x1e223128\n"
        ".long 0xd3b0408a, 0x1e2a312a\n.long 0xd3b0409c, 0x1e72312c\n.long 0xd3b0409e, 0x1e7a312e\n.long 0xd3b14088, 0x18023588\n.long 0xd3b1408a, 0x1802358a\n.long 0xd3b0408c, 0x1e323928\n"
        ".long 0xd3b0408e, 0x1e3a392a\n.long 0xd3b1409c, 0x1802359c\n.long 0xd3b1409e, 0x1802359e\nv_cvt_pk_bf16_f32 v136, v136, v137\nv_cvt_pk_bf16_f32 v137, v138, v139\n.long 0xd3b040a0, 0x1e82392c\n"
        ".long 0xd3b040a2, 0x1e8a392e\n.long 0xd3b1408c, 0x18023d8c\n.long 0xd3b1408e, 0x18023d8e\nv_cvt_pk_bf16_f32 v156, v156, v157\nv_cvt_pk_bf16_f32 v157, v158, v159\n"
        "ds_write_b64 %[sta], v[136:137] offset:24224\n.long 0xd3b0407c, 0x1df21928\n.long 0xd3b0407e, 0x1dfa192a\n.long 0xd3b140a0, 0x18023da0\n.long 0xd3b140a2, 0x18023da2\n"
        "v_cvt_pk_bf16_f32 v140, v140, v141\nv_cvt_pk_bf16_f32 v141, v142, v143\nds_write_b64 %[sta], v[156:157] offset:24256\n.long 0xd3b04090, 0x1e42192c\n.long 0xd3b04092, 0x1e4a192e\n"
        ".long 0xd3b1407c, 0x18021d7c\n.long 0xd3b1407e, 0x18021d7e\nv_cvt_pk_bf16_f32 v160, v160, v161\nv_cvt_pk_bf16_f32 v161, v162, v163\nds_write_b64 %[sta], v[140:141] offset:32160\n"
        ".long 0xd3b04080, 0x1e022128\n.long 0xd3b04082, 0x1e0a212a\n.long 0xd3b14090, 0x18021d90\n.long 0xd3b14092, 0x18021d92\nv_cvt_pk_bf16_f32 v124, v124, v125\nv_cvt_pk_bf16_f32 v125, v126, v127\n"
        "ds_write_b64 %[sta], v[160:161] offset:32192\n.long 0xd3b04094, 0x1e52212c\n.long 0xd3b04096, 0x1e5a212e\n.long 0xd3b14080, 0x18022580\n.long 0xd3b14082, 0x18022582\n"
        "v_cvt_pk_bf16_f32 v144, v144, v145\nv_cvt_pk_bf16_f32 v145, v146, v147\nds_write_b64 %[sta], v[124:125] offset:416\n.long 0xd3b04084, 0x1e122928\n.long 0xd3b04086, 0x1e1a292a\n"
        ".long 0xd3b14094, 0x18022594\n.long 0xd3b14096, 0x18022596\nv_cvt_pk_bf16_f32 v128, v128, v129\nv_cvt_pk_bf16_f32 v129, v130, v131\nds_write_b64 %[sta], v[144:145] offset:448\n"
        ".long 0xd3b04098, 0x1e62292c\n.long 0xd3b0409a, 0x1e6a292e\n.long 0xd3b14084, 0x18022d84\n.long 0xd3b14086, 0x18022d86\nv_cvt_pk_bf16_f32 v148, v148, v149\nv_cvt_pk_bf16_f32 v149, v150, v151\n"
        "ds_write_b64 %[sta], v[128:129] offset:8352\n.long 0xd3b14098, 0x18022d98\n.long 0xd3b1409a, 0x18022d9a\nv_cvt_pk_bf16_f32 v132, v132, v133\nv_cvt_pk_bf16_f32 v133, v134, v135\n"
        "ds_write_b64 %[sta], v[148:149] offset:8384\nv_cvt_pk_bf16_f32 v152, v152, v153\nv_cvt_pk_bf16_f32 v153, v154, v155\nds_write_b64 %[sta], v[132:133] offset:16288\n"
        "ds_write_b64 %[sta], v[152:153] offset:16320\nds_write_b32 %[pg], %[sy]\ns_branch .LE%=\n.LC0t%=:\ns_mov_b32 %[s1], 0x100000\n.LC0q%=:\nds_read_b32 %[pv], %[sy] offset:4\n"
        "s_waitcnt lgkmcnt(0)\nv_readfirstlane_b32 %[s0], %[pv]\ns_nop 0\ns_cmp_ge_i32 %[s0], %[nst]\ns_cbranch_scc1 .LC0u%=\ns_sleep 1\ns_sub_u32 %[s1], %[s1], 1\ns_cbranch_scc0 .LC0q%=\n"
        "s_branch .LC0u%=\n.LE%=:\n"
        : [pv] "=&v"(pv), [xa] "+v"(xa), [xb] "+v"(xb), [sy] "+v"(sy), [so] "=&s"(so), [s0] "=&s"(s0_), [s1] "=&s"(s1_)
        : [wa] "v"(wa), [rs] "s"(wrsrc), [pg] "v"(pga), [cwa] "v"(cwa), [mra] "v"(mra), [sta] "v"(sta), [w] "s"(w), [ow] "i"(SY_W - SY_X), [pfs] "i"(PFS), [nst] "i"(NST0)
        : "memory", "scc", "v12", "v13", "v14", "v15", "v16", "v17", "v18", "v19", "v20", "v21", "v22", "v23", "v24", "v25", "v26", "v27", "v28", "v29", "v30", "v31", "v32", "v33", "v34", "v35", "v36", "v37", "v38", "v39", "v40", "v41", "v42", "v43", "v44", "v45", "v46", "v47", "v48", "v49", "v50", "v51", "v52", "v53", "v54", "v55", "v56", "v57", "v58", "v59", "v60", "v61", "v62", "v63", "v64", "v65", "v66", "v67", "v68", "v69", "v70", "v71", "v72", "v73", "v74", "v75", "v76", "v77", "v78", "v79", "v80", "v81", "v82", "v83", "v84", "v85", "v86", "v87", "v88", "v89", "v90", "v91", "v92", "v93", "v94", "v95", "v96", "v97", "v98", "v99", "v100", "v101", "v102", "v103", "v104", "v105", "v106", "v107", "v108", "v109", "v110", "v111", "v112", "v113", "v114", "v115", "v116", "v117", "v118", "v119", "v120", "v121", "v122", "v123", "v124", "v125", "v126", "v127", "v128", "v129", "v130", "v131", "v132", "v133", "v134", "v135", "v136", "v137", "v138", "v139", "v140", "v141", "v142", "v143", "v144", "v145", "v146", "v147", "v148", "v149", "v150", "v151", "v152", "v153", "v154", "v155", "v156", "v157", "v158", "v159", "v160", "v161", "v162", "v163", "v164", "v165", "v166", "v167");
    } else {
      asm volatile(
        "s_cmp_lt_u32 %[w], 2\ns_cbranch_scc0 .LB%=\ns_mov_b32 %[so], 0\nbuffer_load_dwordx4 v[32:35], %[wa], %[rs], %[so] offen offset:0 sc1\n"
        "buffer_load_dwordx4 v[36:39], %[wa], %[rs], %[so] offen offset:1024 sc1\nbuffer_load_dwordx4 v[40:43], %[wa], %[rs], %[so] offen offset:2048 sc1\n"
        "buffer_load_dwordx4 v[44:47], %[wa], %[rs], %[so] offen offset:3072 sc1\ns_barrier\ns_mov_b32 %[so], 15360\nbuffer_load_dwordx4 v[48:51], %[wa], %[rs], %[so] offen offset:0 sc1\n"
        "buffer_load_dwordx4 v[52:55], %[wa], %[rs], %[so] offen offset:1024 sc1\nbuffer_load_dwordx4 v[56:59], %[wa], %[rs], %[so] offen offset:2048 sc1\n"
        "buffer_load_dwordx4 v[60:63], %[wa], %[rs], %[so] offen offset:3072 sc1\ns_mov_b32 %[so], 30720\nbuffer_load_dwordx4 v[64:67], %[wa], %[rs], %[so] offen offset:0 sc1\n"
        "buffer_load_dwordx4 v[68:71], %[wa], %[rs], %[so] offen offset:1024 sc1\nbuffer_load_dwordx4 v[72:75], %[wa], %[rs], %[so] offen offset:2048 sc1\n"
        "buffer_load_dwordx4 v[76:79], %[wa], %[rs], %[so] offen offset:3072 sc1\ns_mov_b32 %[so], 46080\nbuffer_load_dwordx4 v[80:83], %[wa], %[rs], %[so] offen offset:0 sc1\n"
        "buffer_load_dwordx4 v[84:87], %[wa], %[rs], %[so] offen offset:1024 sc1\nbuffer_load_dwordx4 v[88:91], %[wa], %[rs], %[so] offen offset:2048 sc1\n"
        "buffer_load_dwordx4 v[92:95], %[wa], %[rs], %[so] offen offset:3072 sc1\ns_mov_b32 %[s1], 0x100000\n.LA1w%=:\nds_read_b32 %[pv], %[sy] offset:0\ns_waitcnt lgkmcnt(0)\n"
        "v_readfirstlane_b32 %[s0], %[pv]\ns_nop 0\ns_cmp_ge_i32 %[s0], 10\ns_cbranch_scc1 .LA1g%=\ns_sleep 1\ns_sub_u32 %[s1], %[s1], 1\ns_cbranch_scc0 .LA1w%=\n.LA1g%=:\n"
        "ds_read_b64_tr_b16 v[16:17], %[xa] offset:1056\nds_read_b64_tr_b16 v[18:19], %[xa] offset:1568\nds_read_b64_tr_b16 v[20:21], %[xa] offset:2112\nds_read_b64_tr_b16 v[22:23], %[xa] offset:2624\n"
        "ds_read_b64_tr_b16 v[12:13], %[xa] offset:0\nds_read_b64_tr_b16 v[14:15], %[xa] offset:512\nds_read_b64_tr_b16 v[24:25], %[xa] offset:3168\nds_read_b64_tr_b16 v[26:27], %[xa] offset:3680\n"
        "s_waitcnt vmcnt(12) lgkmcnt(6)\nv_mfma_f32_16x16x32_bf16 v[100:103], v[32:35], v[16:19], 0\nds_read_b64_tr_b16 v[28:29], %[xa] offset:4224\n"
        "v_mfma_f32_16x16x32_bf16 v[116:119], v[36:39], v[16:19], 0\nds_read_b64_tr_b16 v[30:31], %[xa] offset:4736\nv_mfma_f32_16x16x32_bf16 v[136:139], v[40:43], v[16:19], 0\n"
        "ds_read_b32 %[pv], %[sy] offset:4\nv_mfma_f32_16x16x32_bf16 v[152:155], v[44:47], v[16:19], 0\nds_read_b64_tr_b16 v[16:17], %[xa] offset:6336\ns_waitcnt lgkmcnt(8)\n"
        "v_mfma_f32_16x16x32_bf16 v[104:107], v[32:35], v[20:23], 0\nds_read_b64_tr_b16 v[18:19], %[xa] offset:6848\nv_mfma_f32_16x16x32_bf16 v[120:123], v[36:39], v[20:23], 0\n"
        "v_mfma_f32_16x16x32_bf16 v[140:143], v[40:43], v[20:23], 0\nv_mfma_f32_16x16x32_bf16 v[156:159], v[44:47], v[20:23], 0\nds_read_b64_tr_b16 v[20:21], %[xa] offset:7392\ns_waitcnt lgkmcnt(8)\n"
        "v_mfma_f32_16x16x32_bf16 v[96:99], v[32:35], v[12:15], 0\nds_read_b64_tr_b16 v[22:23], %[xa] offset:7904\nv_mfma_f32_16x16x32_bf16 v[132:135], v[40:43], v[12:15], 0\n"
        "ds_read_b64_tr_b16 v[12:13], %[xa] offset:5280\ns_waitcnt lgkmcnt(8)\nv_mfma_f32_16x16x32_bf16 v[108:111], v[32:35], v[24:27], 0\nds_read_b64_tr_b16 v[14:15], %[xa] offset:5792\n"
        "v_mfma_f32_16x16x32_bf16 v[124:127], v[36:39], v[24:27], 0\nv_mfma_f32_16x16x32_bf16 v[144:147], v[40:43], v[24:27], 0\nv_mfma_f32_16x16x32_bf16 v[160:163], v[44:47], v[24:27], 0\n"
        "ds_read_b64_tr_b16 v[24:25], %[xa] offset:8448\ns_waitcnt lgkmcnt(8)\nv_mfma_f32_16x16x32_bf16 v[112:115], v[32:35], v[28:31], 0\nds_read_b64_tr_b16 v[26:27], %[xa] offset:8960\n"
        "v_mfma_f32_16x16x32_bf16 v[128:131], v[36:39], v[28:31], 0\nv_mfma_f32_16x16x32_bf16 v[148:151], v[40:43], v[28:31], 0\nv_mfma_f32_16x16x32_bf16 v[164:167], v[44:47], v[28:31], 0\n.LA1l%=:\n"
        "s_waitcnt vmcnt(8) lgkmcnt(6)\nv_mfma_f32_16x16x32_bf16 v[100:103], v[48:51], v[16:19], v[100:103]\ns_addk_i32 %[so], 15360\nv_readfirstlane_b32 %[s0], %[pv]\n"
        "v_mfma_f32_16x16x32_bf16 v[116:119], v[52:55], v[16:19], v[116:119]\ns_cmp_ge_i32 %[s0], 10\nds_read_b64_tr_b16 v[28:29], %[xa] offset:9504\n"
        "v_mfma_f32_16x16x32_bf16 v[136:139], v[56:59], v[16:19], v[136:139]\ns_cbranch_scc0 .LA1s0_%=\n.LA1r0_%=:\nds_read_b64_tr_b16 v[30:31], %[xa] offset:10016\n"
        "v_mfma_f32_16x16x32_bf16 v[152:155], v[60:63], v[16:19], v[152:155]\nds_read_b64_tr_b16 v[16:17], %[xa] offset:11616\ns_waitcnt lgkmcnt(7)\n"
        "v_mfma_f32_16x16x32_bf16 v[104:107], v[48:51], v[20:23], v[104:107]\nds_read_b64_tr_b16 v[18:19], %[xa] offset:12128\nbuffer_load_dwordx4 v[32:35], %[wa], %[rs], %[so] offen offset:0 sc1\n"
        "v_mfma_f32_16x16x32_bf16 v[120:123], v[52:55], v[20:23], v[120:123]\nbuffer_load_dwordx4 v[36:39], %[wa], %[rs], %[so] offen offset:1024 sc1\n"
        "v_mfma_f32_16x16x32_bf16 v[140:143], v[56:59], v[20:23], v[140:143]\nv_mfma_f32_16x16x32_bf16 v[156:159], v[60:63], v[20:23], v[156:159]\nds_read_b64_tr_b16 v[20:21], %[xa] offset:12672\n"
        "s_waitcnt lgkmcnt(7)\nv_mfma_f32_16x16x32_bf16 v[96:99], v[48:51], v[12:15], v[96:99]\nds_read_b64_tr_b16 v[22:23], %[xa] offset:13184\n"
        "v_mfma_f32_16x16x32_bf16 v[132:135], v[56:59], v[12:15], v[132:135]\nds_read_b64_tr_b16 v[12:13], %[xa] offset:10560\ns_waitcnt lgkmcnt(7)\n"
        "v_mfma_f32_16x16x32_bf16 v[108:111], v[48:51], v[24:27], v[108:111]\nds_read_b64_tr_b16 v[14:15], %[xa] offset:11072\nv_mfma_f32_16x16x32_bf16 v[124:127], v[52:55], v[24:27], v[124:127]\n"
        "buffer_load_dwordx4 v[40:43], %[wa], %[rs], %[so] offen offset:2048 sc1\nv_mfma_f32_16x16x32_bf16 v[144:147], v[56:59], v[24:27], v[144:147]\n"
        "buffer_load_dwordx4 v[44:47], %[wa], %[rs], %[so] offen offset:3072 sc1\nv_mfma_f32_16x16x32_bf16 v[160:163], v[60:63], v[24:27], v[160:163]\nds_read_b64_tr_b16 v[24:25], %[xa] offset:13728\n"
        "s_waitcnt lgkmcnt(7)\nv_mfma_f32_16x16x32_bf16 v[112:115], v[48:51], v[28:31], v[112:115]\nds_read_b64_tr_b16 v[26:27], %[xa] offset:14240\n"
        "v_mfma_f32_16x16x32_bf16 v[128:131], v[52:55], v[28:31], v[128:131]\nv_add_u32 %[sy], 4, %[sy]\nv_mfma_f32_16x16x32_bf16 v[148:151], v[56:59], v[28:31], v[148:151]\nds_write_b32 %[pg], %[sy]\n"
        "v_mfma_f32_16x16x32_bf16 v[164:167], v[60:63], v[28:31], v[164:167]\ns_waitcnt vmcnt(8) lgkmcnt(7)\nv_mfma_f32_16x16x32_bf16 v[100:103], v[64:67], v[16:19], v[100:103]\n"
        "s_addk_i32 %[so], 15360\nds_read_b64_tr_b16 v[28:29], %[xa] offset:14784\nv_mfma_f32_16x16x32_bf16 v[116:119], v[68:71], v[16:19], v[116:119]\nds_read_b64_tr_b16 v[30:31], %[xa] offset:15296\n"
        "v_mfma_f32_16x16x32_bf16 v[136:139], v[72:75], v[16:19], v[136:139]\nds_read_b32 %[pv], %[sy] offset:4\nbuffer_load_dwordx4 v[48:51], %[wa], %[rs], %[so] offen offset:0 sc1\n"
        "v_mfma_f32_16x16x32_bf16 v[152:155], v[76:79], v[16:19], v[152:155]\nds_read_b64_tr_b16 v[16:17], %[xa] offset:16896\ns_waitcnt lgkmcnt(9)\n"
        "v_mfma_f32_16x16x32_bf16 v[104:107], v[64:67], v[20:23], v[104:107]\nds_read_b64_tr_b16 v[18:19], %[xa] offset:17408\nv_mfma_f32_16x16x32_bf16 v[120:123], v[68:71], v[20:23], v[120:123]\n"
        "buffer_load_dwordx4 v[52:55], %[wa], %[rs], %[so] offen offset:1024 sc1\nv_mfma_f32_16x16x32_bf16 v[140:143], v[72:75], v[20:23], v[140:143]\n"
        "v_mfma_f32_16x16x32_bf16 v[156:159], v[76:79], v[20:23], v[156:159]\nds_read_b64_tr_b16 v[20:21], %[xa] offset:17952\ns_waitcnt lgkmcnt(9)\n"
        "v_mfma_f32_16x16x32_bf16 v[96:99], v[64:67], v[12:15], v[96:99]\nds_read_b64_tr_b16 v[22:23], %[xa] offset:18464\nv_mfma_f32_16x16x32_bf16 v[132:135], v[72:75], v[12:15], v[132:135]\n"
        "ds_read_b64_tr_b16 v[12:13], %[xa] offset:15840\ns_waitcnt lgkmcnt(9)\nv_mfma_f32_16x16x32_bf16 v[108:111], v[64:67], v[24:27], v[108:111]\nds_read_b64_tr_b16 v[14:15], %[xa] offset:16352\n"
        "v_mfma_f32_16x16x32_bf16 v[124:127], v[68:71], v[24:27], v[124:127]\nbuffer_load_dwordx4 v[56:59], %[wa], %[rs], %[so] offen offset:2048 sc1\n"
        "v_mfma_f32_16x16x32_bf16 v[144:147], v[72:75], v[24:27], v[144:147]\nbuffer_load_dwordx4 v[60:63], %[wa], %[rs], %[so] offen offset:3072 sc1\n"
        "v_mfma_f32_16x16x32_bf16 v[160:163], v[76:79], v[24:27], v[160:163]\nds_read_b64_tr_b16 v[24:25], %[xa] offset:19008\ns_waitcnt lgkmcnt(8)\n"
        "v_mfma_f32_16x16x32_bf16 v[112:115], v[64:67], v[28:31], v[112:115]\nds_read_b64_tr_b16 v[26:27], %[xa] offset:19520\nv_mfma_f32_16x16x32_bf16 v[128:131], v[68:71], v[28:31], v[128:131]\n"
        "v_mfma_f32_16x16x32_bf16 v[148:151], v[72:75], v[28:31], v[148:151]\nv_mfma_f32_16x16x32_bf16 v[164:167], v[76:79], v[28:31], v[164:167]\ns_waitcnt vmcnt(8) lgkmcnt(6)\n"
        "v_mfma_f32_16x16x32_bf16 v[100:103], v[80:83], v[16:19], v[100:103]\ns_addk_i32 %[so], 15360\nv_readfirstlane_b32 %[s0], %[pv]\n"
        "v_mfma_f32_16x16x32_bf16 v[116:119], v[84:87], v[16:19], v[116:119]\ns_cmp_ge_i32 %[s0], 10\nds_read_b64_tr_b16 v[28:29], %[xa] offset:20064\n"
        "v_mfma_f32_16x16x32_bf16 v[136:139], v[88:91], v[16:19], v[136:139]\ns_cbranch_scc0 .LA1s1_%=\n.LA1r1_%=:\nds_read_b64_tr_b16 v[30:31], %[xa] offset:20576\n"
        "v_mfma_f32_16x16x32_bf16 v[152:155], v[92:95], v[16:19], v[152:155]\nds_read_b64_tr_b16 v[16:17], %[xa] offset:22176\ns_waitcnt lgkmcnt(7)\n"
        "v_mfma_f32_16x16x32_bf16 v[104:107], v[80:83], v[20:23], v[104:107]\nds_read_b64_tr_b16 v[18:19], %[xa] offset:22688\nbuffer_load_dwordx4 v[64:67], %[wa], %[rs], %[so] offen offset:0 sc1\n"
        "v_mfma_f32_16x16x32_bf16 v[120:123], v[84:87], v[20:23], v[120:123]\nbuffer_load_dwordx4 v[68:71], %[wa], %[rs], %[so] offen offset:1024 sc1\n"
        "v_mfma_f32_16x16x32_bf16 v[140:143], v[88:91], v[20:23], v[140:143]\nv_mfma_f32_16x16x32_bf16 v[156:159], v[92:95], v[20:23], v[156:159]\nds_read_b64_tr_b16 v[20:21], %[xa] offset:23232\n"
        "s_waitcnt lgkmcnt(7)\nv_mfma_f32_16x16x32_bf16 v[96:99], v[80:83], v[12:15], v[96:99]\nds_read_b64_tr_b16 v[22:23], %[xa] offset:23744\n"
        "v_mfma_f32_16x16x32_bf16 v[132:135], v[88:91], v[12:15], v[132:135]\nds_read_b64_tr_b16 v[12:13], %[xa] offset:21120\ns_waitcnt lgkmcnt(7)\n"
        "v_mfma_f32_16x16x32_bf16 v[108:111], v[80:83], v[24:27], v[108:111]\nds_read_b64_tr_b16 v[14:15], %[xa] offset:21632\nv_mfma_f32_16x16x32_bf16 v[124:127], v[84:87], v[24:27], v[124:127]\n"
        "buffer_load_dwordx4 v[72:75], %[wa], %[rs], %[so] offen offset:2048 sc1\nv_mfma_f32_16x16x32_bf16 v[144:147], v[88:91], v[24:27], v[144:147]\n"
        "buffer_load_dwordx4 v[76:79], %[wa], %[rs], %[so] offen offset:3072 sc1\nv_mfma_f32_16x16x32_bf16 v[160:163], v[92:95], v[24:27], v[160:163]\nds_read_b64_tr_b16 v[24:25], %[xa] offset:24288\n"
        "s_waitcnt lgkmcnt(7)\nv_mfma_f32_16x16x32_bf16 v[112:115], v[80:83], v[28:31], v[112:115]\nds_read_b64_tr_b16 v[26:27], %[xa] offset:24800\n"
        "v_mfma_f32_16x16x32_bf16 v[128:131], v[84:87], v[28:31], v[128:131]\nv_add_u32 %[sy], 4, %[sy]\nv_mfma_f32_16x16x32_bf16 v[148:151], v[88:91], v[28:31], v[148:151]\nds_write_b32 %[pg], %[sy]\n"
        "v_mfma_f32_16x16x32_bf16 v[164:167], v[92:95], v[28:31], v[164:167]\ns_waitcnt vmcnt(8) lgkmcnt(7)\nv_mfma_f32_16x16x32_bf16 v[100:103], v[32:35], v[16:19], v[100:103]\n"
        "s_addk_i32 %[so], 15360\nds_read_b64_tr_b16 v[28:29], %[xa] offset:25344\nv_mfma_f32_16x16x32_bf16 v[116:119], v[36:39], v[16:19], v[116:119]\nds_read_b64_tr_b16 v[30:31], %[xa] offset:25856\n"
        "v_mfma_f32_16x16x32_bf16 v[136:139], v[40:43], v[16:19], v[136:139]\nds_read_b32 %[pv], %[sy] offset:4\nbuffer_load_dwordx4 v[80:83], %[wa], %[rs], %[so] offen offset:0 sc1\n"
        "v_mfma_f32_16x16x32_bf16 v[152:155], v[44:47], v[16:19], v[152:155]\nds_read_b64_tr_b16 v[16:17], %[xa] offset:27456\ns_waitcnt lgkmcnt(9)\n"
        "v_mfma_f32_16x16x32_bf16 v[104:107], v[32:35], v[20:23], v[104:107]\nds_read_b64_tr_b16 v[18:19], %[xa] offset:27968\nv_mfma_f32_16x16x32_bf16 v[120:123], v[36:39], v[20:23], v[120:123]\n"
        "buffer_load_dwordx4 v[84:87], %[wa], %[rs], %[so] offen offset:1024 sc1\nv_mfma_f32_16x16x32_bf16 v[140:143], v[40:43], v[20:23], v[140:143]\n"
        "v_mfma_f32_16x16x32_bf16 v[156:159], v[44:47], v[20:23], v[156:159]\nds_read_b64_tr_b16 v[20:21], %[xa] offset:28512\ns_waitcnt lgkmcnt(9)\n"
        "v_mfma_f32_16x16x32_bf16 v[96:99], v[32:35], v[12:15], v[96:99]\nds_read_b64_tr_b16 v[22:23], %[xa] offset:29024\nv_mfma_f32_16x16x32_bf16 v[132:135], v[40:43], v[12:15], v[132:135]\n"
        "ds_read_b64_tr_b16 v[12:13], %[xa] offset:26400\ns_waitcnt lgkmcnt(9)\nv_mfma_f32_16x16x32_bf16 v[108:111], v[32:35], v[24:27], v[108:111]\nds_read_b64_tr_b16 v[14:15], %[xa] offset:26912\n"
        "v_mfma_f32_16x16x32_bf16 v[124:127], v[36:39], v[24:27], v[124:127]\nbuffer_load_dwordx4 v[88:91], %[wa], %[rs], %[so] offen offset:2048 sc1\n"
        "v_mfma_f32_16x16x32_bf16 v[144:147], v[40:43], v[24:27], v[144:147]\nbuffer_load_dwordx4 v[92:95], %[wa], %[rs], %[so] offen offset:3072 sc1\n"
        "v_mfma_f32_16x16x32_bf16 v[160:163], v[44:47], v[24:27], v[160:163]\nds_read_b64_tr_b16 v[24:25], %[xa] offset:29568\ns_waitcnt lgkmcnt(8)\n"
        "v_mfma_f32_16x16x32_bf16 v[112:115], v[32:35], v[28:31], v[112:115]\nds_read_b64_tr_b16 v[26:27], %[xa] offset:30080\nv_mfma_f32_16x16x32_bf16 v[128:131], v[36:39], v[28:31], v[128:131]\n"
        "v_mfma_f32_16x16x32_bf16 v[148:151], v[40:43], v[28:31], v[148:151]\ns_cmp_lg_u32 %[so], 291840\nv_mfma_f32_16x16x32_bf16 v[164:167], v[44:47], v[28:31], v[164:167]\n"
        "v_add_u32 %[xa], 21120, %[xa]\ns_cbranch_scc1 .LA1l%=\ns_waitcnt vmcnt(8) lgkmcnt(6)\nv_mfma_f32_16x16x32_bf16 v[100:103], v[48:51], v[16:19], v[100:103]\nv_readfirstlane_b32 %[s0], %[pv]\n"
        "ds_read_b64_tr_b16 v[28:29], %[xa] offset:9504\nv_mfma_f32_16x16x32_bf16 v[116:119], v[52:55], v[16:19], v[116:119]\ns_cmp_ge_i32 %[s0], 10\nds_read_b64_tr_b16 v[30:31], %[xa] offset:10016\n"
        "v_mfma_f32_16x16x32_bf16 v[136:139], v[56:59], v[16:19], v[136:139]\ns_cbranch_scc0 .LA1s2_%=\n.LA1r2_%=:\nv_mfma_f32_16x16x32_bf16 v[152:155], v[60:63], v[16:19], v[152:155]\n"
        "ds_read_b64_tr_b16 v[16:17], %[xa] offset:11616\ns_waitcnt lgkmcnt(7)\nv_mfma_f32_16x16x32_bf16 v[104:107], v[48:51], v[20:23], v[104:107]\nds_read_b64_tr_b16 v[18:19], %[xa] offset:12128\n"
        "v_mfma_f32_16x16x32_bf16 v[120:123], v[52:55], v[20:23], v[120:123]\nv_mfma_f32_16x16x32_bf16 v[140:143], v[56:59], v[20:23], v[140:143]\n"
        "v_mfma_f32_16x16x32_bf16 v[156:159], v[60:63], v[20:23], v[156:159]\nds_read_b64_tr_b16 v[20:21], %[xa] offset:12672\ns_waitcnt lgkmcnt(7)\n"
        "v_mfma_f32_16x16x32_bf16 v[96:99], v[48:51], v[12:15], v[96:99]\nds_read_b64_tr_b16 v[22:23], %[xa] offset:13184\nv_mfma_f32_16x16x32_bf16 v[132:135], v[56:59], v[12:15], v[132:135]\n"
        "ds_read_b64_tr_b16 v[12:13], %[xa] offset:10560\ns_waitcnt lgkmcnt(7)\nv_mfma_f32_16x16x32_bf16 v[108:111], v[48:51], v[24:27], v[108:111]\nds_read_b64_tr_b16 v[14:15], %[xa] offset:11072\n"
        "v_mfma_f32_16x16x32_bf16 v[124:127], v[52:55], v[24:27], v[124:127]\nv_mfma_f32_16x16x32_bf16 v[144:147], v[56:59], v[24:27], v[144:147]\n"
        "v_mfma_f32_16x16x32_bf16 v[160:163], v[60:63], v[24:27], v[160:163]\nds_read_b64_tr_b16 v[24:25], %[xa] offset:13728\ns_waitcnt lgkmcnt(7)\n"
        "v_mfma_f32_16x16x32_bf16 v[112:115], v[48:51], v[28:31], v[112:115]\nds_read_b64_tr_b16 v[26:27], %[xa] offset:14240\nv_mfma_f32_16x16x32_bf16 v[128:131], v[52:55], v[28:31], v[128:131]\n"
        "v_add_u32 %[sy], 4, %[sy]\nv_mfma_f32_16x16x32_bf16 v[148:151], v[56:59], v[28:31], v[148:151]\nv_mfma_f32_16x16x32_bf16 v[164:167], v[60:63], v[28:31], v[164:167]\n"
        "s_waitcnt vmcnt(4) lgkmcnt(6)\nv_mfma_f32_16x16x32_bf16 v[100:103], v[64:67], v[16:19], v[100:103]\nds_read_b64_tr_b16 v[28:29], %[xa] offset:14784\n"
        "v_mfma_f32_16x16x32_bf16 v[116:119], v[68:71], v[16:19], v[116:119]\nds_read_b64_tr_b16 v[30:31], %[xa] offset:15296\nv_mfma_f32_16x16x32_bf16 v[136:139], v[72:75], v[16:19], v[136:139]\n"
        "ds_read_b32 %[pv], %[sy] offset:4\nv_mfma_f32_16x16x32_bf16 v[152:155], v[76:79], v[16:19], v[152:155]\nds_read_b64_tr_b16 v[16:17], %[xa] offset:16896\ns_waitcnt lgkmcnt(8)\n"
        "v_mfma_f32_16x16x32_bf16 v[104:107], v[64:67], v[20:23], v[104:107]\nds_read_b64_tr_b16 v[18:19], %[xa] offset:17408\nv_mfma_f32_16x16x32_bf16 v[120:123], v[68:71], v[20:23], v[120:123]\n"
        "v_mfma_f32_16x16x32_bf16 v[140:143], v[72:75], v[20:23], v[140:143]\nv_mfma_f32_16x16x32_bf16 v[156:159], v[76:79], v[20:23], v[156:159]\nds_read_b64_tr_b16 v[20:21], %[xa] offset:17952\n"
        "s_waitcnt lgkmcnt(8)\nv_mfma_f32_16x16x32_bf16 v[96:99], v[64:67], v[12:15], v[96:99]\nds_read_b64_tr_b16 v[22:23], %[xa] offset:18464\n"
        "v_mfma_f32_16x16x32_bf16 v[132:135], v[72:75], v[12:15], v[132:135]\nds_read_b64_tr_b16 v[12:13], %[xa] offset:15840\ns_waitcnt lgkmcnt(8)\n"
        "v_mfma_f32_16x16x32_bf16 v[108:111], v[64:67], v[24:27], v[108:111]\nds_read_b64_tr_b16 v[14:15], %[xa] offset:16352\nv_mfma_f32_16x16x32_bf16 v[124:127], v[68:71], v[24:27], v[124:127]\n"
        "v_mfma_f32_16x16x32_bf16 v[144:147], v[72:75], v[24:27], v[144:147]\nv_mfma_f32_16x16x32_bf16 v[160:163], v[76:79], v[24:27], v[160:163]\nds_read_b64_tr_b16 v[24:25], %[xa] offset:19008\n"
        "s_waitcnt lgkmcnt(8)\nv_mfma_f32_16x16x32_bf16 v[112:115], v[64:67], v[28:31], v[112:115]\nds_read_b64_tr_b16 v[26:27], %[xa] offset:19520\n"
        "v_mfma_f32_16x16x32_bf16 v[128:131], v[68:71], v[28:31], v[128:131]\nv_mfma_f32_16x16x32_bf16 v[148:151], v[72:75], v[28:31], v[148:151]\n"
        "v_mfma_f32_16x16x32_bf16 v[164:167], v[76:79], v[28:31], v[164:167]\ns_waitcnt vmcnt(0) lgkmcnt(6)\nv_mfma_f32_16x16x32_bf16 v[100:103], v[80:83], v[16:19], v[100:103]\n"
        "v_readfirstlane_b32 %[s0], %[pv]\nds_read_b64_tr_b16 v[28:29], %[xa] offset:20064\nv_mfma_f32_16x16x32_bf16 v[116:119], v[84:87], v[16:19], v[116:119]\n"
        "ds_read_b64_tr_b16 v[30:31], %[xa] offset:20576\nv_mfma_f32_16x16x32_bf16 v[136:139], v[88:91], v[16:19], v[136:139]\ns_cmp_ge_i32 %[s0], %[nst]\n"
        "v_mfma_f32_16x16x32_bf16 v[152:155], v[92:95], v[16:19], v[152:155]\ns_cbranch_scc0 .LA1t%=\n.LA1u%=:\ns_waitcnt lgkmcnt(6)\n"
        "v_mfma_f32_16x16x32_bf16 v[104:107], v[80:83], v[20:23], v[104:107]\nv_add_u32 %[sy], 4, %[sy]\nds_read_b128 v[16:19], %[mra] offset:256\n"
        "v_mfma_f32_16x16x32_bf16 v[120:123], v[84:87], v[20:23], v[120:123]\nv_mfma_f32_16x16x32_bf16 v[140:143], v[88:91], v[20:23], v[140:143]\n"
        "v_mfma_f32_16x16x32_bf16 v[156:159], v[92:95], v[20:23], v[156:159]\nds_read_b128 v[32:35], %[cwa] offset:0\ns_waitcnt lgkmcnt(6)\n"
        "v_mfma_f32_16x16x32_bf16 v[96:99], v[80:83], v[12:15], v[96:99]\nds_read_b128 v[36:39], %[cwa] offset:320\nds_read_b128 v[20:23], %[mra] offset:512\n"
        "v_mfma_f32_16x16x32_bf16 v[132:135], v[88:91], v[12:15], v[132:135]\nds_read_b128 v[40:43], %[cwa] offset:640\ns_waitcnt lgkmcnt(7)\n"
        "v_mfma_f32_16x16x32_bf16 v[108:111], v[80:83], v[24:27], v[108:111]\nds_read_b128 v[44:47], %[cwa] offset:512\nds_read_b128 v[12:15], %[mra] offset:0\n"
        "v_mfma_f32_16x16x32_bf16 v[124:127], v[84:87], v[24:27], v[124:127]\nv_add_u32 %[sy], 4, %[sy]\nv_mfma_f32_16x16x32_bf16 v[144:147], v[88:91], v[24:27], v[144:147]\n"
        "v_mfma_f32_16x16x32_bf16 v[160:163], v[92:95], v[24:27], v[160:163]\nds_read_b128 v[24:27], %[mra] offset:768\ns_waitcnt lgkmcnt(8)\n"
        "v_mfma_f32_16x16x32_bf16 v[112:115], v[80:83], v[28:31], v[112:115]\nv_mfma_f32_16x16x32_bf16 v[128:131], v[84:87], v[28:31], v[128:131]\n"
        "v_mfma_f32_16x16x32_bf16 v[148:151], v[88:91], v[28:31], v[148:151]\nv_mfma_f32_16x16x32_bf16 v[164:167], v[92:95], v[28:31], v[164:167]\nds_read_b128 v[28:31], %[mra] offset:1024\n"
        "s_setprio %[pfs]\ns_waitcnt lgkmcnt(1)\n.long 0xd3b04064, 0x1d922120\n.long 0xd3b04066, 0x1d9a2122\n.long 0xd3b04074, 0x1dd22124\n.long 0xd3b04076, 0x1dda2126\n.long 0xd3b14064, 0x18022564\n"
        ".long 0xd3b14066, 0x18022566\n.long 0xd3b04068, 0x1da22920\n.long 0xd3b0406a, 0x1daa2922\n.long 0xd3b14074, 0x18022574\n.long 0xd3b14076, 0x18022576\nv_cvt_pk_bf16_f32 v100, v100, v101\n"
        "v_cvt_pk_bf16_f32 v101, v102, v103\n.long 0xd3b04078, 0x1de22924\n.long 0xd3b0407a, 0x1dea2926\n.long 0xd3b14068, 0x18022d68\n.long 0xd3b1406a, 0x18022d6a\nv_cvt_pk_bf16_f32 v116, v116, v117\n"
        "v_cvt_pk_bf16_f32 v117, v118, v119\nds_write_b64 %[sta], v[100:101] offset:7936\n.long 0xd3b04060, 0x1d821920\n.long 0xd3b04062, 0x1d8a1922\n.long 0xd3b14078, 0x18022d78\n"
        ".long 0xd3b1407a, 0x18022d7a\nv_cvt_pk_bf16_f32 v104, v104, v105\nv_cvt_pk_bf16_f32 v105, v106, v107\nds_write_b64 %[sta], v[116:117] offset:8096\n.long 0xd3b0406c, 0x1db23120\n"
        ".long 0xd3b0406e, 0x1dba3122\n.long 0xd3b14060, 0x18021d60\n.long 0xd3b14062, 0x18021d62\nv_cvt_pk_bf16_f32 v120, v120, v121\nv_cvt_pk_bf16_f32 v121, v122, v123\n"
        "ds_write_b64 %[sta], v[104:105] offset:15872\n.long 0xd3b0407c, 0x1df23124\n.long 0xd3b0407e, 0x1dfa3126\n.long 0xd3b1406c, 0x1802356c\n.long 0xd3b1406e, 0x1802356e\n"
        "v_cvt_pk_bf16_f32 v96, v96, v97\nv_cvt_pk_bf16_f32 v97, v98, v99\nds_write_b64 %[sta], v[120:121] offset:16032\ns_waitcnt lgkmcnt(4)\n.long 0xd3b04070, 0x1dc23920\n"
        ".long 0xd3b04072, 0x1dca3922\n.long 0xd3b1407c, 0x1802357c\n.long 0xd3b1407e, 0x1802357e\nv_cvt_pk_bf16_f32 v108, v108, v109\nv_cvt_pk_bf16_f32 v109, v110, v111\n"
        "ds_write_b64 %[sta], v[96:97] offset:0\n.long 0xd3b04080, 0x1e023924\n.long 0xd3b04082, 0x1e0a3926\n.long 0xd3b14070, 0x18023d70\n.long 0xd3b14072, 0x18023d72\n"
        "v_cvt_pk_bf16_f32 v124, v124, v125\nv_cvt_pk_bf16_f32 v125, v126, v127\nds_write_b64 %[sta], v[108:109] offset:23808\n.long 0xd3b14080, 0x18023d80\n.long 0xd3b14082, 0x18023d82\n"
        "v_cvt_pk_bf16_f32 v112, v112, v113\nv_cvt_pk_bf16_f32 v113, v114, v115\nds_write_b64 %[sta], v[124:125] offset:23968\nv_cvt_pk_bf16_f32 v128, v128, v129\nv_cvt_pk_bf16_f32 v129, v130, v131\n"
        "ds_write_b64 %[sta], v[112:113] offset:31744\nds_write_b64 %[sta], v[128:129] offset:31904\nds_write_b32 %[pg], %[sy]\nv_add_u32 %[sy], 4, %[sy]\n.long 0xd3b04088, 0x1e222128\n"
        ".long 0xd3b0408a, 0x1e2a212a\n.long 0xd3b04098, 0x1e62212c\n.long 0xd3b0409a, 0x1e6a212e\n.long 0xd3b14088, 0x18022588\n.long 0xd3b1408a, 0x1802258a\n.long 0xd3b0408c, 0x1e322928\n"
        ".long 0xd3b0408e, 0x1e3a292a\n.long 0xd3b14098, 0x18022598\n.long 0xd3b1409a, 0x1802259a\nv_cvt_pk_bf16_f32 v136, v136, v137\nv_cvt_pk_bf16_f32 v137, v138, v139\n.long 0xd3b0409c, 0x1e72292c\n"
        ".long 0xd3b0409e, 0x1e7a292e\n.long 0xd3b1408c, 0x18022d8c\n.long 0xd3b1408e, 0x18022d8e\nv_cvt_pk_bf16_f32 v152, v152, v153\nv_cvt_pk_bf16_f32 v153, v154, v155\n"
        "ds_write_b64 %[sta], v[136:137] offset:8256\n.long 0xd3b04084, 0x1e121928\n.long 0xd3b04086, 0x1e1a192a\n.long 0xd3b1409c, 0x18022d9c\n.long 0xd3b1409e, 0x18022d9e\n"
        "v_cvt_pk_bf16_f32 v140, v140, v141\nv_cvt_pk_bf16_f32 v141, v142, v143\nds_write_b64 %[sta], v[152:153] offset:8192\n.long 0xd3b04090, 0x1e423128\n.long 0xd3b04092, 0x1e4a312a\n"
        ".long 0xd3b14084, 0x18021d84\n.long 0xd3b14086, 0x18021d86\nv_cvt_pk_bf16_f32 v156, v156, v157\nv_cvt_pk_bf16_f32 v157, v158, v159\nds_write_b64 %[sta], v[140:141] offset:16192\n"
        ".long 0xd3b040a0, 0x1e82312c\n.long 0xd3b040a2, 0x1e8a312e\n.long 0xd3b14090, 0x18023590\n.long 0xd3b14092, 0x18023592\nv_cvt_pk_bf16_f32 v132, v132, v133\nv_cvt_pk_bf16_f32 v133, v134, v135\n"
        "ds_write_b64 %[sta], v[156:157] offset:16128\n.long 0xd3b04094, 0x1e523928\n.long 0xd3b04096, 0x1e5a392a\n.long 0xd3b140a0, 0x180235a0\n.long 0xd3b140a2, 0x180235a2\n"
        "v_cvt_pk_bf16_f32 v144, v144, v145\nv_cvt_pk_bf16_f32 v145, v146, v147\nds_write_b64 %[sta], v[132:133] offset:320\n.long 0xd3b040a4, 0x1e92392c\n.long 0xd3b040a6, 0x1e9a392e\n"
        ".long 0xd3b14094, 0x18023d94\n.long 0xd3b14096, 0x18023d96\nv_cvt_pk_bf16_f32 v160, v160, v161\nv_cvt_pk_bf16_f32 v161, v162, v163\nds_write_b64 %[sta], v[144:145] offset:24128\n"
        ".long 0xd3b140a4, 0x18023da4\n.long 0xd3b140a6, 0x18023da6\nv_cvt_pk_bf16_f32 v148, v148, v149\nv_cvt_pk_bf16_f32 v149, v150, v151\nds_write_b64 %[sta], v[160:161] offset:24064\n"
        "v_cvt_pk_bf16_f32 v164, v164, v165\nv_cvt_pk_bf16_f32 v165, v166, v167\nds_write_b64 %[sta], v[148:149] offset:32064\nds_write_b64 %[sta], v[164:165] offset:32000\nds_write_b32 %[pg], %[sy]\n"
        "s_branch .LE%=\n.LA1s0_%=:\ns_mov_b32 %[s1], 0x100000\n.LA1p0_%=:\nds_read_b32 %[pv], %[sy] offset:4\ns_waitcnt lgkmcnt(0)\nv_readfirstlane_b32 %[s0], %[pv]\ns_nop 0\ns_cmp_ge_i32 %[s0], 10\n"
        "s_cbranch_scc1 .LA1r0_%=\ns_sleep 1\ns_sub_u32 %[s1], %[s1], 1\ns_cbranch_scc0 .LA1p0_%=\ns_branch .LA1r0_%=\n.LA1s1_%=:\ns_mov_b32 %[s1], 0x100000\n.LA1p1_%=:\n"
        "ds_read_b32 %[pv], %[sy] offset:4\ns_waitcnt lgkmcnt(0)\nv_readfirstlane_b32 %[s0], %[pv]\ns_nop 0\ns_cmp_ge_i32 %[s0], 10\ns_cbranch_scc1 .LA1r1_%=\ns_sleep 1\ns_sub_u32 %[s1], %[s1], 1\n"
        "s_cbranch_scc0 .LA1p1_%=\ns_branch .LA1r1_%=\n.LA1s2_%=:\ns_mov_b32 %[s1], 0x100000\n.LA1p2_%=:\nds_read_b32 %[pv], %[sy] offset:4\ns_waitcnt lgkmcnt(0)\nv_readfirstlane_b32 %[s0], %[pv]\n"
        "s_nop 0\ns_cmp_ge_i32 %[s0], 10\ns_cbranch_scc1 .LA1r2_%=\ns_sleep 1\ns_sub_u32 %[s1], %[s1], 1\ns_cbranch_scc0 .LA1p2_%=\ns_branch .LA1r2_%=\n.LA1t%=:\ns_mov_b32 %[s1], 0x100000\n.LA1q%=:\n"
        "ds_read_b32 %[pv], %[sy] offset:4\ns_waitcnt lgkmcnt(0)\nv_readfirstlane_b32 %[s0], %[pv]\ns_nop 0\ns_cmp_ge_i32 %[s0], %[nst]\ns_cbranch_scc1 .LA1u%=\ns_sleep 1\ns_sub_u32 %[s1], %[s1], 1\n"
        "s_cbranch_scc0 .LA1q%=\ns_branch .LA1u%=\n.LB%=:\ns_cmp_eq_u32 %[w], 2\ns_cbranch_scc0 .LC%=\ns_mov_b32 %[so], 0\nbuffer_load_dwordx4 v[32:35], %[wa], %[rs], %[so] offen offset:3072 sc1\n"
        "buffer_load_dwordx4 v[36:39], %[wa], %[rs], %[so] offen offset:0 sc1\nbuffer_load_dwordx4 v[40:43], %[wa], %[rs], %[so] offen offset:1024 sc1\n"
        "buffer_load_dwordx4 v[44:47], %[wa], %[rs], %[so] offen offset:2048 sc1\ns_barrier\ns_mov_b32 %[so], 15360\nbuffer_load_dwordx4 v[48:51], %[wa], %[rs], %[so] offen offset:3072 sc1\n"
        "buffer_load_dwordx4 v[52:55], %[wa], %[rs], %[so] offen offset:0 sc1\nbuffer_load_dwordx4 v[56:59], %[wa], %[rs], %[so] offen offset:1024 sc1\n"
        "buffer_load_dwordx4 v[60:63], %[wa], %[rs], %[so] offen offset:2048 sc1\ns_mov_b32 %[so], 30720\nbuffer_load_dwordx4 v[64:67], %[wa], %[rs], %[so] offen offset:3072 sc1\n"
        "buffer_load_dwordx4 v[68:71], %[wa], %[rs], %[so] offen offset:0 sc1\nbuffer_load_dwordx4 v[72:75], %[wa], %[rs], %[so] offen offset:1024 sc1\n"
        "buffer_load_dwordx4 v[76:79], %[wa], %[rs], %[so] offen offset:2048 sc1\ns_mov_b32 %[so], 46080\nbuffer_load_dwordx4 v[80:83], %[wa], %[rs], %[so] offen offset:3072 sc1\n"
        "buffer_load_dwordx4 v[84:87], %[wa], %[rs], %[so] offen offset:0 sc1\nbuffer_load_dwordx4 v[88:91], %[wa], %[rs], %[so] offen offset:1024 sc1\n"
        "buffer_load_dwordx4 v[92:95], %[wa], %[rs], %[so] offen offset:2048 sc1\ns_mov_b32 %[s1], 0x100000\n.LB1w%=:\nds_read_b32 %[pv], %[sy] offset:0\ns_waitcnt lgkmcnt(0)\n"
        "v_readfirstlane_b32 %[s0], %[pv]\ns_nop 0\ns_cmp_ge_i32 %[s0], 10\ns_cbranch_scc1 .LB1g%=\ns_sleep 1\ns_sub_u32 %[s1], %[s1], 1\ns_cbranch_scc0 .LB1w%=\n.LB1g%=:\n"
        "ds_read_b64_tr_b16 v[16:17], %[xa] offset:1056\nds_read_b64_tr_b16 v[18:19], %[xa] offset:1568\nds_read_b64_tr_b16 v[20:21], %[xa] offset:2112\nds_read_b64_tr_b16 v[22:23], %[xa] offset:2624\n"
        "ds_read_b64_tr_b16 v[12:13], %[xa] offset:0\nds_read_b64_tr_b16 v[14:15], %[xa] offset:512\nds_read_b64_tr_b16 v[24:25], %[xa] offset:3168\nds_read_b64_tr_b16 v[26:27], %[xa] offset:3680\n"
        "s_waitcnt vmcnt(12) lgkmcnt(6)\nv_mfma_f32_16x16x32_bf16 v[96:99], v[32:35], v[16:19], 0\nds_read_b64_tr_b16 v[28:29], %[xa] offset:4224\n"
        "v_mfma_f32_16x16x32_bf16 v[108:111], v[36:39], v[16:19], 0\nds_read_b64_tr_b16 v[30:31], %[xa] offset:4736\nv_mfma_f32_16x16x32_bf16 v[128:131], v[40:43], v[16:19], 0\n"
        "ds_read_b32 %[pv], %[sy] offset:4\nv_mfma_f32_16x16x32_bf16 v[148:151], v[44:47], v[16:19], 0\nds_read_b64_tr_b16 v[16:17], %[xa] offset:6336\ns_waitcnt lgkmcnt(8)\n"
        "v_mfma_f32_16x16x32_bf16 v[100:103], v[32:35], v[20:23], 0\nds_read_b64_tr_b16 v[18:19], %[xa] offset:6848\nv_mfma_f32_16x16x32_bf16 v[112:115], v[36:39], v[20:23], 0\n"
        "v_mfma_f32_16x16x32_bf16 v[132:135], v[40:43], v[20:23], 0\nv_mfma_f32_16x16x32_bf16 v[152:155], v[44:47], v[20:23], 0\nds_read_b64_tr_b16 v[20:21], %[xa] offset:7392\ns_waitcnt lgkmcnt(8)\n"
        "v_mfma_f32_16x16x32_bf16 v[104:107], v[36:39], v[12:15], 0\nds_read_b64_tr_b16 v[22:23], %[xa] offset:7904\nv_mfma_f32_16x16x32_bf16 v[124:127], v[40:43], v[12:15], 0\n"
        "v_mfma_f32_16x16x32_bf16 v[144:147], v[44:47], v[12:15], 0\nds_read_b64_tr_b16 v[12:13], %[xa] offset:5280\ns_waitcnt lgkmcnt(8)\nv_mfma_f32_16x16x32_bf16 v[116:119], v[36:39], v[24:27], 0\n"
        "ds_read_b64_tr_b16 v[14:15], %[xa] offset:5792\nv_mfma_f32_16x16x32_bf16 v[136:139], v[40:43], v[24:27], 0\nv_mfma_f32_16x16x32_bf16 v[156:159], v[44:47], v[24:27], 0\n"
        "ds_read_b64_tr_b16 v[24:25], %[xa] offset:8448\ns_waitcnt lgkmcnt(8)\nv_mfma_f32_16x16x32_bf16 v[120:123], v[36:39], v[28:31], 0\nds_read_b64_tr_b16 v[26:27], %[xa] offset:8960\n"
        "v_mfma_f32_16x16x32_bf16 v[140:143], v[40:43], v[28:31], 0\nv_mfma_f32_16x16x32_bf16 v[160:163], v[44:47], v[28:31], 0\n.LB1l%=:\ns_waitcnt vmcnt(8) lgkmcnt(6)\n"
        "v_mfma_f32_16x16x32_bf16 v[96:99], v[48:51], v[16:19], v[96:99]\ns_addk_i32 %[so], 15360\nv_readfirstlane_b32 %[s0], %[pv]\n"
        "v_mfma_f32_16x16x32_bf16 v[108:111], v[52:55], v[16:19], v[108:111]\ns_cmp_ge_i32 %[s0], 10\nds_read_b64_tr_b16 v[28:29], %[xa] offset:9504\n"
        "v_mfma_f32_16x16x32_bf16 v[128:131], v[56:59], v[16:19], v[128:131]\ns_cbranch_scc0 .LB1s0_%=\n.LB1r0_%=:\nds_read_b64_tr_b16 v[30:31], %[xa] offset:10016\n"
        "v_mfma_f32_16x16x32_bf16 v[148:151], v[60:63], v[16:19], v[148:151]\nds_read_b64_tr_b16 v[16:17], %[xa] offset:11616\ns_waitcnt lgkmcnt(7)\n"
        "v_mfma_f32_16x16x32_bf16 v[100:103], v[48:51], v[20:23], v[100:103]\nds_read_b64_tr_b16 v[18:19], %[xa] offset:12128\nbuffer_load_dwordx4 v[32:35], %[wa], %[rs], %[so] offen offset:3072 sc1\n"
        "v_mfma_f32_16x16x32_bf16 v[112:115], v[52:55], v[20:23], v[112:115]\nbuffer_load_dwordx4 v[36:39], %[wa], %[rs], %[so] offen offset:0 sc1\n"
        "v_mfma_f32_16x16x32_bf16 v[132:135], v[56:59], v[20:23], v[132:135]\nv_mfma_f32_16x16x32_bf16 v[152:155], v[60:63], v[20:23], v[152:155]\nds_read_b64_tr_b16 v[20:21], %[xa] offset:12672\n"
        "s_waitcnt lgkmcnt(7)\nv_mfma_f32_16x16x32_bf16 v[104:107], v[52:55], v[12:15], v[104:107]\nds_read_b64_tr_b16 v[22:23], %[xa] offset:13184\n"
        "v_mfma_f32_16x16x32_bf16 v[124:127], v[56:59], v[12:15], v[124:127]\nbuffer_load_dwordx4 v[40:43], %[wa], %[rs], %[so] offen offset:1024 sc1\n"
        "v_mfma_f32_16x16x32_bf16 v[144:147], v[60:63], v[12:15], v[144:147]\nds_read_b64_tr_b16 v[12:13], %[xa] offset:10560\ns_waitcnt lgkmcnt(7)\n"
        "v_mfma_f32_16x16x32_bf16 v[116:119], v[52:55], v[24:27], v[116:119]\nds_read_b64_tr_b16 v[14:15], %[xa] offset:11072\nv_mfma_f32_16x16x32_bf16 v[136:139], v[56:59], v[24:27], v[136:139]\n"
        "buffer_load_dwordx4 v[44:47], %[wa], %[rs], %[so] offen offset:2048 sc1\nv_mfma_f32_16x16x32_bf16 v[156:159], v[60:63], v[24:27], v[156:159]\nds_read_b64_tr_b16 v[24:25], %[xa] offset:13728\n"
        "s_waitcnt lgkmcnt(7)\nv_mfma_f32_16x16x32_bf16 v[120:123], v[52:55], v[28:31], v[120:123]\nv_add_u32 %[sy], 4, %[sy]\nds_read_b64_tr_b16 v[26:27], %[xa] offset:14240\n"
        "v_mfma_f32_16x16x32_bf16 v[140:143], v[56:59], v[28:31], v[140:143]\nds_write_b32 %[pg], %[sy]\nv_mfma_f32_16x16x32_bf16 v[160:163], v[60:63], v[28:31], v[160:163]\n"
        "s_waitcnt vmcnt(8) lgkmcnt(7)\nv_mfma_f32_16x16x32_bf16 v[96:99], v[64:67], v[16:19], v[96:99]\ns_addk_i32 %[so], 15360\nds_read_b64_tr_b16 v[28:29], %[xa] offset:14784\n"
        "v_mfma_f32_16x16x32_bf16 v[108:111], v[68:71], v[16:19], v[108:111]\nds_read_b64_tr_b16 v[30:31], %[xa] offset:15296\nv_mfma_f32_16x16x32_bf16 v[128:131], v[72:75], v[16:19], v[128:131]\n"
        "ds_read_b32 %[pv], %[sy] offset:4\nbuffer_load_dwordx4 v[48:51], %[wa], %[rs], %[so] offen offset:3072 sc1\nv_mfma_f32_16x16x32_bf16 v[148:151], v[76:79], v[16:19], v[148:151]\n"
        "ds_read_b64_tr_b16 v[16:17], %[xa] offset:16896\ns_waitcnt lgkmcnt(9)\nv_mfma_f32_16x16x32_bf16 v[100:103], v[64:67], v[20:23], v[100:103]\nds_read_b64_tr_b16 v[18:19], %[xa] offset:17408\n"
        "v_mfma_f32_16x16x32_bf16 v[112:115], v[68:71], v[20:23], v[112:115]\nbuffer_load_dwordx4 v[52:55], %[wa], %[rs], %[so] offen offset:0 sc1\n"
        "v_mfma_f32_16x16x32_bf16 v[132:135], v[72:75], v[20:23], v[132:135]\nv_mfma_f32_16x16x32_bf16 v[152:155], v[76:79], v[20:23], v[152:155]\nds_read_b64_tr_b16 v[20:21], %[xa] offset:17952\n"
        "s_waitcnt lgkmcnt(9)\nv_mfma_f32_16x16x32_bf16 v[104:107], v[68:71], v[12:15], v[104:107]\nds_read_b64_tr_b16 v[22:23], %[xa] offset:18464\n"
        "v_mfma_f32_16x16x32_bf16 v[124:127], v[72:75], v[12:15], v[124:127]\nbuffer_load_dwordx4 v[56:59], %[wa], %[rs], %[so] offen offset:1024 sc1\n"
        "v_mfma_f32_16x16x32_bf16 v[144:147], v[76:79], v[12:15], v[144:147]\nds_read_b64_tr_b16 v[12:13], %[xa] offset:15840\ns_waitcnt lgkmcnt(9)\n"
        "v_mfma_f32_16x16x32_bf16 v[116:119], v[68:71], v[24:27], v[116:119]\nds_read_b64_tr_b16 v[14:15], %[xa] offset:16352\nv_mfma_f32_16x16x32_bf16 v[136:139], v[72:75], v[24:27], v[136:139]\n"
        "buffer_load_dwordx4 v[60:63], %[wa], %[rs], %[so] offen offset:2048 sc1\nv_mfma_f32_16x16x32_bf16 v[156:159], v[76:79], v[24:27], v[156:159]\nds_read_b64_tr_b16 v[24:25], %[xa] offset:19008\n"
        "s_waitcnt lgkmcnt(8)\nv_mfma_f32_16x16x32_bf16 v[120:123], v[68:71], v[28:31], v[120:123]\nds_read_b64_tr_b16 v[26:27], %[xa] offset:19520\n"
        "v_mfma_f32_16x16x32_bf16 v[140:143], v[72:75], v[28:31], v[140:143]\nv_mfma_f32_16x16x32_bf16 v[160:163], v[76:79], v[28:31], v[160:163]\ns_waitcnt vmcnt(8) lgkmcnt(6)\n"
        "v_mfma_f32_16x16x32_bf16 v[96:99], v[80:83], v[16:19], v[96:99]\ns_addk_i32 %[so], 15360\nv_readfirstlane_b32 %[s0], %[pv]\n"
        "v_mfma_f32_16x16x32_bf16 v[108:111], v[84:87], v[16:19], v[108:111]\ns_cmp_ge_i32 %[s0], 10\nds_read_b64_tr_b16 v[28:29], %[xa] offset:20064\n"
        "v_mfma_f32_16x16x32_bf16 v[128:131], v[88:91], v[16:19], v[128:131]\ns_cbranch_scc0 .LB1s1_%=\n.LB1r1_%=:\nds_read_b64_tr_b16 v[30:31], %[xa] offset:20576\n"
        "v_mfma_f32_16x16x32_bf16 v[148:151], v[92:95], v[16:19], v[148:151]\nds_read_b64_tr_b16 v[16:17], %[xa] offset:22176\ns_waitcnt lgkmcnt(7)\n"
        "v_mfma_f32_16x16x32_bf16 v[100:103], v[80:83], v[20:23], v[100:103]\nds_read_b64_tr_b16 v[18:19], %[xa] offset:22688\nbuffer_load_dwordx4 v[64:67], %[wa], %[rs], %[so] offen offset:3072 sc1\n"
        "v_mfma_f32_16x16x32_bf16 v[112:115], v[84:87], v[20:23], v[112:115]\nbuffer_load_dwordx4 v[68:71], %[wa], %[rs], %[so] offen offset:0 sc1\n"
        "v_mfma_f32_16x16x32_bf16 v[132:135], v[88:91], v[20:23], v[132:135]\nv_mfma_f32_16x16x32_bf16 v[152:155], v[92:95], v[20:23], v[152:155]\nds_read_b64_tr_b16 v[20:21], %[xa] offset:23232\n"
        "s_waitcnt lgkmcnt(7)\nv_mfma_f32_16x16x32_bf16 v[104:107], v[84:87], v[12:15], v[104:107]\nds_read_b64_tr_b16 v[22:23], %[xa] offset:23744\n"
        "v_mfma_f32_16x16x32_bf16 v[124:127], v[88:91], v[12:15], v[124:127]\nbuffer_load_dwordx4 v[72:75], %[wa], %[rs], %[so] offen offset:1024 sc1\n"
        "v_mfma_f32_16x16x32_bf16 v[144:147], v[92:95], v[12:15], v[144:147]\nds_read_b64_tr_b16 v[12:13], %[xa] offset:21120\ns_waitcnt lgkmcnt(7)\n"
        "v_mfma_f32_16x16x32_bf16 v[116:119], v[84:87], v[24:27], v[116:119]\nds_read_b64_tr_b16 v[14:15], %[xa] offset:21632\nv_mfma_f32_16x16x32_bf16 v[136:139], v[88:91], v[24:27], v[136:139]\n"
        "buffer_load_dwordx4 v[76:79], %[wa], %[rs], %[so] offen offset:2048 sc1\nv_mfma_f32_16x16x32_bf16 v[156:159], v[92:95], v[24:27], v[156:159]\nds_read_b64_tr_b16 v[24:25], %[xa] offset:24288\n"
        "s_waitcnt lgkmcnt(7)\nv_mfma_f32_16x16x32_bf16 v[120:123], v[84:87], v[28:31], v[120:123]\nv_add_u32 %[sy], 4, %[sy]\nds_read_b64_tr_b16 v[26:27], %[xa] offset:24800\n"
        "v_mfma_f32_16x16x32_bf16 v[140:143], v[88:91], v[28:31], v[140:143]\nds_write_b32 %[pg], %[sy]\nv_mfma_f32_16x16x32_bf16 v[160:163], v[92:95], v[28:31], v[160:163]\n"
        "s_waitcnt vmcnt(8) lgkmcnt(7)\nv_mfma_f32_16x16x32_bf16 v[96:99], v[32:35], v[16:19], v[96:99]\ns_addk_i32 %[so], 15360\nds_read_b64_tr_b16 v[28:29], %[xa] offset:25344\n"
        "v_mfma_f32_16x16x32_bf16 v[108:111], v[36:39], v[16:19], v[108:111]\nds_read_b64_tr_b16 v[30:31], %[xa] offset:25856\nv_mfma_f32_16x16x32_bf16 v[128:131], v[40:43], v[16:19], v[128:131]\n"
        "ds_read_b32 %[pv], %[sy] offset:4\nbuffer_load_dwordx4 v[80:83], %[wa], %[rs], %[so] offen offset:3072 sc1\nv_mfma_f32_16x16x32_bf16 v[148:151], v[44:47], v[16:19], v[148:151]\n"
        "ds_read_b64_tr_b16 v[16:17], %[xa] offset:27456\ns_waitcnt lgkmcnt(9)\nv_mfma_f32_16x16x32_bf16 v[100:103], v[32:35], v[20:23], v[100:103]\nds_read_b64_tr_b16 v[18:19], %[xa] offset:27968\n"
        "v_mfma_f32_16x16x32_bf16 v[112:115], v[36:39], v[20:23], v[112:115]\nbuffer_load_dwordx4 v[84:87], %[wa], %[rs], %[so] offen offset:0 sc1\n"
        "v_mfma_f32_16x16x32_bf16 v[132:135], v[40:43], v[20:23], v[132:135]\nv_mfma_f32_16x16x32_bf16 v[152:155], v[44:47], v[20:23], v[152:155]\nds_read_b64_tr_b16 v[20:21], %[xa] offset:28512\n"
        "s_waitcnt lgkmcnt(9)\nv_mfma_f32_16x16x32_bf16 v[104:107], v[36:39], v[12:15], v[104:107]\nds_read_b64_tr_b16 v[22:23], %[xa] offset:29024\n"
        "v_mfma_f32_16x16x32_bf16 v[124:127], v[40:43], v[12:15], v[124:127]\nbuffer_load_dwordx4 v[88:91], %[wa], %[rs], %[so] offen offset:1024 sc1\n"
        "v_mfma_f32_16x16x32_bf16 v[144:147], v[44:47], v[12:15], v[144:147]\nds_read_b64_tr_b16 v[12:13], %[xa] offset:26400\ns_waitcnt lgkmcnt(9)\n"
        "v_mfma_f32_16x16x32_bf16 v[116:119], v[36:39], v[24:27], v[116:119]\nds_read_b64_tr_b16 v[14:15], %[xa] offset:26912\nv_mfma_f32_16x16x32_bf16 v[136:139], v[40:43], v[24:27], v[136:139]\n"
        "buffer_load_dwordx4 v[92:95], %[wa], %[rs], %[so] offen offset:2048 sc1\nv_mfma_f32_16x16x32_bf16 v[156:159], v[44:47], v[24:27], v[156:159]\nds_read_b64_tr_b16 v[24:25], %[xa] offset:29568\n"
        "s_waitcnt lgkmcnt(8)\nv_mfma_f32_16x16x32_bf16 v[120:123], v[36:39], v[28:31], v[120:123]\nds_read_b64_tr_b16 v[26:27], %[xa] offset:30080\n"
        "v_mfma_f32_16x16x32_bf16 v[140:143], v[40:43], v[28:31], v[140:143]\ns_cmp_lg_u32 %[so], 291840\nv_mfma_f32_16x16x32_bf16 v[160:163], v[44:47], v[28:31], v[160:163]\n"
        "v_add_u32 %[xa], 21120, %[xa]\ns_cbranch_scc1 .LB1l%=\ns_waitcnt vmcnt(8) lgkmcnt(6)\nv_mfma_f32_16x16x32_bf16 v[96:99], v[48:51], v[16:19], v[96:99]\nv_readfirstlane_b32 %[s0], %[pv]\n"
        "ds_read_b64_tr_b16 v[28:29], %[xa] offset:9504\nv_mfma_f32_16x16x32_bf16 v[108:111], v[52:55], v[16:19], v[108:111]\ns_cmp_ge_i32 %[s0], 10\nds_read_b64_tr_b16 v[30:31], %[xa] offset:10016\n"
        "v_mfma_f32_16x16x32_bf16 v[128:131], v[56:59], v[16:19], v[128:131]\ns_cbranch_scc0 .LB1s2_%=\n.LB1r2_%=:\nv_mfma_f32_16x16x32_bf16 v[148:151], v[60:63], v[16:19], v[148:151]\n"
        "ds_read_b64_tr_b16 v[16:17], %[xa] offset:11616\ns_waitcnt lgkmcnt(7)\nv_mfma_f32_16x16x32_bf16 v[100:103], v[48:51], v[20:23], v[100:103]\nds_read_b64_tr_b16 v[18:19], %[xa] offset:12128\n"
        "v_mfma_f32_16x16x32_bf16 v[112:115], v[52:55], v[20:23], v[112:115]\nv_mfma_f32_16x16x32_bf16 v[132:135], v[56:59], v[20:23], v[132:135]\n"
        "v_mfma_f32_16x16x32_bf16 v[152:155], v[60:63], v[20:23], v[152:155]\nds_read_b64_tr_b16 v[20:21], %[xa] offset:12672\ns_waitcnt lgkmcnt(7)\n"
        "v_mfma_f32_16x16x32_bf16 v[104:107], v[52:55], v[12:15], v[104:107]\nds_read_b64_tr_b16 v[22:23], %[xa] offset:13184\nv_mfma_f32_16x16x32_bf16 v[124:127], v[56:59], v[12:15], v[124:127]\n"
        "v_mfma_f32_16x16x32_bf16 v[144:147], v[60:63], v[12:15], v[144:147]\nds_read_b64_tr_b16 v[12:13], %[xa] offset:10560\ns_waitcnt lgkmcnt(7)\n"
        "v_mfma_f32_16x16x32_bf16 v[116:119], v[52:55], v[24:27], v[116:119]\nds_read_b64_tr_b16 v[14:15], %[xa] offset:11072\nv_mfma_f32_16x16x32_bf16 v[136:139], v[56:59], v[24:27], v[136:139]\n"
        "v_mfma_f32_16x16x32_bf16 v[156:159], v[60:63], v[24:27], v[156:159]\nds_read_b64_tr_b16 v[24:25], %[xa] offset:13728\ns_waitcnt lgkmcnt(7)\n"
        "v_mfma_f32_16x16x32_bf16 v[120:123], v[52:55], v[28:31], v[120:123]\nv_add_u32 %[sy], 4, %[sy]\nds_read_b64_tr_b16 v[26:27], %[xa] offset:14240\n"
        "v_mfma_f32_16x16x32_bf16 v[140:143], v[56:59], v[28:31], v[140:143]\nv_mfma_f32_16x16x32_bf16 v[160:163], v[60:63], v[28:31], v[160:163]\ns_waitcnt vmcnt(4) lgkmcnt(6)\n"
        "v_mfma_f32_16x16x32_bf16 v[96:99], v[64:67], v[16:19], v[96:99]\nds_read_b64_tr_b16 v[28:29], %[xa] offset:14784\nv_mfma_f32_16x16x32_bf16 v[108:111], v[68:71], v[16:19], v[108:111]\n"
        "ds_read_b64_tr_b16 v[30:31], %[xa] offset:15296\nv_mfma_f32_16x16x32_bf16 v[128:131], v[72:75], v[16:19], v[128:131]\nds_read_b32 %[pv], %[sy] offset:4\n"
        "v_mfma_f32_16x16x32_bf16 v[148:151], v[76:79], v[16:19], v[148:151]\nds_read_b64_tr_b16 v[16:17], %[xa] offset:16896\ns_waitcnt lgkmcnt(8)\n"
        "v_mfma_f32_16x16x32_bf16 v[100:103], v[64:67], v[20:23], v[100:103]\nds_read_b64_tr_b16 v[18:19], %[xa] offset:17408\nv_mfma_f32_16x16x32_bf16 v[112:115], v[68:71], v[20:23], v[112:115]\n"
        "v_mfma_f32_16x16x32_bf16 v[132:135], v[72:75], v[20:23], v[132:135]\nv_mfma_f32_16x16x32_bf16 v[152:155], v[76:79], v[20:23], v[152:155]\nds_read_b64_tr_b16 v[20:21], %[xa] offset:17952\n"
        "s_waitcnt lgkmcnt(8)\nv_mfma_f32_16x16x32_bf16 v[104:107], v[68:71], v[12:15], v[104:107]\nds_read_b64_tr_b16 v[22:23], %[xa] offset:18464\n"
        "v_mfma_f32_16x16x32_bf16 v[124:127], v[72:75], v[12:15], v[124:127]\nv_mfma_f32_16x16x32_bf16 v[144:147], v[76:79], v[12:15], v[144:147]\nds_read_b64_tr_b16 v[12:13], %[xa] offset:15840\n"
        "s_waitcnt lgkmcnt(8)\nv_mfma_f32_16x16x32_bf16 v[116:119], v[68:71], v[24:27], v[116:119]\nds_read_b64_tr_b16 v[14:15], %[xa] offset:16352\n"
        "v_mfma_f32_16x16x32_bf16 v[136:139], v[72:75], v[24:27], v[136:139]\nv_mfma_f32_16x16x32_bf16 v[156:159], v[76:79], v[24:27], v[156:159]\nds_read_b64_tr_b16 v[24:25], %[xa] offset:19008\n"
        "s_waitcnt lgkmcnt(8)\nv_mfma_f32_16x16x32_bf16 v[120:123], v[68:71], v[28:31], v[120:123]\nds_read_b64_tr_b16 v[26:27], %[xa] offset:19520\n"
        "v_mfma_f32_16x16x32_bf16 v[140:143], v[72:75], v[28:31], v[140:143]\nv_mfma_f32_16x16x32_bf16 v[160:163], v[76:79], v[28:31], v[160:163]\ns_waitcnt vmcnt(0) lgkmcnt(6)\n"
        "v_mfma_f32_16x16x32_bf16 v[96:99], v[80:83], v[16:19], v[96:99]\nv_readfirstlane_b32 %[s0], %[pv]\nds_read_b64_tr_b16 v[28:29], %[xa] offset:20064\n"
        "v_mfma_f32_16x16x32_bf16 v[108:111], v[84:87], v[16:19], v[108:111]\nds_read_b64_tr_b16 v[30:31], %[xa] offset:20576\nv_mfma_f32_16x16x32_bf16 v[128:131], v[88:91], v[16:19], v[128:131]\n"
        "s_cmp_ge_i32 %[s0], %[nst]\nv_mfma_f32_16x16x32_bf16 v[148:151], v[92:95], v[16:19], v[148:151]\ns_cbranch_scc0 .LB1t%=\n.LB1u%=:\ns_waitcnt lgkmcnt(6)\n"
        "v_mfma_f32_16x16x32_bf16 v[100:103], v[80:83], v[20:23], v[100:103]\nv_add_u32 %[sy], 4, %[sy]\nds_read_b128 v[16:19], %[mra] offset:256\n"
        "v_mfma_f32_16x16x32_bf16 v[112:115], v[84:87], v[20:23], v[112:115]\nv_mfma_f32_16x16x32_bf16 v[132:135], v[88:91], v[20:23], v[132:135]\n"
        "v_mfma_f32_16x16x32_bf16 v[152:155], v[92:95], v[20:23], v[152:155]\nds_read_b128 v[32:35], %[cwa] offset:448\ns_waitcnt lgkmcnt(6)\n"
        "v_mfma_f32_16x16x32_bf16 v[104:107], v[84:87], v[12:15], v[104:107]\nds_read_b128 v[36:39], %[cwa] offset:128\nds_read_b128 v[20:23], %[mra] offset:512\n"
        "v_mfma_f32_16x16x32_bf16 v[124:127], v[88:91], v[12:15], v[124:127]\nds_read_b128 v[40:43], %[cwa] offset:192\nv_mfma_f32_16x16x32_bf16 v[144:147], v[92:95], v[12:15], v[144:147]\n"
        "ds_read_b128 v[44:47], %[cwa] offset:768\ns_waitcnt lgkmcnt(8)\nv_mfma_f32_16x16x32_bf16 v[116:119], v[84:87], v[24:27], v[116:119]\nv_add_u32 %[sy], 4, %[sy]\n"
        "ds_read_b128 v[12:15], %[mra] offset:0\nv_mfma_f32_16x16x32_bf16 v[136:139], v[88:91], v[24:27], v[136:139]\nv_mfma_f32_16x16x32_bf16 v[156:159], v[92:95], v[24:27], v[156:159]\n"
        "ds_read_b128 v[24:27], %[mra] offset:768\ns_waitcnt lgkmcnt(8)\nv_mfma_f32_16x16x32_bf16 v[120:123], v[84:87], v[28:31], v[120:123]\n"
        "v_mfma_f32_16x16x32_bf16 v[140:143], v[88:91], v[28:31], v[140:143]\nv_mfma_f32_16x16x32_bf16 v[160:163], v[92:95], v[28:31], v[160:163]\nds_read_b128 v[28:31], %[mra] offset:1024\n"
        "s_setprio %[pfs]\ns_waitcnt lgkmcnt(1)\n.long 0xd3b04060, 0x1d822120\n.long 0xd3b04062, 0x1d8a2122\n.long 0xd3b0406c, 0x1db22124\n.long 0xd3b0406e, 0x1dba2126\n.long 0xd3b14060, 0x18022560\n"
        ".long 0xd3b14062, 0x18022562\n.long 0xd3b04080, 0x1e022128\n.long 0xd3b04082, 0x1e0a212a\n.long 0xd3b1406c, 0x1802256c\n.long 0xd3b1406e, 0x1802256e\nv_cvt_pk_bf16_f32 v96, v96, v97\n"
        "v_cvt_pk_bf16_f32 v97, v98, v99\n.long 0xd3b04064, 0x1d922920\n.long 0xd3b04066, 0x1d9a2922\n.long 0xd3b14080, 0x18022580\n.long 0xd3b14082, 0x18022582\nv_cvt_pk_bf16_f32 v108, v108, v109\n"
        "v_cvt_pk_bf16_f32 v109, v110, v111\nds_write_b64 %[sta], v[96:97] offset:8160\n.long 0xd3b04070, 0x1dc22924\n.long 0xd3b04072, 0x1dca2926\n.long 0xd3b14064, 0x18022d64\n"
        ".long 0xd3b14066, 0x18022d66\nv_cvt_pk_bf16_f32 v128, v128, v129\nv_cvt_pk_bf16_f32 v129, v130, v131\nds_write_b64 %[sta], v[108:109] offset:8000\n.long 0xd3b04084, 0x1e122928\n"
        ".long 0xd3b04086, 0x1e1a292a\n.long 0xd3b14070, 0x18022d70\n.long 0xd3b14072, 0x18022d72\nv_cvt_pk_bf16_f32 v100, v100, v101\nv_cvt_pk_bf16_f32 v101, v102, v103\n"
        "ds_write_b64 %[sta], v[128:129] offset:8032\n.long 0xd3b04068, 0x1da21924\n.long 0xd3b0406a, 0x1daa1926\n.long 0xd3b14084, 0x18022d84\n.long 0xd3b14086, 0x18022d86\n"
        "v_cvt_pk_bf16_f32 v112, v112, v113\nv_cvt_pk_bf16_f32 v113, v114, v115\nds_write_b64 %[sta], v[100:101] offset:16096\n.long 0xd3b0407c, 0x1df21928\n.long 0xd3b0407e, 0x1dfa192a\n"
        ".long 0xd3b14068, 0x18021d68\n.long 0xd3b1406a, 0x18021d6a\nv_cvt_pk_bf16_f32 v132, v132, v133\nv_cvt_pk_bf16_f32 v133, v134, v135\nds_write_b64 %[sta], v[112:113] offset:15936\n"
        ".long 0xd3b04074, 0x1dd23124\n.long 0xd3b04076, 0x1dda3126\n.long 0xd3b1407c, 0x18021d7c\n.long 0xd3b1407e, 0x18021d7e\nv_cvt_pk_bf16_f32 v104, v104, v105\nv_cvt_pk_bf16_f32 v105, v106, v107\n"
        "ds_write_b64 %[sta], v[132:133] offset:15968\n.long 0xd3b04088, 0x1e223128\n.long 0xd3b0408a, 0x1e2a312a\n.long 0xd3b14074, 0x18023574\n.long 0xd3b14076, 0x18023576\n"
        "v_cvt_pk_bf16_f32 v124, v124, v125\nv_cvt_pk_bf16_f32 v125, v126, v127\nds_write_b64 %[sta], v[104:105] offset:64\ns_waitcnt lgkmcnt(7)\n.long 0xd3b04078, 0x1de23924\n"
        ".long 0xd3b0407a, 0x1dea3926\n.long 0xd3b14088, 0x18023588\n.long 0xd3b1408a, 0x1802358a\nv_cvt_pk_bf16_f32 v116, v116, v117\nv_cvt_pk_bf16_f32 v117, v118, v119\n"
        "ds_write_b64 %[sta], v[124:125] offset:96\n.long 0xd3b0408c, 0x1e323928\n.long 0xd3b0408e, 0x1e3a392a\n.long 0xd3b14078, 0x18023d78\n.long 0xd3b1407a, 0x18023d7a\n"
        "v_cvt_pk_bf16_f32 v136, v136, v137\nv_cvt_pk_bf16_f32 v137, v138, v139\nds_write_b64 %[sta], v[116:117] offset:23872\n.long 0xd3b1408c, 0x18023d8c\n.long 0xd3b1408e, 0x18023d8e\n"
        "v_cvt_pk_bf16_f32 v120, v120, v121\nv_cvt_pk_bf16_f32 v121, v122, v123\nds_write_b64 %[sta], v[136:137] offset:23904\nv_cvt_pk_bf16_f32 v140, v140, v141\nv_cvt_pk_bf16_f32 v141, v142, v143\n"
        "ds_write_b64 %[sta], v[120:121] offset:31808\nds_write_b64 %[sta], v[140:141] offset:31840\nds_write_b32 %[pg], %[sy]\nv_add_u32 %[sy], 4, %[sy]\n.long 0xd3b04094, 0x1e52212c\n"
        ".long 0xd3b04096, 0x1e5a212e\n.long 0xd3b04098, 0x1e62292c\n.long 0xd3b0409a, 0x1e6a292e\n.long 0xd3b14094, 0x18022594\n.long 0xd3b14096, 0x18022596\n.long 0xd3b04090, 0x1e42192c\n"
        ".long 0xd3b04092, 0x1e4a192e\n.long 0xd3b14098, 0x18022d98\n.long 0xd3b1409a, 0x18022d9a\nv_cvt_pk_bf16_f32 v148, v148, v149\nv_cvt_pk_bf16_f32 v149, v150, v151\n.long 0xd3b0409c, 0x1e72312c\n"
        ".long 0xd3b0409e, 0x1e7a312e\n.long 0xd3b14090, 0x18021d90\n.long 0xd3b14092, 0x18021d92\nv_cvt_pk_bf16_f32 v152, v152, v153\nv_cvt_pk_bf16_f32 v153, v154, v155\n"
        "ds_write_b64 %[sta], v[148:149] offset:8320\n.long 0xd3b040a0, 0x1e82392c\n.long 0xd3b040a2, 0x1e8a392e\n.long 0xd3b1409c, 0x1802359c\n.long 0xd3b1409e, 0x1802359e\n"
        "v_cvt_pk_bf16_f32 v144, v144, v145\nv_cvt_pk_bf16_f32 v145, v146, v147\nds_write_b64 %[sta], v[152:153] offset:16256\n.long 0xd3b140a0, 0x18023da0\n.long 0xd3b140a2, 0x18023da2\n"
        "v_cvt_pk_bf16_f32 v156, v156, v157\nv_cvt_pk_bf16_f32 v157, v158, v159\nds_write_b64 %[sta], v[144:145] offset:384\nv_cvt_pk_bf16_f32 v160, v160, v161\nv_cvt_pk_bf16_f32 v161, v162, v163\n"
        "ds_write_b64 %[sta], v[156:157] offset:24192\nds_write_b64 %[sta], v[160:161] offset:32128\nds_write_b32 %[pg], %[sy]\ns_branch .LE%=\n.LB1s0_%=:\ns_mov_b32 %[s1], 0x100000\n.LB1p0_%=:\n"
        "ds_read_b32 %[pv], %[sy] offset:4\ns_waitcnt lgkmcnt(0)\nv_readfirstlane_b32 %[s0], %[pv]\ns_nop 0\ns_cmp_ge_i32 %[s0], 10\ns_cbranch_scc1 .LB1r0_%=\ns_sleep 1\ns_sub_u32 %[s1], %[s1], 1\n"
        "s_cbranch_scc0 .LB1p0_%=\ns_branch .LB1r0_%=\n.LB1s1_%=:\ns_mov_b32 %[s1], 0x100000\n.LB1p1_%=:\nds_read_b32 %[pv], %[sy] offset:4\ns_waitcnt lgkmcnt(0)\nv_readfirstlane_b32 %[s0], %[pv]\n"
        "s_nop 0\ns_cmp_ge_i32 %[s0], 10\ns_cbranch_scc1 .LB1r1_%=\ns_sleep 1\ns_sub_u32 %[s1], %[s1], 1\ns_cbranch_scc0 .LB1p1_%=\ns_branch .LB1r1_%=\n.LB1s2_%=:\ns_mov_b32 %[s1], 0x100000\n"
        ".LB1p2_%=:\nds_read_b32 %[pv], %[sy] offset:4\ns_waitcnt lgkmcnt(0)\nv_readfirstlane_b32 %[s0], %[pv]\ns_nop 0\ns_cmp_ge_i32 %[s0], 10\ns_cbranch_scc1 .LB1r2_%=\ns_sleep 1\n"
        "s_sub_u32 %[s1], %[s1], 1\ns_cbranch_scc0 .LB1p2_%=\ns_branch .LB1r2_%=\n.LB1t%=:\ns_mov_b32 %[s1], 0x100000\n.LB1q%=:\nds_read_b32 %[pv], %[sy] offset:4\ns_waitcnt lgkmcnt(0)\n"
        "v_readfirstlane_b32 %[s0], %[pv]\ns_nop 0\ns_cmp_ge_i32 %[s0], %[nst]\ns_cbranch_scc1 .LB1u%=\ns_sleep 1\ns_sub_u32 %[s1], %[s1], 1\ns_cbranch_scc0 .LB1q%=\ns_branch .LB1u%=\n.LC%=:\n"
        "s_mov_b32 %[so], 0\nbuffer_load_dwordx4 v[32:35], %[wa], %[rs], %[so] offen offset:0 sc1\nbuffer_load_dwordx4 v[36:39], %[wa], %[rs], %[so] offen offset:1024 sc1\n"
        "buffer_load_dwordx4 v[40:43], %[wa], %[rs], %[so] offen offset:2048 sc1\nbuffer_load_dwordx4 v[44:47], %[wa], %[rs], %[so] offen offset:3072 sc1\ns_barrier\ns_mov_b32 %[so], 15360\n"
        "buffer_load_dwordx4 v[48:51], %[wa], %[rs], %[so] offen offset:0 sc1\nbuffer_load_dwordx4 v[52:55], %[wa], %[rs], %[so] offen offset:1024 sc1\n"
        "buffer_load_dwordx4 v[56:59], %[wa], %[rs], %[so] offen offset:2048 sc1\nbuffer_load_dwordx4 v[60:63], %[wa], %[rs], %[so] offen offset:3072 sc1\ns_mov_b32 %[so], 30720\n"
        "buffer_load_dwordx4 v[64:67], %[wa], %[rs], %[so] offen offset:0 sc1\nbuffer_load_dwordx4 v[68:71], %[wa], %[rs], %[so] offen offset:1024 sc1\n"
        "buffer_load_dwordx4 v[72:75], %[wa], %[rs], %[so] offen offset:2048 sc1\nbuffer_load_dwordx4 v[76:79], %[wa], %[rs], %[so] offen offset:3072 sc1\ns_mov_b32 %[so], 46080\n"
        "buffer_load_dwordx4 v[80:83], %[wa], %[rs], %[so] offen offset:0 sc1\nbuffer_load_dwordx4 v[84:87], %[wa], %[rs], %[so] offen offset:1024 sc1\n"
        "buffer_load_dwordx4 v[88:91], %[wa], %[rs], %[so] offen offset:2048 sc1\nbuffer_load_dwordx4 v[92:95], %[wa], %[rs], %[so] offen offset:3072 sc1\ns_mov_b32 %[s1], 0x100000\n.LC1w%=:\n"
        "ds_read_b32 %[pv], %[sy] offset:0\ns_waitcnt lgkmcnt(0)\nv_readfirstlane_b32 %[s0], %[pv]\ns_nop 0\ns_cmp_ge_i32 %[s0], 10\ns_cbranch_scc1 .LC1g%=\ns_sleep 1\ns_sub_u32 %[s1], %[s1], 1\n"
        "s_cbranch_scc0 .LC1w%=\n.LC1g%=:\nds_read_b64_tr_b16 v[24:25], %[xa] offset:3168\nds_read_b64_tr_b16 v[26:27], %[xa] offset:3680\nds_read_b64_tr_b16 v[28:29], %[xa] offset:4224\n"
        "ds_read_b64_tr_b16 v[30:31], %[xa] offset:4736\nds_read_b64_tr_b16 v[12:13], %[xa] offset:0\nds_read_b64_tr_b16 v[14:15], %[xa] offset:512\nds_read_b64_tr_b16 v[16:17], %[xa] offset:1056\n"
        "ds_read_b64_tr_b16 v[18:19], %[xa] offset:1568\ns_waitcnt vmcnt(12) lgkmcnt(6)\nv_mfma_f32_16x16x32_bf16 v[96:99], v[32:35], v[24:27], 0\nds_read_b64_tr_b16 v[20:21], %[xa] offset:2112\n"
        "v_mfma_f32_16x16x32_bf16 v[116:119], v[36:39], v[24:27], 0\nds_read_b64_tr_b16 v[22:23], %[xa] offset:2624\nv_mfma_f32_16x16x32_bf16 v[136:139], v[40:43], v[24:27], 0\n"
        "ds_read_b32 %[pv], %[sy] offset:4\nv_mfma_f32_16x16x32_bf16 v[156:159], v[44:47], v[24:27], 0\nds_read_b64_tr_b16 v[24:25], %[xa] offset:8448\ns_waitcnt lgkmcnt(8)\n"
        "v_mfma_f32_16x16x32_bf16 v[100:103], v[32:35], v[28:31], 0\nds_read_b64_tr_b16 v[26:27], %[xa] offset:8960\nv_mfma_f32_16x16x32_bf16 v[120:123], v[36:39], v[28:31], 0\n"
        "v_mfma_f32_16x16x32_bf16 v[140:143], v[40:43], v[28:31], 0\nv_mfma_f32_16x16x32_bf16 v[160:163], v[44:47], v[28:31], 0\nds_read_b64_tr_b16 v[28:29], %[xa] offset:9504\ns_waitcnt lgkmcnt(8)\n"
        "v_mfma_f32_16x16x32_bf16 v[104:107], v[36:39], v[12:15], 0\nds_read_b64_tr_b16 v[30:31], %[xa] offset:10016\nv_mfma_f32_16x16x32_bf16 v[124:127], v[40:43], v[12:15], 0\n"
        "v_mfma_f32_16x16x32_bf16 v[144:147], v[44:47], v[12:15], 0\nds_read_b64_tr_b16 v[12:13], %[xa] offset:5280\ns_waitcnt lgkmcnt(8)\nv_mfma_f32_16x16x32_bf16 v[108:111], v[36:39], v[16:19], 0\n"
        "ds_read_b64_tr_b16 v[14:15], %[xa] offset:5792\nv_mfma_f32_16x16x32_bf16 v[128:131], v[40:43], v[16:19], 0\nv_mfma_f32_16x16x32_bf16 v[148:151], v[44:47], v[16:19], 0\n"
        "ds_read_b64_tr_b16 v[16:17], %[xa] offset:6336\ns_waitcnt lgkmcnt(8)\nv_mfma_f32_16x16x32_bf16 v[112:115], v[36:39], v[20:23], 0\nds_read_b64_tr_b16 v[18:19], %[xa] offset:6848\n"
        "v_mfma_f32_16x16x32_bf16 v[132:135], v[40:43], v[20:23], 0\nv_mfma_f32_16x16x32_bf16 v[152:155], v[44:47], v[20:23], 0\n.LC1l%=:\ns_waitcnt vmcnt(8) lgkmcnt(6)\n"
        "v_mfma_f32_16x16x32_bf16 v[96:99], v[48:51], v[24:27], v[96:99]\ns_addk_i32 %[so], 15360\nv_readfirstlane_b32 %[s0], %[pv]\n"
        "v_mfma_f32_16x16x32_bf16 v[116:119], v[52:55], v[24:27], v[116:119]\ns_cmp_ge_i32 %[s0], 10\nds_read_b64_tr_b16 v[20:21], %[xa] offset:7392\n"
        "v_mfma_f32_16x16x32_bf16 v[136:139], v[56:59], v[24:27], v[136:139]\ns_cbranch_scc0 .LC1s0_%=\n.LC1r0_%=:\nds_read_b64_tr_b16 v[22:23], %[xa] offset:7904\n"
        "v_mfma_f32_16x16x32_bf16 v[156:159], v[60:63], v[24:27], v[156:159]\nds_read_b64_tr_b16 v[24:25], %[xa] offset:13728\ns_waitcnt lgkmcnt(7)\n"
        "v_mfma_f32_16x16x32_bf16 v[100:103], v[48:51], v[28:31], v[100:103]\nds_read_b64_tr_b16 v[26:27], %[xa] offset:14240\nbuffer_load_dwordx4 v[32:35], %[wa], %[rs], %[so] offen offset:0 sc1\n"
        "v_mfma_f32_16x16x32_bf16 v[120:123], v[52:55], v[28:31], v[120:123]\nbuffer_load_dwordx4 v[36:39], %[wa], %[rs], %[so] offen offset:1024 sc1\n"
        "v_mfma_f32_16x16x32_bf16 v[140:143], v[56:59], v[28:31], v[140:143]\nv_mfma_f32_16x16x32_bf16 v[160:163], v[60:63], v[28:31], v[160:163]\nds_read_b64_tr_b16 v[28:29], %[xa] offset:14784\n"
        "s_waitcnt lgkmcnt(7)\nv_mfma_f32_16x16x32_bf16 v[104:107], v[52:55], v[12:15], v[104:107]\nds_read_b64_tr_b16 v[30:31], %[xa] offset:15296\n"
        "v_mfma_f32_16x16x32_bf16 v[124:127], v[56:59], v[12:15], v[124:127]\nbuffer_load_dwordx4 v[40:43], %[wa], %[rs], %[so] offen offset:2048 sc1\n"
        "v_mfma_f32_16x16x32_bf16 v[144:147], v[60:63], v[12:15], v[144:147]\nds_read_b64_tr_b16 v[12:13], %[xa] offset:10560\ns_waitcnt lgkmcnt(7)\n"
        "v_mfma_f32_16x16x32_bf16 v[108:111], v[52:55], v[16:19], v[108:111]\nds_read_b64_tr_b16 v[14:15], %[xa] offset:11072\nv_mfma_f32_16x16x32_bf16 v[128:131], v[56:59], v[16:19], v[128:131]\n"
        "buffer_load_dwordx4 v[44:47], %[wa], %[rs], %[so] offen offset:3072 sc1\nv_mfma_f32_16x16x32_bf16 v[148:151], v[60:63], v[16:19], v[148:151]\nds_read_b64_tr_b16 v[16:17], %[xa] offset:11616\n"
        "s_waitcnt lgkmcnt(7)\nv_mfma_f32_16x16x32_bf16 v[112:115], v[52:55], v[20:23], v[112:115]\nv_add_u32 %[sy], 4, %[sy]\nds_read_b64_tr_b16 v[18:19], %[xa] offset:12128\n"
        "v_mfma_f32_16x16x32_bf16 v[132:135], v[56:59], v[20:23], v[132:135]\nds_write_b32 %[pg], %[sy]\nv_mfma_f32_16x16x32_bf16 v[152:155], v[60:63], v[20:23], v[152:155]\n"
        "s_waitcnt vmcnt(8) lgkmcnt(7)\nv_mfma_f32_16x16x32_bf16 v[96:99], v[64:67], v[24:27], v[96:99]\ns_addk_i32 %[so], 15360\nds_read_b64_tr_b16 v[20:21], %[xa] offset:12672\n"
        "v_mfma_f32_16x16x32_bf16 v[116:119], v[68:71], v[24:27], v[116:119]\nds_read_b64_tr_b16 v[22:23], %[xa] offset:13184\nv_mfma_f32_16x16x32_bf16 v[136:139], v[72:75], v[24:27], v[136:139]\n"
        "ds_read_b32 %[pv], %[sy] offset:4\nbuffer_load_dwordx4 v[48:51], %[wa], %[rs], %[so] offen offset:0 sc1\nv_mfma_f32_16x16x32_bf16 v[156:159], v[76:79], v[24:27], v[156:159]\n"
        "ds_read_b64_tr_b16 v[24:25], %[xa] offset:19008\ns_waitcnt lgkmcnt(9)\nv_mfma_f32_16x16x32_bf16 v[100:103], v[64:67], v[28:31], v[100:103]\nds_read_b64_tr_b16 v[26:27], %[xa] offset:19520\n"
        "v_mfma_f32_16x16x32_bf16 v[120:123], v[68:71], v[28:31], v[120:123]\nbuffer_load_dwordx4 v[52:55], %[wa], %[rs], %[so] offen offset:1024 sc1\n"
        "v_mfma_f32_16x16x32_bf16 v[140:143], v[72:75], v[28:31], v[140:143]\nv_mfma_f32_16x16x32_bf16 v[160:163], v[76:79], v[28:31], v[160:163]\nds_read_b64_tr_b16 v[28:29], %[xa] offset:20064\n"
        "s_waitcnt lgkmcnt(9)\nv_mfma_f32_16x16x32_bf16 v[104:107], v[68:71], v[12:15], v[104:107]\nds_read_b64_tr_b16 v[30:31], %[xa] offset:20576\n"
        "v_mfma_f32_16x16x32_bf16 v[124:127], v[72:75], v[12:15], v[124:127]\nbuffer_load_dwordx4 v[56:59], %[wa], %[rs], %[so] offen offset:2048 sc1\n"
        "v_mfma_f32_16x16x32_bf16 v[144:147], v[76:79], v[12:15], v[144:147]\nds_read_b64_tr_b16 v[12:13], %[xa] offset:15840\ns_waitcnt lgkmcnt(9)\n"
        "v_mfma_f32_16x16x32_bf16 v[108:111], v[68:71], v[16:19], v[108:111]\nds_read_b64_tr_b16 v[14:15], %[xa] offset:16352\nv_mfma_f32_16x16x32_bf16 v[128:131], v[72:75], v[16:19], v[128:131]\n"
        "buffer_load_dwordx4 v[60:63], %[wa], %[rs], %[so] offen offset:3072 sc1\nv_mfma_f32_16x16x32_bf16 v[148:151], v[76:79], v[16:19], v[148:151]\nds_read_b64_tr_b16 v[16:17], %[xa] offset:16896\n"
        "s_waitcnt lgkmcnt(8)\nv_mfma_f32_16x16x32_bf16 v[112:115], v[68:71], v[20:23], v[112:115]\nds_read_b64_tr_b16 v[18:19], %[xa] offset:17408\n"
        "v_mfma_f32_16x16x32_bf16 v[132:135], v[72:75], v[20:23], v[132:135]\nv_mfma_f32_16x16x32_bf16 v[152:155], v[76:79], v[20:23], v[152:155]\ns_waitcnt vmcnt(8) lgkmcnt(6)\n"
        "v_mfma_f32_16x16x32_bf16 v[96:99], v[80:83], v[24:27], v[96:99]\ns_addk_i32 %[so], 15360\nv_readfirstlane_b32 %[s0], %[pv]\n"
        "v_mfma_f32_16x16x32_bf16 v[116:119], v[84:87], v[24:27], v[116:119]\ns_cmp_ge_i32 %[s0], 10\nds_read_b64_tr_b16 v[20:21], %[xa] offset:17952\n"
        "v_mfma_f32_16x16x32_bf16 v[136:139], v[88:91], v[24:27], v[136:139]\ns_cbranch_scc0 .LC1s1_%=\n.LC1r1_%=:\nds_read_b64_tr_b16 v[22:23], %[xa] offset:18464\n"
        "v_mfma_f32_16x16x32_bf16 v[156:159], v[92:95], v[24:27], v[156:159]\nds_read_b64_tr_b16 v[24:25], %[xa] offset:24288\ns_waitcnt lgkmcnt(7)\n"
        "v_mfma_f32_16x16x32_bf16 v[100:103], v[80:83], v[28:31], v[100:103]\nds_read_b64_tr_b16 v[26:27], %[xa] offset:24800\nbuffer_load_dwordx4 v[64:67], %[wa], %[rs], %[so] offen offset:0 sc1\n"
        "v_mfma_f32_16x16x32_bf16 v[120:123], v[84:87], v[28:31], v[120:123]\nbuffer_load_dwordx4 v[68:71], %[wa], %[rs], %[so] offen offset:1024 sc1\n"
        "v_mfma_f32_16x16x32_bf16 v[140:143], v[88:91], v[28:31], v[140:143]\nv_mfma_f32_16x16x32_bf16 v[160:163], v[92:95], v[28:31], v[160:163]\nds_read_b64_tr_b16 v[28:29], %[xa] offset:25344\n"
        "s_waitcnt lgkmcnt(7)\nv_mfma_f32_16x16x32_bf16 v[104:107], v[84:87], v[12:15], v[104:107]\nds_read_b64_tr_b16 v[30:31], %[xa] offset:25856\n"
        "v_mfma_f32_16x16x32_bf16 v[124:127], v[88:91], v[12:15], v[124:127]\nbuffer_load_dwordx4 v[72:75], %[wa], %[rs], %[so] offen offset:2048 sc1\n"
        "v_mfma_f32_16x16x32_bf16 v[144:147], v[92:95], v[12:15], v[144:147]\nds_read_b64_tr_b16 v[12:13], %[xa] offset:21120\ns_waitcnt lgkmcnt(7)\n"
        "v_mfma_f32_16x16x32_bf16 v[108:111], v[84:87], v[16:19], v[108:111]\nds_read_b64_tr_b16 v[14:15], %[xa] offset:21632\nv_mfma_f32_16x16x32_bf16 v[128:131], v[88:91], v[16:19], v[128:131]\n"
        "buffer_load_dwordx4 v[76:79], %[wa], %[rs], %[so] offen offset:3072 sc1\nv_mfma_f32_16x16x32_bf16 v[148:151], v[92:95], v[16:19], v[148:151]\nds_read_b64_tr_b16 v[16:17], %[xa] offset:22176\n"
        "s_waitcnt lgkmcnt(7)\nv_mfma_f32_16x16x32_bf16 v[112:115], v[84:87], v[20:23], v[112:115]\nv_add_u32 %[sy], 4, %[sy]\nds_read_b64_tr_b16 v[18:19], %[xa] offset:22688\n"
        "v_mfma_f32_16x16x32_bf16 v[132:135], v[88:91], v[20:23], v[132:135]\nds_write_b32 %[pg], %[sy]\nv_mfma_f32_16x16x32_bf16 v[152:155], v[92:95], v[20:23], v[152:155]\n"
        "s_waitcnt vmcnt(8) lgkmcnt(7)\nv_mfma_f32_16x16x32_bf16 v[96:99], v[32:35], v[24:27], v[96:99]\ns_addk_i32 %[so], 15360\nds_read_b64_tr_b16 v[20:21], %[xa] offset:23232\n"
        "v_mfma_f32_16x16x32_bf16 v[116:119], v[36:39], v[24:27], v[116:119]\nds_read_b64_tr_b16 v[22:23], %[xa] offset:23744\nv_mfma_f32_16x16x32_bf16 v[136:139], v[40:43], v[24:27], v[136:139]\n"
        "ds_read_b32 %[pv], %[sy] offset:4\nbuffer_load_dwordx4 v[80:83], %[wa], %[rs], %[so] offen offset:0 sc1\nv_mfma_f32_16x16x32_bf16 v[156:159], v[44:47], v[24:27], v[156:159]\n"
        "ds_read_b64_tr_b16 v[24:25], %[xa] offset:29568\ns_waitcnt lgkmcnt(9)\nv_mfma_f32_16x16x32_bf16 v[100:103], v[32:35], v[28:31], v[100:103]\nds_read_b64_tr_b16 v[26:27], %[xa] offset:30080\n"
        "v_mfma_f32_16x16x32_bf16 v[120:123], v[36:39], v[28:31], v[120:123]\nbuffer_load_dwordx4 v[84:87], %[wa], %[rs], %[so] offen offset:1024 sc1\n"
        "v_mfma_f32_16x16x32_bf16 v[140:143], v[40:43], v[28:31], v[140:143]\nv_mfma_f32_16x16x32_bf16 v[160:163], v[44:47], v[28:31], v[160:163]\nds_read_b64_tr_b16 v[28:29], %[xa] offset:30624\n"
        "s_waitcnt lgkmcnt(9)\nv_mfma_f32_16x16x32_bf16 v[104:107], v[36:39], v[12:15], v[104:107]\nds_read_b64_tr_b16 v[30:31], %[xa] offset:31136\n"
        "v_mfma_f32_16x16x32_bf16 v[124:127], v[40:43], v[12:15], v[124:127]\nbuffer_load_dwordx4 v[88:91], %[wa], %[rs], %[so] offen offset:2048 sc1\n"
        "v_mfma_f32_16x16x32_bf16 v[144:147], v[44:47], v[12:15], v[144:147]\nds_read_b64_tr_b16 v[12:13], %[xa] offset:26400\ns_waitcnt lgkmcnt(9)\n"
        "v_mfma_f32_16x16x32_bf16 v[108:111], v[36:39], v[16:19], v[108:111]\nds_read_b64_tr_b16 v[14:15], %[xa] offset:26912\nv_mfma_f32_16x16x32_bf16 v[128:131], v[40:43], v[16:19], v[128:131]\n"
        "buffer_load_dwordx4 v[92:95], %[wa], %[rs], %[so] offen offset:3072 sc1\nv_mfma_f32_16x16x32_bf16 v[148:151], v[44:47], v[16:19], v[148:151]\nds_read_b64_tr_b16 v[16:17], %[xa] offset:27456\n"
        "s_waitcnt lgkmcnt(8)\nv_mfma_f32_16x16x32_bf16 v[112:115], v[36:39], v[20:23], v[112:115]\nds_read_b64_tr_b16 v[18:19], %[xa] offset:27968\n"
        "v_mfma_f32_16x16x32_bf16 v[132:135], v[40:43], v[20:23], v[132:135]\ns_cmp_lg_u32 %[so], 291840\nv_mfma_f32_16x16x32_bf16 v[152:155], v[44:47], v[20:23], v[152:155]\n"
        "v_add_u32 %[xa], 21120, %[xa]\ns_cbranch_scc1 .LC1l%=\ns_waitcnt vmcnt(8) lgkmcnt(6)\nv_mfma_f32_16x16x32_bf16 v[96:99], v[48:51], v[24:27], v[96:99]\nv_readfirstlane_b32 %[s0], %[pv]\n"
        "ds_read_b64_tr_b16 v[20:21], %[xa] offset:7392\nv_mfma_f32_16x16x32_bf16 v[116:119], v[52:55], v[24:27], v[116:119]\ns_cmp_ge_i32 %[s0], 10\nds_read_b64_tr_b16 v[22:23], %[xa] offset:7904\n"
        "v_mfma_f32_16x16x32_bf16 v[136:139], v[56:59], v[24:27], v[136:139]\ns_cbranch_scc0 .LC1s2_%=\n.LC1r2_%=:\nv_mfma_f32_16x16x32_bf16 v[156:159], v[60:63], v[24:27], v[156:159]\n"
        "ds_read_b64_tr_b16 v[24:25], %[xa] offset:13728\ns_waitcnt lgkmcnt(7)\nv_mfma_f32_16x16x32_bf16 v[100:103], v[48:51], v[28:31], v[100:103]\nds_read_b64_tr_b16 v[26:27], %[xa] offset:14240\n"
        "v_mfma_f32_16x16x32_bf16 v[120:123], v[52:55], v[28:31], v[120:123]\nv_mfma_f32_16x16x32_bf16 v[140:143], v[56:59], v[28:31], v[140:143]\n"
        "v_mfma_f32_16x16x32_bf16 v[160:163], v[60:63], v[28:31], v[160:163]\nds_read_b64_tr_b16 v[28:29], %[xa] offset:14784\ns_waitcnt lgkmcnt(7)\n"
        "v_mfma_f32_16x16x32_bf16 v[104:107], v[52:55], v[12:15], v[104:107]\nds_read_b64_tr_b16 v[30:31], %[xa] offset:15296\nv_mfma_f32_16x16x32_bf16 v[124:127], v[56:59], v[12:15], v[124:127]\n"
        "v_mfma_f32_16x16x32_bf16 v[144:147], v[60:63], v[12:15], v[144:147]\nds_read_b64_tr_b16 v[12:13], %[xa] offset:10560\ns_waitcnt lgkmcnt(7)\n"
        "v_mfma_f32_16x16x32_bf16 v[108:111], v[52:55], v[16:19], v[108:111]\nds_read_b64_tr_b16 v[14:15], %[xa] offset:11072\nv_mfma_f32_16x16x32_bf16 v[128:131], v[56:59], v[16:19], v[128:131]\n"
        "v_mfma_f32_16x16x32_bf16 v[148:151], v[60:63], v[16:19], v[148:151]\nds_read_b64_tr_b16 v[16:17], %[xa] offset:11616\ns_waitcnt lgkmcnt(7)\n"
        "v_mfma_f32_16x16x32_bf16 v[112:115], v[52:55], v[20:23], v[112:115]\nv_add_u32 %[sy], 4, %[sy]\nds_read_b64_tr_b16 v[18:19], %[xa] offset:12128\n"
        "v_mfma_f32_16x16x32_bf16 v[132:135], v[56:59], v[20:23], v[132:135]\nv_mfma_f32_16x16x32_bf16 v[152:155], v[60:63], v[20:23], v[152:155]\ns_waitcnt vmcnt(4) lgkmcnt(6)\n"
        "v_mfma_f32_16x16x32_bf16 v[96:99], v[64:67], v[24:27], v[96:99]\nds_read_b64_tr_b16 v[20:21], %[xa] offset:12672\nv_mfma_f32_16x16x32_bf16 v[116:119], v[68:71], v[24:27], v[116:119]\n"
        "ds_read_b64_tr_b16 v[22:23], %[xa] offset:13184\nv_mfma_f32_16x16x32_bf16 v[136:139], v[72:75], v[24:27], v[136:139]\nds_read_b32 %[pv], %[sy] offset:4\n"
        "v_mfma_f32_16x16x32_bf16 v[156:159], v[76:79], v[24:27], v[156:159]\nds_read_b64_tr_b16 v[24:25], %[xa] offset:19008\ns_waitcnt lgkmcnt(8)\n"
        "v_mfma_f32_16x16x32_bf16 v[100:103], v[64:67], v[28:31], v[100:103]\nds_read_b64_tr_b16 v[26:27], %[xa] offset:19520\nv_mfma_f32_16x16x32_bf16 v[120:123], v[68:71], v[28:31], v[120:123]\n"
        "v_mfma_f32_16x16x32_bf16 v[140:143], v[72:75], v[28:31], v[140:143]\nv_mfma_f32_16x16x32_bf16 v[160:163], v[76:79], v[28:31], v[160:163]\nds_read_b64_tr_b16 v[28:29], %[xa] offset:20064\n"
        "s_waitcnt lgkmcnt(8)\nv_mfma_f32_16x16x32_bf16 v[104:107], v[68:71], v[12:15], v[104:107]\nds_read_b64_tr_b16 v[30:31], %[xa] offset:20576\n"
        "v_mfma_f32_16x16x32_bf16 v[124:127], v[72:75], v[12:15], v[124:127]\nv_mfma_f32_16x16x32_bf16 v[144:147], v[76:79], v[12:15], v[144:147]\nds_read_b64_tr_b16 v[12:13], %[xa] offset:15840\n"
        "s_waitcnt lgkmcnt(8)\nv_mfma_f32_16x16x32_bf16 v[108:111], v[68:71], v[16:19], v[108:111]\nds_read_b64_tr_b16 v[14:15], %[xa] offset:16352\n"
        "v_mfma_f32_16x16x32_bf16 v[128:131], v[72:75], v[16:19], v[128:131]\nv_mfma_f32_16x16x32_bf16 v[148:151], v[76:79], v[16:19], v[148:151]\nds_read_b64_tr_b16 v[16:17], %[xa] offset:16896\n"
        "s_waitcnt lgkmcnt(8)\nv_mfma_f32_16x16x32_bf16 v[112:115], v[68:71], v[20:23], v[112:115]\nds_read_b64_tr_b16 v[18:19], %[xa] offset:17408\n"
        "v_mfma_f32_16x16x32_bf16 v[132:135], v[72:75], v[20:23], v[132:135]\nv_mfma_f32_16x16x32_bf16 v[152:155], v[76:79], v[20:23], v[152:155]\ns_waitcnt vmcnt(0) lgkmcnt(6)\n"
        "v_mfma_f32_16x16x32_bf16 v[96:99], v[80:83], v[24:27], v[96:99]\nv_readfirstlane_b32 %[s0], %[pv]\nds_read_b64_tr_b16 v[20:21], %[xa] offset:17952\n"
        "v_mfma_f32_16x16x32_bf16 v[116:119], v[84:87], v[24:27], v[116:119]\nds_read_b64_tr_b16 v[22:23], %[xa] offset:18464\nv_mfma_f32_16x16x32_bf16 v[136:139], v[88:91], v[24:27], v[136:139]\n"
        "s_cmp_ge_i32 %[s0], %[nst]\nv_mfma_f32_16x16x32_bf16 v[156:159], v[92:95], v[24:27], v[156:159]\ns_cbranch_scc0 .LC1t%=\n.LC1u%=:\ns_waitcnt lgkmcnt(6)\n"
        "v_mfma_f32_16x16x32_bf16 v[100:103], v[80:83], v[28:31], v[100:103]\nv_add_u32 %[sy], 4, %[sy]\nds_read_b128 v[24:27], %[mra] offset:768\n"
        "v_mfma_f32_16x16x32_bf16 v[120:123], v[84:87], v[28:31], v[120:123]\nv_mfma_f32_16x16x32_bf16 v[140:143], v[88:91], v[28:31], v[140:143]\n"
        "v_mfma_f32_16x16x32_bf16 v[160:163], v[92:95], v[28:31], v[160:163]\nds_read_b128 v[32:35], %[cwa] offset:448\ns_waitcnt lgkmcnt(6)\n"
        "v_mfma_f32_16x16x32_bf16 v[104:107], v[84:87], v[12:15], v[104:107]\nds_read_b128 v[36:39], %[cwa] offset:256\nds_read_b128 v[28:31], %[mra] offset:1024\n"
        "v_mfma_f32_16x16x32_bf16 v[124:127], v[88:91], v[12:15], v[124:127]\nds_read_b128 v[40:43], %[cwa] offset:832\nv_mfma_f32_16x16x32_bf16 v[144:147], v[92:95], v[12:15], v[144:147]\n"
        "ds_read_b128 v[44:47], %[cwa] offset:896\ns_waitcnt lgkmcnt(8)\nv_mfma_f32_16x16x32_bf16 v[108:111], v[84:87], v[16:19], v[108:111]\nv_add_u32 %[sy], 4, %[sy]\n"
        "ds_read_b128 v[12:15], %[mra] offset:0\nv_mfma_f32_16x16x32_bf16 v[128:131], v[88:91], v[16:19], v[128:131]\nv_mfma_f32_16x16x32_bf16 v[148:151], v[92:95], v[16:19], v[148:151]\n"
        "ds_read_b128 v[16:19], %[mra] offset:256\ns_waitcnt lgkmcnt(8)\nv_mfma_f32_16x16x32_bf16 v[112:115], v[84:87], v[20:23], v[112:115]\n"
        "v_mfma_f32_16x16x32_bf16 v[132:135], v[88:91], v[20:23], v[132:135]\nv_mfma_f32_16x16x32_bf16 v[152:155], v[92:95], v[20:23], v[152:155]\nds_read_b128 v[20:23], %[mra] offset:512\n"
        "s_setprio %[pfs]\ns_waitcnt lgkmcnt(1)\n.long 0xd3b04060, 0x1d823120\n.long 0xd3b04062, 0x1d8a3122\n.long 0xd3b04074, 0x1dd23124\n.long 0xd3b04076, 0x1dda3126\n.long 0xd3b14060, 0x18023560\n"
        ".long 0xd3b14062, 0x18023562\n.long 0xd3b04064, 0x1d923920\n.long 0xd3b04066, 0x1d9a3922\n.long 0xd3b14074, 0x18023574\n.long 0xd3b14076, 0x18023576\nv_cvt_pk_bf16_f32 v96, v96, v97\n"
        "v_cvt_pk_bf16_f32 v97, v98, v99\n.long 0xd3b04078, 0x1de23924\n.long 0xd3b0407a, 0x1dea3926\n.long 0xd3b14064, 0x18023d64\n.long 0xd3b14066, 0x18023d66\nv_cvt_pk_bf16_f32 v116, v116, v117\n"
        "v_cvt_pk_bf16_f32 v117, v118, v119\nds_write_b64 %[sta], v[96:97] offset:24032\n.long 0xd3b04068, 0x1da21924\n.long 0xd3b0406a, 0x1daa1926\n.long 0xd3b14078, 0x18023d78\n"
        ".long 0xd3b1407a, 0x18023d7a\nv_cvt_pk_bf16_f32 v100, v100, v101\nv_cvt_pk_bf16_f32 v101, v102, v103\nds_write_b64 %[sta], v[116:117] offset:23936\n.long 0xd3b0406c, 0x1db22124\n"
        ".long 0xd3b0406e, 0x1dba2126\n.long 0xd3b14068, 0x18021d68\n.long 0xd3b1406a, 0x18021d6a\nv_cvt_pk_bf16_f32 v120, v120, v121\nv_cvt_pk_bf16_f32 v121, v122, v123\n"
        "ds_write_b64 %[sta], v[100:101] offset:31968\ns_waitcnt lgkmcnt(3)\n.long 0xd3b04070, 0x1dc22924\n.long 0xd3b04072, 0x1dca2926\n.long 0xd3b1406c, 0x1802256c\n.long 0xd3b1406e, 0x1802256e\n"
        "v_cvt_pk_bf16_f32 v104, v104, v105\nv_cvt_pk_bf16_f32 v105, v106, v107\nds_write_b64 %[sta], v[120:121] offset:31872\n.long 0xd3b14070, 0x18022d70\n.long 0xd3b14072, 0x18022d72\n"
        "v_cvt_pk_bf16_f32 v108, v108, v109\nv_cvt_pk_bf16_f32 v109, v110, v111\nds_write_b64 %[sta], v[104:105] offset:128\nv_cvt_pk_bf16_f32 v112, v112, v113\nv_cvt_pk_bf16_f32 v113, v114, v115\n"
        "ds_write_b64 %[sta], v[108:109] offset:8064\nds_write_b64 %[sta], v[112:113] offset:16000\nds_write_b32 %[pg], %[sy]\nv_add_u32 %[sy], 4, %[sy]\n.long 0xd3b04088, 0x1e223128\n"
        ".long 0xd3b0408a, 0x1e2a312a\n.long 0xd3b0409c, 0x1e72312c\n.long 0xd3b0409e, 0x1e7a312e\n.long 0xd3b14088, 0x18023588\n.long 0xd3b1408a, 0x1802358a\n.long 0xd3b0408c, 0x1e323928\n"
        ".long 0xd3b0408e, 0x1e3a392a\n.long 0xd3b1409c, 0x1802359c\n.long 0xd3b1409e, 0x1802359e\nv_cvt_pk_bf16_f32 v136, v136, v137\nv_cvt_pk_bf16_f32 v137, v138, v139\n.long 0xd3b040a0, 0x1e82392c\n"
        ".long 0xd3b040a2, 0x1e8a392e\n.long 0xd3b1408c, 0x18023d8c\n.long 0xd3b1408e, 0x18023d8e\nv_cvt_pk_bf16_f32 v156, v156, v157\nv_cvt_pk_bf16_f32 v157, v158, v159\n"
        "ds_write_b64 %[sta], v[136:137] offset:24224\n.long 0xd3b0407c, 0x1df21928\n.long 0xd3b0407e, 0x1dfa192a\n.long 0xd3b140a0, 0x18023da0\n.long 0xd3b140a2, 0x18023da2\n"
        "v_cvt_pk_bf16_f32 v140, v140, v141\nv_cvt_pk_bf16_f32 v141, v142, v143\nds_write_b64 %[sta], v[156:157] offset:24256\n.long 0xd3b04090, 0x1e42192c\n.long 0xd3b04092, 0x1e4a192e\n"
        ".long 0xd3b1407c, 0x18021d7c\n.long 0xd3b1407e, 0x18021d7e\nv_cvt_pk_bf16_f32 v160, v160, v161\nv_cvt_pk_bf16_f32 v161, v162, v163\nds_write_b64 %[sta], v[140:141] offset:32160\n"
        ".long 0xd3b04080, 0x1e022128\n.long 0xd3b04082, 0x1e0a212a\n.long 0xd3b14090, 0x18021d90\n.long 0xd3b14092, 0x18021d92\nv_cvt_pk_bf16_f32 v124, v124, v125\nv_cvt_pk_bf16_f32 v125, v126, v127\n"
        "ds_write_b64 %[sta], v[160:161] offset:32192\n.long 0xd3b04094, 0x1e52212c\n.long 0xd3b04096, 0x1e5a212e\n.long 0xd3b14080, 0x18022580\n.long 0xd3b14082, 0x18022582\n"
        "v_cvt_pk_bf16_f32 v144, v144, v145\nv_cvt_pk_bf16_f32 v145, v146, v147\nds_write_b64 %[sta], v[124:125] offset:416\n.long 0xd3b04084, 0x1e122928\n.long 0xd3b04086, 0x1e1a292a\n"
        ".long 0xd3b14094, 0x18022594\n.long 0xd3b14096, 0x18022596\nv_cvt_pk_bf16_f32 v128, v128, v129\nv_cvt_pk_bf16_f32 v129, v130, v131\nds_write_b64 %[sta], v[144:145] offset:448\n"
        ".long 0xd3b04098, 0x1e62292c\n.long 0xd3b0409a, 0x1e6a292e\n.long 0xd3b14084, 0x18022d84\n.long 0xd3b14086, 0x18022d86\nv_cvt_pk_bf16_f32 v148, v148, v149\nv_cvt_pk_bf16_f32 v149, v150, v151\n"
        "ds_write_b64 %[sta], v[128:129] offset:8352\n.long 0xd3b14098, 0x18022d98\n.long 0xd3b1409a, 0x18022d9a\nv_cvt_pk_bf16_f32 v132, v132, v133\nv_cvt_pk_bf16_f32 v133, v134, v135\n"
        "ds_write_b64 %[sta], v[148:149] offset:8384\nv_cvt_pk_bf16_f32 v152, v152, v153\nv_cvt_pk_bf16_f32 v153, v154, v155\nds_write_b64 %[sta], v[132:133] offset:16288\n"
        "ds_write_b64 %[sta], v[152:153] offset:16320\nds_write_b32 %[pg], %[sy]\ns_branch .LE%=\n.LC1s0_%=:\ns_mov_b32 %[s1], 0x100000\n.LC1p0_%=:\nds_read_b32 %[pv], %[sy] offset:4\n"
        "s_waitcnt lgkmcnt(0)\nv_readfirstlane_b32 %[s0], %[pv]\ns_nop 0\ns_cmp_ge_i32 %[s0], 10\ns_cbranch_scc1 .LC1r0_%=\ns_sleep 1\ns_sub_u32 %[s1], %[s1], 1\ns_cbranch_scc0 .LC1p0_%=\n"
        "s_branch .LC1r0_%=\n.LC1s1_%=:\ns_mov_b32 %[s1], 0x100000\n.LC1p1_%=:\nds_read_b32 %[pv], %[sy] offset:4\ns_waitcnt lgkmcnt(0)\nv_readfirstlane_b32 %[s0], %[pv]\ns_nop 0\n"
        "s_cmp_ge_i32 %[s0], 10\ns_cbranch_scc1 .LC1r1_%=\ns_sleep 1\ns_sub_u32 %[s1], %[s1], 1\ns_cbranch_scc0 .LC1p1_%=\ns_branch .LC1r1_%=\n.LC1s2_%=:\ns_mov_b32 %[s1], 0x100000\n.LC1p2_%=:\n"
        "ds_read_b32 %[pv], %[sy] offset:4\ns_waitcnt lgkmcnt(0)\nv_readfirstlane_b32 %[s0], %[pv]\ns_nop 0\ns_cmp_ge_i32 %[s0], 10\ns_cbranch_scc1 .LC1r2_%=\ns_sleep 1\ns_sub_u32 %[s1], %[s1], 1\n"
        "s_cbranch_scc0 .LC1p2_%=\ns_branch .LC1r2_%=\n.LC1t%=:\ns_mov_b32 %[s1], 0x100000\n.LC1q%=:\nds_read_b32 %[pv], %[sy] offset:4\ns_waitcnt lgkmcnt(0)\nv_readfirstlane_b32 %[s0], %[pv]\n"
        "s_nop 0\ns_cmp_ge_i32 %[s0], %[nst]\ns_cbranch_scc1 .LC1u%=\ns_sleep 1\ns_sub_u32 %[s1], %[s1], 1\ns_cbranch_scc0 .LC1q%=\ns_branch .LC1u%=\n.LE%=:\n"
        : [pv] "=&v"(pv), [xa] "+v"(xa), [sy] "+v"(sy), [so] "=&s"(so), [s0] "=&s"(s0_), [s1] "=&s"(s1_)
        : [wa] "v"(wa), [rs] "s"(wrsrc), [pg] "v"(pga), [cwa] "v"(cwa), [mra] "v"(mra), [sta] "v"(sta), [w] "s"(w), [ow] "i"(SY_W - SY_X), [pfs] "i"(PFS), [nst] "i"(NST1)
        : "memory", "scc", "v12", "v13", "v14", "v15", "v16", "v17", "v18", "v19", "v20", "v21", "v22", "v23", "v24", "v25", "v26", "v27", "v28", "v29", "v30", "v31", "v32", "v33", "v34", "v35", "v36", "v37", "v38", "v39", "v40", "v41", "v42", "v43", "v44", "v45", "v46", "v47", "v48", "v49", "v50", "v51", "v52", "v53", "v54", "v55", "v56", "v57", "v58", "v59", "v60", "v61", "v62", "v63", "v64", "v65", "v66", "v67", "v68", "v69", "v70", "v71", "v72", "v73", "v74", "v75", "v76", "v77", "v78", "v79", "v80", "v81", "v82", "v83", "v84", "v85", "v86", "v87", "v88", "v89", "v90", "v91", "v92", "v93", "v94", "v95", "v96", "v97", "v98", "v99", "v100", "v101", "v102", "v103", "v104", "v105", "v106", "v107", "v108", "v109", "v110", "v111", "v112", "v113", "v114", "v115", "v116", "v117", "v118", "v119", "v120", "v121", "v122", "v123", "v124", "v125", "v126", "v127", "v128", "v129", "v130", "v131", "v132", "v133", "v134", "v135", "v136", "v137", "v138", "v139", "v140", "v141", "v142", "v143", "v144", "v145", "v146", "v147", "v148", "v149", "v150", "v151", "v152", "v153", "v154", "v155", "v156", "v157", "v158", "v159", "v160", "v161", "v162", "v163", "v164", "v165", "v166", "v167");
    }
    }
    ST(5);
    LANE(lane);
    const int t = 64 * w + lane;
    if (P0E != 2) {
      __builtin_amdgcn_s_setprio(PFD);
      if (P0E) {
        const u16* sp = wot + (size_t)hg * 51200 + 25600 + w * 512 + lane * 8;
        pg_poll<0>(sya, LDSA(smem) + SY_OFF + 256 * layer + SY_X + 48);
#pragma unroll
        for (int i = 0; i < 13; ++i) {
          if (i >= DQ) vmwait<DQ - 1>();
          if (i < 12 || w < 2) __builtin_amdgcn_global_load_lds((const u32*)(sp + i * 2048), (u32*)(At + WO1_OFF / 2 + (4 * i + w) * 512), 16, 0, 16);
        }
      } else {
      const u16* sp = wot + (size_t)hg * 51200 + w * 512 + lane * 8;
      lds_poll<SY_RV, 1>(sya, 8);
      pg_poll<0>(sya, LDSA(smem) + SY_OFF + 256 * layer + SY_X + 48);
#pragma unroll
      for (int i = 0; i < 25; ++i) {
        const int f = 4 * i + w;
        if (i >= DQ) vmwait<DQ - 1>();
        if (i == 12 + DQ) lds_inc<SY_WO>(sya, one);
        __builtin_amdgcn_global_load_lds((const u32*)(sp + i * 2048), (u32*)(At + f * 512 + (i > 12 || (i == 12 && w >= 2) ? (WO1_OFF - 51200) / 2 : 0)), 16, 0, 16);
      }
      }
      VMCNT(0);
      lds_inc<SY_WO + 4>(sya, one);
      ST(11);
    }
    if (!layer && w == 3) {
      int spins = 0;
      _Pragma("unroll 1")
      while (true) {
        asm volatile("" ::: "memory");
        u32 d = 0;
#pragma unroll
        for (int i = 0; i < 2; ++i) {
          const u32x4 f = __builtin_amdgcn_raw_buffer_load_b128(frs, lane * 16, FL_M2 + i * 1024, 16);
          d |= (f[0] ^ tg0) | (f[1] ^ tg1) | (f[2] ^ tg0) | (f[3] ^ tg1);
        }
        if (__builtin_amdgcn_ballot_w64(d != 0) == 0 || ++spins > GCAP) break;
        __builtin_amdgcn_s_sleep(8);
      }
      VMCNT(0);
      ST(8);
    }
    if (layer && b == 0 && w == 1) {
      if (lane == 0) __builtin_amdgcn_raw_buffer_store_b64((u32x2){tg0, tg1}, frs, 0, 0, 16);
    }
    ST_FLUSH(kid);
    return;
  }
  const int j = w - 4, grp = j >> 2, mt2 = j & 3, kh = grp, qt = mt2, tt = t - 256;
  u32 l16 = lane * 16;
  int xso = g == (l15 >> 2) ? (rb * 8 + hg) * 1024 + mt2 * 256 + l15 * 16 : -1;
  const u32 tq1 = tg0 + tg1;
  if (layer) {
    const __amdgpu_buffer_rsrc_t xrs = __builtin_amdgcn_make_buffer_rsrc((void*)(xt + (size_t)rb * 51200), 0, 102400, 0x00020000);
    int go[13];
#pragma unroll
    for (int i = 0; i < 13; ++i) {
      const bool hl = i == 1 || i == 6 || i == 11;
      int ks = (hl ? j + 8 * (i / 5) : 2 * (i - (i + 3) / 5) + grp) + 2 * rot;
      ks -= ks >= 20 ? 20 : 0;
      go[i] = (i == 11 && j >= 4) ? 102400 : ks * 5120 + (hl ? (rb ? 4096 : 0) : mt2 * 1024);
    }
    const int ck = (lane & 7) ^ ((lane >> 3) & 6);
    u32 lm0 = LDSA(At) + grp * 5280 + (1 + (ck >> 1)) * 1056 + (8 * mt2 + (lane >> 3)) * 32 + (ck & 1) * 16, lm1 = lm0 + 52800;
    u32 lh = LDSA(At) + j * 5280 + l16, syh = sya + 4 * (j >> 1), tm;
    u32 xsa = LDSA(At) + (mt2 + 1) * 1056 + (4 * g + ((lane >> 2) & 3)) * 32 + 8 * (lane & 3);
    const int ns = hg & 3;
    const u64 m1 = 0xaaaaaaaaaaaaaaaaull, m2 = 0xccccccccccccccccull;
    int cap, s0_, s1_;
    PIN(lm0); PIN(lm1); PIN(lh); PIN(syh); PIN(xsa); PIN(xso); PIN(l16); PIN(sya); PIN(one);
    __builtin_amdgcn_s_setprio(PTX);
    __builtin_amdgcn_s_barrier();
    __builtin_amdgcn_s_sleep(XSLEEP);
      asm volatile(
        "s_mov_b32 %[cap], 0x10000\nbuffer_load_dwordx4 v[136:139], %[l16], %[xrs], %[g0] offen sc1\nbuffer_load_dwordx4 v[140:143], %[l16], %[xrs], %[g1] offen sc1\n"
        "buffer_load_dwordx4 v[144:147], %[l16], %[xrs], %[g2] offen sc1\n.LP0_%=:\ns_waitcnt vmcnt(2)\nv_max3_u32 %[tm], v136, v137, v138\nv_max_u32 %[tm], %[tm], v139\nv_cmp_eq_u32 vcc, -1, %[tm]\n"
        "s_cbranch_vccnz .LR0_%=\n.LK0_%=:\nds_write_b128 %[lm0], v[136:139] offset:0\ns_mov_b64 exec, 1\nds_add_u32 %[sy], %[one] offset:%[ox]+0\ns_mov_b64 exec, -1\n"
        "buffer_load_dwordx4 v[148:151], %[l16], %[xrs], %[g3] offen sc1\n.LP1_%=:\ns_waitcnt vmcnt(2)\nv_max3_u32 %[tm], v140, v141, v142\nv_max_u32 %[tm], %[tm], v143\nv_cmp_eq_u32 vcc, -1, %[tm]\n"
        "s_cbranch_vccnz .LR1_%=\n.LK1_%=:\nds_write_b128 %[lh], v[140:143] offset:0\ns_mov_b64 exec, 1\nds_add_u32 %[syh], %[one] offset:%[ox]+0\ns_mov_b64 exec, -1\n"
        "buffer_load_dwordx4 v[136:139], %[l16], %[xrs], %[g4] offen sc1\n.LP2_%=:\ns_waitcnt vmcnt(2)\nv_max3_u32 %[tm], v144, v145, v146\nv_max_u32 %[tm], %[tm], v147\nv_cmp_eq_u32 vcc, -1, %[tm]\n"
        "s_cbranch_vccnz .LR2_%=\n.LK2_%=:\nds_write_b128 %[lm0], v[144:147] offset:10560\ns_mov_b64 exec, 1\nds_add_u32 %[sy], %[one] offset:%[ox]+4\ns_mov_b64 exec, -1\n"
        "buffer_load_dwordx4 v[140:143], %[l16], %[xrs], %[g5] offen sc1\n.LP3_%=:\ns_waitcnt vmcnt(2)\nv_max3_u32 %[tm], v148, v149, v150\nv_max_u32 %[tm], %[tm], v151\nv_cmp_eq_u32 vcc, -1, %[tm]\n"
        "s_cbranch_vccnz .LR3_%=\n.LK3_%=:\nds_write_b128 %[lm0], v[148:151] offset:21120\ns_mov_b64 exec, 1\nds_add_u32 %[sy], %[one] offset:%[ox]+8\ns_mov_b64 exec, -1\n"
        "buffer_load_dwordx4 v[144:147], %[l16], %[xrs], %[g6] offen sc1\ns_cmp_lt_u32 %[j], 4\ns_cbranch_scc0 .LT%=\ns_mov_b32 %[s1], 0x100000\n.LU%=:\nds_read_b64 v[160:161], %[sy] offset:%[ox]\n"
        "ds_read_b64_tr_b16 v[152:153], %[xsa] offset:0\nds_read_b64_tr_b16 v[154:155], %[xsa] offset:512\nds_read_b64_tr_b16 v[156:157], %[xsa] offset:5280\n"
        "ds_read_b64_tr_b16 v[158:159], %[xsa] offset:5792\ns_waitcnt lgkmcnt(4)\nv_min_u32 v160, v160, v161\ns_nop 0\nv_readfirstlane_b32 %[s0], v160\ns_waitcnt lgkmcnt(0)\ns_cmp_ge_i32 %[s0], 10\n"
        "s_cbranch_scc1 .LV%=\ns_sleep 1\ns_sub_u32 %[s1], %[s1], 1\ns_cbranch_scc0 .LU%=\n.LV%=:\nv_mfma_f32_16x16x32_bf16 v[160:163], %[ones], v[152:155], 0\n"
        "v_mfma_f32_16x16x32_bf16 v[164:167], v[152:155], v[152:155], 0\ns_cmp_lg_u32 %[ns], 3\nv_mfma_f32_16x16x32_bf16 v[160:163], %[ones], v[156:159], v[160:163]\n"
        "v_mfma_f32_16x16x32_bf16 v[164:167], v[156:159], v[156:159], v[164:167]\ns_cbranch_scc1 .LW%=\nds_read_b64_tr_b16 v[152:153], %[xsa] offset:10560\n"
        "ds_read_b64_tr_b16 v[154:155], %[xsa] offset:11072\nds_read_b64_tr_b16 v[156:157], %[xsa] offset:15840\nds_read_b64_tr_b16 v[158:159], %[xsa] offset:16352\ns_waitcnt lgkmcnt(0)\n"
        "v_mfma_f32_16x16x32_bf16 v[160:163], %[ones], v[152:155], v[160:163]\nv_mfma_f32_16x16x32_bf16 v[164:167], v[152:155], v[152:155], v[164:167]\n"
        "v_mfma_f32_16x16x32_bf16 v[160:163], %[ones], v[156:159], v[160:163]\nv_mfma_f32_16x16x32_bf16 v[164:167], v[156:159], v[156:159], v[164:167]\n.LW%=:\ns_nop 7\n"
        "v_cndmask_b32 v164, v164, v165, %[m1]\nv_cndmask_b32 v166, v166, v167, %[m1]\nv_mov_b32 v161, %[tg0]\nv_mov_b32 v163, %[tq1]\nv_cndmask_b32 v162, v164, v166, %[m2]\n"
        "buffer_store_dwordx4 v[160:163], %[xso], %[srs], 0 offen sc1\n.LT%=:\n.LP4_%=:\ns_waitcnt vmcnt(2)\nv_max3_u32 %[tm], v136, v137, v138\nv_max_u32 %[tm], %[tm], v139\n"
        "v_cmp_eq_u32 vcc, -1, %[tm]\ns_cbranch_vccnz .LR4_%=\n.LK4_%=:\nds_write_b128 %[lm0], v[136:139] offset:31680\ns_mov_b64 exec, 1\nds_add_u32 %[sy], %[one] offset:%[ox]+12\ns_mov_b64 exec, -1\n"
        "buffer_load_dwordx4 v[148:151], %[l16], %[xrs], %[g7] offen sc1\n.LP5_%=:\ns_waitcnt vmcnt(2)\nv_max3_u32 %[tm], v140, v141, v142\nv_max_u32 %[tm], %[tm], v143\nv_cmp_eq_u32 vcc, -1, %[tm]\n"
        "s_cbranch_vccnz .LR5_%=\n.LK5_%=:\nds_write_b128 %[lm0], v[140:143] offset:42240\ns_mov_b64 exec, 1\nds_add_u32 %[sy], %[one] offset:%[ox]+16\ns_mov_b64 exec, -1\n"
        "buffer_load_dwordx4 v[136:139], %[l16], %[xrs], %[g8] offen sc1\n.LP6_%=:\ns_waitcnt vmcnt(2)\nv_max3_u32 %[tm], v144, v145, v146\nv_max_u32 %[tm], %[tm], v147\nv_cmp_eq_u32 vcc, -1, %[tm]\n"
        "s_cbranch_vccnz .LR6_%=\n.LK6_%=:\nds_write_b128 %[lh], v[144:147] offset:42240\ns_mov_b64 exec, 1\nds_add_u32 %[syh], %[one] offset:%[ox]+16\ns_mov_b64 exec, -1\n"
        "buffer_load_dwordx4 v[140:143], %[l16], %[xrs], %[g9] offen sc1\n.LP7_%=:\ns_waitcnt vmcnt(2)\nv_max3_u32 %[tm], v148, v149, v150\nv_max_u32 %[tm], %[tm], v151\nv_cmp_eq_u32 vcc, -1, %[tm]\n"
        "s_cbranch_vccnz .LR7_%=\n.LK7_%=:\nds_write_b128 %[lm1], v[148:151] offset:0\ns_mov_b64 exec, 1\nds_add_u32 %[sy], %[one] offset:%[ox]+20\ns_mov_b64 exec, -1\n"
        "buffer_load_dwordx4 v[144:147], %[l16], %[xrs], %[g10] offen sc1\n.LP8_%=:\ns_waitcnt vmcnt(2)\nv_max3_u32 %[tm], v136, v137, v138\nv_max_u32 %[tm], %[tm], v139\nv_cmp_eq_u32 vcc, -1, %[tm]\n"
        "s_cbranch_vccnz .LR8_%=\n.LK8_%=:\nds_write_b128 %[lm1], v[136:139] offset:10560\ns_mov_b64 exec, 1\nds_add_u32 %[sy], %[one] offset:%[ox]+24\ns_mov_b64 exec, -1\n"
        "buffer_load_dwordx4 v[148:151], %[l16], %[xrs], %[g11] offen sc1\n.LP9_%=:\ns_waitcnt vmcnt(2)\nv_max3_u32 %[tm], v140, v141, v142\nv_max_u32 %[tm], %[tm], v143\nv_cmp_eq_u32 vcc, -1, %[tm]\n"
        "s_cbranch_vccnz .LR9_%=\n.LK9_%=:\nds_write_b128 %[lm1], v[140:143] offset:21120\ns_mov_b64 exec, 1\nds_add_u32 %[sy], %[one] offset:%[ox]+28\ns_mov_b64 exec, -1\n"
        "buffer_load_dwordx4 v[136:139], %[l16], %[xrs], %[g12] offen sc1\n.LP10_%=:\ns_waitcnt vmcnt(2)\nv_max3_u32 %[tm], v144, v145, v146\nv_max_u32 %[tm], %[tm], v147\nv_cmp_eq_u32 vcc, -1, %[tm]\n"
        "s_cbranch_vccnz .LR10_%=\n.LK10_%=:\nds_write_b128 %[lm1], v[144:147] offset:31680\ns_mov_b64 exec, 1\nds_add_u32 %[sy], %[one] offset:%[ox]+32\ns_mov_b64 exec, -1\n.LP11_%=:\n"
        "s_waitcnt vmcnt(1)\nv_max3_u32 %[tm], v148, v149, v150\nv_max_u32 %[tm], %[tm], v151\nv_cmp_eq_u32 vcc, -1, %[tm]\ns_cbranch_vccnz .LR11_%=\n.LK11_%=:\ns_cmp_lt_u32 %[j], 4\n"
        "s_cbranch_scc0 .LS%=\nv_add_u32 %[lh], 42240, %[lh]\nds_write_b128 %[lh], v[148:151] offset:42240\ns_mov_b64 exec, 1\nds_add_u32 %[syh], %[one] offset:%[ox]+32\ns_mov_b64 exec, -1\n.LS%=:\n"
        ".LP12_%=:\ns_waitcnt vmcnt(0)\nv_max3_u32 %[tm], v136, v137, v138\nv_max_u32 %[tm], %[tm], v139\nv_cmp_eq_u32 vcc, -1, %[tm]\ns_cbranch_vccnz .LR12_%=\n.LK12_%=:\n"
        "ds_write_b128 %[lm1], v[136:139] offset:42240\ns_mov_b64 exec, 1\nds_add_u32 %[sy], %[one] offset:%[ox]+36\ns_mov_b64 exec, -1\ns_branch .LE%=\n.LR0_%=:\ns_sub_u32 %[cap], %[cap], 1\n"
        "s_cbranch_scc0 .LQ0_%=\ns_mov_b32 %[cap], 0\ns_branch .LK0_%=\n.LQ0_%=:\ns_sleep %[xp]\nbuffer_load_dwordx4 v[136:139], %[l16], %[xrs], %[g0] offen sc1\n"
        "buffer_load_dwordx4 v[140:143], %[l16], %[xrs], %[g1] offen sc1\nbuffer_load_dwordx4 v[144:147], %[l16], %[xrs], %[g2] offen sc1\ns_branch .LP0_%=\n.LR1_%=:\ns_sub_u32 %[cap], %[cap], 1\n"
        "s_cbranch_scc0 .LQ1_%=\ns_mov_b32 %[cap], 0\ns_branch .LK1_%=\n.LQ1_%=:\ns_sleep %[xp]\nbuffer_load_dwordx4 v[140:143], %[l16], %[xrs], %[g1] offen sc1\n"
        "buffer_load_dwordx4 v[144:147], %[l16], %[xrs], %[g2] offen sc1\nbuffer_load_dwordx4 v[148:151], %[l16], %[xrs], %[g3] offen sc1\ns_branch .LP1_%=\n.LR2_%=:\ns_sub_u32 %[cap], %[cap], 1\n"
        "s_cbranch_scc0 .LQ2_%=\ns_mov_b32 %[cap], 0\ns_branch .LK2_%=\n.LQ2_%=:\ns_sleep %[xp]\nbuffer_load_dwordx4 v[144:147], %[l16], %[xrs], %[g2] offen sc1\n"
        "buffer_load_dwordx4 v[148:151], %[l16], %[xrs], %[g3] offen sc1\nbuffer_load_dwordx4 v[136:139], %[l16], %[xrs], %[g4] offen sc1\ns_branch .LP2_%=\n.LR3_%=:\ns_sub_u32 %[cap], %[cap], 1\n"
        "s_cbranch_scc0 .LQ3_%=\ns_mov_b32 %[cap], 0\ns_branch .LK3_%=\n.LQ3_%=:\ns_sleep %[xp]\nbuffer_load_dwordx4 v[148:151], %[l16], %[xrs], %[g3] offen sc1\n"
        "buffer_load_dwordx4 v[136:139], %[l16], %[xrs], %[g4] offen sc1\nbuffer_load_dwordx4 v[140:143], %[l16], %[xrs], %[g5] offen sc1\ns_branch .LP3_%=\n.LR4_%=:\ns_sub_u32 %[cap], %[cap], 1\n"
        "s_cbranch_scc0 .LQ4_%=\ns_mov_b32 %[cap], 0\ns_branch .LK4_%=\n.LQ4_%=:\ns_sleep %[xp]\nbuffer_load_dwordx4 v[136:139], %[l16], %[xrs], %[g4] offen sc1\n"
        "buffer_load_dwordx4 v[140:143], %[l16], %[xrs], %[g5] offen sc1\nbuffer_load_dwordx4 v[144:147], %[l16], %[xrs], %[g6] offen sc1\ns_branch .LP4_%=\n.LR5_%=:\ns_sub_u32 %[cap], %[cap], 1\n"
        "s_cbranch_scc0 .LQ5_%=\ns_mov_b32 %[cap], 0\ns_branch .LK5_%=\n.LQ5_%=:\ns_sleep %[xp]\nbuffer_load_dwordx4 v[140:143], %[l16], %[xrs], %[g5] offen sc1\n"
        "buffer_load_dwordx4 v[144:147], %[l16], %[xrs], %[g6] offen sc1\nbuffer_load_dwordx4 v[148:151], %[l16], %[xrs], %[g7] offen sc1\ns_branch .LP5_%=\n.LR6_%=:\ns_sub_u32 %[cap], %[cap], 1\n"
        "s_cbranch_scc0 .LQ6_%=\ns_mov_b32 %[cap], 0\ns_branch .LK6_%=\n.LQ6_%=:\ns_sleep %[xp]\nbuffer_load_dwordx4 v[144:147], %[l16], %[xrs], %[g6] offen sc1\n"
        "buffer_load_dwordx4 v[148:151], %[l16], %[xrs], %[g7] offen sc1\nbuffer_load_dwordx4 v[136:139], %[l16], %[xrs], %[g8] offen sc1\ns_branch .LP6_%=\n.LR7_%=:\ns_sub_u32 %[cap], %[cap], 1\n"
        "s_cbranch_scc0 .LQ7_%=\ns_mov_b32 %[cap], 0\ns_branch .LK7_%=\n.LQ7_%=:\ns_sleep %[xp]\nbuffer_load_dwordx4 v[148:151], %[l16], %[xrs], %[g7] offen sc1\n"
        "buffer_load_dwordx4 v[136:139], %[l16], %[xrs], %[g8] offen sc1\nbuffer_load_dwordx4 v[140:143], %[l16], %[xrs], %[g9] offen sc1\ns_branch .LP7_%=\n.LR8_%=:\ns_sub_u32 %[cap], %[cap], 1\n"
        "s_cbranch_scc0 .LQ8_%=\ns_mov_b32 %[cap], 0\ns_branch .LK8_%=\n.LQ8_%=:\ns_sleep %[xp]\nbuffer_load_dwordx4 v[136:139], %[l16], %[xrs], %[g8] offen sc1\n"
        "buffer_load_dwordx4 v[140:143], %[l16], %[xrs], %[g9] offen sc1\nbuffer_load_dwordx4 v[144:147], %[l16], %[xrs], %[g10] offen sc1\ns_branch .LP8_%=\n.LR9_%=:\ns_sub_u32 %[cap], %[cap], 1\n"
        "s_cbranch_scc0 .LQ9_%=\ns_mov_b32 %[cap], 0\ns_branch .LK9_%=\n.LQ9_%=:\ns_sleep %[xp]\nbuffer_load_dwordx4 v[140:143], %[l16], %[xrs], %[g9] offen sc1\n"
        "buffer_load_dwordx4 v[144:147], %[l16], %[xrs], %[g10] offen sc1\nbuffer_load_dwordx4 v[148:151], %[l16], %[xrs], %[g11] offen sc1\ns_branch .LP9_%=\n.LR10_%=:\ns_sub_u32 %[cap], %[cap], 1\n"
        "s_cbranch_scc0 .LQ10_%=\ns_mov_b32 %[cap], 0\ns_branch .LK10_%=\n.LQ10_%=:\ns_sleep %[xp]\nbuffer_load_dwordx4 v[144:147], %[l16], %[xrs], %[g10] offen sc1\n"
        "buffer_load_dwordx4 v[148:151], %[l16], %[xrs], %[g11] offen sc1\nbuffer_load_dwordx4 v[136:139], %[l16], %[xrs], %[g12] offen sc1\ns_branch .LP10_%=\n.LR11_%=:\ns_sub_u32 %[cap], %[cap], 1\n"
        "s_cbranch_scc0 .LQ11_%=\ns_mov_b32 %[cap], 0\ns_branch .LK11_%=\n.LQ11_%=:\ns_sleep %[xp]\nbuffer_load_dwordx4 v[148:151], %[l16], %[xrs], %[g11] offen sc1\n"
        "buffer_load_dwordx4 v[136:139], %[l16], %[xrs], %[g12] offen sc1\ns_branch .LP11_%=\n.LR12_%=:\ns_sub_u32 %[cap], %[cap], 1\ns_cbranch_scc0 .LQ12_%=\ns_mov_b32 %[cap], 0\ns_branch .LK12_%=\n"
        ".LQ12_%=:\ns_sleep %[xp]\nbuffer_load_dwordx4 v[136:139], %[l16], %[xrs], %[g12] offen sc1\ns_branch .LP12_%=\n.LE%=:\n"
        : [tm] "=&v"(tm), [lh] "+v"(lh), [cap] "=&s"(cap), [s0] "=&s"(s0_), [s1] "=&s"(s1_)
        : [l16] "v"(l16), [xrs] "s"(xrs), [g0] "s"(go[0]), [g1] "s"(go[1]), [g2] "s"(go[2]), [g3] "s"(go[3]), [g4] "s"(go[4]), [g5] "s"(go[5]), [g6] "s"(go[6]), [g7] "s"(go[7]), [g8] "s"(go[8]), [g9] "s"(go[9]), [g10] "s"(go[10]), [g11] "s"(go[11]), [g12] "s"(go[12]), [lm0] "v"(lm0), [lm1] "v"(lm1), [sy] "v"(sya), [syh] "v"(syh), [one] "v"(one), [j] "s"(j), [xsa] "v"(xsa), [ns] "s"(ns), [ones] "v"(onesf), [m1] "s"(m1), [m2] "s"(m2), [xso] "v"(xso), [srs] "s"(xsrs), [tg0] "s"(tg0), [tq1] "s"(tq1), [ox] "i"(SY_X), [xp] "i"(XPOLL)
        : "memory", "scc", "vcc", "v136", "v137", "v138", "v139", "v140", "v141", "v142", "v143", "v144", "v145", "v146", "v147", "v148", "v149", "v150", "v151", "v152", "v153", "v154", "v155", "v156", "v157", "v158", "v159", "v160", "v161", "v162", "v163", "v164", "v165", "v166", "v167");
    RETRY(8, 0x10000 - cap);
    ST(3);
  } else {
    ST(5);
    lds_poll<SY_X + 4, 2>(sya, 1);
  }
  __builtin_amdgcn_s_setprio(PTW);
  u32x2 rv[3];
  {
    STX(8);
    {
      const int rr_ = 4 * (g & 1) + ((lane >> 2) & 3), pr_ = 4 * (l15 >> 3) + (l15 & 3) + 4 * (l15 & 4);
      const u32 ro = LDSA(At) + (layer ? (mt2 + 1) * 1056 + pr_ * 32 + 8 * g : (2 * mt2 + 2 + (g >> 1)) * 1024 + rr_ * 128 + ((rr_ & 6) << 4) + (lane & 3) * 8);
      if (layer) {
        lds_poll<SY_X, 1>(sya, 10); lds_poll<SY_X + 4, 1>(sya, 10);
      }
#pragma unroll
      for (int i = 0; i < 3; ++i) {
        const int kr = hg * 80 + 16 * (2 * kh + i) - 64 * rot;
        if (layer) asm volatile("ds_read_b64 %0, %1" : "=v"(rv[i]) : "v"(ro + (kr >> 5) * 5280 + (kr & 16) * 16) : "memory");
        else asm volatile("ds_read_b64_tr_b16 %0, %1" : "=v"(rv[i]) : "v"((ro ^ ((kr & 48) * 2)) + (kr >> 6) * 10240) : "memory");
      }
      asm volatile("s_waitcnt lgkmcnt(0)" : "+v"(rv[0]), "+v"(rv[1]), "+v"(rv[2]));
      if (kh) rv[0] = (u32x2){0u, 0u};
      lds_inc<SY_RV>(sya, one);
      STX(9);
    }
    if (!layer) {
    if (w == MKW) {
      const __amdgpu_buffer_rsrc_t mrs = __builtin_amdgcn_make_buffer_rsrc(obuf, 0, HO_BYTES, 0x00020000);
      const __amdgpu_buffer_rsrc_t irs = __builtin_amdgcn_make_buffer_rsrc(ws + WS_IM1 + b * 12800, 0, 12800, 0x00020000);
      __builtin_amdgcn_raw_buffer_store_b128(MARK16, mrs, lane < 16 ? (lane >> 1) * 8192 + (lane & 1) * 16 : -1, HO_FS + (s0 + 8 * hg) * 4, 17);
      _Pragma("unroll 1")
      for (int i = 0; i < 10; ++i) {
        __builtin_amdgcn_raw_buffer_store_b128(MARK16, mrs, (lane >> 3) * (XP * 2) + (lane & 7) * 16, HO_X1 + (hg * 80 + 8 * i) * (XP * 2) + s0 * 2, 17);
        __builtin_amdgcn_s_sleep(MSL1);
      }
      _Pragma("unroll 1")
      for (int i = 0; i < 13; ++i) {
        __builtin_amdgcn_raw_buffer_store_b128(MARK16, irs, lane * 16, i * 1024, 17);
        __builtin_amdgcn_s_sleep(MSL2);
      }
      VMCNT(0);
      if (lane == 0) __builtin_amdgcn_raw_buffer_store_b64((u32x2){tg0, tg1}, frs, 0, FL_M2 + b * 8, 16);
      ST(1);
    }
      if (w == CWW) {
        const __amdgpu_buffer_rsrc_t crs = __builtin_amdgcn_make_buffer_rsrc(ws + WS_CW + hg * 20480, 0, (EXP & 4) ? 0 : 20480, 0x00020000);
        pg_poll<4>(sya, LDSA(smem) + SY_OFF + SY_X + CWAT);
        f32x4 cv[20];
#pragma unroll
        for (int i = 0; i < 20; ++i) cv[i] = __builtin_bit_cast(f32x4, __builtin_amdgcn_raw_buffer_load_b128(crs, min(lane, 59) * 16, i * 1024, 16));
#pragma unroll
        for (int i = 0; i < 10; ++i) cv[i] += cv[i + 10];
        const f32x4 cs = ((cv[0] + cv[1]) + (cv[2] + cv[3])) + ((cv[4] + cv[5]) + (cv[6] + cv[7])) + (cv[8] + cv[9]);
        asm volatile("ds_write_b128 %0, %1" :: "v"(LDSA(smem) + CW_OFF + lane * 16), "v"(cs) : "memory");
        lds_inc<SY_STAT>(sya, one);
      }
      if (w == 7) {
        const int fb = lane < 8 ? (b & 62) + (lane & 1) + 64 * (lane >> 1) : 256 + 16 * ((lane >> 1) & 7) + 2 * hg + (lane & 1);
        pg_poll<8>(sya, LDSA(smem) + SY_OFF + SY_X + MKAT);
        int spins = 0;
        _Pragma("unroll 1")
        while (true) {
          asm volatile("" ::: "memory");
          const u32x2 f = __builtin_amdgcn_raw_buffer_load_b64(frs, lane < 24 ? fb * 64 : b * 64, FL_M, 16);
          if (__builtin_amdgcn_ballot_w64(f[0] != tg0 || f[1] != tg1) == 0 || ++spins > GCAP) break;
          __builtin_amdgcn_s_sleep(8);
        }
        RETRY(15, spins);
        lds_inc<SY_STAT>(sya, one);
        lds_inc<SY_MK>(sya, one);
      }
    }
    if ((j & 3) == 2) {
      const int xo = j == 2 ? rb * 8192 + l16 : (rb ? rb - 1 : 0) * 8192 + 768 + l15 * 16;
      pg_poll<4>(sya, LDSA(smem) + SY_OFF + 256 * layer + SY_X + XSAT);
      __builtin_amdgcn_s_setprio(3);
      u32x4 pv[8];
      int spins = 0;
      _Pragma("unroll 1")
      while (true) {
        asm volatile("" ::: "memory");
        u32 m = 0;
#pragma unroll
        for (int i = 0; i < 8; ++i) pv[i] = __builtin_amdgcn_raw_buffer_load_b128(xsrs, xo, i * 1024, 16);
#pragma unroll
        for (int i = 0; i < 8; ++i) m |= (pv[i][1] ^ tg0) | (pv[i][3] ^ tq1);
        if (__builtin_amdgcn_ballot_w64(m != 0) == 0 || ++spins > GCAP) break;
        __builtin_amdgcn_s_sleep(8);
      }
      RETRY(15, spins);
      float sv[2];
#pragma unroll
      for (int i = 0; i < 2; ++i)
        sv[i] = ((__uint_as_float(pv[0][2 * i]) + __uint_as_float(pv[1][2 * i])) + (__uint_as_float(pv[2][2 * i]) + __uint_as_float(pv[3][2 * i]))) +
                ((__uint_as_float(pv[4][2 * i]) + __uint_as_float(pv[5][2 * i])) + (__uint_as_float(pv[6][2 * i]) + __uint_as_float(pv[7][2 * i])));
      asm volatile("ds_write_b128 %0, %1" :: "v"(LDSA(smem) + A_R2 + (j == 2 ? 256 + l16 : l15 * 16)), "v"(ln_fin(sv[0], sv[1])) : "memory");
      lds_inc<SY_STAT>(sya, one);
      __builtin_amdgcn_s_setprio(PTW);
      ST(1);
    }
  }
  const __amdgpu_buffer_rsrc_t orsrc = __builtin_amdgcn_make_buffer_rsrc(obuf + (size_t)rb * 40960, 0, 81920, 0x00020000);
  u32 qka = LDSA(St) + ((16 * qt + l15) * X1_P + 160 * grp + 8 * g) * 2;
  u32 vta = LDSA(St) + ((16 * qt + 4 * g + ((lane >> 2) & 3)) * X1_P + 160 * grp + 40 + 4 * (lane & 3)) * 2;
  const bool par = l15 & 1;
  const int kk0 = 4 * g + (l15 & 1), kk1 = kk0 + 2;
  const bool c0 = kk0 >= l15 + 2, c1 = kk1 >= l15 + 2;
  const u32 e0 = par ? 0x01000c0cu : 0x0c0c0100u, e1 = par ? 0x03020c0cu : 0x0c0c0302u;
  u32 sA0 = c0 ? e0 : 0x0c0c0c0cu, sB0 = c0 ? 0x0c0c0c0cu : e0;
  u32 sA1 = c1 ? e1 : 0x0c0c0c0cu, sB1 = c1 ? 0x0c0c0c0cu : e1;
  u32 oha = g * 10240 + l15 * 16, qa[4];
#pragma unroll
  for (int i = 0; i < 4; ++i) { qa[i] = g >= 3 - (i & 1) ? sya + SY_Z : qka + (32 * X1_P + 160 - 240 * grp) + 40 * (i >> 1) + 8 * (i & 1); PIN(qa[i]); }
  const float nb = s0 + qt ? 0.f : -1e30f;
  f32x4 bias = {nb, nb, nb, nb};
  int ohs = g ? -1 : l16;
  u64 c0m = __builtin_amdgcn_ballot_w64(c0), c1m = __builtin_amdgcn_ballot_w64(c1);
  int so0 = (hg * 4 + qt) * 2560 + grp * 1024, so1 = (hg * 4 + qt) * 2560 + 2048 + grp * 256;
  float nc = -0.22360679774997896f * 1.4426950408889634f;
  asm volatile("" : "+s"(c0m), "+s"(c1m), "+s"(so0), "+s"(so1), "+s"(nc));
  PIN(qka); PIN(vta); PIN(sA0); PIN(sA1); PIN(sB0); PIN(sB1); PIN(l16); PIN(oha); PIN(sya); PIN(one); PIN(bias); PIN(ohs);
  PIN(rv[0]); PIN(rv[1]); PIN(rv[2]);
  STX(10);
  u32x4 wv[2][7];
  u32 wd = LDSA(At) + j * 1024 + l16;
  if (P0E == 2) {
    const __amdgpu_buffer_rsrc_t wos = __builtin_amdgcn_make_buffer_rsrc((void*)(wot + (size_t)hg * 51200 + j * 512), 0, 102400 - j * 1024, 0x00020000);
    PIN(wd);
    lds_poll<SY_RV, 2>(sya, 8);
    if (!layer) lds_poll<SY_MK, 2>(sya, 1);
    pg_poll<2>(sya, LDSA(smem) + SY_OFF + 256 * layer + SY_X + WOAT);
    __builtin_amdgcn_s_sleep(WOSL);
    STX(11);
#pragma unroll
    for (int p = 0; p < 2; ++p)
#pragma unroll
      for (int i = 0; i < 7; ++i) wv[p][i] = __builtin_amdgcn_raw_buffer_load_b128(wos, l16, (i == 6 && j >= 2) ? 102400 : p * 51200 + i * 8192, 16);
    asm volatile("" ::: "memory");
#pragma unroll
    for (int i = 0; i < 7; ++i) if (i < 6 || j < 2) asm volatile("ds_write_b128 %0, %1 offset:%2" :: "v"(wd), "v"(wv[0][i]), "i"(i * 8192) : "memory");
    lds_inc<SY_WO>(sya, one);
    wd += WO1_OFF;
    PIN(wd);
  } else if (P0E && (j & 1)) {
    const u16* sp = wot + (size_t)hg * 51200 + (j >> 1) * 512 + lane * 8;
    lds_poll<SY_RV, 2>(sya, 8);
    if (!layer) lds_poll<SY_MK, 2>(sya, 1);
    pg_poll<2>(sya, LDSA(smem) + SY_OFF + 256 * layer + SY_X + WOAT);
    STX(11);
#pragma unroll
    for (int i = 0; i < 13; ++i) if (i < 12 || j < 4) __builtin_amdgcn_global_load_lds((const u32*)(sp + i * 2048), (u32*)(At + (4 * i + (j >> 1)) * 512), 16, 0, 16);
    __builtin_amdgcn_s_waitcnt(0x0070);
    lds_inc<SY_WO>(sya, one);
  } else pg_poll<4>(sya, LDSA(smem) + SY_OFF + 256 * layer + SY_X + 32);
  if (grp) { pg_poll<2>(sya, LDSA(smem) + SY_OFF + 256 * layer + SY_X + 44); __builtin_amdgcn_s_setprio(PTA1); } else __builtin_amdgcn_s_setprio(PTA);
  STX(12);
  pg_poll<APS>(sya, LDSA(smem) + SY_OFF + 256 * layer + SY_X + 44 + 4 * grp);
  if (layer) ST(5);
      asm volatile(
        "ds_read_b64 v[30:31], %[q0]\nds_read_b64 v[32:33], %[q1]\nds_read_b64 v[38:39], %[ka] offset:0\nds_read_b64 v[40:41], %[ka] offset:8\nds_read_b64 v[42:43], %[ka] offset:7936\n"
        "ds_read_b64 v[44:45], %[ka] offset:7944\nds_read_b64 v[34:35], %[q2]\nds_read_b64 v[36:37], %[q3]\nds_read_b64 v[46:47], %[ka] offset:40\nds_read_b64 v[48:49], %[ka] offset:48\n"
        "ds_read_b64 v[50:51], %[ka] offset:7976\nds_read_b64 v[52:53], %[ka] offset:7984\nds_read_b64_tr_b16 v[54:55], %[va] offset:0\nds_read_b64_tr_b16 v[56:57], %[va] offset:7936\n"
        "ds_read_b64_tr_b16 v[58:59], %[va] offset:32\nds_read_b64_tr_b16 v[60:61], %[va] offset:7968\nds_read_b64_tr_b16 v[62:63], %[va] offset:40\nds_read_b64_tr_b16 v[64:65], %[va] offset:7976\n"
        "ds_read_b64_tr_b16 v[66:67], %[va] offset:72\nds_read_b64_tr_b16 v[68:69], %[va] offset:8008\ns_waitcnt lgkmcnt(14)\nv_mfma_f32_16x16x32_bf16 v[70:73], v[38:41], v[30:33], %[bias]\n"
        "v_mfma_f32_16x16x32_bf16 v[74:77], v[42:45], v[30:33], 0\ns_waitcnt lgkmcnt(8)\nv_mfma_f32_16x16x32_bf16 v[78:81], v[46:49], v[34:37], %[bias]\n"
        "v_mfma_f32_16x16x32_bf16 v[82:85], v[50:53], v[34:37], 0\ns_nop 4\nv_cndmask_b32 v70, v70, v71, %[par]\nv_cndmask_b32 v72, v72, v73, %[par]\nv_cndmask_b32 v74, v74, v75, %[par]\n"
        "v_cndmask_b32 v76, v76, v77, %[par]\nv_cndmask_b32 v30, v74, v70, %[c0]\nv_cndmask_b32 v31, v76, v72, %[c1]\nv_max_f32 v34, v30, v31\nv_cndmask_b32 v78, v78, v79, %[par]\n"
        "v_cndmask_b32 v80, v80, v81, %[par]\nv_cndmask_b32 v82, v82, v83, %[par]\nv_cndmask_b32 v84, v84, v85, %[par]\nv_cndmask_b32 v32, v82, v78, %[c0]\nv_cndmask_b32 v33, v84, v80, %[c1]\n"
        "v_max_f32 v35, v32, v33\ns_nop 1\nv_permlane16_swap_b32 v34, v35\nv_max_f32 v34, v34, v35\nv_mov_b32 v35, v34\ns_nop 1\nv_permlane32_swap_b32 v34, v35\nv_max_f32 v34, v34, v35\n"
        "v_mul_f32 v35, %[nc], v34\nv_mul_f32 v34, %[nc], v34\ns_nop 1\nv_permlane16_swap_b32 v34, v35\nv_fma_f32 v30, -v30, %[nc], v34\nv_fma_f32 v31, -v31, %[nc], v34\n"
        "v_fma_f32 v32, -v32, %[nc], v35\nv_fma_f32 v33, -v33, %[nc], v35\nv_exp_f32 v30, v30\nv_exp_f32 v31, v31\nv_exp_f32 v32, v32\nv_exp_f32 v33, v33\nv_add_f32 v34, v30, v31\n"
        "v_add_f32 v35, v32, v33\ns_nop 1\nv_permlane16_swap_b32 v34, v35\nv_add_f32 v34, v34, v35\nv_mov_b32 v35, v34\ns_nop 1\nv_permlane32_swap_b32 v34, v35\nv_add_f32 v34, v34, v35\n"
        "v_rcp_f32 v34, v34\ns_nop 0\nv_mov_b32 v35, v34\ns_nop 1\nv_permlane16_swap_b32 v34, v35\nv_mul_f32 v30, v30, v34\nv_mul_f32 v31, v31, v34\nv_mul_f32 v32, v32, v35\nv_mul_f32 v33, v33, v35\n"
        "v_cvt_pk_bf16_f32 v30, v30, v31\nv_cvt_pk_bf16_f32 v32, v32, v33\nv_perm_b32 v38, v30, v30, %[sa0]\nv_perm_b32 v39, v30, v30, %[sa1]\nv_perm_b32 v40, v30, v30, %[sb0]\n"
        "v_perm_b32 v41, v30, v30, %[sb1]\nv_perm_b32 v42, v32, v32, %[sa0]\nv_perm_b32 v43, v32, v32, %[sa1]\nv_perm_b32 v44, v32, v32, %[sb0]\nv_perm_b32 v45, v32, v32, %[sb1]\ns_waitcnt lgkmcnt(0)\n"
        "v_mfma_f32_16x16x32_bf16 v[70:73], v[54:57], v[38:41], 0\nv_mfma_f32_16x16x32_bf16 v[78:81], v[62:65], v[42:45], 0\nv_mfma_f32_16x16x32_bf16 v[74:77], v[58:61], v[38:41], 0\n"
        "v_mfma_f32_16x16x32_bf16 v[82:85], v[66:69], v[42:45], 0\ns_nop 4\nv_cvt_pk_bf16_f32 v46, v70, v71\nv_cvt_pk_bf16_f32 v47, v72, v73\nv_cvt_pk_bf16_f32 v48, v78, v79\n"
        "v_cvt_pk_bf16_f32 v49, v80, v81\nv_min_u32 v46, -2, v46\nv_min_u32 v47, -2, v47\nv_min_u32 v48, -2, v48\nv_min_u32 v49, -2, v49\n"
        "buffer_store_dwordx4 v[46:49], %[l16], %[ors], %[so0] offen sc1\nv_cvt_pk_bf16_f32 v50, v74, v75\nv_cvt_pk_bf16_f32 v51, v76, v77\nv_cvt_pk_bf16_f32 v52, v82, v83\n"
        "v_cvt_pk_bf16_f32 v53, v84, v85\nv_min_u32 v50, -2, v50\nv_min_u32 v51, -2, v51\nv_min_u32 v52, -2, v52\nv_min_u32 v53, -2, v53\n"
        "buffer_store_dwordx4 v[50:53], %[oh], %[ors], %[so1] offen sc1\ns_nop 1\n"
        :
        : [q0] "v"(qa[0]), [q1] "v"(qa[1]), [q2] "v"(qa[2]), [q3] "v"(qa[3]), [ka] "v"(qka), [va] "v"(vta), [bias] "v"(bias), [par] "s"(0xaaaaaaaaaaaaaaaaull), [c0] "s"(c0m), [c1] "s"(c1m), [nc] "s"(nc), [sa0] "v"(sA0), [sa1] "v"(sA1), [sb0] "v"(sB0), [sb1] "v"(sB1), [l16] "v"(l16), [oh] "v"(ohs), [ors] "s"(orsrc), [so0] "s"(so0), [so1] "s"(so1)
        : "memory", "scc", "v30", "v31", "v32", "v33", "v34", "v35", "v36", "v37", "v38", "v39", "v40", "v41", "v42", "v43", "v44", "v45", "v46", "v47", "v48", "v49", "v50", "v51", "v52", "v53", "v54", "v55", "v56", "v57", "v58", "v59", "v60", "v61", "v62", "v63", "v64", "v65", "v66", "v67", "v68", "v69", "v70", "v71", "v72", "v73", "v74", "v75", "v76", "v77", "v78", "v79", "v80", "v81", "v82", "v83", "v84", "v85");
  ST(6);
  if (P0E == 2) {
#pragma unroll
    for (int i = 0; i < 7; ++i) if (i < 6 || j < 2) asm volatile("ds_write_b128 %0, %1 offset:%2" :: "v"(wd), "v"(wv[1][i]), "i"(i * 8192) : "memory");
    lds_inc<SY_WO + 4>(sya, one);
  }
  one = 1u; sya = LDSA(smem) + SY_OFF + 256 * layer;
  u32 rw = LDSA(smem) + A_R1 + j * 3072 + l16, fw = sya + SY_RED + 4 * j, f0 = 1 - kh;
  u32 xo_ = LDSA(smem + X1T_OFF) + (32 * kh + l15) * 144 + 32 * mt2 + 8 * g;
  u32 xi_ = LDSA(smem + X1T_OFF) + (tt >> 3) * 144 + (tt & 7) * 16;
  const __amdgpu_buffer_rsrc_t x1rs = __builtin_amdgcn_make_buffer_rsrc(xtout + (size_t)hg * 80 * XP + s0, 0, 80 * XP * 2, 0x00020000);
  int xg_ = (tt >> 3) * (XP * 2) + (tt & 7) * 16;
  const __amdgpu_buffer_rsrc_t srsrc = __builtin_amdgcn_make_buffer_rsrc(stats + (size_t)s0 * 16, 0, 8192, 0x00020000);
  u32 rr = LDSA(smem) + A_R1 + (j ^ 4) * 3072 + l16 - 1024 * kh;
  int so_ = (l15 & 3) ? -1 : (lane >> 2) * 8;
  {
    const int ob = (16 * kh + mt2) * 2560;
    u32 bb = LDSA(At) + kh * 25600 + l16, tm;
    int cap, s0_, s1_, rt = 0;
      asm volatile(
        "s_mov_b32 %[cap], 0x10000\ns_cmp_eq_u32 %[kh], 0\ns_cbranch_scc1 .LS0_%=\ns_sleep %[sl1]\nbuffer_load_dwordx4 v[28:31], %[l16], %[ors], %[o0] offen offset:0 sc1\n"
        "buffer_load_dwordx4 v[32:35], %[l16], %[ors], %[o1] offen offset:0 sc1\nbuffer_load_dwordx4 v[36:39], %[l16], %[ors], %[o2] offen offset:0 sc1\n"
        "buffer_load_dwordx4 v[40:43], %[l16], %[ors], %[o3] offen offset:0 sc1\nbuffer_load_dwordx4 v[44:47], %[oha], %[ors], %[o4] offen offset:0 sc1\n.LS0_%=:\ns_mov_b32 %[s1], 0x100000\n.LWa%=:\n"
        "ds_read_b32 %[tm], %[sy] offset:%[ow1]-4\ns_waitcnt lgkmcnt(0)\nv_readfirstlane_b32 %[s0], %[tm]\ns_nop 0\ns_cmp_ge_i32 %[s0], %[nwo]\ns_cbranch_scc1 .LXa%=\ns_sleep 1\n"
        "s_sub_u32 %[s1], %[s1], 1\ns_cbranch_scc0 .LWa%=\n.LXa%=:\nds_read_b128 v[68:71], %[bb] offset:0\nds_read_b128 v[72:75], %[bb] offset:1024\nds_read_b128 v[76:79], %[bb] offset:2048\n"
        "ds_read_b128 v[80:83], %[bb] offset:3072\nds_read_b128 v[84:87], %[bb] offset:4096\nds_read_b128 v[88:91], %[bb] offset:5120\nds_read_b128 v[92:95], %[bb] offset:6144\n"
        "ds_read_b128 v[96:99], %[bb] offset:7168\nds_read_b128 v[100:103], %[bb] offset:8192\nds_read_b128 v[104:107], %[bb] offset:9216\nds_read_b128 v[108:111], %[bb] offset:10240\n"
        "ds_read_b128 v[112:115], %[bb] offset:11264\nds_read_b128 v[116:119], %[bb] offset:12288\nds_read_b128 v[120:123], %[bb] offset:13312\nds_read_b128 v[124:127], %[bb] offset:14336\n"
        "ds_read_b128 v[128:131], %[bb] offset:15360\nds_read_b128 v[132:135], %[bb] offset:16384\nds_read_b128 v[136:139], %[bb] offset:17408\nds_read_b128 v[140:143], %[bb] offset:18432\n"
        "ds_read_b128 v[144:147], %[bb] offset:19456\nds_read_b128 v[148:151], %[bb] offset:20480\nds_read_b128 v[152:155], %[bb] offset:21504\nds_read_b128 v[156:159], %[bb] offset:22528\n"
        "ds_read_b128 v[160:163], %[bb] offset:23552\nds_read_b128 v[164:167], %[bb] offset:24576\ns_mov_b64 exec, 1\nds_add_u32 %[sy], %[one] offset:%[ofr]\ns_mov_b64 exec, -1\n"
        "v_add_u32 %[bb], %[wo1], %[bb]\nv_lshlrev_b32 v48, 16, v22\nv_and_b32 v49, 0xffff0000, v22\nv_lshlrev_b32 v50, 16, v23\nv_and_b32 v51, 0xffff0000, v23\nv_lshlrev_b32 v52, 16, v24\n"
        "v_and_b32 v53, 0xffff0000, v24\nv_lshlrev_b32 v54, 16, v25\nv_and_b32 v55, 0xffff0000, v25\nv_lshlrev_b32 v56, 16, v26\nv_and_b32 v57, 0xffff0000, v26\nv_lshlrev_b32 v58, 16, v27\n"
        "v_and_b32 v59, 0xffff0000, v27\ns_cmp_eq_u32 %[kh], 0\ns_cbranch_scc0 .LS1_%=\ns_sleep %[sl0]\nbuffer_load_dwordx4 v[28:31], %[l16], %[ors], %[o0] offen offset:0 sc1\n"
        "buffer_load_dwordx4 v[32:35], %[l16], %[ors], %[o1] offen offset:0 sc1\nbuffer_load_dwordx4 v[36:39], %[l16], %[ors], %[o2] offen offset:0 sc1\n"
        "buffer_load_dwordx4 v[40:43], %[l16], %[ors], %[o3] offen offset:0 sc1\nbuffer_load_dwordx4 v[44:47], %[oha], %[ors], %[o4] offen offset:0 sc1\n.LS1_%=:\ns_mov_b32 %[s1], 0x100000\n.LWb%=:\n"
        "ds_read_b32 %[tm], %[sy] offset:%[ow1]\ns_waitcnt lgkmcnt(0)\nv_readfirstlane_b32 %[s0], %[tm]\ns_nop 0\ns_cmp_ge_i32 %[s0], %[nwo]\ns_cbranch_scc1 .LXb%=\ns_sleep 1\n"
        "s_sub_u32 %[s1], %[s1], 1\ns_cbranch_scc0 .LWb%=\n.LXb%=:\n.LC0_%=:\ns_waitcnt vmcnt(4)\nv_max_u32 %[tm], v28, v29\nv_max3_u32 %[tm], %[tm], v30, v31\ns_waitcnt vmcnt(3)\n"
        "v_max3_u32 %[tm], %[tm], v32, v33\nv_max3_u32 %[tm], %[tm], v34, v35\ns_waitcnt vmcnt(2)\nv_max3_u32 %[tm], %[tm], v36, v37\nv_max3_u32 %[tm], %[tm], v38, v39\ns_waitcnt vmcnt(1)\n"
        "v_max3_u32 %[tm], %[tm], v40, v41\nv_max3_u32 %[tm], %[tm], v42, v43\ns_waitcnt vmcnt(0)\nv_max3_u32 %[tm], %[tm], v44, v45\nv_max3_u32 %[tm], %[tm], v46, v47\nv_cmp_eq_u32 vcc, -1, %[tm]\n"
        "s_cbranch_vccnz .LR0_%=\n.LK0_%=:\nv_mfma_f32_16x16x32_bf16 v[68:71], v[28:31], v[68:71], 0\nv_mfma_f32_16x16x32_bf16 v[72:75], v[28:31], v[72:75], 0\n"
        "v_mfma_f32_16x16x32_bf16 v[76:79], v[28:31], v[76:79], v[48:51]\nv_mfma_f32_16x16x32_bf16 v[80:83], v[28:31], v[80:83], v[52:55]\n"
        "v_mfma_f32_16x16x32_bf16 v[84:87], v[28:31], v[84:87], v[56:59]\nbuffer_load_dwordx4 v[48:51], %[l16], %[ors], %[o0] offen offset:1024 sc1\n"
        "buffer_load_dwordx4 v[52:55], %[l16], %[ors], %[o1] offen offset:1024 sc1\nbuffer_load_dwordx4 v[56:59], %[l16], %[ors], %[o2] offen offset:1024 sc1\n"
        "buffer_load_dwordx4 v[60:63], %[l16], %[ors], %[o3] offen offset:1024 sc1\nbuffer_load_dwordx4 v[64:67], %[oha], %[ors], %[o4] offen offset:256 sc1\n"
        "v_mfma_f32_16x16x32_bf16 v[68:71], v[32:35], v[88:91], v[68:71]\nds_read_b128 v[88:91], %[bb] offset:0\nv_mfma_f32_16x16x32_bf16 v[72:75], v[32:35], v[92:95], v[72:75]\n"
        "ds_read_b128 v[92:95], %[bb] offset:5120\nv_mfma_f32_16x16x32_bf16 v[76:79], v[32:35], v[96:99], v[76:79]\nds_read_b128 v[96:99], %[bb] offset:10240\n"
        "v_mfma_f32_16x16x32_bf16 v[80:83], v[32:35], v[100:103], v[80:83]\nds_read_b128 v[100:103], %[bb] offset:15360\nv_mfma_f32_16x16x32_bf16 v[84:87], v[32:35], v[104:107], v[84:87]\n"
        "ds_read_b128 v[104:107], %[bb] offset:20480\nv_mfma_f32_16x16x32_bf16 v[68:71], v[36:39], v[108:111], v[68:71]\nds_read_b128 v[108:111], %[bb] offset:1024\n"
        "v_mfma_f32_16x16x32_bf16 v[72:75], v[36:39], v[112:115], v[72:75]\nds_read_b128 v[112:115], %[bb] offset:6144\nv_mfma_f32_16x16x32_bf16 v[76:79], v[36:39], v[116:119], v[76:79]\n"
        "ds_read_b128 v[116:119], %[bb] offset:11264\nv_mfma_f32_16x16x32_bf16 v[80:83], v[36:39], v[120:123], v[80:83]\nds_read_b128 v[120:123], %[bb] offset:16384\n"
        "v_mfma_f32_16x16x32_bf16 v[84:87], v[36:39], v[124:127], v[84:87]\nds_read_b128 v[124:127], %[bb] offset:21504\nv_mfma_f32_16x16x32_bf16 v[68:71], v[40:43], v[128:131], v[68:71]\n"
        "ds_read_b128 v[128:131], %[bb] offset:2048\nv_mfma_f32_16x16x32_bf16 v[72:75], v[40:43], v[132:135], v[72:75]\nds_read_b128 v[132:135], %[bb] offset:7168\n"
        "v_mfma_f32_16x16x32_bf16 v[76:79], v[40:43], v[136:139], v[76:79]\nds_read_b128 v[136:139], %[bb] offset:12288\nv_mfma_f32_16x16x32_bf16 v[80:83], v[40:43], v[140:143], v[80:83]\n"
        "ds_read_b128 v[140:143], %[bb] offset:17408\nv_mfma_f32_16x16x32_bf16 v[84:87], v[40:43], v[144:147], v[84:87]\nds_read_b128 v[144:147], %[bb] offset:22528\n"
        "v_mfma_f32_16x16x32_bf16 v[68:71], v[44:47], v[148:151], v[68:71]\nds_read_b128 v[148:151], %[bb] offset:3072\nv_mfma_f32_16x16x32_bf16 v[72:75], v[44:47], v[152:155], v[72:75]\n"
        "ds_read_b128 v[152:155], %[bb] offset:8192\nv_mfma_f32_16x16x32_bf16 v[76:79], v[44:47], v[156:159], v[76:79]\nds_read_b128 v[156:159], %[bb] offset:13312\n"
        "v_mfma_f32_16x16x32_bf16 v[80:83], v[44:47], v[160:163], v[80:83]\nds_read_b128 v[160:163], %[bb] offset:18432\nv_mfma_f32_16x16x32_bf16 v[84:87], v[44:47], v[164:167], v[84:87]\n"
        "ds_read_b128 v[164:167], %[bb] offset:23552\nds_read_b128 v[28:31], %[bb] offset:4096\nds_read_b128 v[32:35], %[bb] offset:9216\nds_read_b128 v[36:39], %[bb] offset:14336\n"
        "ds_read_b128 v[40:43], %[bb] offset:19456\nds_read_b128 v[44:47], %[bb] offset:24576\ns_mov_b32 %[s1], 0x100000\n.LWc%=:\nds_read_b32 %[tm], %[sy] offset:%[ofr]\ns_waitcnt lgkmcnt(0)\n"
        "v_readfirstlane_b32 %[s0], %[tm]\ns_nop 0\ns_cmp_ge_i32 %[s0], 8\ns_cbranch_scc1 .LXc%=\ns_sleep 1\ns_sub_u32 %[s1], %[s1], 1\ns_cbranch_scc0 .LWc%=\n.LXc%=:\n.LC1_%=:\ns_waitcnt vmcnt(4)\n"
        "v_max_u32 %[tm], v48, v49\nv_max3_u32 %[tm], %[tm], v50, v51\ns_waitcnt vmcnt(3)\nv_max3_u32 %[tm], %[tm], v52, v53\nv_max3_u32 %[tm], %[tm], v54, v55\ns_waitcnt vmcnt(2)\n"
        "v_max3_u32 %[tm], %[tm], v56, v57\nv_max3_u32 %[tm], %[tm], v58, v59\ns_waitcnt vmcnt(1)\nv_max3_u32 %[tm], %[tm], v60, v61\nv_max3_u32 %[tm], %[tm], v62, v63\ns_waitcnt vmcnt(0)\n"
        "v_max3_u32 %[tm], %[tm], v64, v65\nv_max3_u32 %[tm], %[tm], v66, v67\nv_cmp_eq_u32 vcc, -1, %[tm]\ns_cbranch_vccnz .LR1_%=\n.LK1_%=:\n"
        "v_mfma_f32_16x16x32_bf16 v[68:71], v[48:51], v[88:91], v[68:71]\nv_mfma_f32_16x16x32_bf16 v[68:71], v[52:55], v[92:95], v[68:71]\n"
        "v_mfma_f32_16x16x32_bf16 v[68:71], v[56:59], v[96:99], v[68:71]\nv_mfma_f32_16x16x32_bf16 v[68:71], v[60:63], v[100:103], v[68:71]\n"
        "v_mfma_f32_16x16x32_bf16 v[68:71], v[64:67], v[104:107], v[68:71]\nv_mfma_f32_16x16x32_bf16 v[72:75], v[48:51], v[108:111], v[72:75]\n"
        "v_mfma_f32_16x16x32_bf16 v[72:75], v[52:55], v[112:115], v[72:75]\nv_mfma_f32_16x16x32_bf16 v[72:75], v[56:59], v[116:119], v[72:75]\nds_write_b128 %[rw], v[68:71]\n"
        "v_mfma_f32_16x16x32_bf16 v[72:75], v[60:63], v[120:123], v[72:75]\nv_mfma_f32_16x16x32_bf16 v[72:75], v[64:67], v[124:127], v[72:75]\n"
        "v_mfma_f32_16x16x32_bf16 v[76:79], v[48:51], v[128:131], v[76:79]\nv_mfma_f32_16x16x32_bf16 v[76:79], v[52:55], v[132:135], v[76:79]\n"
        "v_mfma_f32_16x16x32_bf16 v[76:79], v[56:59], v[136:139], v[76:79]\nds_write_b128 %[rw], v[72:75] offset:1024\nv_mfma_f32_16x16x32_bf16 v[76:79], v[60:63], v[140:143], v[76:79]\n"
        "ds_write_b32 %[fw], %[f0]\nv_mfma_f32_16x16x32_bf16 v[76:79], v[64:67], v[144:147], v[76:79]\nv_mfma_f32_16x16x32_bf16 v[80:83], v[48:51], v[148:151], v[80:83]\n"
        "v_mfma_f32_16x16x32_bf16 v[80:83], v[52:55], v[152:155], v[80:83]\nv_mfma_f32_16x16x32_bf16 v[80:83], v[56:59], v[156:159], v[80:83]\nds_write_b128 %[rw], v[76:79] offset:2048\n"
        "v_mfma_f32_16x16x32_bf16 v[80:83], v[60:63], v[160:163], v[80:83]\nds_write_b32 %[fw], %[one]\nv_mfma_f32_16x16x32_bf16 v[80:83], v[64:67], v[164:167], v[80:83]\n"
        "v_mfma_f32_16x16x32_bf16 v[84:87], v[48:51], v[28:31], v[84:87]\nv_mfma_f32_16x16x32_bf16 v[84:87], v[52:55], v[32:35], v[84:87]\n"
        "v_mfma_f32_16x16x32_bf16 v[84:87], v[56:59], v[36:39], v[84:87]\nv_mfma_f32_16x16x32_bf16 v[84:87], v[60:63], v[40:43], v[84:87]\n"
        "v_mfma_f32_16x16x32_bf16 v[84:87], v[64:67], v[44:47], v[84:87]\ns_mov_b32 %[s1], 0x100000\n.LY%=:\nds_read_b32 %[tm], %[fr]\nds_read_b128 v[96:99], %[rr] offset:2048\n"
        "ds_read_b128 v[92:95], %[rr] offset:1024\nds_read_b128 v[88:91], %[rr] offset:0\ns_waitcnt lgkmcnt(3)\nv_readfirstlane_b32 %[s0], %[tm]\ns_waitcnt lgkmcnt(0)\ns_cmp_lg_u32 %[s0], 0\n"
        "s_cbranch_scc1 .LZ%=\ns_sub_u32 %[s1], %[s1], 1\ns_cbranch_scc0 .LY%=\n.LZ%=:\nv_add_f32 v80, v80, v92\nv_add_f32 v84, v84, v96\nv_add_f32 v81, v81, v93\nv_add_f32 v85, v85, v97\n"
        "v_add_f32 v82, v82, v94\nv_add_f32 v86, v86, v98\nv_add_f32 v83, v83, v95\nv_add_f32 v87, v87, v99\nv_cvt_pk_bf16_f32 v92, v80, v81\nv_cvt_pk_bf16_f32 v96, v84, v85\n"
        "v_cvt_pk_bf16_f32 v93, v82, v83\nv_cvt_pk_bf16_f32 v97, v86, v87\nv_min_u32 v92, -2, v92\nv_min_u32 v96, -2, v96\nv_min_u32 v93, -2, v93\nv_min_u32 v97, -2, v97\n"
        "ds_write_b64 %[xo], v[92:93] offset:2304\nds_write_b64 %[xo], v[96:97] offset:4608\ns_cmp_eq_u32 %[kh], 0\nv_add_f32 v100, v80, v84\nv_add_f32 v101, v81, v85\nv_add_f32 v102, v82, v86\n"
        "v_add_f32 v103, v83, v87\nv_mul_f32 v104, v84, v84\nv_mul_f32 v105, v85, v85\nv_mul_f32 v106, v86, v86\nv_mul_f32 v107, v87, v87\nv_fmac_f32 v104, v80, v80\nv_fmac_f32 v105, v81, v81\n"
        "v_fmac_f32 v106, v82, v82\nv_fmac_f32 v107, v83, v83\ns_cbranch_scc0 .LV%=\nv_add_f32 v76, v76, v88\nv_add_f32 v77, v77, v89\nv_add_f32 v78, v78, v90\nv_add_f32 v79, v79, v91\n"
        "v_cvt_pk_bf16_f32 v88, v76, v77\nv_cvt_pk_bf16_f32 v89, v78, v79\nv_min_u32 v88, -2, v88\nv_min_u32 v89, -2, v89\nds_write_b64 %[xo], v[88:89] offset:0\nv_add_f32 v100, v100, v76\n"
        "v_add_f32 v101, v101, v77\nv_add_f32 v102, v102, v78\nv_add_f32 v103, v103, v79\nv_fmac_f32 v104, v76, v76\nv_fmac_f32 v105, v77, v77\nv_fmac_f32 v106, v78, v78\nv_fmac_f32 v107, v79, v79\n"
        ".LV%=:\ns_mov_b64 exec, 1\nds_add_u32 %[sy], %[one] offset:%[ox1]\ns_mov_b64 exec, -1\nv_add_f32_dpp v100, v100, v100 row_ror:8 row_mask:0xf bank_mask:0x3\n"
        "v_add_f32_dpp v101, v101, v101 row_ror:8 row_mask:0xf bank_mask:0x3\nv_add_f32_dpp v104, v104, v104 row_ror:8 row_mask:0xf bank_mask:0x3\n"
        "v_add_f32_dpp v105, v105, v105 row_ror:8 row_mask:0xf bank_mask:0x3\nv_add_f32_dpp v100, v102, v102 row_ror:8 row_mask:0xf bank_mask:0xc\n"
        "v_add_f32_dpp v101, v103, v103 row_ror:8 row_mask:0xf bank_mask:0xc\nv_add_f32_dpp v104, v106, v106 row_ror:8 row_mask:0xf bank_mask:0xc\n"
        "v_add_f32_dpp v105, v107, v107 row_ror:8 row_mask:0xf bank_mask:0xc\nv_add_f32_dpp v102, v100, v100 row_half_mirror row_mask:0xf bank_mask:0x5\n"
        "v_add_f32_dpp v103, v104, v104 row_half_mirror row_mask:0xf bank_mask:0x5\ns_nop 0\nv_add_f32_dpp v102, v101, v101 row_half_mirror row_mask:0xf bank_mask:0xa\n"
        "v_add_f32_dpp v103, v105, v105 row_half_mirror row_mask:0xf bank_mask:0xa\ns_nop 0\nv_add_f32_dpp v102, v102, v102 quad_perm:[1,0,3,2] row_mask:0xf bank_mask:0xf\n"
        "v_add_f32_dpp v103, v103, v103 quad_perm:[1,0,3,2] row_mask:0xf bank_mask:0xf\ns_nop 0\nv_add_f32_dpp v102, v102, v102 quad_perm:[2,3,0,1] row_mask:0xf bank_mask:0xf\n"
        "v_add_f32_dpp v103, v103, v103 quad_perm:[2,3,0,1] row_mask:0xf bank_mask:0xf\nv_min_u32 v102, -2, v102\nv_min_u32 v103, -2, v103\n"
        "buffer_store_dwordx2 v[102:103], %[sv], %[srs], %[ss] offen sc1\ns_branch .LE%=\n.LR0_%=:\ns_sub_u32 %[cap], %[cap], 1\ns_cbranch_scc0 .LQ0_%=\ns_mov_b32 %[cap], 0\ns_branch .LK0_%=\n"
        ".LQ0_%=:\ns_sleep 2\nbuffer_load_dwordx4 v[28:31], %[l16], %[ors], %[o0] offen offset:0 sc1\nbuffer_load_dwordx4 v[32:35], %[l16], %[ors], %[o1] offen offset:0 sc1\n"
        "buffer_load_dwordx4 v[36:39], %[l16], %[ors], %[o2] offen offset:0 sc1\nbuffer_load_dwordx4 v[40:43], %[l16], %[ors], %[o3] offen offset:0 sc1\n"
        "buffer_load_dwordx4 v[44:47], %[oha], %[ors], %[o4] offen offset:0 sc1\ns_branch .LC0_%=\n.LR1_%=:\ns_sub_u32 %[cap], %[cap], 1\ns_cbranch_scc0 .LQ1_%=\ns_mov_b32 %[cap], 0\ns_branch .LK1_%=\n"
        ".LQ1_%=:\ns_sleep 2\nbuffer_load_dwordx4 v[48:51], %[l16], %[ors], %[o0] offen offset:1024 sc1\nbuffer_load_dwordx4 v[52:55], %[l16], %[ors], %[o1] offen offset:1024 sc1\n"
        "buffer_load_dwordx4 v[56:59], %[l16], %[ors], %[o2] offen offset:1024 sc1\nbuffer_load_dwordx4 v[60:63], %[l16], %[ors], %[o3] offen offset:1024 sc1\n"
        "buffer_load_dwordx4 v[64:67], %[oha], %[ors], %[o4] offen offset:256 sc1\ns_branch .LC1_%=\n.LE%=:\n"
        : [tm] "=&v"(tm), [bb] "+v"(bb), [cap] "=&s"(cap), [s0] "=&s"(s0_), [s1] "=&s"(s1_), "+v"(xi_), "+v"(xg_)
        : "{v[22:23]}"(rv[0]), "{v[24:25]}"(rv[1]), "{v[26:27]}"(rv[2]), [l16] "v"(l16), [oha] "v"(oha), [ors] "s"(orsrc), [o0] "s"(ob + 0), [o1] "s"(ob + 10240), [o2] "s"(ob + 20480), [o3] "s"(ob + 30720), [o4] "s"(ob + 2048), [sy] "v"(sya), [one] "v"(one), [rw] "v"(rw), [fw] "v"(fw), [f0] "v"(f0), [rr] "v"(rr), [fr] "v"(fw ^ 16), [xo] "v"(xo_), [sv] "v"(so_), [srs] "s"(srsrc), [ss] "s"(mt2 * 128 + (2 * hg + kh) * 512), [kh] "s"(kh), [wo1] "s"(WO1_OFF), [ofr] "i"(SY_FREE), [ox1] "i"(SY_X1), [ow1] "i"(SY_WO + 4), [nwo] "i"(P0E == 2 ? 8 : 4), [sl0] "i"(OSLEEP), [sl1] "i"(OSLEEP1)
        : "memory", "scc", "vcc", "v28", "v29", "v30", "v31", "v32", "v33", "v34", "v35", "v36", "v37", "v38", "v39", "v40", "v41", "v42", "v43", "v44", "v45", "v46", "v47", "v48", "v49", "v50", "v51", "v52", "v53", "v54", "v55", "v56", "v57", "v58", "v59", "v60", "v61", "v62", "v63", "v64", "v65", "v66", "v67", "v68", "v69", "v70", "v71", "v72", "v73", "v74", "v75", "v76", "v77", "v78", "v79", "v80", "v81", "v82", "v83", "v84", "v85", "v86", "v87", "v88", "v89", "v90", "v91", "v92", "v93", "v94", "v95", "v96", "v97", "v98", "v99", "v100", "v101", "v102", "v103", "v104", "v105", "v106", "v107", "v108", "v109", "v110", "v111", "v112", "v113", "v114", "v115", "v116", "v117", "v118", "v119", "v120", "v121", "v122", "v123", "v124", "v125", "v126", "v127", "v128", "v129", "v130", "v131", "v132", "v133", "v134", "v135", "v136", "v137", "v138", "v139", "v140", "v141", "v142", "v143", "v144", "v145", "v146", "v147", "v148", "v149", "v150", "v151", "v152", "v153", "v154", "v155", "v156", "v157", "v158", "v159", "v160", "v161", "v162", "v163", "v164", "v165", "v166", "v167");
    RETRY(8 * layer + 1 + grp, 0x10000 - cap);
    RETRY(8 * layer + 3 + grp, rt >> 16);
  }
  lds_inc<SY_FIN>(sya, one);
  lds_poll<SY_X1, 0>(sya, 8);
  {
    typedef __attribute__((address_space(3))) const u32x4* lp4;
    __builtin_amdgcn_raw_buffer_store_b128(*(lp4)(size_t)xi_, x1rs, xg_, 0, 17);
    if (j < 2) __builtin_amdgcn_raw_buffer_store_b128(*(lp4)(size_t)(xi_ + 64 * 144), x1rs, xg_, 64 * XP * 2, 17);
  }
  STY(13);
  {
    LANE(lane);
    const int t = 64 * w + lane;
    ST_FLUSH(kid);
  }
}

#define BAUX 17
#define PC8 (gq == 0)
#define PC4 (gq == 0)
#define UPAD (9 * 144)
#define UF_N (UPAD + 16 * 144)
#define KY0 1058
#define KC_N 1120
#define KZ0 130
#define K8_N 192
#define X3_P 144
#define CH_EL (UF_N + 4 * KC_N + 2 * K8_N)
#define AUX_B 25600
#define B_CNT (3 * CH_EL * 2 + 3 * AUX_B)

template <int OFF> __device__ __forceinline__ bf16x8 rdk(u32 a) {
  s16x4 l, h;
  asm volatile("ds_read_b64 %0, %2 offset:%3\n\tds_read_b64 %1, %2 offset:%3+8" : "=&v"(l), "=&v"(h) : "v"(a), "i"(OFF) : "memory");
  return __builtin_shufflevector(l, h, 0, 1, 2, 3, 4, 5, 6, 7);
}
template <int N, int NB> struct KLd {
  static __device__ __forceinline__ void go(bf16x8* K, u32 a) { KLd<N - 1, NB>::go(K, a); K[N - 1] = rdk<256 * NB - 64 * (N - 1)>(a); }
};
template <int NB> struct KLd<0, NB> { static __device__ __forceinline__ void go(bf16x8*, u32) {} };

#define BLK4_TAIL \
      "v_mfma_f32_16x16x32_f16 %3, %9, %6, %3\n\t" \
      "v_mfma_f32_16x16x32_f16 %2, %9, %5, %2\n\t" \
      "ds_read_b128 %6, %12 offset:160\n\t" \
      "v_mfma_f32_16x16x32_f16 %1, %9, %4, %1\n\t" \
      "s_waitcnt lgkmcnt(1)\n\t" \
      "v_mfma_f32_16x16x32_f16 %0, %9, %7, %0\n\t" \
      "v_mfma_f32_16x16x32_f16 %3, %10, %5, %3\n\t" \
      "v_mfma_f32_16x16x32_f16 %2, %10, %4, %2\n\t" \
      "ds_read_b128 %5, %12 offset:96\n\t" \
      "v_mfma_f32_16x16x32_f16 %1, %10, %7, %1\n\t" \
      "s_waitcnt lgkmcnt(1)\n\t" \
      "v_mfma_f32_16x16x32_f16 %0, %10, %6, %0\n\t" \
      "v_mfma_f32_16x16x32_f16 %3, %11, %4, %3\n\t" \
      "v_mfma_f32_16x16x32_f16 %2, %11, %7, %2\n\t" \
      "ds_read_b128 %4, %13 offset:0\n\t" \
      "v_mfma_f32_16x16x32_f16 %1, %11, %6, %1\n\t" \
      "s_waitcnt lgkmcnt(1)\n\t" \
      "v_mfma_f32_16x16x32_f16 %0, %11, %5, %0\n\t"
template <bool FIRST>
__device__ __forceinline__ void blk4(f32x4 (&acc)[4], bf16x8 (&F)[4], const bf16x8* k, u32 ub, u32 ubx) {
  if (FIRST)
    asm volatile(
      "v_mfma_f32_16x16x32_f16 %3, %8, %7, 0\n\t"
      "v_mfma_f32_16x16x32_f16 %2, %8, %6, 0\n\t"
      "ds_read_b128 %7, %12 offset:224\n\t"
      "v_mfma_f32_16x16x32_f16 %1, %8, %5, 0\n\t"
      "s_waitcnt lgkmcnt(1)\n\t"
      "v_mfma_f32_16x16x32_f16 %0, %8, %4, 0\n\t"
      BLK4_TAIL
      : "=&v"(acc[0]), "=&v"(acc[1]), "=&v"(acc[2]), "=&v"(acc[3]), "+v"(F[0]), "+v"(F[1]), "+v"(F[2]), "+v"(F[3])
      : "v"(k[0]), "v"(k[1]), "v"(k[2]), "v"(k[3]), "v"(ub), "v"(ubx) : "memory");
  else
    asm volatile(
      "v_mfma_f32_16x16x32_f16 %3, %8, %7, %3\n\t"
      "v_mfma_f32_16x16x32_f16 %2, %8, %6, %2\n\t"
      "ds_read_b128 %7, %12 offset:224\n\t"
      "v_mfma_f32_16x16x32_f16 %1, %8, %5, %1\n\t"
      "s_waitcnt lgkmcnt(1)\n\t"
      "v_mfma_f32_16x16x32_f16 %0, %8, %4, %0\n\t"
      BLK4_TAIL
      : "+v"(acc[0]), "+v"(acc[1]), "+v"(acc[2]), "+v"(acc[3]), "+v"(F[0]), "+v"(F[1]), "+v"(F[2]), "+v"(F[3])
      : "v"(k[0]), "v"(k[1]), "v"(k[2]), "v"(k[3]), "v"(ub), "v"(ubx) : "memory");
}

#define MFH(a, b, c) __builtin_amdgcn_mfma_f32_16x16x32_f16(__builtin_bit_cast(f16x8, a), __builtin_bit_cast(f16x8, b), c, 0, 0, 0)
#define LDSA_(p) ((u32)(size_t)(__attribute__((address_space(3))) u16*)(p))
#define PIN_(x) asm volatile("" : "+v"(x))
__device__ __forceinline__ void kc_store(u16* kd, const u32* d) {
  u32 sw[5], mx[5];
#pragma unroll
  for (int i = 0; i < 5; ++i) { sw[i] = __builtin_amdgcn_alignbit(d[i], d[i], 16); mx[i] = __builtin_amdgcn_perm(d[i], d[i + 1], 0x07060100); }
  *(uint4*)kd = make_uint4(mx[4], mx[3], mx[2], mx[1]);
  *(uint4*)(kd + KC_N) = make_uint4(sw[4], sw[3], sw[2], sw[1]);
  *(uint4*)(kd + 2 * KC_N) = make_uint4(mx[3], mx[2], mx[1], mx[0]);
  *(uint4*)(kd + 3 * KC_N) = make_uint4(sw[3], sw[2], sw[1], sw[0]);
}
#define DUPLO_(x) __builtin_amdgcn_perm(x, x, 0x01000100)
#define DUPHI_(x) __builtin_amdgcn_perm(x, x, 0x03020302)

#define FSLEEP 14
#define TSLEEP 10
#define FPOLL 1
__device__ __forceinline__ void b_tables(unsigned char* smem, const int ch, const int lane) {
  const u16* kc = (const u16*)(smem + KQ_OFF) + ch * 1536;
  u16* Uf = (u16*)smem + ch * CH_EL;
  u16* KC = Uf + UF_N;
  const u16* p = kc + (lane < 32 ? 8 * lane : 256 + 4 * (lane - 32));
  const uint2 a0 = *(const uint2*)p, a1 = *(const uint2*)(p + 4), a2 = *(const uint2*)(p + 8);
  const u32 b0 = *(const u32*)&kc[384 + 2 * lane], b1 = *(const u32*)&kc[386 + 2 * lane];
  const uint2 eh = *(const uint2*)(kc + (lane < 3 ? 0 : 512));
  const u32 c0 = *(const u32*)&kc[638 - 2 * lane], c1 = *(const u32*)&kc[640 - 2 * lane];
  u32 dh[6];
  {
    const bool r1 = lane < 32;
    dh[0] = r1 ? a0.x : DUPLO_(a0.x);
    dh[1] = r1 ? a0.y : __builtin_amdgcn_perm(a0.y, a0.x, 0x05040302);
    dh[2] = r1 ? a1.x : DUPLO_(a0.y);
    dh[3] = r1 ? a1.y : __builtin_amdgcn_perm(a1.x, a0.y, 0x05040302);
    const u32 nx = r1 ? a2.x : a1.x;
    dh[4] = lane < 31 ? nx : DUPLO_(nx);
    dh[5] = lane < 31 ? a2.y : (lane == 63 ? nx : nx >> 16);
    kc_store(KC + 8 * (131 - lane), dh);
  }
  dh[0] = DUPLO_(b0); dh[1] = b0; dh[2] = DUPHI_(b0); dh[3] = DUPLO_(b1); dh[4] = dh[3]; dh[5] = b1;
  kc_store(KC + 8 * (67 - lane), dh);
  {
    u32* K8 = (u32*)(KC + 4 * KC_N);
    const u32 wh = __builtin_amdgcn_perm(c0, c1, 0x07060100);
    K8[lane + 1] = wh;
    K8[K8_N / 2 + lane] = wh;
    K8[lane < 32 ? (lane ? 64 + lane : 0) : 128 + lane] = 0u;
  }
  *(uint4*)&Uf[8 * lane] = make_uint4(0, 0, 0, 0);
  *(uint4*)&Uf[8 * lane + 512] = make_uint4(0, 0, 0, 0);
  *(uint4*)&Uf[8 * lane + UPAD - 512] = make_uint4(0, 0, 0, 0);
  if (lane < 7) {
    const bool e0 = lane == 2, e1 = lane == 3;
    dh[0] = e1 ? DUPLO_(eh.x) : 0u; dh[1] = dh[0]; dh[2] = e1 ? eh.x & 0xffffu : 0u; dh[3] = 0u; dh[4] = e0 ? eh.x : 0u; dh[5] = e0 ? eh.y : 0u;
    kc_store(KC + 8 * (131 - (lane < 3 ? lane - 3 : 125 + lane)), dh);
  }
}
#define PBU 1
#define PBU2 1
#define PBUF 1
#define PBL1 2
#define FSLEEP0 FSLEEP
#define PBL 2
#define PBH 3
#define PBE 1
template <bool FULL, int layer>
__device__ __forceinline__ void b_conv(unsigned char* smem, const int b, const int t, const int gq, const int wq, const int cG, const u16* xtin, float2* stats, u32* fstat,
                                       u16* xtout, u32x4* mark
                                       ) {
  constexpr int NB = FULL ? 4 : 2;
  const int lane = t & 63, n = lane & 15, g = lane >> 4, ck = t & 255;
  const int v = (wq + gq) & 3;
  const int p = FULL ? (v & 1) : (b & 1);
  const int r0 = FULL ? (v & 2) : v;
  const bool xtra = FULL ? (v >= 2) : (v == 3);
  const u32 Uf = LDSA(smem) + gq * (CH_EL * 2);
  const u32 aux = LDSA(smem) + 3 * CH_EL * 2 + gq * AUX_B;
  const u32 sya = LDSA(smem) + SY_OFF + 256 * layer;
  u32 cna = sya + 80 + 16 * gq, one = 1u;
  bf16x8 K[4 * NB + 1], K8f[4], be, bo;
  const int ct = FULL ? 3 - v : ((v == 1 || v == 2) ? 1 : 0), q0 = FULL ? 0 : (v == 2 ? 2 : 0);
  const int np = FULL ? (v < 2 ? 2 : 1) : (v < 3 ? 1 : 0);
  u32 ub = Uf + 2 * (UPAD + 144 * (n - 2 * r0) + 8 * g + 16 * p - 176);
  u32 ubx = ub + ((g >= 2 || p) ? 32 : 0);
  const int h = n + g - 2;
  u32 p1 = Uf + 2 * (UPAD + 288 * wq + 8 * h + (h >= 16 ? 16 : 0) - (h < 0 ? 16 : 0));
  u32 p0 = Uf + 2 * (UPAD + 288 * wq + 8 * h - 4 - (h < 1 ? 16 : 0));
  u32 xd = aux + 2 * (n * X3_P + 32 * wq + 4 * g), xr = aux + 2 * (n * X3_P + 32 * q0 + 8 * g);
  u32 uw_ = Uf + 2 * (UPAD + 8 * ck + 16 * (ck >> 4));
  const int eck = FULL ? ck : (4 * (ck >> 1) + 2 * p + (ck & 1)) & 255;
  u32 ue = Uf + 2 * (UPAD + 8 * eck + 16 * (eck >> 4));
  u32 exw = aux + 4608 + (FULL ? (v >> 1) * 8448 + n * 528 + p * 64 + g * 16 : v * 4096 + n * 256);
  u32 exr = aux + 4608 + (FULL ? (ck >> 4) * 528 + (ck & 15) * 32 : ((ck >> 3) & 15) * 256 + ((8 * (ck & 7) + 4 * (ck >> 3)) & 63) * 4);
  u32 exr1 = FULL ? exr + 16 : aux + 4608 + ((ck >> 3) & 15) * 256 + ((8 * (ck & 7) + 4 * (ck >> 3) + 4) & 63) * 4;
  u32 frw = aux + 21504 + (FULL ? ct * 1024 : (v >= 2 ? 2048 : 0) + ct * 1024) + n * 64 + g * 16;
  u32 frr = aux + 21504 + (FULL ? (ck & 127) * 32 : (ck & 63) * 32);
  float fm = (FULL ? ck >= 128 : (ck & 64) != 0) ? 1.f : 0.f;
  int od, oh;
  if (layer) { od = (((eck >> 3) * 640 + cG) * 64 + 8 * (eck & 7)) * 2; oh = -1; }
  else {
    const int dst = (eck >> 3) * 51200 + (cG >> 5) * 2560;
    const int pr = 4 * ((cG >> 3) & 3) + (cG & 3) + 4 * (cG & 4);
    od = (dst + pr * 64 + 8 * ((eck & 7) ^ (pr & 6))) * 2;
    oh = ((eck & 7) >= 6 && eck < 248) ? (dst + 51200 + 2048 + pr * 16 + 8 * (eck & 1)) * 2 : -1;
  }
  const int y0 = KY0 - 16 - n + 8 * g - 256 * r0;
  u32 kpa = Uf + 2 * (UF_N + (y0 & 3) * KC_N + (y0 & ~3)) - 256 * NB;
  const int z0 = KZ0 - 2 - (FULL ? 2 * n : 4 * n + 2 * p) + 8 * g - (FULL ? 32 : 64) * ct + 32 * q0;
  u32 k8a = Uf + 2 * (UF_N + 4 * KC_N + ((z0 >> 1) & 1) * K8_N + (z0 & ~3)), k8b = np < 2 ? k8a - 128 : k8a;
  u32 bna = LDSA(smem) + BND_OFF + 16 * lane, kfa = LDSA(smem) + KQ_OFF + gq * 3072 + 1536;
  int c16 = ck * 16, c32 = ck * 32;
  const __amdgpu_buffer_rsrc_t xrs = __builtin_amdgcn_make_buffer_rsrc((void*)(xtin + (size_t)cG * XP), 0, XP * 2, 0x00020000);
  const __amdgpu_buffer_rsrc_t frs = __builtin_amdgcn_make_buffer_rsrc((void*)(fstat + (b & 7) * 2048), 0, 8192, 0x00020000);
  const __amdgpu_buffer_rsrc_t ors = __builtin_amdgcn_make_buffer_rsrc(xtout, 0, 32 * 102400, 0x00020000);
  PIN(ub); PIN(ubx); PIN(p0); PIN(p1); PIN(xd); PIN(xr); PIN(uw_); PIN(exw); PIN(exr); PIN(frw); PIN(frr); PIN(fm); PIN(od);
  if (!FULL) { PIN(ue); PIN(exr1); }
  if (!layer) PIN(oh);
  PIN(cna); PIN(one); PIN(kpa); PIN(k8a); PIN(k8b); PIN(bna); PIN(kfa); PIN(c16); PIN(c32);
  ST(6);
  if (!FULL && wq == 3) {
    const int xcd = b & 7, idx = b >> 3;
    const int s0 = 64 * (8 * (xcd >> 1) + (idx & 7)), hg = 4 * (xcd & 1) + (idx >> 3);
    const __amdgpu_buffer_rsrc_t srsrc = __builtin_amdgcn_make_buffer_rsrc(stats + (size_t)s0 * 16, 0, 8192, 0x00020000);
    const __amdgpu_buffer_rsrc_t wrs = __builtin_amdgcn_make_buffer_rsrc(fstat + s0 + 8 * hg, 0, 8 * 8192, 0x00020000);
    int so = (lane >> 2) * 512 + (lane & 3) * 16;
    int wo_ = lane < 32 ? (lane >> 2) * 8192 + (lane & 3) * 8 : -1;
    PIN(so); PIN(wo_);
    lds_poll<SY_FIN, 4>(sya, 8);
    __builtin_amdgcn_s_sleep(SSLEEP);
    u32x4 pv;
    int spins = 0;
    _Pragma("unroll 1")
    while (true) {
      asm volatile("" ::: "memory");
      pv = __builtin_amdgcn_raw_buffer_load_b128(srsrc, so, hg * 64, 16);
      if (__builtin_amdgcn_ballot_w64(umax3(pv[0], pv[1], max(pv[2], pv[3])) == 0xffffffffu) == 0 || ++spins > GCAP) break;
      __builtin_amdgcn_s_sleep(4);
    }
    RETRY(8 * layer + 5, spins);
    ST(7);
    float sv[4];
#pragma unroll
    for (int i = 0; i < 4; ++i) {
      float a = __uint_as_float(pv[i]);
      a += __builtin_bit_cast(float, __builtin_amdgcn_update_dpp(0, __builtin_bit_cast(int, a), 0x124, 0xf, 0xf, false));
      a += dpp_ror8(a);
      sv[i] = xsum16_32(a);
    }
    u32 o[2];
#pragma unroll
    for (int i = 0; i < 2; ++i) {
      const float mean = sv[2 * i] * (1.0f / 640.0f);
      const float var = fmaxf(fmaf(-mean, mean, sv[2 * i + 1] * (1.0f / 640.0f)), 0.f);
      o[i] = pack2(mean, __builtin_amdgcn_sqrtf(rbf(rbf(var) + 1e-5f)));
    }
    __builtin_amdgcn_raw_buffer_store_b64((u32x2){min(o[0], 0xfffffffeu), min(o[1], 0xfffffffeu)}, wrs, wo_, 0, 17);
    lds_inc<SY_FS>(sya, one);
    ST(8);
    __builtin_amdgcn_s_setprio(PBU);
  }
  f32x2 kd;
  lds_poll<12, 1>(cna, 1);
  KLd<4 * NB + 1, NB>::go(K, kpa);
  K8f[0] = rdk<0>(k8a); K8f[1] = rdk<64>(k8a);
  if (FULL) { K8f[2] = rdk<128>(k8b); K8f[3] = rdk<192>(k8b); }
  asm volatile("ds_read_b128 %0, %3\n\tds_read_b128 %1, %3 offset:1024\n\tds_read_b64 %2, %4" : "=&v"(be), "=&v"(bo), "=&v"(kd) : "v"(bna), "v"(kfa) : "memory");
  ST(1);
  lds_poll<SY_FS, 2>(sya, 1);
  u32x4 x4, f0, f1;
  {
    int spins = 0;
    __builtin_amdgcn_s_setprio(FULL ? PBUF : PBU2);
    if (FULL || wq < 3) __builtin_amdgcn_s_sleep(layer ? FSLEEP : FSLEEP0); else __builtin_amdgcn_s_sleep((layer ? FSLEEP : FSLEEP0) - 5);
    _Pragma("unroll 1")
    while (true) {
      asm volatile("" ::: "memory");
      f0 = __builtin_amdgcn_raw_buffer_load_b128(frs, c32, 0, 16);
      f1 = __builtin_amdgcn_raw_buffer_load_b128(frs, c32, 16, 16);
      x4 = __builtin_amdgcn_raw_buffer_load_b128(xrs, c16, 0, 16);
      const u32 m = umax3(umax3(umax3(f0[0], f0[1], f0[2]), f0[3], f1[0]), umax3(umax3(f1[1], f1[2], f1[3]), x4[0], x4[1]), max(x4[2], x4[3]));
      if (__builtin_amdgcn_ballot_w64(m == 0xffffffffu) == 0 || ++spins > GCAP) break;
      __builtin_amdgcn_s_sleep(FPOLL);
    }
    RETRY(8 * layer + 6, spins);
  }
  ST(2);
  u32 ud[4];
  {
#define RS_(x) (256.0f * __builtin_amdgcn_rcpf(hi_f(x)))
    const u32 a0 = pack2(lo_f(x4[0]) - lo_f(f0[0]), hi_f(x4[0]) - lo_f(f0[1])), a1 = pack2(lo_f(x4[1]) - lo_f(f0[2]), hi_f(x4[1]) - lo_f(f0[3]));
    const u32 a2 = pack2(lo_f(x4[2]) - lo_f(f1[0]), hi_f(x4[2]) - lo_f(f1[1])), a3 = pack2(lo_f(x4[3]) - lo_f(f1[2]), hi_f(x4[3]) - lo_f(f1[3]));
    ud[0] = pack2(lo_f(a0) * RS_(f0[0]), hi_f(a0) * RS_(f0[1])); ud[1] = pack2(lo_f(a1) * RS_(f0[2]), hi_f(a1) * RS_(f0[3]));
    ud[2] = pack2(lo_f(a2) * RS_(f1[0]), hi_f(a2) * RS_(f1[1])); ud[3] = pack2(lo_f(a3) * RS_(f1[2]), hi_f(a3) * RS_(f1[3]));
    const u32x4 uv = {packh(lo_f(ud[0]), hi_f(ud[0])), packh(lo_f(ud[1]), hi_f(ud[1])), packh(lo_f(ud[2]), hi_f(ud[2])), packh(lo_f(ud[3]), hi_f(ud[3]))};
    asm volatile("ds_write_b128 %0, %1" :: "v"(uw_), "v"(uv) : "memory");
    lds_inc<0>(cna, one);
  }
  if (FULL && PBL != PBL1 && gq) __builtin_amdgcn_s_setprio(PBL1); else __builtin_amdgcn_s_setprio(FULL ? PBL : PBH);
  lds_poll<0, 0>(cna, 4);
  ST(3);
  typedef __attribute__((address_space(3))) const f32x4* lpf;
  bf16x8 F[4];
  s16x4 wa[4];
  u32x4 ur;
  asm volatile("ds_read_b128 %3, %4 offset:512\n\tds_read_b128 %2, %4 offset:448\n\tds_read_b128 %1, %4 offset:384\n\tds_read_b128 %0, %5 offset:288"
               : "=&v"(F[0]), "=&v"(F[1]), "=&v"(F[2]), "=&v"(F[3]) : "v"(ub), "v"(ubx) : "memory");
  asm volatile("ds_read_b64 %0, %4\n\tds_read_b64 %1, %5\n\tds_read_b64 %2, %4 offset:288\n\tds_read_b64 %3, %5 offset:288\n\ts_waitcnt lgkmcnt(5)"
               : "=&v"(wa[0]), "=&v"(wa[1]), "=&v"(wa[2]), "=&v"(wa[3]) : "v"(p0), "v"(p1) : "memory");
  f32x4 acc[4], far, far1;
  bf16x8 xw[4];
  u32x4 xe;
#pragma unroll
  for (int c = 0; c < NB; ++c) {
    if (c == NB / 2) {
      lds_poll<4, 0>(cna, 4);
      asm volatile("ds_read_b128 %0, %2\n\tds_read_b128 %1, %2 offset:64" : "=&v"(xw[0]), "=&v"(xw[1]) : "v"(xr) : "memory");
      if (FULL) asm volatile("ds_read_b128 %0, %2 offset:128\n\tds_read_b128 %1, %2 offset:192" : "=&v"(xw[2]), "=&v"(xw[3]) : "v"(xr) : "memory");
    }
    if (c == 0) blk4<true>(acc, F, K, ub, ubx); else blk4<false>(acc, F, K + 4 * c, ub, ubx);
    ub -= 288; ubx -= 288;
    ST(10 + c);
    if (c == 0) {
      asm volatile("" : "+v"(wa[0]), "+v"(wa[1]), "+v"(wa[2]), "+v"(wa[3]), "+v"(be), "+v"(bo));
      if (!FULL) asm volatile("ds_read_b128 %0, %1" : "=v"(ur) : "v"(ue) : "memory");
#pragma unroll
      for (int i = 0; i < 2; ++i) {
        const bf16x8 af = __builtin_shufflevector(wa[2 * i], wa[2 * i + 1], 0, 1, 2, 3, 4, 5, 6, 7);
        const f32x4 we = MFH(af, be, ((f32x4){0.f, 0.f, 0.f, 0.f}));
        const f32x4 wo = MFH(af, bo, ((f32x4){0.f, 0.f, 0.f, 0.f}));
        const u32x2 d = {packh(we[0], wo[1]), packh(we[2], wo[3])};
        asm volatile("ds_write_b64 %0, %1 offset:%2" :: "v"(xd), "v"(d), "i"(32 * i) : "memory");
      }
      lds_inc<4>(cna, one);
      xe = FULL ? x4 : __builtin_amdgcn_raw_buffer_load_b128(xrs, eck * 16, 0, 16);
      if (!layer) {
        const __amdgpu_buffer_rsrc_t mrs = __builtin_amdgcn_make_buffer_rsrc(mark + b * 2032, 0, 2032 * 16, 0x00020000);
#pragma unroll
        for (int i = 0; i < 3; ++i) __builtin_amdgcn_raw_buffer_store_b128(MARK16, mrs, t * 16, i * 12288, 17);
      }
    }
    if (c == 1) {
      PIN(kd);
      if (!FULL) PIN(ur);
#pragma unroll
      for (int j = 0; j < 4; ++j) {
        ud[j] = FULL ? pack2(lo_f(ud[j]) * kd[1], hi_f(ud[j]) * kd[1]) : pack2(lo_h(ur[j]) * kd[1], hi_h(ur[j]) * kd[1]);
        PIN(ud[j]);
      }
    }
    if (c == NB / 2) {
      PIN(xw[0]); PIN(xw[1]);
      far = (f32x4){0.f, 0.f, 0.f, 0.f}; far1 = far;
      if (np) {
        far = MFH(xw[0], K8f[0], far);
        far1 = MFH(xw[1], K8f[1], far1);
      }
      if (FULL && np == 2) {
        PIN(xw[2]); PIN(xw[3]);
        far = MFH(xw[2], K8f[FULL ? 2 : 0], far);
        far1 = MFH(xw[3], K8f[FULL ? 3 : 0], far1);
      }
      PIN(far); PIN(far1);
    }
  }
  if (xtra) {
    asm volatile(
        "v_mfma_f32_16x16x32_f16 %3, %8, %7, %3\n\t"
        "v_mfma_f32_16x16x32_f16 %2, %8, %6, %2\n\t"
        "v_mfma_f32_16x16x32_f16 %1, %8, %5, %1\n\t"
        "s_waitcnt lgkmcnt(0)\n\t"
        "v_mfma_f32_16x16x32_f16 %0, %8, %4, %0"
        : "+v"(acc[0]), "+v"(acc[1]), "+v"(acc[2]), "+v"(acc[3]) : "v"(F[0]), "v"(F[1]), "v"(F[2]), "v"(F[3]), "v"(K[4 * NB]));
  }
  far += far1;
  asm volatile("ds_write_b128 %0, %1" :: "v"(frw), "v"(far) : "memory");
  __builtin_amdgcn_s_waitcnt(0x0f70);
  asm volatile("s_waitcnt lgkmcnt(1)\n\ts_nop 7" : "+v"(acc[0]), "+v"(acc[1]), "+v"(acc[2]), "+v"(acc[3]), "+v"(F[0]) :: "memory");
  if (FULL) {
    asm volatile("ds_write_b128 %0, %4 offset:384\n\tds_write_b128 %0, %3 offset:256\n\tds_write_b128 %0, %2 offset:128\n\tds_write_b128 %0, %1"
                 :: "v"(exw), "v"(acc[0]), "v"(acc[1]), "v"(acc[2]), "v"(acc[3]) : "memory");
  } else {
#pragma unroll
    for (int j = 3; j >= 0; --j) asm volatile("ds_write_b128 %0, %1" :: "v"(exw + ((16 * j + 4 * g + 4 * n) & 63) * 4), "v"(acc[j]) : "memory");
  }
  lds_inc<8>(cna, one);
  __builtin_amdgcn_s_setprio(PBE);
  ST(4);
  if (FULL || wq < 2) {
    lds_poll<8, 0>(cna, 4);
    const float kf = kd[0];
    f32x4 sa, sb;
    if (FULL) {
      const f32x4 e0 = *(lpf)(size_t)exr, e1 = *(lpf)(size_t)(exr + 16), e2 = *(lpf)(size_t)(exr + 8448), e3 = *(lpf)(size_t)(exr + 8464);
      const f32x4 g0 = *(lpf)(size_t)frr, g1 = *(lpf)(size_t)(frr + 16);
#pragma unroll
      for (int r = 0; r < 4; ++r) { sa[r] = fmaf(g0[r], fm, e0[r] + e2[r]) * kf; sb[r] = fmaf(g1[r], fm, e1[r] + e3[r]) * kf; }
    } else {
      f32x4 ea[4], eb[4];
#pragma unroll
      for (int i = 0; i < 4; ++i) { ea[i] = *(lpf)(size_t)(exr + 4096 * i); eb[i] = *(lpf)(size_t)(exr1 + 4096 * i); }
      const f32x4 g0 = *(lpf)(size_t)frr, g1 = *(lpf)(size_t)(frr + 16), g2 = *(lpf)(size_t)(frr + 2048), g3 = *(lpf)(size_t)(frr + 2064);
#pragma unroll
      for (int r = 0; r < 4; ++r) {
        sa[r] = fmaf(g0[r] + g2[r], fm, (ea[0][r] + ea[1][r]) + (ea[2][r] + ea[3][r])) * kf;
        sb[r] = fmaf(g1[r] + g3[r], fm, (eb[0][r] + eb[1][r]) + (eb[2][r] + eb[3][r])) * kf;
      }
    }
    const u32 yy[4] = {pack2(sa[0], sa[1]), pack2(sa[2], sa[3]), pack2(sb[0], sb[1]), pack2(sb[2], sb[3])};
    u32x4 ov;
#pragma unroll
    for (int j = 0; j < 4; ++j) {
      const u32 sm2 = pack2(lo_f(yy[j]) + lo_f(ud[j]), hi_f(yy[j]) + hi_f(ud[j]));
      ov[j] = min(pack2(lo_f(xe[j]) + lo_f(sm2), hi_f(xe[j]) + hi_f(sm2)), 0xfffffffeu);
    }
    __builtin_amdgcn_raw_buffer_store_b128(ov, ors, od, 0, BAUX);
    if (!layer) __builtin_amdgcn_raw_buffer_store_b128(ov, ors, oh, 0, BAUX);
  }
}

template <int layer>
__device__ __forceinline__ void b_phase(unsigned char* smem, const int b, const int t, const u16* xtin, float2* stats, u32* fstat,
                                        u16* xtout, u32x4* mark) {
  const int kid = layer ? 3 : 5;
  const int lane = t & 63, w = __builtin_amdgcn_readfirstlane(t >> 6);
  const int gq = w < 4 ? 2 : (w >> 2) - 1, wq = w & 3;
  const int cG = 5 * (b >> 1) + (gq == 2 ? 2 : 3 * (b & 1) + gq);
  ST_DECL
  if (w != 3) __builtin_amdgcn_s_setprio(PBU); else __builtin_amdgcn_s_setprio(3);
  if (w < 3) {
    const u32 sya = LDSA(smem) + SY_OFF + 256 * layer;
    lds_poll<SY_FREE, 2>(sya, 8);
    b_tables(smem, w, lane);
    lds_inc<80 + 12>(sya + 16 * w, 1u);
    ST(9);
  }
#define TSARG
  if (gq < 2) b_conv<true, layer>(smem, b, t, gq, wq, cG, xtin, stats, fstat, xtout, mark TSARG);
  else b_conv<false, layer>(smem, b, t, gq, wq, cG, xtin, stats, fstat, xtout, mark TSARG);
  ST(5);
  ST_FLUSH(kid);
}

struct MainArgs { const u16 *x, *wqkv, *wo; unsigned char* ws; u16* out; };
__global__ __launch_bounds__(768, 3) void k_main(MainArgs ma, KgenArgs ka) {
  __shared__ __attribute__((aligned(16))) unsigned char smem[A_LDS + 64];
  const int b = BIDX;
  u32x4 sink;
  int sb = b, t0 = TIDX;
  asm volatile(
      "s_cmp_lt_u32 %1, 16\n\t"
      "s_cbranch_scc0 1f\n\t"
      "v_cmp_gt_u32 vcc, 0x80, %2\n\t"
      "s_and_b64 exec, exec, vcc\n\t"
      "s_getpc_b64 s[40:41]\n\t"
      "s_add_u32 s40, s40, _Z6k_main8MainArgs8KgenArgs@rel32@lo+4\n\t"
      "s_addc_u32 s41, s41, _Z6k_main8MainArgs8KgenArgs@rel32@hi+12\n\t"
      "s_lshl_b32 s42, %1, 11\n\t"
      "s_and_b32 s42, s42, 0x4000\n\t"
      "v_lshl_add_u32 %3, %2, 7, s42\n\t"
      "s_mov_b32 s42, kmain_tail-_Z6k_main8MainArgs8KgenArgs\n\t"
      "global_load_dword %0, %3, s[40:41]\n\t"
      "v_add_u32 %3, 0x8000, %3\n\t"
      "v_min_u32 %4, s42, %3\n\t"
      "global_load_dword %4, %4, s[40:41]\n\t"
      "s_mov_b64 exec, -1\n\t"
      "1:"
      : "=&v"(sink[0]), "+s"(sb), "+v"(t0), "=&v"(sink[1]), "=&v"(sink[2]) :: "scc", "vcc", "s40", "s41", "s42", "memory");
  unsigned char* ws = ma.ws;
  const int w64 = __builtin_amdgcn_readfirstlane(t0) & ~63;
#define GETT(t) asm volatile("v_mbcnt_lo_u32_b32 %0, -1, %1\n\tv_mbcnt_hi_u32_b32 %0, -1, %0" : "=v"(t) : "s"(w64))
#define LAYER(L) { \
    int t; \
    GETT(t); \
    unsigned char* ho = ws + WS_O + L * HO_BYTES; \
    u16* x1 = (u16*)(ho + HO_X1); \
    u32* fs = (u32*)(ho + HO_FS); \
    u16* nxt = (u16*)(ws + (L ? WS_FIN : WS_IM1)); \
    a_phase<L>(smem, b, t, L ? (const u16*)(ws + WS_IM1) : ma.x, ma.wqkv, ma.wo, ws, (u16*)ho, x1, (float2*)(ho + HO_ST), fs, ka, sink, tg0, tg1); \
    GETT(t); \
    b_phase<L>(smem, b, t, x1, (float2*)(ho + HO_ST), fs, nxt, (u32x4*)(ws + WS_O + HO_BYTES)); }
  u32 tg0, tg1;
  LAYER(0)
  LAYER(1)
  {
    int t;
    GETT(t);
    const int lane = t & 63, w = __builtin_amdgcn_readfirstlane(t >> 6);
    const int rb = 8 * ((b & 7) >> 1) + (b >> 5), hg = 4 * (b & 1) + ((b >> 3) & 3);
    ST_DECL
    u16* sm = (u16*)smem;
    __syncthreads();
    __builtin_amdgcn_s_sleep(TSLEEP);
    if (w < 10) {
      const __amdgpu_buffer_rsrc_t rs = __builtin_amdgcn_make_buffer_rsrc(ws + WS_FIN + (size_t)(rb * 640 + hg * 80) * 128, 0, 10240, 0x00020000);
      u32x4 v;
      int spins = 0;
      _Pragma("unroll 1")
      while (true) {
        asm volatile("" ::: "memory");
        v = __builtin_amdgcn_raw_buffer_load_b128(rs, t * 16, 0, 16);
        if (__builtin_amdgcn_ballot_w64(umax3(v[0], v[1], max(v[2], v[3])) == 0xffffffffu) == 0 || ++spins > GCAP) break;
        __builtin_amdgcn_s_sleep(FPOLL);
      }
      RETRY(7, spins);
      *(u32x4*)&sm[(t >> 3) * 72 + 8 * (t & 7)] = v;
    }
    ST(1);
    __syncthreads();
    if (w < 10) {
      const int u = t >> 4, rg = (u * 205) >> 11, oc = u - 10 * rg, q = (lane >> 2) & 3, p = lane & 3;
      const u16* base = sm + (8 * oc + q) * 72 + 16 * rg + 4 * p;
      *(bf16x8*)(ma.out + (size_t)(rb * 64 + 16 * rg + (lane & 15)) * D_ + hg * 80 + 8 * oc) = tr_frag(base, base + 4 * 72);
    }
    ST(2);
    ST_FLUSH(4);
  }
}
extern "C" __device__ __attribute__((used, noinline)) void kmain_tail() {}
extern "C" void kernel_launch(void* const* d_in, const int* in_sizes, int n_in, void* d_out, int out_size,
                              void* d_ws, size_t ws_size, hipStream_t stream) {
  unsigned char* ws = (unsigned char*)d_ws;
  KgenArgs ka;
  ka.logA = (const u16*)d_in[4]; ka.Aim = (const u16*)d_in[5]; ka.Cre = (const u16*)d_in[6]; ka.Cim = (const u16*)d_in[7];
  ka.logdt = (const u16*)d_in[8]; ka.Dskip = (const u16*)d_in[9];
  MainArgs ma;
  ma.x = (const u16*)d_in[0];
  ma.wqkv = (const u16*)d_in[1]; ma.wo = (const u16*)d_in[2];
  ma.ws = ws; ma.out = (u16*)d_out;
  k_main<<<256, 768, 0, stream>>>(ma, ka);
}
#if defined(__HIP_DEVICE_COMPILE__) && !defined(ALLOW_PK)
#pragma clang attribute pop
#endif
```

```cpp
#if defined(__HIP_DEVICE_COMPILE__) && !defined(ALLOW_PK)
#pragma clang attribute push(__attribute__((target("no-packed-fp32-ops"))), apply_to = function)
#endif
#include <hip/hip_runtime.h>
#include <hip/hip_bf16.h>
#include <stdint.h>

#define BIDX ((int)__builtin_amdgcn_workgroup_id_x())
#define TIDX ((int)__builtin_amdgcn_workitem_id_x())
typedef unsigned short u16;
typedef unsigned int u32;
typedef unsigned long long u64;
typedef __attribute__((ext_vector_type(8))) short bf16x8;
typedef __attribute__((ext_vector_type(4))) short s16x4;
typedef __attribute__((ext_vector_type(4))) float f32x4;
typedef __attribute__((ext_vector_type(2))) float f32x2;
typedef __attribute__((ext_vector_type(2))) __bf16 bf2v;
typedef __attribute__((address_space(3))) s16x4* lds_s16x4_p;
typedef __attribute__((ext_vector_type(8))) u32 u32x8;
typedef __attribute__((ext_vector_type(4))) u32 u32x4;
typedef __attribute__((ext_vector_type(2))) u32 u32x2;
typedef __attribute__((address_space(1))) u32 gu32;
typedef __attribute__((address_space(1))) u64 gu64;

#define S_ 2048
#define XP 2112
#define D_ 640
#define NST 192
#define CHAN(b, q) ((q) == 2 ? (b) >> 1 : 128 + 256 * (q) + (b))

#define WS_WQ  0
#define WS_WOT (WS_WQ + 1920 * 640 * 2)
#define WS_CW  (WS_WOT + 640 * 640 * 2)
#define WS_KP  (WS_CW + 4 * 2048 * 4)
#define WS_IM0 (WS_KP + 640 * 768 * 4)
#define WS_IM1 (WS_IM0 + 32 * 102400)
#define HO_ST (2048 * 640 * 2)
#define HO_X1 (HO_ST + 2048 * 128)
#define HO_FS (HO_X1 + 640 * XP * 2)
#define HO_BYTES (256 * 1392 * 16)
#define WS_O   (WS_IM1 + 32 * 102400)
#define WS_FIN (WS_O + 2 * HO_BYTES)
#define WS_FLG (WS_FIN + 2048 * 640 * 2)
#define FL_W 64
#define FL_M (FL_W + 480 * 64)
#define FL_M2 (FL_M + 384 * 64)
#define FL_END (FL_M2 + 256 * 8)
#define WS_END (WS_FLG + FL_END)

#define RETRY(i, n)
#define ST_DECL
#define ST(i)
#define ST_FLUSH(kid)

__device__ __forceinline__ float bf2f(u16 x) { return __uint_as_float(((u32)x) << 16); }
__device__ __forceinline__ u32 pack2(float a, float b) {
  f32x2 v = {a, b};
  bf2v r = __builtin_convertvector(v, bf2v);
  return __builtin_bit_cast(u32, r);
}
__device__ __forceinline__ float lo_f(u32 w) { return __uint_as_float(w << 16); }
__device__ __forceinline__ float hi_f(u32 w) { return __uint_as_float(w & 0xffff0000u); }
__device__ __forceinline__ float rbf(float f) { return lo_f(pack2(f, f)); }
__device__ __forceinline__ bf16x8 as_frag(uint4 v) {
  union { uint4 u; bf16x8 f; } c; c.u = v; return c.f;
}
__device__ __forceinline__ float2 cmul(float2 a, float2 b) {
  return make_float2(a.x * b.x - a.y * b.y, a.x * b.y + a.y * b.x);
}
__device__ __forceinline__ float2 cexp_scaled(float zr, float zi, float e) {
  float ar = zr * e, ai = zi * e;
  float m = __expf(ar);
  const float INV2PI_HI = 0.15915493667125702f;
  const float INV2PI_LO = 6.4206382432985265e-09f;
  float t = ai * INV2PI_HI;
  float err = fmaf(ai, INV2PI_HI, -t);
  float r = t - rintf(t);
  r = r + (err + ai * INV2PI_LO);
  float s = __builtin_amdgcn_sinf(r), c = __builtin_amdgcn_cosf(r);
  return make_float2(m * c, m * s);
}
__device__ __forceinline__ void cexp2(f32x2 zr, f32x2 zi, float e, f32x2& re, f32x2& im) {
  f32x2 ar = zr * e, ai = zi * e;
  f32x2 m = {__expf(ar.x), __expf(ar.y)};
  const float INV2PI_HI = 0.15915493667125702f;
  const float INV2PI_LO = 6.4206382432985265e-09f;
  f32x2 t = ai * INV2PI_HI;
  f32x2 err = {fmaf(ai.x, INV2PI_HI, -t.x), fmaf(ai.y, INV2PI_HI, -t.y)};
  f32x2 r = t - (f32x2){rintf(t.x), rintf(t.y)};
  r = r + (err + ai * INV2PI_LO);
  re = m * (f32x2){__builtin_amdgcn_cosf(r.x), __builtin_amdgcn_cosf(r.y)};
  im = m * (f32x2){__builtin_amdgcn_sinf(r.x), __builtin_amdgcn_sinf(r.y)};
}
__device__ __forceinline__ float dpp_ror8(float x) {
  return __builtin_bit_cast(float, __builtin_amdgcn_update_dpp(0, __builtin_bit_cast(int, x), 0x128, 0xf, 0xf, false));
}
__device__ __forceinline__ float BCAST0(float x) {
  return __builtin_bit_cast(float, __builtin_amdgcn_update_dpp(0, __builtin_bit_cast(int, x), 0x150, 0xf, 0xf, false));
}
__device__ __forceinline__ float rowsum16(float x) {
  x += __builtin_bit_cast(float, __builtin_amdgcn_update_dpp(0, __builtin_bit_cast(int, x), 0xB1, 0xf, 0xf, false));
  x += __builtin_bit_cast(float, __builtin_amdgcn_update_dpp(0, __builtin_bit_cast(int, x), 0x4E, 0xf, 0xf, false));
  x += __builtin_bit_cast(float, __builtin_amdgcn_update_dpp(0, __builtin_bit_cast(int, x), 0x141, 0xf, 0xf, false));
  x += __builtin_bit_cast(float, __builtin_amdgcn_update_dpp(0, __builtin_bit_cast(int, x), 0x140, 0xf, 0xf, false));
  return x;
}
__device__ __forceinline__ float dpp_xor1(float x) {
  return __builtin_bit_cast(float, __builtin_amdgcn_update_dpp(0, __builtin_bit_cast(int, x), 0xB1, 0xf, 0xf, false));
}
__device__ __forceinline__ bf16x8 tr_frag(const u16* p0, const u16* p1) {
  s16x4 a = __builtin_amdgcn_ds_read_tr16_b64_v4i16((lds_s16x4_p)p0);
  s16x4 b = __builtin_amdgcn_ds_read_tr16_b64_v4i16((lds_s16x4_p)p1);
  return __builtin_shufflevector(a, b, 0, 1, 2, 3, 4, 5, 6, 7);
}

#define X1_P 248
__device__ __forceinline__ float dot2bf(u32 a, u32 b, float c) {
  return __builtin_amdgcn_fdot2_f32_bf16(__builtin_bit_cast(bf2v, a), __builtin_bit_cast(bf2v, b), c, false);
}
__device__ __forceinline__ float xsum16_32(float x) {
  auto s = __builtin_amdgcn_permlane16_swap(__float_as_uint(x), __float_as_uint(x), false, false);
  x = __uint_as_float(s[0]) + __uint_as_float(s[1]);
  auto t = __builtin_amdgcn_permlane32_swap(__float_as_uint(x), __float_as_uint(x), false, false);
  return __uint_as_float(t[0]) + __uint_as_float(t[1]);
}
__device__ __forceinline__ float xmax16_32(float x) {
  auto s = __builtin_amdgcn_permlane16_swap(__float_as_uint(x), __float_as_uint(x), false, false);
  x = fmaxf(__uint_as_float(s[0]), __uint_as_float(s[1]));
  auto t = __builtin_amdgcn_permlane32_swap(__float_as_uint(x), __float_as_uint(x), false, false);
  return fmaxf(__uint_as_float(t[0]), __uint_as_float(t[1]));
}

#define WT " sc0 sc1"
template <class T> __device__ __forceinline__ void st16(void* p, T v) {
  asm volatile("global_store_dwordx4 %0, %1, off" WT "\n\ts_nop 1" :: "v"(p), "v"(v) : "memory");
}
template <class T> __device__ __forceinline__ void st8(void* p, T v) {
  asm volatile("global_store_dwordx2 %0, %1, off" WT :: "v"(p), "v"(v) : "memory");
}

typedef __attribute__((ext_vector_type(8))) _Float16 f16x8;
typedef __attribute__((ext_vector_type(2))) _Float16 f16x2;
__device__ __forceinline__ u32 packh(float a, float b) {
  f32x2 v = {a, b};
  return __builtin_bit_cast(u32, __builtin_convertvector(v, f16x2));
}
__device__ __forceinline__ float lo_h(u32 w) { return (float)__builtin_bit_cast(f16x2, w).x; }
__device__ __forceinline__ float hi_h(u32 w) { return (float)__builtin_bit_cast(f16x2, w).y; }
__device__ __forceinline__ f16x8 as_hfrag(u32 a, u32 b, u32 c, u32 d) { return __builtin_bit_cast(f16x8, (u32x4){a, b, c, d}); }
__device__ __forceinline__ float wave_max(float x) {
  x = fmaxf(x, __builtin_bit_cast(float, __builtin_amdgcn_update_dpp(0, __builtin_bit_cast(int, x), 0xB1, 0xf, 0xf, false)));
  x = fmaxf(x, __builtin_bit_cast(float, __builtin_amdgcn_update_dpp(0, __builtin_bit_cast(int, x), 0x4E, 0xf, 0xf, false)));
  x = fmaxf(x, __builtin_bit_cast(float, __builtin_amdgcn_update_dpp(0, __builtin_bit_cast(int, x), 0x141, 0xf, 0xf, false)));
  x = fmaxf(x, __builtin_bit_cast(float, __builtin_amdgcn_update_dpp(0, __builtin_bit_cast(int, x), 0x140, 0xf, 0xf, false)));
  return xmax16_32(x);
}
template <int T> __device__ __forceinline__ void pow2_scale(float m, float& s, float& inv) {
  int e = (int)(__float_as_uint(m) >> 23);
  e = min(max(e, T + 2), 250);
  s = __uint_as_float((u32)(254 + T - e) << 23);
  inv = __uint_as_float((u32)(e - T) << 23);
}
__device__ __forceinline__ void cexpr(float zl, float e, float zh, float ep, float& re, float& im) {
  const float m = __builtin_amdgcn_exp2f(zl * e);
  const float r = zh * ep;
  re = m * __builtin_amdgcn_cosf(r);
  im = m * __builtin_amdgcn_sinf(r);
}
struct KgenArgs { const u16 *logA, *Aim, *Cre, *Cim, *logdt, *Dskip; };
#define LDSA(p) ((u32)(size_t)(__attribute__((address_space(3))) void*)(p))
#define PIN(x) asm volatile("" : "+v"(x))
template <int OFF> __device__ __forceinline__ void lds_inc(u32 a, u32 one) {
  asm volatile("s_mov_b64 exec, 1\n\tds_add_u32 %0, %1 offset:%2\n\ts_mov_b64 exec, -1" :: "v"(a), "v"(one), "i"(OFF) : "memory");
}
template <int OFF, int SLEEP> __device__ __forceinline__ void lds_poll(u32 a, int target) {
  u32 v; int s, cnt;
  asm volatile(
      "s_mov_b32 %2, 0x100000\n\t"
      "1:\n\t"
      "ds_read_b32 %0, %3 offset:%5\n\t"
      "s_waitcnt lgkmcnt(0)\n\t"
      "v_readfirstlane_b32 %1, %0\n\t"
      "s_nop 0\n\t"
      "s_cmp_ge_i32 %1, %4\n\t"
      "s_cbranch_scc1 2f\n\t"
      "s_sleep %6\n\t"
      "s_sub_u32 %2, %2, 1\n\t"
      "s_cbranch_scc0 1b\n\t"
      "2:\n\t"
      : "=&v"(v), "=&s"(s), "=&s"(cnt) : "v"(a), "s"(target), "i"(OFF), "i"(SLEEP) : "memory", "scc");
}
template <int OFF, int SLEEP> __device__ __forceinline__ void lds_poll2(u32 a, int target, u32& v) {
  int s, cnt;
  asm volatile(
      "v_readfirstlane_b32 %1, %0\n\t"
      "s_mov_b32 %2, 0x100000\n\t"
      "s_cmp_ge_i32 %1, %4\n\t"
      "s_cbranch_scc1 2f\n\t"
      "1:\n\t"
      "ds_read_b32 %0, %3 offset:%5\n\t"
      "s_waitcnt lgkmcnt(0)\n\t"
      "v_readfirstlane_b32 %1, %0\n\t"
      "s_nop 0\n\t"
      "s_cmp_ge_i32 %1, %4\n\t"
      "s_cbranch_scc1 2f\n\t"
      "s_sleep %6\n\t"
      "s_sub_u32 %2, %2, 1\n\t"
      "s_cbranch_scc0 1b\n\t"
      "2:\n\t"
      : "+v"(v), "=&s"(s), "=&s"(cnt) : "v"(a), "s"(target), "i"(OFF), "i"(SLEEP) : "memory", "scc");
}
#define SY_PART 0
#define SY_FREE 4
#define SY_WO 8
#define SY_FIN 24
#define SY_X1 32
#define SY_FS 36
#define SY_WI 40
#define SY_KP 44
#define SY_MK 48
#define SY_RV 52
#define SY_TI 56
#define SY_PG 64
#define SY_X 128
#define SY_STAT 168
#define SY_W 172
#define SY_RED 192
#define SY_Z 224
template <int SLEEP> __device__ __forceinline__ void pg_poll(u32 a, u32 target) {
  int cnt = 1 << 20;
  _Pragma("unroll 1")
  while (true) {
    u32x4 v;
    asm volatile("ds_read_b128 %0, %1 offset:%2\n\ts_waitcnt lgkmcnt(0)" : "=v"(v) : "v"(a), "i"(SY_PG) : "memory");
    if ((u32)__builtin_amdgcn_readfirstlane(min(min(v[0], v[1]), min(v[2], v[3]))) >= target || --cnt < 0) break;
    if (SLEEP) __builtin_amdgcn_s_sleep(SLEEP);
  }
}
__device__ __forceinline__ void kgen_units(int lane, const float* Kd, int nk, f32x4 (&acc)[3]) {
  const int i = lane & 15, kg = lane >> 4;
  const float eE = 8.f * (float)i;
  const int r_ = i & 3;
  const float e0 = (float)((i < 8 ? 2 * r_ : r_ == 0 ? 512 : r_ == 1 ? 516 : 512 * r_) + 32 * (i & 4));
  const float e0p = i < 8 ? e0 : 0.25f * e0;
  typedef __attribute__((address_space(3))) const f32x4* lf4;
  typedef __attribute__((address_space(3))) const u32x4* lu4;
  u32 pp = LDSA(Kd) + 16 * kg, pc = pp + (i < 8 ? 0 : 256);
#pragma unroll 1
  for (int ks = 0; ks < nk; ++ks) {
    PIN(pp); PIN(pc);
    const f32x4 zl = *(lf4)(size_t)pp, zs = ((lf4)(size_t)pc)[16], zh = ((lf4)(size_t)pp)[16];
    const f32x4 lc = ((lf4)(size_t)pp)[48], ph = ((lf4)(size_t)pp)[64];
    const u32x4 ma[2] = {((lu4)(size_t)pc)[80], ((lu4)(size_t)pp)[112]};
    pp += 64; pc += 64;
    float r[4], q[4], gr[4], gi[4];
#pragma unroll
    for (int e = 0; e < 4; ++e) cexpr(zl[e], e0, zs[e], e0p, gr[e], gi[e]);
#pragma unroll
    for (int e = 0; e < 4; ++e) {
      const float m = __builtin_amdgcn_exp2f(fmaf(zl[e], eE, lc[e])), th = fmaf(zh[e], -eE, ph[e]);
      r[e] = m * __builtin_amdgcn_cosf(th);
      q[e] = m * __builtin_amdgcn_sinf(th);
    }
    const f16x8 eh = as_hfrag(packh(r[0], r[1]), packh(r[2], r[3]), packh(q[0], q[1]), packh(q[2], q[3]));
    const u32 g[4] = {packh(gr[0], gr[1]), packh(gr[2], gr[3]), packh(gi[0], gi[1]), packh(gi[2], gi[3])};
    acc[0] = __builtin_amdgcn_mfma_f32_16x16x32_f16(as_hfrag(g[0], g[1], g[2], g[3]), eh, acc[0], 0, 0, 0);
#pragma unroll
    for (int m = 0; m < 2; ++m) {
      u32 d[4];
#pragma unroll
      for (int e = 0; e < 2; ++e) {
        const f16x2 xr = __builtin_bit_cast(f16x2, g[e]), xi = __builtin_bit_cast(f16x2, g[2 + e]);
        const u32 ur = ma[m][e], ui = ma[m][2 + e];
        const f16x2 mr = __builtin_bit_cast(f16x2, ur), mi = __builtin_bit_cast(f16x2, ui);
        d[e] = __builtin_bit_cast(u32, __builtin_elementwise_fma(xr, mr, -(xi * mi)));
        d[2 + e] = __builtin_bit_cast(u32, __builtin_elementwise_fma(xi, mr, xr * mi));
      }
      acc[m + 1] = __builtin_amdgcn_mfma_f32_16x16x32_f16(as_hfrag(d[0], d[1], d[2], d[3]), eh, acc[m + 1], 0, 0, 0);
    }
  }
}
template <int NQ> struct KgenIn { u16 la[NQ], ai[NQ], cr[NQ], ci[NQ], ld, ds; };
template <int NQ, int MODE>
__device__ __forceinline__ void kgen_load(int c, int ks0, int nk, int lane, const KgenArgs& ka, KgenIn<NQ>& in) {
  in.ld = ka.logdt[c];
#pragma unroll
  for (int q = 0; q < NQ; ++q) {
    const int n = 64 * q + lane, gi = c * NST + 16 * ks0 + (n < 16 * nk ? n : 0);
    in.la[q] = ka.logA[gi]; in.ai[q] = ka.Aim[gi]; in.cr[q] = ka.Cre[gi]; in.ci[q] = ka.Cim[gi];
  }
  if (MODE == 1) in.ds = ka.Dskip[c];
}
#define ATC0 1.591508832e-01f
#define ATC1 -5.292989150e-02f
#define ATC2 3.075435195e-02f
#define ATC3 -1.841891266e-02f
#define ATC4 8.268183977e-03f
#define ATC5 -1.824840286e-03f
#define KU 4
__device__ __forceinline__ float kgen_wave(int lane, const KgenIn<1>& in, float* Kd, f32x4 (&acc)[3]) {
#pragma unroll
  for (int m = 0; m < 3; ++m) acc[m] = (f32x4){0.f, 0.f, 0.f, 0.f};
  float cs, csi;
  const u32 za[1] = {in.la[0] | ((u32)in.ai[0] << 16)}, ca[1] = {in.cr[0] | ((u32)in.ci[0] << 16)};
  const float dt = rbf(__expf(bf2f(in.ld)));
  pow2_scale<10>(wave_max(fmaxf(fabsf(bf2f(in.cr[0])), fabsf(bf2f(in.ci[0])))), cs, csi);
  {
    const float zl = (-rbf(__expf(lo_f(za[0]))) * dt) * 1.4426950408889634f, zi = hi_f(za[0]) * dt;
    const float zh = zi * 0.15915494309189535f;
    const float z1 = zh - rintf(zh);
    float ar, ai, br, bi;
    cexpr(zl, 1.0f, z1, 1.0f, ar, ai);
    cexpr(zl, 256.0f, 256.0f * zh - rintf(256.0f * zh), 1.0f, br, bi);
    Kd[lane] = zl;
    Kd[64 + lane] = z1;
    Kd[128 + lane] = 4.0f * zh - rintf(4.0f * zh);
    const float cr = lo_f(ca[0]) * cs, ci = hi_f(ca[0]) * cs, ax = fabsf(cr), ay = fabsf(ci);
    const float tq = fminf(ax, ay) * __builtin_amdgcn_rcpf(fmaxf(fmaxf(ax, ay), 1e-30f)), t2 = tq * tq;
    float an = tq * fmaf(t2, fmaf(t2, fmaf(t2, fmaf(t2, fmaf(t2, ATC5, ATC4), ATC3), ATC2), ATC1), ATC0);
    an = ay > ax ? 0.25f - an : an;
    an = cr < 0.f ? 0.5f - an : an;
    Kd[192 + lane] = 0.5f * __builtin_amdgcn_logf(fmaf(cr, cr, ci * ci));
    Kd[256 + lane] = ci < 0.f ? an : -an;
    u16* kh = (u16*)(Kd + 320) + 8 * (lane >> 2) + (lane & 3);
    const u32 h0 = packh(ar, ai), h1 = packh(br * br - bi * bi, 2.0f * br * bi), h2 = packh(br, bi);
    kh[0] = (u16)h0; kh[4] = (u16)(h0 >> 16);
    kh[128] = (u16)h1; kh[132] = (u16)(h1 >> 16);
    kh[256] = (u16)h2; kh[260] = (u16)(h2 >> 16);
    asm volatile("s_waitcnt lgkmcnt(0)" ::: "memory");
    kgen_units(lane, Kd, KU, acc);
    asm volatile("" ::: "memory");
  }
#pragma unroll
  for (int m = 0; m < 3; ++m) acc[m] *= csi;
  return dt;
}
__device__ __forceinline__ void kgen_fin(int lane, float dt, float dsk, float* Kd, f32x4 (&acc)[3]) {
  const int i = lane & 15, kg = lane >> 4;
  float km = 0.f, ks_, ksi;
#pragma unroll
  for (int m = 0; m < 3; ++m)
#pragma unroll
    for (int r = 0; r < 4; ++r) km = fmaxf(km, fabsf(acc[m][r]));
  pow2_scale<14>(wave_max(km), ks_, ksi);
#pragma unroll
  for (int m = 0; m < 3; ++m) acc[m] *= ks_;
  u16* kq = (u16*)Kd;
  const u32 t0 = packh(acc[2][0], acc[2][1]), t1 = packh(acc[2][2], acc[2][3]);
  if (kg < 2) {
    *(u32x4*)&kq[128 * kg + 8 * i] = (u32x4){packh(acc[0][0], acc[1][0]), packh(acc[0][1], acc[1][1]), packh(acc[0][2], acc[1][2]), packh(acc[0][3], acc[1][3])};
    *(u32x2*)&kq[256 + 64 * kg + 4 * i] = (u32x2){t0, t1};
  } else {
    const u32 s1 = packh(acc[0][2], acc[0][3]);
    *(u32*)&kq[320 + 32 * kg + 2 * i] = packh(acc[0][0], acc[0][1]);
    *(u32*)&kq[384 + 32 * kg + 2 * i] = t0;
    u16* kr = kq + 480 + lane;
    kr[0] = (u16)s1; kr[64] = (u16)(s1 >> 16); kr[32] = (u16)t1; kr[96] = (u16)(t1 >> 16);
    if (lane == 32) kq[640] = (u16)packh(acc[1][3], 0.f);
  }
  if (lane == 0) { Kd[384] = (dt * (2.0f / 256.0f)) * ksi; Kd[385] = dsk * (1.0f / 256.0f); }
}

#define A_R1 105600
#define A_R2 (A_R1 + 80 * X1_P * 2)
#define SY_OFF (A_R2 + 1280)
#define CW_OFF (SY_OFF + 512)
#define KQ_OFF (CW_OFF + 1024)
#define BND_OFF (KQ_OFF + 3 * 3072)
#define WO1_OFF 52800
#define A_LDS (BND_OFF + 2048)
#define VMCNT(n) asm volatile("s_waitcnt vmcnt(" #n ")" ::: "memory")
template <int N> __device__ __forceinline__ void vmwait() { asm volatile("s_waitcnt vmcnt(%0)" :: "i"(N) : "memory"); }
__device__ __forceinline__ void vmwaitn(int n) {
  switch (n) {
    case 0: VMCNT(0); break; case 5: VMCNT(5); break; case 10: VMCNT(10); break; case 15: VMCNT(15); break; default: VMCNT(20); break;
  }
}
__device__ __forceinline__ u32 umax3(u32 a, u32 b, u32 c) { return max(max(a, b), c); }
#define XDEP 2
#define RS0 4
#define RS1 5
#define OSLEEP1 0
#define SSLEEP 8
#define OSLEEP 14
#define PG1B 0
#define PG1 2
#define PG2 2
#define XSLEEP 8
#define WOMODE 2
#define PG1P 3
#define XPOLL 4
#define XEARLY 1
#define WPW 0
#define WSLEEP0 0
#define GCAP (1 << 16)
#define X1T_OFF 130176
#define MARK16 ((u32x4){0xffffffffu, 0xffffffffu, 0xffffffffu, 0xffffffffu})
#define WSLEEP 2
#define IMPOS 2
#define XMPOS 1
#define IMSL 0
#define MSLEEP 32
#define STX(i)
#define STY(i) ST(i)
#define LANE(x) int x; asm volatile("v_mbcnt_lo_u32_b32 %0, -1, 0\n\tv_mbcnt_hi_u32_b32 %0, -1, %0" : "=v"(x))
#define NST0 7
#define NST1 2
#define PF 2
#define PTX 1
#define PTW 1
#define PTA 2
#define WOAT 28
#define FPOS 2
#define PFK 0
#define WARM 0
#define WOSL 0
#define EXP 0
#define XSSL0 24
#define XE 2
#define MSL0 1
#define MSL1 2
#define MSL2 2
#define P0E 2
#define P1F 1
#define MKW 4
#define MKAT 12
#define DQ 5
#define XSAT 20
#define CWAT 4
#define OSLEEP2 0
#define CWW 5
#define PFS 3
#define PTA1 2
#define APS 1
#define OSLEEP3 8
#define PFD 3
#define XSLEEP1 4
#define XJ0 2
#define XJ1 3
#define XSAT0 12
#define PTO 1
#define MKPOS 1
#define PCW 3
#define XSRT 8
#define XSSL 5
__device__ __forceinline__ f32x4 ln_fin(float s, float ss) {
  const float mean = s * (1.0f / 640.0f);
  const float var = fmaxf(ss * (1.0f / 640.0f) - mean * mean, 0.f);
  const float m = -rbf(mean), r = __builtin_amdgcn_rcpf(rbf(__builtin_amdgcn_sqrtf(rbf(rbf(var) + 1e-5f))));
  return (f32x4){m, m, r, r};
}
template <int layer>
__device__ __forceinline__ void a_phase(unsigned char* smem, const int b, const int t, const u16* xt, const u16* wqkv, const u16* wo, unsigned char* ws,
                                        u16* obuf, u16* xtout, float2* stats, u32* fstat, const KgenArgs& ka, u32x4& sink, u32& tg0, u32& tg1) {
  u16* wq = (u16*)(ws + WS_WQ);
  u16* wot = (u16*)(ws + WS_WOT);
  const __amdgpu_buffer_rsrc_t frs = __builtin_amdgcn_make_buffer_rsrc(ws + WS_FLG, 0, FL_END, 0x00020000);
  const int kid = layer ? 2 : 1;
  u16* At = (u16*)smem;
  u16* St = (u16*)(smem + A_R1);
  const int xcd = b & 7, idx = b >> 3;
  const int rb = 8 * (xcd >> 1) + (idx & 7), hg = 4 * (xcd & 1) + (idx >> 3), s0 = rb * 64;
  const int rot = hg >> 1, xs0 = 3 * rot + 2 * (hg & 1);
#define PAIR(p, r) ((p) < 6 ? (p) : 6 + (((p) - 6 + (r)) & 3))
  const int lane = t & 63, w = __builtin_amdgcn_readfirstlane(t >> 6);
  const int l15 = lane & 15, g = lane >> 4;
  u32 sya = LDSA(smem) + SY_OFF + 256 * layer, one = 1u;
  const bf16x8 onesf = as_frag(make_uint4(0x3f803f80u, 0x3f803f80u, 0x3f803f80u, 0x3f803f80u));
  u32x2 tag;
  ST_DECL

  const __amdgpu_buffer_rsrc_t xsrs = __builtin_amdgcn_make_buffer_rsrc(ws + WS_IM0 + layer * 262144, 0, 262144, 0x00020000);
  if (!layer) {
#define XDMA0(P) { const int pc = PAIR(P, rot); \
      const u16* sp = xt + (size_t)(s0 - 16 + (lane >> 3)) * D_ + 8 * ((lane & 7) ^ ((lane >> 3) & 6)) + pc * 64; \
      _Pragma("unroll") for (int i = 0; i < 10; ++i) \
        __builtin_amdgcn_global_load_lds((const u32*)(sp + 8 * i * D_ + (i < 2 && !rb ? 16 * D_ : 0)), (u32*)(At + ((P) * 10 + i) * 512), 16, 0, 0); }
#define WPOLL { \
        __builtin_amdgcn_s_sleep(WSLEEP0); \
        int spins = 0; \
        _Pragma("unroll 1") \
        while (true) { \
          asm volatile("" ::: "memory"); \
          const u32x2 f = __builtin_amdgcn_raw_buffer_load_b64(frs, min(lane, 59) * 64, FL_W + hg * 3840, 16); \
          if (__builtin_amdgcn_ballot_w64(f[0] != tg0 || f[1] != tg1) == 0 || ++spins > GCAP) break; \
          __builtin_amdgcn_s_sleep(WSLEEP); \
        } \
        RETRY(0, spins); \
        lds_inc<SY_W>(sya, one); \
      }
    if (__builtin_expect(w < 2, 1)) {
      const int r6 = (lane * 205) >> 11, c10 = lane - 10 * r6;
      const int q = (lane >> 2) & 3, p4 = lane & 3;
      __builtin_amdgcn_s_setprio(3);
      const bool tw = b >= 16;
      const int j = 2 * b - 32 + w, jq = j / 3, part = j - 3 * jq, jk = jq >> 3, jh = jq & 7;
      if (__builtin_expect(tw, 1)) {
        const u16* src = wqkv + (size_t)(32 * jk + r6) * 1920 + part * 640 + jh * 80 + 8 * c10;
        u16* tl = St + 13952 + 2560 * w;
        if (lane < 60) {
#pragma unroll
          for (int i = 0; i < 5; ++i) __builtin_amdgcn_global_load_lds((const u32*)(src + i * 11520), (u32*)(tl + i * 480), 16, 0, 0);
          if (lane < 20) __builtin_amdgcn_global_load_lds((const u32*)(src + 5 * 11520), (u32*)(tl + 5 * 480), 16, 0, 0);
        }
      }
      asm volatile("buffer_load_dwordx2 %0, off, %1, 0 sc1" : "=v"(tag) : "s"(frs) : "memory");
      __builtin_amdgcn_s_barrier();
      const int pb = l15 + (part ? 40 * part - 40 : 80), pd = part ? 120 : 0;
      int vo[5], co[2];
#pragma unroll
      for (int ct = 0; ct < 5; ++ct) {
        const int col = pb + 16 * ct + (16 * ct + l15 >= 40 ? pd : 0);
        vo[ct] = (int)((0xeda6273b51c9840ull >> (4 * (col >> 4))) & 15) * 1024 + (g * 16 + (col & 15)) * 16;
        PIN(vo[ct]);
      }
      co[0] = (pb + 16 * g + (lane >= 40 ? pd : 0)) * 4; co[1] = g ? -1 : (pb + 64 + pd) * 4;
      u32 ta = LDSA(St + 13952 + 2560 * w) + (8 * g + q) * 160 + 8 * p4;
      PIN(ta); PIN(co[0]); PIN(co[1]);
      asm volatile("s_waitcnt vmcnt(0)" : "+v"(tag), "+v"(sink) :: "memory");
      tag[0] += 1u;
      if (__builtin_expect(tw, 1)) {
        const int kp = 2 * PAIR(jk >> 1, -(jh >> 1)) + (jk & 1);
        const __amdgpu_buffer_rsrc_t trs = __builtin_amdgcn_make_buffer_rsrc((void*)(wq + (size_t)jh * 153600 + kp * 7680), 0, 15360, 0x00020000);
        const __amdgpu_buffer_rsrc_t crs = __builtin_amdgcn_make_buffer_rsrc(ws + WS_CW + (jh * 20 + jk) * 1024, 0, 1024, 0x00020000);
        s16x4 fl[5], fh[5];
        asm volatile(
            "ds_read_b64_tr_b16 %0, %10\n\t"
            "ds_read_b64_tr_b16 %5, %10 offset:640\n\t"
            "ds_read_b64_tr_b16 %1, %10 offset:32\n\t"
            "ds_read_b64_tr_b16 %6, %10 offset:672\n\t"
            "ds_read_b64_tr_b16 %2, %10 offset:64\n\t"
            "ds_read_b64_tr_b16 %7, %10 offset:704\n\t"
            "ds_read_b64_tr_b16 %3, %10 offset:96\n\t"
            "ds_read_b64_tr_b16 %8, %10 offset:736\n\t"
            "ds_read_b64_tr_b16 %4, %10 offset:128\n\t"
            "ds_read_b64_tr_b16 %9, %10 offset:768\n\t"
            "s_waitcnt lgkmcnt(0)"
            : "=&v"(fl[0]), "=&v"(fl[1]), "=&v"(fl[2]), "=&v"(fl[3]), "=&v"(fl[4]),
              "=&v"(fh[0]), "=&v"(fh[1]), "=&v"(fh[2]), "=&v"(fh[3]), "=&v"(fh[4])
            : "v"(ta) : "memory");
        ST(1);
        float cs[5];
#pragma unroll
        for (int ct = 0; ct < 5; ++ct) {
          const bf16x8 fr = __builtin_shufflevector(fl[ct], fh[ct], 0, 1, 2, 3, 4, 5, 6, 7);
          asm volatile("buffer_store_dwordx4 %0, %1, %2, 0 offen sc1\n\ts_nop 1" :: "v"(fr), "v"(vo[ct]), "s"(trs) : "memory");
          cs[ct] = __builtin_amdgcn_mfma_f32_16x16x32_bf16(onesf, fr, (f32x4){0.f, 0.f, 0.f, 0.f}, 0, 0, 0)[0];
        }
        const float c03 = g < 2 ? (g ? cs[1] : cs[0]) : (g == 2 ? cs[2] : cs[3]), c4 = g ? cs[0] : cs[4];
        asm volatile("buffer_store_dword %0, %1, %2, 0 offen sc1\n\tbuffer_store_dword %3, %4, %2, 0 offen sc1" :: "v"(c03), "v"(co[0]), "s"(crs), "v"(c4), "v"(co[1]) : "memory");
      }
      lds_inc<SY_TI>(sya, one);
      tg0 = __builtin_amdgcn_readfirstlane(tag[0]); tg1 = __builtin_amdgcn_readfirstlane(tag[1]);
      ST(3);
      XDMA0(2 + w)
      if (tw) {
        VMCNT(10);
        if (lane == 0) asm volatile("buffer_store_dwordx2 %0, off, %1, %2 sc1" :: "v"(tag), "s"(frs), "s"(FL_W + (jh * 60 + jk * 3 + part) * 64) : "memory");
      }
      ST(7);
      XDMA0(5 + w)
      XDMA0(8 + w)
      if (w == 0) {
        if (WPW == 0) WPOLL
        VMCNT(0);
        lds_inc<SY_X>(sya, one);
      } else {
        const __amdgpu_buffer_rsrc_t mrs = __builtin_amdgcn_make_buffer_rsrc(obuf, 0, HO_BYTES, 0x00020000);
        _Pragma("unroll 1")
        for (int i = 0; i < 11; ++i) {
          __builtin_amdgcn_raw_buffer_store_b128(MARK16, mrs, lane * 16, i < 10 ? (rb * 8 + hg) * 10240 + i * 1024 : HO_ST + rb * 8192 + hg * 1024, 17);
          __builtin_amdgcn_s_sleep(MSL0);
        }
        VMCNT(11);
        lds_inc<SY_X>(sya, one);
        ST(6);
        VMCNT(0);
        if (lane == 0) asm volatile("buffer_store_dwordx2 %0, off, %1, %2 sc1" :: "v"(tag), "s"(frs), "s"(FL_M + b * 64) : "memory");
      }
      ST(9);
    } else if (w == 2) {
      const int r6 = (lane * 205) >> 11, c10 = lane - 10 * r6, q = (lane >> 2) & 3, p4 = lane & 3;
      const bool to = b < 128;
      __builtin_amdgcn_s_setprio(3);
      asm volatile("buffer_load_dwordx2 %0, off, %1, 0 sc1" : "=v"(tag) : "s"(frs) : "memory");
      XDMA0(0)
      __builtin_amdgcn_s_barrier();
      XDMA0(1)
      if (to) {
        const u16* src = wo + (size_t)(80 * (b >> 4) + 40 * (b & 1) + r6) * D_ + ((b >> 1) & 7) * 80 + 8 * c10;
        u16* tl = (u16*)(smem + KQ_OFF);
        if (lane < 60) {
#pragma unroll
          for (int i = 0; i < 6; ++i) __builtin_amdgcn_global_load_lds((const u32*)(src + i * 6 * D_), (u32*)(tl + i * 480), 16, 0, 0);
          if (lane < 40) __builtin_amdgcn_global_load_lds((const u32*)(src + 36 * D_), (u32*)(tl + 6 * 480), 16, 0, 0);
        }
      }
      asm volatile("s_waitcnt vmcnt(0)" : "+v"(tag) :: "memory");
      tag[0] += 1u;
      tg0 = __builtin_amdgcn_readfirstlane(tag[0]); tg1 = __builtin_amdgcn_readfirstlane(tag[1]);
      ST(1);
      lds_poll<SY_TI, 1>(sya, 2);
      if (to) {
        const int s = b >> 4;
        const __amdgpu_buffer_rsrc_t trs = __builtin_amdgcn_make_buffer_rsrc((void*)(wot + (size_t)((b >> 1) & 7) * 51200 + ((b & 1) * 2 + (s >> 2)) * 12800), 0, 25600, 0x00020000);
        const u32 t0 = LDSA(smem) + KQ_OFF + (4 * g + q) * 160 + 8 * p4, t1 = LDSA(smem) + KQ_OFF + (16 + q) * 160 + 8 * p4;
        const int o0 = lane * 16, o1 = g ? -1 : lane * 16;
#pragma unroll
        for (int nt = 0; nt < 5; ++nt) {
          s16x4 f[4];
          const int np = (s < 4 ? (nt + 2) % 5 : nt) * 1024;
          asm volatile("ds_read_b64_tr_b16 %0, %4 offset:%6\n\tds_read_b64_tr_b16 %1, %4 offset:%6+3200\n\tds_read_b64_tr_b16 %2, %5 offset:%6\n\tds_read_b64_tr_b16 %3, %5 offset:%6+3200\n\ts_waitcnt lgkmcnt(0)"
                       : "=&v"(f[0]), "=&v"(f[1]), "=&v"(f[2]), "=&v"(f[3]) : "v"(t0), "v"(t1), "i"(32 * nt) : "memory");
          asm volatile("buffer_store_dwordx4 %0, %2, %4, %5 offen sc1\n\tbuffer_store_dwordx4 %1, %3, %4, %6 offen sc1\n\ts_nop 1"
                       :: "v"(__builtin_shufflevector(f[0], f[1], 0, 1, 2, 3, 4, 5, 6, 7)), "v"(__builtin_shufflevector(f[2], f[3], 0, 1, 2, 3, 4, 5, 6, 7)),
                          "v"(o0), "v"(o1), "s"(trs), "s"((s & 3) * 5120 + np), "s"(20480 + (s & 3) * 256 + np) : "memory");
        }
      }
      ST(3);
      XDMA0(4)
      XDMA0(7)
      __builtin_amdgcn_s_waitcnt(0x0070);
      lds_inc<SY_X>(sya, one);
      if (to && lane == 0) asm volatile("buffer_store_dwordx2 %0, off, %1, %2 sc1" :: "v"(tag), "s"(frs), "s"(FL_M + (256 + b) * 64) : "memory");
      ST(9);
    } else {
      const int ar = w - 3, ci = ar % 3;
      KgenIn<1> kin;
      tag = __builtin_amdgcn_raw_buffer_load_b64(frs, 0, 0, 16);
      kgen_load<1, 1>(CHAN(b, ci), 4 * (ar / 3), 4, lane, ka, kin);
      asm volatile("" ::: "memory");
      if (w == 4) asm volatile("ds_write_b64 %0, %1\n\ts_waitcnt lgkmcnt(0)" :: "v"(sya + 8 * lane), "v"((u32x2){0u, 0u}) : "memory");
      __builtin_amdgcn_s_barrier();
      if (w == 8) {
        bf16x8 be, bo;
#pragma unroll
        for (int e = 0; e < 8; ++e) {
          const int d = 8 * g + e - l15;
          be[e] = (d >= 0 && d <= 8) ? (short)0x3c00 : (short)0;
          bo[e] = (d >= 1 && d <= 7) ? (short)0x3c00 : (short)0;
        }
        asm volatile("ds_write_b128 %0, %1\n\tds_write_b128 %0, %2 offset:1024" :: "v"(LDSA(smem) + BND_OFF + 16 * lane), "v"(be), "v"(bo) : "memory");
      }
      if (w == 3) __builtin_amdgcn_s_setprio(2); else __builtin_amdgcn_s_setprio(1);
      tg0 = __builtin_amdgcn_readfirstlane(tag[0]) + 1u; tg1 = __builtin_amdgcn_readfirstlane(tag[1]);
      f32x4 kacc[3];
      f32x4* ka_ = (f32x4*)(St + ar * 1536);
      const float dt = kgen_wave(lane, kin, (float*)ka_, kacc);
      if (w == 3) ST(6); else ST(4);
      if (w < 9) {
#pragma unroll
        for (int m = 0; m < 3; ++m) ka_[64 * m + lane] = kacc[m];
        lds_inc<SY_KP>(sya, one);
      } else {
        const f32x4* pa = (const f32x4*)St + ci * 192 + lane;
        lds_poll<SY_KP, 2>(sya, 6);
#pragma unroll
        for (int m = 0; m < 3; ++m) { kacc[m] += pa[64 * m] + pa[576 + 64 * m]; PIN(kacc[m]); }
        lds_inc<SY_STAT>(sya, one);
        lds_poll<SY_X, 2>(sya, 3);
        kgen_fin(lane, dt, bf2f(kin.ds), (float*)(smem + KQ_OFF + ci * 3072), kacc);
      }
      if (WPW == 3 && w == 3) { __builtin_amdgcn_s_setprio(3); WPOLL ST(9); }
      __builtin_amdgcn_s_waitcnt(0x0070);
    }
  }
  if (w < 4) {
    {
    LANE(lane);
    const int l15 = lane & 15, g = lane >> 4;
    const __amdgpu_buffer_rsrc_t wrsrc = __builtin_amdgcn_make_buffer_rsrc((void*)(wq + (size_t)hg * 153600), 0, 307200, 0x00020000);
    const int tb = w == 1 ? 1 : 0;
    u32 wa = (w == 3 ? 11 : 4 * w) * 1024 + lane * 16;
    u32 xa = LDSA(At) + (layer ? (4 * g + ((lane >> 2) & 3)) * 32 + 8 * (lane & 3) : (l15 >> 3) * 1024 + (l15 & 7) * 128 + ((g ^ (l15 & 6)) << 4)), xb = xa ^ 64;
    u32 sy = sya + SY_X, pga = sya + SY_PG + 4 * w, cwa = LDSA(smem) + CW_OFF + tb * 64 + g * 16, mra = LDSA(smem) + A_R2 + l15 * 16;
    u32 sta = LDSA(St) + (l15 * X1_P + tb * 16 + 4 * g) * 2, pv;
    int so, s0_, s1_;
    __builtin_amdgcn_s_setprio(PF);
    if (!layer) {
      asm volatile(
        "s_cmp_lt_u32 %[w], 2\ns_cbranch_scc0 .LB%=\ns_mov_b32 %[s1], 0x100000\n.LA0v%=:\nds_read_b32 %[pv], %[sy] offset:0\ns_waitcnt lgkmcnt(0)\nv_readfirstlane_b32 %[s0], %[pv]\ns_nop 0\n"
        "s_cmp_ge_i32 %[s0], 3\ns_cbranch_scc1 .LA0h%=\ns_sleep 1\ns_sub_u32 %[s1], %[s1], 1\ns_cbranch_scc0 .LA0v%=\n.LA0h%=:\ns_mov_b32 %[s1], 0x100000\n.LA0w%=:\n"
        "ds_read_b32 %[pv], %[sy] offset:%[ow]\ns_waitcnt lgkmcnt(0)\nv_readfirstlane_b32 %[s0], %[pv]\ns_nop 0\ns_cmp_ge_i32 %[s0], 1\ns_cbranch_scc1 .LA0g%=\ns_sleep 1\ns_sub_u32 %[s1], %[s1], 1\n"
        "s_cbranch_scc0 .LA0w%=\n.LA0g%=:\ns_mov_b32 %[so], 0\nbuffer_load_dwordx4 v[32:35], %[wa], %[rs], %[so] offen offset:0 sc1\n"
        "buffer_load_dwordx4 v[36:39], %[wa], %[rs], %[so] offen offset:1024 sc1\nbuffer_load_dwordx4 v[40:43], %[wa], %[rs], %[so] offen offset:2048 sc1\n"
        "buffer_load_dwordx4 v[44:47], %[wa], %[rs], %[so] offen offset:3072 sc1\ns_mov_b32 %[so], 15360\nbuffer_load_dwordx4 v[48:51], %[wa], %[rs], %[so] offen offset:0 sc1\n"
        "buffer_load_dwordx4 v[52:55], %[wa], %[rs], %[so] offen offset:1024 sc1\nbuffer_load_dwordx4 v[56:59], %[wa], %[rs], %[so] offen offset:2048 sc1\n"
        "buffer_load_dwordx4 v[60:63], %[wa], %[rs], %[so] offen offset:3072 sc1\ns_mov_b32 %[so], 30720\nbuffer_load_dwordx4 v[64:67], %[wa], %[rs], %[so] offen offset:0 sc1\n"
        "buffer_load_dwordx4 v[68:71], %[wa], %[rs], %[so] offen offset:1024 sc1\nbuffer_load_dwordx4 v[72:75], %[wa], %[rs], %[so] offen offset:2048 sc1\n"
        "buffer_load_dwordx4 v[76:79], %[wa], %[rs], %[so] offen offset:3072 sc1\ns_mov_b32 %[so], 46080\nbuffer_load_dwordx4 v[80:83], %[wa], %[rs], %[so] offen offset:0 sc1\n"
        "buffer_load_dwordx4 v[84:87], %[wa], %[rs], %[so] offen offset:1024 sc1\nbuffer_load_dwordx4 v[88:91], %[wa], %[rs], %[so] offen offset:2048 sc1\n"
        "buffer_load_dwordx4 v[92:95], %[wa], %[rs], %[so] offen offset:3072 sc1\nds_read_b128 v[16:19], %[xa] offset:2048\nds_read_b128 v[20:23], %[xa] offset:4096\n"
        "ds_read_b128 v[12:15], %[xa] offset:0\nds_read_b128 v[24:27], %[xa] offset:6144\ns_waitcnt vmcnt(12) lgkmcnt(3)\nv_mfma_f32_16x16x32_bf16 v[100:103], v[32:35], v[16:19], 0\n"
        "ds_read_b128 v[28:31], %[xa] offset:8192\nv_mfma_f32_16x16x32_bf16 v[116:119], v[36:39], v[16:19], 0\nv_mfma_f32_16x16x32_bf16 v[136:139], v[40:43], v[16:19], 0\n"
        "v_mfma_f32_16x16x32_bf16 v[152:155], v[44:47], v[16:19], 0\nds_read_b128 v[16:19], %[xb] offset:2048\ns_waitcnt lgkmcnt(4)\nv_mfma_f32_16x16x32_bf16 v[104:107], v[32:35], v[20:23], 0\n"
        "v_mfma_f32_16x16x32_bf16 v[120:123], v[36:39], v[20:23], 0\nv_mfma_f32_16x16x32_bf16 v[140:143], v[40:43], v[20:23], 0\nv_mfma_f32_16x16x32_bf16 v[156:159], v[44:47], v[20:23], 0\n"
        "ds_read_b128 v[20:23], %[xb] offset:4096\ns_waitcnt lgkmcnt(4)\nv_mfma_f32_16x16x32_bf16 v[96:99], v[32:35], v[12:15], 0\nv_mfma_f32_16x16x32_bf16 v[132:135], v[40:43], v[12:15], 0\n"
        "ds_read_b128 v[12:15], %[xb] offset:0\ns_waitcnt lgkmcnt(4)\nv_mfma_f32_16x16x32_bf16 v[108:111], v[32:35], v[24:27], 0\nv_mfma_f32_16x16x32_bf16 v[124:127], v[36:39], v[24:27], 0\n"
        "v_mfma_f32_16x16x32_bf16 v[144:147], v[40:43], v[24:27], 0\nv_mfma_f32_16x16x32_bf16 v[160:163], v[44:47], v[24:27], 0\nds_read_b128 v[24:27], %[xb] offset:6144\ns_waitcnt lgkmcnt(4)\n"
        "v_mfma_f32_16x16x32_bf16 v[112:115], v[32:35], v[28:31], 0\nv_mfma_f32_16x16x32_bf16 v[128:131], v[36:39], v[28:31], 0\nv_mfma_f32_16x16x32_bf16 v[148:151], v[40:43], v[28:31], 0\n"
        "v_mfma_f32_16x16x32_bf16 v[164:167], v[44:47], v[28:31], 0\n.LA0l%=:\ns_waitcnt vmcnt(8) lgkmcnt(3)\nv_mfma_f32_16x16x32_bf16 v[100:103], v[48:51], v[16:19], v[100:103]\n"
        "s_addk_i32 %[so], 15360\nds_read_b128 v[28:31], %[xb] offset:8192\nv_mfma_f32_16x16x32_bf16 v[116:119], v[52:55], v[16:19], v[116:119]\n"
        "v_mfma_f32_16x16x32_bf16 v[136:139], v[56:59], v[16:19], v[136:139]\nbuffer_load_dwordx4 v[32:35], %[wa], %[rs], %[so] offen offset:0 sc1\n"
        "v_mfma_f32_16x16x32_bf16 v[152:155], v[60:63], v[16:19], v[152:155]\nds_read_b128 v[16:19], %[xa] offset:12288\ns_waitcnt lgkmcnt(4)\n"
        "v_mfma_f32_16x16x32_bf16 v[104:107], v[48:51], v[20:23], v[104:107]\nv_mfma_f32_16x16x32_bf16 v[120:123], v[52:55], v[20:23], v[120:123]\n"
        "buffer_load_dwordx4 v[36:39], %[wa], %[rs], %[so] offen offset:1024 sc1\nv_mfma_f32_16x16x32_bf16 v[140:143], v[56:59], v[20:23], v[140:143]\n"
        "v_mfma_f32_16x16x32_bf16 v[156:159], v[60:63], v[20:23], v[156:159]\nds_read_b128 v[20:23], %[xa] offset:14336\ns_waitcnt lgkmcnt(4)\n"
        "v_mfma_f32_16x16x32_bf16 v[96:99], v[48:51], v[12:15], v[96:99]\nv_mfma_f32_16x16x32_bf16 v[132:135], v[56:59], v[12:15], v[132:135]\nds_read_b128 v[12:15], %[xa] offset:10240\n"
        "s_waitcnt lgkmcnt(4)\nv_mfma_f32_16x16x32_bf16 v[108:111], v[48:51], v[24:27], v[108:111]\nbuffer_load_dwordx4 v[40:43], %[wa], %[rs], %[so] offen offset:2048 sc1\n"
        "v_mfma_f32_16x16x32_bf16 v[124:127], v[52:55], v[24:27], v[124:127]\nv_mfma_f32_16x16x32_bf16 v[144:147], v[56:59], v[24:27], v[144:147]\n"
        "buffer_load_dwordx4 v[44:47], %[wa], %[rs], %[so] offen offset:3072 sc1\nv_mfma_f32_16x16x32_bf16 v[160:163], v[60:63], v[24:27], v[160:163]\nds_read_b128 v[24:27], %[xa] offset:16384\n"
        "s_waitcnt lgkmcnt(4)\nv_mfma_f32_16x16x32_bf16 v[112:115], v[48:51], v[28:31], v[112:115]\nv_mfma_f32_16x16x32_bf16 v[128:131], v[52:55], v[28:31], v[128:131]\nv_add_u32 %[sy], 4, %[sy]\n"
        "v_mfma_f32_16x16x32_bf16 v[148:151], v[56:59], v[28:31], v[148:151]\nds_write_b32 %[pg], %[sy]\nv_mfma_f32_16x16x32_bf16 v[164:167], v[60:63], v[28:31], v[164:167]\n"
        "s_waitcnt vmcnt(8) lgkmcnt(4)\nv_mfma_f32_16x16x32_bf16 v[100:103], v[64:67], v[16:19], v[100:103]\ns_addk_i32 %[so], 15360\nds_read_b128 v[28:31], %[xa] offset:18432\n"
        "v_mfma_f32_16x16x32_bf16 v[116:119], v[68:71], v[16:19], v[116:119]\nv_mfma_f32_16x16x32_bf16 v[136:139], v[72:75], v[16:19], v[136:139]\n"
        "buffer_load_dwordx4 v[48:51], %[wa], %[rs], %[so] offen offset:0 sc1\nv_mfma_f32_16x16x32_bf16 v[152:155], v[76:79], v[16:19], v[152:155]\nds_read_b128 v[16:19], %[xb] offset:12288\n"
        "s_waitcnt lgkmcnt(5)\nv_mfma_f32_16x16x32_bf16 v[104:107], v[64:67], v[20:23], v[104:107]\nv_mfma_f32_16x16x32_bf16 v[120:123], v[68:71], v[20:23], v[120:123]\n"
        "buffer_load_dwordx4 v[52:55], %[wa], %[rs], %[so] offen offset:1024 sc1\nv_mfma_f32_16x16x32_bf16 v[140:143], v[72:75], v[20:23], v[140:143]\n"
        "v_mfma_f32_16x16x32_bf16 v[156:159], v[76:79], v[20:23], v[156:159]\nds_read_b128 v[20:23], %[xb] offset:14336\ns_waitcnt lgkmcnt(5)\n"
        "v_mfma_f32_16x16x32_bf16 v[96:99], v[64:67], v[12:15], v[96:99]\nv_mfma_f32_16x16x32_bf16 v[132:135], v[72:75], v[12:15], v[132:135]\nds_read_b128 v[12:15], %[xb] offset:10240\n"
        "s_waitcnt lgkmcnt(5)\nv_mfma_f32_16x16x32_bf16 v[108:111], v[64:67], v[24:27], v[108:111]\nbuffer_load_dwordx4 v[56:59], %[wa], %[rs], %[so] offen offset:2048 sc1\n"
        "v_mfma_f32_16x16x32_bf16 v[124:127], v[68:71], v[24:27], v[124:127]\nv_mfma_f32_16x16x32_bf16 v[144:147], v[72:75], v[24:27], v[144:147]\n"
        "buffer_load_dwordx4 v[60:63], %[wa], %[rs], %[so] offen offset:3072 sc1\nv_mfma_f32_16x16x32_bf16 v[160:163], v[76:79], v[24:27], v[160:163]\nds_read_b128 v[24:27], %[xb] offset:16384\n"
        "s_waitcnt lgkmcnt(4)\nv_mfma_f32_16x16x32_bf16 v[112:115], v[64:67], v[28:31], v[112:115]\nv_mfma_f32_16x16x32_bf16 v[128:131], v[68:71], v[28:31], v[128:131]\n"
        "v_mfma_f32_16x16x32_bf16 v[148:151], v[72:75], v[28:31], v[148:151]\nv_mfma_f32_16x16x32_bf16 v[164:167], v[76:79], v[28:31], v[164:167]\ns_waitcnt vmcnt(8) lgkmcnt(3)\n"
        "v_mfma_f32_16x16x32_bf16 v[100:103], v[80:83], v[16:19], v[100:103]\ns_addk_i32 %[so], 15360\nds_read_b128 v[28:31], %[xb] offset:18432\n"
        "v_mfma_f32_16x16x32_bf16 v[116:119], v[84:87], v[16:19], v[116:119]\nv_mfma_f32_16x16x32_bf16 v[136:139], v[88:91], v[16:19], v[136:139]\n"
        "buffer_load_dwordx4 v[64:67], %[wa], %[rs], %[so] offen offset:0 sc1\nv_mfma_f32_16x16x32_bf16 v[152:155], v[92:95], v[16:19], v[152:155]\nds_read_b128 v[16:19], %[xa] offset:22528\n"
        "s_waitcnt lgkmcnt(4)\nv_mfma_f32_16x16x32_bf16 v[104:107], v[80:83], v[20:23], v[104:107]\nv_mfma_f32_16x16x32_bf16 v[120:123], v[84:87], v[20:23], v[120:123]\n"
        "buffer_load_dwordx4 v[68:71], %[wa], %[rs], %[so] offen offset:1024 sc1\nv_mfma_f32_16x16x32_bf16 v[140:143], v[88:91], v[20:23], v[140:143]\n"
        "v_mfma_f32_16x16x32_bf16 v[156:159], v[92:95], v[20:23], v[156:159]\nds_read_b128 v[20:23], %[xa] offset:24576\ns_waitcnt lgkmcnt(4)\n"
        "v_mfma_f32_16x16x32_bf16 v[96:99], v[80:83], v[12:15], v[96:99]\nv_mfma_f32_16x16x32_bf16 v[132:135], v[88:91], v[12:15], v[132:135]\nds_read_b128 v[12:15], %[xa] offset:20480\n"
        "s_waitcnt lgkmcnt(4)\nv_mfma_f32_16x16x32_bf16 v[108:111], v[80:83], v[24:27], v[108:111]\nbuffer_load_dwordx4 v[72:75], %[wa], %[rs], %[so] offen offset:2048 sc1\n"
        "v_mfma_f32_16x16x32_bf16 v[124:127], v[84:87], v[24:27], v[124:127]\nv_mfma_f32_16x16x32_bf16 v[144:147], v[88:91], v[24:27], v[144:147]\n"
        "buffer_load_dwordx4 v[76:79], %[wa], %[rs], %[so] offen offset:3072 sc1\nv_mfma_f32_16x16x32_bf16 v[160:163], v[92:95], v[24:27], v[160:163]\nds_read_b128 v[24:27], %[xa] offset:26624\n"
        "s_waitcnt lgkmcnt(4)\nv_mfma_f32_16x16x32_bf16 v[112:115], v[80:83], v[28:31], v[112:115]\nv_mfma_f32_16x16x32_bf16 v[128:131], v[84:87], v[28:31], v[128:131]\nv_add_u32 %[sy], 4, %[sy]\n"
        "v_mfma_f32_16x16x32_bf16 v[148:151], v[88:91], v[28:31], v[148:151]\nds_write_b32 %[pg], %[sy]\nv_mfma_f32_16x16x32_bf16 v[164:167], v[92:95], v[28:31], v[164:167]\n"
        "s_waitcnt vmcnt(8) lgkmcnt(4)\nv_mfma_f32_16x16x32_bf16 v[100:103], v[32:35], v[16:19], v[100:103]\ns_addk_i32 %[so], 15360\nds_read_b128 v[28:31], %[xa] offset:28672\n"
        "v_mfma_f32_16x16x32_bf16 v[116:119], v[36:39], v[16:19], v[116:119]\nv_mfma_f32_16x16x32_bf16 v[136:139], v[40:43], v[16:19], v[136:139]\n"
        "buffer_load_dwordx4 v[80:83], %[wa], %[rs], %[so] offen offset:0 sc1\nv_mfma_f32_16x16x32_bf16 v[152:155], v[44:47], v[16:19], v[152:155]\nds_read_b128 v[16:19], %[xb] offset:22528\n"
        "s_waitcnt lgkmcnt(5)\nv_mfma_f32_16x16x32_bf16 v[104:107], v[32:35], v[20:23], v[104:107]\nv_mfma_f32_16x16x32_bf16 v[120:123], v[36:39], v[20:23], v[120:123]\n"
        "buffer_load_dwordx4 v[84:87], %[wa], %[rs], %[so] offen offset:1024 sc1\nv_mfma_f32_16x16x32_bf16 v[140:143], v[40:43], v[20:23], v[140:143]\n"
        "v_mfma_f32_16x16x32_bf16 v[156:159], v[44:47], v[20:23], v[156:159]\nds_read_b128 v[20:23], %[xb] offset:24576\ns_waitcnt lgkmcnt(5)\n"
        "v_mfma_f32_16x16x32_bf16 v[96:99], v[32:35], v[12:15], v[96:99]\nv_mfma_f32_16x16x32_bf16 v[132:135], v[40:43], v[12:15], v[132:135]\nds_read_b128 v[12:15], %[xb] offset:20480\n"
        "s_waitcnt lgkmcnt(5)\nv_mfma_f32_16x16x32_bf16 v[108:111], v[32:35], v[24:27], v[108:111]\nbuffer_load_dwordx4 v[88:91], %[wa], %[rs], %[so] offen offset:2048 sc1\n"
        "v_mfma_f32_16x16x32_bf16 v[124:127], v[36:39], v[24:27], v[124:127]\nv_mfma_f32_16x16x32_bf16 v[144:147], v[40:43], v[24:27], v[144:147]\n"
        "buffer_load_dwordx4 v[92:95], %[wa], %[rs], %[so] offen offset:3072 sc1\nv_mfma_f32_16x16x32_bf16 v[160:163], v[44:47], v[24:27], v[160:163]\nds_read_b128 v[24:27], %[xb] offset:26624\n"
        "s_waitcnt lgkmcnt(4)\nv_mfma_f32_16x16x32_bf16 v[112:115], v[32:35], v[28:31], v[112:115]\nv_mfma_f32_16x16x32_bf16 v[128:131], v[36:39], v[28:31], v[128:131]\n"
        "v_mfma_f32_16x16x32_bf16 v[148:151], v[40:43], v[28:31], v[148:151]\ns_cmp_lg_u32 %[so], 291840\nv_mfma_f32_16x16x32_bf16 v[164:167], v[44:47], v[28:31], v[164:167]\n"
        "v_add_u32 %[xa], 20480, %[xa]\nv_add_u32 %[xb], 20480, %[xb]\ns_cbranch_scc1 .LA0l%=\ns_waitcnt vmcnt(8) lgkmcnt(3)\nv_mfma_f32_16x16x32_bf16 v[100:103], v[48:51], v[16:19], v[100:103]\n"
        "ds_read_b128 v[28:31], %[xb] offset:8192\nv_mfma_f32_16x16x32_bf16 v[116:119], v[52:55], v[16:19], v[116:119]\nv_mfma_f32_16x16x32_bf16 v[136:139], v[56:59], v[16:19], v[136:139]\n"
        "v_mfma_f32_16x16x32_bf16 v[152:155], v[60:63], v[16:19], v[152:155]\nds_read_b128 v[16:19], %[xa] offset:12288\ns_waitcnt lgkmcnt(4)\n"
        "v_mfma_f32_16x16x32_bf16 v[104:107], v[48:51], v[20:23], v[104:107]\nv_mfma_f32_16x16x32_bf16 v[120:123], v[52:55], v[20:23], v[120:123]\n"
        "v_mfma_f32_16x16x32_bf16 v[140:143], v[56:59], v[20:23], v[140:143]\nv_mfma_f32_16x16x32_bf16 v[156:159], v[60:63], v[20:23], v[156:159]\nds_read_b128 v[20:23], %[xa] offset:14336\n"
        "s_waitcnt lgkmcnt(4)\nv_mfma_f32_16x16x32_bf16 v[96:99], v[48:51], v[12:15], v[96:99]\nv_mfma_f32_16x16x32_bf16 v[132:135], v[56:59], v[12:15], v[132:135]\n"
        "ds_read_b128 v[12:15], %[xa] offset:10240\ns_waitcnt lgkmcnt(4)\nv_mfma_f32_16x16x32_bf16 v[108:111], v[48:51], v[24:27], v[108:111]\n"
        "v_mfma_f32_16x16x32_bf16 v[124:127], v[52:55], v[24:27], v[124:127]\nv_mfma_f32_16x16x32_bf16 v[144:147], v[56:59], v[24:27], v[144:147]\n"
        "v_mfma_f32_16x16x32_bf16 v[160:163], v[60:63], v[24:27], v[160:163]\nds_read_b128 v[24:27], %[xa] offset:16384\ns_waitcnt lgkmcnt(4)\n"
        "v_mfma_f32_16x16x32_bf16 v[112:115], v[48:51], v[28:31], v[112:115]\nv_mfma_f32_16x16x32_bf16 v[128:131], v[52:55], v[28:31], v[128:131]\nv_add_u32 %[sy], 4, %[sy]\n"
        "v_mfma_f32_16x16x32_bf16 v[148:151], v[56:59], v[28:31], v[148:151]\nv_mfma_f32_16x16x32_bf16 v[164:167], v[60:63], v[28:31], v[164:167]\ns_waitcnt vmcnt(4) lgkmcnt(3)\n"
        "v_mfma_f32_16x16x32_bf16 v[100:103], v[64:67], v[16:19], v[100:103]\nds_read_b128 v[28:31], %[xa] offset:18432\nv_mfma_f32_16x16x32_bf16 v[116:119], v[68:71], v[16:19], v[116:119]\n"
        "v_mfma_f32_16x16x32_bf16 v[136:139], v[72:75], v[16:19], v[136:139]\nds_read_b32 %[pv], %[sy] offset:4\nv_mfma_f32_16x16x32_bf16 v[152:155], v[76:79], v[16:19], v[152:155]\n"
        "ds_read_b128 v[16:19], %[xb] offset:12288\ns_waitcnt lgkmcnt(5)\nv_mfma_f32_16x16x32_bf16 v[104:107], v[64:67], v[20:23], v[104:107]\n"
        "v_mfma_f32_16x16x32_bf16 v[120:123], v[68:71], v[20:23], v[120:123]\nv_mfma_f32_16x16x32_bf16 v[140:143], v[72:75], v[20:23], v[140:143]\n"
        "v_mfma_f32_16x16x32_bf16 v[156:159], v[76:79], v[20:23], v[156:159]\nds_read_b128 v[20:23], %[xb] offset:14336\ns_waitcnt lgkmcnt(5)\n"
        "v_mfma_f32_16x16x32_bf16 v[96:99], v[64:67], v[12:15], v[96:99]\nv_mfma_f32_16x16x32_bf16 v[132:135], v[72:75], v[12:15], v[132:135]\nds_read_b128 v[12:15], %[xb] offset:10240\n"
        "s_waitcnt lgkmcnt(5)\nv_mfma_f32_16x16x32_bf16 v[108:111], v[64:67], v[24:27], v[108:111]\nv_mfma_f32_16x16x32_bf16 v[124:127], v[68:71], v[24:27], v[124:127]\n"
        "v_mfma_f32_16x16x32_bf16 v[144:147], v[72:75], v[24:27], v[144:147]\nv_mfma_f32_16x16x32_bf16 v[160:163], v[76:79], v[24:27], v[160:163]\nds_read_b128 v[24:27], %[xb] offset:16384\n"
        "s_waitcnt lgkmcnt(5)\nv_mfma_f32_16x16x32_bf16 v[112:115], v[64:67], v[28:31], v[112:115]\nv_mfma_f32_16x16x32_bf16 v[128:131], v[68:71], v[28:31], v[128:131]\n"
        "v_mfma_f32_16x16x32_bf16 v[148:151], v[72:75], v[28:31], v[148:151]\nv_mfma_f32_16x16x32_bf16 v[164:167], v[76:79], v[28:31], v[164:167]\ns_waitcnt vmcnt(0) lgkmcnt(3)\n"
        "v_mfma_f32_16x16x32_bf16 v[100:103], v[80:83], v[16:19], v[100:103]\nv_readfirstlane_b32 %[s0], %[pv]\nds_read_b128 v[28:31], %[xb] offset:18432\n"
        "v_mfma_f32_16x16x32_bf16 v[116:119], v[84:87], v[16:19], v[116:119]\nv_mfma_f32_16x16x32_bf16 v[136:139], v[88:91], v[16:19], v[136:139]\ns_cmp_ge_i32 %[s0], %[nst]\n"
        "v_mfma_f32_16x16x32_bf16 v[152:155], v[92:95], v[16:19], v[152:155]\ns_cbranch_scc0 .LA0t%=\n.LA0u%=:\ns_waitcnt lgkmcnt(3)\n"
        "v_mfma_f32_16x16x32_bf16 v[104:107], v[80:83], v[20:23], v[104:107]\nv_add_u32 %[sy], 4, %[sy]\nds_read_b128 v[16:19], %[mra] offset:256\n"
        "v_mfma_f32_16x16x32_bf16 v[120:123], v[84:87], v[20:23], v[120:123]\nv_mfma_f32_16x16x32_bf16 v[140:143], v[88:91], v[20:23], v[140:143]\n"
        "v_mfma_f32_16x16x32_bf16 v[156:159], v[92:95], v[20:23], v[156:159]\nds_read_b128 v[32:35], %[cwa] offset:0\ns_waitcnt lgkmcnt(4)\n"
        "v_mfma_f32_16x16x32_bf16 v[96:99], v[80:83], v[12:15], v[96:99]\nds_read_b128 v[36:39], %[cwa] offset:320\nds_read_b128 v[20:23], %[mra] offset:512\n"
        "v_mfma_f32_16x16x32_bf16 v[132:135], v[88:91], v[12:15], v[132:135]\nds_read_b128 v[40:43], %[cwa] offset:640\ns_waitcnt lgkmcnt(6)\n"
        "v_mfma_f32_16x16x32_bf16 v[108:111], v[80:83], v[24:27], v[108:111]\nds_read_b128 v[44:47], %[cwa] offset:512\nds_read_b128 v[12:15], %[mra] offset:0\n"
        "v_mfma_f32_16x16x32_bf16 v[124:127], v[84:87], v[24:27], v[124:127]\nv_add_u32 %[sy], 4, %[sy]\nv_mfma_f32_16x16x32_bf16 v[144:147], v[88:91], v[24:27], v[144:147]\n"
        "v_mfma_f32_16x16x32_bf16 v[160:163], v[92:95], v[24:27], v[160:163]\nds_read_b128 v[24:27], %[mra] offset:768\ns_waitcnt lgkmcnt(8)\n"
        "v_mfma_f32_16x16x32_bf16 v[112:115], v[80:83], v[28:31], v[112:115]\nv_mfma_f32_16x16x32_bf16 v[128:131], v[84:87], v[28:31], v[128:131]\n"
        "v_mfma_f32_16x16x32_bf16 v[148:151], v[88:91], v[28:31], v[148:151]\nv_mfma_f32_16x16x32_bf16 v[164:167], v[92:95], v[28:31], v[164:167]\nds_read_b128 v[28:31], %[mra] offset:1024\n"
        "s_setprio %[pfs]\ns_waitcnt lgkmcnt(1)\n.long 0xd3b04064, 0x1d922120\n.long 0xd3b04066, 0x1d9a2122\n.long 0xd3b04074, 0x1dd22124\n.long 0xd3b04076, 0x1dda2126\n.long 0xd3b14064, 0x18022564\n"
        ".long 0xd3b14066, 0x18022566\n.long 0xd3b04068, 0x1da22920\n.long 0xd3b0406a, 0x1daa2922\n.long 0xd3b14074, 0x18022574\n.long 0xd3b14076, 0x18022576\nv_cvt_pk_bf16_f32 v100, v100, v101\n"
        "v_cvt_pk_bf16_f32 v101, v102, v103\n.long 0xd3b04078, 0x1de22924\n.long 0xd3b0407a, 0x1dea2926\n.long 0xd3b14068, 0x18022d68\n.long 0xd3b1406a, 0x18022d6a\nv_cvt_pk_bf16_f32 v116, v116, v117\n"
        "v_cvt_pk_bf16_f32 v117, v118, v119\nds_write_b64 %[sta], v[100:101] offset:7936\n.long 0xd3b04060, 0x1d821920\n.long 0xd3b04062, 0x1d8a1922\n.long 0xd3b14078, 0x18022d78\n"
        ".long 0xd3b1407a, 0x18022d7a\nv_cvt_pk_bf16_f32 v104, v104, v105\nv_cvt_pk_bf16_f32 v105, v106, v107\nds_write_b64 %[sta], v[116:117] offset:8096\n.long 0xd3b0406c, 0x1db23120\n"
        ".long 0xd3b0406e, 0x1dba3122\n.long 0xd3b14060, 0x18021d60\n.long 0xd3b14062, 0x18021d62\nv_cvt_pk_bf16_f32 v120, v120, v121\nv_cvt_pk_bf16_f32 v121, v122, v123\n"
        "ds_write_b64 %[sta], v[104:105] offset:15872\n.long 0xd3b0407c, 0x1df23124\n.long 0xd3b0407e, 0x1dfa3126\n.long 0xd3b1406c, 0x1802356c\n.long 0xd3b1406e, 0x1802356e\n"
        "v_cvt_pk_bf16_f32 v96, v96, v97\nv_cvt_pk_bf16_f32 v97, v98, v99\nds_write_b64 %[sta], v[120:121] offset:16032\ns_waitcnt lgkmcnt(4)\n.long 0xd3b04070, 0x1dc23920\n"
        ".long 0xd3b04072, 0x1dca3922\n.long 0xd3b1407c, 0x1802357c\n.long 0xd3b1407e, 0x1802357e\nv_cvt_pk_bf16_f32 v108, v108, v109\nv_cvt_pk_bf16_f32 v109, v110, v111\n"
        "ds_write_b64 %[sta], v[96:97] offset:0\n.long 0xd3b04080, 0x1e023924\n.long 0xd3b04082, 0x1e0a3926\n.long 0xd3b14070, 0x18023d70\n.long 0xd3b14072, 0x18023d72\n"
        "v_cvt_pk_bf16_f32 v124, v124, v125\nv_cvt_pk_bf16_f32 v125, v126, v127\nds_write_b64 %[sta], v[108:109] offset:23808\n.long 0xd3b14080, 0x18023d80\n.long 0xd3b14082, 0x18023d82\n"
        "v_cvt_pk_bf16_f32 v112, v112, v113\nv_cvt_pk_bf16_f32 v113, v114, v115\nds_write_b64 %[sta], v[124:125] offset:23968\nv_cvt_pk_bf16_f32 v128, v128, v129\nv_cvt_pk_bf16_f32 v129, v130, v131\n"
        "ds_write_b64 %[sta], v[112:113] offset:31744\nds_write_b64 %[sta], v[128:129] offset:31904\nds_write_b32 %[pg], %[sy]\nv_add_u32 %[sy], 4, %[sy]\n.long 0xd3b04088, 0x1e222128\n"
        ".long 0xd3b0408a, 0x1e2a212a\n.long 0xd3b04098, 0x1e62212c\n.long 0xd3b0409a, 0x1e6a212e\n.long 0xd3b14088, 0x18022588\n.long 0xd3b1408a, 0x1802258a\n.long 0xd3b0408c, 0x1e322928\n"
        ".long 0xd3b0408e, 0x1e3a292a\n.long 0xd3b14098, 0x18022598\n.long 0xd3b1409a, 0x1802259a\nv_cvt_pk_bf16_f32 v136, v136, v137\nv_cvt_pk_bf16_f32 v137, v138, v139\n.long 0xd3b0409c, 0x1e72292c\n"
        ".long 0xd3b0409e, 0x1e7a292e\n.long 0xd3b1408c, 0x18022d8c\n.long 0xd3b1408e, 0x18022d8e\nv_cvt_pk_bf16_f32 v152, v152, v153\nv_cvt_pk_bf16_f32 v153, v154, v155\n"
        "ds_write_b64 %[sta], v[136:137] offset:8256\n.long 0xd3b04084, 0x1e121928\n.long 0xd3b04086, 0x1e1a192a\n.long 0xd3b1409c, 0x18022d9c\n.long 0xd3b1409e, 0x18022d9e\n"
        "v_cvt_pk_bf16_f32 v140, v140, v141\nv_cvt_pk_bf16_f32 v141, v142, v143\nds_write_b64 %[sta], v[152:153] offset:8192\n.long 0xd3b04090, 0x1e423128\n.long 0xd3b04092, 0x1e4a312a\n"
        ".long 0xd3b14084, 0x18021d84\n.long 0xd3b14086, 0x18021d86\nv_cvt_pk_bf16_f32 v156, v156, v157\nv_cvt_pk_bf16_f32 v157, v158, v159\nds_write_b64 %[sta], v[140:141] offset:16192\n"
        ".long 0xd3b040a0, 0x1e82312c\n.long 0xd3b040a2, 0x1e8a312e\n.long 0xd3b14090, 0x18023590\n.long 0xd3b14092, 0x18023592\nv_cvt_pk_bf16_f32 v132, v132, v133\nv_cvt_pk_bf16_f32 v133, v134, v135\n"
        "ds_write_b64 %[sta], v[156:157] offset:16128\n.long 0xd3b04094, 0x1e523928\n.long 0xd3b04096, 0x1e5a392a\n.long 0xd3b140a0, 0x180235a0\n.long 0xd3b140a2, 0x180235a2\n"
        "v_cvt_pk_bf16_f32 v144, v144, v145\nv_cvt_pk_bf16_f32 v145, v146, v147\nds_write_b64 %[sta], v[132:133] offset:320\n.long 0xd3b040a4, 0x1e92392c\n.long 0xd3b040a6, 0x1e9a392e\n"
        ".long 0xd3b14094, 0x18023d94\n.long 0xd3b14096, 0x18023d96\nv_cvt_pk_bf16_f32 v160, v160, v161\nv_cvt_pk_bf16_f32 v161, v162, v163\nds_write_b64 %[sta], v[144:145] offset:24128\n"
        ".long 0xd3b140a4, 0x18023da4\n.long 0xd3b140a6, 0x18023da6\nv_cvt_pk_bf16_f32 v148, v148, v149\nv_cvt_pk_bf16_f32 v149, v150, v151\nds_write_b64 %[sta], v[160:161] offset:24064\n"
        "v_cvt_pk_bf16_f32 v164, v164, v165\nv_cvt_pk_bf16_f32 v165, v166, v167\nds_write_b64 %[sta], v[148:149] offset:32064\nds_write_b64 %[sta], v[164:165] offset:32000\nds_write_b32 %[pg], %[sy]\n"
        "s_branch .LE%=\n.LA0t%=:\ns_mov_b32 %[s1], 0x100000\n.LA0q%=:\nds_read_b32 %[pv], %[sy] offset:4\ns_waitcnt lgkmcnt(0)\nv_readfirstlane_b32 %[s0], %[pv]\ns_nop 0\ns_cmp_ge_i32 %[s0], %[nst]\n"
        "s_cbranch_scc1 .LA0u%=\ns_sleep 1\ns_sub_u32 %[s1], %[s1], 1\ns_cbranch_scc0 .LA0q%=\ns_branch .LA0u%=\n.LB%=:\ns_cmp_eq_u32 %[w], 2\ns_cbranch_scc0 .LC%=\ns_mov_b32 %[s1], 0x100000\n"
        ".LB0v%=:\nds_read_b32 %[pv], %[sy] offset:0\ns_waitcnt lgkmcnt(0)\nv_readfirstlane_b32 %[s0], %[pv]\ns_nop 0\ns_cmp_ge_i32 %[s0], 3\ns_cbranch_scc1 .LB0h%=\ns_sleep 1\n"
        "s_sub_u32 %[s1], %[s1], 1\ns_cbranch_scc0 .LB0v%=\n.LB0h%=:\ns_mov_b32 %[s1], 0x100000\n.LB0w%=:\nds_read_b32 %[pv], %[sy] offset:%[ow]\ns_waitcnt lgkmcnt(0)\n"
        "v_readfirstlane_b32 %[s0], %[pv]\ns_nop 0\ns_cmp_ge_i32 %[s0], 1\ns_cbranch_scc1 .LB0g%=\ns_sleep 1\ns_sub_u32 %[s1], %[s1], 1\ns_cbranch_scc0 .LB0w%=\n.LB0g%=:\ns_mov_b32 %[so], 0\n"
        "buffer_load_dwordx4 v[32:35], %[wa], %[rs], %[so] offen offset:3072 sc1\nbuffer_load_dwordx4 v[36:39], %[wa], %[rs], %[so] offen offset:0 sc1\n"
        "buffer_load_dwordx4 v[40:43], %[wa], %[rs], %[so] offen offset:1024 sc1\nbuffer_load_dwordx4 v[44:47], %[wa], %[rs], %[so] offen offset:2048 sc1\ns_mov_b32 %[so], 15360\n"
        "buffer_load_dwordx4 v[48:51], %[wa], %[rs], %[so] offen offset:3072 sc1\nbuffer_load_dwordx4 v[52:55], %[wa], %[rs], %[so] offen offset:0 sc1\n"
        "buffer_load_dwordx4 v[56:59], %[wa], %[rs], %[so] offen offset:1024 sc1\nbuffer_load_dwordx4 v[60:63], %[wa], %[rs], %[so] offen offset:2048 sc1\ns_mov_b32 %[so], 30720\n"
        "buffer_load_dwordx4 v[64:67], %[wa], %[rs], %[so] offen offset:3072 sc1\nbuffer_load_dwordx4 v[68:71], %[wa], %[rs], %[so] offen offset:0 sc1\n"
        "buffer_load_dwordx4 v[72:75], %[wa], %[rs], %[so] offen offset:1024 sc1\nbuffer_load_dwordx4 v[76:79], %[wa], %[rs], %[so] offen offset:2048 sc1\ns_mov_b32 %[so], 46080\n"
        "buffer_load_dwordx4 v[80:83], %[wa], %[rs], %[so] offen offset:3072 sc1\nbuffer_load_dwordx4 v[84:87], %[wa], %[rs], %[so] offen offset:0 sc1\n"
        "buffer_load_dwordx4 v[88:91], %[wa], %[rs], %[so] offen offset:1024 sc1\nbuffer_load_dwordx4 v[92:95], %[wa], %[rs], %[so] offen offset:2048 sc1\nds_read_b128 v[16:19], %[xa] offset:2048\n"
        "ds_read_b128 v[20:23], %[xa] offset:4096\nds_read_b128 v[12:15], %[xa] offset:0\nds_read_b128 v[24:27], %[xa] offset:6144\ns_waitcnt vmcnt(12) lgkmcnt(3)\n"
        "v_mfma_f32_16x16x32_bf16 v[96:99], v[32:35], v[16:19], 0\nds_read_b128 v[28:31], %[xa] offset:8192\nv_mfma_f32_16x16x32_bf16 v[108:111], v[36:39], v[16:19], 0\n"
        "v_mfma_f32_16x16x32_bf16 v[128:131], v[40:43], v[16:19], 0\nv_mfma_f32_16x16x32_bf16 v[148:151], v[44:47], v[16:19], 0\nds_read_b128 v[16:19], %[xb] offset:2048\ns_waitcnt lgkmcnt(4)\n"
        "v_mfma_f32_16x16x32_bf16 v[100:103], v[32:35], v[20:23], 0\nv_mfma_f32_16x16x32_bf16 v[112:115], v[36:39], v[20:23], 0\nv_mfma_f32_16x16x32_bf16 v[132:135], v[40:43], v[20:23], 0\n"
        "v_mfma_f32_16x16x32_bf16 v[152:155], v[44:47], v[20:23], 0\nds_read_b128 v[20:23], %[xb] offset:4096\ns_waitcnt lgkmcnt(4)\nv_mfma_f32_16x16x32_bf16 v[104:107], v[36:39], v[12:15], 0\n"
        "v_mfma_f32_16x16x32_bf16 v[124:127], v[40:43], v[12:15], 0\nv_mfma_f32_16x16x32_bf16 v[144:147], v[44:47], v[12:15], 0\nds_read_b128 v[12:15], %[xb] offset:0\ns_waitcnt lgkmcnt(4)\n"
        "v_mfma_f32_16x16x32_bf16 v[116:119], v[36:39], v[24:27], 0\nv_mfma_f32_16x16x32_bf16 v[136:139], v[40:43], v[24:27], 0\nv_mfma_f32_16x16x32_bf16 v[156:159], v[44:47], v[24:27], 0\n"
        "ds_read_b128 v[24:27], %[xb] offset:6144\ns_waitcnt lgkmcnt(4)\nv_mfma_f32_16x16x32_bf16 v[120:123], v[36:39], v[28:31], 0\nv_mfma_f32_16x16x32_bf16 v[140:143], v[40:43], v[28:31], 0\n"
        "v_mfma_f32_16x16x32_bf16 v[160:163], v[44:47], v[28:31], 0\n.LB0l%=:\ns_waitcnt vmcnt(8) lgkmcnt(3)\nv_mfma_f32_16x16x32_bf16 v[96:99], v[48:51], v[16:19], v[96:99]\ns_addk_i32 %[so], 15360\n"
        "ds_read_b128 v[28:31], %[xb] offset:8192\nv_mfma_f32_16x16x32_bf16 v[108:111], v[52:55], v[16:19], v[108:111]\nv_mfma_f32_16x16x32_bf16 v[128:131], v[56:59], v[16:19], v[128:131]\n"
        "buffer_load_dwordx4 v[32:35], %[wa], %[rs], %[so] offen offset:3072 sc1\nv_mfma_f32_16x16x32_bf16 v[148:151], v[60:63], v[16:19], v[148:151]\nds_read_b128 v[16:19], %[xa] offset:12288\n"
        "s_waitcnt lgkmcnt(4)\nv_mfma_f32_16x16x32_bf16 v[100:103], v[48:51], v[20:23], v[100:103]\nv_mfma_f32_16x16x32_bf16 v[112:115], v[52:55], v[20:23], v[112:115]\n"
        "buffer_load_dwordx4 v[36:39], %[wa], %[rs], %[so] offen offset:0 sc1\nv_mfma_f32_16x16x32_bf16 v[132:135], v[56:59], v[20:23], v[132:135]\n"
        "v_mfma_f32_16x16x32_bf16 v[152:155], v[60:63], v[20:23], v[152:155]\nds_read_b128 v[20:23], %[xa] offset:14336\ns_waitcnt lgkmcnt(4)\n"
        "v_mfma_f32_16x16x32_bf16 v[104:107], v[52:55], v[12:15], v[104:107]\nv_mfma_f32_16x16x32_bf16 v[124:127], v[56:59], v[12:15], v[124:127]\n"
        "buffer_load_dwordx4 v[40:43], %[wa], %[rs], %[so] offen offset:1024 sc1\nv_mfma_f32_16x16x32_bf16 v[144:147], v[60:63], v[12:15], v[144:147]\nds_read_b128 v[12:15], %[xa] offset:10240\n"
        "s_waitcnt lgkmcnt(4)\nv_mfma_f32_16x16x32_bf16 v[116:119], v[52:55], v[24:27], v[116:119]\nv_mfma_f32_16x16x32_bf16 v[136:139], v[56:59], v[24:27], v[136:139]\n"
        "buffer_load_dwordx4 v[44:47], %[wa], %[rs], %[so] offen offset:2048 sc1\nv_mfma_f32_16x16x32_bf16 v[156:159], v[60:63], v[24:27], v[156:159]\nds_read_b128 v[24:27], %[xa] offset:16384\n"
        "s_waitcnt lgkmcnt(4)\nv_mfma_f32_16x16x32_bf16 v[120:123], v[52:55], v[28:31], v[120:123]\nv_add_u32 %[sy], 4, %[sy]\nv_mfma_f32_16x16x32_bf16 v[140:143], v[56:59], v[28:31], v[140:143]\n"
        "ds_write_b32 %[pg], %[sy]\nv_mfma_f32_16x16x32_bf16 v[160:163], v[60:63], v[28:31], v[160:163]\ns_waitcnt vmcnt(8) lgkmcnt(4)\nv_mfma_f32_16x16x32_bf16 v[96:99], v[64:67], v[16:19], v[96:99]\n"
        "s_addk_i32 %[so], 15360\nds_read_b128 v[28:31], %[xa] offset:18432\nv_mfma_f32_16x16x32_bf16 v[108:111], v[68:71], v[16:19], v[108:111]\n"
        "v_mfma_f32_16x16x32_bf16 v[128:131], v[72:75], v[16:19], v[128:131]\nbuffer_load_dwordx4 v[48:51], %[wa], %[rs], %[so] offen offset:3072 sc1\n"
        "v_mfma_f32_16x16x32_bf16 v[148:151], v[76:79], v[16:19], v[148:151]\nds_read_b128 v[16:19], %[xb] offset:12288\ns_waitcnt lgkmcnt(5)\n"
        "v_mfma_f32_16x16x32_bf16 v[100:103], v[64:67], v[20:23], v[100:103]\nv_mfma_f32_16x16x32_bf16 v[112:115], v[68:71], v[20:23], v[112:115]\n"
        "buffer_load_dwordx4 v[52:55], %[wa], %[rs], %[so] offen offset:0 sc1\nv_mfma_f32_16x16x32_bf16 v[132:135], v[72:75], v[20:23], v[132:135]\n"
        "v_mfma_f32_16x16x32_bf16 v[152:155], v[76:79], v[20:23], v[152:155]\nds_read_b128 v[20:23], %[xb] offset:14336\ns_waitcnt lgkmcnt(5)\n"
        "v_mfma_f32_16x16x32_bf16 v[104:107], v[68:71], v[12:15], v[104:107]\nv_mfma_f32_16x16x32_bf16 v[124:127], v[72:75], v[12:15], v[124:127]\n"
        "buffer_load_dwordx4 v[56:59], %[wa], %[rs], %[so] offen offset:1024 sc1\nv_mfma_f32_16x16x32_bf16 v[144:147], v[76:79], v[12:15], v[144:147]\nds_read_b128 v[12:15], %[xb] offset:10240\n"
        "s_waitcnt lgkmcnt(5)\nv_mfma_f32_16x16x32_bf16 v[116:119], v[68:71], v[24:27], v[116:119]\nv_mfma_f32_16x16x32_bf16 v[136:139], v[72:75], v[24:27], v[136:139]\n"
        "buffer_load_dwordx4 v[60:63], %[wa], %[rs], %[so] offen offset:2048 sc1\nv_mfma_f32_16x16x32_bf16 v[156:159], v[76:79], v[24:27], v[156:159]\nds_read_b128 v[24:27], %[xb] offset:16384\n"
        "s_waitcnt lgkmcnt(4)\nv_mfma_f32_16x16x32_bf16 v[120:123], v[68:71], v[28:31], v[120:123]\nv_mfma_f32_16x16x32_bf16 v[140:143], v[72:75], v[28:31], v[140:143]\n"
        "v_mfma_f32_16x16x32_bf16 v[160:163], v[76:79], v[28:31], v[160:163]\ns_waitcnt vmcnt(8) lgkmcnt(3)\nv_mfma_f32_16x16x32_bf16 v[96:99], v[80:83], v[16:19], v[96:99]\ns_addk_i32 %[so], 15360\n"
        "ds_read_b128 v[28:31], %[xb] offset:18432\nv_mfma_f32_16x16x32_bf16 v[108:111], v[84:87], v[16:19], v[108:111]\nv_mfma_f32_16x16x32_bf16 v[128:131], v[88:91], v[16:19], v[128:131]\n"
        "buffer_load_dwordx4 v[64:67], %[wa], %[rs], %[so] offen offset:3072 sc1\nv_mfma_f32_16x16x32_bf16 v[148:151], v[92:95], v[16:19], v[148:151]\nds_read_b128 v[16:19], %[xa] offset:22528\n"
        "s_waitcnt lgkmcnt(4)\nv_mfma_f32_16x16x32_bf16 v[100:103], v[80:83], v[20:23], v[100:103]\nv_mfma_f32_16x16x32_bf16 v[112:115], v[84:87], v[20:23], v[112:115]\n"
        "buffer_load_dwordx4 v[68:71], %[wa], %[rs], %[so] offen offset:0 sc1\nv_mfma_f32_16x16x32_bf16 v[132:135], v[88:91], v[20:23], v[132:135]\n"
        "v_mfma_f32_16x16x32_bf16 v[152:155], v[92:95], v[20:23], v[152:155]\nds_read_b128 v[20:23], %[xa] offset:24576\ns_waitcnt lgkmcnt(4)\n"
        "v_mfma_f32_16x16x32_bf16 v[104:107], v[84:87], v[12:15], v[104:107]\nv_mfma_f32_16x16x32_bf16 v[124:127], v[88:91], v[12:15], v[124:127]\n"
        "buffer_load_dwordx4 v[72:75], %[wa], %[rs], %[so] offen offset:1024 sc1\nv_mfma_f32_16x16x32_bf16 v[144:147], v[92:95], v[12:15], v[144:147]\nds_read_b128 v[12:15], %[xa] offset:20480\n"
        "s_waitcnt lgkmcnt(4)\nv_mfma_f32_16x16x32_bf16 v[116:119], v[84:87], v[24:27], v[116:119]\nv_mfma_f32_16x16x32_bf16 v[136:139], v[88:91], v[24:27], v[136:139]\n"
        "buffer_load_dwordx4 v[76:79], %[wa], %[rs], %[so] offen offset:2048 sc1\nv_mfma_f32_16x16x32_bf16 v[156:159], v[92:95], v[24:27], v[156:159]\nds_read_b128 v[24:27], %[xa] offset:26624\n"
        "s_waitcnt lgkmcnt(4)\nv_mfma_f32_16x16x32_bf16 v[120:123], v[84:87], v[28:31], v[120:123]\nv_add_u32 %[sy], 4, %[sy]\nv_mfma_f32_16x16x32_bf16 v[140:143], v[88:91], v[28:31], v[140:143]\n"
        "ds_write_b32 %[pg], %[sy]\nv_mfma_f32_16x16x32_bf16 v[160:163], v[92:95], v[28:31], v[160:163]\ns_waitcnt vmcnt(8) lgkmcnt(4)\nv_mfma_f32_16x16x32_bf16 v[96:99], v[32:35], v[16:19], v[96:99]\n"
        "s_addk_i32 %[so], 15360\nds_read_b128 v[28:31], %[xa] offset:28672\nv_mfma_f32_16x16x32_bf16 v[108:111], v[36:39], v[16:19], v[108:111]\n"
        "v_mfma_f32_16x16x32_bf16 v[128:131], v[40:43], v[16:19], v[128:131]\nbuffer_load_dwordx4 v[80:83], %[wa], %[rs], %[so] offen offset:3072 sc1\n"
        "v_mfma_f32_16x16x32_bf16 v[148:151], v[44:47], v[16:19], v[148:151]\nds_read_b128 v[16:19], %[xb] offset:22528\ns_waitcnt lgkmcnt(5)\n"
        "v_mfma_f32_16x16x32_bf16 v[100:103], v[32:35], v[20:23], v[100:103]\nv_mfma_f32_16x16x32_bf16 v[112:115], v[36:39], v[20:23], v[112:115]\n"
        "buffer_load_dwordx4 v[84:87], %[wa], %[rs], %[so] offen offset:0 sc1\nv_mfma_f32_16x16x32_bf16 v[132:135], v[40:43], v[20:23], v[132:135]\n"
        "v_mfma_f32_16x16x32_bf16 v[152:155], v[44:47], v[20:23], v[152:155]\nds_read_b128 v[20:23], %[xb] offset:24576\ns_waitcnt lgkmcnt(5)\n"
        "v_mfma_f32_16x16x32_bf16 v[104:107], v[36:39], v[12:15], v[104:107]\nv_mfma_f32_16x16x32_bf16 v[124:127], v[40:43], v[12:15], v[124:127]\n"
        "buffer_load_dwordx4 v[88:91], %[wa], %[rs], %[so] offen offset:1024 sc1\nv_mfma_f32_16x16x32_bf16 v[144:147], v[44:47], v[12:15], v[144:147]\nds_read_b128 v[12:15], %[xb] offset:20480\n"
        "s_waitcnt lgkmcnt(5)\nv_mfma_f32_16x16x32_bf16 v[116:119], v[36:39], v[24:27], v[116:119]\nv_mfma_f32_16x16x32_bf16 v[136:139], v[40:43], v[24:27], v[136:139]\n"
        "buffer_load_dwordx4 v[92:95], %[wa], %[rs], %[so] offen offset:2048 sc1\nv_mfma_f32_16x16x32_bf16 v[156:159], v[44:47], v[24:27], v[156:159]\nds_read_b128 v[24:27], %[xb] offset:26624\n"
        "s_waitcnt lgkmcnt(4)\nv_mfma_f32_16x16x32_bf16 v[120:123], v[36:39], v[28:31], v[120:123]\nv_mfma_f32_16x16x32_bf16 v[140:143], v[40:43], v[28:31], v[140:143]\ns_cmp_lg_u32 %[so], 291840\n"
        "v_mfma_f32_16x16x32_bf16 v[160:163], v[44:47], v[28:31], v[160:163]\nv_add_u32 %[xa], 20480, %[xa]\nv_add_u32 %[xb], 20480, %[xb]\ns_cbranch_scc1 .LB0l%=\ns_waitcnt vmcnt(8) lgkmcnt(3)\n"
        "v_mfma_f32_16x16x32_bf16 v[96:99], v[48:51], v[16:19], v[96:99]\nds_read_b128 v[28:31], %[xb] offset:8192\nv_mfma_f32_16x16x32_bf16 v[108:111], v[52:55], v[16:19], v[108:111]\n"
        "v_mfma_f32_16x16x32_bf16 v[128:131], v[56:59], v[16:19], v[128:131]\nv_mfma_f32_16x16x32_bf16 v[148:151], v[60:63], v[16:19], v[148:151]\nds_read_b128 v[16:19], %[xa] offset:12288\n"
        "s_waitcnt lgkmcnt(4)\nv_mfma_f32_16x16x32_bf16 v[100:103], v[48:51], v[20:23], v[100:103]\nv_mfma_f32_16x16x32_bf16 v[112:115], v[52:55], v[20:23], v[112:115]\n"
        "v_mfma_f32_16x16x32_bf16 v[132:135], v[56:59], v[20:23], v[132:135]\nv_mfma_f32_16x16x32_bf16 v[152:155], v[60:63], v[20:23], v[152:155]\nds_read_b128 v[20:23], %[xa] offset:14336\n"
        "s_waitcnt lgkmcnt(4)\nv_mfma_f32_16x16x32_bf16 v[104:107], v[52:55], v[12:15], v[104:107]\nv_mfma_f32_16x16x32_bf16 v[124:127], v[56:59], v[12:15], v[124:127]\n"
        "v_mfma_f32_16x16x32_bf16 v[144:147], v[60:63], v[12:15], v[144:147]\nds_read_b128 v[12:15], %[xa] offset:10240\ns_waitcnt lgkmcnt(4)\n"
        "v_mfma_f32_16x16x32_bf16 v[116:119], v[52:55], v[24:27], v[116:119]\nv_mfma_f32_16x16x32_bf16 v[136:139], v[56:59], v[24:27], v[136:139]\n"
        "v_mfma_f32_16x16x32_bf16 v[156:159], v[60:63], v[24:27], v[156:159]\nds_read_b128 v[24:27], %[xa] offset:16384\ns_waitcnt lgkmcnt(4)\n"
        "v_mfma_f32_16x16x32_bf16 v[120:123], v[52:55], v[28:31], v[120:123]\nv_add_u32 %[sy], 4, %[sy]\nv_mfma_f32_16x16x32_bf16 v[140:143], v[56:59], v[28:31], v[140:143]\n"
        "v_mfma_f32_16x16x32_bf16 v[160:163], v[60:63], v[28:31], v[160:163]\ns_waitcnt vmcnt(4) lgkmcnt(3)\nv_mfma_f32_16x16x32_bf16 v[96:99], v[64:67], v[16:19], v[96:99]\n"
        "ds_read_b128 v[28:31], %[xa] offset:18432\nv_mfma_f32_16x16x32_bf16 v[108:111], v[68:71], v[16:19], v[108:111]\nv_mfma_f32_16x16x32_bf16 v[128:131], v[72:75], v[16:19], v[128:131]\n"
        "ds_read_b32 %[pv], %[sy] offset:4\nv_mfma_f32_16x16x32_bf16 v[148:151], v[76:79], v[16:19], v[148:151]\nds_read_b128 v[16:19], %[xb] offset:12288\ns_waitcnt lgkmcnt(5)\n"
        "v_mfma_f32_16x16x32_bf16 v[100:103], v[64:67], v[20:23], v[100:103]\nv_mfma_f32_16x16x32_bf16 v[112:115], v[68:71], v[20:23], v[112:115]\n"
        "v_mfma_f32_16x16x32_bf16 v[132:135], v[72:75], v[20:23], v[132:135]\nv_mfma_f32_16x16x32_bf16 v[152:155], v[76:79], v[20:23], v[152:155]\nds_read_b128 v[20:23], %[xb] offset:14336\n"
        "s_waitcnt lgkmcnt(5)\nv_mfma_f32_16x16x32_bf16 v[104:107], v[68:71], v[12:15], v[104:107]\nv_mfma_f32_16x16x32_bf16 v[124:127], v[72:75], v[12:15], v[124:127]\n"
        "v_mfma_f32_16x16x32_bf16 v[144:147], v[76:79], v[12:15], v[144:147]\nds_read_b128 v[12:15], %[xb] offset:10240\ns_waitcnt lgkmcnt(5)\n"
        "v_mfma_f32_16x16x32_bf16 v[116:119], v[68:71], v[24:27], v[116:119]\nv_mfma_f32_16x16x32_bf16 v[136:139], v[72:75], v[24:27], v[136:139]\n"
        "v_mfma_f32_16x16x32_bf16 v[156:159], v[76:79], v[24:27], v[156:159]\nds_read_b128 v[24:27], %[xb] offset:16384\ns_waitcnt lgkmcnt(5)\n"
        "v_mfma_f32_16x16x32_bf16 v[120:123], v[68:71], v[28:31], v[120:123]\nv_mfma_f32_16x16x32_bf16 v[140:143], v[72:75], v[28:31], v[140:143]\n"
        "v_mfma_f32_16x16x32_bf16 v[160:163], v[76:79], v[28:31], v[160:163]\ns_waitcnt vmcnt(0) lgkmcnt(3)\nv_mfma_f32_16x16x32_bf16 v[96:99], v[80:83], v[16:19], v[96:99]\n"
        "v_readfirstlane_b32 %[s0], %[pv]\nds_read_b128 v[28:31], %[xb] offset:18432\nv_mfma_f32_16x16x32_bf16 v[108:111], v[84:87], v[16:19], v[108:111]\n"
        "v_mfma_f32_16x16x32_bf16 v[128:131], v[88:91], v[16:19], v[128:131]\ns_cmp_ge_i32 %[s0], %[nst]\nv_mfma_f32_16x16x32_bf16 v[148:151], v[92:95], v[16:19], v[148:151]\ns_cbranch_scc0 .LB0t%=\n"
        ".LB0u%=:\ns_waitcnt lgkmcnt(3)\nv_mfma_f32_16x16x32_bf16 v[100:103], v[80:83], v[20:23], v[100:103]\nv_add_u32 %[sy], 4, %[sy]\nds_read_b128 v[16:19], %[mra] offset:256\n"
        "v_mfma_f32_16x16x32_bf16 v[112:115], v[84:87], v[20:23], v[112:115]\nv_mfma_f32_16x16x32_bf16 v[132:135], v[88:91], v[20:23], v[132:135]\n"
        "v_mfma_f32_16x16x32_bf16 v[152:155], v[92:95], v[20:23], v[152:155]\nds_read_b128 v[32:35], %[cwa] offset:448\ns_waitcnt lgkmcnt(4)\n"
        "v_mfma_f32_16x16x32_bf16 v[104:107], v[84:87], v[12:15], v[104:107]\nds_read_b128 v[36:39], %[cwa] offset:128\nds_read_b128 v[20:23], %[mra] offset:512\n"
        "v_mfma_f32_16x16x32_bf16 v[124:127], v[88:91], v[12:15], v[124:127]\nds_read_b128 v[40:43], %[cwa] offset:192\nv_mfma_f32_16x16x32_bf16 v[144:147], v[92:95], v[12:15], v[144:147]\n"
        "ds_read_b128 v[44:47], %[cwa] offset:768\ns_waitcnt lgkmcnt(7)\nv_mfma_f32_16x16x32_bf16 v[116:119], v[84:87], v[24:27], v[116:119]\nv_add_u32 %[sy], 4, %[sy]\n"
        "ds_read_b128 v[12:15], %[mra] offset:0\nv_mfma_f32_16x16x32_bf16 v[136:139], v[88:91], v[24:27], v[136:139]\nv_mfma_f32_16x16x32_bf16 v[156:159], v[92:95], v[24:27], v[156:159]\n"
        "ds_read_b128 v[24:27], %[mra] offset:768\ns_waitcnt lgkmcnt(8)\nv_mfma_f32_16x16x32_bf16 v[120:123], v[84:87], v[28:31], v[120:123]\n"
        "v_mfma_f32_16x16x32_bf16 v[140:143], v[88:91], v[28:31], v[140:143]\nv_mfma_f32_16x16x32_bf16 v[160:163], v[92:95], v[28:31], v[160:163]\nds_read_b128 v[28:31], %[mra] offset:1024\n"
        "s_setprio %[pfs]\ns_waitcnt lgkmcnt(1)\n.long 0xd3b04060, 0x1d822120\n.long 0xd3b04062, 0x1d8a2122\n.long 0xd3b0406c, 0x1db22124\n.long 0xd3b0406e, 0x1dba2126\n.long 0xd3b14060, 0x18022560\n"
        ".long 0xd3b14062, 0x18022562\n.long 0xd3b04080, 0x1e022128\n.long 0xd3b04082, 0x1e0a212a\n.long 0xd3b1406c, 0x1802256c\n.long 0xd3b1406e, 0x1802256e\nv_cvt_pk_bf16_f32 v96, v96, v97\n"
        "v_cvt_pk_bf16_f32 v97, v98, v99\n.long 0xd3b04064, 0x1d922920\n.long 0xd3b04066, 0x1d9a2922\n.long 0xd3b14080, 0x18022580\n.long 0xd3b14082, 0x18022582\nv_cvt_pk_bf16_f32 v108, v108, v109\n"
        "v_cvt_pk_bf16_f32 v109, v110, v111\nds_write_b64 %[sta], v[96:97] offset:8160\n.long 0xd3b04070, 0x1dc22924\n.long 0xd3b04072, 0x1dca2926\n.long 0xd3b14064, 0x18022d64\n"
        ".long 0xd3b14066, 0x18022d66\nv_cvt_pk_bf16_f32 v128, v128, v129\nv_cvt_pk_bf16_f32 v129, v130, v131\nds_write_b64 %[sta], v[108:109] offset:8000\n.long 0xd3b04084, 0x1e122928\n"
        ".long 0xd3b04086, 0x1e1a292a\n.long 0xd3b14070, 0x18022d70\n.long 0xd3b14072, 0x18022d72\nv_cvt_pk_bf16_f32 v100, v100, v101\nv_cvt_pk_bf16_f32 v101, v102, v103\n"
        "ds_write_b64 %[sta], v[128:129] offset:8032\n.long 0xd3b04068, 0x1da21924\n.long 0xd3b0406a, 0x1daa1926\n.long 0xd3b14084, 0x18022d84\n.long 0xd3b14086, 0x18022d86\n"
        "v_cvt_pk_bf16_f32 v112, v112, v113\nv_cvt_pk_bf16_f32 v113, v114, v115\nds_write_b64 %[sta], v[100:101] offset:16096\n.long 0xd3b0407c, 0x1df21928\n.long 0xd3b0407e, 0x1dfa192a\n"
        ".long 0xd3b14068, 0x18021d68\n.long 0xd3b1406a, 0x18021d6a\nv_cvt_pk_bf16_f32 v132, v132, v133\nv_cvt_pk_bf16_f32 v133, v134, v135\nds_write_b64 %[sta], v[112:113] offset:15936\n"
        ".long 0xd3b04074, 0x1dd23124\n.long 0xd3b04076, 0x1dda3126\n.long 0xd3b1407c, 0x18021d7c\n.long 0xd3b1407e, 0x18021d7e\nv_cvt_pk_bf16_f32 v104, v104, v105\nv_cvt_pk_bf16_f32 v105, v106, v107\n"
        "ds_write_b64 %[sta], v[132:133] offset:15968\n.long 0xd3b04088, 0x1e223128\n.long 0xd3b0408a, 0x1e2a312a\n.long 0xd3b14074, 0x18023574\n.long 0xd3b14076, 0x18023576\n"
        "v_cvt_pk_bf16_f32 v124, v124, v125\nv_cvt_pk_bf16_f32 v125, v126, v127\nds_write_b64 %[sta], v[104:105] offset:64\ns_waitcnt lgkmcnt(7)\n.long 0xd3b04078, 0x1de23924\n"
        ".long 0xd3b0407a, 0x1dea3926\n.long 0xd3b14088, 0x18023588\n.long 0xd3b1408a, 0x1802358a\nv_cvt_pk_bf16_f32 v116, v116, v117\nv_cvt_pk_bf16_f32 v117, v118, v119\n"
        "ds_write_b64 %[sta], v[124:125] offset:96\n.long 0xd3b0408c, 0x1e323928\n.long 0xd3b0408e, 0x1e3a392a\n.long 0xd3b14078, 0x18023d78\n.long 0xd3b1407a, 0x18023d7a\n"
        "v_cvt_pk_bf16_f32 v136, v136, v137\nv_cvt_pk_bf16_f32 v137, v138, v139\nds_write_b64 %[sta], v[116:117] offset:23872\n.long 0xd3b1408c, 0x18023d8c\n.long 0xd3b1408e, 0x18023d8e\n"
        "v_cvt_pk_bf16_f32 v120, v120, v121\nv_cvt_pk_bf16_f32 v121, v122, v123\nds_write_b64 %[sta], v[136:137] offset:23904\nv_cvt_pk_bf16_f32 v140, v140, v141\nv_cvt_pk_bf16_f32 v141, v142, v143\n"
        "ds_write_b64 %[sta], v[120:121] offset:31808\nds_write_b64 %[sta], v[140:141] offset:31840\nds_write_b32 %[pg], %[sy]\nv_add_u32 %[sy], 4, %[sy]\n.long 0xd3b04094, 0x1e52212c\n"
        ".long 0xd3b04096, 0x1e5a212e\n.long 0xd3b04098, 0x1e62292c\n.long 0xd3b0409a, 0x1e6a292e\n.long 0xd3b14094, 0x18022594\n.long 0xd3b14096, 0x18022596\n.long 0xd3b04090, 0x1e42192c\n"
        ".long 0xd3b04092, 0x1e4a192e\n.long 0xd3b14098, 0x18022d98\n.long 0xd3b1409a, 0x18022d9a\nv_cvt_pk_bf16_f32 v148, v148, v149\nv_cvt_pk_bf16_f32 v149, v150, v151\n.long 0xd3b0409c, 0x1e72312c\n"
        ".long 0xd3b0409e, 0x1e7a312e\n.long 0xd3b14090, 0x18021d90\n.long 0xd3b14092, 0x18021d92\nv_cvt_pk_bf16_f32 v152, v152, v153\nv_cvt_pk_bf16_f32 v153, v154, v155\n"
        "ds_write_b64 %[sta], v[148:149] offset:8320\n.long 0xd3b040a0, 0x1e82392c\n.long 0xd3b040a2, 0x1e8a392e\n.long 0xd3b1409c, 0x1802359c\n.long 0xd3b1409e, 0x1802359e\n"
        "v_cvt_pk_bf16_f32 v144, v144, v145\nv_cvt_pk_bf16_f32 v145, v146, v147\nds_write_b64 %[sta], v[152:153] offset:16256\n.long 0xd3b140a0, 0x18023da0\n.long 0xd3b140a2, 0x18023da2\n"
        "v_cvt_pk_bf16_f32 v156, v156, v157\nv_cvt_pk_bf16_f32 v157, v158, v159\nds_write_b64 %[sta], v[144:145] offset:384\nv_cvt_pk_bf16_f32 v160, v160, v161\nv_cvt_pk_bf16_f32 v161, v162, v163\n"
        "ds_write_b64 %[sta], v[156:157] offset:24192\nds_write_b64 %[sta], v[160:161] offset:32128\nds_write_b32 %[pg], %[sy]\ns_branch .LE%=\n.LB0t%=:\ns_mov_b32 %[s1], 0x100000\n.LB0q%=:\n"
        "ds_read_b32 %[pv], %[sy] offset:4\ns_waitcnt lgkmcnt(0)\nv_readfirstlane_b32 %[s0], %[pv]\ns_nop 0\ns_cmp_ge_i32 %[s0], %[nst]\ns_cbranch_scc1 .LB0u%=\ns_sleep 1\ns_sub_u32 %[s1], %[s1], 1\n"
        "s_cbranch_scc0 .LB0q%=\ns_branch .LB0u%=\n.LC%=:\ns_mov_b32 %[s1], 0x100000\n.LC0v%=:\nds_read_b32 %[pv], %[sy] offset:0\ns_waitcnt lgkmcnt(0)\nv_readfirstlane_b32 %[s0], %[pv]\ns_nop 0\n"
        "s_cmp_ge_i32 %[s0], 3\ns_cbranch_scc1 .LC0h%=\ns_sleep 1\ns_sub_u32 %[s1], %[s1], 1\ns_cbranch_scc0 .LC0v%=\n.LC0h%=:\ns_mov_b32 %[s1], 0x100000\n.LC0w%=:\n"
        "ds_read_b32 %[pv], %[sy] offset:%[ow]\ns_waitcnt lgkmcnt(0)\nv_readfirstlane_b32 %[s0], %[pv]\ns_nop 0\ns_cmp_ge_i32 %[s0], 1\ns_cbranch_scc1 .LC0g%=\ns_sleep 1\ns_sub_u32 %[s1], %[s1], 1\n"
        "s_cbranch_scc0 .LC0w%=\n.LC0g%=:\ns_mov_b32 %[so], 0\nbuffer_load_dwordx4 v[32:35], %[wa], %[rs], %[so] offen offset:0 sc1\n"
        "buffer_load_dwordx4 v[36:39], %[wa], %[rs], %[so] offen offset:1024 sc1\nbuffer_load_dwordx4 v[40:43], %[wa], %[rs], %[so] offen offset:2048 sc1\n"
        "buffer_load_dwordx4 v[44:47], %[wa], %[rs], %[so] offen offset:3072 sc1\ns_mov_b32 %[so], 15360\nbuffer_load_dwordx4 v[48:51], %[wa], %[rs], %[so] offen offset:0 sc1\n"
        "buffer_load_dwordx4 v[52:55], %[wa], %[rs], %[so] offen offset:1024 sc1\nbuffer_load_dwordx4 v[56:59], %[wa], %[rs], %[so] offen offset:2048 sc1\n"
        "buffer_load_dwordx4 v[60:63], %[wa], %[rs], %[so] offen offset:3072 sc1\ns_mov_b32 %[so], 30720\nbuffer_load_dwordx4 v[64:67], %[wa], %[rs], %[so] offen offset:0 sc1\n"
        "buffer_load_dwordx4 v[68:71], %[wa], %[rs], %[so] offen offset:1024 sc1\nbuffer_load_dwordx4 v[72:75], %[wa], %[rs], %[so] offen offset:2048 sc1\n"
        "buffer_load_dwordx4 v[76:79], %[wa], %[rs], %[so] offen offset:3072 sc1\ns_mov_b32 %[so], 46080\nbuffer_load_dwordx4 v[80:83], %[wa], %[rs], %[so] offen offset:0 sc1\n"
        "buffer_load_dwordx4 v[84:87], %[wa], %[rs], %[so] offen offset:1024 sc1\nbuffer_load_dwordx4 v[88:91], %[wa], %[rs], %[so] offen offset:2048 sc1\n"
        "buffer_load_dwordx4 v[92:95], %[wa], %[rs], %[so] offen offset:3072 sc1\nds_read_b128 v[24:27], %[xa] offset:6144\nds_read_b128 v[28:31], %[xa] offset:8192\n"
        "ds_read_b128 v[12:15], %[xa] offset:0\nds_read_b128 v[16:19], %[xa] offset:2048\ns_waitcnt vmcnt(12) lgkmcnt(3)\nv_mfma_f32_16x16x32_bf16 v[96:99], v[32:35], v[24:27], 0\n"
        "ds_read_b128 v[20:23], %[xa] offset:4096\nv_mfma_f32_16x16x32_bf16 v[116:119], v[36:39], v[24:27], 0\nv_mfma_f32_16x16x32_bf16 v[136:139], v[40:43], v[24:27], 0\n"
        "v_mfma_f32_16x16x32_bf16 v[156:159], v[44:47], v[24:27], 0\nds_read_b128 v[24:27], %[xb] offset:6144\ns_waitcnt lgkmcnt(4)\nv_mfma_f32_16x16x32_bf16 v[100:103], v[32:35], v[28:31], 0\n"
        "v_mfma_f32_16x16x32_bf16 v[120:123], v[36:39], v[28:31], 0\nv_mfma_f32_16x16x32_bf16 v[140:143], v[40:43], v[28:31], 0\nv_mfma_f32_16x16x32_bf16 v[160:163], v[44:47], v[28:31], 0\n"
        "ds_read_b128 v[28:31], %[xb] offset:8192\ns_waitcnt lgkmcnt(4)\nv_mfma_f32_16x16x32_bf16 v[104:107], v[36:39], v[12:15], 0\nv_mfma_f32_16x16x32_bf16 v[124:127], v[40:43], v[12:15], 0\n"
        "v_mfma_f32_16x16x32_bf16 v[144:147], v[44:47], v[12:15], 0\nds_read_b128 v[12:15], %[xb] offset:0\ns_waitcnt lgkmcnt(4)\nv_mfma_f32_16x16x32_bf16 v[108:111], v[36:39], v[16:19], 0\n"
        "v_mfma_f32_16x16x32_bf16 v[128:131], v[40:43], v[16:19], 0\nv_mfma_f32_16x16x32_bf16 v[148:151], v[44:47], v[16:19], 0\nds_read_b128 v[16:19], %[xb] offset:2048\ns_waitcnt lgkmcnt(4)\n"
        "v_mfma_f32_16x16x32_bf16 v[112:115], v[36:39], v[20:23], 0\nv_mfma_f32_16x16x32_bf16 v[132:135], v[40:43], v[20:23], 0\nv_mfma_f32_16x16x32_bf16 v[152:155], v[44:47], v[20:23], 0\n.LC0l%=:\n"
        "s_waitcnt vmcnt(8) lgkmcnt(3)\nv_mfma_f32_16x16x32_bf16 v[96:99], v[48:51], v[24:27], v[96:99]\ns_addk_i32 %[so], 15360\nds_read_b128 v[20:23], %[xb] offset:4096\n"
        "v_mfma_f32_16x16x32_bf16 v[116:119], v[52:55], v[24:27], v[116:119]\nv_mfma_f32_16x16x32_bf16 v[136:139], v[56:59], v[24:27], v[136:139]\n"
        "buffer_load_dwordx4 v[32:35], %[wa], %[rs], %[so] offen offset:0 sc1\nv_mfma_f32_16x16x32_bf16 v[156:159], v[60:63], v[24:27], v[156:159]\nds_read_b128 v[24:27], %[xa] offset:16384\n"
        "s_waitcnt lgkmcnt(4)\nv_mfma_f32_16x16x32_bf16 v[100:103], v[48:51], v[28:31], v[100:103]\nv_mfma_f32_16x16x32_bf16 v[120:123], v[52:55], v[28:31], v[120:123]\n"
        "buffer_load_dwordx4 v[36:39], %[wa], %[rs], %[so] offen offset:1024 sc1\nv_mfma_f32_16x16x32_bf16 v[140:143], v[56:59], v[28:31], v[140:143]\n"
        "v_mfma_f32_16x16x32_bf16 v[160:163], v[60:63], v[28:31], v[160:163]\nds_read_b128 v[28:31], %[xa] offset:18432\ns_waitcnt lgkmcnt(4)\n"
        "v_mfma_f32_16x16x32_bf16 v[104:107], v[52:55], v[12:15], v[104:107]\nv_mfma_f32_16x16x32_bf16 v[124:127], v[56:59], v[12:15], v[124:127]\n"
        "buffer_load_dwordx4 v[40:43], %[wa], %[rs], %[so] offen offset:2048 sc1\nv_mfma_f32_16x16x32_bf16 v[144:147], v[60:63], v[12:15], v[144:147]\nds_read_b128 v[12:15], %[xa] offset:10240\n"
        "s_waitcnt lgkmcnt(4)\nv_mfma_f32_16x16x32_bf16 v[108:111], v[52:55], v[16:19], v[108:111]\nv_mfma_f32_16x16x32_bf16 v[128:131], v[56:59], v[16:19], v[128:131]\n"
        "buffer_load_dwordx4 v[44:47], %[wa], %[rs], %[so] offen offset:3072 sc1\nv_mfma_f32_16x16x32_bf16 v[148:151], v[60:63], v[16:19], v[148:151]\nds_read_b128 v[16:19], %[xa] offset:12288\n"
        "s_waitcnt lgkmcnt(4)\nv_mfma_f32_16x16x32_bf16 v[112:115], v[52:55], v[20:23], v[112:115]\nv_add_u32 %[sy], 4, %[sy]\nv_mfma_f32_16x16x32_bf16 v[132:135], v[56:59], v[20:23], v[132:135]\n"
        "ds_write_b32 %[pg], %[sy]\nv_mfma_f32_16x16x32_bf16 v[152:155], v[60:63], v[20:23], v[152:155]\ns_waitcnt vmcnt(8) lgkmcnt(4)\nv_mfma_f32_16x16x32_bf16 v[96:99], v[64:67], v[24:27], v[96:99]\n"
        "s_addk_i32 %[so], 15360\nds_read_b128 v[20:23], %[xa] offset:14336\nv_mfma_f32_16x16x32_bf16 v[116:119], v[68:71], v[24:27], v[116:119]\n"
        "v_mfma_f32_16x16x32_bf16 v[136:139], v[72:75], v[24:27], v[136:139]\nbuffer_load_dwordx4 v[48:51], %[wa], %[rs], %[so] offen offset:0 sc1\n"
        "v_mfma_f32_16x16x32_bf16 v[156:159], v[76:79], v[24:27], v[156:159]\nds_read_b128 v[24:27], %[xb] offset:16384\ns_waitcnt lgkmcnt(5)\n"
        "v_mfma_f32_16x16x32_bf16 v[100:103], v[64:67], v[28:31], v[100:103]\nv_mfma_f32_16x16x32_bf16 v[120:123], v[68:71], v[28:31], v[120:123]\n"
        "buffer_load_dwordx4 v[52:55], %[wa], %[rs], %[so] offen offset:1024 sc1\nv_mfma_f32_16x16x32_bf16 v[140:143], v[72:75], v[28:31], v[140:143]\n"
        "v_mfma_f32_16x16x32_bf16 v[160:163], v[76:79], v[28:31], v[160:163]\nds_read_b128 v[28:31], %[xb] offset:18432\ns_waitcnt lgkmcnt(5)\n"
        "v_mfma_f32_16x16x32_bf16 v[104:107], v[68:71], v[12:15], v[104:107]\nv_mfma_f32_16x16x32_bf16 v[124:127], v[72:75], v[12:15], v[124:127]\n"
        "buffer_load_dwordx4 v[56:59], %[wa], %[rs], %[so] offen offset:2048 sc1\nv_mfma_f32_16x16x32_bf16 v[144:147], v[76:79], v[12:15], v[144:147]\nds_read_b128 v[12:15], %[xb] offset:10240\n"
        "s_waitcnt lgkmcnt(5)\nv_mfma_f32_16x16x32_bf16 v[108:111], v[68:71], v[16:19], v[108:111]\nv_mfma_f32_16x16x32_bf16 v[128:131], v[72:75], v[16:19], v[128:131]\n"
        "buffer_load_dwordx4 v[60:63], %[wa], %[rs], %[so] offen offset:3072 sc1\nv_mfma_f32_16x16x32_bf16 v[148:151], v[76:79], v[16:19], v[148:151]\nds_read_b128 v[16:19], %[xb] offset:12288\n"
        "s_waitcnt lgkmcnt(4)\nv_mfma_f32_16x16x32_bf16 v[112:115], v[68:71], v[20:23], v[112:115]\nv_mfma_f32_16x16x32_bf16 v[132:135], v[72:75], v[20:23], v[132:135]\n"
        "v_mfma_f32_16x16x32_bf16 v[152:155], v[76:79], v[20:23], v[152:155]\ns_waitcnt vmcnt(8) lgkmcnt(3)\nv_mfma_f32_16x16x32_bf16 v[96:99], v[80:83], v[24:27], v[96:99]\ns_addk_i32 %[so], 15360\n"
        "ds_read_b128 v[20:23], %[xb] offset:14336\nv_mfma_f32_16x16x32_bf16 v[116:119], v[84:87], v[24:27], v[116:119]\nv_mfma_f32_16x16x32_bf16 v[136:139], v[88:91], v[24:27], v[136:139]\n"
        "buffer_load_dwordx4 v[64:67], %[wa], %[rs], %[so] offen offset:0 sc1\nv_mfma_f32_16x16x32_bf16 v[156:159], v[92:95], v[24:27], v[156:159]\nds_read_b128 v[24:27], %[xa] offset:26624\n"
        "s_waitcnt lgkmcnt(4)\nv_mfma_f32_16x16x32_bf16 v[100:103], v[80:83], v[28:31], v[100:103]\nv_mfma_f32_16x16x32_bf16 v[120:123], v[84:87], v[28:31], v[120:123]\n"
        "buffer_load_dwordx4 v[68:71], %[wa], %[rs], %[so] offen offset:1024 sc1\nv_mfma_f32_16x16x32_bf16 v[140:143], v[88:91], v[28:31], v[140:143]\n"
        "v_mfma_f32_16x16x32_bf16 v[160:163], v[92:95], v[28:31], v[160:163]\nds_read_b128 v[28:31], %[xa] offset:28672\ns_waitcnt lgkmcnt(4)\n"
        "v_mfma_f32_16x16x32_bf16 v[104:107], v[84:87], v[12:15], v[104:107]\nv_mfma_f32_16x16x32_bf16 v[124:127], v[88:91], v[12:15], v[124:127]\n"
        "buffer_load_dwordx4 v[72:75], %[wa], %[rs], %[so] offen offset:2048 sc1\nv_mfma_f32_16x16x32_bf16 v[144:147], v[92:95], v[12:15], v[144:147]\nds_read_b128 v[12:15], %[xa] offset:20480\n"
        "s_waitcnt lgkmcnt(4)\nv_mfma_f32_16x16x32_bf16 v[108:111], v[84:87], v[16:19], v[108:111]\nv_mfma_f32_16x16x32_bf16 v[128:131], v[88:91], v[16:19], v[128:131]\n"
        "buffer_load_dwordx4 v[76:79], %[wa], %[rs], %[so] offen offset:3072 sc1\nv_mfma_f32_16x16x32_bf16 v[148:151], v[92:95], v[16:19], v[148:151]\nds_read_b128 v[16:19], %[xa] offset:22528\n"
        "s_waitcnt lgkmcnt(4)\nv_mfma_f32_16x16x32_bf16 v[112:115], v[84:87], v[20:23], v[112:115]\nv_add_u32 %[sy], 4, %[sy]\nv_mfma_f32_16x16x32_bf16 v[132:135], v[88:91], v[20:23], v[132:135]\n"
        "ds_write_b32 %[pg], %[sy]\nv_mfma_f32_16x16x32_bf16 v[152:155], v[92:95], v[20:23], v[152:155]\ns_waitcnt vmcnt(8) lgkmcnt(4)\nv_mfma_f32_16x16x32_bf16 v[96:99], v[32:35], v[24:27], v[96:99]\n"
        "s_addk_i32 %[so], 15360\nds_read_b128 v[20:23], %[xa] offset:24576\nv_mfma_f32_16x16x32_bf16 v[116:119], v[36:39], v[24:27], v[116:119]\n"
        "v_mfma_f32_16x16x32_bf16 v[136:139], v[40:43], v[24:27], v[136:139]\nbuffer_load_dwordx4 v[80:83], %[wa], %[rs], %[so] offen offset:0 sc1\n"
        "v_mfma_f32_16x16x32_bf16 v[156:159], v[44:47], v[24:27], v[156:159]\nds_read_b128 v[24:27], %[xb] offset:26624\ns_waitcnt lgkmcnt(5)\n"
        "v_mfma_f32_16x16x32_bf16 v[100:103], v[32:35], v[28:31], v[100:103]\nv_mfma_f32_16x16x32_bf16 v[120:123], v[36:39], v[28:31], v[120:123]\n"
        "buffer_load_dwordx4 v[84:87], %[wa], %[rs], %[so] offen offset:1024 sc1\nv_mfma_f32_16x16x32_bf16 v[140:143], v[40:43], v[28:31], v[140:143]\n"
        "v_mfma_f32_16x16x32_bf16 v[160:163], v[44:47], v[28:31], v[160:163]\nds_read_b128 v[28:31], %[xb] offset:28672\ns_waitcnt lgkmcnt(5)\n"
        "v_mfma_f32_16x16x32_bf16 v[104:107], v[36:39], v[12:15], v[104:107]\nv_mfma_f32_16x16x32_bf16 v[124:127], v[40:43], v[12:15], v[124:127]\n"
        "buffer_load_dwordx4 v[88:91], %[wa], %[rs], %[so] offen offset:2048 sc1\nv_mfma_f32_16x16x32_bf16 v[144:147], v[44:47], v[12:15], v[144:147]\nds_read_b128 v[12:15], %[xb] offset:20480\n"
        "s_waitcnt lgkmcnt(5)\nv_mfma_f32_16x16x32_bf16 v[108:111], v[36:39], v[16:19], v[108:111]\nv_mfma_f32_16x16x32_bf16 v[128:131], v[40:43], v[16:19], v[128:131]\n"
        "buffer_load_dwordx4 v[92:95], %[wa], %[rs], %[so] offen offset:3072 sc1\nv_mfma_f32_16x16x32_bf16 v[148:151], v[44:47], v[16:19], v[148:151]\nds_read_b128 v[16:19], %[xb] offset:22528\n"
        "s_waitcnt lgkmcnt(4)\nv_mfma_f32_16x16x32_bf16 v[112:115], v[36:39], v[20:23], v[112:115]\nv_mfma_f32_16x16x32_bf16 v[132:135], v[40:43], v[20:23], v[132:135]\ns_cmp_lg_u32 %[so], 291840\n"
        "v_mfma_f32_16x16x32_bf16 v[152:155], v[44:47], v[20:23], v[152:155]\nv_add_u32 %[xa], 20480, %[xa]\nv_add_u32 %[xb], 20480, %[xb]\ns_cbranch_scc1 .LC0l%=\ns_waitcnt vmcnt(8) lgkmcnt(3)\n"
        "v_mfma_f32_16x16x32_bf16 v[96:99], v[48:51], v[24:27], v[96:99]\nds_read_b128 v[20:23], %[xb] offset:4096\nv_mfma_f32_16x16x32_bf16 v[116:119], v[52:55], v[24:27], v[116:119]\n"
        "v_mfma_f32_16x16x32_bf16 v[136:139], v[56:59], v[24:27], v[136:139]\nv_mfma_f32_16x16x32_bf16 v[156:159], v[60:63], v[24:27], v[156:159]\nds_read_b128 v[24:27], %[xa] offset:16384\n"
        "s_waitcnt lgkmcnt(4)\nv_mfma_f32_16x16x32_bf16 v[100:103], v[48:51], v[28:31], v[100:103]\nv_mfma_f32_16x16x32_bf16 v[120:123], v[52:55], v[28:31], v[120:123]\n"
        "v_mfma_f32_16x16x32_bf16 v[140:143], v[56:59], v[28:31], v[140:143]\nv_mfma_f32_16x16x32_bf16 v[160:163], v[60:63], v[28:31], v[160:163]\nds_read_b128 v[28:31], %[xa] offset:18432\n"
        "s_waitcnt lgkmcnt(4)\nv_mfma_f32_16x16x32_bf16 v[104:107], v[52:55], v[12:15], v[104:107]\nv_mfma_f32_16x16x32_bf16 v[124:127], v[56:59], v[12:15], v[124:127]\n"
        "v_mfma_f32_16x16x32_bf16 v[144:147], v[60:63], v[12:15], v[144:147]\nds_read_b128 v[12:15], %[xa] offset:10240\ns_waitcnt lgkmcnt(4)\n"
        "v_mfma_f32_16x16x32_bf16 v[108:111], v[52:55], v[16:19], v[108:111]\nv_mfma_f32_16x16x32_bf16 v[128:131], v[56:59], v[16:19], v[128:131]\n"
        "v_mfma_f32_16x16x32_bf16 v[148:151], v[60:63], v[16:19], v[148:151]\nds_read_b128 v[16:19], %[xa] offset:12288\ns_waitcnt lgkmcnt(4)\n"
        "v_mfma_f32_16x16x32_bf16 v[112:115], v[52:55], v[20:23], v[112:115]\nv_add_u32 %[sy], 4, %[sy]\nv_mfma_f32_16x16x32_bf16 v[132:135], v[56:59], v[20:23], v[132:135]\n"
        "v_mfma_f32_16x16x32_bf16 v[152:155], v[60:63], v[20:23], v[152:155]\ns_waitcnt vmcnt(4) lgkmcnt(3)\nv_mfma_f32_16x16x32_bf16 v[96:99], v[64:67], v[24:27], v[96:99]\n"
        "ds_read_b128 v[20:23], %[xa] offset:14336\nv_mfma_f32_16x16x32_bf16 v[116:119], v[68:71], v[24:27], v[116:119]\nv_mfma_f32_16x16x32_bf16 v[136:139], v[72:75], v[24:27], v[136:139]\n"
        "ds_read_b32 %[pv], %[sy] offset:4\nv_mfma_f32_16x16x32_bf16 v[156:159], v[76:79], v[24:27], v[156:159]\nds_read_b128 v[24:27], %[xb] offset:16384\ns_waitcnt lgkmcnt(5)\n"
        "v_mfma_f32_16x16x32_bf16 v[100:103], v[64:67], v[28:31], v[100:103]\nv_mfma_f32_16x16x32_bf16 v[120:123], v[68:71], v[28:31], v[120:123]\n"
        "v_mfma_f32_16x16x32_bf16 v[140:143], v[72:75], v[28:31], v[140:143]\nv_mfma_f32_16x16x32_bf16 v[160:163], v[76:79], v[28:31], v[160:163]\nds_read_b128 v[28:31], %[xb] offset:18432\n"
        "s_waitcnt lgkmcnt(5)\nv_mfma_f32_16x16x32_bf16 v[104:107], v[68:71], v[12:15], v[104:107]\nv_mfma_f32_16x16x32_bf16 v[124:127], v[72:75], v[12:15], v[124:127]\n"
        "v_mfma_f32_16x16x32_bf16 v[144:147], v[76:79], v[12:15], v[144:147]\nds_read_b128 v[12:15], %[xb] offset:10240\ns_waitcnt lgkmcnt(5)\n"
        "v_mfma_f32_16x16x32_bf16 v[108:111], v[68:71], v[16:19], v[108:111]\nv_mfma_f32_16x16x32_bf16 v[128:131], v[72:75], v[16:19], v[128:131]\n"
        "v_mfma_f32_16x16x32_bf16 v[148:151], v[76:79], v[16:19], v[148:151]\nds_read_b128 v[16:19], %[xb] offset:12288\ns_waitcnt lgkmcnt(5)\n"
        "v_mfma_f32_16x16x32_bf16 v[112:115], v[68:71], v[20:23], v[112:115]\nv_mfma_f32_16x16x32_bf16 v[132:135], v[72:75], v[20:23], v[132:135]\n"
        "v_mfma_f32_16x16x32_bf16 v[152:155], v[76:79], v[20:23], v[152:155]\ns_waitcnt vmcnt(0) lgkmcnt(3)\nv_mfma_f32_16x16x32_bf16 v[96:99], v[80:83], v[24:27], v[96:99]\n"
        "v_readfirstlane_b32 %[s0], %[pv]\nds_read_b128 v[20:23], %[xb] offset:14336\nv_mfma_f32_16x16x32_bf16 v[116:119], v[84:87], v[24:27], v[116:119]\n"
        "v_mfma_f32_16x16x32_bf16 v[136:139], v[88:91], v[24:27], v[136:139]\ns_cmp_ge_i32 %[s0], %[nst]\nv_mfma_f32_16x16x32_bf16 v[156:159], v[92:95], v[24:27], v[156:159]\ns_cbranch_scc0 .LC0t%=\n"
        ".LC0u%=:\ns_waitcnt lgkmcnt(3)\nv_mfma_f32_16x16x32_bf16 v[100:103], v[80:83], v[28:31], v[100:103]\nv_add_u32 %[sy], 4, %[sy]\nds_read_b128 v[24:27], %[mra] offset:768\n"
        "v_mfma_f32_16x16x32_bf16 v[120:123], v[84:87], v[28:31], v[120:123]\nv_mfma_f32_16x16x32_bf16 v[140:143], v[88:91], v[28:31], v[140:143]\n"
        "v_mfma_f32_16x16x32_bf16 v[160:163], v[92:95], v[28:31], v[160:163]\nds_read_b128 v[32:35], %[cwa] offset:448\ns_waitcnt lgkmcnt(4)\n"
        "v_mfma_f32_16x16x32_bf16 v[104:107], v[84:87], v[12:15], v[104:107]\nds_read_b128 v[36:39], %[cwa] offset:256\nds_read_b128 v[28:31], %[mra] offset:1024\n"
        "v_mfma_f32_16x16x32_bf16 v[124:127], v[88:91], v[12:15], v[124:127]\nds_read_b128 v[40:43], %[cwa] offset:832\nv_mfma_f32_16x16x32_bf16 v[144:147], v[92:95], v[12:15], v[144:147]\n"
        "ds_read_b128 v[44:47], %[cwa] offset:896\ns_waitcnt lgkmcnt(7)\nv_mfma_f32_16x16x32_bf16 v[108:111], v[84:87], v[16:19], v[108:111]\nv_add_u32 %[sy], 4, %[sy]\n"
        "ds_read_b128 v[12:15], %[mra] offset:0\nv_mfma_f32_16x16x32_bf16 v[128:131], v[88:91], v[16:19], v[128:131]\nv_mfma_f32_16x16x32_bf16 v[148:151], v[92:95], v[16:19], v[148:151]\n"
        "ds_read_b128 v[16:19], %[mra] offset:256\ns_waitcnt lgkmcnt(8)\nv_mfma_f32_16x16x32_bf16 v[112:115], v[84:87], v[20:23], v[112:115]\n"
        "v_mfma_f32_16x16x32_bf16 v[132:135], v[88:91], v[20:23], v[132:135]\nv_mfma_f32_16x16x32_bf16 v[152:155], v[92:95], v[20:23], v[152:155]\nds_read_b128 v[20:23], %[mra] offset:512\n"
        "s_setprio %[pfs]\ns_waitcnt lgkmcnt(1)\n.long 0xd3b04060, 0x1d823120\n.long 0xd3b04062, 0x1d8a3122\n.long 0xd3b04074, 0x1dd23124\n.long 0xd3b04076, 0x1dda3126\n.long 0xd3b14060, 0x18023560\n"
        ".long 0xd3b14062, 0x18023562\n.long 0xd3b04064, 0x1d923920\n.long 0xd3b04066, 0x1d9a3922\n.long 0xd3b14074, 0x18023574\n.long 0xd3b14076, 0x18023576\nv_cvt_pk_bf16_f32 v96, v96, v97\n"
        "v_cvt_pk_bf16_f32 v97, v98, v99\n.long 0xd3b04078, 0x1de23924\n.long 0xd3b0407a, 0x1dea3926\n.long 0xd3b14064, 0x18023d64\n.long 0xd3b14066, 0x18023d66\nv_cvt_pk_bf16_f32 v116, v116, v117\n"
        "v_cvt_pk_bf16_f32 v117, v118, v119\nds_write_b64 %[sta], v[96:97] offset:24032\n.long 0xd3b04068, 0x1da21924\n.long 0xd3b0406a, 0x1daa1926\n.long 0xd3b14078, 0x18023d78\n"
        ".long 0xd3b1407a, 0x18023d7a\nv_cvt_pk_bf16_f32 v100, v100, v101\nv_cvt_pk_bf16_f32 v101, v102, v103\nds_write_b64 %[sta], v[116:117] offset:23936\n.long 0xd3b0406c, 0x1db22124\n"
        ".long 0xd3b0406e, 0x1dba2126\n.long 0xd3b14068, 0x18021d68\n.long 0xd3b1406a, 0x18021d6a\nv_cvt_pk_bf16_f32 v120, v120, v121\nv_cvt_pk_bf16_f32 v121, v122, v123\n"
        "ds_write_b64 %[sta], v[100:101] offset:31968\ns_waitcnt lgkmcnt(3)\n.long 0xd3b04070, 0x1dc22924\n.long 0xd3b04072, 0x1dca2926\n.long 0xd3b1406c, 0x1802256c\n.long 0xd3b1406e, 0x1802256e\n"
        "v_cvt_pk_bf16_f32 v104, v104, v105\nv_cvt_pk_bf16_f32 v105, v106, v107\nds_write_b64 %[sta], v[120:121] offset:31872\n.long 0xd3b14070, 0x18022d70\n.long 0xd3b14072, 0x18022d72\n"
        "v_cvt_pk_bf16_f32 v108, v108, v109\nv_cvt_pk_bf16_f32 v109, v110, v111\nds_write_b64 %[sta], v[104:105] offset:128\nv_cvt_pk_bf16_f32 v112, v112, v113\nv_cvt_pk_bf16_f32 v113, v114, v115\n"
        "ds_write_b64 %[sta], v[108:109] offset:8064\nds_write_b64 %[sta], v[112:113] offset:16000\nds_write_b32 %[pg], %[sy]\nv_add_u32 %[sy], 4, %[sy]\n.long 0xd3b04088, 0x1e223128\n"
        ".long 0xd3b0408a, 0x1e2a312a\n.long 0xd3b0409c, 0x1e72312c\n.long 0xd3b0409e, 0x1e7a312e\n.long 0xd3b14088, 0x18023588\n.long 0xd3b1408a, 0x1802358a\n.long 0xd3b0408c, 0x1e323928\n"
        ".long 0xd3b0408e, 0x1e3a392a\n.long 0xd3b1409c, 0x1802359c\n.long 0xd3b1409e, 0x1802359e\nv_cvt_pk_bf16_f32 v136, v136, v137\nv_cvt_pk_bf16_f32 v137, v138, v139\n.long 0xd3b040a0, 0x1e82392c\n"
        ".long 0xd3b040a2, 0x1e8a392e\n.long 0xd3b1408c, 0x18023d8c\n.long 0xd3b1408e, 0x18023d8e\nv_cvt_pk_bf16_f32 v156, v156, v157\nv_cvt_pk_bf16_f32 v157, v158, v159\n"
        "ds_write_b64 %[sta], v[136:137] offset:24224\n.long 0xd3b0407c, 0x1df21928\n.long 0xd3b0407e, 0x1dfa192a\n.long 0xd3b140a0, 0x18023da0\n.long 0xd3b140a2, 0x18023da2\n"
        "v_cvt_pk_bf16_f32 v140, v140, v141\nv_cvt_pk_bf16_f32 v141, v142, v143\nds_write_b64 %[sta], v[156:157] offset:24256\n.long 0xd3b04090, 0x1e42192c\n.long 0xd3b04092, 0x1e4a192e\n"
        ".long 0xd3b1407c, 0x18021d7c\n.long 0xd3b1407e, 0x18021d7e\nv_cvt_pk_bf16_f32 v160, v160, v161\nv_cvt_pk_bf16_f32 v161, v162, v163\nds_write_b64 %[sta], v[140:141] offset:32160\n"
        ".long 0xd3b04080, 0x1e022128\n.long 0xd3b04082, 0x1e0a212a\n.long 0xd3b14090, 0x18021d90\n.long 0xd3b14092, 0x18021d92\nv_cvt_pk_bf16_f32 v124, v124, v125\nv_cvt_pk_bf16_f32 v125, v126, v127\n"
        "ds_write_b64 %[sta], v[160:161] offset:32192\n.long 0xd3b04094, 0x1e52212c\n.long 0xd3b04096, 0x1e5a212e\n.long 0xd3b14080, 0x18022580\n.long 0xd3b14082, 0x18022582\n"
        "v_cvt_pk_bf16_f32 v144, v144, v145\nv_cvt_pk_bf16_f32 v145, v146, v147\nds_write_b64 %[sta], v[124:125] offset:416\n.long 0xd3b04084, 0x1e122928\n.long 0xd3b04086, 0x1e1a292a\n"
        ".long 0xd3b14094, 0x18022594\n.long 0xd3b14096, 0x18022596\nv_cvt_pk_bf16_f32 v128, v128, v129\nv_cvt_pk_bf16_f32 v129, v130, v131\nds_write_b64 %[sta], v[144:145] offset:448\n"
        ".long 0xd3b04098, 0x1e62292c\n.long 0xd3b0409a, 0x1e6a292e\n.long 0xd3b14084, 0x18022d84\n.long 0xd3b14086, 0x18022d86\nv_cvt_pk_bf16_f32 v148, v148, v149\nv_cvt_pk_bf16_f32 v149, v150, v151\n"
        "ds_write_b64 %[sta], v[128:129] offset:8352\n.long 0xd3b14098, 0x18022d98\n.long 0xd3b1409a, 0x18022d9a\nv_cvt_pk_bf16_f32 v132, v132, v133\nv_cvt_pk_bf16_f32 v133, v134, v135\n"
        "ds_write_b64 %[sta], v[148:149] offset:8384\nv_cvt_pk_bf16_f32 v152, v152, v153\nv_cvt_pk_bf16_f32 v153, v154, v155\nds_write_b64 %[sta], v[132:133] offset:16288\n"
        "ds_write_b64 %[sta], v[152:153] offset:16320\nds_write_b32 %[pg], %[sy]\ns_branch .LE%=\n.LC0t%=:\ns_mov_b32 %[s1], 0x100000\n.LC0q%=:\nds_read_b32 %[pv], %[sy] offset:4\n"
        "s_waitcnt lgkmcnt(0)\nv_readfirstlane_b32 %[s0], %[pv]\ns_nop 0\ns_cmp_ge_i32 %[s0], %[nst]\ns_cbranch_scc1 .LC0u%=\ns_sleep 1\ns_sub_u32 %[s1], %[s1], 1\ns_cbranch_scc0 .LC0q%=\n"
        "s_branch .LC0u%=\n.LE%=:\n"
        : [pv] "=&v"(pv), [xa] "+v"(xa), [xb] "+v"(xb), [sy] "+v"(sy), [so] "=&s"(so), [s0] "=&s"(s0_), [s1] "=&s"(s1_)
        : [wa] "v"(wa), [rs] "s"(wrsrc), [pg] "v"(pga), [cwa] "v"(cwa), [mra] "v"(mra), [sta] "v"(sta), [w] "s"(w), [ow] "i"(SY_W - SY_X), [pfs] "i"(PFS), [nst] "i"(NST0)
        : "memory", "scc", "v12", "v13", "v14", "v15", "v16", "v17", "v18", "v19", "v20", "v21", "v22", "v23", "v24", "v25", "v26", "v27", "v28", "v29", "v30", "v31", "v32", "v33", "v34", "v35", "v36", "v37", "v38", "v39", "v40", "v41", "v42", "v43", "v44", "v45", "v46", "v47", "v48", "v49", "v50", "v51", "v52", "v53", "v54", "v55", "v56", "v57", "v58", "v59", "v60", "v61", "v62", "v63", "v64", "v65", "v66", "v67", "v68", "v69", "v70", "v71", "v72", "v73", "v74", "v75", "v76", "v77", "v78", "v79", "v80", "v81", "v82", "v83", "v84", "v85", "v86", "v87", "v88", "v89", "v90", "v91", "v92", "v93", "v94", "v95", "v96", "v97", "v98", "v99", "v100", "v101", "v102", "v103", "v104", "v105", "v106", "v107", "v108", "v109", "v110", "v111", "v112", "v113", "v114", "v115", "v116", "v117", "v118", "v119", "v120", "v121", "v122", "v123", "v124", "v125", "v126", "v127", "v128", "v129", "v130", "v131", "v132", "v133", "v134", "v135", "v136", "v137", "v138", "v139", "v140", "v141", "v142", "v143", "v144", "v145", "v146", "v147", "v148", "v149", "v150", "v151", "v152", "v153", "v154", "v155", "v156", "v157", "v158", "v159", "v160", "v161", "v162", "v163", "v164", "v165", "v166", "v167");
    } else {
      asm volatile(
        "s_cmp_lt_u32 %[w], 2\ns_cbranch_scc0 .LB%=\ns_mov_b32 %[so], 0\nbuffer_load_dwordx4 v[32:35], %[wa], %[rs], %[so] offen offset:0 sc1\n"
        "buffer_load_dwordx4 v[36:39], %[wa], %[rs], %[so] offen offset:1024 sc1\nbuffer_load_dwordx4 v[40:43], %[wa], %[rs], %[so] offen offset:2048 sc1\n"
        "buffer_load_dwordx4 v[44:47], %[wa], %[rs], %[so] offen offset:3072 sc1\ns_mov_b32 %[so], 15360\nbuffer_load_dwordx4 v[48:51], %[wa], %[rs], %[so] offen offset:0 sc1\n"
        "buffer_load_dwordx4 v[52:55], %[wa], %[rs], %[so] offen offset:1024 sc1\nbuffer_load_dwordx4 v[56:59], %[wa], %[rs], %[so] offen offset:2048 sc1\n"
        "buffer_load_dwordx4 v[60:63], %[wa], %[rs], %[so] offen offset:3072 sc1\ns_mov_b32 %[so], 30720\nbuffer_load_dwordx4 v[64:67], %[wa], %[rs], %[so] offen offset:0 sc1\n"
        "buffer_load_dwordx4 v[68:71], %[wa], %[rs], %[so] offen offset:1024 sc1\nbuffer_load_dwordx4 v[72:75], %[wa], %[rs], %[so] offen offset:2048 sc1\n"
        "buffer_load_dwordx4 v[76:79], %[wa], %[rs], %[so] offen offset:3072 sc1\ns_mov_b32 %[so], 46080\nbuffer_load_dwordx4 v[80:83], %[wa], %[rs], %[so] offen offset:0 sc1\n"
        "buffer_load_dwordx4 v[84:87], %[wa], %[rs], %[so] offen offset:1024 sc1\nbuffer_load_dwordx4 v[88:91], %[wa], %[rs], %[so] offen offset:2048 sc1\n"
        "buffer_load_dwordx4 v[92:95], %[wa], %[rs], %[so] offen offset:3072 sc1\ns_barrier\ns_mov_b32 %[s1], 0x100000\n.LA1w%=:\nds_read_b32 %[pv], %[sy] offset:0\ns_waitcnt lgkmcnt(0)\n"
        "v_readfirstlane_b32 %[s0], %[pv]\ns_nop 0\ns_cmp_ge_i32 %[s0], 6\ns_cbranch_scc1 .LA1g%=\ns_sleep 1\ns_sub_u32 %[s1], %[s1], 1\ns_cbranch_scc0 .LA1w%=\n.LA1g%=:\n"
        "ds_read_b64_tr_b16 v[16:17], %[xa] offset:1056\nds_read_b64_tr_b16 v[18:19], %[xa] offset:1568\nds_read_b64_tr_b16 v[20:21], %[xa] offset:2112\nds_read_b64_tr_b16 v[22:23], %[xa] offset:2624\n"
        "ds_read_b64_tr_b16 v[12:13], %[xa] offset:0\nds_read_b64_tr_b16 v[14:15], %[xa] offset:512\nds_read_b64_tr_b16 v[24:25], %[xa] offset:3168\nds_read_b64_tr_b16 v[26:27], %[xa] offset:3680\n"
        "s_waitcnt vmcnt(12) lgkmcnt(6)\nv_mfma_f32_16x16x32_bf16 v[100:103], v[32:35], v[16:19], 0\nds_read_b64_tr_b16 v[28:29], %[xa] offset:4224\n"
        "v_mfma_f32_16x16x32_bf16 v[116:119], v[36:39], v[16:19], 0\nds_read_b64_tr_b16 v[30:31], %[xa] offset:4736\nv_mfma_f32_16x16x32_bf16 v[136:139], v[40:43], v[16:19], 0\n"
        "ds_read_b32 %[pv], %[sy] offset:4\nv_mfma_f32_16x16x32_bf16 v[152:155], v[44:47], v[16:19], 0\nds_read_b64_tr_b16 v[16:17], %[xa] offset:6336\ns_waitcnt lgkmcnt(8)\n"
        "v_mfma_f32_16x16x32_bf16 v[104:107], v[32:35], v[20:23], 0\nds_read_b64_tr_b16 v[18:19], %[xa] offset:6848\nv_mfma_f32_16x16x32_bf16 v[120:123], v[36:39], v[20:23], 0\n"
        "v_mfma_f32_16x16x32_bf16 v[140:143], v[40:43], v[20:23], 0\nv_mfma_f32_16x16x32_bf16 v[156:159], v[44:47], v[20:23], 0\nds_read_b64_tr_b16 v[20:21], %[xa] offset:7392\ns_waitcnt lgkmcnt(8)\n"
        "v_mfma_f32_16x16x32_bf16 v[96:99], v[32:35], v[12:15], 0\nds_read_b64_tr_b16 v[22:23], %[xa] offset:7904\nv_mfma_f32_16x16x32_bf16 v[132:135], v[40:43], v[12:15], 0\n"
        "ds_read_b64_tr_b16 v[12:13], %[xa] offset:5280\ns_waitcnt lgkmcnt(8)\nv_mfma_f32_16x16x32_bf16 v[108:111], v[32:35], v[24:27], 0\nds_read_b64_tr_b16 v[14:15], %[xa] offset:5792\n"
        "v_mfma_f32_16x16x32_bf16 v[124:127], v[36:39], v[24:27], 0\nv_mfma_f32_16x16x32_bf16 v[144:147], v[40:43], v[24:27], 0\nv_mfma_f32_16x16x32_bf16 v[160:163], v[44:47], v[24:27], 0\n"
        "ds_read_b64_tr_b16 v[24:25], %[xa] offset:8448\ns_waitcnt lgkmcnt(8)\nv_mfma_f32_16x16x32_bf16 v[112:115], v[32:35], v[28:31], 0\nds_read_b64_tr_b16 v[26:27], %[xa] offset:8960\n"
        "v_mfma_f32_16x16x32_bf16 v[128:131], v[36:39], v[28:31], 0\nv_mfma_f32_16x16x32_bf16 v[148:151], v[40:43], v[28:31], 0\nv_mfma_f32_16x16x32_bf16 v[164:167], v[44:47], v[28:31], 0\n.LA1l%=:\n"
        "s_waitcnt vmcnt(8) lgkmcnt(6)\nv_mfma_f32_16x16x32_bf16 v[100:103], v[48:51], v[16:19], v[100:103]\ns_addk_i32 %[so], 15360\nv_readfirstlane_b32 %[s0], %[pv]\n"
        "v_mfma_f32_16x16x32_bf16 v[116:119], v[52:55], v[16:19], v[116:119]\ns_cmp_ge_i32 %[s0], 6\nds_read_b64_tr_b16 v[28:29], %[xa] offset:9504\n"
        "v_mfma_f32_16x16x32_bf16 v[136:139], v[56:59], v[16:19], v[136:139]\ns_cbranch_scc0 .LA1s0_%=\n.LA1r0_%=:\nds_read_b64_tr_b16 v[30:31], %[xa] offset:10016\n"
        "v_mfma_f32_16x16x32_bf16 v[152:155], v[60:63], v[16:19], v[152:155]\nds_read_b64_tr_b16 v[16:17], %[xa] offset:11616\ns_waitcnt lgkmcnt(7)\n"
        "v_mfma_f32_16x16x32_bf16 v[104:107], v[48:51], v[20:23], v[104:107]\nds_read_b64_tr_b16 v[18:19], %[xa] offset:12128\nbuffer_load_dwordx4 v[32:35], %[wa], %[rs], %[so] offen offset:0 sc1\n"
        "v_mfma_f32_16x16x32_bf16 v[120:123], v[52:55], v[20:23], v[120:123]\nbuffer_load_dwordx4 v[36:39], %[wa], %[rs], %[so] offen offset:1024 sc1\n"
        "v_mfma_f32_16x16x32_bf16 v[140:143], v[56:59], v[20:23], v[140:143]\nv_mfma_f32_16x16x32_bf16 v[156:159], v[60:63], v[20:23], v[156:159]\nds_read_b64_tr_b16 v[20:21], %[xa] offset:12672\n"
        "s_waitcnt lgkmcnt(7)\nv_mfma_f32_16x16x32_bf16 v[96:99], v[48:51], v[12:15], v[96:99]\nds_read_b64_tr_b16 v[22:23], %[xa] offset:13184\n"
        "v_mfma_f32_16x16x32_bf16 v[132:135], v[56:59], v[12:15], v[132:135]\nds_read_b64_tr_b16 v[12:13], %[xa] offset:10560\ns_waitcnt lgkmcnt(7)\n"
        "v_mfma_f32_16x16x32_bf16 v[108:111], v[48:51], v[24:27], v[108:111]\nds_read_b64_tr_b16 v[14:15], %[xa] offset:11072\nv_mfma_f32_16x16x32_bf16 v[124:127], v[52:55], v[24:27], v[124:127]\n"
        "buffer_load_dwordx4 v[40:43], %[wa], %[rs], %[so] offen offset:2048 sc1\nv_mfma_f32_16x16x32_bf16 v[144:147], v[56:59], v[24:27], v[144:147]\n"
        "buffer_load_dwordx4 v[44:47], %[wa], %[rs], %[so] offen offset:3072 sc1\nv_mfma_f32_16x16x32_bf16 v[160:163], v[60:63], v[24:27], v[160:163]\nds_read_b64_tr_b16 v[24:25], %[xa] offset:13728\n"
        "s_waitcnt lgkmcnt(7)\nv_mfma_f32_16x16x32_bf16 v[112:115], v[48:51], v[28:31], v[112:115]\nds_read_b64_tr_b16 v[26:27], %[xa] offset:14240\n"
        "v_mfma_f32_16x16x32_bf16 v[128:131], v[52:55], v[28:31], v[128:131]\nv_add_u32 %[sy], 4, %[sy]\nv_mfma_f32_16x16x32_bf16 v[148:151], v[56:59], v[28:31], v[148:151]\nds_write_b32 %[pg], %[sy]\n"
        "v_mfma_f32_16x16x32_bf16 v[164:167], v[60:63], v[28:31], v[164:167]\ns_waitcnt vmcnt(8) lgkmcnt(7)\nv_mfma_f32_16x16x32_bf16 v[100:103], v[64:67], v[16:19], v[100:103]\n"
        "s_addk_i32 %[so], 15360\nds_read_b64_tr_b16 v[28:29], %[xa] offset:14784\nv_mfma_f32_16x16x32_bf16 v[116:119], v[68:71], v[16:19], v[116:119]\nds_read_b64_tr_b16 v[30:31], %[xa] offset:15296\n"
        "v_mfma_f32_16x16x32_bf16 v[136:139], v[72:75], v[16:19], v[136:139]\nds_read_b32 %[pv], %[sy] offset:4\nbuffer_load_dwordx4 v[48:51], %[wa], %[rs], %[so] offen offset:0 sc1\n"
        "v_mfma_f32_16x16x32_bf16 v[152:155], v[76:79], v[16:19], v[152:155]\nds_read_b64_tr_b16 v[16:17], %[xa] offset:16896\ns_waitcnt lgkmcnt(9)\n"
        "v_mfma_f32_16x16x32_bf16 v[104:107], v[64:67], v[20:23], v[104:107]\nds_read_b64_tr_b16 v[18:19], %[xa] offset:17408\nv_mfma_f32_16x16x32_bf16 v[120:123], v[68:71], v[20:23], v[120:123]\n"
        "buffer_load_dwordx4 v[52:55], %[wa], %[rs], %[so] offen offset:1024 sc1\nv_mfma_f32_16x16x32_bf16 v[140:143], v[72:75], v[20:23], v[140:143]\n"
        "v_mfma_f32_16x16x32_bf16 v[156:159], v[76:79], v[20:23], v[156:159]\nds_read_b64_tr_b16 v[20:21], %[xa] offset:17952\ns_waitcnt lgkmcnt(9)\n"
        "v_mfma_f32_16x16x32_bf16 v[96:99], v[64:67], v[12:15], v[96:99]\nds_read_b64_tr_b16 v[22:23], %[xa] offset:18464\nv_mfma_f32_16x16x32_bf16 v[132:135], v[72:75], v[12:15], v[132:135]\n"
        "ds_read_b64_tr_b16 v[12:13], %[xa] offset:15840\ns_waitcnt lgkmcnt(9)\nv_mfma_f32_16x16x32_bf16 v[108:111], v[64:67], v[24:27], v[108:111]\nds_read_b64_tr_b16 v[14:15], %[xa] offset:16352\n"
        "v_mfma_f32_16x16x32_bf16 v[124:127], v[68:71], v[24:27], v[124:127]\nbuffer_load_dwordx4 v[56:59], %[wa], %[rs], %[so] offen offset:2048 sc1\n"
        "v_mfma_f32_16x16x32_bf16 v[144:147], v[72:75], v[24:27], v[144:147]\nbuffer_load_dwordx4 v[60:63], %[wa], %[rs], %[so] offen offset:3072 sc1\n"
        "v_mfma_f32_16x16x32_bf16 v[160:163], v[76:79], v[24:27], v[160:163]\nds_read_b64_tr_b16 v[24:25], %[xa] offset:19008\ns_waitcnt lgkmcnt(8)\n"
        "v_mfma_f32_16x16x32_bf16 v[112:115], v[64:67], v[28:31], v[112:115]\nds_read_b64_tr_b16 v[26:27], %[xa] offset:19520\nv_mfma_f32_16x16x32_bf16 v[128:131], v[68:71], v[28:31], v[128:131]\n"
        "v_mfma_f32_16x16x32_bf16 v[148:151], v[72:75], v[28:31], v[148:151]\nv_mfma_f32_16x16x32_bf16 v[164:167], v[76:79], v[28:31], v[164:167]\ns_waitcnt vmcnt(8) lgkmcnt(6)\n"
        "v_mfma_f32_16x16x32_bf16 v[100:103], v[80:83], v[16:19], v[100:103]\ns_addk_i32 %[so], 15360\nv_readfirstlane_b32 %[s0], %[pv]\n"
        "v_mfma_f32_16x16x32_bf16 v[116:119], v[84:87], v[16:19], v[116:119]\ns_cmp_ge_i32 %[s0], 6\nds_read_b64_tr_b16 v[28:29], %[xa] offset:20064\n"
        "v_mfma_f32_16x16x32_bf16 v[136:139], v[88:91], v[16:19], v[136:139]\ns_cbranch_scc0 .LA1s1_%=\n.LA1r1_%=:\nds_read_b64_tr_b16 v[30:31], %[xa] offset:20576\n"
        "v_mfma_f32_16x16x32_bf16 v[152:155], v[92:95], v[16:19], v[152:155]\nds_read_b64_tr_b16 v[16:17], %[xa] offset:22176\ns_waitcnt lgkmcnt(7)\n"
        "v_mfma_f32_16x16x32_bf16 v[104:107], v[80:83], v[20:23], v[104:107]\nds_read_b64_tr_b16 v[18:19], %[xa] offset:22688\nbuffer_load_dwordx4 v[64:67], %[wa], %[rs], %[so] offen offset:0 sc1\n"
        "v_mfma_f32_16x16x32_bf16 v[120:123], v[84:87], v[20:23], v[120:123]\nbuffer_load_dwordx4 v[68:71], %[wa], %[rs], %[so] offen offset:1024 sc1\n"
        "v_mfma_f32_16x16x32_bf16 v[140:143], v[88:91], v[20:23], v[140:143]\nv_mfma_f32_16x16x32_bf16 v[156:159], v[92:95], v[20:23], v[156:159]\nds_read_b64_tr_b16 v[20:21], %[xa] offset:23232\n"
        "s_waitcnt lgkmcnt(7)\nv_mfma_f32_16x16x32_bf16 v[96:99], v[80:83], v[12:15], v[96:99]\nds_read_b64_tr_b16 v[22:23], %[xa] offset:23744\n"
        "v_mfma_f32_16x16x32_bf16 v[132:135], v[88:91], v[12:15], v[132:135]\nds_read_b64_tr_b16 v[12:13], %[xa] offset:21120\ns_waitcnt lgkmcnt(7)\n"
        "v_mfma_f32_16x16x32_bf16 v[108:111], v[80:83], v[24:27], v[108:111]\nds_read_b64_tr_b16 v[14:15], %[xa] offset:21632\nv_mfma_f32_16x16x32_bf16 v[124:127], v[84:87], v[24:27], v[124:127]\n"
        "buffer_load_dwordx4 v[72:75], %[wa], %[rs], %[so] offen offset:2048 sc1\nv_mfma_f32_16x16x32_bf16 v[144:147], v[88:91], v[24:27], v[144:147]\n"
        "buffer_load_dwordx4 v[76:79], %[wa], %[rs], %[so] offen offset:3072 sc1\nv_mfma_f32_16x16x32_bf16 v[160:163], v[92:95], v[24:27], v[160:163]\nds_read_b64_tr_b16 v[24:25], %[xa] offset:24288\n"
        "s_waitcnt lgkmcnt(7)\nv_mfma_f32_16x16x32_bf16 v[112:115], v[80:83], v[28:31], v[112:115]\nds_read_b64_tr_b16 v[26:27], %[xa] offset:24800\n"
        "v_mfma_f32_16x16x32_bf16 v[128:131], v[84:87], v[28:31], v[128:131]\nv_add_u32 %[sy], 4, %[sy]\nv_mfma_f32_16x16x32_bf16 v[148:151], v[88:91], v[28:31], v[148:151]\nds_write_b32 %[pg], %[sy]\n"
        "v_mfma_f32_16x16x32_bf16 v[164:167], v[92:95], v[28:31], v[164:167]\ns_waitcnt vmcnt(8) lgkmcnt(7)\nv_mfma_f32_16x16x32_bf16 v[100:103], v[32:35], v[16:19], v[100:103]\n"
        "s_addk_i32 %[so], 15360\nds_read_b64_tr_b16 v[28:29], %[xa] offset:25344\nv_mfma_f32_16x16x32_bf16 v[116:119], v[36:39], v[16:19], v[116:119]\nds_read_b64_tr_b16 v[30:31], %[xa] offset:25856\n"
        "v_mfma_f32_16x16x32_bf16 v[136:139], v[40:43], v[16:19], v[136:139]\nds_read_b32 %[pv], %[sy] offset:4\nbuffer_load_dwordx4 v[80:83], %[wa], %[rs], %[so] offen offset:0 sc1\n"
        "v_mfma_f32_16x16x32_bf16 v[152:155], v[44:47], v[16:19], v[152:155]\nds_read_b64_tr_b16 v[16:17], %[xa] offset:27456\ns_waitcnt lgkmcnt(9)\n"
        "v_mfma_f32_16x16x32_bf16 v[104:107], v[32:35], v[20:23], v[104:107]\nds_read_b64_tr_b16 v[18:19], %[xa] offset:27968\nv_mfma_f32_16x16x32_bf16 v[120:123], v[36:39], v[20:23], v[120:123]\n"
        "buffer_load_dwordx4 v[84:87], %[wa], %[rs], %[so] offen offset:1024 sc1\nv_mfma_f32_16x16x32_bf16 v[140:143], v[40:43], v[20:23], v[140:143]\n"
        "v_mfma_f32_16x16x32_bf16 v[156:159], v[44:47], v[20:23], v[156:159]\nds_read_b64_tr_b16 v[20:21], %[xa] offset:28512\ns_waitcnt lgkmcnt(9)\n"
        "v_mfma_f32_16x16x32_bf16 v[96:99], v[32:35], v[12:15], v[96:99]\nds_read_b64_tr_b16 v[22:23], %[xa] offset:29024\nv_mfma_f32_16x16x32_bf16 v[132:135], v[40:43], v[12:15], v[132:135]\n"
        "ds_read_b64_tr_b16 v[12:13], %[xa] offset:26400\ns_waitcnt lgkmcnt(9)\nv_mfma_f32_16x16x32_bf16 v[108:111], v[32:35], v[24:27], v[108:111]\nds_read_b64_tr_b16 v[14:15], %[xa] offset:26912\n"
        "v_mfma_f32_16x16x32_bf16 v[124:127], v[36:39], v[24:27], v[124:127]\nbuffer_load_dwordx4 v[88:91], %[wa], %[rs], %[so] offen offset:2048 sc1\n"
        "v_mfma_f32_16x16x32_bf16 v[144:147], v[40:43], v[24:27], v[144:147]\nbuffer_load_dwordx4 v[92:95], %[wa], %[rs], %[so] offen offset:3072 sc1\n"
        "v_mfma_f32_16x16x32_bf16 v[160:163], v[44:47], v[24:27], v[160:163]\nds_read_b64_tr_b16 v[24:25], %[xa] offset:29568\ns_waitcnt lgkmcnt(8)\n"
        "v_mfma_f32_16x16x32_bf16 v[112:115], v[32:35], v[28:31], v[112:115]\nds_read_b64_tr_b16 v[26:27], %[xa] offset:30080\nv_mfma_f32_16x16x32_bf16 v[128:131], v[36:39], v[28:31], v[128:131]\n"
        "v_mfma_f32_16x16x32_bf16 v[148:151], v[40:43], v[28:31], v[148:151]\ns_cmp_lg_u32 %[so], 291840\nv_mfma_f32_16x16x32_bf16 v[164:167], v[44:47], v[28:31], v[164:167]\n"
        "v_add_u32 %[xa], 21120, %[xa]\ns_cbranch_scc1 .LA1l%=\ns_waitcnt vmcnt(8) lgkmcnt(6)\nv_mfma_f32_16x16x32_bf16 v[100:103], v[48:51], v[16:19], v[100:103]\nv_readfirstlane_b32 %[s0], %[pv]\n"
        "ds_read_b64_tr_b16 v[28:29], %[xa] offset:9504\nv_mfma_f32_16x16x32_bf16 v[116:119], v[52:55], v[16:19], v[116:119]\ns_cmp_ge_i32 %[s0], 6\nds_read_b64_tr_b16 v[30:31], %[xa] offset:10016\n"
        "v_mfma_f32_16x16x32_bf16 v[136:139], v[56:59], v[16:19], v[136:139]\ns_cbranch_scc0 .LA1s2_%=\n.LA1r2_%=:\nv_mfma_f32_16x16x32_bf16 v[152:155], v[60:63], v[16:19], v[152:155]\n"
        "ds_read_b64_tr_b16 v[16:17], %[xa] offset:11616\ns_waitcnt lgkmcnt(7)\nv_mfma_f32_16x16x32_bf16 v[104:107], v[48:51], v[20:23], v[104:107]\nds_read_b64_tr_b16 v[18:19], %[xa] offset:12128\n"
        "v_mfma_f32_16x16x32_bf16 v[120:123], v[52:55], v[20:23], v[120:123]\nv_mfma_f32_16x16x32_bf16 v[140:143], v[56:59], v[20:23], v[140:143]\n"
        "v_mfma_f32_16x16x32_bf16 v[156:159], v[60:63], v[20:23], v[156:159]\nds_read_b64_tr_b16 v[20:21], %[xa] offset:12672\ns_waitcnt lgkmcnt(7)\n"
        "v_mfma_f32_16x16x32_bf16 v[96:99], v[48:51], v[12:15], v[96:99]\nds_read_b64_tr_b16 v[22:23], %[xa] offset:13184\nv_mfma_f32_16x16x32_bf16 v[132:135], v[56:59], v[12:15], v[132:135]\n"
        "ds_read_b64_tr_b16 v[12:13], %[xa] offset:10560\ns_waitcnt lgkmcnt(7)\nv_mfma_f32_16x16x32_bf16 v[108:111], v[48:51], v[24:27], v[108:111]\nds_read_b64_tr_b16 v[14:15], %[xa] offset:11072\n"
        "v_mfma_f32_16x16x32_bf16 v[124:127], v[52:55], v[24:27], v[124:127]\nv_mfma_f32_16x16x32_bf16 v[144:147], v[56:59], v[24:27], v[144:147]\n"
        "v_mfma_f32_16x16x32_bf16 v[160:163], v[60:63], v[24:27], v[160:163]\nds_read_b64_tr_b16 v[24:25], %[xa] offset:13728\ns_waitcnt lgkmcnt(7)\n"
        "v_mfma_f32_16x16x32_bf16 v[112:115], v[48:51], v[28:31], v[112:115]\nds_read_b64_tr_b16 v[26:27], %[xa] offset:14240\nv_mfma_f32_16x16x32_bf16 v[128:131], v[52:55], v[28:31], v[128:131]\n"
        "v_add_u32 %[sy], 4, %[sy]\nv_mfma_f32_16x16x32_bf16 v[148:151], v[56:59], v[28:31], v[148:151]\nv_mfma_f32_16x16x32_bf16 v[164:167], v[60:63], v[28:31], v[164:167]\n"
        "s_waitcnt vmcnt(4) lgkmcnt(6)\nv_mfma_f32_16x16x32_bf16 v[100:103], v[64:67], v[16:19], v[100:103]\nds_read_b64_tr_b16 v[28:29], %[xa] offset:14784\n"
        "v_mfma_f32_16x16x32_bf16 v[116:119], v[68:71], v[16:19], v[116:119]\nds_read_b64_tr_b16 v[30:31], %[xa] offset:15296\nv_mfma_f32_16x16x32_bf16 v[136:139], v[72:75], v[16:19], v[136:139]\n"
        "ds_read_b32 %[pv], %[sy] offset:4\nv_mfma_f32_16x16x32_bf16 v[152:155], v[76:79], v[16:19], v[152:155]\nds_read_b64_tr_b16 v[16:17], %[xa] offset:16896\ns_waitcnt lgkmcnt(8)\n"
        "v_mfma_f32_16x16x32_bf16 v[104:107], v[64:67], v[20:23], v[104:107]\nds_read_b64_tr_b16 v[18:19], %[xa] offset:17408\nv_mfma_f32_16x16x32_bf16 v[120:123], v[68:71], v[20:23], v[120:123]\n"
        "v_mfma_f32_16x16x32_bf16 v[140:143], v[72:75], v[20:23], v[140:143]\nv_mfma_f32_16x16x32_bf16 v[156:159], v[76:79], v[20:23], v[156:159]\nds_read_b64_tr_b16 v[20:21], %[xa] offset:17952\n"
        "s_waitcnt lgkmcnt(8)\nv_mfma_f32_16x16x32_bf16 v[96:99], v[64:67], v[12:15], v[96:99]\nds_read_b64_tr_b16 v[22:23], %[xa] offset:18464\n"
        "v_mfma_f32_16x16x32_bf16 v[132:135], v[72:75], v[12:15], v[132:135]\nds_read_b64_tr_b16 v[12:13], %[xa] offset:15840\ns_waitcnt lgkmcnt(8)\n"
        "v_mfma_f32_16x16x32_bf16 v[108:111], v[64:67], v[24:27], v[108:111]\nds_read_b64_tr_b16 v[14:15], %[xa] offset:16352\nv_mfma_f32_16x16x32_bf16 v[124:127], v[68:71], v[24:27], v[124:127]\n"
        "v_mfma_f32_16x16x32_bf16 v[144:147], v[72:75], v[24:27], v[144:147]\nv_mfma_f32_16x16x32_bf16 v[160:163], v[76:79], v[24:27], v[160:163]\nds_read_b64_tr_b16 v[24:25], %[xa] offset:19008\n"
        "s_waitcnt lgkmcnt(8)\nv_mfma_f32_16x16x32_bf16 v[112:115], v[64:67], v[28:31], v[112:115]\nds_read_b64_tr_b16 v[26:27], %[xa] offset:19520\n"
        "v_mfma_f32_16x16x32_bf16 v[128:131], v[68:71], v[28:31], v[128:131]\nv_mfma_f32_16x16x32_bf16 v[148:151], v[72:75], v[28:31], v[148:151]\n"
        "v_mfma_f32_16x16x32_bf16 v[164:167], v[76:79], v[28:31], v[164:167]\ns_waitcnt vmcnt(0) lgkmcnt(6)\nv_mfma_f32_16x16x32_bf16 v[100:103], v[80:83], v[16:19], v[100:103]\n"
        "v_readfirstlane_b32 %[s0], %[pv]\nds_read_b64_tr_b16 v[28:29], %[xa] offset:20064\nv_mfma_f32_16x16x32_bf16 v[116:119], v[84:87], v[16:19], v[116:119]\n"
        "ds_read_b64_tr_b16 v[30:31], %[xa] offset:20576\nv_mfma_f32_16x16x32_bf16 v[136:139], v[88:91], v[16:19], v[136:139]\ns_cmp_ge_i32 %[s0], %[nst]\n"
        "v_mfma_f32_16x16x32_bf16 v[152:155], v[92:95], v[16:19], v[152:155]\ns_cbranch_scc0 .LA1t%=\n.LA1u%=:\ns_waitcnt lgkmcnt(6)\n"
        "v_mfma_f32_16x16x32_bf16 v[104:107], v[80:83], v[20:23], v[104:107]\nv_add_u32 %[sy], 4, %[sy]\nds_read_b128 v[16:19], %[mra] offset:256\n"
        "v_mfma_f32_16x16x32_bf16 v[120:123], v[84:87], v[20:23], v[120:123]\nv_mfma_f32_16x16x32_bf16 v[140:143], v[88:91], v[20:23], v[140:143]\n"
        "v_mfma_f32_16x16x32_bf16 v[156:159], v[92:95], v[20:23], v[156:159]\nds_read_b128 v[32:35], %[cwa] offset:0\ns_waitcnt lgkmcnt(6)\n"
        "v_mfma_f32_16x16x32_bf16 v[96:99], v[80:83], v[12:15], v[96:99]\nds_read_b128 v[36:39], %[cwa] offset:320\nds_read_b128 v[20:23], %[mra] offset:512\n"
        "v_mfma_f32_16x16x32_bf16 v[132:135], v[88:91], v[12:15], v[132:135]\nds_read_b128 v[40:43], %[cwa] offset:640\ns_waitcnt lgkmcnt(7)\n"
        "v_mfma_f32_16x16x32_bf16 v[108:111], v[80:83], v[24:27], v[108:111]\nds_read_b128 v[44:47], %[cwa] offset:512\nds_read_b128 v[12:15], %[mra] offset:0\n"
        "v_mfma_f32_16x16x32_bf16 v[124:127], v[84:87], v[24:27], v[124:127]\nv_add_u32 %[sy], 4, %[sy]\nv_mfma_f32_16x16x32_bf16 v[144:147], v[88:91], v[24:27], v[144:147]\n"
        "v_mfma_f32_16x16x32_bf16 v[160:163], v[92:95], v[24:27], v[160:163]\nds_read_b128 v[24:27], %[mra] offset:768\ns_waitcnt lgkmcnt(8)\n"
        "v_mfma_f32_16x16x32_bf16 v[112:115], v[80:83], v[28:31], v[112:115]\nv_mfma_f32_16x16x32_bf16 v[128:131], v[84:87], v[28:31], v[128:131]\n"
        "v_mfma_f32_16x16x32_bf16 v[148:151], v[88:91], v[28:31], v[148:151]\nv_mfma_f32_16x16x32_bf16 v[164:167], v[92:95], v[28:31], v[164:167]\nds_read_b128 v[28:31], %[mra] offset:1024\n"
        "s_setprio %[pfs]\ns_waitcnt lgkmcnt(1)\n.long 0xd3b04064, 0x1d922120\n.long 0xd3b04066, 0x1d9a2122\n.long 0xd3b04074, 0x1dd22124\n.long 0xd3b04076, 0x1dda2126\n.long 0xd3b14064, 0x18022564\n"
        ".long 0xd3b14066, 0x18022566\n.long 0xd3b04068, 0x1da22920\n.long 0xd3b0406a, 0x1daa2922\n.long 0xd3b14074, 0x18022574\n.long 0xd3b14076, 0x18022576\nv_cvt_pk_bf16_f32 v100, v100, v101\n"
        "v_cvt_pk_bf16_f32 v101, v102, v103\n.long 0xd3b04078, 0x1de22924\n.long 0xd3b0407a, 0x1dea2926\n.long 0xd3b14068, 0x18022d68\n.long 0xd3b1406a, 0x18022d6a\nv_cvt_pk_bf16_f32 v116, v116, v117\n"
        "v_cvt_pk_bf16_f32 v117, v118, v119\nds_write_b64 %[sta], v[100:101] offset:7936\n.long 0xd3b04060, 0x1d821920\n.long 0xd3b04062, 0x1d8a1922\n.long 0xd3b14078, 0x18022d78\n"
        ".long 0xd3b1407a, 0x18022d7a\nv_cvt_pk_bf16_f32 v104, v104, v105\nv_cvt_pk_bf16_f32 v105, v106, v107\nds_write_b64 %[sta], v[116:117] offset:8096\n.long 0xd3b0406c, 0x1db23120\n"
        ".long 0xd3b0406e, 0x1dba3122\n.long 0xd3b14060, 0x18021d60\n.long 0xd3b14062, 0x18021d62\nv_cvt_pk_bf16_f32 v120, v120, v121\nv_cvt_pk_bf16_f32 v121, v122, v123\n"
        "ds_write_b64 %[sta], v[104:105] offset:15872\n.long 0xd3b0407c, 0x1df23124\n.long 0xd3b0407e, 0x1dfa3126\n.long 0xd3b1406c, 0x1802356c\n.long 0xd3b1406e, 0x1802356e\n"
        "v_cvt_pk_bf16_f32 v96, v96, v97\nv_cvt_pk_bf16_f32 v97, v98, v99\nds_write_b64 %[sta], v[120:121] offset:16032\ns_waitcnt lgkmcnt(4)\n.long 0xd3b04070, 0x1dc23920\n"
        ".long 0xd3b04072, 0x1dca3922\n.long 0xd3b1407c, 0x1802357c\n.long 0xd3b1407e, 0x1802357e\nv_cvt_pk_bf16_f32 v108, v108, v109\nv_cvt_pk_bf16_f32 v109, v110, v111\n"
        "ds_write_b64 %[sta], v[96:97] offset:0\n.long 0xd3b04080, 0x1e023924\n.long 0xd3b04082, 0x1e0a3926\n.long 0xd3b14070, 0x18023d70\n.long 0xd3b14072, 0x18023d72\n"
        "v_cvt_pk_bf16_f32 v124, v124, v125\nv_cvt_pk_bf16_f32 v125, v126, v127\nds_write_b64 %[sta], v[108:109] offset:23808\n.long 0xd3b14080, 0x18023d80\n.long 0xd3b14082, 0x18023d82\n"
        "v_cvt_pk_bf16_f32 v112, v112, v113\nv_cvt_pk_bf16_f32 v113, v114, v115\nds_write_b64 %[sta], v[124:125] offset:23968\nv_cvt_pk_bf16_f32 v128, v128, v129\nv_cvt_pk_bf16_f32 v129, v130, v131\n"
        "ds_write_b64 %[sta], v[112:113] offset:31744\nds_write_b64 %[sta], v[128:129] offset:31904\nds_write_b32 %[pg], %[sy]\nv_add_u32 %[sy], 4, %[sy]\n.long 0xd3b04088, 0x1e222128\n"
        ".long 0xd3b0408a, 0x1e2a212a\n.long 0xd3b04098, 0x1e62212c\n.long 0xd3b0409a, 0x1e6a212e\n.long 0xd3b14088, 0x18022588\n.long 0xd3b1408a, 0x1802258a\n.long 0xd3b0408c, 0x1e322928\n"
        ".long 0xd3b0408e, 0x1e3a292a\n.long 0xd3b14098, 0x18022598\n.long 0xd3b1409a, 0x1802259a\nv_cvt_pk_bf16_f32 v136, v136, v137\nv_cvt_pk_bf16_f32 v137, v138, v139\n.long 0xd3b0409c, 0x1e72292c\n"
        ".long 0xd3b0409e, 0x1e7a292e\n.long 0xd3b1408c, 0x18022d8c\n.long 0xd3b1408e, 0x18022d8e\nv_cvt_pk_bf16_f32 v152, v152, v153\nv_cvt_pk_bf16_f32 v153, v154, v155\n"
        "ds_write_b64 %[sta], v[136:137] offset:8256\n.long 0xd3b04084, 0x1e121928\n.long 0xd3b04086, 0x1e1a192a\n.long 0xd3b1409c, 0x18022d9c\n.long 0xd3b1409e, 0x18022d9e\n"
        "v_cvt_pk_bf16_f32 v140, v140, v141\nv_cvt_pk_bf16_f32 v141, v142, v143\nds_write_b64 %[sta], v[152:153] offset:8192\n.long 0xd3b04090, 0x1e423128\n.long 0xd3b04092, 0x1e4a312a\n"
        ".long 0xd3b14084, 0x18021d84\n.long 0xd3b14086, 0x18021d86\nv_cvt_pk_bf16_f32 v156, v156, v157\nv_cvt_pk_bf16_f32 v157, v158, v159\nds_write_b64 %[sta], v[140:141] offset:16192\n"
        ".long 0xd3b040a0, 0x1e82312c\n.long 0xd3b040a2, 0x1e8a312e\n.long 0xd3b14090, 0x18023590\n.long 0xd3b14092, 0x18023592\nv_cvt_pk_bf16_f32 v132, v132, v133\nv_cvt_pk_bf16_f32 v133, v134, v135\n"
        "ds_write_b64 %[sta], v[156:157] offset:16128\n.long 0xd3b04094, 0x1e523928\n.long 0xd3b04096, 0x1e5a392a\n.long 0xd3b140a0, 0x180235a0\n.long 0xd3b140a2, 0x180235a2\n"
        "v_cvt_pk_bf16_f32 v144, v144, v145\nv_cvt_pk_bf16_f32 v145, v146, v147\nds_write_b64 %[sta], v[132:133] offset:320\n.long 0xd3b040a4, 0x1e92392c\n.long 0xd3b040a6, 0x1e9a392e\n"
        ".long 0xd3b14094, 0x18023d94\n.long 0xd3b14096, 0x18023d96\nv_cvt_pk_bf16_f32 v160, v160, v161\nv_cvt_pk_bf16_f32 v161, v162, v163\nds_write_b64 %[sta], v[144:145] offset:24128\n"
        ".long 0xd3b140a4, 0x18023da4\n.long 0xd3b140a6, 0x18023da6\nv_cvt_pk_bf16_f32 v148, v148, v149\nv_cvt_pk_bf16_f32 v149, v150, v151\nds_write_b64 %[sta], v[160:161] offset:24064\n"
        "v_cvt_pk_bf16_f32 v164, v164, v165\nv_cvt_pk_bf16_f32 v165, v166, v167\nds_write_b64 %[sta], v[148:149] offset:32064\nds_write_b64 %[sta], v[164:165] offset:32000\nds_write_b32 %[pg], %[sy]\n"
        "s_branch .LE%=\n.LA1s0_%=:\ns_mov_b32 %[s1], 0x100000\n.LA1p0_%=:\nds_read_b32 %[pv], %[sy] offset:4\ns_waitcnt lgkmcnt(0)\nv_readfirstlane_b32 %[s0], %[pv]\ns_nop 0\ns_cmp_ge_i32 %[s0], 6\n"
        "s_cbranch_scc1 .LA1r0_%=\ns_sleep 1\ns_sub_u32 %[s1], %[s1], 1\ns_cbranch_scc0 .LA1p0_%=\ns_branch .LA1r0_%=\n.LA1s1_%=:\ns_mov_b32 %[s1], 0x100000\n.LA1p1_%=:\n"
        "ds_read_b32 %[pv], %[sy] offset:4\ns_waitcnt lgkmcnt(0)\nv_readfirstlane_b32 %[s0], %[pv]\ns_nop 0\ns_cmp_ge_i32 %[s0], 6\ns_cbranch_scc1 .LA1r1_%=\ns_sleep 1\ns_sub_u32 %[s1], %[s1], 1\n"
        "s_cbranch_scc0 .LA1p1_%=\ns_branch .LA1r1_%=\n.LA1s2_%=:\ns_mov_b32 %[s1], 0x100000\n.LA1p2_%=:\nds_read_b32 %[pv], %[sy] offset:4\ns_waitcnt lgkmcnt(0)\nv_readfirstlane_b32 %[s0], %[pv]\n"
        "s_nop 0\ns_cmp_ge_i32 %[s0], 6\ns_cbranch_scc1 .LA1r2_%=\ns_sleep 1\ns_sub_u32 %[s1], %[s1], 1\ns_cbranch_scc0 .LA1p2_%=\ns_branch .LA1r2_%=\n.LA1t%=:\ns_mov_b32 %[s1], 0x100000\n.LA1q%=:\n"
        "ds_read_b32 %[pv], %[sy] offset:4\ns_waitcnt lgkmcnt(0)\nv_readfirstlane_b32 %[s0], %[pv]\ns_nop 0\ns_cmp_ge_i32 %[s0], %[nst]\ns_cbranch_scc1 .LA1u%=\ns_sleep 1\ns_sub_u32 %[s1], %[s1], 1\n"
        "s_cbranch_scc0 .LA1q%=\ns_branch .LA1u%=\n.LB%=:\ns_cmp_eq_u32 %[w], 2\ns_cbranch_scc0 .LC%=\ns_mov_b32 %[so], 0\nbuffer_load_dwordx4 v[32:35], %[wa], %[rs], %[so] offen offset:3072 sc1\n"
        "buffer_load_dwordx4 v[36:39], %[wa], %[rs], %[so] offen offset:0 sc1\nbuffer_load_dwordx4 v[40:43], %[wa], %[rs], %[so] offen offset:1024 sc1\n"
        "buffer_load_dwordx4 v[44:47], %[wa], %[rs], %[so] offen offset:2048 sc1\ns_mov_b32 %[so], 15360\nbuffer_load_dwordx4 v[48:51], %[wa], %[rs], %[so] offen offset:3072 sc1\n"
        "buffer_load_dwordx4 v[52:55], %[wa], %[rs], %[so] offen offset:0 sc1\nbuffer_load_dwordx4 v[56:59], %[wa], %[rs], %[so] offen offset:1024 sc1\n"
        "buffer_load_dwordx4 v[60:63], %[wa], %[rs], %[so] offen offset:2048 sc1\ns_mov_b32 %[so], 30720\nbuffer_load_dwordx4 v[64:67], %[wa], %[rs], %[so] offen offset:3072 sc1\n"
        "buffer_load_dwordx4 v[68:71], %[wa], %[rs], %[so] offen offset:0 sc1\nbuffer_load_dwordx4 v[72:75], %[wa], %[rs], %[so] offen offset:1024 sc1\n"
        "buffer_load_dwordx4 v[76:79], %[wa], %[rs], %[so] offen offset:2048 sc1\ns_mov_b32 %[so], 46080\nbuffer_load_dwordx4 v[80:83], %[wa], %[rs], %[so] offen offset:3072 sc1\n"
        "buffer_load_dwordx4 v[84:87], %[wa], %[rs], %[so] offen offset:0 sc1\nbuffer_load_dwordx4 v[88:91], %[wa], %[rs], %[so] offen offset:1024 sc1\n"
        "buffer_load_dwordx4 v[92:95], %[wa], %[rs], %[so] offen offset:2048 sc1\ns_barrier\ns_mov_b32 %[s1], 0x100000\n.LB1w%=:\nds_read_b32 %[pv], %[sy] offset:0\ns_waitcnt lgkmcnt(0)\n"
        "v_readfirstlane_b32 %[s0], %[pv]\ns_nop 0\ns_cmp_ge_i32 %[s0], 6\ns_cbranch_scc1 .LB1g%=\ns_sleep 1\ns_sub_u32 %[s1], %[s1], 1\ns_cbranch_scc0 .LB1w%=\n.LB1g%=:\n"
        "ds_read_b64_tr_b16 v[16:17], %[xa] offset:1056\nds_read_b64_tr_b16 v[18:19], %[xa] offset:1568\nds_read_b64_tr_b16 v[20:21], %[xa] offset:2112\nds_read_b64_tr_b16 v[22:23], %[xa] offset:2624\n"
        "ds_read_b64_tr_b16 v[12:13], %[xa] offset:0\nds_read_b64_tr_b16 v[14:15], %[xa] offset:512\nds_read_b64_tr_b16 v[24:25], %[xa] offset:3168\nds_read_b64_tr_b16 v[26:27], %[xa] offset:3680\n"
        "s_waitcnt vmcnt(12) lgkmcnt(6)\nv_mfma_f32_16x16x32_bf16 v[96:99], v[32:35], v[16:19], 0\nds_read_b64_tr_b16 v[28:29], %[xa] offset:4224\n"
        "v_mfma_f32_16x16x32_bf16 v[108:111], v[36:39], v[16:19], 0\nds_read_b64_tr_b16 v[30:31], %[xa] offset:4736\nv_mfma_f32_16x16x32_bf16 v[128:131], v[40:43], v[16:19], 0\n"
        "ds_read_b32 %[pv], %[sy] offset:4\nv_mfma_f32_16x16x32_bf16 v[148:151], v[44:47], v[16:19], 0\nds_read_b64_tr_b16 v[16:17], %[xa] offset:6336\ns_waitcnt lgkmcnt(8)\n"
        "v_mfma_f32_16x16x32_bf16 v[100:103], v[32:35], v[20:23], 0\nds_read_b64_tr_b16 v[18:19], %[xa] offset:6848\nv_mfma_f32_16x16x32_bf16 v[112:115], v[36:39], v[20:23], 0\n"
        "v_mfma_f32_16x16x32_bf16 v[132:135], v[40:43], v[20:23], 0\nv_mfma_f32_16x16x32_bf16 v[152:155], v[44:47], v[20:23], 0\nds_read_b64_tr_b16 v[20:21], %[xa] offset:7392\ns_waitcnt lgkmcnt(8)\n"
        "v_mfma_f32_16x16x32_bf16 v[104:107], v[36:39], v[12:15], 0\nds_read_b64_tr_b16 v[22:23], %[xa] offset:7904\nv_mfma_f32_16x16x32_bf16 v[124:127], v[40:43], v[12:15], 0\n"
        "v_mfma_f32_16x16x32_bf16 v[144:147], v[44:47], v[12:15], 0\nds_read_b64_tr_b16 v[12:13], %[xa] offset:5280\ns_waitcnt lgkmcnt(8)\nv_mfma_f32_16x16x32_bf16 v[116:119], v[36:39], v[24:27], 0\n"
        "ds_read_b64_tr_b16 v[14:15], %[xa] offset:5792\nv_mfma_f32_16x16x32_bf16 v[136:139], v[40:43], v[24:27], 0\nv_mfma_f32_16x16x32_bf16 v[156:159], v[44:47], v[24:27], 0\n"
        "ds_read_b64_tr_b16 v[24:25], %[xa] offset:8448\ns_waitcnt lgkmcnt(8)\nv_mfma_f32_16x16x32_bf16 v[120:123], v[36:39], v[28:31], 0\nds_read_b64_tr_b16 v[26:27], %[xa] offset:8960\n"
        "v_mfma_f32_16x16x32_bf16 v[140:143], v[40:43], v[28:31], 0\nv_mfma_f32_16x16x32_bf16 v[160:163], v[44:47], v[28:31], 0\n.LB1l%=:\ns_waitcnt vmcnt(8) lgkmcnt(6)\n"
        "v_mfma_f32_16x16x32_bf16 v[96:99], v[48:51], v[16:19], v[96:99]\ns_addk_i32 %[so], 15360\nv_readfirstlane_b32 %[s0], %[pv]\n"
        "v_mfma_f32_16x16x32_bf16 v[108:111], v[52:55], v[16:19], v[108:111]\ns_cmp_ge_i32 %[s0], 6\nds_read_b64_tr_b16 v[28:29], %[xa] offset:9504\n"
        "v_mfma_f32_16x16x32_bf16 v[128:131], v[56:59], v[16:19], v[128:131]\ns_cbranch_scc0 .LB1s0_%=\n.LB1r0_%=:\nds_read_b64_tr_b16 v[30:31], %[xa] offset:10016\n"
        "v_mfma_f32_16x16x32_bf16 v[148:151], v[60:63], v[16:19], v[148:151]\nds_read_b64_tr_b16 v[16:17], %[xa] offset:11616\ns_waitcnt lgkmcnt(7)\n"
        "v_mfma_f32_16x16x32_bf16 v[100:103], v[48:51], v[20:23], v[100:103]\nds_read_b64_tr_b16 v[18:19], %[xa] offset:12128\nbuffer_load_dwordx4 v[32:35], %[wa], %[rs], %[so] offen offset:3072 sc1\n"
        "v_mfma_f32_16x16x32_bf16 v[112:115], v[52:55], v[20:23], v[112:115]\nbuffer_load_dwordx4 v[36:39], %[wa], %[rs], %[so] offen offset:0 sc1\n"
        "v_mfma_f32_16x16x32_bf16 v[132:135], v[56:59], v[20:23], v[132:135]\nv_mfma_f32_16x16x32_bf16 v[152:155], v[60:63], v[20:23], v[152:155]\nds_read_b64_tr_b16 v[20:21], %[xa] offset:12672\n"
        "s_waitcnt lgkmcnt(7)\nv_mfma_f32_16x16x32_bf16 v[104:107], v[52:55], v[12:15], v[104:107]\nds_read_b64_tr_b16 v[22:23], %[xa] offset:13184\n"
        "v_mfma_f32_16x16x32_bf16 v[124:127], v[56:59], v[12:15], v[124:127]\nbuffer_load_dwordx4 v[40:43], %[wa], %[rs], %[so] offen offset:1024 sc1\n"
        "v_mfma_f32_16x16x32_bf16 v[144:147], v[60:63], v[12:15], v[144:147]\nds_read_b64_tr_b16 v[12:13], %[xa] offset:10560\ns_waitcnt lgkmcnt(7)\n"
        "v_mfma_f32_16x16x32_bf16 v[116:119], v[52:55], v[24:27], v[116:119]\nds_read_b64_tr_b16 v[14:15], %[xa] offset:11072\nv_mfma_f32_16x16x32_bf16 v[136:139], v[56:59], v[24:27], v[136:139]\n"
        "buffer_load_dwordx4 v[44:47], %[wa], %[rs], %[so] offen offset:2048 sc1\nv_mfma_f32_16x16x32_bf16 v[156:159], v[60:63], v[24:27], v[156:159]\nds_read_b64_tr_b16 v[24:25], %[xa] offset:13728\n"
        "s_waitcnt lgkmcnt(7)\nv_mfma_f32_16x16x32_bf16 v[120:123], v[52:55], v[28:31], v[120:123]\nv_add_u32 %[sy], 4, %[sy]\nds_read_b64_tr_b16 v[26:27], %[xa] offset:14240\n"
        "v_mfma_f32_16x16x32_bf16 v[140:143], v[56:59], v[28:31], v[140:143]\nds_write_b32 %[pg], %[sy]\nv_mfma_f32_16x16x32_bf16 v[160:163], v[60:63], v[28:31], v[160:163]\n"
        "s_waitcnt vmcnt(8) lgkmcnt(7)\nv_mfma_f32_16x16x32_bf16 v[96:99], v[64:67], v[16:19], v[96:99]\ns_addk_i32 %[so], 15360\nds_read_b64_tr_b16 v[28:29], %[xa] offset:14784\n"
        "v_mfma_f32_16x16x32_bf16 v[108:111], v[68:71], v[16:19], v[108:111]\nds_read_b64_tr_b16 v[30:31], %[xa] offset:15296\nv_mfma_f32_16x16x32_bf16 v[128:131], v[72:75], v[16:19], v[128:131]\n"
        "ds_read_b32 %[pv], %[sy] offset:4\nbuffer_load_dwordx4 v[48:51], %[wa], %[rs], %[so] offen offset:3072 sc1\nv_mfma_f32_16x16x32_bf16 v[148:151], v[76:79], v[16:19], v[148:151]\n"
        "ds_read_b64_tr_b16 v[16:17], %[xa] offset:16896\ns_waitcnt lgkmcnt(9)\nv_mfma_f32_16x16x32_bf16 v[100:103], v[64:67], v[20:23], v[100:103]\nds_read_b64_tr_b16 v[18:19], %[xa] offset:17408\n"
        "v_mfma_f32_16x16x32_bf16 v[112:115], v[68:71], v[20:23], v[112:115]\nbuffer_load_dwordx4 v[52:55], %[wa], %[rs], %[so] offen offset:0 sc1\n"
        "v_mfma_f32_16x16x32_bf16 v[132:135], v[72:75], v[20:23], v[132:135]\nv_mfma_f32_16x16x32_bf16 v[152:155], v[76:79], v[20:23], v[152:155]\nds_read_b64_tr_b16 v[20:21], %[xa] offset:17952\n"
        "s_waitcnt lgkmcnt(9)\nv_mfma_f32_16x16x32_bf16 v[104:107], v[68:71], v[12:15], v[104:107]\nds_read_b64_tr_b16 v[22:23], %[xa] offset:18464\n"
        "v_mfma_f32_16x16x32_bf16 v[124:127], v[72:75], v[12:15], v[124:127]\nbuffer_load_dwordx4 v[56:59], %[wa], %[rs], %[so] offen offset:1024 sc1\n"
        "v_mfma_f32_16x16x32_bf16 v[144:147], v[76:79], v[12:15], v[144:147]\nds_read_b64_tr_b16 v[12:13], %[xa] offset:15840\ns_waitcnt lgkmcnt(9)\n"
        "v_mfma_f32_16x16x32_bf16 v[116:119], v[68:71], v[24:27], v[116:119]\nds_read_b64_tr_b16 v[14:15], %[xa] offset:16352\nv_mfma_f32_16x16x32_bf16 v[136:139], v[72:75], v[24:27], v[136:139]\n"
        "buffer_load_dwordx4 v[60:63], %[wa], %[rs], %[so] offen offset:2048 sc1\nv_mfma_f32_16x16x32_bf16 v[156:159], v[76:79], v[24:27], v[156:159]\nds_read_b64_tr_b16 v[24:25], %[xa] offset:19008\n"
        "s_waitcnt lgkmcnt(8)\nv_mfma_f32_16x16x32_bf16 v[120:123], v[68:71], v[28:31], v[120:123]\nds_read_b64_tr_b16 v[26:27], %[xa] offset:19520\n"
        "v_mfma_f32_16x16x32_bf16 v[140:143], v[72:75], v[28:31], v[140:143]\nv_mfma_f32_16x16x32_bf16 v[160:163], v[76:79], v[28:31], v[160:163]\ns_waitcnt vmcnt(8) lgkmcnt(6)\n"
        "v_mfma_f32_16x16x32_bf16 v[96:99], v[80:83], v[16:19], v[96:99]\ns_addk_i32 %[so], 15360\nv_readfirstlane_b32 %[s0], %[pv]\n"
        "v_mfma_f32_16x16x32_bf16 v[108:111], v[84:87], v[16:19], v[108:111]\ns_cmp_ge_i32 %[s0], 6\nds_read_b64_tr_b16 v[28:29], %[xa] offset:20064\n"
        "v_mfma_f32_16x16x32_bf16 v[128:131], v[88:91], v[16:19], v[128:131]\ns_cbranch_scc0 .LB1s1_%=\n.LB1r1_%=:\nds_read_b64_tr_b16 v[30:31], %[xa] offset:20576\n"
        "v_mfma_f32_16x16x32_bf16 v[148:151], v[92:95], v[16:19], v[148:151]\nds_read_b64_tr_b16 v[16:17], %[xa] offset:22176\ns_waitcnt lgkmcnt(7)\n"
        "v_mfma_f32_16x16x32_bf16 v[100:103], v[80:83], v[20:23], v[100:103]\nds_read_b64_tr_b16 v[18:19], %[xa] offset:22688\nbuffer_load_dwordx4 v[64:67], %[wa], %[rs], %[so] offen offset:3072 sc1\n"
        "v_mfma_f32_16x16x32_bf16 v[112:115], v[84:87], v[20:23], v[112:115]\nbuffer_load_dwordx4 v[68:71], %[wa], %[rs], %[so] offen offset:0 sc1\n"
        "v_mfma_f32_16x16x32_bf16 v[132:135], v[88:91], v[20:23], v[132:135]\nv_mfma_f32_16x16x32_bf16 v[152:155], v[92:95], v[20:23], v[152:155]\nds_read_b64_tr_b16 v[20:21], %[xa] offset:23232\n"
        "s_waitcnt lgkmcnt(7)\nv_mfma_f32_16x16x32_bf16 v[104:107], v[84:87], v[12:15], v[104:107]\nds_read_b64_tr_b16 v[22:23], %[xa] offset:23744\n"
        "v_mfma_f32_16x16x32_bf16 v[124:127], v[88:91], v[12:15], v[124:127]\nbuffer_load_dwordx4 v[72:75], %[wa], %[rs], %[so] offen offset:1024 sc1\n"
        "v_mfma_f32_16x16x32_bf16 v[144:147], v[92:95], v[12:15], v[144:147]\nds_read_b64_tr_b16 v[12:13], %[xa] offset:21120\ns_waitcnt lgkmcnt(7)\n"
        "v_mfma_f32_16x16x32_bf16 v[116:119], v[84:87], v[24:27], v[116:119]\nds_read_b64_tr_b16 v[14:15], %[xa] offset:21632\nv_mfma_f32_16x16x32_bf16 v[136:139], v[88:91], v[24:27], v[136:139]\n"
        "buffer_load_dwordx4 v[76:79], %[wa], %[rs], %[so] offen offset:2048 sc1\nv_mfma_f32_16x16x32_bf16 v[156:159], v[92:95], v[24:27], v[156:159]\nds_read_b64_tr_b16 v[24:25], %[xa] offset:24288\n"
        "s_waitcnt lgkmcnt(7)\nv_mfma_f32_16x16x32_bf16 v[120:123], v[84:87], v[28:31], v[120:123]\nv_add_u32 %[sy], 4, %[sy]\nds_read_b64_tr_b16 v[26:27], %[xa] offset:24800\n"
        "v_mfma_f32_16x16x32_bf16 v[140:143], v[88:91], v[28:31], v[140:143]\nds_write_b32 %[pg], %[sy]\nv_mfma_f32_16x16x32_bf16 v[160:163], v[92:95], v[28:31], v[160:163]\n"
        "s_waitcnt vmcnt(8) lgkmcnt(7)\nv_mfma_f32_16x16x32_bf16 v[96:99], v[32:35], v[16:19], v[96:99]\ns_addk_i32 %[so], 15360\nds_read_b64_tr_b16 v[28:29], %[xa] offset:25344\n"
        "v_mfma_f32_16x16x32_bf16 v[108:111], v[36:39], v[16:19], v[108:111]\nds_read_b64_tr_b16 v[30:31], %[xa] offset:25856\nv_mfma_f32_16x16x32_bf16 v[128:131], v[40:43], v[16:19], v[128:131]\n"
        "ds_read_b32 %[pv], %[sy] offset:4\nbuffer_load_dwordx4 v[80:83], %[wa], %[rs], %[so] offen offset:3072 sc1\nv_mfma_f32_16x16x32_bf16 v[148:151], v[44:47], v[16:19], v[148:151]\n"
        "ds_read_b64_tr_b16 v[16:17], %[xa] offset:27456\ns_waitcnt lgkmcnt(9)\nv_mfma_f32_16x16x32_bf16 v[100:103], v[32:35], v[20:23], v[100:103]\nds_read_b64_tr_b16 v[18:19], %[xa] offset:27968\n"
        "v_mfma_f32_16x16x32_bf16 v[112:115], v[36:39], v[20:23], v[112:115]\nbuffer_load_dwordx4 v[84:87], %[wa], %[rs], %[so] offen offset:0 sc1\n"
        "v_mfma_f32_16x16x32_bf16 v[132:135], v[40:43], v[20:23], v[132:135]\nv_mfma_f32_16x16x32_bf16 v[152:155], v[44:47], v[20:23], v[152:155]\nds_read_b64_tr_b16 v[20:21], %[xa] offset:28512\n"
        "s_waitcnt lgkmcnt(9)\nv_mfma_f32_16x16x32_bf16 v[104:107], v[36:39], v[12:15], v[104:107]\nds_read_b64_tr_b16 v[22:23], %[xa] offset:29024\n"
        "v_mfma_f32_16x16x32_bf16 v[124:127], v[40:43], v[12:15], v[124:127]\nbuffer_load_dwordx4 v[88:91], %[wa], %[rs], %[so] offen offset:1024 sc1\n"
        "v_mfma_f32_16x16x32_bf16 v[144:147], v[44:47], v[12:15], v[144:147]\nds_read_b64_tr_b16 v[12:13], %[xa] offset:26400\ns_waitcnt lgkmcnt(9)\n"
        "v_mfma_f32_16x16x32_bf16 v[116:119], v[36:39], v[24:27], v[116:119]\nds_read_b64_tr_b16 v[14:15], %[xa] offset:26912\nv_mfma_f32_16x16x32_bf16 v[136:139], v[40:43], v[24:27], v[136:139]\n"
        "buffer_load_dwordx4 v[92:95], %[wa], %[rs], %[so] offen offset:2048 sc1\nv_mfma_f32_16x16x32_bf16 v[156:159], v[44:47], v[24:27], v[156:159]\nds_read_b64_tr_b16 v[24:25], %[xa] offset:29568\n"
        "s_waitcnt lgkmcnt(8)\nv_mfma_f32_16x16x32_bf16 v[120:123], v[36:39], v[28:31], v[120:123]\nds_read_b64_tr_b16 v[26:27], %[xa] offset:30080\n"
        "v_mfma_f32_16x16x32_bf16 v[140:143], v[40:43], v[28:31], v[140:143]\ns_cmp_lg_u32 %[so], 291840\nv_mfma_f32_16x16x32_bf16 v[160:163], v[44:47], v[28:31], v[160:163]\n"
        "v_add_u32 %[xa], 21120, %[xa]\ns_cbranch_scc1 .LB1l%=\ns_waitcnt vmcnt(8) lgkmcnt(6)\nv_mfma_f32_16x16x32_bf16 v[96:99], v[48:51], v[16:19], v[96:99]\nv_readfirstlane_b32 %[s0], %[pv]\n"
        "ds_read_b64_tr_b16 v[28:29], %[xa] offset:9504\nv_mfma_f32_16x16x32_bf16 v[108:111], v[52:55], v[16:19], v[108:111]\ns_cmp_ge_i32 %[s0], 6\nds_read_b64_tr_b16 v[30:31], %[xa] offset:10016\n"
        "v_mfma_f32_16x16x32_bf16 v[128:131], v[56:59], v[16:19], v[128:131]\ns_cbranch_scc0 .LB1s2_%=\n.LB1r2_%=:\nv_mfma_f32_16x16x32_bf16 v[148:151], v[60:63], v[16:19], v[148:151]\n"
        "ds_read_b64_tr_b16 v[16:17], %[xa] offset:11616\ns_waitcnt lgkmcnt(7)\nv_mfma_f32_16x16x32_bf16 v[100:103], v[48:51], v[20:23], v[100:103]\nds_read_b64_tr_b16 v[18:19], %[xa] offset:12128\n"
        "v_mfma_f32_16x16x32_bf16 v[112:115], v[52:55], v[20:23], v[112:115]\nv_mfma_f32_16x16x32_bf16 v[132:135], v[56:59], v[20:23], v[132:135]\n"
        "v_mfma_f32_16x16x32_bf16 v[152:155], v[60:63], v[20:23], v[152:155]\nds_read_b64_tr_b16 v[20:21], %[xa] offset:12672\ns_waitcnt lgkmcnt(7)\n"
        "v_mfma_f32_16x16x32_bf16 v[104:107], v[52:55], v[12:15], v[104:107]\nds_read_b64_tr_b16 v[22:23], %[xa] offset:13184\nv_mfma_f32_16x16x32_bf16 v[124:127], v[56:59], v[12:15], v[124:127]\n"
        "v_mfma_f32_16x16x32_bf16 v[144:147], v[60:63], v[12:15], v[144:147]\nds_read_b64_tr_b16 v[12:13], %[xa] offset:10560\ns_waitcnt lgkmcnt(7)\n"
        "v_mfma_f32_16x16x32_bf16 v[116:119], v[52:55], v[24:27], v[116:119]\nds_read_b64_tr_b16 v[14:15], %[xa] offset:11072\nv_mfma_f32_16x16x32_bf16 v[136:139], v[56:59], v[24:27], v[136:139]\n"
        "v_mfma_f32_16x16x32_bf16 v[156:159], v[60:63], v[24:27], v[156:159]\nds_read_b64_tr_b16 v[24:25], %[xa] offset:13728\ns_waitcnt lgkmcnt(7)\n"
        "v_mfma_f32_16x16x32_bf16 v[120:123], v[52:55], v[28:31], v[120:123]\nv_add_u32 %[sy], 4, %[sy]\nds_read_b64_tr_b16 v[26:27], %[xa] offset:14240\n"
        "v_mfma_f32_16x16x32_bf16 v[140:143], v[56:59], v[28:31], v[140:143]\nv_mfma_f32_16x16x32_bf16 v[160:163], v[60:63], v[28:31], v[160:163]\ns_waitcnt vmcnt(4) lgkmcnt(6)\n"
        "v_mfma_f32_16x16x32_bf16 v[96:99], v[64:67], v[16:19], v[96:99]\nds_read_b64_tr_b16 v[28:29], %[xa] offset:14784\nv_mfma_f32_16x16x32_bf16 v[108:111], v[68:71], v[16:19], v[108:111]\n"
        "ds_read_b64_tr_b16 v[30:31], %[xa] offset:15296\nv_mfma_f32_16x16x32_bf16 v[128:131], v[72:75], v[16:19], v[128:131]\nds_read_b32 %[pv], %[sy] offset:4\n"
        "v_mfma_f32_16x16x32_bf16 v[148:151], v[76:79], v[16:19], v[148:151]\nds_read_b64_tr_b16 v[16:17], %[xa] offset:16896\ns_waitcnt lgkmcnt(8)\n"
        "v_mfma_f32_16x16x32_bf16 v[100:103], v[64:67], v[20:23], v[100:103]\nds_read_b64_tr_b16 v[18:19], %[xa] offset:17408\nv_mfma_f32_16x16x32_bf16 v[112:115], v[68:71], v[20:23], v[112:115]\n"
        "v_mfma_f32_16x16x32_bf16 v[132:135], v[72:75], v[20:23], v[132:135]\nv_mfma_f32_16x16x32_bf16 v[152:155], v[76:79], v[20:23], v[152:155]\nds_read_b64_tr_b16 v[20:21], %[xa] offset:17952\n"
        "s_waitcnt lgkmcnt(8)\nv_mfma_f32_16x16x32_bf16 v[104:107], v[68:71], v[12:15], v[104:107]\nds_read_b64_tr_b16 v[22:23], %[xa] offset:18464\n"
        "v_mfma_f32_16x16x32_bf16 v[124:127], v[72:75], v[12:15], v[124:127]\nv_mfma_f32_16x16x32_bf16 v[144:147], v[76:79], v[12:15], v[144:147]\nds_read_b64_tr_b16 v[12:13], %[xa] offset:15840\n"
        "s_waitcnt lgkmcnt(8)\nv_mfma_f32_16x16x32_bf16 v[116:119], v[68:71], v[24:27], v[116:119]\nds_read_b64_tr_b16 v[14:15], %[xa] offset:16352\n"
        "v_mfma_f32_16x16x32_bf16 v[136:139], v[72:75], v[24:27], v[136:139]\nv_mfma_f32_16x16x32_bf16 v[156:159], v[76:79], v[24:27], v[156:159]\nds_read_b64_tr_b16 v[24:25], %[xa] offset:19008\n"
        "s_waitcnt lgkmcnt(8)\nv_mfma_f32_16x16x32_bf16 v[120:123], v[68:71], v[28:31], v[120:123]\nds_read_b64_tr_b16 v[26:27], %[xa] offset:19520\n"
        "v_mfma_f32_16x16x32_bf16 v[140:143], v[72:75], v[28:31], v[140:143]\nv_mfma_f32_16x16x32_bf16 v[160:163], v[76:79], v[28:31], v[160:163]\ns_waitcnt vmcnt(0) lgkmcnt(6)\n"
        "v_mfma_f32_16x16x32_bf16 v[96:99], v[80:83], v[16:19], v[96:99]\nv_readfirstlane_b32 %[s0], %[pv]\nds_read_b64_tr_b16 v[28:29], %[xa] offset:20064\n"
        "v_mfma_f32_16x16x32_bf16 v[108:111], v[84:87], v[16:19], v[108:111]\nds_read_b64_tr_b16 v[30:31], %[xa] offset:20576\nv_mfma_f32_16x16x32_bf16 v[128:131], v[88:91], v[16:19], v[128:131]\n"
        "s_cmp_ge_i32 %[s0], %[nst]\nv_mfma_f32_16x16x32_bf16 v[148:151], v[92:95], v[16:19], v[148:151]\ns_cbranch_scc0 .LB1t%=\n.LB1u%=:\ns_waitcnt lgkmcnt(6)\n"
        "v_mfma_f32_16x16x32_bf16 v[100:103], v[80:83], v[20:23], v[100:103]\nv_add_u32 %[sy], 4, %[sy]\nds_read_b128 v[16:19], %[mra] offset:256\n"
        "v_mfma_f32_16x16x32_bf16 v[112:115], v[84:87], v[20:23], v[112:115]\nv_mfma_f32_16x16x32_bf16 v[132:135], v[88:91], v[20:23], v[132:135]\n"
        "v_mfma_f32_16x16x32_bf16 v[152:155], v[92:95], v[20:23], v[152:155]\nds_read_b128 v[32:35], %[cwa] offset:448\ns_waitcnt lgkmcnt(6)\n"
        "v_mfma_f32_16x16x32_bf16 v[104:107], v[84:87], v[12:15], v[104:107]\nds_read_b128 v[36:39], %[cwa] offset:128\nds_read_b128 v[20:23], %[mra] offset:512\n"
        "v_mfma_f32_16x16x32_bf16 v[124:127], v[88:91], v[12:15], v[124:127]\nds_read_b128 v[40:43], %[cwa] offset:192\nv_mfma_f32_16x16x32_bf16 v[144:147], v[92:95], v[12:15], v[144:147]\n"
        "ds_read_b128 v[44:47], %[cwa] offset:768\ns_waitcnt lgkmcnt(8)\nv_mfma_f32_16x16x32_bf16 v[116:119], v[84:87], v[24:27], v[116:119]\nv_add_u32 %[sy], 4, %[sy]\n"
        "ds_read_b128 v[12:15], %[mra] offset:0\nv_mfma_f32_16x16x32_bf16 v[136:139], v[88:91], v[24:27], v[136:139]\nv_mfma_f32_16x16x32_bf16 v[156:159], v[92:95], v[24:27], v[156:159]\n"
        "ds_read_b128 v[24:27], %[mra] offset:768\ns_waitcnt lgkmcnt(8)\nv_mfma_f32_16x16x32_bf16 v[120:123], v[84:87], v[28:31], v[120:123]\n"
        "v_mfma_f32_16x16x32_bf16 v[140:143], v[88:91], v[28:31], v[140:143]\nv_mfma_f32_16x16x32_bf16 v[160:163], v[92:95], v[28:31], v[160:163]\nds_read_b128 v[28:31], %[mra] offset:1024\n"
        "s_setprio %[pfs]\ns_waitcnt lgkmcnt(1)\n.long 0xd3b04060, 0x1d822120\n.long 0xd3b04062, 0x1d8a2122\n.long 0xd3b0406c, 0x1db22124\n.long 0xd3b0406e, 0x1dba2126\n.long 0xd3b14060, 0x18022560\n"
        ".long 0xd3b14062, 0x18022562\n.long 0xd3b04080, 0x1e022128\n.long 0xd3b04082, 0x1e0a212a\n.long 0xd3b1406c, 0x1802256c\n.long 0xd3b1406e, 0x1802256e\nv_cvt_pk_bf16_f32 v96, v96, v97\n"
        "v_cvt_pk_bf16_f32 v97, v98, v99\n.long 0xd3b04064, 0x1d922920\n.long 0xd3b04066, 0x1d9a2922\n.long 0xd3b14080, 0x18022580\n.long 0xd3b14082, 0x18022582\nv_cvt_pk_bf16_f32 v108, v108, v109\n"
        "v_cvt_pk_bf16_f32 v109, v110, v111\nds_write_b64 %[sta], v[96:97] offset:8160\n.long 0xd3b04070, 0x1dc22924\n.long 0xd3b04072, 0x1dca2926\n.long 0xd3b14064, 0x18022d64\n"
        ".long 0xd3b14066, 0x18022d66\nv_cvt_pk_bf16_f32 v128, v128, v129\nv_cvt_pk_bf16_f32 v129, v130, v131\nds_write_b64 %[sta], v[108:109] offset:8000\n.long 0xd3b04084, 0x1e122928\n"
        ".long 0xd3b04086, 0x1e1a292a\n.long 0xd3b14070, 0x18022d70\n.long 0xd3b14072, 0x18022d72\nv_cvt_pk_bf16_f32 v100, v100, v101\nv_cvt_pk_bf16_f32 v101, v102, v103\n"
        "ds_write_b64 %[sta], v[128:129] offset:8032\n.long 0xd3b04068, 0x1da21924\n.long 0xd3b0406a, 0x1daa1926\n.long 0xd3b14084, 0x18022d84\n.long 0xd3b14086, 0x18022d86\n"
        "v_cvt_pk_bf16_f32 v112, v112, v113\nv_cvt_pk_bf16_f32 v113, v114, v115\nds_write_b64 %[sta], v[100:101] offset:16096\n.long 0xd3b0407c, 0x1df21928\n.long 0xd3b0407e, 0x1dfa192a\n"
        ".long 0xd3b14068, 0x18021d68\n.long 0xd3b1406a, 0x18021d6a\nv_cvt_pk_bf16_f32 v132, v132, v133\nv_cvt_pk_bf16_f32 v133, v134, v135\nds_write_b64 %[sta], v[112:113] offset:15936\n"
        ".long 0xd3b04074, 0x1dd23124\n.long 0xd3b04076, 0x1dda3126\n.long 0xd3b1407c, 0x18021d7c\n.long 0xd3b1407e, 0x18021d7e\nv_cvt_pk_bf16_f32 v104, v104, v105\nv_cvt_pk_bf16_f32 v105, v106, v107\n"
        "ds_write_b64 %[sta], v[132:133] offset:15968\n.long 0xd3b04088, 0x1e223128\n.long 0xd3b0408a, 0x1e2a312a\n.long 0xd3b14074, 0x18023574\n.long 0xd3b14076, 0x18023576\n"
        "v_cvt_pk_bf16_f32 v124, v124, v125\nv_cvt_pk_bf16_f32 v125, v126, v127\nds_write_b64 %[sta], v[104:105] offset:64\ns_waitcnt lgkmcnt(7)\n.long 0xd3b04078, 0x1de23924\n"
        ".long 0xd3b0407a, 0x1dea3926\n.long 0xd3b14088, 0x18023588\n.long 0xd3b1408a, 0x1802358a\nv_cvt_pk_bf16_f32 v116, v116, v117\nv_cvt_pk_bf16_f32 v117, v118, v119\n"
        "ds_write_b64 %[sta], v[124:125] offset:96\n.long 0xd3b0408c, 0x1e323928\n.long 0xd3b0408e, 0x1e3a392a\n.long 0xd3b14078, 0x18023d78\n.long 0xd3b1407a, 0x18023d7a\n"
        "v_cvt_pk_bf16_f32 v136, v136, v137\nv_cvt_pk_bf16_f32 v137, v138, v139\nds_write_b64 %[sta], v[116:117] offset:23872\n.long 0xd3b1408c, 0x18023d8c\n.long 0xd3b1408e, 0x18023d8e\n"
        "v_cvt_pk_bf16_f32 v120, v120, v121\nv_cvt_pk_bf16_f32 v121, v122, v123\nds_write_b64 %[sta], v[136:137] offset:23904\nv_cvt_pk_bf16_f32 v140, v140, v141\nv_cvt_pk_bf16_f32 v141, v142, v143\n"
        "ds_write_b64 %[sta], v[120:121] offset:31808\nds_write_b64 %[sta], v[140:141] offset:31840\nds_write_b32 %[pg], %[sy]\nv_add_u32 %[sy], 4, %[sy]\n.long 0xd3b04094, 0x1e52212c\n"
        ".long 0xd3b04096, 0x1e5a212e\n.long 0xd3b04098, 0x1e62292c\n.long 0xd3b0409a, 0x1e6a292e\n.long 0xd3b14094, 0x18022594\n.long 0xd3b14096, 0x18022596\n.long 0xd3b04090, 0x1e42192c\n"
        ".long 0xd3b04092, 0x1e4a192e\n.long 0xd3b14098, 0x18022d98\n.long 0xd3b1409a, 0x18022d9a\nv_cvt_pk_bf16_f32 v148, v148, v149\nv_cvt_pk_bf16_f32 v149, v150, v151\n.long 0xd3b0409c, 0x1e72312c\n"
        ".long 0xd3b0409e, 0x1e7a312e\n.long 0xd3b14090, 0x18021d90\n.long 0xd3b14092, 0x18021d92\nv_cvt_pk_bf16_f32 v152, v152, v153\nv_cvt_pk_bf16_f32 v153, v154, v155\n"
        "ds_write_b64 %[sta], v[148:149] offset:8320\n.long 0xd3b040a0, 0x1e82392c\n.long 0xd3b040a2, 0x1e8a392e\n.long 0xd3b1409c, 0x1802359c\n.long 0xd3b1409e, 0x1802359e\n"
        "v_cvt_pk_bf16_f32 v144, v144, v145\nv_cvt_pk_bf16_f32 v145, v146, v147\nds_write_b64 %[sta], v[152:153] offset:16256\n.long 0xd3b140a0, 0x18023da0\n.long 0xd3b140a2, 0x18023da2\n"
        "v_cvt_pk_bf16_f32 v156, v156, v157\nv_cvt_pk_bf16_f32 v157, v158, v159\nds_write_b64 %[sta], v[144:145] offset:384\nv_cvt_pk_bf16_f32 v160, v160, v161\nv_cvt_pk_bf16_f32 v161, v162, v163\n"
        "ds_write_b64 %[sta], v[156:157] offset:24192\nds_write_b64 %[sta], v[160:161] offset:32128\nds_write_b32 %[pg], %[sy]\ns_branch .LE%=\n.LB1s0_%=:\ns_mov_b32 %[s1], 0x100000\n.LB1p0_%=:\n"
        "ds_read_b32 %[pv], %[sy] offset:4\ns_waitcnt lgkmcnt(0)\nv_readfirstlane_b32 %[s0], %[pv]\ns_nop 0\ns_cmp_ge_i32 %[s0], 6\ns_cbranch_scc1 .LB1r0_%=\ns_sleep 1\ns_sub_u32 %[s1], %[s1], 1\n"
        "s_cbranch_scc0 .LB1p0_%=\ns_branch .LB1r0_%=\n.LB1s1_%=:\ns_mov_b32 %[s1], 0x100000\n.LB1p1_%=:\nds_read_b32 %[pv], %[sy] offset:4\ns_waitcnt lgkmcnt(0)\nv_readfirstlane_b32 %[s0], %[pv]\n"
        "s_nop 0\ns_cmp_ge_i32 %[s0], 6\ns_cbranch_scc1 .LB1r1_%=\ns_sleep 1\ns_sub_u32 %[s1], %[s1], 1\ns_cbranch_scc0 .LB1p1_%=\ns_branch .LB1r1_%=\n.LB1s2_%=:\ns_mov_b32 %[s1], 0x100000\n"
        ".LB1p2_%=:\nds_read_b32 %[pv], %[sy] offset:4\ns_waitcnt lgkmcnt(0)\nv_readfirstlane_b32 %[s0], %[pv]\ns_nop 0\ns_cmp_ge_i32 %[s0], 6\ns_cbranch_scc1 .LB1r2_%=\ns_sleep 1\n"
        "s_sub_u32 %[s1], %[s1], 1\ns_cbranch_scc0 .LB1p2_%=\ns_branch .LB1r2_%=\n.LB1t%=:\ns_mov_b32 %[s1], 0x100000\n.LB1q%=:\nds_read_b32 %[pv], %[sy] offset:4\ns_waitcnt lgkmcnt(0)\n"
        "v_readfirstlane_b32 %[s0], %[pv]\ns_nop 0\ns_cmp_ge_i32 %[s0], %[nst]\ns_cbranch_scc1 .LB1u%=\ns_sleep 1\ns_sub_u32 %[s1], %[s1], 1\ns_cbranch_scc0 .LB1q%=\ns_branch .LB1u%=\n.LC%=:\n"
        "s_mov_b32 %[so], 0\nbuffer_load_dwordx4 v[32:35], %[wa], %[rs], %[so] offen offset:0 sc1\nbuffer_load_dwordx4 v[36:39], %[wa], %[rs], %[so] offen offset:1024 sc1\n"
        "buffer_load_dwordx4 v[40:43], %[wa], %[rs], %[so] offen offset:2048 sc1\nbuffer_load_dwordx4 v[44:47], %[wa], %[rs], %[so] offen offset:3072 sc1\ns_mov_b32 %[so], 15360\n"
        "buffer_load_dwordx4 v[48:51], %[wa], %[rs], %[so] offen offset:0 sc1\nbuffer_load_dwordx4 v[52:55], %[wa], %[rs], %[so] offen offset:1024 sc1\n"
        "buffer_load_dwordx4 v[56:59], %[wa], %[rs], %[so] offen offset:2048 sc1\nbuffer_load_dwordx4 v[60:63], %[wa], %[rs], %[so] offen offset:3072 sc1\ns_mov_b32 %[so], 30720\n"
        "buffer_load_dwordx4 v[64:67], %[wa], %[rs], %[so] offen offset:0 sc1\nbuffer_load_dwordx4 v[68:71], %[wa], %[rs], %[so] offen offset:1024 sc1\n"
        "buffer_load_dwordx4 v[72:75], %[wa], %[rs], %[so] offen offset:2048 sc1\nbuffer_load_dwordx4 v[76:79], %[wa], %[rs], %[so] offen offset:3072 sc1\ns_mov_b32 %[so], 46080\n"
        "buffer_load_dwordx4 v[80:83], %[wa], %[rs], %[so] offen offset:0 sc1\nbuffer_load_dwordx4 v[84:87], %[wa], %[rs], %[so] offen offset:1024 sc1\n"
        "buffer_load_dwordx4 v[88:91], %[wa], %[rs], %[so] offen offset:2048 sc1\nbuffer_load_dwordx4 v[92:95], %[wa], %[rs], %[so] offen offset:3072 sc1\ns_barrier\ns_mov_b32 %[s1], 0x100000\n"
        ".LC1w%=:\nds_read_b32 %[pv], %[sy] offset:0\ns_waitcnt lgkmcnt(0)\nv_readfirstlane_b32 %[s0], %[pv]\ns_nop 0\ns_cmp_ge_i32 %[s0], 6\ns_cbranch_scc1 .LC1g%=\ns_sleep 1\n"
        "s_sub_u32 %[s1], %[s1], 1\ns_cbranch_scc0 .LC1w%=\n.LC1g%=:\nds_read_b64_tr_b16 v[24:25], %[xa] offset:3168\nds_read_b64_tr_b16 v[26:27], %[xa] offset:3680\n"
        "ds_read_b64_tr_b16 v[28:29], %[xa] offset:4224\nds_read_b64_tr_b16 v[30:31], %[xa] offset:4736\nds_read_b64_tr_b16 v[12:13], %[xa] offset:0\nds_read_b64_tr_b16 v[14:15], %[xa] offset:512\n"
        "ds_read_b64_tr_b16 v[16:17], %[xa] offset:1056\nds_read_b64_tr_b16 v[18:19], %[xa] offset:1568\ns_waitcnt vmcnt(12) lgkmcnt(6)\nv_mfma_f32_16x16x32_bf16 v[96:99], v[32:35], v[24:27], 0\n"
        "ds_read_b64_tr_b16 v[20:21], %[xa] offset:2112\nv_mfma_f32_16x16x32_bf16 v[116:119], v[36:39], v[24:27], 0\nds_read_b64_tr_b16 v[22:23], %[xa] offset:2624\n"
        "v_mfma_f32_16x16x32_bf16 v[136:139], v[40:43], v[24:27], 0\nds_read_b32 %[pv], %[sy] offset:4\nv_mfma_f32_16x16x32_bf16 v[156:159], v[44:47], v[24:27], 0\n"
        "ds_read_b64_tr_b16 v[24:25], %[xa] offset:8448\ns_waitcnt lgkmcnt(8)\nv_mfma_f32_16x16x32_bf16 v[100:103], v[32:35], v[28:31], 0\nds_read_b64_tr_b16 v[26:27], %[xa] offset:8960\n"
        "v_mfma_f32_16x16x32_bf16 v[120:123], v[36:39], v[28:31], 0\nv_mfma_f32_16x16x32_bf16 v[140:143], v[40:43], v[28:31], 0\nv_mfma_f32_16x16x32_bf16 v[160:163], v[44:47], v[28:31], 0\n"
        "ds_read_b64_tr_b16 v[28:29], %[xa] offset:9504\ns_waitcnt lgkmcnt(8)\nv_mfma_f32_16x16x32_bf16 v[104:107], v[36:39], v[12:15], 0\nds_read_b64_tr_b16 v[30:31], %[xa] offset:10016\n"
        "v_mfma_f32_16x16x32_bf16 v[124:127], v[40:43], v[12:15], 0\nv_mfma_f32_16x16x32_bf16 v[144:147], v[44:47], v[12:15], 0\nds_read_b64_tr_b16 v[12:13], %[xa] offset:5280\ns_waitcnt lgkmcnt(8)\n"
        "v_mfma_f32_16x16x32_bf16 v[108:111], v[36:39], v[16:19], 0\nds_read_b64_tr_b16 v[14:15], %[xa] offset:5792\nv_mfma_f32_16x16x32_bf16 v[128:131], v[40:43], v[16:19], 0\n"
        "v_mfma_f32_16x16x32_bf16 v[148:151], v[44:47], v[16:19], 0\nds_read_b64_tr_b16 v[16:17], %[xa] offset:6336\ns_waitcnt lgkmcnt(8)\nv_mfma_f32_16x16x32_bf16 v[112:115], v[36:39], v[20:23], 0\n"
        "ds_read_b64_tr_b16 v[18:19], %[xa] offset:6848\nv_mfma_f32_16x16x32_bf16 v[132:135], v[40:43], v[20:23], 0\nv_mfma_f32_16x16x32_bf16 v[152:155], v[44:47], v[20:23], 0\n.LC1l%=:\n"
        "s_waitcnt vmcnt(8) lgkmcnt(6)\nv_mfma_f32_16x16x32_bf16 v[96:99], v[48:51], v[24:27], v[96:99]\ns_addk_i32 %[so], 15360\nv_readfirstlane_b32 %[s0], %[pv]\n"
        "v_mfma_f32_16x16x32_bf16 v[116:119], v[52:55], v[24:27], v[116:119]\ns_cmp_ge_i32 %[s0], 6\nds_read_b64_tr_b16 v[20:21], %[xa] offset:7392\n"
        "v_mfma_f32_16x16x32_bf16 v[136:139], v[56:59], v[24:27], v[136:139]\ns_cbranch_scc0 .LC1s0_%=\n.LC1r0_%=:\nds_read_b64_tr_b16 v[22:23], %[xa] offset:7904\n"
        "v_mfma_f32_16x16x32_bf16 v[156:159], v[60:63], v[24:27], v[156:159]\nds_read_b64_tr_b16 v[24:25], %[xa] offset:13728\ns_waitcnt lgkmcnt(7)\n"
        "v_mfma_f32_16x16x32_bf16 v[100:103], v[48:51], v[28:31], v[100:103]\nds_read_b64_tr_b16 v[26:27], %[xa] offset:14240\nbuffer_load_dwordx4 v[32:35], %[wa], %[rs], %[so] offen offset:0 sc1\n"
        "v_mfma_f32_16x16x32_bf16 v[120:123], v[52:55], v[28:31], v[120:123]\nbuffer_load_dwordx4 v[36:39], %[wa], %[rs], %[so] offen offset:1024 sc1\n"
        "v_mfma_f32_16x16x32_bf16 v[140:143], v[56:59], v[28:31], v[140:143]\nv_mfma_f32_16x16x32_bf16 v[160:163], v[60:63], v[28:31], v[160:163]\nds_read_b64_tr_b16 v[28:29], %[xa] offset:14784\n"
        "s_waitcnt lgkmcnt(7)\nv_mfma_f32_16x16x32_bf16 v[104:107], v[52:55], v[12:15], v[104:107]\nds_read_b64_tr_b16 v[30:31], %[xa] offset:15296\n"
        "v_mfma_f32_16x16x32_bf16 v[124:127], v[56:59], v[12:15], v[124:127]\nbuffer_load_dwordx4 v[40:43], %[wa], %[rs], %[so] offen offset:2048 sc1\n"
        "v_mfma_f32_16x16x32_bf16 v[144:147], v[60:63], v[12:15], v[144:147]\nds_read_b64_tr_b16 v[12:13], %[xa] offset:10560\ns_waitcnt lgkmcnt(7)\n"
        "v_mfma_f32_16x16x32_bf16 v[108:111], v[52:55], v[16:19], v[108:111]\nds_read_b64_tr_b16 v[14:15], %[xa] offset:11072\nv_mfma_f32_16x16x32_bf16 v[128:131], v[56:59], v[16:19], v[128:131]\n"
        "buffer_load_dwordx4 v[44:47], %[wa], %[rs], %[so] offen offset:3072 sc1\nv_mfma_f32_16x16x32_bf16 v[148:151], v[60:63], v[16:19], v[148:151]\nds_read_b64_tr_b16 v[16:17], %[xa] offset:11616\n"
        "s_waitcnt lgkmcnt(7)\nv_mfma_f32_16x16x32_bf16 v[112:115], v[52:55], v[20:23], v[112:115]\nv_add_u32 %[sy], 4, %[sy]\nds_read_b64_tr_b16 v[18:19], %[xa] offset:12128\n"
        "v_mfma_f32_16x16x32_bf16 v[132:135], v[56:59], v[20:23], v[132:135]\nds_write_b32 %[pg], %[sy]\nv_mfma_f32_16x16x32_bf16 v[152:155], v[60:63], v[20:23], v[152:155]\n"
        "s_waitcnt vmcnt(8) lgkmcnt(7)\nv_mfma_f32_16x16x32_bf16 v[96:99], v[64:67], v[24:27], v[96:99]\ns_addk_i32 %[so], 15360\nds_read_b64_tr_b16 v[20:21], %[xa] offset:12672\n"
        "v_mfma_f32_16x16x32_bf16 v[116:119], v[68:71], v[24:27], v[116:119]\nds_read_b64_tr_b16 v[22:23], %[xa] offset:13184\nv_mfma_f32_16x16x32_bf16 v[136:139], v[72:75], v[24:27], v[136:139]\n"
        "ds_read_b32 %[pv], %[sy] offset:4\nbuffer_load_dwordx4 v[48:51], %[wa], %[rs], %[so] offen offset:0 sc1\nv_mfma_f32_16x16x32_bf16 v[156:159], v[76:79], v[24:27], v[156:159]\n"
        "ds_read_b64_tr_b16 v[24:25], %[xa] offset:19008\ns_waitcnt lgkmcnt(9)\nv_mfma_f32_16x16x32_bf16 v[100:103], v[64:67], v[28:31], v[100:103]\nds_read_b64_tr_b16 v[26:27], %[xa] offset:19520\n"
        "v_mfma_f32_16x16x32_bf16 v[120:123], v[68:71], v[28:31], v[120:123]\nbuffer_load_dwordx4 v[52:55], %[wa], %[rs], %[so] offen offset:1024 sc1\n"
        "v_mfma_f32_16x16x32_bf16 v[140:143], v[72:75], v[28:31], v[140:143]\nv_mfma_f32_16x16x32_bf16 v[160:163], v[76:79], v[28:31], v[160:163]\nds_read_b64_tr_b16 v[28:29], %[xa] offset:20064\n"
        "s_waitcnt lgkmcnt(9)\nv_mfma_f32_16x16x32_bf16 v[104:107], v[68:71], v[12:15], v[104:107]\nds_read_b64_tr_b16 v[30:31], %[xa] offset:20576\n"
        "v_mfma_f32_16x16x32_bf16 v[124:127], v[72:75], v[12:15], v[124:127]\nbuffer_load_dwordx4 v[56:59], %[wa], %[rs], %[so] offen offset:2048 sc1\n"
        "v_mfma_f32_16x16x32_bf16 v[144:147], v[76:79], v[12:15], v[144:147]\nds_read_b64_tr_b16 v[12:13], %[xa] offset:15840\ns_waitcnt lgkmcnt(9)\n"
        "v_mfma_f32_16x16x32_bf16 v[108:111], v[68:71], v[16:19], v[108:111]\nds_read_b64_tr_b16 v[14:15], %[xa] offset:16352\nv_mfma_f32_16x16x32_bf16 v[128:131], v[72:75], v[16:19], v[128:131]\n"
        "buffer_load_dwordx4 v[60:63], %[wa], %[rs], %[so] offen offset:3072 sc1\nv_mfma_f32_16x16x32_bf16 v[148:151], v[76:79], v[16:19], v[148:151]\nds_read_b64_tr_b16 v[16:17], %[xa] offset:16896\n"
        "s_waitcnt lgkmcnt(8)\nv_mfma_f32_16x16x32_bf16 v[112:115], v[68:71], v[20:23], v[112:115]\nds_read_b64_tr_b16 v[18:19], %[xa] offset:17408\n"
        "v_mfma_f32_16x16x32_bf16 v[132:135], v[72:75], v[20:23], v[132:135]\nv_mfma_f32_16x16x32_bf16 v[152:155], v[76:79], v[20:23], v[152:155]\ns_waitcnt vmcnt(8) lgkmcnt(6)\n"
        "v_mfma_f32_16x16x32_bf16 v[96:99], v[80:83], v[24:27], v[96:99]\ns_addk_i32 %[so], 15360\nv_readfirstlane_b32 %[s0], %[pv]\n"
        "v_mfma_f32_16x16x32_bf16 v[116:119], v[84:87], v[24:27], v[116:119]\ns_cmp_ge_i32 %[s0], 6\nds_read_b64_tr_b16 v[20:21], %[xa] offset:17952\n"
        "v_mfma_f32_16x16x32_bf16 v[136:139], v[88:91], v[24:27], v[136:139]\ns_cbranch_scc0 .LC1s1_%=\n.LC1r1_%=:\nds_read_b64_tr_b16 v[22:23], %[xa] offset:18464\n"
        "v_mfma_f32_16x16x32_bf16 v[156:159], v[92:95], v[24:27], v[156:159]\nds_read_b64_tr_b16 v[24:25], %[xa] offset:24288\ns_waitcnt lgkmcnt(7)\n"
        "v_mfma_f32_16x16x32_bf16 v[100:103], v[80:83], v[28:31], v[100:103]\nds_read_b64_tr_b16 v[26:27], %[xa] offset:24800\nbuffer_load_dwordx4 v[64:67], %[wa], %[rs], %[so] offen offset:0 sc1\n"
        "v_mfma_f32_16x16x32_bf16 v[120:123], v[84:87], v[28:31], v[120:123]\nbuffer_load_dwordx4 v[68:71], %[wa], %[rs], %[so] offen offset:1024 sc1\n"
        "v_mfma_f32_16x16x32_bf16 v[140:143], v[88:91], v[28:31], v[140:143]\nv_mfma_f32_16x16x32_bf16 v[160:163], v[92:95], v[28:31], v[160:163]\nds_read_b64_tr_b16 v[28:29], %[xa] offset:25344\n"
        "s_waitcnt lgkmcnt(7)\nv_mfma_f32_16x16x32_bf16 v[104:107], v[84:87], v[12:15], v[104:107]\nds_read_b64_tr_b16 v[30:31], %[xa] offset:25856\n"
        "v_mfma_f32_16x16x32_bf16 v[124:127], v[88:91], v[12:15], v[124:127]\nbuffer_load_dwordx4 v[72:75], %[wa], %[rs], %[so] offen offset:2048 sc1\n"
        "v_mfma_f32_16x16x32_bf16 v[144:147], v[92:95], v[12:15], v[144:147]\nds_read_b64_tr_b16 v[12:13], %[xa] offset:21120\ns_waitcnt lgkmcnt(7)\n"
        "v_mfma_f32_16x16x32_bf16 v[108:111], v[84:87], v[16:19], v[108:111]\nds_read_b64_tr_b16 v[14:15], %[xa] offset:21632\nv_mfma_f32_16x16x32_bf16 v[128:131], v[88:91], v[16:19], v[128:131]\n"
        "buffer_load_dwordx4 v[76:79], %[wa], %[rs], %[so] offen offset:3072 sc1\nv_mfma_f32_16x16x32_bf16 v[148:151], v[92:95], v[16:19], v[148:151]\nds_read_b64_tr_b16 v[16:17], %[xa] offset:22176\n"
        "s_waitcnt lgkmcnt(7)\nv_mfma_f32_16x16x32_bf16 v[112:115], v[84:87], v[20:23], v[112:115]\nv_add_u32 %[sy], 4, %[sy]\nds_read_b64_tr_b16 v[18:19], %[xa] offset:22688\n"
        "v_mfma_f32_16x16x32_bf16 v[132:135], v[88:91], v[20:23], v[132:135]\nds_write_b32 %[pg], %[sy]\nv_mfma_f32_16x16x32_bf16 v[152:155], v[92:95], v[20:23], v[152:155]\n"
        "s_waitcnt vmcnt(8) lgkmcnt(7)\nv_mfma_f32_16x16x32_bf16 v[96:99], v[32:35], v[24:27], v[96:99]\ns_addk_i32 %[so], 15360\nds_read_b64_tr_b16 v[20:21], %[xa] offset:23232\n"
        "v_mfma_f32_16x16x32_bf16 v[116:119], v[36:39], v[24:27], v[116:119]\nds_read_b64_tr_b16 v[22:23], %[xa] offset:23744\nv_mfma_f32_16x16x32_bf16 v[136:139], v[40:43], v[24:27], v[136:139]\n"
        "ds_read_b32 %[pv], %[sy] offset:4\nbuffer_load_dwordx4 v[80:83], %[wa], %[rs], %[so] offen offset:0 sc1\nv_mfma_f32_16x16x32_bf16 v[156:159], v[44:47], v[24:27], v[156:159]\n"
        "ds_read_b64_tr_b16 v[24:25], %[xa] offset:29568\ns_waitcnt lgkmcnt(9)\nv_mfma_f32_16x16x32_bf16 v[100:103], v[32:35], v[28:31], v[100:103]\nds_read_b64_tr_b16 v[26:27], %[xa] offset:30080\n"
        "v_mfma_f32_16x16x32_bf16 v[120:123], v[36:39], v[28:31], v[120:123]\nbuffer_load_dwordx4 v[84:87], %[wa], %[rs], %[so] offen offset:1024 sc1\n"
        "v_mfma_f32_16x16x32_bf16 v[140:143], v[40:43], v[28:31], v[140:143]\nv_mfma_f32_16x16x32_bf16 v[160:163], v[44:47], v[28:31], v[160:163]\nds_read_b64_tr_b16 v[28:29], %[xa] offset:30624\n"
        "s_waitcnt lgkmcnt(9)\nv_mfma_f32_16x16x32_bf16 v[104:107], v[36:39], v[12:15], v[104:107]\nds_read_b64_tr_b16 v[30:31], %[xa] offset:31136\n"
        "v_mfma_f32_16x16x32_bf16 v[124:127], v[40:43], v[12:15], v[124:127]\nbuffer_load_dwordx4 v[88:91], %[wa], %[rs], %[so] offen offset:2048 sc1\n"
        "v_mfma_f32_16x16x32_bf16 v[144:147], v[44:47], v[12:15], v[144:147]\nds_read_b64_tr_b16 v[12:13], %[xa] offset:26400\ns_waitcnt lgkmcnt(9)\n"
        "v_mfma_f32_16x16x32_bf16 v[108:111], v[36:39], v[16:19], v[108:111]\nds_read_b64_tr_b16 v[14:15], %[xa] offset:26912\nv_mfma_f32_16x16x32_bf16 v[128:131], v[40:43], v[16:19], v[128:131]\n"
        "buffer_load_dwordx4 v[92:95], %[wa], %[rs], %[so] offen offset:3072 sc1\nv_mfma_f32_16x16x32_bf16 v[148:151], v[44:47], v[16:19], v[148:151]\nds_read_b64_tr_b16 v[16:17], %[xa] offset:27456\n"
        "s_waitcnt lgkmcnt(8)\nv_mfma_f32_16x16x32_bf16 v[112:115], v[36:39], v[20:23], v[112:115]\nds_read_b64_tr_b16 v[18:19], %[xa] offset:27968\n"
        "v_mfma_f32_16x16x32_bf16 v[132:135], v[40:43], v[20:23], v[132:135]\ns_cmp_lg_u32 %[so], 291840\nv_mfma_f32_16x16x32_bf16 v[152:155], v[44:47], v[20:23], v[152:155]\n"
        "v_add_u32 %[xa], 21120, %[xa]\ns_cbranch_scc1 .LC1l%=\ns_waitcnt vmcnt(8) lgkmcnt(6)\nv_mfma_f32_16x16x32_bf16 v[96:99], v[48:51], v[24:27], v[96:99]\nv_readfirstlane_b32 %[s0], %[pv]\n"
        "ds_read_b64_tr_b16 v[20:21], %[xa] offset:7392\nv_mfma_f32_16x16x32_bf16 v[116:119], v[52:55], v[24:27], v[116:119]\ns_cmp_ge_i32 %[s0], 6\nds_read_b64_tr_b16 v[22:23], %[xa] offset:7904\n"
        "v_mfma_f32_16x16x32_bf16 v[136:139], v[56:59], v[24:27], v[136:139]\ns_cbranch_scc0 .LC1s2_%=\n.LC1r2_%=:\nv_mfma_f32_16x16x32_bf16 v[156:159], v[60:63], v[24:27], v[156:159]\n"
        "ds_read_b64_tr_b16 v[24:25], %[xa] offset:13728\ns_waitcnt lgkmcnt(7)\nv_mfma_f32_16x16x32_bf16 v[100:103], v[48:51], v[28:31], v[100:103]\nds_read_b64_tr_b16 v[26:27], %[xa] offset:14240\n"
        "v_mfma_f32_16x16x32_bf16 v[120:123], v[52:55], v[28:31], v[120:123]\nv_mfma_f32_16x16x32_bf16 v[140:143], v[56:59], v[28:31], v[140:143]\n"
        "v_mfma_f32_16x16x32_bf16 v[160:163], v[60:63], v[28:31], v[160:163]\nds_read_b64_tr_b16 v[28:29], %[xa] offset:14784\ns_waitcnt lgkmcnt(7)\n"
        "v_mfma_f32_16x16x32_bf16 v[104:107], v[52:55], v[12:15], v[104:107]\nds_read_b64_tr_b16 v[30:31], %[xa] offset:15296\nv_mfma_f32_16x16x32_bf16 v[124:127], v[56:59], v[12:15], v[124:127]\n"
        "v_mfma_f32_16x16x32_bf16 v[144:147], v[60:63], v[12:15], v[144:147]\nds_read_b64_tr_b16 v[12:13], %[xa] offset:10560\ns_waitcnt lgkmcnt(7)\n"
        "v_mfma_f32_16x16x32_bf16 v[108:111], v[52:55], v[16:19], v[108:111]\nds_read_b64_tr_b16 v[14:15], %[xa] offset:11072\nv_mfma_f32_16x16x32_bf16 v[128:131], v[56:59], v[16:19], v[128:131]\n"
        "v_mfma_f32_16x16x32_bf16 v[148:151], v[60:63], v[16:19], v[148:151]\nds_read_b64_tr_b16 v[16:17], %[xa] offset:11616\ns_waitcnt lgkmcnt(7)\n"
        "v_mfma_f32_16x16x32_bf16 v[112:115], v[52:55], v[20:23], v[112:115]\nv_add_u32 %[sy], 4, %[sy]\nds_read_b64_tr_b16 v[18:19], %[xa] offset:12128\n"
        "v_mfma_f32_16x16x32_bf16 v[132:135], v[56:59], v[20:23], v[132:135]\nv_mfma_f32_16x16x32_bf16 v[152:155], v[60:63], v[20:23], v[152:155]\ns_waitcnt vmcnt(4) lgkmcnt(6)\n"
        "v_mfma_f32_16x16x32_bf16 v[96:99], v[64:67], v[24:27], v[96:99]\nds_read_b64_tr_b16 v[20:21], %[xa] offset:12672\nv_mfma_f32_16x16x32_bf16 v[116:119], v[68:71], v[24:27], v[116:119]\n"
        "ds_read_b64_tr_b16 v[22:23], %[xa] offset:13184\nv_mfma_f32_16x16x32_bf16 v[136:139], v[72:75], v[24:27], v[136:139]\nds_read_b32 %[pv], %[sy] offset:4\n"
        "v_mfma_f32_16x16x32_bf16 v[156:159], v[76:79], v[24:27], v[156:159]\nds_read_b64_tr_b16 v[24:25], %[xa] offset:19008\ns_waitcnt lgkmcnt(8)\n"
        "v_mfma_f32_16x16x32_bf16 v[100:103], v[64:67], v[28:31], v[100:103]\nds_read_b64_tr_b16 v[26:27], %[xa] offset:19520\nv_mfma_f32_16x16x32_bf16 v[120:123], v[68:71], v[28:31], v[120:123]\n"
        "v_mfma_f32_16x16x32_bf16 v[140:143], v[72:75], v[28:31], v[140:143]\nv_mfma_f32_16x16x32_bf16 v[160:163], v[76:79], v[28:31], v[160:163]\nds_read_b64_tr_b16 v[28:29], %[xa] offset:20064\n"
        "s_waitcnt lgkmcnt(8)\nv_mfma_f32_16x16x32_bf16 v[104:107], v[68:71], v[12:15], v[104:107]\nds_read_b64_tr_b16 v[30:31], %[xa] offset:20576\n"
        "v_mfma_f32_16x16x32_bf16 v[124:127], v[72:75], v[12:15], v[124:127]\nv_mfma_f32_16x16x32_bf16 v[144:147], v[76:79], v[12:15], v[144:147]\nds_read_b64_tr_b16 v[12:13], %[xa] offset:15840\n"
        "s_waitcnt lgkmcnt(8)\nv_mfma_f32_16x16x32_bf16 v[108:111], v[68:71], v[16:19], v[108:111]\nds_read_b64_tr_b16 v[14:15], %[xa] offset:16352\n"
        "v_mfma_f32_16x16x32_bf16 v[128:131], v[72:75], v[16:19], v[128:131]\nv_mfma_f32_16x16x32_bf16 v[148:151], v[76:79], v[16:19], v[148:151]\nds_read_b64_tr_b16 v[16:17], %[xa] offset:16896\n"
        "s_waitcnt lgkmcnt(8)\nv_mfma_f32_16x16x32_bf16 v[112:115], v[68:71], v[20:23], v[112:115]\nds_read_b64_tr_b16 v[18:19], %[xa] offset:17408\n"
        "v_mfma_f32_16x16x32_bf16 v[132:135], v[72:75], v[20:23], v[132:135]\nv_mfma_f32_16x16x32_bf16 v[152:155], v[76:79], v[20:23], v[152:155]\ns_waitcnt vmcnt(0) lgkmcnt(6)\n"
        "v_mfma_f32_16x16x32_bf16 v[96:99], v[80:83], v[24:27], v[96:99]\nv_readfirstlane_b32 %[s0], %[pv]\nds_read_b64_tr_b16 v[20:21], %[xa] offset:17952\n"
        "v_mfma_f32_16x16x32_bf16 v[116:119], v[84:87], v[24:27], v[116:119]\nds_read_b64_tr_b16 v[22:23], %[xa] offset:18464\nv_mfma_f32_16x16x32_bf16 v[136:139], v[88:91], v[24:27], v[136:139]\n"
        "s_cmp_ge_i32 %[s0], %[nst]\nv_mfma_f32_16x16x32_bf16 v[156:159], v[92:95], v[24:27], v[156:159]\ns_cbranch_scc0 .LC1t%=\n.LC1u%=:\ns_waitcnt lgkmcnt(6)\n"
        "v_mfma_f32_16x16x32_bf16 v[100:103], v[80:83], v[28:31], v[100:103]\nv_add_u32 %[sy], 4, %[sy]\nds_read_b128 v[24:27], %[mra] offset:768\n"
        "v_mfma_f32_16x16x32_bf16 v[120:123], v[84:87], v[28:31], v[120:123]\nv_mfma_f32_16x16x32_bf16 v[140:143], v[88:91], v[28:31], v[140:143]\n"
        "v_mfma_f32_16x16x32_bf16 v[160:163], v[92:95], v[28:31], v[160:163]\nds_read_b128 v[32:35], %[cwa] offset:448\ns_waitcnt lgkmcnt(6)\n"
        "v_mfma_f32_16x16x32_bf16 v[104:107], v[84:87], v[12:15], v[104:107]\nds_read_b128 v[36:39], %[cwa] offset:256\nds_read_b128 v[28:31], %[mra] offset:1024\n"
        "v_mfma_f32_16x16x32_bf16 v[124:127], v[88:91], v[12:15], v[124:127]\nds_read_b128 v[40:43], %[cwa] offset:832\nv_mfma_f32_16x16x32_bf16 v[144:147], v[92:95], v[12:15], v[144:147]\n"
        "ds_read_b128 v[44:47], %[cwa] offset:896\ns_waitcnt lgkmcnt(8)\nv_mfma_f32_16x16x32_bf16 v[108:111], v[84:87], v[16:19], v[108:111]\nv_add_u32 %[sy], 4, %[sy]\n"
        "ds_read_b128 v[12:15], %[mra] offset:0\nv_mfma_f32_16x16x32_bf16 v[128:131], v[88:91], v[16:19], v[128:131]\nv_mfma_f32_16x16x32_bf16 v[148:151], v[92:95], v[16:19], v[148:151]\n"
        "ds_read_b128 v[16:19], %[mra] offset:256\ns_waitcnt lgkmcnt(8)\nv_mfma_f32_16x16x32_bf16 v[112:115], v[84:87], v[20:23], v[112:115]\n"
        "v_mfma_f32_16x16x32_bf16 v[132:135], v[88:91], v[20:23], v[132:135]\nv_mfma_f32_16x16x32_bf16 v[152:155], v[92:95], v[20:23], v[152:155]\nds_read_b128 v[20:23], %[mra] offset:512\n"
        "s_setprio %[pfs]\ns_waitcnt lgkmcnt(1)\n.long 0xd3b04060, 0x1d823120\n.long 0xd3b04062, 0x1d8a3122\n.long 0xd3b04074, 0x1dd23124\n.long 0xd3b04076, 0x1dda3126\n.long 0xd3b14060, 0x18023560\n"
        ".long 0xd3b14062, 0x18023562\n.long 0xd3b04064, 0x1d923920\n.long 0xd3b04066, 0x1d9a3922\n.long 0xd3b14074, 0x18023574\n.long 0xd3b14076, 0x18023576\nv_cvt_pk_bf16_f32 v96, v96, v97\n"
        "v_cvt_pk_bf16_f32 v97, v98, v99\n.long 0xd3b04078, 0x1de23924\n.long 0xd3b0407a, 0x1dea3926\n.long 0xd3b14064, 0x18023d64\n.long 0xd3b14066, 0x18023d66\nv_cvt_pk_bf16_f32 v116, v116, v117\n"
        "v_cvt_pk_bf16_f32 v117, v118, v119\nds_write_b64 %[sta], v[96:97] offset:24032\n.long 0xd3b04068, 0x1da21924\n.long 0xd3b0406a, 0x1daa1926\n.long 0xd3b14078, 0x18023d78\n"
        ".long 0xd3b1407a, 0x18023d7a\nv_cvt_pk_bf16_f32 v100, v100, v101\nv_cvt_pk_bf16_f32 v101, v102, v103\nds_write_b64 %[sta], v[116:117] offset:23936\n.long 0xd3b0406c, 0x1db22124\n"
        ".long 0xd3b0406e, 0x1dba2126\n.long 0xd3b14068, 0x18021d68\n.long 0xd3b1406a, 0x18021d6a\nv_cvt_pk_bf16_f32 v120, v120, v121\nv_cvt_pk_bf16_f32 v121, v122, v123\n"
        "ds_write_b64 %[sta], v[100:101] offset:31968\ns_waitcnt lgkmcnt(3)\n.long 0xd3b04070, 0x1dc22924\n.long 0xd3b04072, 0x1dca2926\n.long 0xd3b1406c, 0x1802256c\n.long 0xd3b1406e, 0x1802256e\n"
        "v_cvt_pk_bf16_f32 v104, v104, v105\nv_cvt_pk_bf16_f32 v105, v106, v107\nds_write_b64 %[sta], v[120:121] offset:31872\n.long 0xd3b14070, 0x18022d70\n.long 0xd3b14072, 0x18022d72\n"
        "v_cvt_pk_bf16_f32 v108, v108, v109\nv_cvt_pk_bf16_f32 v109, v110, v111\nds_write_b64 %[sta], v[104:105] offset:128\nv_cvt_pk_bf16_f32 v112, v112, v113\nv_cvt_pk_bf16_f32 v113, v114, v115\n"
        "ds_write_b64 %[sta], v[108:109] offset:8064\nds_write_b64 %[sta], v[112:113] offset:16000\nds_write_b32 %[pg], %[sy]\nv_add_u32 %[sy], 4, %[sy]\n.long 0xd3b04088, 0x1e223128\n"
        ".long 0xd3b0408a, 0x1e2a312a\n.long 0xd3b0409c, 0x1e72312c\n.long 0xd3b0409e, 0x1e7a312e\n.long 0xd3b14088, 0x18023588\n.long 0xd3b1408a, 0x1802358a\n.long 0xd3b0408c, 0x1e323928\n"
        ".long 0xd3b0408e, 0x1e3a392a\n.long 0xd3b1409c, 0x1802359c\n.long 0xd3b1409e, 0x1802359e\nv_cvt_pk_bf16_f32 v136, v136, v137\nv_cvt_pk_bf16_f32 v137, v138, v139\n.long 0xd3b040a0, 0x1e82392c\n"
        ".long 0xd3b040a2, 0x1e8a392e\n.long 0xd3b1408c, 0x18023d8c\n.long 0xd3b1408e, 0x18023d8e\nv_cvt_pk_bf16_f32 v156, v156, v157\nv_cvt_pk_bf16_f32 v157, v158, v159\n"
        "ds_write_b64 %[sta], v[136:137] offset:24224\n.long 0xd3b0407c, 0x1df21928\n.long 0xd3b0407e, 0x1dfa192a\n.long 0xd3b140a0, 0x18023da0\n.long 0xd3b140a2, 0x18023da2\n"
        "v_cvt_pk_bf16_f32 v140, v140, v141\nv_cvt_pk_bf16_f32 v141, v142, v143\nds_write_b64 %[sta], v[156:157] offset:24256\n.long 0xd3b04090, 0x1e42192c\n.long 0xd3b04092, 0x1e4a192e\n"
        ".long 0xd3b1407c, 0x18021d7c\n.long 0xd3b1407e, 0x18021d7e\nv_cvt_pk_bf16_f32 v160, v160, v161\nv_cvt_pk_bf16_f32 v161, v162, v163\nds_write_b64 %[sta], v[140:141] offset:32160\n"
        ".long 0xd3b04080, 0x1e022128\n.long 0xd3b04082, 0x1e0a212a\n.long 0xd3b14090, 0x18021d90\n.long 0xd3b14092, 0x18021d92\nv_cvt_pk_bf16_f32 v124, v124, v125\nv_cvt_pk_bf16_f32 v125, v126, v127\n"
        "ds_write_b64 %[sta], v[160:161] offset:32192\n.long 0xd3b04094, 0x1e52212c\n.long 0xd3b04096, 0x1e5a212e\n.long 0xd3b14080, 0x18022580\n.long 0xd3b14082, 0x18022582\n"
        "v_cvt_pk_bf16_f32 v144, v144, v145\nv_cvt_pk_bf16_f32 v145, v146, v147\nds_write_b64 %[sta], v[124:125] offset:416\n.long 0xd3b04084, 0x1e122928\n.long 0xd3b04086, 0x1e1a292a\n"
        ".long 0xd3b14094, 0x18022594\n.long 0xd3b14096, 0x18022596\nv_cvt_pk_bf16_f32 v128, v128, v129\nv_cvt_pk_bf16_f32 v129, v130, v131\nds_write_b64 %[sta], v[144:145] offset:448\n"
        ".long 0xd3b04098, 0x1e62292c\n.long 0xd3b0409a, 0x1e6a292e\n.long 0xd3b14084, 0x18022d84\n.long 0xd3b14086, 0x18022d86\nv_cvt_pk_bf16_f32 v148, v148, v149\nv_cvt_pk_bf16_f32 v149, v150, v151\n"
        "ds_write_b64 %[sta], v[128:129] offset:8352\n.long 0xd3b14098, 0x18022d98\n.long 0xd3b1409a, 0x18022d9a\nv_cvt_pk_bf16_f32 v132, v132, v133\nv_cvt_pk_bf16_f32 v133, v134, v135\n"
        "ds_write_b64 %[sta], v[148:149] offset:8384\nv_cvt_pk_bf16_f32 v152, v152, v153\nv_cvt_pk_bf16_f32 v153, v154, v155\nds_write_b64 %[sta], v[132:133] offset:16288\n"
        "ds_write_b64 %[sta], v[152:153] offset:16320\nds_write_b32 %[pg], %[sy]\ns_branch .LE%=\n.LC1s0_%=:\ns_mov_b32 %[s1], 0x100000\n.LC1p0_%=:\nds_read_b32 %[pv], %[sy] offset:4\n"
        "s_waitcnt lgkmcnt(0)\nv_readfirstlane_b32 %[s0], %[pv]\ns_nop 0\ns_cmp_ge_i32 %[s0], 6\ns_cbranch_scc1 .LC1r0_%=\ns_sleep 1\ns_sub_u32 %[s1], %[s1], 1\ns_cbranch_scc0 .LC1p0_%=\n"
        "s_branch .LC1r0_%=\n.LC1s1_%=:\ns_mov_b32 %[s1], 0x100000\n.LC1p1_%=:\nds_read_b32 %[pv], %[sy] offset:4\ns_waitcnt lgkmcnt(0)\nv_readfirstlane_b32 %[s0], %[pv]\ns_nop 0\n"
        "s_cmp_ge_i32 %[s0], 6\ns_cbranch_scc1 .LC1r1_%=\ns_sleep 1\ns_sub_u32 %[s1], %[s1], 1\ns_cbranch_scc0 .LC1p1_%=\ns_branch .LC1r1_%=\n.LC1s2_%=:\ns_mov_b32 %[s1], 0x100000\n.LC1p2_%=:\n"
        "ds_read_b32 %[pv], %[sy] offset:4\ns_waitcnt lgkmcnt(0)\nv_readfirstlane_b32 %[s0], %[pv]\ns_nop 0\ns_cmp_ge_i32 %[s0], 6\ns_cbranch_scc1 .LC1r2_%=\ns_sleep 1\ns_sub_u32 %[s1], %[s1], 1\n"
        "s_cbranch_scc0 .LC1p2_%=\ns_branch .LC1r2_%=\n.LC1t%=:\ns_mov_b32 %[s1], 0x100000\n.LC1q%=:\nds_read_b32 %[pv], %[sy] offset:4\ns_waitcnt lgkmcnt(0)\nv_readfirstlane_b32 %[s0], %[pv]\n"
        "s_nop 0\ns_cmp_ge_i32 %[s0], %[nst]\ns_cbranch_scc1 .LC1u%=\ns_sleep 1\ns_sub_u32 %[s1], %[s1], 1\ns_cbranch_scc0 .LC1q%=\ns_branch .LC1u%=\n.LE%=:\n"
        : [pv] "=&v"(pv), [xa] "+v"(xa), [sy] "+v"(sy), [so] "=&s"(so), [s0] "=&s"(s0_), [s1] "=&s"(s1_)
        : [wa] "v"(wa), [rs] "s"(wrsrc), [pg] "v"(pga), [cwa] "v"(cwa), [mra] "v"(mra), [sta] "v"(sta), [w] "s"(w), [ow] "i"(SY_W - SY_X), [pfs] "i"(PFS), [nst] "i"(NST1)
        : "memory", "scc", "v12", "v13", "v14", "v15", "v16", "v17", "v18", "v19", "v20", "v21", "v22", "v23", "v24", "v25", "v26", "v27", "v28", "v29", "v30", "v31", "v32", "v33", "v34", "v35", "v36", "v37", "v38", "v39", "v40", "v41", "v42", "v43", "v44", "v45", "v46", "v47", "v48", "v49", "v50", "v51", "v52", "v53", "v54", "v55", "v56", "v57", "v58", "v59", "v60", "v61", "v62", "v63", "v64", "v65", "v66", "v67", "v68", "v69", "v70", "v71", "v72", "v73", "v74", "v75", "v76", "v77", "v78", "v79", "v80", "v81", "v82", "v83", "v84", "v85", "v86", "v87", "v88", "v89", "v90", "v91", "v92", "v93", "v94", "v95", "v96", "v97", "v98", "v99", "v100", "v101", "v102", "v103", "v104", "v105", "v106", "v107", "v108", "v109", "v110", "v111", "v112", "v113", "v114", "v115", "v116", "v117", "v118", "v119", "v120", "v121", "v122", "v123", "v124", "v125", "v126", "v127", "v128", "v129", "v130", "v131", "v132", "v133", "v134", "v135", "v136", "v137", "v138", "v139", "v140", "v141", "v142", "v143", "v144", "v145", "v146", "v147", "v148", "v149", "v150", "v151", "v152", "v153", "v154", "v155", "v156", "v157", "v158", "v159", "v160", "v161", "v162", "v163", "v164", "v165", "v166", "v167");
    }
    }
    ST(5);
    LANE(lane);
    const int t = 64 * w + lane;
    if (!layer && w == 3) {
      int spins = 0;
      _Pragma("unroll 1")
      while (true) {
        asm volatile("" ::: "memory");
        u32 d = 0;
#pragma unroll
        for (int i = 0; i < 2; ++i) {
          const u32x4 f = __builtin_amdgcn_raw_buffer_load_b128(frs, lane * 16, FL_M2 + i * 1024, 16);
          d |= (f[0] ^ tg0) | (f[1] ^ tg1) | (f[2] ^ tg0) | (f[3] ^ tg1);
        }
        if (__builtin_amdgcn_ballot_w64(d != 0) == 0 || ++spins > GCAP) break;
        __builtin_amdgcn_s_sleep(8);
      }
      VMCNT(0);
      ST(8);
    }
    if (layer && b == 0 && w == 1) {
      if (lane == 0) __builtin_amdgcn_raw_buffer_store_b64((u32x2){tg0, tg1}, frs, 0, 0, 16);
    }
    ST_FLUSH(kid);
    return;
  }
  const int j = w - 4, grp = j >> 2, mt2 = j & 3, kh = grp, qt = mt2, tt = t - 256;
  u32 l16 = lane * 16;
  int xso = g == (l15 >> 2) ? (rb * 8 + hg) * 1024 + mt2 * 256 + l15 * 16 : -1;
  const u32 tq1 = tg0 + tg1;
  if (layer) {
    const __amdgpu_buffer_rsrc_t xrs = __builtin_amdgcn_make_buffer_rsrc((void*)(xt + (size_t)rb * 51200), 0, 102400, 0x00020000);
    if (grp) __builtin_amdgcn_s_barrier();
    int gp[5], gh[3];
#pragma unroll
    for (int i = 0; i < 5; ++i) { const int p = i < 4 ? 4 * (i >> 1) + (i & 1) + 2 * grp : 8 + grp; gp[i] = PAIR(p, rot) * 10240; }
    const int ho = (mt2 & 1) * 5120 + (rb ? 4096 : 0);
    gh[0] = (mt2 & 2 ? gp[1] : gp[0]) + ho; gh[1] = (mt2 & 2 ? gp[3] : gp[2]) + ho; gh[2] = mt2 & 2 ? 102400 : gp[4] + ho;
    const int ck = (lane & 7) ^ ((lane >> 3) & 6);
    u32 vm0 = l16 + mt2 * 1024, vm1 = vm0 + 5120;
    u32 lm0 = LDSA(At) + grp * 21120 + (1 + (ck >> 1)) * 1056 + (8 * mt2 + (lane >> 3)) * 32 + (ck & 1) * 16, lm1 = lm0 + 84480 - grp * 10560;
    u32 lh0 = LDSA(At) + j * 5280 + l16, lh1 = lh0 + 84480 - grp * 10560, sy0 = sya + 8 * grp, sy1 = sya + 32 + 4 * grp, syh = sy0 + 2 * (mt2 & 2), tm;
    u32 xsb = LDSA(At) + (mt2 + 1) * 1056 + (4 * g + ((lane >> 2) & 3)) * 32 + 8 * (lane & 3), xsa = xsb + (12 + (hg & 1)) * 5280;
    xsb += xs0 * 5280;
    const int ns = hg;
    const u64 m1 = 0xaaaaaaaaaaaaaaaaull, m2 = 0xccccccccccccccccull;
    int cap, s0_, s1_;
    __builtin_amdgcn_s_setprio(PTX);
    if (grp) __builtin_amdgcn_s_sleep(XSLEEP1); else __builtin_amdgcn_s_sleep(XSLEEP);
      asm volatile(
        "s_mov_b32 %[cap], 0x10000\nbuffer_load_dwordx4 v[132:135], %[vm0], %[xrs], %[gp0] offen sc1\nbuffer_load_dwordx4 v[136:139], %[vm1], %[xrs], %[gp0] offen sc1\n"
        "buffer_load_dwordx4 v[140:143], %[l16], %[xrs], %[gh0] offen sc1\nbuffer_load_dwordx4 v[144:147], %[vm0], %[xrs], %[gp1] offen sc1\ns_cmp_lt_u32 %[j], 4\ns_cbranch_scc0 .LP0_%=\ns_barrier\n"
        ".LP0_%=:\ns_waitcnt vmcnt(3)\nv_max3_u32 %[tm], v132, v133, v134\nv_max_u32 %[tm], %[tm], v135\nv_cmp_eq_u32 vcc, -1, %[tm]\ns_cbranch_vccnz .LR0_%=\n.LK0_%=:\n"
        "ds_write_b128 %[lm0], v[132:135] offset:0\nbuffer_load_dwordx4 v[148:151], %[vm1], %[xrs], %[gp1] offen sc1\n.LP1_%=:\ns_waitcnt vmcnt(3)\nv_max3_u32 %[tm], v136, v137, v138\n"
        "v_max_u32 %[tm], %[tm], v139\nv_cmp_eq_u32 vcc, -1, %[tm]\ns_cbranch_vccnz .LR1_%=\n.LK1_%=:\nds_write_b128 %[lm0], v[136:139] offset:5280\ns_mov_b64 exec, 1\n"
        "ds_add_u32 %[sy0], %[one] offset:%[ox]+0\ns_mov_b64 exec, -1\nbuffer_load_dwordx4 v[132:135], %[vm0], %[xrs], %[gp2] offen sc1\n.LP2_%=:\ns_waitcnt vmcnt(3)\n"
        "v_max3_u32 %[tm], v140, v141, v142\nv_max_u32 %[tm], %[tm], v143\nv_cmp_eq_u32 vcc, -1, %[tm]\ns_cbranch_vccnz .LR2_%=\n.LK2_%=:\nds_write_b128 %[lh0], v[140:143] offset:0\ns_mov_b64 exec, 1\n"
        "ds_add_u32 %[syh], %[one] offset:%[ox]+0\ns_mov_b64 exec, -1\nbuffer_load_dwordx4 v[136:139], %[vm1], %[xrs], %[gp2] offen sc1\n.LP3_%=:\ns_waitcnt vmcnt(3)\n"
        "v_max3_u32 %[tm], v144, v145, v146\nv_max_u32 %[tm], %[tm], v147\nv_cmp_eq_u32 vcc, -1, %[tm]\ns_cbranch_vccnz .LR3_%=\n.LK3_%=:\nds_write_b128 %[lm0], v[144:147] offset:10560\n"
        "buffer_load_dwordx4 v[140:143], %[l16], %[xrs], %[gh1] offen sc1\n.LP4_%=:\ns_waitcnt vmcnt(3)\nv_max3_u32 %[tm], v148, v149, v150\nv_max_u32 %[tm], %[tm], v151\nv_cmp_eq_u32 vcc, -1, %[tm]\n"
        "s_cbranch_vccnz .LR4_%=\n.LK4_%=:\nds_write_b128 %[lm0], v[148:151] offset:15840\ns_mov_b64 exec, 1\nds_add_u32 %[sy0], %[one] offset:%[ox]+4\ns_mov_b64 exec, -1\n"
        "buffer_load_dwordx4 v[144:147], %[vm0], %[xrs], %[gp3] offen sc1\n.LP5_%=:\ns_waitcnt vmcnt(3)\nv_max3_u32 %[tm], v132, v133, v134\nv_max_u32 %[tm], %[tm], v135\nv_cmp_eq_u32 vcc, -1, %[tm]\n"
        "s_cbranch_vccnz .LR5_%=\n.LK5_%=:\nds_write_b128 %[lm0], v[132:135] offset:42240\nbuffer_load_dwordx4 v[148:151], %[vm1], %[xrs], %[gp3] offen sc1\n.LP6_%=:\ns_waitcnt vmcnt(3)\n"
        "v_max3_u32 %[tm], v136, v137, v138\nv_max_u32 %[tm], %[tm], v139\nv_cmp_eq_u32 vcc, -1, %[tm]\ns_cbranch_vccnz .LR6_%=\n.LK6_%=:\nds_write_b128 %[lm0], v[136:139] offset:47520\n"
        "s_mov_b64 exec, 1\nds_add_u32 %[sy0], %[one] offset:%[ox]+16\ns_mov_b64 exec, -1\nbuffer_load_dwordx4 v[132:135], %[vm0], %[xrs], %[gp4] offen sc1\n.LP7_%=:\ns_waitcnt vmcnt(3)\n"
        "v_max3_u32 %[tm], v140, v141, v142\nv_max_u32 %[tm], %[tm], v143\nv_cmp_eq_u32 vcc, -1, %[tm]\ns_cbranch_vccnz .LR7_%=\n.LK7_%=:\nds_write_b128 %[lh0], v[140:143] offset:42240\n"
        "s_mov_b64 exec, 1\nds_add_u32 %[syh], %[one] offset:%[ox]+16\ns_mov_b64 exec, -1\nbuffer_load_dwordx4 v[136:139], %[vm1], %[xrs], %[gp4] offen sc1\n.LP8_%=:\ns_waitcnt vmcnt(3)\n"
        "v_max3_u32 %[tm], v144, v145, v146\nv_max_u32 %[tm], %[tm], v147\nv_cmp_eq_u32 vcc, -1, %[tm]\ns_cbranch_vccnz .LR8_%=\n.LK8_%=:\nds_write_b128 %[lm0], v[144:147] offset:52800\n"
        "buffer_load_dwordx4 v[140:143], %[l16], %[xrs], %[gh2] offen sc1\ns_cmp_lt_u32 %[j], 4\ns_cbranch_scc1 .LT%=\ns_mov_b32 %[s1], 0x100000\n.LU%=:\n"
        "ds_read_b128 v[160:163], %[sy0] offset:%[ox]-8\nds_read_b128 v[164:167], %[sy0] offset:%[ox]+8\nds_read_b64_tr_b16 v[152:153], %[xsa]\nds_read_b64_tr_b16 v[154:155], %[xsa] offset:512\n"
        "ds_read_b64_tr_b16 v[156:157], %[xsb]\nds_read_b64_tr_b16 v[158:159], %[xsb] offset:512\ns_waitcnt lgkmcnt(4)\nv_min3_u32 v160, v160, v161, v162\nv_min3_u32 v164, v164, v165, v166\n"
        "v_min3_u32 v160, v160, v163, v164\ns_nop 0\nv_readfirstlane_b32 %[s0], v160\ns_waitcnt lgkmcnt(0)\ns_cmp_ge_i32 %[s0], 6\ns_cbranch_scc1 .LV%=\ns_sleep 1\ns_sub_u32 %[s1], %[s1], 1\n"
        "s_cbranch_scc0 .LU%=\n.LV%=:\nv_mfma_f32_16x16x32_bf16 v[160:163], %[ones], v[152:155], 0\nv_mfma_f32_16x16x32_bf16 v[164:167], v[152:155], v[152:155], 0\n"
        "ds_read_b64_tr_b16 v[152:153], %[xsb] offset:5280\nds_read_b64_tr_b16 v[154:155], %[xsb] offset:5792\ns_bitcmp1_b32 %[ns], 0\n"
        "v_mfma_f32_16x16x32_bf16 v[160:163], %[ones], v[156:159], v[160:163]\nv_mfma_f32_16x16x32_bf16 v[164:167], v[156:159], v[156:159], v[164:167]\ns_waitcnt lgkmcnt(0)\ns_cbranch_scc1 .LW%=\n"
        "v_mfma_f32_16x16x32_bf16 v[160:163], %[ones], v[152:155], v[160:163]\nv_mfma_f32_16x16x32_bf16 v[164:167], v[152:155], v[152:155], v[164:167]\n.LW%=:\ns_nop 7\n"
        "v_cndmask_b32 v164, v164, v165, %[m1]\nv_cndmask_b32 v166, v166, v167, %[m1]\nv_mov_b32 v161, %[tg0]\nv_mov_b32 v163, %[tq1]\nv_cndmask_b32 v162, v164, v166, %[m2]\n"
        "buffer_store_dwordx4 v[160:163], %[xso], %[srs], 0 offen sc1\n.LT%=:\n.LP9_%=:\ns_waitcnt vmcnt(3)\nv_max3_u32 %[tm], v148, v149, v150\nv_max_u32 %[tm], %[tm], v151\n"
        "v_cmp_eq_u32 vcc, -1, %[tm]\ns_cbranch_vccnz .LR9_%=\n.LK9_%=:\nds_write_b128 %[lm0], v[148:151] offset:58080\ns_mov_b64 exec, 1\nds_add_u32 %[sy0], %[one] offset:%[ox]+20\n"
        "s_mov_b64 exec, -1\n.LP10_%=:\ns_waitcnt vmcnt(2)\nv_max3_u32 %[tm], v132, v133, v134\nv_max_u32 %[tm], %[tm], v135\nv_cmp_eq_u32 vcc, -1, %[tm]\ns_cbranch_vccnz .LR10_%=\n.LK10_%=:\n"
        "ds_write_b128 %[lm1], v[132:135] offset:0\n.LP11_%=:\ns_waitcnt vmcnt(1)\nv_max3_u32 %[tm], v136, v137, v138\nv_max_u32 %[tm], %[tm], v139\nv_cmp_eq_u32 vcc, -1, %[tm]\n"
        "s_cbranch_vccnz .LR11_%=\n.LK11_%=:\nds_write_b128 %[lm1], v[136:139] offset:5280\ns_mov_b64 exec, 1\nds_add_u32 %[sy1], %[one] offset:%[ox]+0\ns_mov_b64 exec, -1\n.LP12_%=:\n"
        "s_waitcnt vmcnt(0)\nv_max3_u32 %[tm], v140, v141, v142\nv_max_u32 %[tm], %[tm], v143\nv_cmp_eq_u32 vcc, -1, %[tm]\ns_cbranch_vccnz .LR12_%=\n.LK12_%=:\ns_bitcmp1_b32 %[j], 1\n"
        "s_cbranch_scc1 .LE%=\nds_write_b128 %[lh1], v[140:143] offset:0\ns_mov_b64 exec, 1\nds_add_u32 %[sy1], %[one] offset:%[ox]\ns_mov_b64 exec, -1\ns_branch .LE%=\n.LR0_%=:\n"
        "s_sub_u32 %[cap], %[cap], 1\ns_cbranch_scc0 .LQ0_%=\ns_mov_b32 %[cap], 0\ns_branch .LK0_%=\n.LQ0_%=:\ns_sleep %[xp]\nbuffer_load_dwordx4 v[132:135], %[vm0], %[xrs], %[gp0] offen sc1\n"
        "buffer_load_dwordx4 v[136:139], %[vm1], %[xrs], %[gp0] offen sc1\nbuffer_load_dwordx4 v[140:143], %[l16], %[xrs], %[gh0] offen sc1\n"
        "buffer_load_dwordx4 v[144:147], %[vm0], %[xrs], %[gp1] offen sc1\ns_branch .LP0_%=\n.LR1_%=:\ns_sub_u32 %[cap], %[cap], 1\ns_cbranch_scc0 .LQ1_%=\ns_mov_b32 %[cap], 0\ns_branch .LK1_%=\n"
        ".LQ1_%=:\ns_sleep %[xp]\nbuffer_load_dwordx4 v[136:139], %[vm1], %[xrs], %[gp0] offen sc1\nbuffer_load_dwordx4 v[140:143], %[l16], %[xrs], %[gh0] offen sc1\n"
        "buffer_load_dwordx4 v[144:147], %[vm0], %[xrs], %[gp1] offen sc1\nbuffer_load_dwordx4 v[148:151], %[vm1], %[xrs], %[gp1] offen sc1\ns_branch .LP1_%=\n.LR2_%=:\ns_sub_u32 %[cap], %[cap], 1\n"
        "s_cbranch_scc0 .LQ2_%=\ns_mov_b32 %[cap], 0\ns_branch .LK2_%=\n.LQ2_%=:\ns_sleep %[xp]\nbuffer_load_dwordx4 v[140:143], %[l16], %[xrs], %[gh0] offen sc1\n"
        "buffer_load_dwordx4 v[144:147], %[vm0], %[xrs], %[gp1] offen sc1\nbuffer_load_dwordx4 v[148:151], %[vm1], %[xrs], %[gp1] offen sc1\n"
        "buffer_load_dwordx4 v[132:135], %[vm0], %[xrs], %[gp2] offen sc1\ns_branch .LP2_%=\n.LR3_%=:\ns_sub_u32 %[cap], %[cap], 1\ns_cbranch_scc0 .LQ3_%=\ns_mov_b32 %[cap], 0\ns_branch .LK3_%=\n"
        ".LQ3_%=:\ns_sleep %[xp]\nbuffer_load_dwordx4 v[144:147], %[vm0], %[xrs], %[gp1] offen sc1\nbuffer_load_dwordx4 v[148:151], %[vm1], %[xrs], %[gp1] offen sc1\n"
        "buffer_load_dwordx4 v[132:135], %[vm0], %[xrs], %[gp2] offen sc1\nbuffer_load_dwordx4 v[136:139], %[vm1], %[xrs], %[gp2] offen sc1\ns_branch .LP3_%=\n.LR4_%=:\ns_sub_u32 %[cap], %[cap], 1\n"
        "s_cbranch_scc0 .LQ4_%=\ns_mov_b32 %[cap], 0\ns_branch .LK4_%=\n.LQ4_%=:\ns_sleep %[xp]\nbuffer_load_dwordx4 v[148:151], %[vm1], %[xrs], %[gp1] offen sc1\n"
        "buffer_load_dwordx4 v[132:135], %[vm0], %[xrs], %[gp2] offen sc1\nbuffer_load_dwordx4 v[136:139], %[vm1], %[xrs], %[gp2] offen sc1\n"
        "buffer_load_dwordx4 v[140:143], %[l16], %[xrs], %[gh1] offen sc1\ns_branch .LP4_%=\n.LR5_%=:\ns_sub_u32 %[cap], %[cap], 1\ns_cbranch_scc0 .LQ5_%=\ns_mov_b32 %[cap], 0\ns_branch .LK5_%=\n"
        ".LQ5_%=:\ns_sleep %[xp]\nbuffer_load_dwordx4 v[132:135], %[vm0], %[xrs], %[gp2] offen sc1\nbuffer_load_dwordx4 v[136:139], %[vm1], %[xrs], %[gp2] offen sc1\n"
        "buffer_load_dwordx4 v[140:143], %[l16], %[xrs], %[gh1] offen sc1\nbuffer_load_dwordx4 v[144:147], %[vm0], %[xrs], %[gp3] offen sc1\ns_branch .LP5_%=\n.LR6_%=:\ns_sub_u32 %[cap], %[cap], 1\n"
        "s_cbranch_scc0 .LQ6_%=\ns_mov_b32 %[cap], 0\ns_branch .LK6_%=\n.LQ6_%=:\ns_sleep %[xp]\nbuffer_load_dwordx4 v[136:139], %[vm1], %[xrs], %[gp2] offen sc1\n"
        "buffer_load_dwordx4 v[140:143], %[l16], %[xrs], %[gh1] offen sc1\nbuffer_load_dwordx4 v[144:147], %[vm0], %[xrs], %[gp3] offen sc1\n"
        "buffer_load_dwordx4 v[148:151], %[vm1], %[xrs], %[gp3] offen sc1\ns_branch .LP6_%=\n.LR7_%=:\ns_sub_u32 %[cap], %[cap], 1\ns_cbranch_scc0 .LQ7_%=\ns_mov_b32 %[cap], 0\ns_branch .LK7_%=\n"
        ".LQ7_%=:\ns_sleep %[xp]\nbuffer_load_dwordx4 v[140:143], %[l16], %[xrs], %[gh1] offen sc1\nbuffer_load_dwordx4 v[144:147], %[vm0], %[xrs], %[gp3] offen sc1\n"
        "buffer_load_dwordx4 v[148:151], %[vm1], %[xrs], %[gp3] offen sc1\nbuffer_load_dwordx4 v[132:135], %[vm0], %[xrs], %[gp4] offen sc1\ns_branch .LP7_%=\n.LR8_%=:\ns_sub_u32 %[cap], %[cap], 1\n"
        "s_cbranch_scc0 .LQ8_%=\ns_mov_b32 %[cap], 0\ns_branch .LK8_%=\n.LQ8_%=:\ns_sleep %[xp]\nbuffer_load_dwordx4 v[144:147], %[vm0], %[xrs], %[gp3] offen sc1\n"
        "buffer_load_dwordx4 v[148:151], %[vm1], %[xrs], %[gp3] offen sc1\nbuffer_load_dwordx4 v[132:135], %[vm0], %[xrs], %[gp4] offen sc1\n"
        "buffer_load_dwordx4 v[136:139], %[vm1], %[xrs], %[gp4] offen sc1\ns_branch .LP8_%=\n.LR9_%=:\ns_sub_u32 %[cap], %[cap], 1\ns_cbranch_scc0 .LQ9_%=\ns_mov_b32 %[cap], 0\ns_branch .LK9_%=\n"
        ".LQ9_%=:\ns_sleep %[xp]\nbuffer_load_dwordx4 v[148:151], %[vm1], %[xrs], %[gp3] offen sc1\nbuffer_load_dwordx4 v[132:135], %[vm0], %[xrs], %[gp4] offen sc1\n"
        "buffer_load_dwordx4 v[136:139], %[vm1], %[xrs], %[gp4] offen sc1\nbuffer_load_dwordx4 v[140:143], %[l16], %[xrs], %[gh2] offen sc1\ns_branch .LP9_%=\n.LR10_%=:\ns_sub_u32 %[cap], %[cap], 1\n"
        "s_cbranch_scc0 .LQ10_%=\ns_mov_b32 %[cap], 0\ns_branch .LK10_%=\n.LQ10_%=:\ns_sleep %[xp]\nbuffer_load_dwordx4 v[132:135], %[vm0], %[xrs], %[gp4] offen sc1\n"
        "buffer_load_dwordx4 v[136:139], %[vm1], %[xrs], %[gp4] offen sc1\nbuffer_load_dwordx4 v[140:143], %[l16], %[xrs], %[gh2] offen sc1\ns_branch .LP10_%=\n.LR11_%=:\ns_sub_u32 %[cap], %[cap], 1\n"
        "s_cbranch_scc0 .LQ11_%=\ns_mov_b32 %[cap], 0\ns_branch .LK11_%=\n.LQ11_%=:\ns_sleep %[xp]\nbuffer_load_dwordx4 v[136:139], %[vm1], %[xrs], %[gp4] offen sc1\n"
        "buffer_load_dwordx4 v[140:143], %[l16], %[xrs], %[gh2] offen sc1\ns_branch .LP11_%=\n.LR12_%=:\ns_sub_u32 %[cap], %[cap], 1\ns_cbranch_scc0 .LQ12_%=\ns_mov_b32 %[cap], 0\ns_branch .LK12_%=\n"
        ".LQ12_%=:\ns_sleep %[xp]\nbuffer_load_dwordx4 v[140:143], %[l16], %[xrs], %[gh2] offen sc1\ns_branch .LP12_%=\n.LE%=:\n"
        : [tm] "=&v"(tm), [cap] "=&s"(cap), [s0] "=&s"(s0_), [s1] "=&s"(s1_)
        : [l16] "v"(l16), [vm0] "v"(vm0), [vm1] "v"(vm1), [xrs] "s"(xrs), [gp0] "s"(gp[0]), [gp1] "s"(gp[1]), [gp2] "s"(gp[2]), [gp3] "s"(gp[3]), [gp4] "s"(gp[4]), [gh0] "s"(gh[0]), [gh1] "s"(gh[1]), [gh2] "s"(gh[2]), [lm0] "v"(lm0), [lm1] "v"(lm1), [lh0] "v"(lh0), [lh1] "v"(lh1), [sy0] "v"(sy0), [sy1] "v"(sy1), [syh] "v"(syh), [xsb] "v"(xsb), [one] "v"(one), [j] "s"(j), [xsa] "v"(xsa), [ns] "s"(ns), [ones] "v"(onesf), [m1] "s"(m1), [m2] "s"(m2), [xso] "v"(xso), [srs] "s"(xsrs), [tg0] "s"(tg0), [tq1] "s"(tq1), [ox] "i"(SY_X), [xp] "i"(XPOLL)
        : "memory", "scc", "vcc", "v132", "v133", "v134", "v135", "v136", "v137", "v138", "v139", "v140", "v141", "v142", "v143", "v144", "v145", "v146", "v147", "v148", "v149", "v150", "v151", "v152", "v153", "v154", "v155", "v156", "v157", "v158", "v159", "v160", "v161", "v162", "v163", "v164", "v165", "v166", "v167");
    RETRY(8, 0x10000 - cap);
    ST(3);
  } else {
    ST(5);
#define MARKS \
    if (w == MKW) { \
      const __amdgpu_buffer_rsrc_t mrs = __builtin_amdgcn_make_buffer_rsrc(obuf, 0, HO_BYTES, 0x00020000); \
      const __amdgpu_buffer_rsrc_t irs = __builtin_amdgcn_make_buffer_rsrc(ws + WS_IM1 + b * 12800, 0, 12800, 0x00020000); \
      __builtin_amdgcn_raw_buffer_store_b128(MARK16, mrs, lane < 16 ? lane * 4096 : -1, HO_FS + (s0 + 8 * hg) * 2, 17); \
      _Pragma("unroll 1") \
      for (int i = 0; i < 10; ++i) { \
        __builtin_amdgcn_raw_buffer_store_b128(MARK16, mrs, (lane >> 3) * (XP * 2) + (lane & 7) * 16, HO_X1 + (hg * 80 + 8 * i) * (XP * 2) + s0 * 2, 17); \
        __builtin_amdgcn_s_sleep(MSL1); \
      } \
      _Pragma("unroll 1") \
      for (int i = 0; i < 13; ++i) { \
        __builtin_amdgcn_raw_buffer_store_b128(MARK16, irs, lane * 16, i * 1024, 17); \
        __builtin_amdgcn_s_sleep(MSL2); \
      } \
      VMCNT(0); \
      if (lane == 0) __builtin_amdgcn_raw_buffer_store_b64((u32x2){tg0, tg1}, frs, 0, FL_M2 + b * 8, 16); \
      ST(1); \
    } \

    if (MKPOS == 0) MARKS
    lds_poll<SY_X, 2>(sya, 3);
    if (j == 0 ? MKPOS : j == 4 ? !MKPOS : j < 4) {
      const u32 xa = LDSA(At) + (2 * mt2 + 2 + (l15 >> 3)) * 1024 + (l15 & 7) * 128 + ((g ^ (l15 & 6)) << 4);
      bf16x8 fv[3];
      asm volatile("ds_read_b128 %0, %3 offset:61440\n\tds_read_b128 %1, %4\n\tds_read_b128 %2, %5\n\ts_waitcnt lgkmcnt(0)" : "=&v"(fv[0]), "=&v"(fv[1]), "=&v"(fv[2])
                   : "v"(xa ^ (64 * (hg & 1))), "v"((xa ^ (64 * (xs0 & 1))) + (xs0 >> 1) * 10240), "v"((xa ^ (64 * (~xs0 & 1))) + ((xs0 + 1) >> 1) * 10240) : "memory");
      f32x4 s1 = {0.f, 0.f, 0.f, 0.f}, s2 = s1;
#pragma unroll
      for (int q = 0; q < 3; ++q) {
        if (q < 2 || !(hg & 1)) {
          s1 = __builtin_amdgcn_mfma_f32_16x16x32_bf16(onesf, fv[q], s1, 0, 0, 0);
          s2 = __builtin_amdgcn_mfma_f32_16x16x32_bf16(fv[q], fv[q], s2, 0, 0, 0);
        }
      }
      const float d2 = (l15 & 2) ? ((l15 & 1) ? s2[3] : s2[2]) : ((l15 & 1) ? s2[1] : s2[0]);
      __builtin_amdgcn_raw_buffer_store_b128((u32x4){__float_as_uint(s1[0]), tg0, __float_as_uint(d2), tq1}, xsrs, xso, 0, 16);
    }
  }
  __builtin_amdgcn_s_setprio(PTW);
  if (!layer && MKPOS == 1) MARKS
  if (layer) lds_inc<SY_PART>(sya, one);
  const __amdgpu_buffer_rsrc_t orsrc = __builtin_amdgcn_make_buffer_rsrc(obuf + (size_t)rb * 40960, 0, 81920, 0x00020000);
  u32 qka = LDSA(St) + ((16 * qt + l15) * X1_P + 160 * grp + 8 * g) * 2;
  u32 vta = LDSA(St) + ((16 * qt + 4 * g + ((lane >> 2) & 3)) * X1_P + 160 * grp + 40 + 4 * (lane & 3)) * 2;
  const bool par = l15 & 1;
  const int kk0 = 4 * g + (l15 & 1), kk1 = kk0 + 2;
  const bool c0 = kk0 >= l15 + 2, c1 = kk1 >= l15 + 2;
  const u32 e0 = par ? 0x01000c0cu : 0x0c0c0100u, e1 = par ? 0x03020c0cu : 0x0c0c0302u;
  u32 sA0 = c0 ? e0 : 0x0c0c0c0cu, sB0 = c0 ? 0x0c0c0c0cu : e0;
  u32 sA1 = c1 ? e1 : 0x0c0c0c0cu, sB1 = c1 ? 0x0c0c0c0cu : e1;
  u32 oha = g * 10240 + l15 * 16, qa[4];
#pragma unroll
  for (int i = 0; i < 4; ++i) { qa[i] = g >= 3 - (i & 1) ? sya + SY_Z : qka + (32 * X1_P + 160 - 240 * grp) + 40 * (i >> 1) + 8 * (i & 1); PIN(qa[i]); }
  const float nb = s0 + qt ? 0.f : -1e30f;
  f32x4 bias = {nb, nb, nb, nb};
  int ohs = g ? -1 : l16;
  u64 c0m = __builtin_amdgcn_ballot_w64(c0), c1m = __builtin_amdgcn_ballot_w64(c1);
  int so0 = (hg * 4 + qt) * 2560 + grp * 1024, so1 = (hg * 4 + qt) * 2560 + 2048 + grp * 256;
  float nc = -0.22360679774997896f * 1.4426950408889634f;
  asm volatile("" : "+s"(c0m), "+s"(c1m), "+s"(so0), "+s"(so1), "+s"(nc));
  PIN(qka); PIN(vta); PIN(sA0); PIN(sA1); PIN(sB0); PIN(sB1); PIN(l16); PIN(oha); PIN(sya); PIN(one); PIN(bias); PIN(ohs);
  u32 rw = LDSA(smem) + A_R1 + j * 3072 + l16, fw = sya + SY_RED + 4 * j, f0 = 1 - kh;
  u32 xo_ = LDSA(smem + X1T_OFF) + (32 * kh + l15) * 144 + 32 * mt2 + 8 * g;
  u32 xi_ = LDSA(smem + X1T_OFF) + (tt >> 3) * 144 + (tt & 7) * 16;
  const __amdgpu_buffer_rsrc_t x1rs = __builtin_amdgcn_make_buffer_rsrc(xtout + (size_t)hg * 80 * XP + s0, 0, 80 * XP * 2, 0x00020000);
  int xg_ = (tt >> 3) * (XP * 2) + (tt & 7) * 16;
  const __amdgpu_buffer_rsrc_t srsrc = __builtin_amdgcn_make_buffer_rsrc(stats + (size_t)s0 * 16, 0, 8192, 0x00020000);
  u32 rr = LDSA(smem) + A_R1 + (j ^ 4) * 3072 + l16 - 1024 * kh;
  int so_ = (l15 & 3) ? -1 : (lane >> 2) * 8;
  u32 bb = LDSA(At) + kh * 25600 + l16, fr = fw ^ 16, wd = LDSA(At) + j * 1024 + l16, ra[3];
  {
    const int rr_ = 4 * (g & 1) + ((lane >> 2) & 3), pr_ = 4 * (l15 >> 3) + (l15 & 3) + 4 * (l15 & 4);
    const u32 ro = LDSA(At) + (layer ? (mt2 + 1) * 1056 + pr_ * 32 + 8 * g : (2 * mt2 + 2 + (g >> 1)) * 1024 + rr_ * 128 + ((rr_ & 6) << 4) + (lane & 3) * 8);
#pragma unroll
    for (int i = 0; i < 3; ++i) {
      const int kc = hg * 80 + 16 * (2 * kh + i), kr = 64 * PAIR(kc >> 6, -rot) + (kc & 63);
      ra[i] = layer ? ro + (kr >> 5) * 5280 + (kr & 16) * 16 : (ro ^ ((kr & 48) * 2)) + (kr >> 6) * 10240;
      if (i == 0 && kh) ra[i] = sya + SY_Z;
      PIN(ra[i]);
    }
  }
  PIN(rw); PIN(fw); PIN(f0); PIN(xo_); PIN(xi_); PIN(xg_); PIN(rr); PIN(so_); PIN(bb); PIN(fr); PIN(wd);
  STX(8);
  STX(10);
    if (!layer) {
    if (MKPOS == 2) MARKS
      if (w == CWW) {
        const __amdgpu_buffer_rsrc_t crs = __builtin_amdgcn_make_buffer_rsrc(ws + WS_CW + hg * 20480, 0, (EXP & 4) ? 0 : 20480, 0x00020000);
        pg_poll<4>(sya, LDSA(smem) + SY_OFF + SY_X + CWAT);
        __builtin_amdgcn_s_setprio(PCW);
        f32x4 cv[20];
#pragma unroll
        for (int i = 0; i < 20; ++i) cv[i] = __builtin_bit_cast(f32x4, __builtin_amdgcn_raw_buffer_load_b128(crs, min(lane, 59) * 16, i * 1024, 16));
#pragma unroll
        for (int i = 0; i < 10; ++i) cv[i] += cv[i + 10];
        const f32x4 cs = ((cv[0] + cv[1]) + (cv[2] + cv[3])) + ((cv[4] + cv[5]) + (cv[6] + cv[7])) + (cv[8] + cv[9]);
        asm volatile("ds_write_b128 %0, %1" :: "v"(LDSA(smem) + CW_OFF + lane * 16), "v"(cs) : "memory");
        lds_inc<SY_STAT>(sya, one);
        __builtin_amdgcn_s_setprio(PTW);
        ST(1);
      }
      if (w == 9) {
        const int fb = lane < 8 ? (b & 62) + (lane & 1) + 64 * (lane >> 1) : 256 + 16 * ((lane >> 1) & 7) + 2 * hg + (lane & 1);
        pg_poll<8>(sya, LDSA(smem) + SY_OFF + SY_X + MKAT);
        int spins = 0;
        _Pragma("unroll 1")
        while (true) {
          asm volatile("" ::: "memory");
          const u32x2 f = __builtin_amdgcn_raw_buffer_load_b64(frs, lane < 24 ? fb * 64 : b * 64, FL_M, 16);
          if (__builtin_amdgcn_ballot_w64(f[0] != tg0 || f[1] != tg1) == 0 || ++spins > GCAP) break;
          __builtin_amdgcn_s_sleep(8);
        }
        RETRY(15, spins);
        lds_inc<SY_STAT>(sya, one);
        lds_inc<SY_MK>(sya, one);
        ST(1);
      }
    }
    if (j == XJ0 || j == XJ1) {
      const int xo = j == XJ0 ? rb * 8192 + l16 : (rb ? rb - 1 : 0) * 8192 + 768 + l15 * 16;
      pg_poll<4>(sya, LDSA(smem) + SY_OFF + 256 * layer + SY_X + (layer ? XSAT : XSAT0));
      __builtin_amdgcn_s_sleep(layer ? XSSL : 0);
      __builtin_amdgcn_s_setprio(3);
      u32x4 pv[8];
      int spins = 0;
      _Pragma("unroll 1")
      while (true) {
        asm volatile("" ::: "memory");
        u32 m = 0;
#pragma unroll
        for (int i = 0; i < 8; ++i) pv[i] = __builtin_amdgcn_raw_buffer_load_b128(xsrs, xo, i * 1024, 16);
#pragma unroll
        for (int i = 0; i < 8; ++i) m |= (pv[i][1] ^ tg0) | (pv[i][3] ^ tq1);
        if (__builtin_amdgcn_ballot_w64(m != 0) == 0 || ++spins > GCAP) break;
        __builtin_amdgcn_s_sleep(XSRT);
      }
      RETRY(15, spins);
      float sv[2];
#pragma unroll
      for (int i = 0; i < 2; ++i)
        sv[i] = ((__uint_as_float(pv[0][2 * i]) + __uint_as_float(pv[1][2 * i])) + (__uint_as_float(pv[2][2 * i]) + __uint_as_float(pv[3][2 * i]))) +
                ((__uint_as_float(pv[4][2 * i]) + __uint_as_float(pv[5][2 * i])) + (__uint_as_float(pv[6][2 * i]) + __uint_as_float(pv[7][2 * i])));
      asm volatile("ds_write_b128 %0, %1" :: "v"(LDSA(smem) + A_R2 + (j == XJ0 ? 256 + l16 : l15 * 16)), "v"(ln_fin(sv[0], sv[1])) : "memory");
      lds_inc<SY_STAT>(sya, one);
      __builtin_amdgcn_s_setprio(PTW);
      ST(1);
    }
  u32x2 rv[3];
  if (layer) {
    lds_poll<SY_PART, 2>(sya, 8);
    asm volatile("ds_read_b64 %0, %3\n\tds_read_b64 %1, %4\n\tds_read_b64 %2, %5\n\ts_waitcnt lgkmcnt(0)" : "=&v"(rv[0]), "=&v"(rv[1]), "=&v"(rv[2]) : "v"(ra[0]), "v"(ra[1]), "v"(ra[2]) : "memory");
  } else
    asm volatile("ds_read_b64_tr_b16 %0, %3\n\tds_read_b64_tr_b16 %1, %4\n\tds_read_b64_tr_b16 %2, %5\n\ts_waitcnt lgkmcnt(0)" : "=&v"(rv[0]), "=&v"(rv[1]), "=&v"(rv[2]) : "v"(ra[0]), "v"(ra[1]), "v"(ra[2]) : "memory");
  lds_inc<SY_RV>(sya, one);
  STX(9);
  u32x4 wv[2][7];
  {
    const __amdgpu_buffer_rsrc_t wos = __builtin_amdgcn_make_buffer_rsrc((void*)(wot + (size_t)hg * 51200 + j * 512), 0, 102400 - j * 1024, 0x00020000);
    if (!layer) lds_poll<SY_MK, 2>(sya, 1);
    pg_poll<2>(sya, LDSA(smem) + SY_OFF + 256 * layer + SY_X + WOAT);
    __builtin_amdgcn_s_setprio(PTO);
    STX(11);
#pragma unroll
    for (int p = 0; p < 2; ++p)
#pragma unroll
      for (int i = 0; i < 7; ++i) wv[p][i] = __builtin_amdgcn_raw_buffer_load_b128(wos, l16, (i == 6 && j >= 2) ? 102400 : p * 51200 + i * 8192, 16);
    lds_poll<SY_RV, 1>(sya, 8);
#pragma unroll
    for (int i = 0; i < 7; ++i) if (i < 6 || j < 2) asm volatile("ds_write_b128 %0, %1 offset:%2" :: "v"(wd), "v"(wv[0][i]), "i"(i * 8192) : "memory");
    lds_inc<SY_WO>(sya, one);
    wd += WO1_OFF;
    PIN(wd);
  }
  if (grp) { pg_poll<2>(sya, LDSA(smem) + SY_OFF + 256 * layer + SY_X + 44); __builtin_amdgcn_s_setprio(PTA1); } else __builtin_amdgcn_s_setprio(PTA);
  STX(12);
  pg_poll<APS>(sya, LDSA(smem) + SY_OFF + 256 * layer + SY_X + 44 + 4 * grp);
  if (layer) ST(5);
      asm volatile(
        "ds_read_b64 v[30:31], %[q0]\nds_read_b64 v[32:33], %[q1]\nds_read_b64 v[38:39], %[ka] offset:0\nds_read_b64 v[40:41], %[ka] offset:8\nds_read_b64 v[42:43], %[ka] offset:7936\n"
        "ds_read_b64 v[44:45], %[ka] offset:7944\nds_read_b64 v[34:35], %[q2]\nds_read_b64 v[36:37], %[q3]\nds_read_b64 v[46:47], %[ka] offset:40\nds_read_b64 v[48:49], %[ka] offset:48\n"
        "ds_read_b64 v[50:51], %[ka] offset:7976\nds_read_b64 v[52:53], %[ka] offset:7984\nds_read_b64_tr_b16 v[54:55], %[va] offset:0\nds_read_b64_tr_b16 v[56:57], %[va] offset:7936\n"
        "ds_read_b64_tr_b16 v[58:59], %[va] offset:32\nds_read_b64_tr_b16 v[60:61], %[va] offset:7968\nds_read_b64_tr_b16 v[62:63], %[va] offset:40\nds_read_b64_tr_b16 v[64:65], %[va] offset:7976\n"
        "ds_read_b64_tr_b16 v[66:67], %[va] offset:72\nds_read_b64_tr_b16 v[68:69], %[va] offset:8008\ns_waitcnt lgkmcnt(14)\nv_mfma_f32_16x16x32_bf16 v[70:73], v[38:41], v[30:33], %[bias]\n"
        "v_mfma_f32_16x16x32_bf16 v[74:77], v[42:45], v[30:33], 0\ns_waitcnt lgkmcnt(8)\nv_mfma_f32_16x16x32_bf16 v[78:81], v[46:49], v[34:37], %[bias]\n"
        "v_mfma_f32_16x16x32_bf16 v[82:85], v[50:53], v[34:37], 0\ns_nop 4\nv_cndmask_b32 v70, v70, v71, %[par]\nv_cndmask_b32 v72, v72, v73, %[par]\nv_cndmask_b32 v74, v74, v75, %[par]\n"
        "v_cndmask_b32 v76, v76, v77, %[par]\nv_cndmask_b32 v30, v74, v70, %[c0]\nv_cndmask_b32 v31, v76, v72, %[c1]\nv_max_f32 v34, v30, v31\nv_cndmask_b32 v78, v78, v79, %[par]\n"
        "v_cndmask_b32 v80, v80, v81, %[par]\nv_cndmask_b32 v82, v82, v83, %[par]\nv_cndmask_b32 v84, v84, v85, %[par]\nv_cndmask_b32 v32, v82, v78, %[c0]\nv_cndmask_b32 v33, v84, v80, %[c1]\n"
        "v_max_f32 v35, v32, v33\ns_nop 1\nv_permlane16_swap_b32 v34, v35\nv_max_f32 v34, v34, v35\nv_mov_b32 v35, v34\ns_nop 1\nv_permlane32_swap_b32 v34, v35\nv_max_f32 v34, v34, v35\n"
        "v_mul_f32 v35, %[nc], v34\nv_mul_f32 v34, %[nc], v34\ns_nop 1\nv_permlane16_swap_b32 v34, v35\nv_fma_f32 v30, -v30, %[nc], v34\nv_fma_f32 v31, -v31, %[nc], v34\n"
        "v_fma_f32 v32, -v32, %[nc], v35\nv_fma_f32 v33, -v33, %[nc], v35\nv_exp_f32 v30, v30\nv_exp_f32 v31, v31\nv_exp_f32 v32, v32\nv_exp_f32 v33, v33\nv_add_f32 v34, v30, v31\n"
        "v_add_f32 v35, v32, v33\ns_nop 1\nv_permlane16_swap_b32 v34, v35\nv_add_f32 v34, v34, v35\nv_mov_b32 v35, v34\ns_nop 1\nv_permlane32_swap_b32 v34, v35\nv_add_f32 v34, v34, v35\n"
        "v_rcp_f32 v34, v34\ns_nop 0\nv_mov_b32 v35, v34\ns_nop 1\nv_permlane16_swap_b32 v34, v35\nv_mul_f32 v30, v30, v34\nv_mul_f32 v31, v31, v34\nv_mul_f32 v32, v32, v35\nv_mul_f32 v33, v33, v35\n"
        "v_cvt_pk_bf16_f32 v30, v30, v31\nv_cvt_pk_bf16_f32 v32, v32, v33\nv_perm_b32 v38, v30, v30, %[sa0]\nv_perm_b32 v39, v30, v30, %[sa1]\nv_perm_b32 v40, v30, v30, %[sb0]\n"
        "v_perm_b32 v41, v30, v30, %[sb1]\nv_perm_b32 v42, v32, v32, %[sa0]\nv_perm_b32 v43, v32, v32, %[sa1]\nv_perm_b32 v44, v32, v32, %[sb0]\nv_perm_b32 v45, v32, v32, %[sb1]\ns_waitcnt lgkmcnt(0)\n"
        "v_mfma_f32_16x16x32_bf16 v[70:73], v[54:57], v[38:41], 0\nv_mfma_f32_16x16x32_bf16 v[78:81], v[62:65], v[42:45], 0\nv_mfma_f32_16x16x32_bf16 v[74:77], v[58:61], v[38:41], 0\n"
        "v_mfma_f32_16x16x32_bf16 v[82:85], v[66:69], v[42:45], 0\ns_nop 4\nv_cvt_pk_bf16_f32 v46, v70, v71\nv_cvt_pk_bf16_f32 v47, v72, v73\nv_cvt_pk_bf16_f32 v48, v78, v79\n"
        "v_cvt_pk_bf16_f32 v49, v80, v81\nv_min_u32 v46, -2, v46\nv_min_u32 v47, -2, v47\nv_min_u32 v48, -2, v48\nv_min_u32 v49, -2, v49\n"
        "buffer_store_dwordx4 v[46:49], %[l16], %[ors], %[so0] offen sc1\nv_cvt_pk_bf16_f32 v50, v74, v75\nv_cvt_pk_bf16_f32 v51, v76, v77\nv_cvt_pk_bf16_f32 v52, v82, v83\n"
        "v_cvt_pk_bf16_f32 v53, v84, v85\nv_min_u32 v50, -2, v50\nv_min_u32 v51, -2, v51\nv_min_u32 v52, -2, v52\nv_min_u32 v53, -2, v53\n"
        "buffer_store_dwordx4 v[50:53], %[oh], %[ors], %[so1] offen sc1\ns_nop 1\n"
        :
        : [q0] "v"(qa[0]), [q1] "v"(qa[1]), [q2] "v"(qa[2]), [q3] "v"(qa[3]), [ka] "v"(qka), [va] "v"(vta), [bias] "v"(bias), [par] "s"(0xaaaaaaaaaaaaaaaaull), [c0] "s"(c0m), [c1] "s"(c1m), [nc] "s"(nc), [sa0] "v"(sA0), [sa1] "v"(sA1), [sb0] "v"(sB0), [sb1] "v"(sB1), [l16] "v"(l16), [oh] "v"(ohs), [ors] "s"(orsrc), [so0] "s"(so0), [so1] "s"(so1)
        : "memory", "scc", "v30", "v31", "v32", "v33", "v34", "v35", "v36", "v37", "v38", "v39", "v40", "v41", "v42", "v43", "v44", "v45", "v46", "v47", "v48", "v49", "v50", "v51", "v52", "v53", "v54", "v55", "v56", "v57", "v58", "v59", "v60", "v61", "v62", "v63", "v64", "v65", "v66", "v67", "v68", "v69", "v70", "v71", "v72", "v73", "v74", "v75", "v76", "v77", "v78", "v79", "v80", "v81", "v82", "v83", "v84", "v85");
  ST(6);
  {
#pragma unroll
    for (int i = 0; i < 7; ++i) if (i < 6 || j < 2) asm volatile("ds_write_b128 %0, %1 offset:%2" :: "v"(wd), "v"(wv[1][i]), "i"(i * 8192) : "memory");
    lds_inc<SY_WO + 4>(sya, one);
  }
  {
    const int ob = (16 * kh + mt2) * 2560;
    u32 tm;
    int cap, s0_, s1_, rt = 0;
      asm volatile(
        "s_mov_b32 %[cap], 0x10000\ns_cmp_eq_u32 %[kh], 0\ns_cbranch_scc1 .LS0_%=\ns_sleep %[sl1]\nbuffer_load_dwordx4 v[28:31], %[l16], %[ors], %[o0] offen offset:0 sc1\n"
        "buffer_load_dwordx4 v[32:35], %[l16], %[ors], %[o1] offen offset:0 sc1\nbuffer_load_dwordx4 v[36:39], %[l16], %[ors], %[o2] offen offset:0 sc1\n"
        "buffer_load_dwordx4 v[40:43], %[l16], %[ors], %[o3] offen offset:0 sc1\nbuffer_load_dwordx4 v[44:47], %[oha], %[ors], %[o4] offen offset:0 sc1\n.LS0_%=:\ns_mov_b32 %[s1], 0x100000\n.LWa%=:\n"
        "ds_read_b32 %[tm], %[sy] offset:%[ow1]-4\ns_waitcnt lgkmcnt(0)\nv_readfirstlane_b32 %[s0], %[tm]\ns_nop 0\ns_cmp_ge_i32 %[s0], %[nwo]\ns_cbranch_scc1 .LXa%=\ns_sleep 1\n"
        "s_sub_u32 %[s1], %[s1], 1\ns_cbranch_scc0 .LWa%=\n.LXa%=:\nds_read_b128 v[68:71], %[bb] offset:0\nds_read_b128 v[72:75], %[bb] offset:1024\nds_read_b128 v[76:79], %[bb] offset:2048\n"
        "ds_read_b128 v[80:83], %[bb] offset:3072\nds_read_b128 v[84:87], %[bb] offset:4096\nds_read_b128 v[88:91], %[bb] offset:5120\nds_read_b128 v[92:95], %[bb] offset:6144\n"
        "ds_read_b128 v[96:99], %[bb] offset:7168\nds_read_b128 v[100:103], %[bb] offset:8192\nds_read_b128 v[104:107], %[bb] offset:9216\nds_read_b128 v[108:111], %[bb] offset:10240\n"
        "ds_read_b128 v[112:115], %[bb] offset:11264\nds_read_b128 v[116:119], %[bb] offset:12288\nds_read_b128 v[120:123], %[bb] offset:13312\nds_read_b128 v[124:127], %[bb] offset:14336\n"
        "ds_read_b128 v[128:131], %[bb] offset:15360\nds_read_b128 v[132:135], %[bb] offset:16384\nds_read_b128 v[136:139], %[bb] offset:17408\nds_read_b128 v[140:143], %[bb] offset:18432\n"
        "ds_read_b128 v[144:147], %[bb] offset:19456\nds_read_b128 v[148:151], %[bb] offset:20480\nds_read_b128 v[152:155], %[bb] offset:21504\nds_read_b128 v[156:159], %[bb] offset:22528\n"
        "ds_read_b128 v[160:163], %[bb] offset:23552\nds_read_b128 v[164:167], %[bb] offset:24576\ns_mov_b64 exec, 1\nds_add_u32 %[sy], %[one] offset:%[ofr]\ns_mov_b64 exec, -1\n"
        "v_add_u32 %[bb], %[wo1], %[bb]\nv_lshlrev_b32 v48, 16, v22\nv_and_b32 v49, 0xffff0000, v22\nv_lshlrev_b32 v50, 16, v23\nv_and_b32 v51, 0xffff0000, v23\nv_lshlrev_b32 v52, 16, v24\n"
        "v_and_b32 v53, 0xffff0000, v24\nv_lshlrev_b32 v54, 16, v25\nv_and_b32 v55, 0xffff0000, v25\nv_lshlrev_b32 v56, 16, v26\nv_and_b32 v57, 0xffff0000, v26\nv_lshlrev_b32 v58, 16, v27\n"
        "v_and_b32 v59, 0xffff0000, v27\ns_cmp_eq_u32 %[kh], 0\ns_cbranch_scc0 .LS1_%=\ns_sleep %[sl0]\nbuffer_load_dwordx4 v[28:31], %[l16], %[ors], %[o0] offen offset:0 sc1\n"
        "buffer_load_dwordx4 v[32:35], %[l16], %[ors], %[o1] offen offset:0 sc1\nbuffer_load_dwordx4 v[36:39], %[l16], %[ors], %[o2] offen offset:0 sc1\n"
        "buffer_load_dwordx4 v[40:43], %[l16], %[ors], %[o3] offen offset:0 sc1\nbuffer_load_dwordx4 v[44:47], %[oha], %[ors], %[o4] offen offset:0 sc1\n.LS1_%=:\ns_mov_b32 %[s1], 0x100000\n.LWb%=:\n"
        "ds_read_b32 %[tm], %[sy] offset:%[ow1]\ns_waitcnt lgkmcnt(0)\nv_readfirstlane_b32 %[s0], %[tm]\ns_nop 0\ns_cmp_ge_i32 %[s0], %[nwo]\ns_cbranch_scc1 .LXb%=\ns_sleep 1\n"
        "s_sub_u32 %[s1], %[s1], 1\ns_cbranch_scc0 .LWb%=\n.LXb%=:\n.LC0_%=:\ns_waitcnt vmcnt(4)\nv_max_u32 %[tm], v28, v29\nv_max3_u32 %[tm], %[tm], v30, v31\ns_waitcnt vmcnt(3)\n"
        "v_max3_u32 %[tm], %[tm], v32, v33\nv_max3_u32 %[tm], %[tm], v34, v35\ns_waitcnt vmcnt(2)\nv_max3_u32 %[tm], %[tm], v36, v37\nv_max3_u32 %[tm], %[tm], v38, v39\ns_waitcnt vmcnt(1)\n"
        "v_max3_u32 %[tm], %[tm], v40, v41\nv_max3_u32 %[tm], %[tm], v42, v43\ns_waitcnt vmcnt(0)\nv_max3_u32 %[tm], %[tm], v44, v45\nv_max3_u32 %[tm], %[tm], v46, v47\nv_cmp_eq_u32 vcc, -1, %[tm]\n"
        "s_cbranch_vccnz .LR0_%=\n.LK0_%=:\nv_mfma_f32_16x16x32_bf16 v[68:71], v[28:31], v[68:71], 0\nv_mfma_f32_16x16x32_bf16 v[72:75], v[28:31], v[72:75], 0\n"
        "v_mfma_f32_16x16x32_bf16 v[76:79], v[28:31], v[76:79], v[48:51]\nv_mfma_f32_16x16x32_bf16 v[80:83], v[28:31], v[80:83], v[52:55]\n"
        "v_mfma_f32_16x16x32_bf16 v[84:87], v[28:31], v[84:87], v[56:59]\nbuffer_load_dwordx4 v[48:51], %[l16], %[ors], %[o0] offen offset:1024 sc1\n"
        "buffer_load_dwordx4 v[52:55], %[l16], %[ors], %[o1] offen offset:1024 sc1\nbuffer_load_dwordx4 v[56:59], %[l16], %[ors], %[o2] offen offset:1024 sc1\n"
        "buffer_load_dwordx4 v[60:63], %[l16], %[ors], %[o3] offen offset:1024 sc1\nbuffer_load_dwordx4 v[64:67], %[oha], %[ors], %[o4] offen offset:256 sc1\n"
        "v_mfma_f32_16x16x32_bf16 v[68:71], v[32:35], v[88:91], v[68:71]\nds_read_b128 v[88:91], %[bb] offset:0\nv_mfma_f32_16x16x32_bf16 v[72:75], v[32:35], v[92:95], v[72:75]\n"
        "ds_read_b128 v[92:95], %[bb] offset:5120\nv_mfma_f32_16x16x32_bf16 v[76:79], v[32:35], v[96:99], v[76:79]\nds_read_b128 v[96:99], %[bb] offset:10240\n"
        "v_mfma_f32_16x16x32_bf16 v[80:83], v[32:35], v[100:103], v[80:83]\nds_read_b128 v[100:103], %[bb] offset:15360\nv_mfma_f32_16x16x32_bf16 v[84:87], v[32:35], v[104:107], v[84:87]\n"
        "ds_read_b128 v[104:107], %[bb] offset:20480\nv_mfma_f32_16x16x32_bf16 v[68:71], v[36:39], v[108:111], v[68:71]\nds_read_b128 v[108:111], %[bb] offset:1024\n"
        "v_mfma_f32_16x16x32_bf16 v[72:75], v[36:39], v[112:115], v[72:75]\nds_read_b128 v[112:115], %[bb] offset:6144\nv_mfma_f32_16x16x32_bf16 v[76:79], v[36:39], v[116:119], v[76:79]\n"
        "ds_read_b128 v[116:119], %[bb] offset:11264\nv_mfma_f32_16x16x32_bf16 v[80:83], v[36:39], v[120:123], v[80:83]\nds_read_b128 v[120:123], %[bb] offset:16384\n"
        "v_mfma_f32_16x16x32_bf16 v[84:87], v[36:39], v[124:127], v[84:87]\nds_read_b128 v[124:127], %[bb] offset:21504\nv_mfma_f32_16x16x32_bf16 v[68:71], v[40:43], v[128:131], v[68:71]\n"
        "ds_read_b128 v[128:131], %[bb] offset:2048\nv_mfma_f32_16x16x32_bf16 v[72:75], v[40:43], v[132:135], v[72:75]\nds_read_b128 v[132:135], %[bb] offset:7168\n"
        "v_mfma_f32_16x16x32_bf16 v[76:79], v[40:43], v[136:139], v[76:79]\nds_read_b128 v[136:139], %[bb] offset:12288\nv_mfma_f32_16x16x32_bf16 v[80:83], v[40:43], v[140:143], v[80:83]\n"
        "ds_read_b128 v[140:143], %[bb] offset:17408\nv_mfma_f32_16x16x32_bf16 v[84:87], v[40:43], v[144:147], v[84:87]\nds_read_b128 v[144:147], %[bb] offset:22528\n"
        "v_mfma_f32_16x16x32_bf16 v[68:71], v[44:47], v[148:151], v[68:71]\nds_read_b128 v[148:151], %[bb] offset:3072\nv_mfma_f32_16x16x32_bf16 v[72:75], v[44:47], v[152:155], v[72:75]\n"
        "ds_read_b128 v[152:155], %[bb] offset:8192\nv_mfma_f32_16x16x32_bf16 v[76:79], v[44:47], v[156:159], v[76:79]\nds_read_b128 v[156:159], %[bb] offset:13312\n"
        "v_mfma_f32_16x16x32_bf16 v[80:83], v[44:47], v[160:163], v[80:83]\nds_read_b128 v[160:163], %[bb] offset:18432\nv_mfma_f32_16x16x32_bf16 v[84:87], v[44:47], v[164:167], v[84:87]\n"
        "ds_read_b128 v[164:167], %[bb] offset:23552\nds_read_b128 v[28:31], %[bb] offset:4096\nds_read_b128 v[32:35], %[bb] offset:9216\nds_read_b128 v[36:39], %[bb] offset:14336\n"
        "ds_read_b128 v[40:43], %[bb] offset:19456\nds_read_b128 v[44:47], %[bb] offset:24576\ns_mov_b32 %[s1], 0x100000\n.LWc%=:\nds_read_b32 %[tm], %[sy] offset:%[ofr]\ns_waitcnt lgkmcnt(0)\n"
        "v_readfirstlane_b32 %[s0], %[tm]\ns_nop 0\ns_cmp_ge_i32 %[s0], 8\ns_cbranch_scc1 .LXc%=\ns_sleep 1\ns_sub_u32 %[s1], %[s1], 1\ns_cbranch_scc0 .LWc%=\n.LXc%=:\n.LC1_%=:\ns_waitcnt vmcnt(4)\n"
        "v_max_u32 %[tm], v48, v49\nv_max3_u32 %[tm], %[tm], v50, v51\ns_waitcnt vmcnt(3)\nv_max3_u32 %[tm], %[tm], v52, v53\nv_max3_u32 %[tm], %[tm], v54, v55\ns_waitcnt vmcnt(2)\n"
        "v_max3_u32 %[tm], %[tm], v56, v57\nv_max3_u32 %[tm], %[tm], v58, v59\ns_waitcnt vmcnt(1)\nv_max3_u32 %[tm], %[tm], v60, v61\nv_max3_u32 %[tm], %[tm], v62, v63\ns_waitcnt vmcnt(0)\n"
        "v_max3_u32 %[tm], %[tm], v64, v65\nv_max3_u32 %[tm], %[tm], v66, v67\nv_cmp_eq_u32 vcc, -1, %[tm]\ns_cbranch_vccnz .LR1_%=\n.LK1_%=:\n"
        "v_mfma_f32_16x16x32_bf16 v[68:71], v[48:51], v[88:91], v[68:71]\nv_mfma_f32_16x16x32_bf16 v[68:71], v[52:55], v[92:95], v[68:71]\n"
        "v_mfma_f32_16x16x32_bf16 v[68:71], v[56:59], v[96:99], v[68:71]\nv_mfma_f32_16x16x32_bf16 v[68:71], v[60:63], v[100:103], v[68:71]\n"
        "v_mfma_f32_16x16x32_bf16 v[68:71], v[64:67], v[104:107], v[68:71]\nv_mfma_f32_16x16x32_bf16 v[72:75], v[48:51], v[108:111], v[72:75]\n"
        "v_mfma_f32_16x16x32_bf16 v[72:75], v[52:55], v[112:115], v[72:75]\nv_mfma_f32_16x16x32_bf16 v[72:75], v[56:59], v[116:119], v[72:75]\nds_write_b128 %[rw], v[68:71]\n"
        "v_mfma_f32_16x16x32_bf16 v[72:75], v[60:63], v[120:123], v[72:75]\nv_mfma_f32_16x16x32_bf16 v[72:75], v[64:67], v[124:127], v[72:75]\n"
        "v_mfma_f32_16x16x32_bf16 v[76:79], v[48:51], v[128:131], v[76:79]\nv_mfma_f32_16x16x32_bf16 v[76:79], v[52:55], v[132:135], v[76:79]\n"
        "v_mfma_f32_16x16x32_bf16 v[76:79], v[56:59], v[136:139], v[76:79]\nds_write_b128 %[rw], v[72:75] offset:1024\nv_mfma_f32_16x16x32_bf16 v[76:79], v[60:63], v[140:143], v[76:79]\n"
        "ds_write_b32 %[fw], %[f0]\nv_mfma_f32_16x16x32_bf16 v[76:79], v[64:67], v[144:147], v[76:79]\nv_mfma_f32_16x16x32_bf16 v[80:83], v[48:51], v[148:151], v[80:83]\n"
        "v_mfma_f32_16x16x32_bf16 v[80:83], v[52:55], v[152:155], v[80:83]\nv_mfma_f32_16x16x32_bf16 v[80:83], v[56:59], v[156:159], v[80:83]\nds_write_b128 %[rw], v[76:79] offset:2048\n"
        "v_mfma_f32_16x16x32_bf16 v[80:83], v[60:63], v[160:163], v[80:83]\nds_write_b32 %[fw], %[one]\nv_mfma_f32_16x16x32_bf16 v[80:83], v[64:67], v[164:167], v[80:83]\n"
        "v_mfma_f32_16x16x32_bf16 v[84:87], v[48:51], v[28:31], v[84:87]\nv_mfma_f32_16x16x32_bf16 v[84:87], v[52:55], v[32:35], v[84:87]\n"
        "v_mfma_f32_16x16x32_bf16 v[84:87], v[56:59], v[36:39], v[84:87]\nv_mfma_f32_16x16x32_bf16 v[84:87], v[60:63], v[40:43], v[84:87]\n"
        "v_mfma_f32_16x16x32_bf16 v[84:87], v[64:67], v[44:47], v[84:87]\ns_mov_b32 %[s1], 0x100000\n.LY%=:\nds_read_b32 %[tm], %[fr]\nds_read_b128 v[96:99], %[rr] offset:2048\n"
        "ds_read_b128 v[92:95], %[rr] offset:1024\nds_read_b128 v[88:91], %[rr] offset:0\ns_waitcnt lgkmcnt(3)\nv_readfirstlane_b32 %[s0], %[tm]\ns_waitcnt lgkmcnt(0)\ns_cmp_lg_u32 %[s0], 0\n"
        "s_cbranch_scc1 .LZ%=\ns_sub_u32 %[s1], %[s1], 1\ns_cbranch_scc0 .LY%=\n.LZ%=:\nv_add_f32 v80, v80, v92\nv_add_f32 v84, v84, v96\nv_add_f32 v81, v81, v93\nv_add_f32 v85, v85, v97\n"
        "v_add_f32 v82, v82, v94\nv_add_f32 v86, v86, v98\nv_add_f32 v83, v83, v95\nv_add_f32 v87, v87, v99\nv_cvt_pk_bf16_f32 v92, v80, v81\nv_cvt_pk_bf16_f32 v96, v84, v85\n"
        "v_cvt_pk_bf16_f32 v93, v82, v83\nv_cvt_pk_bf16_f32 v97, v86, v87\nv_min_u32 v92, -2, v92\nv_min_u32 v96, -2, v96\nv_min_u32 v93, -2, v93\nv_min_u32 v97, -2, v97\n"
        "ds_write_b64 %[xo], v[92:93] offset:2304\nds_write_b64 %[xo], v[96:97] offset:4608\ns_cmp_eq_u32 %[kh], 0\nv_add_f32 v100, v80, v84\nv_add_f32 v101, v81, v85\nv_add_f32 v102, v82, v86\n"
        "v_add_f32 v103, v83, v87\nv_mul_f32 v104, v84, v84\nv_mul_f32 v105, v85, v85\nv_mul_f32 v106, v86, v86\nv_mul_f32 v107, v87, v87\nv_fmac_f32 v104, v80, v80\nv_fmac_f32 v105, v81, v81\n"
        "v_fmac_f32 v106, v82, v82\nv_fmac_f32 v107, v83, v83\ns_cbranch_scc0 .LV%=\nv_add_f32 v76, v76, v88\nv_add_f32 v77, v77, v89\nv_add_f32 v78, v78, v90\nv_add_f32 v79, v79, v91\n"
        "v_cvt_pk_bf16_f32 v88, v76, v77\nv_cvt_pk_bf16_f32 v89, v78, v79\nv_min_u32 v88, -2, v88\nv_min_u32 v89, -2, v89\nds_write_b64 %[xo], v[88:89] offset:0\nv_add_f32 v100, v100, v76\n"
        "v_add_f32 v101, v101, v77\nv_add_f32 v102, v102, v78\nv_add_f32 v103, v103, v79\nv_fmac_f32 v104, v76, v76\nv_fmac_f32 v105, v77, v77\nv_fmac_f32 v106, v78, v78\nv_fmac_f32 v107, v79, v79\n"
        ".LV%=:\ns_mov_b64 exec, 1\nds_add_u32 %[sy], %[one] offset:%[ox1]\ns_mov_b64 exec, -1\nv_add_f32_dpp v100, v100, v100 row_ror:8 row_mask:0xf bank_mask:0x3\n"
        "v_add_f32_dpp v101, v101, v101 row_ror:8 row_mask:0xf bank_mask:0x3\nv_add_f32_dpp v104, v104, v104 row_ror:8 row_mask:0xf bank_mask:0x3\n"
        "v_add_f32_dpp v105, v105, v105 row_ror:8 row_mask:0xf bank_mask:0x3\nv_add_f32_dpp v100, v102, v102 row_ror:8 row_mask:0xf bank_mask:0xc\n"
        "v_add_f32_dpp v101, v103, v103 row_ror:8 row_mask:0xf bank_mask:0xc\nv_add_f32_dpp v104, v106, v106 row_ror:8 row_mask:0xf bank_mask:0xc\n"
        "v_add_f32_dpp v105, v107, v107 row_ror:8 row_mask:0xf bank_mask:0xc\nv_add_f32_dpp v102, v100, v100 row_half_mirror row_mask:0xf bank_mask:0x5\n"
        "v_add_f32_dpp v103, v104, v104 row_half_mirror row_mask:0xf bank_mask:0x5\ns_nop 0\nv_add_f32_dpp v102, v101, v101 row_half_mirror row_mask:0xf bank_mask:0xa\n"
        "v_add_f32_dpp v103, v105, v105 row_half_mirror row_mask:0xf bank_mask:0xa\ns_nop 0\nv_add_f32_dpp v102, v102, v102 quad_perm:[1,0,3,2] row_mask:0xf bank_mask:0xf\n"
        "v_add_f32_dpp v103, v103, v103 quad_perm:[1,0,3,2] row_mask:0xf bank_mask:0xf\ns_nop 0\nv_add_f32_dpp v102, v102, v102 quad_perm:[2,3,0,1] row_mask:0xf bank_mask:0xf\n"
        "v_add_f32_dpp v103, v103, v103 quad_perm:[2,3,0,1] row_mask:0xf bank_mask:0xf\nv_min_u32 v102, -2, v102\nv_min_u32 v103, -2, v103\n"
        "buffer_store_dwordx2 v[102:103], %[sv], %[srs], %[ss] offen sc1\ns_branch .LE%=\n.LR0_%=:\ns_sub_u32 %[cap], %[cap], 1\ns_cbranch_scc0 .LQ0_%=\ns_mov_b32 %[cap], 0\ns_branch .LK0_%=\n"
        ".LQ0_%=:\ns_sleep 2\nbuffer_load_dwordx4 v[28:31], %[l16], %[ors], %[o0] offen offset:0 sc1\nbuffer_load_dwordx4 v[32:35], %[l16], %[ors], %[o1] offen offset:0 sc1\n"
        "buffer_load_dwordx4 v[36:39], %[l16], %[ors], %[o2] offen offset:0 sc1\nbuffer_load_dwordx4 v[40:43], %[l16], %[ors], %[o3] offen offset:0 sc1\n"
        "buffer_load_dwordx4 v[44:47], %[oha], %[ors], %[o4] offen offset:0 sc1\ns_branch .LC0_%=\n.LR1_%=:\ns_sub_u32 %[cap], %[cap], 1\ns_cbranch_scc0 .LQ1_%=\ns_mov_b32 %[cap], 0\ns_branch .LK1_%=\n"
        ".LQ1_%=:\ns_sleep 2\nbuffer_load_dwordx4 v[48:51], %[l16], %[ors], %[o0] offen offset:1024 sc1\nbuffer_load_dwordx4 v[52:55], %[l16], %[ors], %[o1] offen offset:1024 sc1\n"
        "buffer_load_dwordx4 v[56:59], %[l16], %[ors], %[o2] offen offset:1024 sc1\nbuffer_load_dwordx4 v[60:63], %[l16], %[ors], %[o3] offen offset:1024 sc1\n"
        "buffer_load_dwordx4 v[64:67], %[oha], %[ors], %[o4] offen offset:256 sc1\ns_branch .LC1_%=\n.LE%=:\n"
        : [tm] "=&v"(tm), [bb] "+{v19}"(bb), [cap] "=&s"(cap), [s0] "=&s"(s0_), [s1] "=&s"(s1_), "+{v20}"(xi_), "+{v21}"(xg_)
        : "{v[22:23]}"(rv[0]), "{v[24:25]}"(rv[1]), "{v[26:27]}"(rv[2]), [l16] "{v8}"(l16), [oha] "{v9}"(oha), [ors] "s"(orsrc), [o0] "s"(ob + 0), [o1] "s"(ob + 10240), [o2] "s"(ob + 20480), [o3] "s"(ob + 30720), [o4] "s"(ob + 2048), [sy] "{v10}"(sya), [one] "{v11}"(one), [rw] "{v12}"(rw), [fw] "{v13}"(fw), [f0] "{v14}"(f0), [rr] "{v15}"(rr), [fr] "{v18}"(fr), [xo] "{v16}"(xo_), [sv] "{v17}"(so_), [srs] "s"(srsrc), [ss] "s"(mt2 * 128 + (2 * hg + kh) * 512), [kh] "s"(kh), [wo1] "s"(WO1_OFF), [ofr] "i"(SY_FREE), [ox1] "i"(SY_X1), [ow1] "i"(SY_WO + 4), [nwo] "i"(8), [sl0] "i"(OSLEEP), [sl1] "i"(OSLEEP1)
        : "memory", "scc", "vcc", "v28", "v29", "v30", "v31", "v32", "v33", "v34", "v35", "v36", "v37", "v38", "v39", "v40", "v41", "v42", "v43", "v44", "v45", "v46", "v47", "v48", "v49", "v50", "v51", "v52", "v53", "v54", "v55", "v56", "v57", "v58", "v59", "v60", "v61", "v62", "v63", "v64", "v65", "v66", "v67", "v68", "v69", "v70", "v71", "v72", "v73", "v74", "v75", "v76", "v77", "v78", "v79", "v80", "v81", "v82", "v83", "v84", "v85", "v86", "v87", "v88", "v89", "v90", "v91", "v92", "v93", "v94", "v95", "v96", "v97", "v98", "v99", "v100", "v101", "v102", "v103", "v104", "v105", "v106", "v107", "v108", "v109", "v110", "v111", "v112", "v113", "v114", "v115", "v116", "v117", "v118", "v119", "v120", "v121", "v122", "v123", "v124", "v125", "v126", "v127", "v128", "v129", "v130", "v131", "v132", "v133", "v134", "v135", "v136", "v137", "v138", "v139", "v140", "v141", "v142", "v143", "v144", "v145", "v146", "v147", "v148", "v149", "v150", "v151", "v152", "v153", "v154", "v155", "v156", "v157", "v158", "v159", "v160", "v161", "v162", "v163", "v164", "v165", "v166", "v167");
    RETRY(8 * layer + 1 + grp, 0x10000 - cap);
    RETRY(8 * layer + 3 + grp, rt >> 16);
  }
  lds_inc<SY_FIN>(sya, one);
  lds_poll<SY_X1, 0>(sya, 8);
  {
    typedef __attribute__((address_space(3))) const u32x4* lp4;
    __builtin_amdgcn_raw_buffer_store_b128(*(lp4)(size_t)xi_, x1rs, xg_, 0, 17);
    if (j < 2) __builtin_amdgcn_raw_buffer_store_b128(*(lp4)(size_t)(xi_ + 64 * 144), x1rs, xg_, 64 * XP * 2, 17);
  }
  STY(13);
  {
    LANE(lane);
    const int t = 64 * w + lane;
    ST_FLUSH(kid);
  }
}

#define BAUX 17
#define PC8 (gq == 0)
#define PC4 (gq == 0)
#define UPAD (9 * 144)
#define UF_N (UPAD + 16 * 144)
#define KY0 1058
#define KC_N 1120
#define KZ0 130
#define K8_N 192
#define X3_P 144
#define CH_EL (UF_N + 4 * KC_N + 2 * K8_N)
#define AUX_B 25600
#define B_CNT (3 * CH_EL * 2 + 3 * AUX_B)

template <int OFF> __device__ __forceinline__ bf16x8 rdk(u32 a) {
  s16x4 l, h;
  asm volatile("ds_read_b64 %0, %2 offset:%3\n\tds_read_b64 %1, %2 offset:%3+8" : "=&v"(l), "=&v"(h) : "v"(a), "i"(OFF) : "memory");
  return __builtin_shufflevector(l, h, 0, 1, 2, 3, 4, 5, 6, 7);
}
template <int N, int NB> struct KLd {
  static __device__ __forceinline__ void go(bf16x8* K, u32 a) { KLd<N - 1, NB>::go(K, a); K[N - 1] = rdk<256 * NB - 64 * (N - 1)>(a); }
};
template <int NB> struct KLd<0, NB> { static __device__ __forceinline__ void go(bf16x8*, u32) {} };

#define BLK4_TAIL \
      "v_mfma_f32_16x16x32_f16 %3, %9, %6, %3\n\t" \
      "v_mfma_f32_16x16x32_f16 %2, %9, %5, %2\n\t" \
      "ds_read_b128 %6, %12 offset:160\n\t" \
      "v_mfma_f32_16x16x32_f16 %1, %9, %4, %1\n\t" \
      "s_waitcnt lgkmcnt(1)\n\t" \
      "v_mfma_f32_16x16x32_f16 %0, %9, %7, %0\n\t" \
      "v_mfma_f32_16x16x32_f16 %3, %10, %5, %3\n\t" \
      "v_mfma_f32_16x16x32_f16 %2, %10, %4, %2\n\t" \
      "ds_read_b128 %5, %12 offset:96\n\t" \
      "v_mfma_f32_16x16x32_f16 %1, %10, %7, %1\n\t" \
      "s_waitcnt lgkmcnt(1)\n\t" \
      "v_mfma_f32_16x16x32_f16 %0, %10, %6, %0\n\t" \
      "v_mfma_f32_16x16x32_f16 %3, %11, %4, %3\n\t" \
      "v_mfma_f32_16x16x32_f16 %2, %11, %7, %2\n\t" \
      "ds_read_b128 %4, %13 offset:0\n\t" \
      "v_mfma_f32_16x16x32_f16 %1, %11, %6, %1\n\t" \
      "s_waitcnt lgkmcnt(1)\n\t" \
      "v_mfma_f32_16x16x32_f16 %0, %11, %5, %0\n\t"
template <bool FIRST>
__device__ __forceinline__ void blk4(f32x4 (&acc)[4], bf16x8 (&F)[4], const bf16x8* k, u32 ub, u32 ubx) {
  if (FIRST)
    asm volatile(
      "v_mfma_f32_16x16x32_f16 %3, %8, %7, 0\n\t"
      "v_mfma_f32_16x16x32_f16 %2, %8, %6, 0\n\t"
      "ds_read_b128 %7, %12 offset:224\n\t"
      "v_mfma_f32_16x16x32_f16 %1, %8, %5, 0\n\t"
      "s_waitcnt lgkmcnt(1)\n\t"
      "v_mfma_f32_16x16x32_f16 %0, %8, %4, 0\n\t"
      BLK4_TAIL
      : "=&v"(acc[0]), "=&v"(acc[1]), "=&v"(acc[2]), "=&v"(acc[3]), "+v"(F[0]), "+v"(F[1]), "+v"(F[2]), "+v"(F[3])
      : "v"(k[0]), "v"(k[1]), "v"(k[2]), "v"(k[3]), "v"(ub), "v"(ubx) : "memory");
  else
    asm volatile(
      "v_mfma_f32_16x16x32_f16 %3, %8, %7, %3\n\t"
      "v_mfma_f32_16x16x32_f16 %2, %8, %6, %2\n\t"
      "ds_read_b128 %7, %12 offset:224\n\t"
      "v_mfma_f32_16x16x32_f16 %1, %8, %5, %1\n\t"
      "s_waitcnt lgkmcnt(1)\n\t"
      "v_mfma_f32_16x16x32_f16 %0, %8, %4, %0\n\t"
      BLK4_TAIL
      : "+v"(acc[0]), "+v"(acc[1]), "+v"(acc[2]), "+v"(acc[3]), "+v"(F[0]), "+v"(F[1]), "+v"(F[2]), "+v"(F[3])
      : "v"(k[0]), "v"(k[1]), "v"(k[2]), "v"(k[3]), "v"(ub), "v"(ubx) : "memory");
}

#define MFH(a, b, c) __builtin_amdgcn_mfma_f32_16x16x32_f16(__builtin_bit_cast(f16x8, a), __builtin_bit_cast(f16x8, b), c, 0, 0, 0)
#define LDSA_(p) ((u32)(size_t)(__attribute__((address_space(3))) u16*)(p))
#define PIN_(x) asm volatile("" : "+v"(x))
__device__ __forceinline__ void kc_store(u16* kd, const u32* d) {
  u32 sw[5], mx[5];
#pragma unroll
  for (int i = 0; i < 5; ++i) { sw[i] = __builtin_amdgcn_alignbit(d[i], d[i], 16); mx[i] = __builtin_amdgcn_perm(d[i], d[i + 1], 0x07060100); }
  *(uint4*)kd = make_uint4(mx[4], mx[3], mx[2], mx[1]);
  *(uint4*)(kd + KC_N) = make_uint4(sw[4], sw[3], sw[2], sw[1]);
  *(uint4*)(kd + 2 * KC_N) = make_uint4(mx[3], mx[2], mx[1], mx[0]);
  *(uint4*)(kd + 3 * KC_N) = make_uint4(sw[3], sw[2], sw[1], sw[0]);
}
#define DUPLO_(x) __builtin_amdgcn_perm(x, x, 0x01000100)
#define DUPHI_(x) __builtin_amdgcn_perm(x, x, 0x03020302)

#define FSLEEP 18
#define TSLEEP 10
#define FPOLL 4
__device__ __forceinline__ void b_tables(unsigned char* smem, const int ch, const int lane) {
  const u16* kc = (const u16*)(smem + KQ_OFF) + ch * 1536;
  u16* Uf = (u16*)smem + ch * CH_EL;
  u16* KC = Uf + UF_N;
  const u16* p = kc + (lane < 32 ? 8 * lane : 256 + 4 * (lane - 32));
  const uint2 a0 = *(const uint2*)p, a1 = *(const uint2*)(p + 4), a2 = *(const uint2*)(p + 8);
  const u32 b0 = *(const u32*)&kc[384 + 2 * lane], b1 = *(const u32*)&kc[386 + 2 * lane];
  const uint2 eh = *(const uint2*)(kc + (lane < 3 ? 0 : 512));
  const u32 c0 = *(const u32*)&kc[638 - 2 * lane], c1 = *(const u32*)&kc[640 - 2 * lane];
  u32 dh[6];
  {
    const bool r1 = lane < 32;
    dh[0] = r1 ? a0.x : DUPLO_(a0.x);
    dh[1] = r1 ? a0.y : __builtin_amdgcn_perm(a0.y, a0.x, 0x05040302);
    dh[2] = r1 ? a1.x : DUPLO_(a0.y);
    dh[3] = r1 ? a1.y : __builtin_amdgcn_perm(a1.x, a0.y, 0x05040302);
    const u32 nx = r1 ? a2.x : a1.x;
    dh[4] = lane < 31 ? nx : DUPLO_(nx);
    dh[5] = lane < 31 ? a2.y : (lane == 63 ? nx : nx >> 16);
    kc_store(KC + 8 * (131 - lane), dh);
  }
  dh[0] = DUPLO_(b0); dh[1] = b0; dh[2] = DUPHI_(b0); dh[3] = DUPLO_(b1); dh[4] = dh[3]; dh[5] = b1;
  kc_store(KC + 8 * (67 - lane), dh);
  {
    u32* K8 = (u32*)(KC + 4 * KC_N);
    const u32 wh = __builtin_amdgcn_perm(c0, c1, 0x07060100);
    K8[lane + 1] = wh;
    K8[K8_N / 2 + lane] = wh;
    K8[lane < 32 ? (lane ? 64 + lane : 0) : 128 + lane] = 0u;
  }
  *(uint4*)&Uf[8 * lane] = make_uint4(0, 0, 0, 0);
  *(uint4*)&Uf[8 * lane + 512] = make_uint4(0, 0, 0, 0);
  *(uint4*)&Uf[8 * lane + UPAD - 512] = make_uint4(0, 0, 0, 0);
  if (lane < 7) {
    const bool e0 = lane == 2, e1 = lane == 3;
    dh[0] = e1 ? DUPLO_(eh.x) : 0u; dh[1] = dh[0]; dh[2] = e1 ? eh.x & 0xffffu : 0u; dh[3] = 0u; dh[4] = e0 ? eh.x : 0u; dh[5] = e0 ? eh.y : 0u;
    kc_store(KC + 8 * (131 - (lane < 3 ? lane - 3 : 125 + lane)), dh);
  }
}
#define PBU 1
#define PBU2 1
#define PBUF 1
#define PBL1 2
#define FSLEEP0 FSLEEP
#define PBL 2
#define PBH 3
#define PBE 1
template <bool FULL, int layer>
__device__ __forceinline__ void b_conv(unsigned char* smem, const int b, const int t, const int gq, const int wq, const int cG, const u16* xtin, float2* stats, u32* fstat,
                                       u16* xtout, u32x4* mark
                                       ) {
  constexpr int NB = FULL ? 4 : 2;
  const int lane = t & 63, n = lane & 15, g = lane >> 4, ck = t & 255;
  const int v = (wq + gq) & 3;
  const int p = FULL ? (v & 1) : (b & 1);
  const int r0 = FULL ? (v & 2) : v;
  const bool xtra = FULL ? (v >= 2) : (v == 3);
  const u32 Uf = LDSA(smem) + gq * (CH_EL * 2);
  const u32 aux = LDSA(smem) + 3 * CH_EL * 2 + gq * AUX_B;
  const u32 sya = LDSA(smem) + SY_OFF + 256 * layer;
  u32 cna = sya + 80 + 16 * gq, one = 1u;
  bf16x8 K[4 * NB + 1], K8f[4], be, bo;
  const int ct = FULL ? 3 - v : ((v == 1 || v == 2) ? 1 : 0), q0 = FULL ? 0 : (v == 2 ? 2 : 0);
  const int np = FULL ? (v < 2 ? 2 : 1) : (v < 3 ? 1 : 0);
  u32 ub = Uf + 2 * (UPAD + 144 * (n - 2 * r0) + 8 * g + 16 * p - 176);
  u32 ubx = ub + ((g >= 2 || p) ? 32 : 0);
  const int h = n + g - 2;
  u32 p1 = Uf + 2 * (UPAD + 288 * wq + 8 * h + (h >= 16 ? 16 : 0) - (h < 0 ? 16 : 0));
  u32 p0 = Uf + 2 * (UPAD + 288 * wq + 8 * h - 4 - (h < 1 ? 16 : 0));
  u32 xd = aux + 2 * (n * X3_P + 32 * wq + 4 * g), xr = aux + 2 * (n * X3_P + 32 * q0 + 8 * g);
  u32 uw_ = Uf + 2 * (UPAD + 8 * ck + 16 * (ck >> 4));
  const int eck = FULL ? ck : (4 * (ck >> 1) + 2 * p + (ck & 1)) & 255;
  u32 ue = Uf + 2 * (UPAD + 8 * eck + 16 * (eck >> 4));
  u32 exw = aux + 4608 + (FULL ? (v >> 1) * 8448 + n * 528 + p * 64 + g * 16 : v * 4096 + n * 256);
  u32 exr = aux + 4608 + (FULL ? (ck >> 4) * 528 + (ck & 15) * 32 : ((ck >> 3) & 15) * 256 + ((8 * (ck & 7) + 4 * (ck >> 3)) & 63) * 4);
  u32 exr1 = FULL ? exr + 16 : aux + 4608 + ((ck >> 3) & 15) * 256 + ((8 * (ck & 7) + 4 * (ck >> 3) + 4) & 63) * 4;
  u32 frw = aux + 21504 + (FULL ? ct * 1024 : (v >= 2 ? 2048 : 0) + ct * 1024) + n * 64 + g * 16;
  u32 frr = aux + 21504 + (FULL ? (ck & 127) * 32 : (ck & 63) * 32);
  float fm = (FULL ? ck >= 128 : (ck & 64) != 0) ? 1.f : 0.f;
  int od, oh;
  if (layer) { od = (((eck >> 3) * 640 + cG) * 64 + 8 * (eck & 7)) * 2; oh = -1; }
  else {
    const int dst = (eck >> 3) * 51200 + (cG >> 5) * 2560;
    const int pr = 4 * ((cG >> 3) & 3) + (cG & 3) + 4 * (cG & 4);
    od = (dst + pr * 64 + 8 * ((eck & 7) ^ (pr & 6))) * 2;
    oh = ((eck & 7) >= 6 && eck < 248) ? (dst + 51200 + 2048 + pr * 16 + 8 * (eck & 1)) * 2 : -1;
  }
  const int y0 = KY0 - 16 - n + 8 * g - 256 * r0;
  u32 kpa = Uf + 2 * (UF_N + (y0 & 3) * KC_N + (y0 & ~3)) - 256 * NB;
  const int z0 = KZ0 - 2 - (FULL ? 2 * n : 4 * n + 2 * p) + 8 * g - (FULL ? 32 : 64) * ct + 32 * q0;
  u32 k8a = Uf + 2 * (UF_N + 4 * KC_N + ((z0 >> 1) & 1) * K8_N + (z0 & ~3)), k8b = np < 2 ? k8a - 128 : k8a;
  u32 bna = LDSA(smem) + BND_OFF + 16 * lane, kfa = LDSA(smem) + KQ_OFF + gq * 3072 + 1536;
  int c16 = ck * 16;
  const __amdgpu_buffer_rsrc_t xrs = __builtin_amdgcn_make_buffer_rsrc((void*)(xtin + (size_t)cG * XP), 0, XP * 2, 0x00020000);
  const __amdgpu_buffer_rsrc_t frs = __builtin_amdgcn_make_buffer_rsrc((void*)(fstat + (b & 7) * 2048), 0, 8192, 0x00020000);
  const __amdgpu_buffer_rsrc_t ors = __builtin_amdgcn_make_buffer_rsrc(xtout, 0, 32 * 102400, 0x00020000);
  PIN(ub); PIN(ubx); PIN(p0); PIN(p1); PIN(xd); PIN(xr); PIN(uw_); PIN(exw); PIN(exr); PIN(frw); PIN(frr); PIN(fm); PIN(od);
  if (!FULL) { PIN(ue); PIN(exr1); }
  if (!layer) PIN(oh);
  PIN(cna); PIN(one); PIN(kpa); PIN(k8a); PIN(k8b); PIN(bna); PIN(kfa); PIN(c16);
  ST(6);
  if (!FULL && wq == 3) {
    const int xcd = b & 7, idx = b >> 3;
    const int s0 = 64 * (8 * (xcd >> 1) + (idx & 7)), hg = 4 * (xcd & 1) + (idx >> 3);
    const __amdgpu_buffer_rsrc_t srsrc = __builtin_amdgcn_make_buffer_rsrc(stats + (size_t)s0 * 16, 0, 8192, 0x00020000);
    const __amdgpu_buffer_rsrc_t wrs = __builtin_amdgcn_make_buffer_rsrc(fstat + s0 / 2 + 4 * hg, 0, 8 * 8192 - 4080, 0x00020000);
    int so = (lane >> 2) * 512 + (lane & 3) * 16;
    int wo_ = lane < 32 ? (lane >> 1) * 4096 + (lane & 1) * 8 : -1;
    PIN(so); PIN(wo_);
    lds_poll<SY_FIN, 4>(sya, 8);
    __builtin_amdgcn_s_sleep(SSLEEP);
    u32x4 pv;
    int spins = 0;
    _Pragma("unroll 1")
    while (true) {
      asm volatile("" ::: "memory");
      pv = __builtin_amdgcn_raw_buffer_load_b128(srsrc, so, hg * 64, 16);
      if (__builtin_amdgcn_ballot_w64(umax3(pv[0], pv[1], max(pv[2], pv[3])) == 0xffffffffu) == 0 || ++spins > GCAP) break;
      __builtin_amdgcn_s_sleep(4);
    }
    RETRY(8 * layer + 5, spins);
    ST(7);
    float sv[4];
#pragma unroll
    for (int i = 0; i < 4; ++i) {
      float a = __uint_as_float(pv[i]);
      a += __builtin_bit_cast(float, __builtin_amdgcn_update_dpp(0, __builtin_bit_cast(int, a), 0x124, 0xf, 0xf, false));
      a += dpp_ror8(a);
      sv[i] = xsum16_32(a);
    }
    u32 o[2];
#pragma unroll
    for (int i = 0; i < 2; ++i) {
      const float mean = sv[2 * i] * (1.0f / 640.0f);
      const float var = fmaxf(fmaf(-mean, mean, sv[2 * i + 1] * (1.0f / 640.0f)), 0.f);
      const u32 d = pack2(__builtin_amdgcn_sqrtf(rbf(rbf(var) + 1e-5f)) * (1.0f / 256.0f), 0.f), mb = __float_as_uint(mean);
      o[i] = ((max((mb & 0x7fffffffu) + 0x8000u, d) - d) & 0xffff0000u) | d | (mb & 0x80000000u);
    }
    __builtin_amdgcn_raw_buffer_store_b64((u32x2){min(o[0], 0xfffffffeu), min(o[1], 0xfffffffeu)}, wrs, wo_, 0, 17);
    lds_inc<SY_FS>(sya, one);
    ST(8);
    __builtin_amdgcn_s_setprio(PBU);
  }
  f32x2 kd;
  lds_poll<12, 1>(cna, 1);
  KLd<4 * NB + 1, NB>::go(K, kpa);
  K8f[0] = rdk<0>(k8a); K8f[1] = rdk<64>(k8a);
  if (FULL) { K8f[2] = rdk<128>(k8b); K8f[3] = rdk<192>(k8b); }
  asm volatile("ds_read_b128 %0, %3\n\tds_read_b128 %1, %3 offset:1024\n\tds_read_b64 %2, %4" : "=&v"(be), "=&v"(bo), "=&v"(kd) : "v"(bna), "v"(kfa) : "memory");
  ST(1);
  lds_poll<SY_FS, 2>(sya, 1);
  u32x4 x4, f0, f1;
  {
    int spins = 0;
    __builtin_amdgcn_s_setprio(FULL ? PBUF : PBU2);
    if (FULL || wq < 3) __builtin_amdgcn_s_sleep(layer ? FSLEEP : FSLEEP0); else __builtin_amdgcn_s_sleep((layer ? FSLEEP : FSLEEP0) - 5);
    _Pragma("unroll 1")
    while (true) {
      asm volatile("" ::: "memory");
      f0 = __builtin_amdgcn_raw_buffer_load_b128(frs, c16, 0, 16);
      f1 = __builtin_amdgcn_raw_buffer_load_b128(frs, c16, 4096, 16);
      x4 = __builtin_amdgcn_raw_buffer_load_b128(xrs, c16, 0, 16);
      const u32 m = umax3(umax3(umax3(f0[0], f0[1], f0[2]), f0[3], f1[0]), umax3(umax3(f1[1], f1[2], f1[3]), x4[0], x4[1]), max(x4[2], x4[3]));
      if (__builtin_amdgcn_ballot_w64(m == 0xffffffffu) == 0 || ++spins > GCAP) break;
      __builtin_amdgcn_s_sleep(FPOLL);
    }
    RETRY(8 * layer + 6, spins);
  }
  ST(2);
  float bs[8];
  {
    float uf[8];
#pragma unroll
    for (int e = 0; e < 8; ++e) {
      const u32 f = e < 4 ? f0[e & 3] : f1[e & 3];
      bs[e] = (e & 1) ? hi_f(x4[e >> 1]) : lo_f(x4[e >> 1]);
      uf[e] = (bs[e] - __uint_as_float(f)) * __builtin_amdgcn_rcpf(lo_f(f));
    }
    const u32x4 uv = {packh(uf[0], uf[1]), packh(uf[2], uf[3]), packh(uf[4], uf[5]), packh(uf[6], uf[7])};
    asm volatile("ds_write_b128 %0, %1" :: "v"(uw_), "v"(uv) : "memory");
    lds_inc<0>(cna, one);
    PIN(kd);
    if (FULL) {
#pragma unroll
      for (int e = 0; e < 8; ++e) { bs[e] = fmaf(uf[e], kd[1], bs[e]); PIN(bs[e]); }
    }
  }
  if (FULL && PBL != PBL1 && gq) __builtin_amdgcn_s_setprio(PBL1); else __builtin_amdgcn_s_setprio(FULL ? PBL : PBH);
  lds_poll<0, 0>(cna, 4);
  ST(3);
  typedef __attribute__((address_space(3))) const f32x4* lpf;
  bf16x8 F[4];
  s16x4 wa[4];
  u32x4 ur;
  asm volatile("ds_read_b128 %3, %4 offset:512\n\tds_read_b128 %2, %4 offset:448\n\tds_read_b128 %1, %4 offset:384\n\tds_read_b128 %0, %5 offset:288"
               : "=&v"(F[0]), "=&v"(F[1]), "=&v"(F[2]), "=&v"(F[3]) : "v"(ub), "v"(ubx) : "memory");
  asm volatile("ds_read_b64 %0, %4\n\tds_read_b64 %1, %5\n\tds_read_b64 %2, %4 offset:288\n\tds_read_b64 %3, %5 offset:288\n\ts_waitcnt lgkmcnt(5)"
               : "=&v"(wa[0]), "=&v"(wa[1]), "=&v"(wa[2]), "=&v"(wa[3]) : "v"(p0), "v"(p1) : "memory");
  f32x4 acc[4], far, far1;
  bf16x8 xw[4];
  u32x4 xe;
#pragma unroll
  for (int c = 0; c < NB; ++c) {
    if (c == NB / 2) {
      lds_poll<4, 0>(cna, 4);
      asm volatile("ds_read_b128 %0, %2\n\tds_read_b128 %1, %2 offset:64" : "=&v"(xw[0]), "=&v"(xw[1]) : "v"(xr) : "memory");
      if (FULL) asm volatile("ds_read_b128 %0, %2 offset:128\n\tds_read_b128 %1, %2 offset:192" : "=&v"(xw[2]), "=&v"(xw[3]) : "v"(xr) : "memory");
    }
    if (c == 0) blk4<true>(acc, F, K, ub, ubx); else blk4<false>(acc, F, K + 4 * c, ub, ubx);
    ub -= 288; ubx -= 288;
    ST(10 + c);
    if (c == 0) {
      asm volatile("" : "+v"(wa[0]), "+v"(wa[1]), "+v"(wa[2]), "+v"(wa[3]), "+v"(be), "+v"(bo));
      if (!FULL) asm volatile("ds_read_b128 %0, %1" : "=v"(ur) : "v"(ue) : "memory");
#pragma unroll
      for (int i = 0; i < 2; ++i) {
        const bf16x8 af = __builtin_shufflevector(wa[2 * i], wa[2 * i + 1], 0, 1, 2, 3, 4, 5, 6, 7);
        const f32x4 we = MFH(af, be, ((f32x4){0.f, 0.f, 0.f, 0.f}));
        const f32x4 wo = MFH(af, bo, ((f32x4){0.f, 0.f, 0.f, 0.f}));
        const u32x2 d = {packh(we[0], wo[1]), packh(we[2], wo[3])};
        asm volatile("ds_write_b64 %0, %1 offset:%2" :: "v"(xd), "v"(d), "i"(32 * i) : "memory");
      }
      lds_inc<4>(cna, one);
      if (!FULL) xe = __builtin_amdgcn_raw_buffer_load_b128(xrs, eck * 16, 0, 16);
      if (!layer) {
        const __amdgpu_buffer_rsrc_t mrs = __builtin_amdgcn_make_buffer_rsrc(mark + b * 2032, 0, 2032 * 16, 0x00020000);
#pragma unroll
        for (int i = 0; i < 3; ++i) __builtin_amdgcn_raw_buffer_store_b128(MARK16, mrs, t * 16, i * 12288, 17);
      }
    }
    if (c == NB / 2) {
      PIN(xw[0]); PIN(xw[1]);
      far = (f32x4){0.f, 0.f, 0.f, 0.f}; far1 = far;
      if (np) {
        far = MFH(xw[0], K8f[0], far);
        far1 = MFH(xw[1], K8f[1], far1);
      }
      if (FULL && np == 2) {
        PIN(xw[2]); PIN(xw[3]);
        far = MFH(xw[2], K8f[FULL ? 2 : 0], far);
        far1 = MFH(xw[3], K8f[FULL ? 3 : 0], far1);
      }
      PIN(far); PIN(far1);
    }
  }
  if (xtra) {
    asm volatile(
        "v_mfma_f32_16x16x32_f16 %3, %8, %7, %3\n\t"
        "v_mfma_f32_16x16x32_f16 %2, %8, %6, %2\n\t"
        "v_mfma_f32_16x16x32_f16 %1, %8, %5, %1\n\t"
        "s_waitcnt lgkmcnt(0)\n\t"
        "v_mfma_f32_16x16x32_f16 %0, %8, %4, %0"
        : "+v"(acc[0]), "+v"(acc[1]), "+v"(acc[2]), "+v"(acc[3]) : "v"(F[0]), "v"(F[1]), "v"(F[2]), "v"(F[3]), "v"(K[4 * NB]));
  }
  far += far1;
  asm volatile("ds_write_b128 %0, %1" :: "v"(frw), "v"(far) : "memory");
  __builtin_amdgcn_s_waitcnt(0x0f70);
  asm volatile("s_waitcnt lgkmcnt(1)\n\ts_nop 7" : "+v"(acc[0]), "+v"(acc[1]), "+v"(acc[2]), "+v"(acc[3]), "+v"(F[0]) :: "memory");
  if (FULL) {
    asm volatile("ds_write_b128 %0, %4 offset:384\n\tds_write_b128 %0, %3 offset:256\n\tds_write_b128 %0, %2 offset:128\n\tds_write_b128 %0, %1"
                 :: "v"(exw), "v"(acc[0]), "v"(acc[1]), "v"(acc[2]), "v"(acc[3]) : "memory");
  } else {
#pragma unroll
    for (int j = 3; j >= 0; --j) asm volatile("ds_write_b128 %0, %1" :: "v"(exw + ((16 * j + 4 * g + 4 * n) & 63) * 4), "v"(acc[j]) : "memory");
  }
  lds_inc<8>(cna, one);
  __builtin_amdgcn_s_setprio(PBE);
  ST(4);
  if (FULL || wq < 2) {
    if (!FULL) {
      PIN(ur);
#pragma unroll
      for (int e = 0; e < 8; ++e) {
        bs[e] = (e & 1) ? fmaf(hi_h(ur[e >> 1]), kd[1], hi_f(xe[e >> 1])) : fmaf(lo_h(ur[e >> 1]), kd[1], lo_f(xe[e >> 1]));
        PIN(bs[e]);
      }
    }
    lds_poll<8, 0>(cna, 4);
    const float kf = kd[0];
    f32x4 sa, sb;
    if (FULL) {
      const f32x4 e0 = *(lpf)(size_t)exr, e1 = *(lpf)(size_t)(exr + 16), e2 = *(lpf)(size_t)(exr + 8448), e3 = *(lpf)(size_t)(exr + 8464);
      const f32x4 g0 = *(lpf)(size_t)frr, g1 = *(lpf)(size_t)(frr + 16);
#pragma unroll
      for (int r = 0; r < 4; ++r) { sa[r] = fmaf(fmaf(g0[r], fm, e0[r] + e2[r]), kf, bs[r]); sb[r] = fmaf(fmaf(g1[r], fm, e1[r] + e3[r]), kf, bs[4 + r]); }
    } else {
      f32x4 ea[4], eb[4];
#pragma unroll
      for (int i = 0; i < 4; ++i) { ea[i] = *(lpf)(size_t)(exr + 4096 * i); eb[i] = *(lpf)(size_t)(exr1 + 4096 * i); }
      const f32x4 g0 = *(lpf)(size_t)frr, g1 = *(lpf)(size_t)(frr + 16), g2 = *(lpf)(size_t)(frr + 2048), g3 = *(lpf)(size_t)(frr + 2064);
#pragma unroll
      for (int r = 0; r < 4; ++r) {
        sa[r] = fmaf(fmaf(g0[r] + g2[r], fm, (ea[0][r] + ea[1][r]) + (ea[2][r] + ea[3][r])), kf, bs[r]);
        sb[r] = fmaf(fmaf(g1[r] + g3[r], fm, (eb[0][r] + eb[1][r]) + (eb[2][r] + eb[3][r])), kf, bs[4 + r]);
      }
    }
    const u32x4 ov = {min(pack2(sa[0], sa[1]), 0xfffffffeu), min(pack2(sa[2], sa[3]), 0xfffffffeu), min(pack2(sb[0], sb[1]), 0xfffffffeu), min(pack2(sb[2], sb[3]), 0xfffffffeu)};
    __builtin_amdgcn_raw_buffer_store_b128(ov, ors, od, 0, BAUX);
    if (!layer) __builtin_amdgcn_raw_buffer_store_b128(ov, ors, oh, 0, BAUX);
  }
}

template <int layer>
__device__ __forceinline__ void b_phase(unsigned char* smem, const int b, const int t, const u16* xtin, float2* stats, u32* fstat,
                                        u16* xtout, u32x4* mark) {
  const int kid = layer ? 3 : 5;
  const int lane = t & 63, w = __builtin_amdgcn_readfirstlane(t >> 6);
  const int gq = w < 4 ? 2 : (w >> 2) - 1, wq = w & 3;
  const int cG = CHAN(b, gq);
  ST_DECL
  if (w != 3) __builtin_amdgcn_s_setprio(PBU); else __builtin_amdgcn_s_setprio(3);
  if (w < 3) {
    const u32 sya = LDSA(smem) + SY_OFF + 256 * layer;
    lds_poll<SY_FREE, 2>(sya, 8);
    b_tables(smem, w, lane);
    lds_inc<80 + 12>(sya + 16 * w, 1u);
    ST(9);
  }
#define TSARG
  if (gq < 2) b_conv<true, layer>(smem, b, t, gq, wq, cG, xtin, stats, fstat, xtout, mark TSARG);
  else b_conv<false, layer>(smem, b, t, gq, wq, cG, xtin, stats, fstat, xtout, mark TSARG);
  ST(5);
  ST_FLUSH(kid);
}

struct MainArgs { const u16 *x, *wqkv, *wo; unsigned char* ws; u16* out; };
__global__ __launch_bounds__(768, 3) void k_main(MainArgs ma, KgenArgs ka) {
  __shared__ __attribute__((aligned(16))) unsigned char smem[A_LDS + 64];
  const int b = BIDX;
  u32x4 sink;
  int sb = b, t0 = TIDX;
  asm volatile(
      "s_cmp_lt_u32 %1, 16\n\t"
      "s_cbranch_scc0 1f\n\t"
      "v_cmp_gt_u32 vcc, 0x80, %2\n\t"
      "s_and_b64 exec, exec, vcc\n\t"
      "s_getpc_b64 s[40:41]\n\t"
      "s_add_u32 s40, s40, _Z6k_main8MainArgs8KgenArgs@rel32@lo+4\n\t"
      "s_addc_u32 s41, s41, _Z6k_main8MainArgs8KgenArgs@rel32@hi+12\n\t"
      "s_lshl_b32 s42, %1, 11\n\t"
      "s_and_b32 s42, s42, 0x4000\n\t"
      "v_lshl_add_u32 %3, %2, 7, s42\n\t"
      "s_mov_b32 s42, kmain_tail-_Z6k_main8MainArgs8KgenArgs\n\t"
      "global_load_dword %0, %3, s[40:41]\n\t"
      "v_add_u32 %3, 0x8000, %3\n\t"
      "v_min_u32 %4, s42, %3\n\t"
      "global_load_dword %4, %4, s[40:41]\n\t"
      "s_mov_b64 exec, -1\n\t"
      "1:"
      : "=&v"(sink[0]), "+s"(sb), "+v"(t0), "=&v"(sink[1]), "=&v"(sink[2]) :: "scc", "vcc", "s40", "s41", "s42", "memory");
  unsigned char* ws = ma.ws;
  const int w64 = __builtin_amdgcn_readfirstlane(t0) & ~63;
#define GETT(t) asm volatile("v_mbcnt_lo_u32_b32 %0, -1, %1\n\tv_mbcnt_hi_u32_b32 %0, -1, %0" : "=v"(t) : "s"(w64))
#define LAYER(L) { \
    int t; \
    GETT(t); \
    unsigned char* ho = ws + WS_O + L * HO_BYTES; \
    u16* x1 = (u16*)(ho + HO_X1); \
    u32* fs = (u32*)(ho + HO_FS); \
    u16* nxt = (u16*)(ws + (L ? WS_FIN : WS_IM1)); \
    a_phase<L>(smem, b, t, L ? (const u16*)(ws + WS_IM1) : ma.x, ma.wqkv, ma.wo, ws, (u16*)ho, x1, (float2*)(ho + HO_ST), fs, ka, sink, tg0, tg1); \
    GETT(t); \
    b_phase<L>(smem, b, t, x1, (float2*)(ho + HO_ST), fs, nxt, (u32x4*)(ws + WS_O + HO_BYTES)); }
  u32 tg0, tg1;
  LAYER(0)
  LAYER(1)
  {
    int t;
    GETT(t);
    const int lane = t & 63, w = __builtin_amdgcn_readfirstlane(t >> 6);
    const int rb = 8 * ((b & 7) >> 1) + (b >> 5), hg = 4 * (b & 1) + ((b >> 3) & 3);
    ST_DECL
    u16* sm = (u16*)smem;
    __syncthreads();
    __builtin_amdgcn_s_sleep(TSLEEP);
    if (w < 10) {
      const __amdgpu_buffer_rsrc_t rs = __builtin_amdgcn_make_buffer_rsrc(ws + WS_FIN + (size_t)(rb * 640 + hg * 80) * 128, 0, 10240, 0x00020000);
      u32x4 v;
      int spins = 0;
      _Pragma("unroll 1")
      while (true) {
        asm volatile("" ::: "memory");
        v = __builtin_amdgcn_raw_buffer_load_b128(rs, t * 16, 0, 16);
        if (__builtin_amdgcn_ballot_w64(umax3(v[0], v[1], max(v[2], v[3])) == 0xffffffffu) == 0 || ++spins > GCAP) break;
        __builtin_amdgcn_s_sleep(FPOLL);
      }
      RETRY(7, spins);
      *(u32x4*)&sm[(t >> 3) * 72 + 8 * (t & 7)] = v;
    }
    ST(1);
    __syncthreads();
    if (w < 10) {
      const int u = t >> 4, rg = (u * 205) >> 11, oc = u - 10 * rg, q = (lane >> 2) & 3, p = lane & 3;
      const u16* base = sm + (8 * oc + q) * 72 + 16 * rg + 4 * p;
      *(bf16x8*)(ma.out + (size_t)(rb * 64 + 16 * rg + (lane & 15)) * D_ + hg * 80 + 8 * oc) = tr_frag(base, base + 4 * 72);
    }
    ST(2);
    ST_FLUSH(4);
  }
}
extern "C" __device__ __attribute__((used, noinline)) void kmain_tail() {}
extern "C" void kernel_launch(void* const* d_in, const int* in_sizes, int n_in, void* d_out, int out_size,
                              void* d_ws, size_t ws_size, hipStream_t stream) {
  unsigned char* ws = (unsigned char*)d_ws;
  KgenArgs ka;
  ka.logA = (const u16*)d_in[4]; ka.Aim = (const u16*)d_in[5]; ka.Cre = (const u16*)d_in[6]; ka.Cim = (const u16*)d_in[7];
  ka.logdt = (const u16*)d_in[8]; ka.Dskip = (const u16*)d_in[9];
  MainArgs ma;
  ma.x = (const u16*)d_in[0];
  ma.wqkv = (const u16*)d_in[1]; ma.wo = (const u16*)d_in[2];
  ma.ws = ws; ma.out = (u16*)d_out;
  k_main<<<256, 768, 0, stream>>>(ma, ka);
}
#if defined(__HIP_DEVICE_COMPILE__) && !defined(ALLOW_PK)
#pragma clang attribute pop
#endif
```

```cpp
#if defined(__HIP_DEVICE_COMPILE__) && !defined(ALLOW_PK)
#pragma clang attribute push(__attribute__((target("no-packed-fp32-ops"))), apply_to = function)
#endif
#include <hip/hip_runtime.h>
#include <hip/hip_bf16.h>
#include <stdint.h>

#define BIDX ((int)__builtin_amdgcn_workgroup_id_x())
#define TIDX ((int)__builtin_amdgcn_workitem_id_x())
typedef unsigned short u16;
typedef unsigned int u32;
typedef unsigned long long u64;
typedef __attribute__((ext_vector_type(8))) short bf16x8;
typedef __attribute__((ext_vector_type(4))) short s16x4;
typedef __attribute__((ext_vector_type(4))) float f32x4;
typedef __attribute__((ext_vector_type(2))) float f32x2;
typedef __attribute__((ext_vector_type(2))) __bf16 bf2v;
typedef __attribute__((address_space(3))) s16x4* lds_s16x4_p;
typedef __attribute__((ext_vector_type(8))) u32 u32x8;
typedef __attribute__((ext_vector_type(4))) u32 u32x4;
typedef __attribute__((ext_vector_type(2))) u32 u32x2;
typedef __attribute__((address_space(1))) u32 gu32;
typedef __attribute__((address_space(1))) u64 gu64;

#define S_ 2048
#define XP 2112
#define D_ 640
#define NST 192
#define CHAN(b, q) ((q) == 2 ? (b) >> 1 : 128 + 256 * (q) + (b))

#define WS_WQ  0
#define WS_WOT (WS_WQ + 1920 * 640 * 2)
#define WS_CW  (WS_WOT + 640 * 640 * 2)
#define WS_KP  (WS_CW + 4 * 2048 * 4)
#define WS_IM0 (WS_KP + 640 * 768 * 4)
#define WS_IM1 (WS_IM0 + 32 * 102400)
#define HO_ST (2048 * 640 * 2)
#define HO_X1 (HO_ST + 2048 * 128)
#define HO_FS (HO_X1 + 640 * XP * 2)
#define HO_BYTES (256 * 1392 * 16)
#define WS_O   (WS_IM1 + 32 * 102400)
#define WS_FIN (WS_O + 2 * HO_BYTES)
#define WS_FLG (WS_FIN + 2048 * 640 * 2)
#define FL_W 64
#define FL_M (FL_W + 480 * 64)
#define FL_M2 (FL_M + 384 * 64)
#define FL_END (FL_M2 + 256 * 8)
#define WS_END (WS_FLG + FL_END)

#define RETRY(i, n)
#define ST_DECL
#define ST(i)
#define ST_FLUSH(kid)

__device__ __forceinline__ float bf2f(u16 x) { return __uint_as_float(((u32)x) << 16); }
__device__ __forceinline__ u32 pack2(float a, float b) {
  f32x2 v = {a, b};
  bf2v r = __builtin_convertvector(v, bf2v);
  return __builtin_bit_cast(u32, r);
}
__device__ __forceinline__ float lo_f(u32 w) { return __uint_as_float(w << 16); }
__device__ __forceinline__ float hi_f(u32 w) { return __uint_as_float(w & 0xffff0000u); }
__device__ __forceinline__ float rbf(float f) { return lo_f(pack2(f, f)); }
__device__ __forceinline__ bf16x8 as_frag(uint4 v) {
  union { uint4 u; bf16x8 f; } c; c.u = v; return c.f;
}
__device__ __forceinline__ float2 cmul(float2 a, float2 b) {
  return make_float2(a.x * b.x - a.y * b.y, a.x * b.y + a.y * b.x);
}
__device__ __forceinline__ float2 cexp_scaled(float zr, float zi, float e) {
  float ar = zr * e, ai = zi * e;
  float m = __expf(ar);
  const float INV2PI_HI = 0.15915493667125702f;
  const float INV2PI_LO = 6.4206382432985265e-09f;
  float t = ai * INV2PI_HI;
  float err = fmaf(ai, INV2PI_HI, -t);
  float r = t - rintf(t);
  r = r + (err + ai * INV2PI_LO);
  float s = __builtin_amdgcn_sinf(r), c = __builtin_amdgcn_cosf(r);
  return make_float2(m * c, m * s);
}
__device__ __forceinline__ void cexp2(f32x2 zr, f32x2 zi, float e, f32x2& re, f32x2& im) {
  f32x2 ar = zr * e, ai = zi * e;
  f32x2 m = {__expf(ar.x), __expf(ar.y)};
  const float INV2PI_HI = 0.15915493667125702f;
  const float INV2PI_LO = 6.4206382432985265e-09f;
  f32x2 t = ai * INV2PI_HI;
  f32x2 err = {fmaf(ai.x, INV2PI_HI, -t.x), fmaf(ai.y, INV2PI_HI, -t.y)};
  f32x2 r = t - (f32x2){rintf(t.x), rintf(t.y)};
  r = r + (err + ai * INV2PI_LO);
  re = m * (f32x2){__builtin_amdgcn_cosf(r.x), __builtin_amdgcn_cosf(r.y)};
  im = m * (f32x2){__builtin_amdgcn_sinf(r.x), __builtin_amdgcn_sinf(r.y)};
}
__device__ __forceinline__ float dpp_ror8(float x) {
  return __builtin_bit_cast(float, __builtin_amdgcn_update_dpp(0, __builtin_bit_cast(int, x), 0x128, 0xf, 0xf, false));
}
__device__ __forceinline__ float BCAST0(float x) {
  return __builtin_bit_cast(float, __builtin_amdgcn_update_dpp(0, __builtin_bit_cast(int, x), 0x150, 0xf, 0xf, false));
}
__device__ __forceinline__ float rowsum16(float x) {
  x += __builtin_bit_cast(float, __builtin_amdgcn_update_dpp(0, __builtin_bit_cast(int, x), 0xB1, 0xf, 0xf, false));
  x += __builtin_bit_cast(float, __builtin_amdgcn_update_dpp(0, __builtin_bit_cast(int, x), 0x4E, 0xf, 0xf, false));
  x += __builtin_bit_cast(float, __builtin_amdgcn_update_dpp(0, __builtin_bit_cast(int, x), 0x141, 0xf, 0xf, false));
  x += __builtin_bit_cast(float, __builtin_amdgcn_update_dpp(0, __builtin_bit_cast(int, x), 0x140, 0xf, 0xf, false));
  return x;
}
__device__ __forceinline__ float dpp_xor1(float x) {
  return __builtin_bit_cast(float, __builtin_amdgcn_update_dpp(0, __builtin_bit_cast(int, x), 0xB1, 0xf, 0xf, false));
}
__device__ __forceinline__ bf16x8 tr_frag(const u16* p0, const u16* p1) {
  s16x4 a = __builtin_amdgcn_ds_read_tr16_b64_v4i16((lds_s16x4_p)p0);
  s16x4 b = __builtin_amdgcn_ds_read_tr16_b64_v4i16((lds_s16x4_p)p1);
  return __builtin_shufflevector(a, b, 0, 1, 2, 3, 4, 5, 6, 7);
}

#define X1_P 248
__device__ __forceinline__ float dot2bf(u32 a, u32 b, float c) {
  return __builtin_amdgcn_fdot2_f32_bf16(__builtin_bit_cast(bf2v, a), __builtin_bit_cast(bf2v, b), c, false);
}
__device__ __forceinline__ float xsum16_32(float x) {
  auto s = __builtin_amdgcn_permlane16_swap(__float_as_uint(x), __float_as_uint(x), false, false);
  x = __uint_as_float(s[0]) + __uint_as_float(s[1]);
  auto t = __builtin_amdgcn_permlane32_swap(__float_as_uint(x), __float_as_uint(x), false, false);
  return __uint_as_float(t[0]) + __uint_as_float(t[1]);
}
__device__ __forceinline__ float xmax16_32(float x) {
  auto s = __builtin_amdgcn_permlane16_swap(__float_as_uint(x), __float_as_uint(x), false, false);
  x = fmaxf(__uint_as_float(s[0]), __uint_as_float(s[1]));
  auto t = __builtin_amdgcn_permlane32_swap(__float_as_uint(x), __float_as_uint(x), false, false);
  return fmaxf(__uint_as_float(t[0]), __uint_as_float(t[1]));
}

#define WT " sc0 sc1"
template <class T> __device__ __forceinline__ void st16(void* p, T v) {
  asm volatile("global_store_dwordx4 %0, %1, off" WT "\n\ts_nop 1" :: "v"(p), "v"(v) : "memory");
}
template <class T> __device__ __forceinline__ void st8(void* p, T v) {
  asm volatile("global_store_dwordx2 %0, %1, off" WT :: "v"(p), "v"(v) : "memory");
}

typedef __attribute__((ext_vector_type(8))) _Float16 f16x8;
typedef __attribute__((ext_vector_type(2))) _Float16 f16x2;
__device__ __forceinline__ u32 packh(float a, float b) {
  f32x2 v = {a, b};
  return __builtin_bit_cast(u32, __builtin_convertvector(v, f16x2));
}
__device__ __forceinline__ float lo_h(u32 w) { return (float)__builtin_bit_cast(f16x2, w).x; }
__device__ __forceinline__ float hi_h(u32 w) { return (float)__builtin_bit_cast(f16x2, w).y; }
__device__ __forceinline__ f16x8 as_hfrag(u32 a, u32 b, u32 c, u32 d) { return __builtin_bit_cast(f16x8, (u32x4){a, b, c, d}); }
__device__ __forceinline__ float wave_max(float x) {
  x = fmaxf(x, __builtin_bit_cast(float, __builtin_amdgcn_update_dpp(0, __builtin_bit_cast(int, x), 0xB1, 0xf, 0xf, false)));
  x = fmaxf(x, __builtin_bit_cast(float, __builtin_amdgcn_update_dpp(0, __builtin_bit_cast(int, x), 0x4E, 0xf, 0xf, false)));
  x = fmaxf(x, __builtin_bit_cast(float, __builtin_amdgcn_update_dpp(0, __builtin_bit_cast(int, x), 0x141, 0xf, 0xf, false)));
  x = fmaxf(x, __builtin_bit_cast(float, __builtin_amdgcn_update_dpp(0, __builtin_bit_cast(int, x), 0x140, 0xf, 0xf, false)));
  return xmax16_32(x);
}
template <int T> __device__ __forceinline__ void pow2_scale(float m, float& s, float& inv) {
  int e = (int)(__float_as_uint(m) >> 23);
  e = min(max(e, T + 2), 250);
  s = __uint_as_float((u32)(254 + T - e) << 23);
  inv = __uint_as_float((u32)(e - T) << 23);
}
__device__ __forceinline__ void cexpr(float zl, float e, float zh, float ep, float& re, float& im) {
  const float m = __builtin_amdgcn_exp2f(zl * e);
  const float r = zh * ep;
  re = m * __builtin_amdgcn_cosf(r);
  im = m * __builtin_amdgcn_sinf(r);
}
struct KgenArgs { const u16 *logA, *Aim, *Cre, *Cim, *logdt, *Dskip; };
#define LDSA(p) ((u32)(size_t)(__attribute__((address_space(3))) void*)(p))
#define PIN(x) asm volatile("" : "+v"(x))
template <int OFF> __device__ __forceinline__ void lds_inc(u32 a, u32 one) {
  asm volatile("s_mov_b64 exec, 1\n\tds_add_u32 %0, %1 offset:%2\n\ts_mov_b64 exec, -1" :: "v"(a), "v"(one), "i"(OFF) : "memory");
}
template <int OFF, int SLEEP> __device__ __forceinline__ void lds_poll(u32 a, int target) {
  u32 v; int s, cnt;
  asm volatile(
      "s_mov_b32 %2, 0x100000\n\t"
      "1:\n\t"
      "ds_read_b32 %0, %3 offset:%5\n\t"
      "s_waitcnt lgkmcnt(0)\n\t"
      "v_readfirstlane_b32 %1, %0\n\t"
      "s_nop 0\n\t"
      "s_cmp_ge_i32 %1, %4\n\t"
      "s_cbranch_scc1 2f\n\t"
      "s_sleep %6\n\t"
      "s_sub_u32 %2, %2, 1\n\t"
      "s_cbranch_scc0 1b\n\t"
      "2:\n\t"
      : "=&v"(v), "=&s"(s), "=&s"(cnt) : "v"(a), "s"(target), "i"(OFF), "i"(SLEEP) : "memory", "scc");
}
template <int OFF, int SLEEP> __device__ __forceinline__ void lds_poll2(u32 a, int target, u32& v) {
  int s, cnt;
  asm volatile(
      "v_readfirstlane_b32 %1, %0\n\t"
      "s_mov_b32 %2, 0x100000\n\t"
      "s_cmp_ge_i32 %1, %4\n\t"
      "s_cbranch_scc1 2f\n\t"
      "1:\n\t"
      "ds_read_b32 %0, %3 offset:%5\n\t"
      "s_waitcnt lgkmcnt(0)\n\t"
      "v_readfirstlane_b32 %1, %0\n\t"
      "s_nop 0\n\t"
      "s_cmp_ge_i32 %1, %4\n\t"
      "s_cbranch_scc1 2f\n\t"
      "s_sleep %6\n\t"
      "s_sub_u32 %2, %2, 1\n\t"
      "s_cbranch_scc0 1b\n\t"
      "2:\n\t"
      : "+v"(v), "=&s"(s), "=&s"(cnt) : "v"(a), "s"(target), "i"(OFF), "i"(SLEEP) : "memory", "scc");
}
#define SY_PART 0
#define SY_FREE 4
#define SY_WO 8
#define SY_FIN 24
#define SY_X1 32
#define SY_FS 36
#define SY_WI 40
#define SY_KP 44
#define SY_MK 48
#define SY_RV 52
#define SY_TI 56
#define SY_PG 64
#define SY_X 128
#define SY_STAT 168
#define SY_W 172
#define SY_RED 192
#define SY_Z 224
template <int SLEEP> __device__ __forceinline__ void pg_poll(u32 a, u32 target) {
  int cnt = 1 << 20;
  _Pragma("unroll 1")
  while (true) {
    u32x4 v;
    asm volatile("ds_read_b128 %0, %1 offset:%2\n\ts_waitcnt lgkmcnt(0)" : "=v"(v) : "v"(a), "i"(SY_PG) : "memory");
    if ((u32)__builtin_amdgcn_readfirstlane(min(min(v[0], v[1]), min(v[2], v[3]))) >= target || --cnt < 0) break;
    if (SLEEP) __builtin_amdgcn_s_sleep(SLEEP);
  }
}
__device__ __forceinline__ void kgen_units(int lane, const float* Kd, int nk, f32x4 (&acc)[3]) {
  const int i = lane & 15, kg = lane >> 4;
  const float eE = 8.f * (float)i;
  const int r_ = i & 3;
  const float e0 = (float)((i < 8 ? 2 * r_ : r_ == 0 ? 512 : r_ == 1 ? 516 : 512 * r_) + 32 * (i & 4));
  const float e0p = i < 8 ? e0 : 0.25f * e0;
  typedef __attribute__((address_space(3))) const f32x4* lf4;
  typedef __attribute__((address_space(3))) const u32x4* lu4;
  u32 pp = LDSA(Kd) + 16 * kg, pc = pp + (i < 8 ? 0 : 256);
#pragma unroll 1
  for (int ks = 0; ks < nk; ++ks) {
    PIN(pp); PIN(pc);
    const f32x4 zl = *(lf4)(size_t)pp, zs = ((lf4)(size_t)pc)[16], zh = ((lf4)(size_t)pp)[16];
    const f32x4 lc = ((lf4)(size_t)pp)[48], ph = ((lf4)(size_t)pp)[64];
    const u32x4 ma[2] = {((lu4)(size_t)pc)[80], ((lu4)(size_t)pp)[112]};
    pp += 64; pc += 64;
    float r[4], q[4], gr[4], gi[4];
#pragma unroll
    for (int e = 0; e < 4; ++e) cexpr(zl[e], e0, zs[e], e0p, gr[e], gi[e]);
#pragma unroll
    for (int e = 0; e < 4; ++e) {
      const float m = __builtin_amdgcn_exp2f(fmaf(zl[e], eE, lc[e])), th = fmaf(zh[e], -eE, ph[e]);
      r[e] = m * __builtin_amdgcn_cosf(th);
      q[e] = m * __builtin_amdgcn_sinf(th);
    }
    const f16x8 eh = as_hfrag(packh(r[0], r[1]), packh(r[2], r[3]), packh(q[0], q[1]), packh(q[2], q[3]));
    const u32 g[4] = {packh(gr[0], gr[1]), packh(gr[2], gr[3]), packh(gi[0], gi[1]), packh(gi[2], gi[3])};
    acc[0] = __builtin_amdgcn_mfma_f32_16x16x32_f16(as_hfrag(g[0], g[1], g[2], g[3]), eh, acc[0], 0, 0, 0);
#pragma unroll
    for (int m = 0; m < 2; ++m) {
      u32 d[4];
#pragma unroll
      for (int e = 0; e < 2; ++e) {
        const f16x2 xr = __builtin_bit_cast(f16x2, g[e]), xi = __builtin_bit_cast(f16x2, g[2 + e]);
        const u32 ur = ma[m][e], ui = ma[m][2 + e];
        const f16x2 mr = __builtin_bit_cast(f16x2, ur), mi = __builtin_bit_cast(f16x2, ui);
        d[e] = __builtin_bit_cast(u32, __builtin_elementwise_fma(xr, mr, -(xi * mi)));
        d[2 + e] = __builtin_bit_cast(u32, __builtin_elementwise_fma(xi, mr, xr * mi));
      }
      acc[m + 1] = __builtin_amdgcn_mfma_f32_16x16x32_f16(as_hfrag(d[0], d[1], d[2], d[3]), eh, acc[m + 1], 0, 0, 0);
    }
  }
}
template <int NQ> struct KgenIn { u16 la[NQ], ai[NQ], cr[NQ], ci[NQ], ld, ds; };
template <int NQ, int MODE>
__device__ __forceinline__ void kgen_load(int c, int ks0, int nk, int lane, const KgenArgs& ka, KgenIn<NQ>& in) {
  in.ld = ka.logdt[c];
#pragma unroll
  for (int q = 0; q < NQ; ++q) {
    const int n = 64 * q + lane, gi = c * NST + 16 * ks0 + (n < 16 * nk ? n : 0);
    in.la[q] = ka.logA[gi]; in.ai[q] = ka.Aim[gi]; in.cr[q] = ka.Cre[gi]; in.ci[q] = ka.Cim[gi];
  }
  if (MODE == 1) in.ds = ka.Dskip[c];
}
#define ATC0 1.591508832e-01f
#define ATC1 -5.292989150e-02f
#define ATC2 3.075435195e-02f
#define ATC3 -1.841891266e-02f
#define ATC4 8.268183977e-03f
#define ATC5 -1.824840286e-03f
#define KU 4
__device__ __forceinline__ float kgen_wave(int lane, const KgenIn<1>& in, float* Kd, f32x4 (&acc)[3]) {
#pragma unroll
  for (int m = 0; m < 3; ++m) acc[m] = (f32x4){0.f, 0.f, 0.f, 0.f};
  float cs, csi;
  const u32 za[1] = {in.la[0] | ((u32)in.ai[0] << 16)}, ca[1] = {in.cr[0] | ((u32)in.ci[0] << 16)};
  const float dt = rbf(__expf(bf2f(in.ld)));
  pow2_scale<10>(wave_max(fmaxf(fabsf(bf2f(in.cr[0])), fabsf(bf2f(in.ci[0])))), cs, csi);
  {
    const float zl = (-rbf(__expf(lo_f(za[0]))) * dt) * 1.4426950408889634f, zi = hi_f(za[0]) * dt;
    const float zh = zi * 0.15915494309189535f;
    const float z1 = zh - rintf(zh);
    float ar, ai, br, bi;
    cexpr(zl, 1.0f, z1, 1.0f, ar, ai);
    cexpr(zl, 256.0f, 256.0f * zh - rintf(256.0f * zh), 1.0f, br, bi);
    Kd[lane] = zl;
    Kd[64 + lane] = z1;
    Kd[128 + lane] = 4.0f * zh - rintf(4.0f * zh);
    const float cr = lo_f(ca[0]) * cs, ci = hi_f(ca[0]) * cs, ax = fabsf(cr), ay = fabsf(ci);
    const float tq = fminf(ax, ay) * __builtin_amdgcn_rcpf(fmaxf(fmaxf(ax, ay), 1e-30f)), t2 = tq * tq;
    float an = tq * fmaf(t2, fmaf(t2, fmaf(t2, fmaf(t2, fmaf(t2, ATC5, ATC4), ATC3), ATC2), ATC1), ATC0);
    an = ay > ax ? 0.25f - an : an;
    an = cr < 0.f ? 0.5f - an : an;
    Kd[192 + lane] = 0.5f * __builtin_amdgcn_logf(fmaf(cr, cr, ci * ci));
    Kd[256 + lane] = ci < 0.f ? an : -an;
    u16* kh = (u16*)(Kd + 320) + 8 * (lane >> 2) + (lane & 3);
    const u32 h0 = packh(ar, ai), h1 = packh(br * br - bi * bi, 2.0f * br * bi), h2 = packh(br, bi);
    kh[0] = (u16)h0; kh[4] = (u16)(h0 >> 16);
    kh[128] = (u16)h1; kh[132] = (u16)(h1 >> 16);
    kh[256] = (u16)h2; kh[260] = (u16)(h2 >> 16);
    asm volatile("s_waitcnt lgkmcnt(0)" ::: "memory");
    kgen_units(lane, Kd, KU, acc);
    asm volatile("" ::: "memory");
  }
#pragma unroll
  for (int m = 0; m < 3; ++m) acc[m] *= csi;
  return dt;
}
__device__ __forceinline__ void kgen_fin(int lane, float dt, float dsk, float* Kd, f32x4 (&acc)[3]) {
  const int i = lane & 15, kg = lane >> 4;
  float km = 0.f, ks_, ksi;
#pragma unroll
  for (int m = 0; m < 3; ++m)
#pragma unroll
    for (int r = 0; r < 4; ++r) km = fmaxf(km, fabsf(acc[m][r]));
  pow2_scale<14>(wave_max(km), ks_, ksi);
#pragma unroll
  for (int m = 0; m < 3; ++m) acc[m] *= ks_;
  u16* kq = (u16*)Kd;
  const u32 t0 = packh(acc[2][0], acc[2][1]), t1 = packh(acc[2][2], acc[2][3]);
  if (kg < 2) {
    *(u32x4*)&kq[128 * kg + 8 * i] = (u32x4){packh(acc[0][0], acc[1][0]), packh(acc[0][1], acc[1][1]), packh(acc[0][2], acc[1][2]), packh(acc[0][3], acc[1][3])};
    *(u32x2*)&kq[256 + 64 * kg + 4 * i] = (u32x2){t0, t1};
  } else {
    const u32 s1 = packh(acc[0][2], acc[0][3]);
    *(u32*)&kq[320 + 32 * kg + 2 * i] = packh(acc[0][0], acc[0][1]);
    *(u32*)&kq[384 + 32 * kg + 2 * i] = t0;
    u16* kr = kq + 480 + lane;
    kr[0] = (u16)s1; kr[64] = (u16)(s1 >> 16); kr[32] = (u16)t1; kr[96] = (u16)(t1 >> 16);
    if (lane == 32) kq[640] = (u16)packh(acc[1][3], 0.f);
  }
  if (lane == 0) { Kd[384] = (dt * (2.0f / 256.0f)) * ksi; Kd[385] = dsk * (1.0f / 256.0f); }
}

#define A_R1 105600
#define A_R2 (A_R1 + 80 * X1_P * 2)
#define SY_OFF (A_R2 + 1280)
#define CW_OFF (SY_OFF + 512)
#define KQ_OFF (CW_OFF + 1024)
#define BND_OFF (KQ_OFF + 3 * 3072)
#define WO1_OFF 52800
#define A_LDS (BND_OFF + 3072)
#define VMCNT(n) asm volatile("s_waitcnt vmcnt(" #n ")" ::: "memory")
template <int N> __device__ __forceinline__ void vmwait() { asm volatile("s_waitcnt vmcnt(%0)" :: "i"(N) : "memory"); }
__device__ __forceinline__ void vmwaitn(int n) {
  switch (n) {
    case 0: VMCNT(0); break; case 5: VMCNT(5); break; case 10: VMCNT(10); break; case 15: VMCNT(15); break; default: VMCNT(20); break;
  }
}
__device__ __forceinline__ u32 umax3(u32 a, u32 b, u32 c) { return max(max(a, b), c); }
#define XDEP 2
#define RS0 4
#define RS1 5
#define OSLEEP1 0
#define SSLEEP 11
#define OSLEEP 14
#define PG1B 0
#define PG1 2
#define PG2 2
#define XSLEEP 12
#define WOMODE 2
#define PG1P 3
#define XPOLL 4
#define XEARLY 1
#define WPW 0
#define WSLEEP0 0
#define GCAP (1 << 16)
#define X1T_OFF 130176
#define MARK16 ((u32x4){0xffffffffu, 0xffffffffu, 0xffffffffu, 0xffffffffu})
#define WSLEEP 2
#define IMPOS 2
#define XMPOS 1
#define IMSL 0
#define MSLEEP 32
#define STX(i)
#define STY(i) ST(i)
#define LANE(x) int x; asm volatile("v_mbcnt_lo_u32_b32 %0, -1, 0\n\tv_mbcnt_hi_u32_b32 %0, -1, %0" : "=v"(x))
#define NST0 7
#define NST1 2
#define PF 2
#define PTX 1
#define PTW 1
#define PTA 2
#define WOAT 28
#define FPOS 2
#define PFK 0
#define WARM 0
#define WOSL 0
#define EXP 0
#define XSSL0 24
#define XE 2
#define MSL0 1
#define MSL1 2
#define MSL2 2
#define P0E 2
#define P1F 1
#define MKW 4
#define MKAT 12
#define DQ 5
#define XSAT 20
#define CWAT 4
#define OSLEEP2 0
#define CWW 7
#define PFS 3
#define PTA1 2
#define APS 1
#define OSLEEP3 8
#define PFD 3
#define XSLEEP1 6
#define XJ0 2
#define XJ1 3
#define XSAT0 12
#define PTO 1
#define MKPOS 1
#define PCW 3
#define XSRT 8
#define XJ2 XJ1
#define XSSL 5
__device__ __forceinline__ f32x4 ln_fin(float s, float ss) {
  const float mean = s * (1.0f / 640.0f);
  const float var = fmaxf(ss * (1.0f / 640.0f) - mean * mean, 0.f);
  const float m = -rbf(mean), r = __builtin_amdgcn_rcpf(rbf(__builtin_amdgcn_sqrtf(rbf(rbf(var) + 1e-5f))));
  return (f32x4){m, m, r, r};
}
template <int layer>
__device__ __forceinline__ void a_phase(unsigned char* smem, const int b, const int t, const u16* xt, const u16* wqkv, const u16* wo, unsigned char* ws,
                                        u16* obuf, u16* xtout, float2* stats, const KgenArgs& ka, u32x4& sink, u32& tg0, u32& tg1) {
  u16* wq = (u16*)(ws + WS_WQ);
  u16* wot = (u16*)(ws + WS_WOT);
  const __amdgpu_buffer_rsrc_t frs = __builtin_amdgcn_make_buffer_rsrc(ws + WS_FLG, 0, FL_END, 0x00020000);
  const int kid = layer ? 2 : 1;
  u16* At = (u16*)smem;
  u16* St = (u16*)(smem + A_R1);
  const int xcd = b & 7, idx = b >> 3;
  const int rb = 8 * (xcd >> 1) + (idx & 7), hg = 4 * (xcd & 1) + (idx >> 3), s0 = rb * 64;
  const int rot = hg >> 1, xs0 = 3 * rot + 2 * (hg & 1);
#define PAIR(p, r) ((p) < 6 ? (p) : 6 + (((p) - 6 + (r)) & 3))
  const int lane = t & 63, w = __builtin_amdgcn_readfirstlane(t >> 6);
  const int l15 = lane & 15, g = lane >> 4;
  u32 sya = LDSA(smem) + SY_OFF + 256 * layer, one = 1u;
  const bf16x8 onesf = as_frag(make_uint4(0x3f803f80u, 0x3f803f80u, 0x3f803f80u, 0x3f803f80u));
  u32x2 tag;
  ST_DECL

  const __amdgpu_buffer_rsrc_t xsrs = __builtin_amdgcn_make_buffer_rsrc(ws + WS_IM0 + layer * 262144, 0, 262144, 0x00020000);
  if (!layer) {
#define XDMA0(P) { const int pc = PAIR(P, rot); \
      const u16* sp = xt + (size_t)(s0 - 16 + (lane >> 3)) * D_ + 8 * ((lane & 7) ^ ((lane >> 3) & 6)) + pc * 64; \
      _Pragma("unroll") for (int i = 0; i < 10; ++i) \
        __builtin_amdgcn_global_load_lds((const u32*)(sp + 8 * i * D_ + (i < 2 && !rb ? 16 * D_ : 0)), (u32*)(At + ((P) * 10 + i) * 512), 16, 0, 0); }
#define WPOLL { \
        __builtin_amdgcn_s_sleep(WSLEEP0); \
        int spins = 0; \
        _Pragma("unroll 1") \
        while (true) { \
          asm volatile("" ::: "memory"); \
          const u32x2 f = __builtin_amdgcn_raw_buffer_load_b64(frs, min(lane, 59) * 64, FL_W + hg * 3840, 16); \
          if (__builtin_amdgcn_ballot_w64(f[0] != tg0 || f[1] != tg1) == 0 || ++spins > GCAP) break; \
          __builtin_amdgcn_s_sleep(WSLEEP); \
        } \
        RETRY(0, spins); \
        lds_inc<SY_W>(sya, one); \
      }
    if (__builtin_expect(w < 2, 1)) {
      const int r6 = (lane * 205) >> 11, c10 = lane - 10 * r6;
      const int q = (lane >> 2) & 3, p4 = lane & 3;
      __builtin_amdgcn_s_setprio(3);
      const bool tw = b >= 16;
      const int j = 2 * b - 32 + w, jq = j / 3, part = j - 3 * jq, jk = jq >> 3, jh = jq & 7;
      if (__builtin_expect(tw, 1)) {
        const u16* src = wqkv + (size_t)(32 * jk + r6) * 1920 + part * 640 + jh * 80 + 8 * c10;
        u16* tl = St + 13952 + 2560 * w;
        if (lane < 60) {
#pragma unroll
          for (int i = 0; i < 5; ++i) __builtin_amdgcn_global_load_lds((const u32*)(src + i * 11520), (u32*)(tl + i * 480), 16, 0, 0);
          if (lane < 20) __builtin_amdgcn_global_load_lds((const u32*)(src + 5 * 11520), (u32*)(tl + 5 * 480), 16, 0, 0);
        }
      }
      asm volatile("buffer_load_dwordx2 %0, off, %1, 0 sc1" : "=v"(tag) : "s"(frs) : "memory");
      __builtin_amdgcn_s_barrier();
      const int pb = l15 + (part ? 40 * part - 40 : 80), pd = part ? 120 : 0;
      int vo[5], co[2];
#pragma unroll
      for (int ct = 0; ct < 5; ++ct) {
        const int col = pb + 16 * ct + (16 * ct + l15 >= 40 ? pd : 0);
        vo[ct] = (int)((0xeda6273b51c9840ull >> (4 * (col >> 4))) & 15) * 1024 + (g * 16 + (col & 15)) * 16;
        PIN(vo[ct]);
      }
      co[0] = (pb + 16 * g + (lane >= 40 ? pd : 0)) * 4; co[1] = g ? -1 : (pb + 64 + pd) * 4;
      u32 ta = LDSA(St + 13952 + 2560 * w) + (8 * g + q) * 160 + 8 * p4;
      PIN(ta); PIN(co[0]); PIN(co[1]);
      asm volatile("s_waitcnt vmcnt(0)" : "+v"(tag), "+v"(sink) :: "memory");
      tag[0] += 1u;
      if (__builtin_expect(tw, 1)) {
        const int kp = 2 * PAIR(jk >> 1, -(jh >> 1)) + (jk & 1);
        const __amdgpu_buffer_rsrc_t trs = __builtin_amdgcn_make_buffer_rsrc((void*)(wq + (size_t)jh * 153600 + kp * 7680), 0, 15360, 0x00020000);
        const __amdgpu_buffer_rsrc_t crs = __builtin_amdgcn_make_buffer_rsrc(ws + WS_CW + (jh * 20 + jk) * 1024, 0, 1024, 0x00020000);
        s16x4 fl[5], fh[5];
        asm volatile(
            "ds_read_b64_tr_b16 %0, %10\n\t"
            "ds_read_b64_tr_b16 %5, %10 offset:640\n\t"
            "ds_read_b64_tr_b16 %1, %10 offset:32\n\t"
            "ds_read_b64_tr_b16 %6, %10 offset:672\n\t"
            "ds_read_b64_tr_b16 %2, %10 offset:64\n\t"
            "ds_read_b64_tr_b16 %7, %10 offset:704\n\t"
            "ds_read_b64_tr_b16 %3, %10 offset:96\n\t"
            "ds_read_b64_tr_b16 %8, %10 offset:736\n\t"
            "ds_read_b64_tr_b16 %4, %10 offset:128\n\t"
            "ds_read_b64_tr_b16 %9, %10 offset:768\n\t"
            "s_waitcnt lgkmcnt(0)"
            : "=&v"(fl[0]), "=&v"(fl[1]), "=&v"(fl[2]), "=&v"(fl[3]), "=&v"(fl[4]),
              "=&v"(fh[0]), "=&v"(fh[1]), "=&v"(fh[2]), "=&v"(fh[3]), "=&v"(fh[4])
            : "v"(ta) : "memory");
        ST(1);
        float cs[5];
#pragma unroll
        for (int ct = 0; ct < 5; ++ct) {
          const bf16x8 fr = __builtin_shufflevector(fl[ct], fh[ct], 0, 1, 2, 3, 4, 5, 6, 7);
          asm volatile("buffer_store_dwordx4 %0, %1, %2, 0 offen sc1\n\ts_nop 1" :: "v"(fr), "v"(vo[ct]), "s"(trs) : "memory");
          cs[ct] = __builtin_amdgcn_mfma_f32_16x16x32_bf16(onesf, fr, (f32x4){0.f, 0.f, 0.f, 0.f}, 0, 0, 0)[0];
        }
        const float c03 = g < 2 ? (g ? cs[1] : cs[0]) : (g == 2 ? cs[2] : cs[3]), c4 = g ? cs[0] : cs[4];
        asm volatile("buffer_store_dword %0, %1, %2, 0 offen sc1\n\tbuffer_store_dword %3, %4, %2, 0 offen sc1" :: "v"(c03), "v"(co[0]), "s"(crs), "v"(c4), "v"(co[1]) : "memory");
      }
      lds_inc<SY_TI>(sya, one);
      tg0 = __builtin_amdgcn_readfirstlane(tag[0]); tg1 = __builtin_amdgcn_readfirstlane(tag[1]);
      ST(3);
      XDMA0(2 + w)
      if (tw) {
        VMCNT(10);
        if (lane == 0) asm volatile("buffer_store_dwordx2 %0, off, %1, %2 sc1" :: "v"(tag), "s"(frs), "s"(FL_W + (jh * 60 + jk * 3 + part) * 64) : "memory");
      }
      ST(7);
      XDMA0(5 + w)
      XDMA0(8 + w)
      if (w == 0) {
        if (WPW == 0) WPOLL
        VMCNT(0);
        lds_inc<SY_X>(sya, one);
      } else {
        const __amdgpu_buffer_rsrc_t mrs = __builtin_amdgcn_make_buffer_rsrc(obuf, 0, HO_BYTES, 0x00020000);
        _Pragma("unroll 1")
        for (int i = 0; i < 11; ++i) {
          __builtin_amdgcn_raw_buffer_store_b128(MARK16, mrs, lane * 16, i < 10 ? (rb * 8 + hg) * 10240 + i * 1024 : HO_ST + rb * 8192 + hg * 1024, 17);
          __builtin_amdgcn_s_sleep(MSL0);
        }
        VMCNT(11);
        lds_inc<SY_X>(sya, one);
        ST(6);
        VMCNT(0);
        if (lane == 0) asm volatile("buffer_store_dwordx2 %0, off, %1, %2 sc1" :: "v"(tag), "s"(frs), "s"(FL_M + b * 64) : "memory");
      }
      ST(9);
    } else if (w == 2) {
      const int r6 = (lane * 205) >> 11, c10 = lane - 10 * r6, q = (lane >> 2) & 3, p4 = lane & 3;
      const bool to = b < 128;
      __builtin_amdgcn_s_setprio(3);
      asm volatile("buffer_load_dwordx2 %0, off, %1, 0 sc1" : "=v"(tag) : "s"(frs) : "memory");
      XDMA0(0)
      __builtin_amdgcn_s_barrier();
      XDMA0(1)
      if (to) {
        const u16* src = wo + (size_t)(80 * (b >> 4) + 40 * (b & 1) + r6) * D_ + ((b >> 1) & 7) * 80 + 8 * c10;
        u16* tl = (u16*)(smem + KQ_OFF);
        if (lane < 60) {
#pragma unroll
          for (int i = 0; i < 6; ++i) __builtin_amdgcn_global_load_lds((const u32*)(src + i * 6 * D_), (u32*)(tl + i * 480), 16, 0, 0);
          if (lane < 40) __builtin_amdgcn_global_load_lds((const u32*)(src + 36 * D_), (u32*)(tl + 6 * 480), 16, 0, 0);
        }
      }
      asm volatile("s_waitcnt vmcnt(0)" : "+v"(tag) :: "memory");
      tag[0] += 1u;
      tg0 = __builtin_amdgcn_readfirstlane(tag[0]); tg1 = __builtin_amdgcn_readfirstlane(tag[1]);
      ST(1);
      lds_poll<SY_TI, 1>(sya, 2);
      if (to) {
        const int s = b >> 4;
        const __amdgpu_buffer_rsrc_t trs = __builtin_amdgcn_make_buffer_rsrc((void*)(wot + (size_t)((b >> 1) & 7) * 51200 + ((b & 1) * 2 + (s >> 2)) * 12800), 0, 25600, 0x00020000);
        const u32 t0 = LDSA(smem) + KQ_OFF + (4 * g + q) * 160 + 8 * p4, t1 = LDSA(smem) + KQ_OFF + (16 + q) * 160 + 8 * p4;
        const int o0 = lane * 16, o1 = g ? -1 : lane * 16;
#pragma unroll
        for (int nt = 0; nt < 5; ++nt) {
          s16x4 f[4];
          const int np = (s < 4 ? (nt + 2) % 5 : nt) * 1024;
          asm volatile("ds_read_b64_tr_b16 %0, %4 offset:%6\n\tds_read_b64_tr_b16 %1, %4 offset:%6+3200\n\tds_read_b64_tr_b16 %2, %5 offset:%6\n\tds_read_b64_tr_b16 %3, %5 offset:%6+3200\n\ts_waitcnt lgkmcnt(0)"
                       : "=&v"(f[0]), "=&v"(f[1]), "=&v"(f[2]), "=&v"(f[3]) : "v"(t0), "v"(t1), "i"(32 * nt) : "memory");
          asm volatile("buffer_store_dwordx4 %0, %2, %4, %5 offen sc1\n\tbuffer_store_dwordx4 %1, %3, %4, %6 offen sc1\n\ts_nop 1"
                       :: "v"(__builtin_shufflevector(f[0], f[1], 0, 1, 2, 3, 4, 5, 6, 7)), "v"(__builtin_shufflevector(f[2], f[3], 0, 1, 2, 3, 4, 5, 6, 7)),
                          "v"(o0), "v"(o1), "s"(trs), "s"((s & 3) * 5120 + np), "s"(20480 + (s & 3) * 256 + np) : "memory");
        }
      }
      ST(3);
      XDMA0(4)
      XDMA0(7)
      __builtin_amdgcn_s_waitcnt(0x0070);
      lds_inc<SY_X>(sya, one);
      if (to && lane == 0) asm volatile("buffer_store_dwordx2 %0, off, %1, %2 sc1" :: "v"(tag), "s"(frs), "s"(FL_M + (256 + b) * 64) : "memory");
      ST(9);
    } else {
      const int ar = w - 3, ci = ar % 3;
      KgenIn<1> kin;
      tag = __builtin_amdgcn_raw_buffer_load_b64(frs, 0, 0, 16);
      kgen_load<1, 1>(CHAN(b, ci), 4 * (ar / 3), 4, lane, ka, kin);
      asm volatile("" ::: "memory");
      if (w == 4) asm volatile("ds_write_b64 %0, %1\n\ts_waitcnt lgkmcnt(0)" :: "v"(sya + 8 * lane), "v"((u32x2){0u, 0u}) : "memory");
      __builtin_amdgcn_s_barrier();
      if (w == 8) {
        bf16x8 be, bo, b4;
#pragma unroll
        for (int e = 0; e < 8; ++e) {
          const int d = 8 * g + e - l15;
          be[e] = (d >= 4 && d <= 12) ? (short)0x3c00 : (short)0;
          bo[e] = (d >= 5 && d <= 11) ? (short)0x3c00 : (short)0;
          b4[e] = (l15 < 8 ? d >= 14 && d <= 18 : d >= 11 && d <= 13) ? (short)0x3c00 : (short)0;
        }
        asm volatile("ds_write_b128 %0, %1\n\tds_write_b128 %0, %2 offset:1024\n\tds_write_b128 %0, %3 offset:2048" :: "v"(LDSA(smem) + BND_OFF + 16 * lane), "v"(be), "v"(bo), "v"(b4) : "memory");
      }
      if (w == 3) __builtin_amdgcn_s_setprio(2); else __builtin_amdgcn_s_setprio(1);
      tg0 = __builtin_amdgcn_readfirstlane(tag[0]) + 1u; tg1 = __builtin_amdgcn_readfirstlane(tag[1]);
      f32x4 kacc[3];
      f32x4* ka_ = (f32x4*)(St + ar * 1536);
      const float dt = kgen_wave(lane, kin, (float*)ka_, kacc);
      if (w == 3) ST(6); else ST(4);
      if (w < 9) {
#pragma unroll
        for (int m = 0; m < 3; ++m) ka_[64 * m + lane] = kacc[m];
        lds_inc<SY_KP>(sya, one);
      } else {
        const f32x4* pa = (const f32x4*)St + ci * 192 + lane;
        lds_poll<SY_KP, 2>(sya, 6);
#pragma unroll
        for (int m = 0; m < 3; ++m) { kacc[m] += pa[64 * m] + pa[576 + 64 * m]; PIN(kacc[m]); }
        lds_inc<SY_STAT>(sya, one);
        lds_poll<SY_X, 2>(sya, 3);
        kgen_fin(lane, dt, bf2f(kin.ds), (float*)(smem + KQ_OFF + ci * 3072), kacc);
      }
      if (WPW == 3 && w == 3) { __builtin_amdgcn_s_setprio(3); WPOLL ST(9); }
      __builtin_amdgcn_s_waitcnt(0x0070);
    }
  }
  if (w < 4) {
    {
    LANE(lane);
    const int l15 = lane & 15, g = lane >> 4;
    const __amdgpu_buffer_rsrc_t wrsrc = __builtin_amdgcn_make_buffer_rsrc((void*)(wq + (size_t)hg * 153600), 0, 307200, 0x00020000);
    const int tb = w == 1 ? 1 : 0;
    u32 wa = (w == 3 ? 11 : 4 * w) * 1024 + lane * 16;
    u32 xa = LDSA(At) + (layer ? (4 * g + ((lane >> 2) & 3)) * 32 + 8 * (lane & 3) : (l15 >> 3) * 1024 + (l15 & 7) * 128 + ((g ^ (l15 & 6)) << 4)), xb = xa ^ 64;
    u32 sy = sya + SY_X, pga = sya + SY_PG + 4 * w, cwa = LDSA(smem) + CW_OFF + tb * 64 + g * 16, mra = LDSA(smem) + A_R2 + l15 * 16;
    u32 sta = LDSA(St) + (l15 * X1_P + tb * 16 + 4 * g) * 2, pv;
    int so, s0_, s1_;
    __builtin_amdgcn_s_setprio(PF);
    if (!layer) {
      asm volatile(
        "s_cmp_lt_u32 %[w], 2\ns_cbranch_scc0 .LB%=\ns_mov_b32 %[s1], 0x100000\n.LA0v%=:\nds_read_b32 %[pv], %[sy] offset:0\ns_waitcnt lgkmcnt(0)\nv_readfirstlane_b32 %[s0], %[pv]\ns_nop 0\n"
        "s_cmp_ge_i32 %[s0], 3\ns_cbranch_scc1 .LA0h%=\ns_sleep 1\ns_sub_u32 %[s1], %[s1], 1\ns_cbranch_scc0 .LA0v%=\n.LA0h%=:\ns_mov_b32 %[s1], 0x100000\n.LA0w%=:\n"
        "ds_read_b32 %[pv], %[sy] offset:%[ow]\ns_waitcnt lgkmcnt(0)\nv_readfirstlane_b32 %[s0], %[pv]\ns_nop 0\ns_cmp_ge_i32 %[s0], 1\ns_cbranch_scc1 .LA0g%=\ns_sleep 1\ns_sub_u32 %[s1], %[s1], 1\n"
        "s_cbranch_scc0 .LA0w%=\n.LA0g%=:\ns_mov_b32 %[so], 0\nbuffer_load_dwordx4 v[32:35], %[wa], %[rs], %[so] offen offset:0 sc1\n"
        "buffer_load_dwordx4 v[36:39], %[wa], %[rs], %[so] offen offset:1024 sc1\nbuffer_load_dwordx4 v[40:43], %[wa], %[rs], %[so] offen offset:2048 sc1\n"
        "buffer_load_dwordx4 v[44:47], %[wa], %[rs], %[so] offen offset:3072 sc1\ns_mov_b32 %[so], 15360\nbuffer_load_dwordx4 v[48:51], %[wa], %[rs], %[so] offen offset:0 sc1\n"
        "buffer_load_dwordx4 v[52:55], %[wa], %[rs], %[so] offen offset:1024 sc1\nbuffer_load_dwordx4 v[56:59], %[wa], %[rs], %[so] offen offset:2048 sc1\n"
        "buffer_load_dwordx4 v[60:63], %[wa], %[rs], %[so] offen offset:3072 sc1\ns_mov_b32 %[so], 30720\nbuffer_load_dwordx4 v[64:67], %[wa], %[rs], %[so] offen offset:0 sc1\n"
        "buffer_load_dwordx4 v[68:71], %[wa], %[rs], %[so] offen offset:1024 sc1\nbuffer_load_dwordx4 v[72:75], %[wa], %[rs], %[so] offen offset:2048 sc1\n"
        "buffer_load_dwordx4 v[76:79], %[wa], %[rs], %[so] offen offset:3072 sc1\ns_mov_b32 %[so], 46080\nbuffer_load_dwordx4 v[80:83], %[wa], %[rs], %[so] offen offset:0 sc1\n"
        "buffer_load_dwordx4 v[84:87], %[wa], %[rs], %[so] offen offset:1024 sc1\nbuffer_load_dwordx4 v[88:91], %[wa], %[rs], %[so] offen offset:2048 sc1\n"
        "buffer_load_dwordx4 v[92:95], %[wa], %[rs], %[so] offen offset:3072 sc1\nds_read_b128 v[16:19], %[xa] offset:2048\nds_read_b128 v[20:23], %[xa] offset:4096\n"
        "ds_read_b128 v[12:15], %[xa] offset:0\nds_read_b128 v[24:27], %[xa] offset:6144\ns_waitcnt vmcnt(12) lgkmcnt(3)\nv_mfma_f32_16x16x32_bf16 v[100:103], v[32:35], v[16:19], 0\n"
        "ds_read_b128 v[28:31], %[xa] offset:8192\nv_mfma_f32_16x16x32_bf16 v[116:119], v[36:39], v[16:19], 0\nv_mfma_f32_16x16x32_bf16 v[136:139], v[40:43], v[16:19], 0\n"
        "v_mfma_f32_16x16x32_bf16 v[152:155], v[44:47], v[16:19], 0\nds_read_b128 v[16:19], %[xb] offset:2048\ns_waitcnt lgkmcnt(4)\nv_mfma_f32_16x16x32_bf16 v[104:107], v[32:35], v[20:23], 0\n"
        "v_mfma_f32_16x16x32_bf16 v[120:123], v[36:39], v[20:23], 0\nv_mfma_f32_16x16x32_bf16 v[140:143], v[40:43], v[20:23], 0\nv_mfma_f32_16x16x32_bf16 v[156:159], v[44:47], v[20:23], 0\n"
        "ds_read_b128 v[20:23], %[xb] offset:4096\ns_waitcnt lgkmcnt(4)\nv_mfma_f32_16x16x32_bf16 v[96:99], v[32:35], v[12:15], 0\nv_mfma_f32_16x16x32_bf16 v[132:135], v[40:43], v[12:15], 0\n"
        "ds_read_b128 v[12:15], %[xb] offset:0\ns_waitcnt lgkmcnt(4)\nv_mfma_f32_16x16x32_bf16 v[108:111], v[32:35], v[24:27], 0\nv_mfma_f32_16x16x32_bf16 v[124:127], v[36:39], v[24:27], 0\n"
        "v_mfma_f32_16x16x32_bf16 v[144:147], v[40:43], v[24:27], 0\nv_mfma_f32_16x16x32_bf16 v[160:163], v[44:47], v[24:27], 0\nds_read_b128 v[24:27], %[xb] offset:6144\ns_waitcnt lgkmcnt(4)\n"
        "v_mfma_f32_16x16x32_bf16 v[112:115], v[32:35], v[28:31], 0\nv_mfma_f32_16x16x32_bf16 v[128:131], v[36:39], v[28:31], 0\nv_mfma_f32_16x16x32_bf16 v[148:151], v[40:43], v[28:31], 0\n"
        "v_mfma_f32_16x16x32_bf16 v[164:167], v[44:47], v[28:31], 0\n.LA0l%=:\ns_waitcnt vmcnt(8) lgkmcnt(3)\nv_mfma_f32_16x16x32_bf16 v[100:103], v[48:51], v[16:19], v[100:103]\n"
        "s_addk_i32 %[so], 15360\nds_read_b128 v[28:31], %[xb] offset:8192\nv_mfma_f32_16x16x32_bf16 v[116:119], v[52:55], v[16:19], v[116:119]\n"
        "v_mfma_f32_16x16x32_bf16 v[136:139], v[56:59], v[16:19], v[136:139]\nbuffer_load_dwordx4 v[32:35], %[wa], %[rs], %[so] offen offset:0 sc1\n"
        "v_mfma_f32_16x16x32_bf16 v[152:155], v[60:63], v[16:19], v[152:155]\nds_read_b128 v[16:19], %[xa] offset:12288\ns_waitcnt lgkmcnt(4)\n"
        "v_mfma_f32_16x16x32_bf16 v[104:107], v[48:51], v[20:23], v[104:107]\nv_mfma_f32_16x16x32_bf16 v[120:123], v[52:55], v[20:23], v[120:123]\n"
        "buffer_load_dwordx4 v[36:39], %[wa], %[rs], %[so] offen offset:1024 sc1\nv_mfma_f32_16x16x32_bf16 v[140:143], v[56:59], v[20:23], v[140:143]\n"
        "v_mfma_f32_16x16x32_bf16 v[156:159], v[60:63], v[20:23], v[156:159]\nds_read_b128 v[20:23], %[xa] offset:14336\ns_waitcnt lgkmcnt(4)\n"
        "v_mfma_f32_16x16x32_bf16 v[96:99], v[48:51], v[12:15], v[96:99]\nv_mfma_f32_16x16x32_bf16 v[132:135], v[56:59], v[12:15], v[132:135]\nds_read_b128 v[12:15], %[xa] offset:10240\n"
        "s_waitcnt lgkmcnt(4)\nv_mfma_f32_16x16x32_bf16 v[108:111], v[48:51], v[24:27], v[108:111]\nbuffer_load_dwordx4 v[40:43], %[wa], %[rs], %[so] offen offset:2048 sc1\n"
        "v_mfma_f32_16x16x32_bf16 v[124:127], v[52:55], v[24:27], v[124:127]\nv_mfma_f32_16x16x32_bf16 v[144:147], v[56:59], v[24:27], v[144:147]\n"
        "buffer_load_dwordx4 v[44:47], %[wa], %[rs], %[so] offen offset:3072 sc1\nv_mfma_f32_16x16x32_bf16 v[160:163], v[60:63], v[24:27], v[160:163]\nds_read_b128 v[24:27], %[xa] offset:16384\n"
        "s_waitcnt lgkmcnt(4)\nv_mfma_f32_16x16x32_bf16 v[112:115], v[48:51], v[28:31], v[112:115]\nv_mfma_f32_16x16x32_bf16 v[128:131], v[52:55], v[28:31], v[128:131]\nv_add_u32 %[sy], 4, %[sy]\n"
        "v_mfma_f32_16x16x32_bf16 v[148:151], v[56:59], v[28:31], v[148:151]\nds_write_b32 %[pg], %[sy]\nv_mfma_f32_16x16x32_bf16 v[164:167], v[60:63], v[28:31], v[164:167]\n"
        "s_waitcnt vmcnt(8) lgkmcnt(4)\nv_mfma_f32_16x16x32_bf16 v[100:103], v[64:67], v[16:19], v[100:103]\ns_addk_i32 %[so], 15360\nds_read_b128 v[28:31], %[xa] offset:18432\n"
        "v_mfma_f32_16x16x32_bf16 v[116:119], v[68:71], v[16:19], v[116:119]\nv_mfma_f32_16x16x32_bf16 v[136:139], v[72:75], v[16:19], v[136:139]\n"
        "buffer_load_dwordx4 v[48:51], %[wa], %[rs], %[so] offen offset:0 sc1\nv_mfma_f32_16x16x32_bf16 v[152:155], v[76:79], v[16:19], v[152:155]\nds_read_b128 v[16:19], %[xb] offset:12288\n"
        "s_waitcnt lgkmcnt(5)\nv_mfma_f32_16x16x32_bf16 v[104:107], v[64:67], v[20:23], v[104:107]\nv_mfma_f32_16x16x32_bf16 v[120:123], v[68:71], v[20:23], v[120:123]\n"
        "buffer_load_dwordx4 v[52:55], %[wa], %[rs], %[so] offen offset:1024 sc1\nv_mfma_f32_16x16x32_bf16 v[140:143], v[72:75], v[20:23], v[140:143]\n"
        "v_mfma_f32_16x16x32_bf16 v[156:159], v[76:79], v[20:23], v[156:159]\nds_read_b128 v[20:23], %[xb] offset:14336\ns_waitcnt lgkmcnt(5)\n"
        "v_mfma_f32_16x16x32_bf16 v[96:99], v[64:67], v[12:15], v[96:99]\nv_mfma_f32_16x16x32_bf16 v[132:135], v[72:75], v[12:15], v[132:135]\nds_read_b128 v[12:15], %[xb] offset:10240\n"
        "s_waitcnt lgkmcnt(5)\nv_mfma_f32_16x16x32_bf16 v[108:111], v[64:67], v[24:27], v[108:111]\nbuffer_load_dwordx4 v[56:59], %[wa], %[rs], %[so] offen offset:2048 sc1\n"
        "v_mfma_f32_16x16x32_bf16 v[124:127], v[68:71], v[24:27], v[124:127]\nv_mfma_f32_16x16x32_bf16 v[144:147], v[72:75], v[24:27], v[144:147]\n"
        "buffer_load_dwordx4 v[60:63], %[wa], %[rs], %[so] offen offset:3072 sc1\nv_mfma_f32_16x16x32_bf16 v[160:163], v[76:79], v[24:27], v[160:163]\nds_read_b128 v[24:27], %[xb] offset:16384\n"
        "s_waitcnt lgkmcnt(4)\nv_mfma_f32_16x16x32_bf16 v[112:115], v[64:67], v[28:31], v[112:115]\nv_mfma_f32_16x16x32_bf16 v[128:131], v[68:71], v[28:31], v[128:131]\n"
        "v_mfma_f32_16x16x32_bf16 v[148:151], v[72:75], v[28:31], v[148:151]\nv_mfma_f32_16x16x32_bf16 v[164:167], v[76:79], v[28:31], v[164:167]\ns_waitcnt vmcnt(8) lgkmcnt(3)\n"
        "v_mfma_f32_16x16x32_bf16 v[100:103], v[80:83], v[16:19], v[100:103]\ns_addk_i32 %[so], 15360\nds_read_b128 v[28:31], %[xb] offset:18432\n"
        "v_mfma_f32_16x16x32_bf16 v[116:119], v[84:87], v[16:19], v[116:119]\nv_mfma_f32_16x16x32_bf16 v[136:139], v[88:91], v[16:19], v[136:139]\n"
        "buffer_load_dwordx4 v[64:67], %[wa], %[rs], %[so] offen offset:0 sc1\nv_mfma_f32_16x16x32_bf16 v[152:155], v[92:95], v[16:19], v[152:155]\nds_read_b128 v[16:19], %[xa] offset:22528\n"
        "s_waitcnt lgkmcnt(4)\nv_mfma_f32_16x16x32_bf16 v[104:107], v[80:83], v[20:23], v[104:107]\nv_mfma_f32_16x16x32_bf16 v[120:123], v[84:87], v[20:23], v[120:123]\n"
        "buffer_load_dwordx4 v[68:71], %[wa], %[rs], %[so] offen offset:1024 sc1\nv_mfma_f32_16x16x32_bf16 v[140:143], v[88:91], v[20:23], v[140:143]\n"
        "v_mfma_f32_16x16x32_bf16 v[156:159], v[92:95], v[20:23], v[156:159]\nds_read_b128 v[20:23], %[xa] offset:24576\ns_waitcnt lgkmcnt(4)\n"
        "v_mfma_f32_16x16x32_bf16 v[96:99], v[80:83], v[12:15], v[96:99]\nv_mfma_f32_16x16x32_bf16 v[132:135], v[88:91], v[12:15], v[132:135]\nds_read_b128 v[12:15], %[xa] offset:20480\n"
        "s_waitcnt lgkmcnt(4)\nv_mfma_f32_16x16x32_bf16 v[108:111], v[80:83], v[24:27], v[108:111]\nbuffer_load_dwordx4 v[72:75], %[wa], %[rs], %[so] offen offset:2048 sc1\n"
        "v_mfma_f32_16x16x32_bf16 v[124:127], v[84:87], v[24:27], v[124:127]\nv_mfma_f32_16x16x32_bf16 v[144:147], v[88:91], v[24:27], v[144:147]\n"
        "buffer_load_dwordx4 v[76:79], %[wa], %[rs], %[so] offen offset:3072 sc1\nv_mfma_f32_16x16x32_bf16 v[160:163], v[92:95], v[24:27], v[160:163]\nds_read_b128 v[24:27], %[xa] offset:26624\n"
        "s_waitcnt lgkmcnt(4)\nv_mfma_f32_16x16x32_bf16 v[112:115], v[80:83], v[28:31], v[112:115]\nv_mfma_f32_16x16x32_bf16 v[128:131], v[84:87], v[28:31], v[128:131]\nv_add_u32 %[sy], 4, %[sy]\n"
        "v_mfma_f32_16x16x32_bf16 v[148:151], v[88:91], v[28:31], v[148:151]\nds_write_b32 %[pg], %[sy]\nv_mfma_f32_16x16x32_bf16 v[164:167], v[92:95], v[28:31], v[164:167]\n"
        "s_waitcnt vmcnt(8) lgkmcnt(4)\nv_mfma_f32_16x16x32_bf16 v[100:103], v[32:35], v[16:19], v[100:103]\ns_addk_i32 %[so], 15360\nds_read_b128 v[28:31], %[xa] offset:28672\n"
        "v_mfma_f32_16x16x32_bf16 v[116:119], v[36:39], v[16:19], v[116:119]\nv_mfma_f32_16x16x32_bf16 v[136:139], v[40:43], v[16:19], v[136:139]\n"
        "buffer_load_dwordx4 v[80:83], %[wa], %[rs], %[so] offen offset:0 sc1\nv_mfma_f32_16x16x32_bf16 v[152:155], v[44:47], v[16:19], v[152:155]\nds_read_b128 v[16:19], %[xb] offset:22528\n"
        "s_waitcnt lgkmcnt(5)\nv_mfma_f32_16x16x32_bf16 v[104:107], v[32:35], v[20:23], v[104:107]\nv_mfma_f32_16x16x32_bf16 v[120:123], v[36:39], v[20:23], v[120:123]\n"
        "buffer_load_dwordx4 v[84:87], %[wa], %[rs], %[so] offen offset:1024 sc1\nv_mfma_f32_16x16x32_bf16 v[140:143], v[40:43], v[20:23], v[140:143]\n"
        "v_mfma_f32_16x16x32_bf16 v[156:159], v[44:47], v[20:23], v[156:159]\nds_read_b128 v[20:23], %[xb] offset:24576\ns_waitcnt lgkmcnt(5)\n"
        "v_mfma_f32_16x16x32_bf16 v[96:99], v[32:35], v[12:15], v[96:99]\nv_mfma_f32_16x16x32_bf16 v[132:135], v[40:43], v[12:15], v[132:135]\nds_read_b128 v[12:15], %[xb] offset:20480\n"
        "s_waitcnt lgkmcnt(5)\nv_mfma_f32_16x16x32_bf16 v[108:111], v[32:35], v[24:27], v[108:111]\nbuffer_load_dwordx4 v[88:91], %[wa], %[rs], %[so] offen offset:2048 sc1\n"
        "v_mfma_f32_16x16x32_bf16 v[124:127], v[36:39], v[24:27], v[124:127]\nv_mfma_f32_16x16x32_bf16 v[144:147], v[40:43], v[24:27], v[144:147]\n"
        "buffer_load_dwordx4 v[92:95], %[wa], %[rs], %[so] offen offset:3072 sc1\nv_mfma_f32_16x16x32_bf16 v[160:163], v[44:47], v[24:27], v[160:163]\nds_read_b128 v[24:27], %[xb] offset:26624\n"
        "s_waitcnt lgkmcnt(4)\nv_mfma_f32_16x16x32_bf16 v[112:115], v[32:35], v[28:31], v[112:115]\nv_mfma_f32_16x16x32_bf16 v[128:131], v[36:39], v[28:31], v[128:131]\n"
        "v_mfma_f32_16x16x32_bf16 v[148:151], v[40:43], v[28:31], v[148:151]\ns_cmp_lg_u32 %[so], 291840\nv_mfma_f32_16x16x32_bf16 v[164:167], v[44:47], v[28:31], v[164:167]\n"
        "v_add_u32 %[xa], 20480, %[xa]\nv_add_u32 %[xb], 20480, %[xb]\ns_cbranch_scc1 .LA0l%=\ns_waitcnt vmcnt(8) lgkmcnt(3)\nv_mfma_f32_16x16x32_bf16 v[100:103], v[48:51], v[16:19], v[100:103]\n"
        "ds_read_b128 v[28:31], %[xb] offset:8192\nv_mfma_f32_16x16x32_bf16 v[116:119], v[52:55], v[16:19], v[116:119]\nv_mfma_f32_16x16x32_bf16 v[136:139], v[56:59], v[16:19], v[136:139]\n"
        "v_mfma_f32_16x16x32_bf16 v[152:155], v[60:63], v[16:19], v[152:155]\nds_read_b128 v[16:19], %[xa] offset:12288\ns_waitcnt lgkmcnt(4)\n"
        "v_mfma_f32_16x16x32_bf16 v[104:107], v[48:51], v[20:23], v[104:107]\nv_mfma_f32_16x16x32_bf16 v[120:123], v[52:55], v[20:23], v[120:123]\n"
        "v_mfma_f32_16x16x32_bf16 v[140:143], v[56:59], v[20:23], v[140:143]\nv_mfma_f32_16x16x32_bf16 v[156:159], v[60:63], v[20:23], v[156:159]\nds_read_b128 v[20:23], %[xa] offset:14336\n"
        "s_waitcnt lgkmcnt(4)\nv_mfma_f32_16x16x32_bf16 v[96:99], v[48:51], v[12:15], v[96:99]\nv_mfma_f32_16x16x32_bf16 v[132:135], v[56:59], v[12:15], v[132:135]\n"
        "ds_read_b128 v[12:15], %[xa] offset:10240\ns_waitcnt lgkmcnt(4)\nv_mfma_f32_16x16x32_bf16 v[108:111], v[48:51], v[24:27], v[108:111]\n"
        "v_mfma_f32_16x16x32_bf16 v[124:127], v[52:55], v[24:27], v[124:127]\nv_mfma_f32_16x16x32_bf16 v[144:147], v[56:59], v[24:27], v[144:147]\n"
        "v_mfma_f32_16x16x32_bf16 v[160:163], v[60:63], v[24:27], v[160:163]\nds_read_b128 v[24:27], %[xa] offset:16384\ns_waitcnt lgkmcnt(4)\n"
        "v_mfma_f32_16x16x32_bf16 v[112:115], v[48:51], v[28:31], v[112:115]\nv_mfma_f32_16x16x32_bf16 v[128:131], v[52:55], v[28:31], v[128:131]\nv_add_u32 %[sy], 4, %[sy]\n"
        "v_mfma_f32_16x16x32_bf16 v[148:151], v[56:59], v[28:31], v[148:151]\nv_mfma_f32_16x16x32_bf16 v[164:167], v[60:63], v[28:31], v[164:167]\ns_waitcnt vmcnt(4) lgkmcnt(3)\n"
        "v_mfma_f32_16x16x32_bf16 v[100:103], v[64:67], v[16:19], v[100:103]\nds_read_b128 v[28:31], %[xa] offset:18432\nv_mfma_f32_16x16x32_bf16 v[116:119], v[68:71], v[16:19], v[116:119]\n"
        "v_mfma_f32_16x16x32_bf16 v[136:139], v[72:75], v[16:19], v[136:139]\nds_read_b32 %[pv], %[sy] offset:4\nv_mfma_f32_16x16x32_bf16 v[152:155], v[76:79], v[16:19], v[152:155]\n"
        "ds_read_b128 v[16:19], %[xb] offset:12288\ns_waitcnt lgkmcnt(5)\nv_mfma_f32_16x16x32_bf16 v[104:107], v[64:67], v[20:23], v[104:107]\n"
        "v_mfma_f32_16x16x32_bf16 v[120:123], v[68:71], v[20:23], v[120:123]\nv_mfma_f32_16x16x32_bf16 v[140:143], v[72:75], v[20:23], v[140:143]\n"
        "v_mfma_f32_16x16x32_bf16 v[156:159], v[76:79], v[20:23], v[156:159]\nds_read_b128 v[20:23], %[xb] offset:14336\ns_waitcnt lgkmcnt(5)\n"
        "v_mfma_f32_16x16x32_bf16 v[96:99], v[64:67], v[12:15], v[96:99]\nv_mfma_f32_16x16x32_bf16 v[132:135], v[72:75], v[12:15], v[132:135]\nds_read_b128 v[12:15], %[xb] offset:10240\n"
        "s_waitcnt lgkmcnt(5)\nv_mfma_f32_16x16x32_bf16 v[108:111], v[64:67], v[24:27], v[108:111]\nv_mfma_f32_16x16x32_bf16 v[124:127], v[68:71], v[24:27], v[124:127]\n"
        "v_mfma_f32_16x16x32_bf16 v[144:147], v[72:75], v[24:27], v[144:147]\nv_mfma_f32_16x16x32_bf16 v[160:163], v[76:79], v[24:27], v[160:163]\nds_read_b128 v[24:27], %[xb] offset:16384\n"
        "s_waitcnt lgkmcnt(5)\nv_mfma_f32_16x16x32_bf16 v[112:115], v[64:67], v[28:31], v[112:115]\nv_mfma_f32_16x16x32_bf16 v[128:131], v[68:71], v[28:31], v[128:131]\n"
        "v_mfma_f32_16x16x32_bf16 v[148:151], v[72:75], v[28:31], v[148:151]\nv_mfma_f32_16x16x32_bf16 v[164:167], v[76:79], v[28:31], v[164:167]\ns_waitcnt vmcnt(0) lgkmcnt(3)\n"
        "v_mfma_f32_16x16x32_bf16 v[100:103], v[80:83], v[16:19], v[100:103]\nv_readfirstlane_b32 %[s0], %[pv]\nds_read_b128 v[28:31], %[xb] offset:18432\n"
        "v_mfma_f32_16x16x32_bf16 v[116:119], v[84:87], v[16:19], v[116:119]\nv_mfma_f32_16x16x32_bf16 v[136:139], v[88:91], v[16:19], v[136:139]\ns_cmp_ge_i32 %[s0], %[nst]\n"
        "v_mfma_f32_16x16x32_bf16 v[152:155], v[92:95], v[16:19], v[152:155]\ns_cbranch_scc0 .LA0t%=\n.LA0u%=:\ns_waitcnt lgkmcnt(3)\n"
        "v_mfma_f32_16x16x32_bf16 v[104:107], v[80:83], v[20:23], v[104:107]\nv_add_u32 %[sy], 4, %[sy]\nds_read_b128 v[16:19], %[mra] offset:256\n"
        "v_mfma_f32_16x16x32_bf16 v[120:123], v[84:87], v[20:23], v[120:123]\nv_mfma_f32_16x16x32_bf16 v[140:143], v[88:91], v[20:23], v[140:143]\n"
        "v_mfma_f32_16x16x32_bf16 v[156:159], v[92:95], v[20:23], v[156:159]\nds_read_b128 v[32:35], %[cwa] offset:0\ns_waitcnt lgkmcnt(4)\n"
        "v_mfma_f32_16x16x32_bf16 v[96:99], v[80:83], v[12:15], v[96:99]\nds_read_b128 v[36:39], %[cwa] offset:320\nds_read_b128 v[20:23], %[mra] offset:512\n"
        "v_mfma_f32_16x16x32_bf16 v[132:135], v[88:91], v[12:15], v[132:135]\nds_read_b128 v[40:43], %[cwa] offset:640\ns_waitcnt lgkmcnt(6)\n"
        "v_mfma_f32_16x16x32_bf16 v[108:111], v[80:83], v[24:27], v[108:111]\nds_read_b128 v[44:47], %[cwa] offset:512\nds_read_b128 v[12:15], %[mra] offset:0\n"
        "v_mfma_f32_16x16x32_bf16 v[124:127], v[84:87], v[24:27], v[124:127]\nv_add_u32 %[sy], 4, %[sy]\nv_mfma_f32_16x16x32_bf16 v[144:147], v[88:91], v[24:27], v[144:147]\n"
        "v_mfma_f32_16x16x32_bf16 v[160:163], v[92:95], v[24:27], v[160:163]\nds_read_b128 v[24:27], %[mra] offset:768\ns_waitcnt lgkmcnt(8)\n"
        "v_mfma_f32_16x16x32_bf16 v[112:115], v[80:83], v[28:31], v[112:115]\nv_mfma_f32_16x16x32_bf16 v[128:131], v[84:87], v[28:31], v[128:131]\n"
        "v_mfma_f32_16x16x32_bf16 v[148:151], v[88:91], v[28:31], v[148:151]\nv_mfma_f32_16x16x32_bf16 v[164:167], v[92:95], v[28:31], v[164:167]\nds_read_b128 v[28:31], %[mra] offset:1024\n"
        "s_setprio %[pfs]\ns_waitcnt lgkmcnt(1)\n.long 0xd3b04064, 0x1d922120\n.long 0xd3b04066, 0x1d9a2122\n.long 0xd3b04074, 0x1dd22124\n.long 0xd3b04076, 0x1dda2126\n.long 0xd3b14064, 0x18022564\n"
        ".long 0xd3b14066, 0x18022566\n.long 0xd3b04068, 0x1da22920\n.long 0xd3b0406a, 0x1daa2922\n.long 0xd3b14074, 0x18022574\n.long 0xd3b14076, 0x18022576\nv_cvt_pk_bf16_f32 v100, v100, v101\n"
        "v_cvt_pk_bf16_f32 v101, v102, v103\n.long 0xd3b04078, 0x1de22924\n.long 0xd3b0407a, 0x1dea2926\n.long 0xd3b14068, 0x18022d68\n.long 0xd3b1406a, 0x18022d6a\nv_cvt_pk_bf16_f32 v116, v116, v117\n"
        "v_cvt_pk_bf16_f32 v117, v118, v119\nds_write_b64 %[sta], v[100:101] offset:7936\n.long 0xd3b04060, 0x1d821920\n.long 0xd3b04062, 0x1d8a1922\n.long 0xd3b14078, 0x18022d78\n"
        ".long 0xd3b1407a, 0x18022d7a\nv_cvt_pk_bf16_f32 v104, v104, v105\nv_cvt_pk_bf16_f32 v105, v106, v107\nds_write_b64 %[sta], v[116:117] offset:8096\n.long 0xd3b0406c, 0x1db23120\n"
        ".long 0xd3b0406e, 0x1dba3122\n.long 0xd3b14060, 0x18021d60\n.long 0xd3b14062, 0x18021d62\nv_cvt_pk_bf16_f32 v120, v120, v121\nv_cvt_pk_bf16_f32 v121, v122, v123\n"
        "ds_write_b64 %[sta], v[104:105] offset:15872\n.long 0xd3b0407c, 0x1df23124\n.long 0xd3b0407e, 0x1dfa3126\n.long 0xd3b1406c, 0x1802356c\n.long 0xd3b1406e, 0x1802356e\n"
        "v_cvt_pk_bf16_f32 v96, v96, v97\nv_cvt_pk_bf16_f32 v97, v98, v99\nds_write_b64 %[sta], v[120:121] offset:16032\ns_waitcnt lgkmcnt(4)\n.long 0xd3b04070, 0x1dc23920\n"
        ".long 0xd3b04072, 0x1dca3922\n.long 0xd3b1407c, 0x1802357c\n.long 0xd3b1407e, 0x1802357e\nv_cvt_pk_bf16_f32 v108, v108, v109\nv_cvt_pk_bf16_f32 v109, v110, v111\n"
        "ds_write_b64 %[sta], v[96:97] offset:0\n.long 0xd3b04080, 0x1e023924\n.long 0xd3b04082, 0x1e0a3926\n.long 0xd3b14070, 0x18023d70\n.long 0xd3b14072, 0x18023d72\n"
        "v_cvt_pk_bf16_f32 v124, v124, v125\nv_cvt_pk_bf16_f32 v125, v126, v127\nds_write_b64 %[sta], v[108:109] offset:23808\n.long 0xd3b14080, 0x18023d80\n.long 0xd3b14082, 0x18023d82\n"
        "v_cvt_pk_bf16_f32 v112, v112, v113\nv_cvt_pk_bf16_f32 v113, v114, v115\nds_write_b64 %[sta], v[124:125] offset:23968\nv_cvt_pk_bf16_f32 v128, v128, v129\nv_cvt_pk_bf16_f32 v129, v130, v131\n"
        "ds_write_b64 %[sta], v[112:113] offset:31744\nds_write_b64 %[sta], v[128:129] offset:31904\nds_write_b32 %[pg], %[sy]\nv_add_u32 %[sy], 4, %[sy]\n.long 0xd3b04088, 0x1e222128\n"
        ".long 0xd3b0408a, 0x1e2a212a\n.long 0xd3b04098, 0x1e62212c\n.long 0xd3b0409a, 0x1e6a212e\n.long 0xd3b14088, 0x18022588\n.long 0xd3b1408a, 0x1802258a\n.long 0xd3b0408c, 0x1e322928\n"
        ".long 0xd3b0408e, 0x1e3a292a\n.long 0xd3b14098, 0x18022598\n.long 0xd3b1409a, 0x1802259a\nv_cvt_pk_bf16_f32 v136, v136, v137\nv_cvt_pk_bf16_f32 v137, v138, v139\n.long 0xd3b0409c, 0x1e72292c\n"
        ".long 0xd3b0409e, 0x1e7a292e\n.long 0xd3b1408c, 0x18022d8c\n.long 0xd3b1408e, 0x18022d8e\nv_cvt_pk_bf16_f32 v152, v152, v153\nv_cvt_pk_bf16_f32 v153, v154, v155\n"
        "ds_write_b64 %[sta], v[136:137] offset:8256\n.long 0xd3b04084, 0x1e121928\n.long 0xd3b04086, 0x1e1a192a\n.long 0xd3b1409c, 0x18022d9c\n.long 0xd3b1409e, 0x18022d9e\n"
        "v_cvt_pk_bf16_f32 v140, v140, v141\nv_cvt_pk_bf16_f32 v141, v142, v143\nds_write_b64 %[sta], v[152:153] offset:8192\n.long 0xd3b04090, 0x1e423128\n.long 0xd3b04092, 0x1e4a312a\n"
        ".long 0xd3b14084, 0x18021d84\n.long 0xd3b14086, 0x18021d86\nv_cvt_pk_bf16_f32 v156, v156, v157\nv_cvt_pk_bf16_f32 v157, v158, v159\nds_write_b64 %[sta], v[140:141] offset:16192\n"
        ".long 0xd3b040a0, 0x1e82312c\n.long 0xd3b040a2, 0x1e8a312e\n.long 0xd3b14090, 0x18023590\n.long 0xd3b14092, 0x18023592\nv_cvt_pk_bf16_f32 v132, v132, v133\nv_cvt_pk_bf16_f32 v133, v134, v135\n"
        "ds_write_b64 %[sta], v[156:157] offset:16128\n.long 0xd3b04094, 0x1e523928\n.long 0xd3b04096, 0x1e5a392a\n.long 0xd3b140a0, 0x180235a0\n.long 0xd3b140a2, 0x180235a2\n"
        "v_cvt_pk_bf16_f32 v144, v144, v145\nv_cvt_pk_bf16_f32 v145, v146, v147\nds_write_b64 %[sta], v[132:133] offset:320\n.long 0xd3b040a4, 0x1e92392c\n.long 0xd3b040a6, 0x1e9a392e\n"
        ".long 0xd3b14094, 0x18023d94\n.long 0xd3b14096, 0x18023d96\nv_cvt_pk_bf16_f32 v160, v160, v161\nv_cvt_pk_bf16_f32 v161, v162, v163\nds_write_b64 %[sta], v[144:145] offset:24128\n"
        ".long 0xd3b140a4, 0x18023da4\n.long 0xd3b140a6, 0x18023da6\nv_cvt_pk_bf16_f32 v148, v148, v149\nv_cvt_pk_bf16_f32 v149, v150, v151\nds_write_b64 %[sta], v[160:161] offset:24064\n"
        "v_cvt_pk_bf16_f32 v164, v164, v165\nv_cvt_pk_bf16_f32 v165, v166, v167\nds_write_b64 %[sta], v[148:149] offset:32064\nds_write_b64 %[sta], v[164:165] offset:32000\nds_write_b32 %[pg], %[sy]\n"
        "s_branch .LE%=\n.LA0t%=:\ns_mov_b32 %[s1], 0x100000\n.LA0q%=:\nds_read_b32 %[pv], %[sy] offset:4\ns_waitcnt lgkmcnt(0)\nv_readfirstlane_b32 %[s0], %[pv]\ns_nop 0\ns_cmp_ge_i32 %[s0], %[nst]\n"
        "s_cbranch_scc1 .LA0u%=\ns_sleep 1\ns_sub_u32 %[s1], %[s1], 1\ns_cbranch_scc0 .LA0q%=\ns_branch .LA0u%=\n.LB%=:\ns_cmp_eq_u32 %[w], 2\ns_cbranch_scc0 .LC%=\ns_mov_b32 %[s1], 0x100000\n"
        ".LB0v%=:\nds_read_b32 %[pv], %[sy] offset:0\ns_waitcnt lgkmcnt(0)\nv_readfirstlane_b32 %[s0], %[pv]\ns_nop 0\ns_cmp_ge_i32 %[s0], 3\ns_cbranch_scc1 .LB0h%=\ns_sleep 1\n"
        "s_sub_u32 %[s1], %[s1], 1\ns_cbranch_scc0 .LB0v%=\n.LB0h%=:\ns_mov_b32 %[s1], 0x100000\n.LB0w%=:\nds_read_b32 %[pv], %[sy] offset:%[ow]\ns_waitcnt lgkmcnt(0)\n"
        "v_readfirstlane_b32 %[s0], %[pv]\ns_nop 0\ns_cmp_ge_i32 %[s0], 1\ns_cbranch_scc1 .LB0g%=\ns_sleep 1\ns_sub_u32 %[s1], %[s1], 1\ns_cbranch_scc0 .LB0w%=\n.LB0g%=:\ns_mov_b32 %[so], 0\n"
        "buffer_load_dwordx4 v[32:35], %[wa], %[rs], %[so] offen offset:3072 sc1\nbuffer_load_dwordx4 v[36:39], %[wa], %[rs], %[so] offen offset:0 sc1\n"
        "buffer_load_dwordx4 v[40:43], %[wa], %[rs], %[so] offen offset:1024 sc1\nbuffer_load_dwordx4 v[44:47], %[wa], %[rs], %[so] offen offset:2048 sc1\ns_mov_b32 %[so], 15360\n"
        "buffer_load_dwordx4 v[48:51], %[wa], %[rs], %[so] offen offset:3072 sc1\nbuffer_load_dwordx4 v[52:55], %[wa], %[rs], %[so] offen offset:0 sc1\n"
        "buffer_load_dwordx4 v[56:59], %[wa], %[rs], %[so] offen offset:1024 sc1\nbuffer_load_dwordx4 v[60:63], %[wa], %[rs], %[so] offen offset:2048 sc1\ns_mov_b32 %[so], 30720\n"
        "buffer_load_dwordx4 v[64:67], %[wa], %[rs], %[so] offen offset:3072 sc1\nbuffer_load_dwordx4 v[68:71], %[wa], %[rs], %[so] offen offset:0 sc1\n"
        "buffer_load_dwordx4 v[72:75], %[wa], %[rs], %[so] offen offset:1024 sc1\nbuffer_load_dwordx4 v[76:79], %[wa], %[rs], %[so] offen offset:2048 sc1\ns_mov_b32 %[so], 46080\n"
        "buffer_load_dwordx4 v[80:83], %[wa], %[rs], %[so] offen offset:3072 sc1\nbuffer_load_dwordx4 v[84:87], %[wa], %[rs], %[so] offen offset:0 sc1\n"
        "buffer_load_dwordx4 v[88:91], %[wa], %[rs], %[so] offen offset:1024 sc1\nbuffer_load_dwordx4 v[92:95], %[wa], %[rs], %[so] offen offset:2048 sc1\nds_read_b128 v[16:19], %[xa] offset:2048\n"
        "ds_read_b128 v[20:23], %[xa] offset:4096\nds_read_b128 v[12:15], %[xa] offset:0\nds_read_b128 v[24:27], %[xa] offset:6144\ns_waitcnt vmcnt(12) lgkmcnt(3)\n"
        "v_mfma_f32_16x16x32_bf16 v[96:99], v[32:35], v[16:19], 0\nds_read_b128 v[28:31], %[xa] offset:8192\nv_mfma_f32_16x16x32_bf16 v[108:111], v[36:39], v[16:19], 0\n"
        "v_mfma_f32_16x16x32_bf16 v[128:131], v[40:43], v[16:19], 0\nv_mfma_f32_16x16x32_bf16 v[148:151], v[44:47], v[16:19], 0\nds_read_b128 v[16:19], %[xb] offset:2048\ns_waitcnt lgkmcnt(4)\n"
        "v_mfma_f32_16x16x32_bf16 v[100:103], v[32:35], v[20:23], 0\nv_mfma_f32_16x16x32_bf16 v[112:115], v[36:39], v[20:23], 0\nv_mfma_f32_16x16x32_bf16 v[132:135], v[40:43], v[20:23], 0\n"
        "v_mfma_f32_16x16x32_bf16 v[152:155], v[44:47], v[20:23], 0\nds_read_b128 v[20:23], %[xb] offset:4096\ns_waitcnt lgkmcnt(4)\nv_mfma_f32_16x16x32_bf16 v[104:107], v[36:39], v[12:15], 0\n"
        "v_mfma_f32_16x16x32_bf16 v[124:127], v[40:43], v[12:15], 0\nv_mfma_f32_16x16x32_bf16 v[144:147], v[44:47], v[12:15], 0\nds_read_b128 v[12:15], %[xb] offset:0\ns_waitcnt lgkmcnt(4)\n"
        "v_mfma_f32_16x16x32_bf16 v[116:119], v[36:39], v[24:27], 0\nv_mfma_f32_16x16x32_bf16 v[136:139], v[40:43], v[24:27], 0\nv_mfma_f32_16x16x32_bf16 v[156:159], v[44:47], v[24:27], 0\n"
        "ds_read_b128 v[24:27], %[xb] offset:6144\ns_waitcnt lgkmcnt(4)\nv_mfma_f32_16x16x32_bf16 v[120:123], v[36:39], v[28:31], 0\nv_mfma_f32_16x16x32_bf16 v[140:143], v[40:43], v[28:31], 0\n"
        "v_mfma_f32_16x16x32_bf16 v[160:163], v[44:47], v[28:31], 0\n.LB0l%=:\ns_waitcnt vmcnt(8) lgkmcnt(3)\nv_mfma_f32_16x16x32_bf16 v[96:99], v[48:51], v[16:19], v[96:99]\ns_addk_i32 %[so], 15360\n"
        "ds_read_b128 v[28:31], %[xb] offset:8192\nv_mfma_f32_16x16x32_bf16 v[108:111], v[52:55], v[16:19], v[108:111]\nv_mfma_f32_16x16x32_bf16 v[128:131], v[56:59], v[16:19], v[128:131]\n"
        "buffer_load_dwordx4 v[32:35], %[wa], %[rs], %[so] offen offset:3072 sc1\nv_mfma_f32_16x16x32_bf16 v[148:151], v[60:63], v[16:19], v[148:151]\nds_read_b128 v[16:19], %[xa] offset:12288\n"
        "s_waitcnt lgkmcnt(4)\nv_mfma_f32_16x16x32_bf16 v[100:103], v[48:51], v[20:23], v[100:103]\nv_mfma_f32_16x16x32_bf16 v[112:115], v[52:55], v[20:23], v[112:115]\n"
        "buffer_load_dwordx4 v[36:39], %[wa], %[rs], %[so] offen offset:0 sc1\nv_mfma_f32_16x16x32_bf16 v[132:135], v[56:59], v[20:23], v[132:135]\n"
        "v_mfma_f32_16x16x32_bf16 v[152:155], v[60:63], v[20:23], v[152:155]\nds_read_b128 v[20:23], %[xa] offset:14336\ns_waitcnt lgkmcnt(4)\n"
        "v_mfma_f32_16x16x32_bf16 v[104:107], v[52:55], v[12:15], v[104:107]\nv_mfma_f32_16x16x32_bf16 v[124:127], v[56:59], v[12:15], v[124:127]\n"
        "buffer_load_dwordx4 v[40:43], %[wa], %[rs], %[so] offen offset:1024 sc1\nv_mfma_f32_16x16x32_bf16 v[144:147], v[60:63], v[12:15], v[144:147]\nds_read_b128 v[12:15], %[xa] offset:10240\n"
        "s_waitcnt lgkmcnt(4)\nv_mfma_f32_16x16x32_bf16 v[116:119], v[52:55], v[24:27], v[116:119]\nv_mfma_f32_16x16x32_bf16 v[136:139], v[56:59], v[24:27], v[136:139]\n"
        "buffer_load_dwordx4 v[44:47], %[wa], %[rs], %[so] offen offset:2048 sc1\nv_mfma_f32_16x16x32_bf16 v[156:159], v[60:63], v[24:27], v[156:159]\nds_read_b128 v[24:27], %[xa] offset:16384\n"
        "s_waitcnt lgkmcnt(4)\nv_mfma_f32_16x16x32_bf16 v[120:123], v[52:55], v[28:31], v[120:123]\nv_add_u32 %[sy], 4, %[sy]\nv_mfma_f32_16x16x32_bf16 v[140:143], v[56:59], v[28:31], v[140:143]\n"
        "ds_write_b32 %[pg], %[sy]\nv_mfma_f32_16x16x32_bf16 v[160:163], v[60:63], v[28:31], v[160:163]\ns_waitcnt vmcnt(8) lgkmcnt(4)\nv_mfma_f32_16x16x32_bf16 v[96:99], v[64:67], v[16:19], v[96:99]\n"
        "s_addk_i32 %[so], 15360\nds_read_b128 v[28:31], %[xa] offset:18432\nv_mfma_f32_16x16x32_bf16 v[108:111], v[68:71], v[16:19], v[108:111]\n"
        "v_mfma_f32_16x16x32_bf16 v[128:131], v[72:75], v[16:19], v[128:131]\nbuffer_load_dwordx4 v[48:51], %[wa], %[rs], %[so] offen offset:3072 sc1\n"
        "v_mfma_f32_16x16x32_bf16 v[148:151], v[76:79], v[16:19], v[148:151]\nds_read_b128 v[16:19], %[xb] offset:12288\ns_waitcnt lgkmcnt(5)\n"
        "v_mfma_f32_16x16x32_bf16 v[100:103], v[64:67], v[20:23], v[100:103]\nv_mfma_f32_16x16x32_bf16 v[112:115], v[68:71], v[20:23], v[112:115]\n"
        "buffer_load_dwordx4 v[52:55], %[wa], %[rs], %[so] offen offset:0 sc1\nv_mfma_f32_16x16x32_bf16 v[132:135], v[72:75], v[20:23], v[132:135]\n"
        "v_mfma_f32_16x16x32_bf16 v[152:155], v[76:79], v[20:23], v[152:155]\nds_read_b128 v[20:23], %[xb] offset:14336\ns_waitcnt lgkmcnt(5)\n"
        "v_mfma_f32_16x16x32_bf16 v[104:107], v[68:71], v[12:15], v[104:107]\nv_mfma_f32_16x16x32_bf16 v[124:127], v[72:75], v[12:15], v[124:127]\n"
        "buffer_load_dwordx4 v[56:59], %[wa], %[rs], %[so] offen offset:1024 sc1\nv_mfma_f32_16x16x32_bf16 v[144:147], v[76:79], v[12:15], v[144:147]\nds_read_b128 v[12:15], %[xb] offset:10240\n"
        "s_waitcnt lgkmcnt(5)\nv_mfma_f32_16x16x32_bf16 v[116:119], v[68:71], v[24:27], v[116:119]\nv_mfma_f32_16x16x32_bf16 v[136:139], v[72:75], v[24:27], v[136:139]\n"
        "buffer_load_dwordx4 v[60:63], %[wa], %[rs], %[so] offen offset:2048 sc1\nv_mfma_f32_16x16x32_bf16 v[156:159], v[76:79], v[24:27], v[156:159]\nds_read_b128 v[24:27], %[xb] offset:16384\n"
        "s_waitcnt lgkmcnt(4)\nv_mfma_f32_16x16x32_bf16 v[120:123], v[68:71], v[28:31], v[120:123]\nv_mfma_f32_16x16x32_bf16 v[140:143], v[72:75], v[28:31], v[140:143]\n"
        "v_mfma_f32_16x16x32_bf16 v[160:163], v[76:79], v[28:31], v[160:163]\ns_waitcnt vmcnt(8) lgkmcnt(3)\nv_mfma_f32_16x16x32_bf16 v[96:99], v[80:83], v[16:19], v[96:99]\ns_addk_i32 %[so], 15360\n"
        "ds_read_b128 v[28:31], %[xb] offset:18432\nv_mfma_f32_16x16x32_bf16 v[108:111], v[84:87], v[16:19], v[108:111]\nv_mfma_f32_16x16x32_bf16 v[128:131], v[88:91], v[16:19], v[128:131]\n"
        "buffer_load_dwordx4 v[64:67], %[wa], %[rs], %[so] offen offset:3072 sc1\nv_mfma_f32_16x16x32_bf16 v[148:151], v[92:95], v[16:19], v[148:151]\nds_read_b128 v[16:19], %[xa] offset:22528\n"
        "s_waitcnt lgkmcnt(4)\nv_mfma_f32_16x16x32_bf16 v[100:103], v[80:83], v[20:23], v[100:103]\nv_mfma_f32_16x16x32_bf16 v[112:115], v[84:87], v[20:23], v[112:115]\n"
        "buffer_load_dwordx4 v[68:71], %[wa], %[rs], %[so] offen offset:0 sc1\nv_mfma_f32_16x16x32_bf16 v[132:135], v[88:91], v[20:23], v[132:135]\n"
        "v_mfma_f32_16x16x32_bf16 v[152:155], v[92:95], v[20:23], v[152:155]\nds_read_b128 v[20:23], %[xa] offset:24576\ns_waitcnt lgkmcnt(4)\n"
        "v_mfma_f32_16x16x32_bf16 v[104:107], v[84:87], v[12:15], v[104:107]\nv_mfma_f32_16x16x32_bf16 v[124:127], v[88:91], v[12:15], v[124:127]\n"
        "buffer_load_dwordx4 v[72:75], %[wa], %[rs], %[so] offen offset:1024 sc1\nv_mfma_f32_16x16x32_bf16 v[144:147], v[92:95], v[12:15], v[144:147]\nds_read_b128 v[12:15], %[xa] offset:20480\n"
        "s_waitcnt lgkmcnt(4)\nv_mfma_f32_16x16x32_bf16 v[116:119], v[84:87], v[24:27], v[116:119]\nv_mfma_f32_16x16x32_bf16 v[136:139], v[88:91], v[24:27], v[136:139]\n"
        "buffer_load_dwordx4 v[76:79], %[wa], %[rs], %[so] offen offset:2048 sc1\nv_mfma_f32_16x16x32_bf16 v[156:159], v[92:95], v[24:27], v[156:159]\nds_read_b128 v[24:27], %[xa] offset:26624\n"
        "s_waitcnt lgkmcnt(4)\nv_mfma_f32_16x16x32_bf16 v[120:123], v[84:87], v[28:31], v[120:123]\nv_add_u32 %[sy], 4, %[sy]\nv_mfma_f32_16x16x32_bf16 v[140:143], v[88:91], v[28:31], v[140:143]\n"
        "ds_write_b32 %[pg], %[sy]\nv_mfma_f32_16x16x32_bf16 v[160:163], v[92:95], v[28:31], v[160:163]\ns_waitcnt vmcnt(8) lgkmcnt(4)\nv_mfma_f32_16x16x32_bf16 v[96:99], v[32:35], v[16:19], v[96:99]\n"
        "s_addk_i32 %[so], 15360\nds_read_b128 v[28:31], %[xa] offset:28672\nv_mfma_f32_16x16x32_bf16 v[108:111], v[36:39], v[16:19], v[108:111]\n"
        "v_mfma_f32_16x16x32_bf16 v[128:131], v[40:43], v[16:19], v[128:131]\nbuffer_load_dwordx4 v[80:83], %[wa], %[rs], %[so] offen offset:3072 sc1\n"
        "v_mfma_f32_16x16x32_bf16 v[148:151], v[44:47], v[16:19], v[148:151]\nds_read_b128 v[16:19], %[xb] offset:22528\ns_waitcnt lgkmcnt(5)\n"
        "v_mfma_f32_16x16x32_bf16 v[100:103], v[32:35], v[20:23], v[100:103]\nv_mfma_f32_16x16x32_bf16 v[112:115], v[36:39], v[20:23], v[112:115]\n"
        "buffer_load_dwordx4 v[84:87], %[wa], %[rs], %[so] offen offset:0 sc1\nv_mfma_f32_16x16x32_bf16 v[132:135], v[40:43], v[20:23], v[132:135]\n"
        "v_mfma_f32_16x16x32_bf16 v[152:155], v[44:47], v[20:23], v[152:155]\nds_read_b128 v[20:23], %[xb] offset:24576\ns_waitcnt lgkmcnt(5)\n"
        "v_mfma_f32_16x16x32_bf16 v[104:107], v[36:39], v[12:15], v[104:107]\nv_mfma_f32_16x16x32_bf16 v[124:127], v[40:43], v[12:15], v[124:127]\n"
        "buffer_load_dwordx4 v[88:91], %[wa], %[rs], %[so] offen offset:1024 sc1\nv_mfma_f32_16x16x32_bf16 v[144:147], v[44:47], v[12:15], v[144:147]\nds_read_b128 v[12:15], %[xb] offset:20480\n"
        "s_waitcnt lgkmcnt(5)\nv_mfma_f32_16x16x32_bf16 v[116:119], v[36:39], v[24:27], v[116:119]\nv_mfma_f32_16x16x32_bf16 v[136:139], v[40:43], v[24:27], v[136:139]\n"
        "buffer_load_dwordx4 v[92:95], %[wa], %[rs], %[so] offen offset:2048 sc1\nv_mfma_f32_16x16x32_bf16 v[156:159], v[44:47], v[24:27], v[156:159]\nds_read_b128 v[24:27], %[xb] offset:26624\n"
        "s_waitcnt lgkmcnt(4)\nv_mfma_f32_16x16x32_bf16 v[120:123], v[36:39], v[28:31], v[120:123]\nv_mfma_f32_16x16x32_bf16 v[140:143], v[40:43], v[28:31], v[140:143]\ns_cmp_lg_u32 %[so], 291840\n"
        "v_mfma_f32_16x16x32_bf16 v[160:163], v[44:47], v[28:31], v[160:163]\nv_add_u32 %[xa], 20480, %[xa]\nv_add_u32 %[xb], 20480, %[xb]\ns_cbranch_scc1 .LB0l%=\ns_waitcnt vmcnt(8) lgkmcnt(3)\n"
        "v_mfma_f32_16x16x32_bf16 v[96:99], v[48:51], v[16:19], v[96:99]\nds_read_b128 v[28:31], %[xb] offset:8192\nv_mfma_f32_16x16x32_bf16 v[108:111], v[52:55], v[16:19], v[108:111]\n"
        "v_mfma_f32_16x16x32_bf16 v[128:131], v[56:59], v[16:19], v[128:131]\nv_mfma_f32_16x16x32_bf16 v[148:151], v[60:63], v[16:19], v[148:151]\nds_read_b128 v[16:19], %[xa] offset:12288\n"
        "s_waitcnt lgkmcnt(4)\nv_mfma_f32_16x16x32_bf16 v[100:103], v[48:51], v[20:23], v[100:103]\nv_mfma_f32_16x16x32_bf16 v[112:115], v[52:55], v[20:23], v[112:115]\n"
        "v_mfma_f32_16x16x32_bf16 v[132:135], v[56:59], v[20:23], v[132:135]\nv_mfma_f32_16x16x32_bf16 v[152:155], v[60:63], v[20:23], v[152:155]\nds_read_b128 v[20:23], %[xa] offset:14336\n"
        "s_waitcnt lgkmcnt(4)\nv_mfma_f32_16x16x32_bf16 v[104:107], v[52:55], v[12:15], v[104:107]\nv_mfma_f32_16x16x32_bf16 v[124:127], v[56:59], v[12:15], v[124:127]\n"
        "v_mfma_f32_16x16x32_bf16 v[144:147], v[60:63], v[12:15], v[144:147]\nds_read_b128 v[12:15], %[xa] offset:10240\ns_waitcnt lgkmcnt(4)\n"
        "v_mfma_f32_16x16x32_bf16 v[116:119], v[52:55], v[24:27], v[116:119]\nv_mfma_f32_16x16x32_bf16 v[136:139], v[56:59], v[24:27], v[136:139]\n"
        "v_mfma_f32_16x16x32_bf16 v[156:159], v[60:63], v[24:27], v[156:159]\nds_read_b128 v[24:27], %[xa] offset:16384\ns_waitcnt lgkmcnt(4)\n"
        "v_mfma_f32_16x16x32_bf16 v[120:123], v[52:55], v[28:31], v[120:123]\nv_add_u32 %[sy], 4, %[sy]\nv_mfma_f32_16x16x32_bf16 v[140:143], v[56:59], v[28:31], v[140:143]\n"
        "v_mfma_f32_16x16x32_bf16 v[160:163], v[60:63], v[28:31], v[160:163]\ns_waitcnt vmcnt(4) lgkmcnt(3)\nv_mfma_f32_16x16x32_bf16 v[96:99], v[64:67], v[16:19], v[96:99]\n"
        "ds_read_b128 v[28:31], %[xa] offset:18432\nv_mfma_f32_16x16x32_bf16 v[108:111], v[68:71], v[16:19], v[108:111]\nv_mfma_f32_16x16x32_bf16 v[128:131], v[72:75], v[16:19], v[128:131]\n"
        "ds_read_b32 %[pv], %[sy] offset:4\nv_mfma_f32_16x16x32_bf16 v[148:151], v[76:79], v[16:19], v[148:151]\nds_read_b128 v[16:19], %[xb] offset:12288\ns_waitcnt lgkmcnt(5)\n"
        "v_mfma_f32_16x16x32_bf16 v[100:103], v[64:67], v[20:23], v[100:103]\nv_mfma_f32_16x16x32_bf16 v[112:115], v[68:71], v[20:23], v[112:115]\n"
        "v_mfma_f32_16x16x32_bf16 v[132:135], v[72:75], v[20:23], v[132:135]\nv_mfma_f32_16x16x32_bf16 v[152:155], v[76:79], v[20:23], v[152:155]\nds_read_b128 v[20:23], %[xb] offset:14336\n"
        "s_waitcnt lgkmcnt(5)\nv_mfma_f32_16x16x32_bf16 v[104:107], v[68:71], v[12:15], v[104:107]\nv_mfma_f32_16x16x32_bf16 v[124:127], v[72:75], v[12:15], v[124:127]\n"
        "v_mfma_f32_16x16x32_bf16 v[144:147], v[76:79], v[12:15], v[144:147]\nds_read_b128 v[12:15], %[xb] offset:10240\ns_waitcnt lgkmcnt(5)\n"
        "v_mfma_f32_16x16x32_bf16 v[116:119], v[68:71], v[24:27], v[116:119]\nv_mfma_f32_16x16x32_bf16 v[136:139], v[72:75], v[24:27], v[136:139]\n"
        "v_mfma_f32_16x16x32_bf16 v[156:159], v[76:79], v[24:27], v[156:159]\nds_read_b128 v[24:27], %[xb] offset:16384\ns_waitcnt lgkmcnt(5)\n"
        "v_mfma_f32_16x16x32_bf16 v[120:123], v[68:71], v[28:31], v[120:123]\nv_mfma_f32_16x16x32_bf16 v[140:143], v[72:75], v[28:31], v[140:143]\n"
        "v_mfma_f32_16x16x32_bf16 v[160:163], v[76:79], v[28:31], v[160:163]\ns_waitcnt vmcnt(0) lgkmcnt(3)\nv_mfma_f32_16x16x32_bf16 v[96:99], v[80:83], v[16:19], v[96:99]\n"
        "v_readfirstlane_b32 %[s0], %[pv]\nds_read_b128 v[28:31], %[xb] offset:18432\nv_mfma_f32_16x16x32_bf16 v[108:111], v[84:87], v[16:19], v[108:111]\n"
        "v_mfma_f32_16x16x32_bf16 v[128:131], v[88:91], v[16:19], v[128:131]\ns_cmp_ge_i32 %[s0], %[nst]\nv_mfma_f32_16x16x32_bf16 v[148:151], v[92:95], v[16:19], v[148:151]\ns_cbranch_scc0 .LB0t%=\n"
        ".LB0u%=:\ns_waitcnt lgkmcnt(3)\nv_mfma_f32_16x16x32_bf16 v[100:103], v[80:83], v[20:23], v[100:103]\nv_add_u32 %[sy], 4, %[sy]\nds_read_b128 v[16:19], %[mra] offset:256\n"
        "v_mfma_f32_16x16x32_bf16 v[112:115], v[84:87], v[20:23], v[112:115]\nv_mfma_f32_16x16x32_bf16 v[132:135], v[88:91], v[20:23], v[132:135]\n"
        "v_mfma_f32_16x16x32_bf16 v[152:155], v[92:95], v[20:23], v[152:155]\nds_read_b128 v[32:35], %[cwa] offset:448\ns_waitcnt lgkmcnt(4)\n"
        "v_mfma_f32_16x16x32_bf16 v[104:107], v[84:87], v[12:15], v[104:107]\nds_read_b128 v[36:39], %[cwa] offset:128\nds_read_b128 v[20:23], %[mra] offset:512\n"
        "v_mfma_f32_16x16x32_bf16 v[124:127], v[88:91], v[12:15], v[124:127]\nds_read_b128 v[40:43], %[cwa] offset:192\nv_mfma_f32_16x16x32_bf16 v[144:147], v[92:95], v[12:15], v[144:147]\n"
        "ds_read_b128 v[44:47], %[cwa] offset:768\ns_waitcnt lgkmcnt(7)\nv_mfma_f32_16x16x32_bf16 v[116:119], v[84:87], v[24:27], v[116:119]\nv_add_u32 %[sy], 4, %[sy]\n"
        "ds_read_b128 v[12:15], %[mra] offset:0\nv_mfma_f32_16x16x32_bf16 v[136:139], v[88:91], v[24:27], v[136:139]\nv_mfma_f32_16x16x32_bf16 v[156:159], v[92:95], v[24:27], v[156:159]\n"
        "ds_read_b128 v[24:27], %[mra] offset:768\ns_waitcnt lgkmcnt(8)\nv_mfma_f32_16x16x32_bf16 v[120:123], v[84:87], v[28:31], v[120:123]\n"
        "v_mfma_f32_16x16x32_bf16 v[140:143], v[88:91], v[28:31], v[140:143]\nv_mfma_f32_16x16x32_bf16 v[160:163], v[92:95], v[28:31], v[160:163]\nds_read_b128 v[28:31], %[mra] offset:1024\n"
        "s_setprio %[pfs]\ns_waitcnt lgkmcnt(1)\n.long 0xd3b04060, 0x1d822120\n.long 0xd3b04062, 0x1d8a2122\n.long 0xd3b0406c, 0x1db22124\n.long 0xd3b0406e, 0x1dba2126\n.long 0xd3b14060, 0x18022560\n"
        ".long 0xd3b14062, 0x18022562\n.long 0xd3b04080, 0x1e022128\n.long 0xd3b04082, 0x1e0a212a\n.long 0xd3b1406c, 0x1802256c\n.long 0xd3b1406e, 0x1802256e\nv_cvt_pk_bf16_f32 v96, v96, v97\n"
        "v_cvt_pk_bf16_f32 v97, v98, v99\n.long 0xd3b04064, 0x1d922920\n.long 0xd3b04066, 0x1d9a2922\n.long 0xd3b14080, 0x18022580\n.long 0xd3b14082, 0x18022582\nv_cvt_pk_bf16_f32 v108, v108, v109\n"
        "v_cvt_pk_bf16_f32 v109, v110, v111\nds_write_b64 %[sta], v[96:97] offset:8160\n.long 0xd3b04070, 0x1dc22924\n.long 0xd3b04072, 0x1dca2926\n.long 0xd3b14064, 0x18022d64\n"
        ".long 0xd3b14066, 0x18022d66\nv_cvt_pk_bf16_f32 v128, v128, v129\nv_cvt_pk_bf16_f32 v129, v130, v131\nds_write_b64 %[sta], v[108:109] offset:8000\n.long 0xd3b04084, 0x1e122928\n"
        ".long 0xd3b04086, 0x1e1a292a\n.long 0xd3b14070, 0x18022d70\n.long 0xd3b14072, 0x18022d72\nv_cvt_pk_bf16_f32 v100, v100, v101\nv_cvt_pk_bf16_f32 v101, v102, v103\n"
        "ds_write_b64 %[sta], v[128:129] offset:8032\n.long 0xd3b04068, 0x1da21924\n.long 0xd3b0406a, 0x1daa1926\n.long 0xd3b14084, 0x18022d84\n.long 0xd3b14086, 0x18022d86\n"
        "v_cvt_pk_bf16_f32 v112, v112, v113\nv_cvt_pk_bf16_f32 v113, v114, v115\nds_write_b64 %[sta], v[100:101] offset:16096\n.long 0xd3b0407c, 0x1df21928\n.long 0xd3b0407e, 0x1dfa192a\n"
        ".long 0xd3b14068, 0x18021d68\n.long 0xd3b1406a, 0x18021d6a\nv_cvt_pk_bf16_f32 v132, v132, v133\nv_cvt_pk_bf16_f32 v133, v134, v135\nds_write_b64 %[sta], v[112:113] offset:15936\n"
        ".long 0xd3b04074, 0x1dd23124\n.long 0xd3b04076, 0x1dda3126\n.long 0xd3b1407c, 0x18021d7c\n.long 0xd3b1407e, 0x18021d7e\nv_cvt_pk_bf16_f32 v104, v104, v105\nv_cvt_pk_bf16_f32 v105, v106, v107\n"
        "ds_write_b64 %[sta], v[132:133] offset:15968\n.long 0xd3b04088, 0x1e223128\n.long 0xd3b0408a, 0x1e2a312a\n.long 0xd3b14074, 0x18023574\n.long 0xd3b14076, 0x18023576\n"
        "v_cvt_pk_bf16_f32 v124, v124, v125\nv_cvt_pk_bf16_f32 v125, v126, v127\nds_write_b64 %[sta], v[104:105] offset:64\ns_waitcnt lgkmcnt(7)\n.long 0xd3b04078, 0x1de23924\n"
        ".long 0xd3b0407a, 0x1dea3926\n.long 0xd3b14088, 0x18023588\n.long 0xd3b1408a, 0x1802358a\nv_cvt_pk_bf16_f32 v116, v116, v117\nv_cvt_pk_bf16_f32 v117, v118, v119\n"
        "ds_write_b64 %[sta], v[124:125] offset:96\n.long 0xd3b0408c, 0x1e323928\n.long 0xd3b0408e, 0x1e3a392a\n.long 0xd3b14078, 0x18023d78\n.long 0xd3b1407a, 0x18023d7a\n"
        "v_cvt_pk_bf16_f32 v136, v136, v137\nv_cvt_pk_bf16_f32 v137, v138, v139\nds_write_b64 %[sta], v[116:117] offset:23872\n.long 0xd3b1408c, 0x18023d8c\n.long 0xd3b1408e, 0x18023d8e\n"
        "v_cvt_pk_bf16_f32 v120, v120, v121\nv_cvt_pk_bf16_f32 v121, v122, v123\nds_write_b64 %[sta], v[136:137] offset:23904\nv_cvt_pk_bf16_f32 v140, v140, v141\nv_cvt_pk_bf16_f32 v141, v142, v143\n"
        "ds_write_b64 %[sta], v[120:121] offset:31808\nds_write_b64 %[sta], v[140:141] offset:31840\nds_write_b32 %[pg], %[sy]\nv_add_u32 %[sy], 4, %[sy]\n.long 0xd3b04094, 0x1e52212c\n"
        ".long 0xd3b04096, 0x1e5a212e\n.long 0xd3b04098, 0x1e62292c\n.long 0xd3b0409a, 0x1e6a292e\n.long 0xd3b14094, 0x18022594\n.long 0xd3b14096, 0x18022596\n.long 0xd3b04090, 0x1e42192c\n"
        ".long 0xd3b04092, 0x1e4a192e\n.long 0xd3b14098, 0x18022d98\n.long 0xd3b1409a, 0x18022d9a\nv_cvt_pk_bf16_f32 v148, v148, v149\nv_cvt_pk_bf16_f32 v149, v150, v151\n.long 0xd3b0409c, 0x1e72312c\n"
        ".long 0xd3b0409e, 0x1e7a312e\n.long 0xd3b14090, 0x18021d90\n.long 0xd3b14092, 0x18021d92\nv_cvt_pk_bf16_f32 v152, v152, v153\nv_cvt_pk_bf16_f32 v153, v154, v155\n"
        "ds_write_b64 %[sta], v[148:149] offset:8320\n.long 0xd3b040a0, 0x1e82392c\n.long 0xd3b040a2, 0x1e8a392e\n.long 0xd3b1409c, 0x1802359c\n.long 0xd3b1409e, 0x1802359e\n"
        "v_cvt_pk_bf16_f32 v144, v144, v145\nv_cvt_pk_bf16_f32 v145, v146, v147\nds_write_b64 %[sta], v[152:153] offset:16256\n.long 0xd3b140a0, 0x18023da0\n.long 0xd3b140a2, 0x18023da2\n"
        "v_cvt_pk_bf16_f32 v156, v156, v157\nv_cvt_pk_bf16_f32 v157, v158, v159\nds_write_b64 %[sta], v[144:145] offset:384\nv_cvt_pk_bf16_f32 v160, v160, v161\nv_cvt_pk_bf16_f32 v161, v162, v163\n"
        "ds_write_b64 %[sta], v[156:157] offset:24192\nds_write_b64 %[sta], v[160:161] offset:32128\nds_write_b32 %[pg], %[sy]\ns_branch .LE%=\n.LB0t%=:\ns_mov_b32 %[s1], 0x100000\n.LB0q%=:\n"
        "ds_read_b32 %[pv], %[sy] offset:4\ns_waitcnt lgkmcnt(0)\nv_readfirstlane_b32 %[s0], %[pv]\ns_nop 0\ns_cmp_ge_i32 %[s0], %[nst]\ns_cbranch_scc1 .LB0u%=\ns_sleep 1\ns_sub_u32 %[s1], %[s1], 1\n"
        "s_cbranch_scc0 .LB0q%=\ns_branch .LB0u%=\n.LC%=:\ns_mov_b32 %[s1], 0x100000\n.LC0v%=:\nds_read_b32 %[pv], %[sy] offset:0\ns_waitcnt lgkmcnt(0)\nv_readfirstlane_b32 %[s0], %[pv]\ns_nop 0\n"
        "s_cmp_ge_i32 %[s0], 3\ns_cbranch_scc1 .LC0h%=\ns_sleep 1\ns_sub_u32 %[s1], %[s1], 1\ns_cbranch_scc0 .LC0v%=\n.LC0h%=:\ns_mov_b32 %[s1], 0x100000\n.LC0w%=:\n"
        "ds_read_b32 %[pv], %[sy] offset:%[ow]\ns_waitcnt lgkmcnt(0)\nv_readfirstlane_b32 %[s0], %[pv]\ns_nop 0\ns_cmp_ge_i32 %[s0], 1\ns_cbranch_scc1 .LC0g%=\ns_sleep 1\ns_sub_u32 %[s1], %[s1], 1\n"
        "s_cbranch_scc0 .LC0w%=\n.LC0g%=:\ns_mov_b32 %[so], 0\nbuffer_load_dwordx4 v[32:35], %[wa], %[rs], %[so] offen offset:0 sc1\n"
        "buffer_load_dwordx4 v[36:39], %[wa], %[rs], %[so] offen offset:1024 sc1\nbuffer_load_dwordx4 v[40:43], %[wa], %[rs], %[so] offen offset:2048 sc1\n"
        "buffer_load_dwordx4 v[44:47], %[wa], %[rs], %[so] offen offset:3072 sc1\ns_mov_b32 %[so], 15360\nbuffer_load_dwordx4 v[48:51], %[wa], %[rs], %[so] offen offset:0 sc1\n"
        "buffer_load_dwordx4 v[52:55], %[wa], %[rs], %[so] offen offset:1024 sc1\nbuffer_load_dwordx4 v[56:59], %[wa], %[rs], %[so] offen offset:2048 sc1\n"
        "buffer_load_dwordx4 v[60:63], %[wa], %[rs], %[so] offen offset:3072 sc1\ns_mov_b32 %[so], 30720\nbuffer_load_dwordx4 v[64:67], %[wa], %[rs], %[so] offen offset:0 sc1\n"
        "buffer_load_dwordx4 v[68:71], %[wa], %[rs], %[so] offen offset:1024 sc1\nbuffer_load_dwordx4 v[72:75], %[wa], %[rs], %[so] offen offset:2048 sc1\n"
        "buffer_load_dwordx4 v[76:79], %[wa], %[rs], %[so] offen offset:3072 sc1\ns_mov_b32 %[so], 46080\nbuffer_load_dwordx4 v[80:83], %[wa], %[rs], %[so] offen offset:0 sc1\n"
        "buffer_load_dwordx4 v[84:87], %[wa], %[rs], %[so] offen offset:1024 sc1\nbuffer_load_dwordx4 v[88:91], %[wa], %[rs], %[so] offen offset:2048 sc1\n"
        "buffer_load_dwordx4 v[92:95], %[wa], %[rs], %[so] offen offset:3072 sc1\nds_read_b128 v[24:27], %[xa] offset:6144\nds_read_b128 v[28:31], %[xa] offset:8192\n"
        "ds_read_b128 v[12:15], %[xa] offset:0\nds_read_b128 v[16:19], %[xa] offset:2048\ns_waitcnt vmcnt(12) lgkmcnt(3)\nv_mfma_f32_16x16x32_bf16 v[96:99], v[32:35], v[24:27], 0\n"
        "ds_read_b128 v[20:23], %[xa] offset:4096\nv_mfma_f32_16x16x32_bf16 v[116:119], v[36:39], v[24:27], 0\nv_mfma_f32_16x16x32_bf16 v[136:139], v[40:43], v[24:27], 0\n"
        "v_mfma_f32_16x16x32_bf16 v[156:159], v[44:47], v[24:27], 0\nds_read_b128 v[24:27], %[xb] offset:6144\ns_waitcnt lgkmcnt(4)\nv_mfma_f32_16x16x32_bf16 v[100:103], v[32:35], v[28:31], 0\n"
        "v_mfma_f32_16x16x32_bf16 v[120:123], v[36:39], v[28:31], 0\nv_mfma_f32_16x16x32_bf16 v[140:143], v[40:43], v[28:31], 0\nv_mfma_f32_16x16x32_bf16 v[160:163], v[44:47], v[28:31], 0\n"
        "ds_read_b128 v[28:31], %[xb] offset:8192\ns_waitcnt lgkmcnt(4)\nv_mfma_f32_16x16x32_bf16 v[104:107], v[36:39], v[12:15], 0\nv_mfma_f32_16x16x32_bf16 v[124:127], v[40:43], v[12:15], 0\n"
        "v_mfma_f32_16x16x32_bf16 v[144:147], v[44:47], v[12:15], 0\nds_read_b128 v[12:15], %[xb] offset:0\ns_waitcnt lgkmcnt(4)\nv_mfma_f32_16x16x32_bf16 v[108:111], v[36:39], v[16:19], 0\n"
        "v_mfma_f32_16x16x32_bf16 v[128:131], v[40:43], v[16:19], 0\nv_mfma_f32_16x16x32_bf16 v[148:151], v[44:47], v[16:19], 0\nds_read_b128 v[16:19], %[xb] offset:2048\ns_waitcnt lgkmcnt(4)\n"
        "v_mfma_f32_16x16x32_bf16 v[112:115], v[36:39], v[20:23], 0\nv_mfma_f32_16x16x32_bf16 v[132:135], v[40:43], v[20:23], 0\nv_mfma_f32_16x16x32_bf16 v[152:155], v[44:47], v[20:23], 0\n.LC0l%=:\n"
        "s_waitcnt vmcnt(8) lgkmcnt(3)\nv_mfma_f32_16x16x32_bf16 v[96:99], v[48:51], v[24:27], v[96:99]\ns_addk_i32 %[so], 15360\nds_read_b128 v[20:23], %[xb] offset:4096\n"
        "v_mfma_f32_16x16x32_bf16 v[116:119], v[52:55], v[24:27], v[116:119]\nv_mfma_f32_16x16x32_bf16 v[136:139], v[56:59], v[24:27], v[136:139]\n"
        "buffer_load_dwordx4 v[32:35], %[wa], %[rs], %[so] offen offset:0 sc1\nv_mfma_f32_16x16x32_bf16 v[156:159], v[60:63], v[24:27], v[156:159]\nds_read_b128 v[24:27], %[xa] offset:16384\n"
        "s_waitcnt lgkmcnt(4)\nv_mfma_f32_16x16x32_bf16 v[100:103], v[48:51], v[28:31], v[100:103]\nv_mfma_f32_16x16x32_bf16 v[120:123], v[52:55], v[28:31], v[120:123]\n"
        "buffer_load_dwordx4 v[36:39], %[wa], %[rs], %[so] offen offset:1024 sc1\nv_mfma_f32_16x16x32_bf16 v[140:143], v[56:59], v[28:31], v[140:143]\n"
        "v_mfma_f32_16x16x32_bf16 v[160:163], v[60:63], v[28:31], v[160:163]\nds_read_b128 v[28:31], %[xa] offset:18432\ns_waitcnt lgkmcnt(4)\n"
        "v_mfma_f32_16x16x32_bf16 v[104:107], v[52:55], v[12:15], v[104:107]\nv_mfma_f32_16x16x32_bf16 v[124:127], v[56:59], v[12:15], v[124:127]\n"
        "buffer_load_dwordx4 v[40:43], %[wa], %[rs], %[so] offen offset:2048 sc1\nv_mfma_f32_16x16x32_bf16 v[144:147], v[60:63], v[12:15], v[144:147]\nds_read_b128 v[12:15], %[xa] offset:10240\n"
        "s_waitcnt lgkmcnt(4)\nv_mfma_f32_16x16x32_bf16 v[108:111], v[52:55], v[16:19], v[108:111]\nv_mfma_f32_16x16x32_bf16 v[128:131], v[56:59], v[16:19], v[128:131]\n"
        "buffer_load_dwordx4 v[44:47], %[wa], %[rs], %[so] offen offset:3072 sc1\nv_mfma_f32_16x16x32_bf16 v[148:151], v[60:63], v[16:19], v[148:151]\nds_read_b128 v[16:19], %[xa] offset:12288\n"
        "s_waitcnt lgkmcnt(4)\nv_mfma_f32_16x16x32_bf16 v[112:115], v[52:55], v[20:23], v[112:115]\nv_add_u32 %[sy], 4, %[sy]\nv_mfma_f32_16x16x32_bf16 v[132:135], v[56:59], v[20:23], v[132:135]\n"
        "ds_write_b32 %[pg], %[sy]\nv_mfma_f32_16x16x32_bf16 v[152:155], v[60:63], v[20:23], v[152:155]\ns_waitcnt vmcnt(8) lgkmcnt(4)\nv_mfma_f32_16x16x32_bf16 v[96:99], v[64:67], v[24:27], v[96:99]\n"
        "s_addk_i32 %[so], 15360\nds_read_b128 v[20:23], %[xa] offset:14336\nv_mfma_f32_16x16x32_bf16 v[116:119], v[68:71], v[24:27], v[116:119]\n"
        "v_mfma_f32_16x16x32_bf16 v[136:139], v[72:75], v[24:27], v[136:139]\nbuffer_load_dwordx4 v[48:51], %[wa], %[rs], %[so] offen offset:0 sc1\n"
        "v_mfma_f32_16x16x32_bf16 v[156:159], v[76:79], v[24:27], v[156:159]\nds_read_b128 v[24:27], %[xb] offset:16384\ns_waitcnt lgkmcnt(5)\n"
        "v_mfma_f32_16x16x32_bf16 v[100:103], v[64:67], v[28:31], v[100:103]\nv_mfma_f32_16x16x32_bf16 v[120:123], v[68:71], v[28:31], v[120:123]\n"
        "buffer_load_dwordx4 v[52:55], %[wa], %[rs], %[so] offen offset:1024 sc1\nv_mfma_f32_16x16x32_bf16 v[140:143], v[72:75], v[28:31], v[140:143]\n"
        "v_mfma_f32_16x16x32_bf16 v[160:163], v[76:79], v[28:31], v[160:163]\nds_read_b128 v[28:31], %[xb] offset:18432\ns_waitcnt lgkmcnt(5)\n"
        "v_mfma_f32_16x16x32_bf16 v[104:107], v[68:71], v[12:15], v[104:107]\nv_mfma_f32_16x16x32_bf16 v[124:127], v[72:75], v[12:15], v[124:127]\n"
        "buffer_load_dwordx4 v[56:59], %[wa], %[rs], %[so] offen offset:2048 sc1\nv_mfma_f32_16x16x32_bf16 v[144:147], v[76:79], v[12:15], v[144:147]\nds_read_b128 v[12:15], %[xb] offset:10240\n"
        "s_waitcnt lgkmcnt(5)\nv_mfma_f32_16x16x32_bf16 v[108:111], v[68:71], v[16:19], v[108:111]\nv_mfma_f32_16x16x32_bf16 v[128:131], v[72:75], v[16:19], v[128:131]\n"
        "buffer_load_dwordx4 v[60:63], %[wa], %[rs], %[so] offen offset:3072 sc1\nv_mfma_f32_16x16x32_bf16 v[148:151], v[76:79], v[16:19], v[148:151]\nds_read_b128 v[16:19], %[xb] offset:12288\n"
        "s_waitcnt lgkmcnt(4)\nv_mfma_f32_16x16x32_bf16 v[112:115], v[68:71], v[20:23], v[112:115]\nv_mfma_f32_16x16x32_bf16 v[132:135], v[72:75], v[20:23], v[132:135]\n"
        "v_mfma_f32_16x16x32_bf16 v[152:155], v[76:79], v[20:23], v[152:155]\ns_waitcnt vmcnt(8) lgkmcnt(3)\nv_mfma_f32_16x16x32_bf16 v[96:99], v[80:83], v[24:27], v[96:99]\ns_addk_i32 %[so], 15360\n"
        "ds_read_b128 v[20:23], %[xb] offset:14336\nv_mfma_f32_16x16x32_bf16 v[116:119], v[84:87], v[24:27], v[116:119]\nv_mfma_f32_16x16x32_bf16 v[136:139], v[88:91], v[24:27], v[136:139]\n"
        "buffer_load_dwordx4 v[64:67], %[wa], %[rs], %[so] offen offset:0 sc1\nv_mfma_f32_16x16x32_bf16 v[156:159], v[92:95], v[24:27], v[156:159]\nds_read_b128 v[24:27], %[xa] offset:26624\n"
        "s_waitcnt lgkmcnt(4)\nv_mfma_f32_16x16x32_bf16 v[100:103], v[80:83], v[28:31], v[100:103]\nv_mfma_f32_16x16x32_bf16 v[120:123], v[84:87], v[28:31], v[120:123]\n"
        "buffer_load_dwordx4 v[68:71], %[wa], %[rs], %[so] offen offset:1024 sc1\nv_mfma_f32_16x16x32_bf16 v[140:143], v[88:91], v[28:31], v[140:143]\n"
        "v_mfma_f32_16x16x32_bf16 v[160:163], v[92:95], v[28:31], v[160:163]\nds_read_b128 v[28:31], %[xa] offset:28672\ns_waitcnt lgkmcnt(4)\n"
        "v_mfma_f32_16x16x32_bf16 v[104:107], v[84:87], v[12:15], v[104:107]\nv_mfma_f32_16x16x32_bf16 v[124:127], v[88:91], v[12:15], v[124:127]\n"
        "buffer_load_dwordx4 v[72:75], %[wa], %[rs], %[so] offen offset:2048 sc1\nv_mfma_f32_16x16x32_bf16 v[144:147], v[92:95], v[12:15], v[144:147]\nds_read_b128 v[12:15], %[xa] offset:20480\n"
        "s_waitcnt lgkmcnt(4)\nv_mfma_f32_16x16x32_bf16 v[108:111], v[84:87], v[16:19], v[108:111]\nv_mfma_f32_16x16x32_bf16 v[128:131], v[88:91], v[16:19], v[128:131]\n"
        "buffer_load_dwordx4 v[76:79], %[wa], %[rs], %[so] offen offset:3072 sc1\nv_mfma_f32_16x16x32_bf16 v[148:151], v[92:95], v[16:19], v[148:151]\nds_read_b128 v[16:19], %[xa] offset:22528\n"
        "s_waitcnt lgkmcnt(4)\nv_mfma_f32_16x16x32_bf16 v[112:115], v[84:87], v[20:23], v[112:115]\nv_add_u32 %[sy], 4, %[sy]\nv_mfma_f32_16x16x32_bf16 v[132:135], v[88:91], v[20:23], v[132:135]\n"
        "ds_write_b32 %[pg], %[sy]\nv_mfma_f32_16x16x32_bf16 v[152:155], v[92:95], v[20:23], v[152:155]\ns_waitcnt vmcnt(8) lgkmcnt(4)\nv_mfma_f32_16x16x32_bf16 v[96:99], v[32:35], v[24:27], v[96:99]\n"
        "s_addk_i32 %[so], 15360\nds_read_b128 v[20:23], %[xa] offset:24576\nv_mfma_f32_16x16x32_bf16 v[116:119], v[36:39], v[24:27], v[116:119]\n"
        "v_mfma_f32_16x16x32_bf16 v[136:139], v[40:43], v[24:27], v[136:139]\nbuffer_load_dwordx4 v[80:83], %[wa], %[rs], %[so] offen offset:0 sc1\n"
        "v_mfma_f32_16x16x32_bf16 v[156:159], v[44:47], v[24:27], v[156:159]\nds_read_b128 v[24:27], %[xb] offset:26624\ns_waitcnt lgkmcnt(5)\n"
        "v_mfma_f32_16x16x32_bf16 v[100:103], v[32:35], v[28:31], v[100:103]\nv_mfma_f32_16x16x32_bf16 v[120:123], v[36:39], v[28:31], v[120:123]\n"
        "buffer_load_dwordx4 v[84:87], %[wa], %[rs], %[so] offen offset:1024 sc1\nv_mfma_f32_16x16x32_bf16 v[140:143], v[40:43], v[28:31], v[140:143]\n"
        "v_mfma_f32_16x16x32_bf16 v[160:163], v[44:47], v[28:31], v[160:163]\nds_read_b128 v[28:31], %[xb] offset:28672\ns_waitcnt lgkmcnt(5)\n"
        "v_mfma_f32_16x16x32_bf16 v[104:107], v[36:39], v[12:15], v[104:107]\nv_mfma_f32_16x16x32_bf16 v[124:127], v[40:43], v[12:15], v[124:127]\n"
        "buffer_load_dwordx4 v[88:91], %[wa], %[rs], %[so] offen offset:2048 sc1\nv_mfma_f32_16x16x32_bf16 v[144:147], v[44:47], v[12:15], v[144:147]\nds_read_b128 v[12:15], %[xb] offset:20480\n"
        "s_waitcnt lgkmcnt(5)\nv_mfma_f32_16x16x32_bf16 v[108:111], v[36:39], v[16:19], v[108:111]\nv_mfma_f32_16x16x32_bf16 v[128:131], v[40:43], v[16:19], v[128:131]\n"
        "buffer_load_dwordx4 v[92:95], %[wa], %[rs], %[so] offen offset:3072 sc1\nv_mfma_f32_16x16x32_bf16 v[148:151], v[44:47], v[16:19], v[148:151]\nds_read_b128 v[16:19], %[xb] offset:22528\n"
        "s_waitcnt lgkmcnt(4)\nv_mfma_f32_16x16x32_bf16 v[112:115], v[36:39], v[20:23], v[112:115]\nv_mfma_f32_16x16x32_bf16 v[132:135], v[40:43], v[20:23], v[132:135]\ns_cmp_lg_u32 %[so], 291840\n"
        "v_mfma_f32_16x16x32_bf16 v[152:155], v[44:47], v[20:23], v[152:155]\nv_add_u32 %[xa], 20480, %[xa]\nv_add_u32 %[xb], 20480, %[xb]\ns_cbranch_scc1 .LC0l%=\ns_waitcnt vmcnt(8) lgkmcnt(3)\n"
        "v_mfma_f32_16x16x32_bf16 v[96:99], v[48:51], v[24:27], v[96:99]\nds_read_b128 v[20:23], %[xb] offset:4096\nv_mfma_f32_16x16x32_bf16 v[116:119], v[52:55], v[24:27], v[116:119]\n"
        "v_mfma_f32_16x16x32_bf16 v[136:139], v[56:59], v[24:27], v[136:139]\nv_mfma_f32_16x16x32_bf16 v[156:159], v[60:63], v[24:27], v[156:159]\nds_read_b128 v[24:27], %[xa] offset:16384\n"
        "s_waitcnt lgkmcnt(4)\nv_mfma_f32_16x16x32_bf16 v[100:103], v[48:51], v[28:31], v[100:103]\nv_mfma_f32_16x16x32_bf16 v[120:123], v[52:55], v[28:31], v[120:123]\n"
        "v_mfma_f32_16x16x32_bf16 v[140:143], v[56:59], v[28:31], v[140:143]\nv_mfma_f32_16x16x32_bf16 v[160:163], v[60:63], v[28:31], v[160:163]\nds_read_b128 v[28:31], %[xa] offset:18432\n"
        "s_waitcnt lgkmcnt(4)\nv_mfma_f32_16x16x32_bf16 v[104:107], v[52:55], v[12:15], v[104:107]\nv_mfma_f32_16x16x32_bf16 v[124:127], v[56:59], v[12:15], v[124:127]\n"
        "v_mfma_f32_16x16x32_bf16 v[144:147], v[60:63], v[12:15], v[144:147]\nds_read_b128 v[12:15], %[xa] offset:10240\ns_waitcnt lgkmcnt(4)\n"
        "v_mfma_f32_16x16x32_bf16 v[108:111], v[52:55], v[16:19], v[108:111]\nv_mfma_f32_16x16x32_bf16 v[128:131], v[56:59], v[16:19], v[128:131]\n"
        "v_mfma_f32_16x16x32_bf16 v[148:151], v[60:63], v[16:19], v[148:151]\nds_read_b128 v[16:19], %[xa] offset:12288\ns_waitcnt lgkmcnt(4)\n"
        "v_mfma_f32_16x16x32_bf16 v[112:115], v[52:55], v[20:23], v[112:115]\nv_add_u32 %[sy], 4, %[sy]\nv_mfma_f32_16x16x32_bf16 v[132:135], v[56:59], v[20:23], v[132:135]\n"
        "v_mfma_f32_16x16x32_bf16 v[152:155], v[60:63], v[20:23], v[152:155]\ns_waitcnt vmcnt(4) lgkmcnt(3)\nv_mfma_f32_16x16x32_bf16 v[96:99], v[64:67], v[24:27], v[96:99]\n"
        "ds_read_b128 v[20:23], %[xa] offset:14336\nv_mfma_f32_16x16x32_bf16 v[116:119], v[68:71], v[24:27], v[116:119]\nv_mfma_f32_16x16x32_bf16 v[136:139], v[72:75], v[24:27], v[136:139]\n"
        "ds_read_b32 %[pv], %[sy] offset:4\nv_mfma_f32_16x16x32_bf16 v[156:159], v[76:79], v[24:27], v[156:159]\nds_read_b128 v[24:27], %[xb] offset:16384\ns_waitcnt lgkmcnt(5)\n"
        "v_mfma_f32_16x16x32_bf16 v[100:103], v[64:67], v[28:31], v[100:103]\nv_mfma_f32_16x16x32_bf16 v[120:123], v[68:71], v[28:31], v[120:123]\n"
        "v_mfma_f32_16x16x32_bf16 v[140:143], v[72:75], v[28:31], v[140:143]\nv_mfma_f32_16x16x32_bf16 v[160:163], v[76:79], v[28:31], v[160:163]\nds_read_b128 v[28:31], %[xb] offset:18432\n"
        "s_waitcnt lgkmcnt(5)\nv_mfma_f32_16x16x32_bf16 v[104:107], v[68:71], v[12:15], v[104:107]\nv_mfma_f32_16x16x32_bf16 v[124:127], v[72:75], v[12:15], v[124:127]\n"
        "v_mfma_f32_16x16x32_bf16 v[144:147], v[76:79], v[12:15], v[144:147]\nds_read_b128 v[12:15], %[xb] offset:10240\ns_waitcnt lgkmcnt(5)\n"
        "v_mfma_f32_16x16x32_bf16 v[108:111], v[68:71], v[16:19], v[108:111]\nv_mfma_f32_16x16x32_bf16 v[128:131], v[72:75], v[16:19], v[128:131]\n"
        "v_mfma_f32_16x16x32_bf16 v[148:151], v[76:79], v[16:19], v[148:151]\nds_read_b128 v[16:19], %[xb] offset:12288\ns_waitcnt lgkmcnt(5)\n"
        "v_mfma_f32_16x16x32_bf16 v[112:115], v[68:71], v[20:23], v[112:115]\nv_mfma_f32_16x16x32_bf16 v[132:135], v[72:75], v[20:23], v[132:135]\n"
        "v_mfma_f32_16x16x32_bf16 v[152:155], v[76:79], v[20:23], v[152:155]\ns_waitcnt vmcnt(0) lgkmcnt(3)\nv_mfma_f32_16x16x32_bf16 v[96:99], v[80:83], v[24:27], v[96:99]\n"
        "v_readfirstlane_b32 %[s0], %[pv]\nds_read_b128 v[20:23], %[xb] offset:14336\nv_mfma_f32_16x16x32_bf16 v[116:119], v[84:87], v[24:27], v[116:119]\n"
        "v_mfma_f32_16x16x32_bf16 v[136:139], v[88:91], v[24:27], v[136:139]\ns_cmp_ge_i32 %[s0], %[nst]\nv_mfma_f32_16x16x32_bf16 v[156:159], v[92:95], v[24:27], v[156:159]\ns_cbranch_scc0 .LC0t%=\n"
        ".LC0u%=:\ns_waitcnt lgkmcnt(3)\nv_mfma_f32_16x16x32_bf16 v[100:103], v[80:83], v[28:31], v[100:103]\nv_add_u32 %[sy], 4, %[sy]\nds_read_b128 v[24:27], %[mra] offset:768\n"
        "v_mfma_f32_16x16x32_bf16 v[120:123], v[84:87], v[28:31], v[120:123]\nv_mfma_f32_16x16x32_bf16 v[140:143], v[88:91], v[28:31], v[140:143]\n"
        "v_mfma_f32_16x16x32_bf16 v[160:163], v[92:95], v[28:31], v[160:163]\nds_read_b128 v[32:35], %[cwa] offset:448\ns_waitcnt lgkmcnt(4)\n"
        "v_mfma_f32_16x16x32_bf16 v[104:107], v[84:87], v[12:15], v[104:107]\nds_read_b128 v[36:39], %[cwa] offset:256\nds_read_b128 v[28:31], %[mra] offset:1024\n"
        "v_mfma_f32_16x16x32_bf16 v[124:127], v[88:91], v[12:15], v[124:127]\nds_read_b128 v[40:43], %[cwa] offset:832\nv_mfma_f32_16x16x32_bf16 v[144:147], v[92:95], v[12:15], v[144:147]\n"
        "ds_read_b128 v[44:47], %[cwa] offset:896\ns_waitcnt lgkmcnt(7)\nv_mfma_f32_16x16x32_bf16 v[108:111], v[84:87], v[16:19], v[108:111]\nv_add_u32 %[sy], 4, %[sy]\n"
        "ds_read_b128 v[12:15], %[mra] offset:0\nv_mfma_f32_16x16x32_bf16 v[128:131], v[88:91], v[16:19], v[128:131]\nv_mfma_f32_16x16x32_bf16 v[148:151], v[92:95], v[16:19], v[148:151]\n"
        "ds_read_b128 v[16:19], %[mra] offset:256\ns_waitcnt lgkmcnt(8)\nv_mfma_f32_16x16x32_bf16 v[112:115], v[84:87], v[20:23], v[112:115]\n"
        "v_mfma_f32_16x16x32_bf16 v[132:135], v[88:91], v[20:23], v[132:135]\nv_mfma_f32_16x16x32_bf16 v[152:155], v[92:95], v[20:23], v[152:155]\nds_read_b128 v[20:23], %[mra] offset:512\n"
        "s_setprio %[pfs]\ns_waitcnt lgkmcnt(1)\n.long 0xd3b04060, 0x1d823120\n.long 0xd3b04062, 0x1d8a3122\n.long 0xd3b04074, 0x1dd23124\n.long 0xd3b04076, 0x1dda3126\n.long 0xd3b14060, 0x18023560\n"
        ".long 0xd3b14062, 0x18023562\n.long 0xd3b04064, 0x1d923920\n.long 0xd3b04066, 0x1d9a3922\n.long 0xd3b14074, 0x18023574\n.long 0xd3b14076, 0x18023576\nv_cvt_pk_bf16_f32 v96, v96, v97\n"
        "v_cvt_pk_bf16_f32 v97, v98, v99\n.long 0xd3b04078, 0x1de23924\n.long 0xd3b0407a, 0x1dea3926\n.long 0xd3b14064, 0x18023d64\n.long 0xd3b14066, 0x18023d66\nv_cvt_pk_bf16_f32 v116, v116, v117\n"
        "v_cvt_pk_bf16_f32 v117, v118, v119\nds_write_b64 %[sta], v[96:97] offset:24032\n.long 0xd3b04068, 0x1da21924\n.long 0xd3b0406a, 0x1daa1926\n.long 0xd3b14078, 0x18023d78\n"
        ".long 0xd3b1407a, 0x18023d7a\nv_cvt_pk_bf16_f32 v100, v100, v101\nv_cvt_pk_bf16_f32 v101, v102, v103\nds_write_b64 %[sta], v[116:117] offset:23936\n.long 0xd3b0406c, 0x1db22124\n"
        ".long 0xd3b0406e, 0x1dba2126\n.long 0xd3b14068, 0x18021d68\n.long 0xd3b1406a, 0x18021d6a\nv_cvt_pk_bf16_f32 v120, v120, v121\nv_cvt_pk_bf16_f32 v121, v122, v123\n"
        "ds_write_b64 %[sta], v[100:101] offset:31968\ns_waitcnt lgkmcnt(3)\n.long 0xd3b04070, 0x1dc22924\n.long 0xd3b04072, 0x1dca2926\n.long 0xd3b1406c, 0x1802256c\n.long 0xd3b1406e, 0x1802256e\n"
        "v_cvt_pk_bf16_f32 v104, v104, v105\nv_cvt_pk_bf16_f32 v105, v106, v107\nds_write_b64 %[sta], v[120:121] offset:31872\n.long 0xd3b14070, 0x18022d70\n.long 0xd3b14072, 0x18022d72\n"
        "v_cvt_pk_bf16_f32 v108, v108, v109\nv_cvt_pk_bf16_f32 v109, v110, v111\nds_write_b64 %[sta], v[104:105] offset:128\nv_cvt_pk_bf16_f32 v112, v112, v113\nv_cvt_pk_bf16_f32 v113, v114, v115\n"
        "ds_write_b64 %[sta], v[108:109] offset:8064\nds_write_b64 %[sta], v[112:113] offset:16000\nds_write_b32 %[pg], %[sy]\nv_add_u32 %[sy], 4, %[sy]\n.long 0xd3b04088, 0x1e223128\n"
        ".long 0xd3b0408a, 0x1e2a312a\n.long 0xd3b0409c, 0x1e72312c\n.long 0xd3b0409e, 0x1e7a312e\n.long 0xd3b14088, 0x18023588\n.long 0xd3b1408a, 0x1802358a\n.long 0xd3b0408c, 0x1e323928\n"
        ".long 0xd3b0408e, 0x1e3a392a\n.long 0xd3b1409c, 0x1802359c\n.long 0xd3b1409e, 0x1802359e\nv_cvt_pk_bf16_f32 v136, v136, v137\nv_cvt_pk_bf16_f32 v137, v138, v139\n.long 0xd3b040a0, 0x1e82392c\n"
        ".long 0xd3b040a2, 0x1e8a392e\n.long 0xd3b1408c, 0x18023d8c\n.long 0xd3b1408e, 0x18023d8e\nv_cvt_pk_bf16_f32 v156, v156, v157\nv_cvt_pk_bf16_f32 v157, v158, v159\n"
        "ds_write_b64 %[sta], v[136:137] offset:24224\n.long 0xd3b0407c, 0x1df21928\n.long 0xd3b0407e, 0x1dfa192a\n.long 0xd3b140a0, 0x18023da0\n.long 0xd3b140a2, 0x18023da2\n"
        "v_cvt_pk_bf16_f32 v140, v140, v141\nv_cvt_pk_bf16_f32 v141, v142, v143\nds_write_b64 %[sta], v[156:157] offset:24256\n.long 0xd3b04090, 0x1e42192c\n.long 0xd3b04092, 0x1e4a192e\n"
        ".long 0xd3b1407c, 0x18021d7c\n.long 0xd3b1407e, 0x18021d7e\nv_cvt_pk_bf16_f32 v160, v160, v161\nv_cvt_pk_bf16_f32 v161, v162, v163\nds_write_b64 %[sta], v[140:141] offset:32160\n"
        ".long 0xd3b04080, 0x1e022128\n.long 0xd3b04082, 0x1e0a212a\n.long 0xd3b14090, 0x18021d90\n.long 0xd3b14092, 0x18021d92\nv_cvt_pk_bf16_f32 v124, v124, v125\nv_cvt_pk_bf16_f32 v125, v126, v127\n"
        "ds_write_b64 %[sta], v[160:161] offset:32192\n.long 0xd3b04094, 0x1e52212c\n.long 0xd3b04096, 0x1e5a212e\n.long 0xd3b14080, 0x18022580\n.long 0xd3b14082, 0x18022582\n"
        "v_cvt_pk_bf16_f32 v144, v144, v145\nv_cvt_pk_bf16_f32 v145, v146, v147\nds_write_b64 %[sta], v[124:125] offset:416\n.long 0xd3b04084, 0x1e122928\n.long 0xd3b04086, 0x1e1a292a\n"
        ".long 0xd3b14094, 0x18022594\n.long 0xd3b14096, 0x18022596\nv_cvt_pk_bf16_f32 v128, v128, v129\nv_cvt_pk_bf16_f32 v129, v130, v131\nds_write_b64 %[sta], v[144:145] offset:448\n"
        ".long 0xd3b04098, 0x1e62292c\n.long 0xd3b0409a, 0x1e6a292e\n.long 0xd3b14084, 0x18022d84\n.long 0xd3b14086, 0x18022d86\nv_cvt_pk_bf16_f32 v148, v148, v149\nv_cvt_pk_bf16_f32 v149, v150, v151\n"
        "ds_write_b64 %[sta], v[128:129] offset:8352\n.long 0xd3b14098, 0x18022d98\n.long 0xd3b1409a, 0x18022d9a\nv_cvt_pk_bf16_f32 v132, v132, v133\nv_cvt_pk_bf16_f32 v133, v134, v135\n"
        "ds_write_b64 %[sta], v[148:149] offset:8384\nv_cvt_pk_bf16_f32 v152, v152, v153\nv_cvt_pk_bf16_f32 v153, v154, v155\nds_write_b64 %[sta], v[132:133] offset:16288\n"
        "ds_write_b64 %[sta], v[152:153] offset:16320\nds_write_b32 %[pg], %[sy]\ns_branch .LE%=\n.LC0t%=:\ns_mov_b32 %[s1], 0x100000\n.LC0q%=:\nds_read_b32 %[pv], %[sy] offset:4\n"
        "s_waitcnt lgkmcnt(0)\nv_readfirstlane_b32 %[s0], %[pv]\ns_nop 0\ns_cmp_ge_i32 %[s0], %[nst]\ns_cbranch_scc1 .LC0u%=\ns_sleep 1\ns_sub_u32 %[s1], %[s1], 1\ns_cbranch_scc0 .LC0q%=\n"
        "s_branch .LC0u%=\n.LE%=:\n"
        : [pv] "=&v"(pv), [xa] "+v"(xa), [xb] "+v"(xb), [sy] "+v"(sy), [so] "=&s"(so), [s0] "=&s"(s0_), [s1] "=&s"(s1_)
        : [wa] "v"(wa), [rs] "s"(wrsrc), [pg] "v"(pga), [cwa] "v"(cwa), [mra] "v"(mra), [sta] "v"(sta), [w] "s"(w), [ow] "i"(SY_W - SY_X), [pfs] "i"(PFS), [nst] "i"(NST0)
        : "memory", "scc", "v12", "v13", "v14", "v15", "v16", "v17", "v18", "v19", "v20", "v21", "v22", "v23", "v24", "v25", "v26", "v27", "v28", "v29", "v30", "v31", "v32", "v33", "v34", "v35", "v36", "v37", "v38", "v39", "v40", "v41", "v42", "v43", "v44", "v45", "v46", "v47", "v48", "v49", "v50", "v51", "v52", "v53", "v54", "v55", "v56", "v57", "v58", "v59", "v60", "v61", "v62", "v63", "v64", "v65", "v66", "v67", "v68", "v69", "v70", "v71", "v72", "v73", "v74", "v75", "v76", "v77", "v78", "v79", "v80", "v81", "v82", "v83", "v84", "v85", "v86", "v87", "v88", "v89", "v90", "v91", "v92", "v93", "v94", "v95", "v96", "v97", "v98", "v99", "v100", "v101", "v102", "v103", "v104", "v105", "v106", "v107", "v108", "v109", "v110", "v111", "v112", "v113", "v114", "v115", "v116", "v117", "v118", "v119", "v120", "v121", "v122", "v123", "v124", "v125", "v126", "v127", "v128", "v129", "v130", "v131", "v132", "v133", "v134", "v135", "v136", "v137", "v138", "v139", "v140", "v141", "v142", "v143", "v144", "v145", "v146", "v147", "v148", "v149", "v150", "v151", "v152", "v153", "v154", "v155", "v156", "v157", "v158", "v159", "v160", "v161", "v162", "v163", "v164", "v165", "v166", "v167");
    } else {
      asm volatile(
        "s_cmp_lt_u32 %[w], 2\ns_cbranch_scc0 .LB%=\ns_mov_b32 %[so], 0\nbuffer_load_dwordx4 v[32:35], %[wa], %[rs], %[so] offen offset:0 sc1\n"
        "buffer_load_dwordx4 v[36:39], %[wa], %[rs], %[so] offen offset:1024 sc1\nbuffer_load_dwordx4 v[40:43], %[wa], %[rs], %[so] offen offset:2048 sc1\n"
        "buffer_load_dwordx4 v[44:47], %[wa], %[rs], %[so] offen offset:3072 sc1\ns_mov_b32 %[so], 15360\nbuffer_load_dwordx4 v[48:51], %[wa], %[rs], %[so] offen offset:0 sc1\n"
        "buffer_load_dwordx4 v[52:55], %[wa], %[rs], %[so] offen offset:1024 sc1\nbuffer_load_dwordx4 v[56:59], %[wa], %[rs], %[so] offen offset:2048 sc1\n"
        "buffer_load_dwordx4 v[60:63], %[wa], %[rs], %[so] offen offset:3072 sc1\ns_mov_b32 %[so], 30720\nbuffer_load_dwordx4 v[64:67], %[wa], %[rs], %[so] offen offset:0 sc1\n"
        "buffer_load_dwordx4 v[68:71], %[wa], %[rs], %[so] offen offset:1024 sc1\nbuffer_load_dwordx4 v[72:75], %[wa], %[rs], %[so] offen offset:2048 sc1\n"
        "buffer_load_dwordx4 v[76:79], %[wa], %[rs], %[so] offen offset:3072 sc1\ns_mov_b32 %[so], 46080\nbuffer_load_dwordx4 v[80:83], %[wa], %[rs], %[so] offen offset:0 sc1\n"
        "buffer_load_dwordx4 v[84:87], %[wa], %[rs], %[so] offen offset:1024 sc1\nbuffer_load_dwordx4 v[88:91], %[wa], %[rs], %[so] offen offset:2048 sc1\n"
        "buffer_load_dwordx4 v[92:95], %[wa], %[rs], %[so] offen offset:3072 sc1\ns_barrier\ns_mov_b32 %[s1], 0x100000\n.LA1w%=:\nds_read_b32 %[pv], %[sy] offset:0\ns_waitcnt lgkmcnt(0)\n"
        "v_readfirstlane_b32 %[s0], %[pv]\ns_nop 0\ns_cmp_ge_i32 %[s0], 6\ns_cbranch_scc1 .LA1g%=\ns_sleep 1\ns_sub_u32 %[s1], %[s1], 1\ns_cbranch_scc0 .LA1w%=\n.LA1g%=:\n"
        "ds_read_b64_tr_b16 v[16:17], %[xa] offset:1056\nds_read_b64_tr_b16 v[18:19], %[xa] offset:1568\nds_read_b64_tr_b16 v[20:21], %[xa] offset:2112\nds_read_b64_tr_b16 v[22:23], %[xa] offset:2624\n"
        "ds_read_b64_tr_b16 v[12:13], %[xa] offset:0\nds_read_b64_tr_b16 v[14:15], %[xa] offset:512\nds_read_b64_tr_b16 v[24:25], %[xa] offset:3168\nds_read_b64_tr_b16 v[26:27], %[xa] offset:3680\n"
        "s_waitcnt vmcnt(12) lgkmcnt(6)\nv_mfma_f32_16x16x32_bf16 v[100:103], v[32:35], v[16:19], 0\nds_read_b64_tr_b16 v[28:29], %[xa] offset:4224\n"
        "v_mfma_f32_16x16x32_bf16 v[116:119], v[36:39], v[16:19], 0\nds_read_b64_tr_b16 v[30:31], %[xa] offset:4736\nv_mfma_f32_16x16x32_bf16 v[136:139], v[40:43], v[16:19], 0\n"
        "ds_read_b32 %[pv], %[sy] offset:4\nv_mfma_f32_16x16x32_bf16 v[152:155], v[44:47], v[16:19], 0\nds_read_b64_tr_b16 v[16:17], %[xa] offset:6336\ns_waitcnt lgkmcnt(8)\n"
        "v_mfma_f32_16x16x32_bf16 v[104:107], v[32:35], v[20:23], 0\nds_read_b64_tr_b16 v[18:19], %[xa] offset:6848\nv_mfma_f32_16x16x32_bf16 v[120:123], v[36:39], v[20:23], 0\n"
        "v_mfma_f32_16x16x32_bf16 v[140:143], v[40:43], v[20:23], 0\nv_mfma_f32_16x16x32_bf16 v[156:159], v[44:47], v[20:23], 0\nds_read_b64_tr_b16 v[20:21], %[xa] offset:7392\ns_waitcnt lgkmcnt(8)\n"
        "v_mfma_f32_16x16x32_bf16 v[96:99], v[32:35], v[12:15], 0\nds_read_b64_tr_b16 v[22:23], %[xa] offset:7904\nv_mfma_f32_16x16x32_bf16 v[132:135], v[40:43], v[12:15], 0\n"
        "ds_read_b64_tr_b16 v[12:13], %[xa] offset:5280\ns_waitcnt lgkmcnt(8)\nv_mfma_f32_16x16x32_bf16 v[108:111], v[32:35], v[24:27], 0\nds_read_b64_tr_b16 v[14:15], %[xa] offset:5792\n"
        "v_mfma_f32_16x16x32_bf16 v[124:127], v[36:39], v[24:27], 0\nv_mfma_f32_16x16x32_bf16 v[144:147], v[40:43], v[24:27], 0\nv_mfma_f32_16x16x32_bf16 v[160:163], v[44:47], v[24:27], 0\n"
        "ds_read_b64_tr_b16 v[24:25], %[xa] offset:8448\ns_waitcnt lgkmcnt(8)\nv_mfma_f32_16x16x32_bf16 v[112:115], v[32:35], v[28:31], 0\nds_read_b64_tr_b16 v[26:27], %[xa] offset:8960\n"
        "v_mfma_f32_16x16x32_bf16 v[128:131], v[36:39], v[28:31], 0\nv_mfma_f32_16x16x32_bf16 v[148:151], v[40:43], v[28:31], 0\nv_mfma_f32_16x16x32_bf16 v[164:167], v[44:47], v[28:31], 0\n.LA1l%=:\n"
        "s_waitcnt vmcnt(8) lgkmcnt(6)\nv_mfma_f32_16x16x32_bf16 v[100:103], v[48:51], v[16:19], v[100:103]\ns_addk_i32 %[so], 15360\nv_readfirstlane_b32 %[s0], %[pv]\n"
        "v_mfma_f32_16x16x32_bf16 v[116:119], v[52:55], v[16:19], v[116:119]\ns_cmp_ge_i32 %[s0], 6\nds_read_b64_tr_b16 v[28:29], %[xa] offset:9504\n"
        "v_mfma_f32_16x16x32_bf16 v[136:139], v[56:59], v[16:19], v[136:139]\ns_cbranch_scc0 .LA1s0_%=\n.LA1r0_%=:\nds_read_b64_tr_b16 v[30:31], %[xa] offset:10016\n"
        "v_mfma_f32_16x16x32_bf16 v[152:155], v[60:63], v[16:19], v[152:155]\nds_read_b64_tr_b16 v[16:17], %[xa] offset:11616\ns_waitcnt lgkmcnt(7)\n"
        "v_mfma_f32_16x16x32_bf16 v[104:107], v[48:51], v[20:23], v[104:107]\nds_read_b64_tr_b16 v[18:19], %[xa] offset:12128\nbuffer_load_dwordx4 v[32:35], %[wa], %[rs], %[so] offen offset:0 sc1\n"
        "v_mfma_f32_16x16x32_bf16 v[120:123], v[52:55], v[20:23], v[120:123]\nbuffer_load_dwordx4 v[36:39], %[wa], %[rs], %[so] offen offset:1024 sc1\n"
        "v_mfma_f32_16x16x32_bf16 v[140:143], v[56:59], v[20:23], v[140:143]\nv_mfma_f32_16x16x32_bf16 v[156:159], v[60:63], v[20:23], v[156:159]\nds_read_b64_tr_b16 v[20:21], %[xa] offset:12672\n"
        "s_waitcnt lgkmcnt(7)\nv_mfma_f32_16x16x32_bf16 v[96:99], v[48:51], v[12:15], v[96:99]\nds_read_b64_tr_b16 v[22:23], %[xa] offset:13184\n"
        "v_mfma_f32_16x16x32_bf16 v[132:135], v[56:59], v[12:15], v[132:135]\nds_read_b64_tr_b16 v[12:13], %[xa] offset:10560\ns_waitcnt lgkmcnt(7)\n"
        "v_mfma_f32_16x16x32_bf16 v[108:111], v[48:51], v[24:27], v[108:111]\nds_read_b64_tr_b16 v[14:15], %[xa] offset:11072\nv_mfma_f32_16x16x32_bf16 v[124:127], v[52:55], v[24:27], v[124:127]\n"
        "buffer_load_dwordx4 v[40:43], %[wa], %[rs], %[so] offen offset:2048 sc1\nv_mfma_f32_16x16x32_bf16 v[144:147], v[56:59], v[24:27], v[144:147]\n"
        "buffer_load_dwordx4 v[44:47], %[wa], %[rs], %[so] offen offset:3072 sc1\nv_mfma_f32_16x16x32_bf16 v[160:163], v[60:63], v[24:27], v[160:163]\nds_read_b64_tr_b16 v[24:25], %[xa] offset:13728\n"
        "s_waitcnt lgkmcnt(7)\nv_mfma_f32_16x16x32_bf16 v[112:115], v[48:51], v[28:31], v[112:115]\nds_read_b64_tr_b16 v[26:27], %[xa] offset:14240\n"
        "v_mfma_f32_16x16x32_bf16 v[128:131], v[52:55], v[28:31], v[128:131]\nv_add_u32 %[sy], 4, %[sy]\nv_mfma_f32_16x16x32_bf16 v[148:151], v[56:59], v[28:31], v[148:151]\nds_write_b32 %[pg], %[sy]\n"
        "v_mfma_f32_16x16x32_bf16 v[164:167], v[60:63], v[28:31], v[164:167]\ns_waitcnt vmcnt(8) lgkmcnt(7)\nv_mfma_f32_16x16x32_bf16 v[100:103], v[64:67], v[16:19], v[100:103]\n"
        "s_addk_i32 %[so], 15360\nds_read_b64_tr_b16 v[28:29], %[xa] offset:14784\nv_mfma_f32_16x16x32_bf16 v[116:119], v[68:71], v[16:19], v[116:119]\nds_read_b64_tr_b16 v[30:31], %[xa] offset:15296\n"
        "v_mfma_f32_16x16x32_bf16 v[136:139], v[72:75], v[16:19], v[136:139]\nds_read_b32 %[pv], %[sy] offset:4\nbuffer_load_dwordx4 v[48:51], %[wa], %[rs], %[so] offen offset:0 sc1\n"
        "v_mfma_f32_16x16x32_bf16 v[152:155], v[76:79], v[16:19], v[152:155]\nds_read_b64_tr_b16 v[16:17], %[xa] offset:16896\ns_waitcnt lgkmcnt(9)\n"
        "v_mfma_f32_16x16x32_bf16 v[104:107], v[64:67], v[20:23], v[104:107]\nds_read_b64_tr_b16 v[18:19], %[xa] offset:17408\nv_mfma_f32_16x16x32_bf16 v[120:123], v[68:71], v[20:23], v[120:123]\n"
        "buffer_load_dwordx4 v[52:55], %[wa], %[rs], %[so] offen offset:1024 sc1\nv_mfma_f32_16x16x32_bf16 v[140:143], v[72:75], v[20:23], v[140:143]\n"
        "v_mfma_f32_16x16x32_bf16 v[156:159], v[76:79], v[20:23], v[156:159]\nds_read_b64_tr_b16 v[20:21], %[xa] offset:17952\ns_waitcnt lgkmcnt(9)\n"
        "v_mfma_f32_16x16x32_bf16 v[96:99], v[64:67], v[12:15], v[96:99]\nds_read_b64_tr_b16 v[22:23], %[xa] offset:18464\nv_mfma_f32_16x16x32_bf16 v[132:135], v[72:75], v[12:15], v[132:135]\n"
        "ds_read_b64_tr_b16 v[12:13], %[xa] offset:15840\ns_waitcnt lgkmcnt(9)\nv_mfma_f32_16x16x32_bf16 v[108:111], v[64:67], v[24:27], v[108:111]\nds_read_b64_tr_b16 v[14:15], %[xa] offset:16352\n"
        "v_mfma_f32_16x16x32_bf16 v[124:127], v[68:71], v[24:27], v[124:127]\nbuffer_load_dwordx4 v[56:59], %[wa], %[rs], %[so] offen offset:2048 sc1\n"
        "v_mfma_f32_16x16x32_bf16 v[144:147], v[72:75], v[24:27], v[144:147]\nbuffer_load_dwordx4 v[60:63], %[wa], %[rs], %[so] offen offset:3072 sc1\n"
        "v_mfma_f32_16x16x32_bf16 v[160:163], v[76:79], v[24:27], v[160:163]\nds_read_b64_tr_b16 v[24:25], %[xa] offset:19008\ns_waitcnt lgkmcnt(8)\n"
        "v_mfma_f32_16x16x32_bf16 v[112:115], v[64:67], v[28:31], v[112:115]\nds_read_b64_tr_b16 v[26:27], %[xa] offset:19520\nv_mfma_f32_16x16x32_bf16 v[128:131], v[68:71], v[28:31], v[128:131]\n"
        "v_mfma_f32_16x16x32_bf16 v[148:151], v[72:75], v[28:31], v[148:151]\nv_mfma_f32_16x16x32_bf16 v[164:167], v[76:79], v[28:31], v[164:167]\ns_waitcnt vmcnt(8) lgkmcnt(6)\n"
        "v_mfma_f32_16x16x32_bf16 v[100:103], v[80:83], v[16:19], v[100:103]\ns_addk_i32 %[so], 15360\nv_readfirstlane_b32 %[s0], %[pv]\n"
        "v_mfma_f32_16x16x32_bf16 v[116:119], v[84:87], v[16:19], v[116:119]\ns_cmp_ge_i32 %[s0], 6\nds_read_b64_tr_b16 v[28:29], %[xa] offset:20064\n"
        "v_mfma_f32_16x16x32_bf16 v[136:139], v[88:91], v[16:19], v[136:139]\ns_cbranch_scc0 .LA1s1_%=\n.LA1r1_%=:\nds_read_b64_tr_b16 v[30:31], %[xa] offset:20576\n"
        "v_mfma_f32_16x16x32_bf16 v[152:155], v[92:95], v[16:19], v[152:155]\nds_read_b64_tr_b16 v[16:17], %[xa] offset:22176\ns_waitcnt lgkmcnt(7)\n"
        "v_mfma_f32_16x16x32_bf16 v[104:107], v[80:83], v[20:23], v[104:107]\nds_read_b64_tr_b16 v[18:19], %[xa] offset:22688\nbuffer_load_dwordx4 v[64:67], %[wa], %[rs], %[so] offen offset:0 sc1\n"
        "v_mfma_f32_16x16x32_bf16 v[120:123], v[84:87], v[20:23], v[120:123]\nbuffer_load_dwordx4 v[68:71], %[wa], %[rs], %[so] offen offset:1024 sc1\n"
        "v_mfma_f32_16x16x32_bf16 v[140:143], v[88:91], v[20:23], v[140:143]\nv_mfma_f32_16x16x32_bf16 v[156:159], v[92:95], v[20:23], v[156:159]\nds_read_b64_tr_b16 v[20:21], %[xa] offset:23232\n"
        "s_waitcnt lgkmcnt(7)\nv_mfma_f32_16x16x32_bf16 v[96:99], v[80:83], v[12:15], v[96:99]\nds_read_b64_tr_b16 v[22:23], %[xa] offset:23744\n"
        "v_mfma_f32_16x16x32_bf16 v[132:135], v[88:91], v[12:15], v[132:135]\nds_read_b64_tr_b16 v[12:13], %[xa] offset:21120\ns_waitcnt lgkmcnt(7)\n"
        "v_mfma_f32_16x16x32_bf16 v[108:111], v[80:83], v[24:27], v[108:111]\nds_read_b64_tr_b16 v[14:15], %[xa] offset:21632\nv_mfma_f32_16x16x32_bf16 v[124:127], v[84:87], v[24:27], v[124:127]\n"
        "buffer_load_dwordx4 v[72:75], %[wa], %[rs], %[so] offen offset:2048 sc1\nv_mfma_f32_16x16x32_bf16 v[144:147], v[88:91], v[24:27], v[144:147]\n"
        "buffer_load_dwordx4 v[76:79], %[wa], %[rs], %[so] offen offset:3072 sc1\nv_mfma_f32_16x16x32_bf16 v[160:163], v[92:95], v[24:27], v[160:163]\nds_read_b64_tr_b16 v[24:25], %[xa] offset:24288\n"
        "s_waitcnt lgkmcnt(7)\nv_mfma_f32_16x16x32_bf16 v[112:115], v[80:83], v[28:31], v[112:115]\nds_read_b64_tr_b16 v[26:27], %[xa] offset:24800\n"
        "v_mfma_f32_16x16x32_bf16 v[128:131], v[84:87], v[28:31], v[128:131]\nv_add_u32 %[sy], 4, %[sy]\nv_mfma_f32_16x16x32_bf16 v[148:151], v[88:91], v[28:31], v[148:151]\nds_write_b32 %[pg], %[sy]\n"
        "v_mfma_f32_16x16x32_bf16 v[164:167], v[92:95], v[28:31], v[164:167]\ns_waitcnt vmcnt(8) lgkmcnt(7)\nv_mfma_f32_16x16x32_bf16 v[100:103], v[32:35], v[16:19], v[100:103]\n"
        "s_addk_i32 %[so], 15360\nds_read_b64_tr_b16 v[28:29], %[xa] offset:25344\nv_mfma_f32_16x16x32_bf16 v[116:119], v[36:39], v[16:19], v[116:119]\nds_read_b64_tr_b16 v[30:31], %[xa] offset:25856\n"
        "v_mfma_f32_16x16x32_bf16 v[136:139], v[40:43], v[16:19], v[136:139]\nds_read_b32 %[pv], %[sy] offset:4\nbuffer_load_dwordx4 v[80:83], %[wa], %[rs], %[so] offen offset:0 sc1\n"
        "v_mfma_f32_16x16x32_bf16 v[152:155], v[44:47], v[16:19], v[152:155]\nds_read_b64_tr_b16 v[16:17], %[xa] offset:27456\ns_waitcnt lgkmcnt(9)\n"
        "v_mfma_f32_16x16x32_bf16 v[104:107], v[32:35], v[20:23], v[104:107]\nds_read_b64_tr_b16 v[18:19], %[xa] offset:27968\nv_mfma_f32_16x16x32_bf16 v[120:123], v[36:39], v[20:23], v[120:123]\n"
        "buffer_load_dwordx4 v[84:87], %[wa], %[rs], %[so] offen offset:1024 sc1\nv_mfma_f32_16x16x32_bf16 v[140:143], v[40:43], v[20:23], v[140:143]\n"
        "v_mfma_f32_16x16x32_bf16 v[156:159], v[44:47], v[20:23], v[156:159]\nds_read_b64_tr_b16 v[20:21], %[xa] offset:28512\ns_waitcnt lgkmcnt(9)\n"
        "v_mfma_f32_16x16x32_bf16 v[96:99], v[32:35], v[12:15], v[96:99]\nds_read_b64_tr_b16 v[22:23], %[xa] offset:29024\nv_mfma_f32_16x16x32_bf16 v[132:135], v[40:43], v[12:15], v[132:135]\n"
        "ds_read_b64_tr_b16 v[12:13], %[xa] offset:26400\ns_waitcnt lgkmcnt(9)\nv_mfma_f32_16x16x32_bf16 v[108:111], v[32:35], v[24:27], v[108:111]\nds_read_b64_tr_b16 v[14:15], %[xa] offset:26912\n"
        "v_mfma_f32_16x16x32_bf16 v[124:127], v[36:39], v[24:27], v[124:127]\nbuffer_load_dwordx4 v[88:91], %[wa], %[rs], %[so] offen offset:2048 sc1\n"
        "v_mfma_f32_16x16x32_bf16 v[144:147], v[40:43], v[24:27], v[144:147]\nbuffer_load_dwordx4 v[92:95], %[wa], %[rs], %[so] offen offset:3072 sc1\n"
        "v_mfma_f32_16x16x32_bf16 v[160:163], v[44:47], v[24:27], v[160:163]\nds_read_b64_tr_b16 v[24:25], %[xa] offset:29568\ns_waitcnt lgkmcnt(8)\n"
        "v_mfma_f32_16x16x32_bf16 v[112:115], v[32:35], v[28:31], v[112:115]\nds_read_b64_tr_b16 v[26:27], %[xa] offset:30080\nv_mfma_f32_16x16x32_bf16 v[128:131], v[36:39], v[28:31], v[128:131]\n"
        "v_mfma_f32_16x16x32_bf16 v[148:151], v[40:43], v[28:31], v[148:151]\ns_cmp_lg_u32 %[so], 291840\nv_mfma_f32_16x16x32_bf16 v[164:167], v[44:47], v[28:31], v[164:167]\n"
        "v_add_u32 %[xa], 21120, %[xa]\ns_cbranch_scc1 .LA1l%=\ns_waitcnt vmcnt(8) lgkmcnt(6)\nv_mfma_f32_16x16x32_bf16 v[100:103], v[48:51], v[16:19], v[100:103]\nv_readfirstlane_b32 %[s0], %[pv]\n"
        "ds_read_b64_tr_b16 v[28:29], %[xa] offset:9504\nv_mfma_f32_16x16x32_bf16 v[116:119], v[52:55], v[16:19], v[116:119]\ns_cmp_ge_i32 %[s0], 6\nds_read_b64_tr_b16 v[30:31], %[xa] offset:10016\n"
        "v_mfma_f32_16x16x32_bf16 v[136:139], v[56:59], v[16:19], v[136:139]\ns_cbranch_scc0 .LA1s2_%=\n.LA1r2_%=:\nv_mfma_f32_16x16x32_bf16 v[152:155], v[60:63], v[16:19], v[152:155]\n"
        "ds_read_b64_tr_b16 v[16:17], %[xa] offset:11616\ns_waitcnt lgkmcnt(7)\nv_mfma_f32_16x16x32_bf16 v[104:107], v[48:51], v[20:23], v[104:107]\nds_read_b64_tr_b16 v[18:19], %[xa] offset:12128\n"
        "v_mfma_f32_16x16x32_bf16 v[120:123], v[52:55], v[20:23], v[120:123]\nv_mfma_f32_16x16x32_bf16 v[140:143], v[56:59], v[20:23], v[140:143]\n"
        "v_mfma_f32_16x16x32_bf16 v[156:159], v[60:63], v[20:23], v[156:159]\nds_read_b64_tr_b16 v[20:21], %[xa] offset:12672\ns_waitcnt lgkmcnt(7)\n"
        "v_mfma_f32_16x16x32_bf16 v[96:99], v[48:51], v[12:15], v[96:99]\nds_read_b64_tr_b16 v[22:23], %[xa] offset:13184\nv_mfma_f32_16x16x32_bf16 v[132:135], v[56:59], v[12:15], v[132:135]\n"
        "ds_read_b64_tr_b16 v[12:13], %[xa] offset:10560\ns_waitcnt lgkmcnt(7)\nv_mfma_f32_16x16x32_bf16 v[108:111], v[48:51], v[24:27], v[108:111]\nds_read_b64_tr_b16 v[14:15], %[xa] offset:11072\n"
        "v_mfma_f32_16x16x32_bf16 v[124:127], v[52:55], v[24:27], v[124:127]\nv_mfma_f32_16x16x32_bf16 v[144:147], v[56:59], v[24:27], v[144:147]\n"
        "v_mfma_f32_16x16x32_bf16 v[160:163], v[60:63], v[24:27], v[160:163]\nds_read_b64_tr_b16 v[24:25], %[xa] offset:13728\ns_waitcnt lgkmcnt(7)\n"
        "v_mfma_f32_16x16x32_bf16 v[112:115], v[48:51], v[28:31], v[112:115]\nds_read_b64_tr_b16 v[26:27], %[xa] offset:14240\nv_mfma_f32_16x16x32_bf16 v[128:131], v[52:55], v[28:31], v[128:131]\n"
        "v_add_u32 %[sy], 4, %[sy]\nv_mfma_f32_16x16x32_bf16 v[148:151], v[56:59], v[28:31], v[148:151]\nv_mfma_f32_16x16x32_bf16 v[164:167], v[60:63], v[28:31], v[164:167]\n"
        "s_waitcnt vmcnt(4) lgkmcnt(6)\nv_mfma_f32_16x16x32_bf16 v[100:103], v[64:67], v[16:19], v[100:103]\nds_read_b64_tr_b16 v[28:29], %[xa] offset:14784\n"
        "v_mfma_f32_16x16x32_bf16 v[116:119], v[68:71], v[16:19], v[116:119]\nds_read_b64_tr_b16 v[30:31], %[xa] offset:15296\nv_mfma_f32_16x16x32_bf16 v[136:139], v[72:75], v[16:19], v[136:139]\n"
        "ds_read_b32 %[pv], %[sy] offset:4\nv_mfma_f32_16x16x32_bf16 v[152:155], v[76:79], v[16:19], v[152:155]\nds_read_b64_tr_b16 v[16:17], %[xa] offset:16896\ns_waitcnt lgkmcnt(8)\n"
        "v_mfma_f32_16x16x32_bf16 v[104:107], v[64:67], v[20:23], v[104:107]\nds_read_b64_tr_b16 v[18:19], %[xa] offset:17408\nv_mfma_f32_16x16x32_bf16 v[120:123], v[68:71], v[20:23], v[120:123]\n"
        "v_mfma_f32_16x16x32_bf16 v[140:143], v[72:75], v[20:23], v[140:143]\nv_mfma_f32_16x16x32_bf16 v[156:159], v[76:79], v[20:23], v[156:159]\nds_read_b64_tr_b16 v[20:21], %[xa] offset:17952\n"
        "s_waitcnt lgkmcnt(8)\nv_mfma_f32_16x16x32_bf16 v[96:99], v[64:67], v[12:15], v[96:99]\nds_read_b64_tr_b16 v[22:23], %[xa] offset:18464\n"
        "v_mfma_f32_16x16x32_bf16 v[132:135], v[72:75], v[12:15], v[132:135]\nds_read_b64_tr_b16 v[12:13], %[xa] offset:15840\ns_waitcnt lgkmcnt(8)\n"
        "v_mfma_f32_16x16x32_bf16 v[108:111], v[64:67], v[24:27], v[108:111]\nds_read_b64_tr_b16 v[14:15], %[xa] offset:16352\nv_mfma_f32_16x16x32_bf16 v[124:127], v[68:71], v[24:27], v[124:127]\n"
        "v_mfma_f32_16x16x32_bf16 v[144:147], v[72:75], v[24:27], v[144:147]\nv_mfma_f32_16x16x32_bf16 v[160:163], v[76:79], v[24:27], v[160:163]\nds_read_b64_tr_b16 v[24:25], %[xa] offset:19008\n"
        "s_waitcnt lgkmcnt(8)\nv_mfma_f32_16x16x32_bf16 v[112:115], v[64:67], v[28:31], v[112:115]\nds_read_b64_tr_b16 v[26:27], %[xa] offset:19520\n"
        "v_mfma_f32_16x16x32_bf16 v[128:131], v[68:71], v[28:31], v[128:131]\nv_mfma_f32_16x16x32_bf16 v[148:151], v[72:75], v[28:31], v[148:151]\n"
        "v_mfma_f32_16x16x32_bf16 v[164:167], v[76:79], v[28:31], v[164:167]\ns_waitcnt vmcnt(0) lgkmcnt(6)\nv_mfma_f32_16x16x32_bf16 v[100:103], v[80:83], v[16:19], v[100:103]\n"
        "v_readfirstlane_b32 %[s0], %[pv]\nds_read_b64_tr_b16 v[28:29], %[xa] offset:20064\nv_mfma_f32_16x16x32_bf16 v[116:119], v[84:87], v[16:19], v[116:119]\n"
        "ds_read_b64_tr_b16 v[30:31], %[xa] offset:20576\nv_mfma_f32_16x16x32_bf16 v[136:139], v[88:91], v[16:19], v[136:139]\ns_cmp_ge_i32 %[s0], %[nst]\n"
        "v_mfma_f32_16x16x32_bf16 v[152:155], v[92:95], v[16:19], v[152:155]\ns_cbranch_scc0 .LA1t%=\n.LA1u%=:\ns_waitcnt lgkmcnt(6)\n"
        "v_mfma_f32_16x16x32_bf16 v[104:107], v[80:83], v[20:23], v[104:107]\nv_add_u32 %[sy], 4, %[sy]\nds_read_b128 v[16:19], %[mra] offset:256\n"
        "v_mfma_f32_16x16x32_bf16 v[120:123], v[84:87], v[20:23], v[120:123]\nv_mfma_f32_16x16x32_bf16 v[140:143], v[88:91], v[20:23], v[140:143]\n"
        "v_mfma_f32_16x16x32_bf16 v[156:159], v[92:95], v[20:23], v[156:159]\nds_read_b128 v[32:35], %[cwa] offset:0\ns_waitcnt lgkmcnt(6)\n"
        "v_mfma_f32_16x16x32_bf16 v[96:99], v[80:83], v[12:15], v[96:99]\nds_read_b128 v[36:39], %[cwa] offset:320\nds_read_b128 v[20:23], %[mra] offset:512\n"
        "v_mfma_f32_16x16x32_bf16 v[132:135], v[88:91], v[12:15], v[132:135]\nds_read_b128 v[40:43], %[cwa] offset:640\ns_waitcnt lgkmcnt(7)\n"
        "v_mfma_f32_16x16x32_bf16 v[108:111], v[80:83], v[24:27], v[108:111]\nds_read_b128 v[44:47], %[cwa] offset:512\nds_read_b128 v[12:15], %[mra] offset:0\n"
        "v_mfma_f32_16x16x32_bf16 v[124:127], v[84:87], v[24:27], v[124:127]\nv_add_u32 %[sy], 4, %[sy]\nv_mfma_f32_16x16x32_bf16 v[144:147], v[88:91], v[24:27], v[144:147]\n"
        "v_mfma_f32_16x16x32_bf16 v[160:163], v[92:95], v[24:27], v[160:163]\nds_read_b128 v[24:27], %[mra] offset:768\ns_waitcnt lgkmcnt(8)\n"
        "v_mfma_f32_16x16x32_bf16 v[112:115], v[80:83], v[28:31], v[112:115]\nv_mfma_f32_16x16x32_bf16 v[128:131], v[84:87], v[28:31], v[128:131]\n"
        "v_mfma_f32_16x16x32_bf16 v[148:151], v[88:91], v[28:31], v[148:151]\nv_mfma_f32_16x16x32_bf16 v[164:167], v[92:95], v[28:31], v[164:167]\nds_read_b128 v[28:31], %[mra] offset:1024\n"
        "s_setprio %[pfs]\ns_waitcnt lgkmcnt(1)\n.long 0xd3b04064, 0x1d922120\n.long 0xd3b04066, 0x1d9a2122\n.long 0xd3b04074, 0x1dd22124\n.long 0xd3b04076, 0x1dda2126\n.long 0xd3b14064, 0x18022564\n"
        ".long 0xd3b14066, 0x18022566\n.long 0xd3b04068, 0x1da22920\n.long 0xd3b0406a, 0x1daa2922\n.long 0xd3b14074, 0x18022574\n.long 0xd3b14076, 0x18022576\nv_cvt_pk_bf16_f32 v100, v100, v101\n"
        "v_cvt_pk_bf16_f32 v101, v102, v103\n.long 0xd3b04078, 0x1de22924\n.long 0xd3b0407a, 0x1dea2926\n.long 0xd3b14068, 0x18022d68\n.long 0xd3b1406a, 0x18022d6a\nv_cvt_pk_bf16_f32 v116, v116, v117\n"
        "v_cvt_pk_bf16_f32 v117, v118, v119\nds_write_b64 %[sta], v[100:101] offset:7936\n.long 0xd3b04060, 0x1d821920\n.long 0xd3b04062, 0x1d8a1922\n.long 0xd3b14078, 0x18022d78\n"
        ".long 0xd3b1407a, 0x18022d7a\nv_cvt_pk_bf16_f32 v104, v104, v105\nv_cvt_pk_bf16_f32 v105, v106, v107\nds_write_b64 %[sta], v[116:117] offset:8096\n.long 0xd3b0406c, 0x1db23120\n"
        ".long 0xd3b0406e, 0x1dba3122\n.long 0xd3b14060, 0x18021d60\n.long 0xd3b14062, 0x18021d62\nv_cvt_pk_bf16_f32 v120, v120, v121\nv_cvt_pk_bf16_f32 v121, v122, v123\n"
        "ds_write_b64 %[sta], v[104:105] offset:15872\n.long 0xd3b0407c, 0x1df23124\n.long 0xd3b0407e, 0x1dfa3126\n.long 0xd3b1406c, 0x1802356c\n.long 0xd3b1406e, 0x1802356e\n"
        "v_cvt_pk_bf16_f32 v96, v96, v97\nv_cvt_pk_bf16_f32 v97, v98, v99\nds_write_b64 %[sta], v[120:121] offset:16032\ns_waitcnt lgkmcnt(4)\n.long 0xd3b04070, 0x1dc23920\n"
        ".long 0xd3b04072, 0x1dca3922\n.long 0xd3b1407c, 0x1802357c\n.long 0xd3b1407e, 0x1802357e\nv_cvt_pk_bf16_f32 v108, v108, v109\nv_cvt_pk_bf16_f32 v109, v110, v111\n"
        "ds_write_b64 %[sta], v[96:97] offset:0\n.long 0xd3b04080, 0x1e023924\n.long 0xd3b04082, 0x1e0a3926\n.long 0xd3b14070, 0x18023d70\n.long 0xd3b14072, 0x18023d72\n"
        "v_cvt_pk_bf16_f32 v124, v124, v125\nv_cvt_pk_bf16_f32 v125, v126, v127\nds_write_b64 %[sta], v[108:109] offset:23808\n.long 0xd3b14080, 0x18023d80\n.long 0xd3b14082, 0x18023d82\n"
        "v_cvt_pk_bf16_f32 v112, v112, v113\nv_cvt_pk_bf16_f32 v113, v114, v115\nds_write_b64 %[sta], v[124:125] offset:23968\nv_cvt_pk_bf16_f32 v128, v128, v129\nv_cvt_pk_bf16_f32 v129, v130, v131\n"
        "ds_write_b64 %[sta], v[112:113] offset:31744\nds_write_b64 %[sta], v[128:129] offset:31904\nds_write_b32 %[pg], %[sy]\nv_add_u32 %[sy], 4, %[sy]\n.long 0xd3b04088, 0x1e222128\n"
        ".long 0xd3b0408a, 0x1e2a212a\n.long 0xd3b04098, 0x1e62212c\n.long 0xd3b0409a, 0x1e6a212e\n.long 0xd3b14088, 0x18022588\n.long 0xd3b1408a, 0x1802258a\n.long 0xd3b0408c, 0x1e322928\n"
        ".long 0xd3b0408e, 0x1e3a292a\n.long 0xd3b14098, 0x18022598\n.long 0xd3b1409a, 0x1802259a\nv_cvt_pk_bf16_f32 v136, v136, v137\nv_cvt_pk_bf16_f32 v137, v138, v139\n.long 0xd3b0409c, 0x1e72292c\n"
        ".long 0xd3b0409e, 0x1e7a292e\n.long 0xd3b1408c, 0x18022d8c\n.long 0xd3b1408e, 0x18022d8e\nv_cvt_pk_bf16_f32 v152, v152, v153\nv_cvt_pk_bf16_f32 v153, v154, v155\n"
        "ds_write_b64 %[sta], v[136:137] offset:8256\n.long 0xd3b04084, 0x1e121928\n.long 0xd3b04086, 0x1e1a192a\n.long 0xd3b1409c, 0x18022d9c\n.long 0xd3b1409e, 0x18022d9e\n"
        "v_cvt_pk_bf16_f32 v140, v140, v141\nv_cvt_pk_bf16_f32 v141, v142, v143\nds_write_b64 %[sta], v[152:153] offset:8192\n.long 0xd3b04090, 0x1e423128\n.long 0xd3b04092, 0x1e4a312a\n"
        ".long 0xd3b14084, 0x18021d84\n.long 0xd3b14086, 0x18021d86\nv_cvt_pk_bf16_f32 v156, v156, v157\nv_cvt_pk_bf16_f32 v157, v158, v159\nds_write_b64 %[sta], v[140:141] offset:16192\n"
        ".long 0xd3b040a0, 0x1e82312c\n.long 0xd3b040a2, 0x1e8a312e\n.long 0xd3b14090, 0x18023590\n.long 0xd3b14092, 0x18023592\nv_cvt_pk_bf16_f32 v132, v132, v133\nv_cvt_pk_bf16_f32 v133, v134, v135\n"
        "ds_write_b64 %[sta], v[156:157] offset:16128\n.long 0xd3b04094, 0x1e523928\n.long 0xd3b04096, 0x1e5a392a\n.long 0xd3b140a0, 0x180235a0\n.long 0xd3b140a2, 0x180235a2\n"
        "v_cvt_pk_bf16_f32 v144, v144, v145\nv_cvt_pk_bf16_f32 v145, v146, v147\nds_write_b64 %[sta], v[132:133] offset:320\n.long 0xd3b040a4, 0x1e92392c\n.long 0xd3b040a6, 0x1e9a392e\n"
        ".long 0xd3b14094, 0x18023d94\n.long 0xd3b14096, 0x18023d96\nv_cvt_pk_bf16_f32 v160, v160, v161\nv_cvt_pk_bf16_f32 v161, v162, v163\nds_write_b64 %[sta], v[144:145] offset:24128\n"
        ".long 0xd3b140a4, 0x18023da4\n.long 0xd3b140a6, 0x18023da6\nv_cvt_pk_bf16_f32 v148, v148, v149\nv_cvt_pk_bf16_f32 v149, v150, v151\nds_write_b64 %[sta], v[160:161] offset:24064\n"
        "v_cvt_pk_bf16_f32 v164, v164, v165\nv_cvt_pk_bf16_f32 v165, v166, v167\nds_write_b64 %[sta], v[148:149] offset:32064\nds_write_b64 %[sta], v[164:165] offset:32000\nds_write_b32 %[pg], %[sy]\n"
        "s_branch .LE%=\n.LA1s0_%=:\ns_mov_b32 %[s1], 0x100000\n.LA1p0_%=:\nds_read_b32 %[pv], %[sy] offset:4\ns_waitcnt lgkmcnt(0)\nv_readfirstlane_b32 %[s0], %[pv]\ns_nop 0\ns_cmp_ge_i32 %[s0], 6\n"
        "s_cbranch_scc1 .LA1r0_%=\ns_sleep 1\ns_sub_u32 %[s1], %[s1], 1\ns_cbranch_scc0 .LA1p0_%=\ns_branch .LA1r0_%=\n.LA1s1_%=:\ns_mov_b32 %[s1], 0x100000\n.LA1p1_%=:\n"
        "ds_read_b32 %[pv], %[sy] offset:4\ns_waitcnt lgkmcnt(0)\nv_readfirstlane_b32 %[s0], %[pv]\ns_nop 0\ns_cmp_ge_i32 %[s0], 6\ns_cbranch_scc1 .LA1r1_%=\ns_sleep 1\ns_sub_u32 %[s1], %[s1], 1\n"
        "s_cbranch_scc0 .LA1p1_%=\ns_branch .LA1r1_%=\n.LA1s2_%=:\ns_mov_b32 %[s1], 0x100000\n.LA1p2_%=:\nds_read_b32 %[pv], %[sy] offset:4\ns_waitcnt lgkmcnt(0)\nv_readfirstlane_b32 %[s0], %[pv]\n"
        "s_nop 0\ns_cmp_ge_i32 %[s0], 6\ns_cbranch_scc1 .LA1r2_%=\ns_sleep 1\ns_sub_u32 %[s1], %[s1], 1\ns_cbranch_scc0 .LA1p2_%=\ns_branch .LA1r2_%=\n.LA1t%=:\ns_mov_b32 %[s1], 0x100000\n.LA1q%=:\n"
        "ds_read_b32 %[pv], %[sy] offset:4\ns_waitcnt lgkmcnt(0)\nv_readfirstlane_b32 %[s0], %[pv]\ns_nop 0\ns_cmp_ge_i32 %[s0], %[nst]\ns_cbranch_scc1 .LA1u%=\ns_sleep 1\ns_sub_u32 %[s1], %[s1], 1\n"
        "s_cbranch_scc0 .LA1q%=\ns_branch .LA1u%=\n.LB%=:\ns_cmp_eq_u32 %[w], 2\ns_cbranch_scc0 .LC%=\ns_mov_b32 %[so], 0\nbuffer_load_dwordx4 v[32:35], %[wa], %[rs], %[so] offen offset:3072 sc1\n"
        "buffer_load_dwordx4 v[36:39], %[wa], %[rs], %[so] offen offset:0 sc1\nbuffer_load_dwordx4 v[40:43], %[wa], %[rs], %[so] offen offset:1024 sc1\n"
        "buffer_load_dwordx4 v[44:47], %[wa], %[rs], %[so] offen offset:2048 sc1\ns_mov_b32 %[so], 15360\nbuffer_load_dwordx4 v[48:51], %[wa], %[rs], %[so] offen offset:3072 sc1\n"
        "buffer_load_dwordx4 v[52:55], %[wa], %[rs], %[so] offen offset:0 sc1\nbuffer_load_dwordx4 v[56:59], %[wa], %[rs], %[so] offen offset:1024 sc1\n"
        "buffer_load_dwordx4 v[60:63], %[wa], %[rs], %[so] offen offset:2048 sc1\ns_mov_b32 %[so], 30720\nbuffer_load_dwordx4 v[64:67], %[wa], %[rs], %[so] offen offset:3072 sc1\n"
        "buffer_load_dwordx4 v[68:71], %[wa], %[rs], %[so] offen offset:0 sc1\nbuffer_load_dwordx4 v[72:75], %[wa], %[rs], %[so] offen offset:1024 sc1\n"
        "buffer_load_dwordx4 v[76:79], %[wa], %[rs], %[so] offen offset:2048 sc1\ns_mov_b32 %[so], 46080\nbuffer_load_dwordx4 v[80:83], %[wa], %[rs], %[so] offen offset:3072 sc1\n"
        "buffer_load_dwordx4 v[84:87], %[wa], %[rs], %[so] offen offset:0 sc1\nbuffer_load_dwordx4 v[88:91], %[wa], %[rs], %[so] offen offset:1024 sc1\n"
        "buffer_load_dwordx4 v[92:95], %[wa], %[rs], %[so] offen offset:2048 sc1\ns_barrier\ns_mov_b32 %[s1], 0x100000\n.LB1w%=:\nds_read_b32 %[pv], %[sy] offset:0\ns_waitcnt lgkmcnt(0)\n"
        "v_readfirstlane_b32 %[s0], %[pv]\ns_nop 0\ns_cmp_ge_i32 %[s0], 6\ns_cbranch_scc1 .LB1g%=\ns_sleep 1\ns_sub_u32 %[s1], %[s1], 1\ns_cbranch_scc0 .LB1w%=\n.LB1g%=:\n"
        "ds_read_b64_tr_b16 v[16:17], %[xa] offset:1056\nds_read_b64_tr_b16 v[18:19], %[xa] offset:1568\nds_read_b64_tr_b16 v[20:21], %[xa] offset:2112\nds_read_b64_tr_b16 v[22:23], %[xa] offset:2624\n"
        "ds_read_b64_tr_b16 v[12:13], %[xa] offset:0\nds_read_b64_tr_b16 v[14:15], %[xa] offset:512\nds_read_b64_tr_b16 v[24:25], %[xa] offset:3168\nds_read_b64_tr_b16 v[26:27], %[xa] offset:3680\n"
        "s_waitcnt vmcnt(12) lgkmcnt(6)\nv_mfma_f32_16x16x32_bf16 v[96:99], v[32:35], v[16:19], 0\nds_read_b64_tr_b16 v[28:29], %[xa] offset:4224\n"
        "v_mfma_f32_16x16x32_bf16 v[108:111], v[36:39], v[16:19], 0\nds_read_b64_tr_b16 v[30:31], %[xa] offset:4736\nv_mfma_f32_16x16x32_bf16 v[128:131], v[40:43], v[16:19], 0\n"
        "ds_read_b32 %[pv], %[sy] offset:4\nv_mfma_f32_16x16x32_bf16 v[148:151], v[44:47], v[16:19], 0\nds_read_b64_tr_b16 v[16:17], %[xa] offset:6336\ns_waitcnt lgkmcnt(8)\n"
        "v_mfma_f32_16x16x32_bf16 v[100:103], v[32:35], v[20:23], 0\nds_read_b64_tr_b16 v[18:19], %[xa] offset:6848\nv_mfma_f32_16x16x32_bf16 v[112:115], v[36:39], v[20:23], 0\n"
        "v_mfma_f32_16x16x32_bf16 v[132:135], v[40:43], v[20:23], 0\nv_mfma_f32_16x16x32_bf16 v[152:155], v[44:47], v[20:23], 0\nds_read_b64_tr_b16 v[20:21], %[xa] offset:7392\ns_waitcnt lgkmcnt(8)\n"
        "v_mfma_f32_16x16x32_bf16 v[104:107], v[36:39], v[12:15], 0\nds_read_b64_tr_b16 v[22:23], %[xa] offset:7904\nv_mfma_f32_16x16x32_bf16 v[124:127], v[40:43], v[12:15], 0\n"
        "v_mfma_f32_16x16x32_bf16 v[144:147], v[44:47], v[12:15], 0\nds_read_b64_tr_b16 v[12:13], %[xa] offset:5280\ns_waitcnt lgkmcnt(8)\nv_mfma_f32_16x16x32_bf16 v[116:119], v[36:39], v[24:27], 0\n"
        "ds_read_b64_tr_b16 v[14:15], %[xa] offset:5792\nv_mfma_f32_16x16x32_bf16 v[136:139], v[40:43], v[24:27], 0\nv_mfma_f32_16x16x32_bf16 v[156:159], v[44:47], v[24:27], 0\n"
        "ds_read_b64_tr_b16 v[24:25], %[xa] offset:8448\ns_waitcnt lgkmcnt(8)\nv_mfma_f32_16x16x32_bf16 v[120:123], v[36:39], v[28:31], 0\nds_read_b64_tr_b16 v[26:27], %[xa] offset:8960\n"
        "v_mfma_f32_16x16x32_bf16 v[140:143], v[40:43], v[28:31], 0\nv_mfma_f32_16x16x32_bf16 v[160:163], v[44:47], v[28:31], 0\n.LB1l%=:\ns_waitcnt vmcnt(8) lgkmcnt(6)\n"
        "v_mfma_f32_16x16x32_bf16 v[96:99], v[48:51], v[16:19], v[96:99]\ns_addk_i32 %[so], 15360\nv_readfirstlane_b32 %[s0], %[pv]\n"
        "v_mfma_f32_16x16x32_bf16 v[108:111], v[52:55], v[16:19], v[108:111]\ns_cmp_ge_i32 %[s0], 6\nds_read_b64_tr_b16 v[28:29], %[xa] offset:9504\n"
        "v_mfma_f32_16x16x32_bf16 v[128:131], v[56:59], v[16:19], v[128:131]\ns_cbranch_scc0 .LB1s0_%=\n.LB1r0_%=:\nds_read_b64_tr_b16 v[30:31], %[xa] offset:10016\n"
        "v_mfma_f32_16x16x32_bf16 v[148:151], v[60:63], v[16:19], v[148:151]\nds_read_b64_tr_b16 v[16:17], %[xa] offset:11616\ns_waitcnt lgkmcnt(7)\n"
        "v_mfma_f32_16x16x32_bf16 v[100:103], v[48:51], v[20:23], v[100:103]\nds_read_b64_tr_b16 v[18:19], %[xa] offset:12128\nbuffer_load_dwordx4 v[32:35], %[wa], %[rs], %[so] offen offset:3072 sc1\n"
        "v_mfma_f32_16x16x32_bf16 v[112:115], v[52:55], v[20:23], v[112:115]\nbuffer_load_dwordx4 v[36:39], %[wa], %[rs], %[so] offen offset:0 sc1\n"
        "v_mfma_f32_16x16x32_bf16 v[132:135], v[56:59], v[20:23], v[132:135]\nv_mfma_f32_16x16x32_bf16 v[152:155], v[60:63], v[20:23], v[152:155]\nds_read_b64_tr_b16 v[20:21], %[xa] offset:12672\n"
        "s_waitcnt lgkmcnt(7)\nv_mfma_f32_16x16x32_bf16 v[104:107], v[52:55], v[12:15], v[104:107]\nds_read_b64_tr_b16 v[22:23], %[xa] offset:13184\n"
        "v_mfma_f32_16x16x32_bf16 v[124:127], v[56:59], v[12:15], v[124:127]\nbuffer_load_dwordx4 v[40:43], %[wa], %[rs], %[so] offen offset:1024 sc1\n"
        "v_mfma_f32_16x16x32_bf16 v[144:147], v[60:63], v[12:15], v[144:147]\nds_read_b64_tr_b16 v[12:13], %[xa] offset:10560\ns_waitcnt lgkmcnt(7)\n"
        "v_mfma_f32_16x16x32_bf16 v[116:119], v[52:55], v[24:27], v[116:119]\nds_read_b64_tr_b16 v[14:15], %[xa] offset:11072\nv_mfma_f32_16x16x32_bf16 v[136:139], v[56:59], v[24:27], v[136:139]\n"
        "buffer_load_dwordx4 v[44:47], %[wa], %[rs], %[so] offen offset:2048 sc1\nv_mfma_f32_16x16x32_bf16 v[156:159], v[60:63], v[24:27], v[156:159]\nds_read_b64_tr_b16 v[24:25], %[xa] offset:13728\n"
        "s_waitcnt lgkmcnt(7)\nv_mfma_f32_16x16x32_bf16 v[120:123], v[52:55], v[28:31], v[120:123]\nv_add_u32 %[sy], 4, %[sy]\nds_read_b64_tr_b16 v[26:27], %[xa] offset:14240\n"
        "v_mfma_f32_16x16x32_bf16 v[140:143], v[56:59], v[28:31], v[140:143]\nds_write_b32 %[pg], %[sy]\nv_mfma_f32_16x16x32_bf16 v[160:163], v[60:63], v[28:31], v[160:163]\n"
        "s_waitcnt vmcnt(8) lgkmcnt(7)\nv_mfma_f32_16x16x32_bf16 v[96:99], v[64:67], v[16:19], v[96:99]\ns_addk_i32 %[so], 15360\nds_read_b64_tr_b16 v[28:29], %[xa] offset:14784\n"
        "v_mfma_f32_16x16x32_bf16 v[108:111], v[68:71], v[16:19], v[108:111]\nds_read_b64_tr_b16 v[30:31], %[xa] offset:15296\nv_mfma_f32_16x16x32_bf16 v[128:131], v[72:75], v[16:19], v[128:131]\n"
        "ds_read_b32 %[pv], %[sy] offset:4\nbuffer_load_dwordx4 v[48:51], %[wa], %[rs], %[so] offen offset:3072 sc1\nv_mfma_f32_16x16x32_bf16 v[148:151], v[76:79], v[16:19], v[148:151]\n"
        "ds_read_b64_tr_b16 v[16:17], %[xa] offset:16896\ns_waitcnt lgkmcnt(9)\nv_mfma_f32_16x16x32_bf16 v[100:103], v[64:67], v[20:23], v[100:103]\nds_read_b64_tr_b16 v[18:19], %[xa] offset:17408\n"
        "v_mfma_f32_16x16x32_bf16 v[112:115], v[68:71], v[20:23], v[112:115]\nbuffer_load_dwordx4 v[52:55], %[wa], %[rs], %[so] offen offset:0 sc1\n"
        "v_mfma_f32_16x16x32_bf16 v[132:135], v[72:75], v[20:23], v[132:135]\nv_mfma_f32_16x16x32_bf16 v[152:155], v[76:79], v[20:23], v[152:155]\nds_read_b64_tr_b16 v[20:21], %[xa] offset:17952\n"
        "s_waitcnt lgkmcnt(9)\nv_mfma_f32_16x16x32_bf16 v[104:107], v[68:71], v[12:15], v[104:107]\nds_read_b64_tr_b16 v[22:23], %[xa] offset:18464\n"
        "v_mfma_f32_16x16x32_bf16 v[124:127], v[72:75], v[12:15], v[124:127]\nbuffer_load_dwordx4 v[56:59], %[wa], %[rs], %[so] offen offset:1024 sc1\n"
        "v_mfma_f32_16x16x32_bf16 v[144:147], v[76:79], v[12:15], v[144:147]\nds_read_b64_tr_b16 v[12:13], %[xa] offset:15840\ns_waitcnt lgkmcnt(9)\n"
        "v_mfma_f32_16x16x32_bf16 v[116:119], v[68:71], v[24:27], v[116:119]\nds_read_b64_tr_b16 v[14:15], %[xa] offset:16352\nv_mfma_f32_16x16x32_bf16 v[136:139], v[72:75], v[24:27], v[136:139]\n"
        "buffer_load_dwordx4 v[60:63], %[wa], %[rs], %[so] offen offset:2048 sc1\nv_mfma_f32_16x16x32_bf16 v[156:159], v[76:79], v[24:27], v[156:159]\nds_read_b64_tr_b16 v[24:25], %[xa] offset:19008\n"
        "s_waitcnt lgkmcnt(8)\nv_mfma_f32_16x16x32_bf16 v[120:123], v[68:71], v[28:31], v[120:123]\nds_read_b64_tr_b16 v[26:27], %[xa] offset:19520\n"
        "v_mfma_f32_16x16x32_bf16 v[140:143], v[72:75], v[28:31], v[140:143]\nv_mfma_f32_16x16x32_bf16 v[160:163], v[76:79], v[28:31], v[160:163]\ns_waitcnt vmcnt(8) lgkmcnt(6)\n"
        "v_mfma_f32_16x16x32_bf16 v[96:99], v[80:83], v[16:19], v[96:99]\ns_addk_i32 %[so], 15360\nv_readfirstlane_b32 %[s0], %[pv]\n"
        "v_mfma_f32_16x16x32_bf16 v[108:111], v[84:87], v[16:19], v[108:111]\ns_cmp_ge_i32 %[s0], 6\nds_read_b64_tr_b16 v[28:29], %[xa] offset:20064\n"
        "v_mfma_f32_16x16x32_bf16 v[128:131], v[88:91], v[16:19], v[128:131]\ns_cbranch_scc0 .LB1s1_%=\n.LB1r1_%=:\nds_read_b64_tr_b16 v[30:31], %[xa] offset:20576\n"
        "v_mfma_f32_16x16x32_bf16 v[148:151], v[92:95], v[16:19], v[148:151]\nds_read_b64_tr_b16 v[16:17], %[xa] offset:22176\ns_waitcnt lgkmcnt(7)\n"
        "v_mfma_f32_16x16x32_bf16 v[100:103], v[80:83], v[20:23], v[100:103]\nds_read_b64_tr_b16 v[18:19], %[xa] offset:22688\nbuffer_load_dwordx4 v[64:67], %[wa], %[rs], %[so] offen offset:3072 sc1\n"
        "v_mfma_f32_16x16x32_bf16 v[112:115], v[84:87], v[20:23], v[112:115]\nbuffer_load_dwordx4 v[68:71], %[wa], %[rs], %[so] offen offset:0 sc1\n"
        "v_mfma_f32_16x16x32_bf16 v[132:135], v[88:91], v[20:23], v[132:135]\nv_mfma_f32_16x16x32_bf16 v[152:155], v[92:95], v[20:23], v[152:155]\nds_read_b64_tr_b16 v[20:21], %[xa] offset:23232\n"
        "s_waitcnt lgkmcnt(7)\nv_mfma_f32_16x16x32_bf16 v[104:107], v[84:87], v[12:15], v[104:107]\nds_read_b64_tr_b16 v[22:23], %[xa] offset:23744\n"
        "v_mfma_f32_16x16x32_bf16 v[124:127], v[88:91], v[12:15], v[124:127]\nbuffer_load_dwordx4 v[72:75], %[wa], %[rs], %[so] offen offset:1024 sc1\n"
        "v_mfma_f32_16x16x32_bf16 v[144:147], v[92:95], v[12:15], v[144:147]\nds_read_b64_tr_b16 v[12:13], %[xa] offset:21120\ns_waitcnt lgkmcnt(7)\n"
        "v_mfma_f32_16x16x32_bf16 v[116:119], v[84:87], v[24:27], v[116:119]\nds_read_b64_tr_b16 v[14:15], %[xa] offset:21632\nv_mfma_f32_16x16x32_bf16 v[136:139], v[88:91], v[24:27], v[136:139]\n"
        "buffer_load_dwordx4 v[76:79], %[wa], %[rs], %[so] offen offset:2048 sc1\nv_mfma_f32_16x16x32_bf16 v[156:159], v[92:95], v[24:27], v[156:159]\nds_read_b64_tr_b16 v[24:25], %[xa] offset:24288\n"
        "s_waitcnt lgkmcnt(7)\nv_mfma_f32_16x16x32_bf16 v[120:123], v[84:87], v[28:31], v[120:123]\nv_add_u32 %[sy], 4, %[sy]\nds_read_b64_tr_b16 v[26:27], %[xa] offset:24800\n"
        "v_mfma_f32_16x16x32_bf16 v[140:143], v[88:91], v[28:31], v[140:143]\nds_write_b32 %[pg], %[sy]\nv_mfma_f32_16x16x32_bf16 v[160:163], v[92:95], v[28:31], v[160:163]\n"
        "s_waitcnt vmcnt(8) lgkmcnt(7)\nv_mfma_f32_16x16x32_bf16 v[96:99], v[32:35], v[16:19], v[96:99]\ns_addk_i32 %[so], 15360\nds_read_b64_tr_b16 v[28:29], %[xa] offset:25344\n"
        "v_mfma_f32_16x16x32_bf16 v[108:111], v[36:39], v[16:19], v[108:111]\nds_read_b64_tr_b16 v[30:31], %[xa] offset:25856\nv_mfma_f32_16x16x32_bf16 v[128:131], v[40:43], v[16:19], v[128:131]\n"
        "ds_read_b32 %[pv], %[sy] offset:4\nbuffer_load_dwordx4 v[80:83], %[wa], %[rs], %[so] offen offset:3072 sc1\nv_mfma_f32_16x16x32_bf16 v[148:151], v[44:47], v[16:19], v[148:151]\n"
        "ds_read_b64_tr_b16 v[16:17], %[xa] offset:27456\ns_waitcnt lgkmcnt(9)\nv_mfma_f32_16x16x32_bf16 v[100:103], v[32:35], v[20:23], v[100:103]\nds_read_b64_tr_b16 v[18:19], %[xa] offset:27968\n"
        "v_mfma_f32_16x16x32_bf16 v[112:115], v[36:39], v[20:23], v[112:115]\nbuffer_load_dwordx4 v[84:87], %[wa], %[rs], %[so] offen offset:0 sc1\n"
        "v_mfma_f32_16x16x32_bf16 v[132:135], v[40:43], v[20:23], v[132:135]\nv_mfma_f32_16x16x32_bf16 v[152:155], v[44:47], v[20:23], v[152:155]\nds_read_b64_tr_b16 v[20:21], %[xa] offset:28512\n"
        "s_waitcnt lgkmcnt(9)\nv_mfma_f32_16x16x32_bf16 v[104:107], v[36:39], v[12:15], v[104:107]\nds_read_b64_tr_b16 v[22:23], %[xa] offset:29024\n"
        "v_mfma_f32_16x16x32_bf16 v[124:127], v[40:43], v[12:15], v[124:127]\nbuffer_load_dwordx4 v[88:91], %[wa], %[rs], %[so] offen offset:1024 sc1\n"
        "v_mfma_f32_16x16x32_bf16 v[144:147], v[44:47], v[12:15], v[144:147]\nds_read_b64_tr_b16 v[12:13], %[xa] offset:26400\ns_waitcnt lgkmcnt(9)\n"
        "v_mfma_f32_16x16x32_bf16 v[116:119], v[36:39], v[24:27], v[116:119]\nds_read_b64_tr_b16 v[14:15], %[xa] offset:26912\nv_mfma_f32_16x16x32_bf16 v[136:139], v[40:43], v[24:27], v[136:139]\n"
        "buffer_load_dwordx4 v[92:95], %[wa], %[rs], %[so] offen offset:2048 sc1\nv_mfma_f32_16x16x32_bf16 v[156:159], v[44:47], v[24:27], v[156:159]\nds_read_b64_tr_b16 v[24:25], %[xa] offset:29568\n"
        "s_waitcnt lgkmcnt(8)\nv_mfma_f32_16x16x32_bf16 v[120:123], v[36:39], v[28:31], v[120:123]\nds_read_b64_tr_b16 v[26:27], %[xa] offset:30080\n"
        "v_mfma_f32_16x16x32_bf16 v[140:143], v[40:43], v[28:31], v[140:143]\ns_cmp_lg_u32 %[so], 291840\nv_mfma_f32_16x16x32_bf16 v[160:163], v[44:47], v[28:31], v[160:163]\n"
        "v_add_u32 %[xa], 21120, %[xa]\ns_cbranch_scc1 .LB1l%=\ns_waitcnt vmcnt(8) lgkmcnt(6)\nv_mfma_f32_16x16x32_bf16 v[96:99], v[48:51], v[16:19], v[96:99]\nv_readfirstlane_b32 %[s0], %[pv]\n"
        "ds_read_b64_tr_b16 v[28:29], %[xa] offset:9504\nv_mfma_f32_16x16x32_bf16 v[108:111], v[52:55], v[16:19], v[108:111]\ns_cmp_ge_i32 %[s0], 6\nds_read_b64_tr_b16 v[30:31], %[xa] offset:10016\n"
        "v_mfma_f32_16x16x32_bf16 v[128:131], v[56:59], v[16:19], v[128:131]\ns_cbranch_scc0 .LB1s2_%=\n.LB1r2_%=:\nv_mfma_f32_16x16x32_bf16 v[148:151], v[60:63], v[16:19], v[148:151]\n"
        "ds_read_b64_tr_b16 v[16:17], %[xa] offset:11616\ns_waitcnt lgkmcnt(7)\nv_mfma_f32_16x16x32_bf16 v[100:103], v[48:51], v[20:23], v[100:103]\nds_read_b64_tr_b16 v[18:19], %[xa] offset:12128\n"
        "v_mfma_f32_16x16x32_bf16 v[112:115], v[52:55], v[20:23], v[112:115]\nv_mfma_f32_16x16x32_bf16 v[132:135], v[56:59], v[20:23], v[132:135]\n"
        "v_mfma_f32_16x16x32_bf16 v[152:155], v[60:63], v[20:23], v[152:155]\nds_read_b64_tr_b16 v[20:21], %[xa] offset:12672\ns_waitcnt lgkmcnt(7)\n"
        "v_mfma_f32_16x16x32_bf16 v[104:107], v[52:55], v[12:15], v[104:107]\nds_read_b64_tr_b16 v[22:23], %[xa] offset:13184\nv_mfma_f32_16x16x32_bf16 v[124:127], v[56:59], v[12:15], v[124:127]\n"
        "v_mfma_f32_16x16x32_bf16 v[144:147], v[60:63], v[12:15], v[144:147]\nds_read_b64_tr_b16 v[12:13], %[xa] offset:10560\ns_waitcnt lgkmcnt(7)\n"
        "v_mfma_f32_16x16x32_bf16 v[116:119], v[52:55], v[24:27], v[116:119]\nds_read_b64_tr_b16 v[14:15], %[xa] offset:11072\nv_mfma_f32_16x16x32_bf16 v[136:139], v[56:59], v[24:27], v[136:139]\n"
        "v_mfma_f32_16x16x32_bf16 v[156:159], v[60:63], v[24:27], v[156:159]\nds_read_b64_tr_b16 v[24:25], %[xa] offset:13728\ns_waitcnt lgkmcnt(7)\n"
        "v_mfma_f32_16x16x32_bf16 v[120:123], v[52:55], v[28:31], v[120:123]\nv_add_u32 %[sy], 4, %[sy]\nds_read_b64_tr_b16 v[26:27], %[xa] offset:14240\n"
        "v_mfma_f32_16x16x32_bf16 v[140:143], v[56:59], v[28:31], v[140:143]\nv_mfma_f32_16x16x32_bf16 v[160:163], v[60:63], v[28:31], v[160:163]\ns_waitcnt vmcnt(4) lgkmcnt(6)\n"
        "v_mfma_f32_16x16x32_bf16 v[96:99], v[64:67], v[16:19], v[96:99]\nds_read_b64_tr_b16 v[28:29], %[xa] offset:14784\nv_mfma_f32_16x16x32_bf16 v[108:111], v[68:71], v[16:19], v[108:111]\n"
        "ds_read_b64_tr_b16 v[30:31], %[xa] offset:15296\nv_mfma_f32_16x16x32_bf16 v[128:131], v[72:75], v[16:19], v[128:131]\nds_read_b32 %[pv], %[sy] offset:4\n"
        "v_mfma_f32_16x16x32_bf16 v[148:151], v[76:79], v[16:19], v[148:151]\nds_read_b64_tr_b16 v[16:17], %[xa] offset:16896\ns_waitcnt lgkmcnt(8)\n"
        "v_mfma_f32_16x16x32_bf16 v[100:103], v[64:67], v[20:23], v[100:103]\nds_read_b64_tr_b16 v[18:19], %[xa] offset:17408\nv_mfma_f32_16x16x32_bf16 v[112:115], v[68:71], v[20:23], v[112:115]\n"
        "v_mfma_f32_16x16x32_bf16 v[132:135], v[72:75], v[20:23], v[132:135]\nv_mfma_f32_16x16x32_bf16 v[152:155], v[76:79], v[20:23], v[152:155]\nds_read_b64_tr_b16 v[20:21], %[xa] offset:17952\n"
        "s_waitcnt lgkmcnt(8)\nv_mfma_f32_16x16x32_bf16 v[104:107], v[68:71], v[12:15], v[104:107]\nds_read_b64_tr_b16 v[22:23], %[xa] offset:18464\n"
        "v_mfma_f32_16x16x32_bf16 v[124:127], v[72:75], v[12:15], v[124:127]\nv_mfma_f32_16x16x32_bf16 v[144:147], v[76:79], v[12:15], v[144:147]\nds_read_b64_tr_b16 v[12:13], %[xa] offset:15840\n"
        "s_waitcnt lgkmcnt(8)\nv_mfma_f32_16x16x32_bf16 v[116:119], v[68:71], v[24:27], v[116:119]\nds_read_b64_tr_b16 v[14:15], %[xa] offset:16352\n"
        "v_mfma_f32_16x16x32_bf16 v[136:139], v[72:75], v[24:27], v[136:139]\nv_mfma_f32_16x16x32_bf16 v[156:159], v[76:79], v[24:27], v[156:159]\nds_read_b64_tr_b16 v[24:25], %[xa] offset:19008\n"
        "s_waitcnt lgkmcnt(8)\nv_mfma_f32_16x16x32_bf16 v[120:123], v[68:71], v[28:31], v[120:123]\nds_read_b64_tr_b16 v[26:27], %[xa] offset:19520\n"
        "v_mfma_f32_16x16x32_bf16 v[140:143], v[72:75], v[28:31], v[140:143]\nv_mfma_f32_16x16x32_bf16 v[160:163], v[76:79], v[28:31], v[160:163]\ns_waitcnt vmcnt(0) lgkmcnt(6)\n"
        "v_mfma_f32_16x16x32_bf16 v[96:99], v[80:83], v[16:19], v[96:99]\nv_readfirstlane_b32 %[s0], %[pv]\nds_read_b64_tr_b16 v[28:29], %[xa] offset:20064\n"
        "v_mfma_f32_16x16x32_bf16 v[108:111], v[84:87], v[16:19], v[108:111]\nds_read_b64_tr_b16 v[30:31], %[xa] offset:20576\nv_mfma_f32_16x16x32_bf16 v[128:131], v[88:91], v[16:19], v[128:131]\n"
        "s_cmp_ge_i32 %[s0], %[nst]\nv_mfma_f32_16x16x32_bf16 v[148:151], v[92:95], v[16:19], v[148:151]\ns_cbranch_scc0 .LB1t%=\n.LB1u%=:\ns_waitcnt lgkmcnt(6)\n"
        "v_mfma_f32_16x16x32_bf16 v[100:103], v[80:83], v[20:23], v[100:103]\nv_add_u32 %[sy], 4, %[sy]\nds_read_b128 v[16:19], %[mra] offset:256\n"
        "v_mfma_f32_16x16x32_bf16 v[112:115], v[84:87], v[20:23], v[112:115]\nv_mfma_f32_16x16x32_bf16 v[132:135], v[88:91], v[20:23], v[132:135]\n"
        "v_mfma_f32_16x16x32_bf16 v[152:155], v[92:95], v[20:23], v[152:155]\nds_read_b128 v[32:35], %[cwa] offset:448\ns_waitcnt lgkmcnt(6)\n"
        "v_mfma_f32_16x16x32_bf16 v[104:107], v[84:87], v[12:15], v[104:107]\nds_read_b128 v[36:39], %[cwa] offset:128\nds_read_b128 v[20:23], %[mra] offset:512\n"
        "v_mfma_f32_16x16x32_bf16 v[124:127], v[88:91], v[12:15], v[124:127]\nds_read_b128 v[40:43], %[cwa] offset:192\nv_mfma_f32_16x16x32_bf16 v[144:147], v[92:95], v[12:15], v[144:147]\n"
        "ds_read_b128 v[44:47], %[cwa] offset:768\ns_waitcnt lgkmcnt(8)\nv_mfma_f32_16x16x32_bf16 v[116:119], v[84:87], v[24:27], v[116:119]\nv_add_u32 %[sy], 4, %[sy]\n"
        "ds_read_b128 v[12:15], %[mra] offset:0\nv_mfma_f32_16x16x32_bf16 v[136:139], v[88:91], v[24:27], v[136:139]\nv_mfma_f32_16x16x32_bf16 v[156:159], v[92:95], v[24:27], v[156:159]\n"
        "ds_read_b128 v[24:27], %[mra] offset:768\ns_waitcnt lgkmcnt(8)\nv_mfma_f32_16x16x32_bf16 v[120:123], v[84:87], v[28:31], v[120:123]\n"
        "v_mfma_f32_16x16x32_bf16 v[140:143], v[88:91], v[28:31], v[140:143]\nv_mfma_f32_16x16x32_bf16 v[160:163], v[92:95], v[28:31], v[160:163]\nds_read_b128 v[28:31], %[mra] offset:1024\n"
        "s_setprio %[pfs]\ns_waitcnt lgkmcnt(1)\n.long 0xd3b04060, 0x1d822120\n.long 0xd3b04062, 0x1d8a2122\n.long 0xd3b0406c, 0x1db22124\n.long 0xd3b0406e, 0x1dba2126\n.long 0xd3b14060, 0x18022560\n"
        ".long 0xd3b14062, 0x18022562\n.long 0xd3b04080, 0x1e022128\n.long 0xd3b04082, 0x1e0a212a\n.long 0xd3b1406c, 0x1802256c\n.long 0xd3b1406e, 0x1802256e\nv_cvt_pk_bf16_f32 v96, v96, v97\n"
        "v_cvt_pk_bf16_f32 v97, v98, v99\n.long 0xd3b04064, 0x1d922920\n.long 0xd3b04066, 0x1d9a2922\n.long 0xd3b14080, 0x18022580\n.long 0xd3b14082, 0x18022582\nv_cvt_pk_bf16_f32 v108, v108, v109\n"
        "v_cvt_pk_bf16_f32 v109, v110, v111\nds_write_b64 %[sta], v[96:97] offset:8160\n.long 0xd3b04070, 0x1dc22924\n.long 0xd3b04072, 0x1dca2926\n.long 0xd3b14064, 0x18022d64\n"
        ".long 0xd3b14066, 0x18022d66\nv_cvt_pk_bf16_f32 v128, v128, v129\nv_cvt_pk_bf16_f32 v129, v130, v131\nds_write_b64 %[sta], v[108:109] offset:8000\n.long 0xd3b04084, 0x1e122928\n"
        ".long 0xd3b04086, 0x1e1a292a\n.long 0xd3b14070, 0x18022d70\n.long 0xd3b14072, 0x18022d72\nv_cvt_pk_bf16_f32 v100, v100, v101\nv_cvt_pk_bf16_f32 v101, v102, v103\n"
        "ds_write_b64 %[sta], v[128:129] offset:8032\n.long 0xd3b04068, 0x1da21924\n.long 0xd3b0406a, 0x1daa1926\n.long 0xd3b14084, 0x18022d84\n.long 0xd3b14086, 0x18022d86\n"
        "v_cvt_pk_bf16_f32 v112, v112, v113\nv_cvt_pk_bf16_f32 v113, v114, v115\nds_write_b64 %[sta], v[100:101] offset:16096\n.long 0xd3b0407c, 0x1df21928\n.long 0xd3b0407e, 0x1dfa192a\n"
        ".long 0xd3b14068, 0x18021d68\n.long 0xd3b1406a, 0x18021d6a\nv_cvt_pk_bf16_f32 v132, v132, v133\nv_cvt_pk_bf16_f32 v133, v134, v135\nds_write_b64 %[sta], v[112:113] offset:15936\n"
        ".long 0xd3b04074, 0x1dd23124\n.long 0xd3b04076, 0x1dda3126\n.long 0xd3b1407c, 0x18021d7c\n.long 0xd3b1407e, 0x18021d7e\nv_cvt_pk_bf16_f32 v104, v104, v105\nv_cvt_pk_bf16_f32 v105, v106, v107\n"
        "ds_write_b64 %[sta], v[132:133] offset:15968\n.long 0xd3b04088, 0x1e223128\n.long 0xd3b0408a, 0x1e2a312a\n.long 0xd3b14074, 0x18023574\n.long 0xd3b14076, 0x18023576\n"
        "v_cvt_pk_bf16_f32 v124, v124, v125\nv_cvt_pk_bf16_f32 v125, v126, v127\nds_write_b64 %[sta], v[104:105] offset:64\ns_waitcnt lgkmcnt(7)\n.long 0xd3b04078, 0x1de23924\n"
        ".long 0xd3b0407a, 0x1dea3926\n.long 0xd3b14088, 0x18023588\n.long 0xd3b1408a, 0x1802358a\nv_cvt_pk_bf16_f32 v116, v116, v117\nv_cvt_pk_bf16_f32 v117, v118, v119\n"
        "ds_write_b64 %[sta], v[124:125] offset:96\n.long 0xd3b0408c, 0x1e323928\n.long 0xd3b0408e, 0x1e3a392a\n.long 0xd3b14078, 0x18023d78\n.long 0xd3b1407a, 0x18023d7a\n"
        "v_cvt_pk_bf16_f32 v136, v136, v137\nv_cvt_pk_bf16_f32 v137, v138, v139\nds_write_b64 %[sta], v[116:117] offset:23872\n.long 0xd3b1408c, 0x18023d8c\n.long 0xd3b1408e, 0x18023d8e\n"
        "v_cvt_pk_bf16_f32 v120, v120, v121\nv_cvt_pk_bf16_f32 v121, v122, v123\nds_write_b64 %[sta], v[136:137] offset:23904\nv_cvt_pk_bf16_f32 v140, v140, v141\nv_cvt_pk_bf16_f32 v141, v142, v143\n"
        "ds_write_b64 %[sta], v[120:121] offset:31808\nds_write_b64 %[sta], v[140:141] offset:31840\nds_write_b32 %[pg], %[sy]\nv_add_u32 %[sy], 4, %[sy]\n.long 0xd3b04094, 0x1e52212c\n"
        ".long 0xd3b04096, 0x1e5a212e\n.long 0xd3b04098, 0x1e62292c\n.long 0xd3b0409a, 0x1e6a292e\n.long 0xd3b14094, 0x18022594\n.long 0xd3b14096, 0x18022596\n.long 0xd3b04090, 0x1e42192c\n"
        ".long 0xd3b04092, 0x1e4a192e\n.long 0xd3b14098, 0x18022d98\n.long 0xd3b1409a, 0x18022d9a\nv_cvt_pk_bf16_f32 v148, v148, v149\nv_cvt_pk_bf16_f32 v149, v150, v151\n.long 0xd3b0409c, 0x1e72312c\n"
        ".long 0xd3b0409e, 0x1e7a312e\n.long 0xd3b14090, 0x18021d90\n.long 0xd3b14092, 0x18021d92\nv_cvt_pk_bf16_f32 v152, v152, v153\nv_cvt_pk_bf16_f32 v153, v154, v155\n"
        "ds_write_b64 %[sta], v[148:149] offset:8320\n.long 0xd3b040a0, 0x1e82392c\n.long 0xd3b040a2, 0x1e8a392e\n.long 0xd3b1409c, 0x1802359c\n.long 0xd3b1409e, 0x1802359e\n"
        "v_cvt_pk_bf16_f32 v144, v144, v145\nv_cvt_pk_bf16_f32 v145, v146, v147\nds_write_b64 %[sta], v[152:153] offset:16256\n.long 0xd3b140a0, 0x18023da0\n.long 0xd3b140a2, 0x18023da2\n"
        "v_cvt_pk_bf16_f32 v156, v156, v157\nv_cvt_pk_bf16_f32 v157, v158, v159\nds_write_b64 %[sta], v[144:145] offset:384\nv_cvt_pk_bf16_f32 v160, v160, v161\nv_cvt_pk_bf16_f32 v161, v162, v163\n"
        "ds_write_b64 %[sta], v[156:157] offset:24192\nds_write_b64 %[sta], v[160:161] offset:32128\nds_write_b32 %[pg], %[sy]\ns_branch .LE%=\n.LB1s0_%=:\ns_mov_b32 %[s1], 0x100000\n.LB1p0_%=:\n"
        "ds_read_b32 %[pv], %[sy] offset:4\ns_waitcnt lgkmcnt(0)\nv_readfirstlane_b32 %[s0], %[pv]\ns_nop 0\ns_cmp_ge_i32 %[s0], 6\ns_cbranch_scc1 .LB1r0_%=\ns_sleep 1\ns_sub_u32 %[s1], %[s1], 1\n"
        "s_cbranch_scc0 .LB1p0_%=\ns_branch .LB1r0_%=\n.LB1s1_%=:\ns_mov_b32 %[s1], 0x100000\n.LB1p1_%=:\nds_read_b32 %[pv], %[sy] offset:4\ns_waitcnt lgkmcnt(0)\nv_readfirstlane_b32 %[s0], %[pv]\n"
        "s_nop 0\ns_cmp_ge_i32 %[s0], 6\ns_cbranch_scc1 .LB1r1_%=\ns_sleep 1\ns_sub_u32 %[s1], %[s1], 1\ns_cbranch_scc0 .LB1p1_%=\ns_branch .LB1r1_%=\n.LB1s2_%=:\ns_mov_b32 %[s1], 0x100000\n"
        ".LB1p2_%=:\nds_read_b32 %[pv], %[sy] offset:4\ns_waitcnt lgkmcnt(0)\nv_readfirstlane_b32 %[s0], %[pv]\ns_nop 0\ns_cmp_ge_i32 %[s0], 6\ns_cbranch_scc1 .LB1r2_%=\ns_sleep 1\n"
        "s_sub_u32 %[s1], %[s1], 1\ns_cbranch_scc0 .LB1p2_%=\ns_branch .LB1r2_%=\n.LB1t%=:\ns_mov_b32 %[s1], 0x100000\n.LB1q%=:\nds_read_b32 %[pv], %[sy] offset:4\ns_waitcnt lgkmcnt(0)\n"
        "v_readfirstlane_b32 %[s0], %[pv]\ns_nop 0\ns_cmp_ge_i32 %[s0], %[nst]\ns_cbranch_scc1 .LB1u%=\ns_sleep 1\ns_sub_u32 %[s1], %[s1], 1\ns_cbranch_scc0 .LB1q%=\ns_branch .LB1u%=\n.LC%=:\n"
        "s_mov_b32 %[so], 0\nbuffer_load_dwordx4 v[32:35], %[wa], %[rs], %[so] offen offset:0 sc1\nbuffer_load_dwordx4 v[36:39], %[wa], %[rs], %[so] offen offset:1024 sc1\n"
        "buffer_load_dwordx4 v[40:43], %[wa], %[rs], %[so] offen offset:2048 sc1\nbuffer_load_dwordx4 v[44:47], %[wa], %[rs], %[so] offen offset:3072 sc1\ns_mov_b32 %[so], 15360\n"
        "buffer_load_dwordx4 v[48:51], %[wa], %[rs], %[so] offen offset:0 sc1\nbuffer_load_dwordx4 v[52:55], %[wa], %[rs], %[so] offen offset:1024 sc1\n"
        "buffer_load_dwordx4 v[56:59], %[wa], %[rs], %[so] offen offset:2048 sc1\nbuffer_load_dwordx4 v[60:63], %[wa], %[rs], %[so] offen offset:3072 sc1\ns_mov_b32 %[so], 30720\n"
        "buffer_load_dwordx4 v[64:67], %[wa], %[rs], %[so] offen offset:0 sc1\nbuffer_load_dwordx4 v[68:71], %[wa], %[rs], %[so] offen offset:1024 sc1\n"
        "buffer_load_dwordx4 v[72:75], %[wa], %[rs], %[so] offen offset:2048 sc1\nbuffer_load_dwordx4 v[76:79], %[wa], %[rs], %[so] offen offset:3072 sc1\ns_mov_b32 %[so], 46080\n"
        "buffer_load_dwordx4 v[80:83], %[wa], %[rs], %[so] offen offset:0 sc1\nbuffer_load_dwordx4 v[84:87], %[wa], %[rs], %[so] offen offset:1024 sc1\n"
        "buffer_load_dwordx4 v[88:91], %[wa], %[rs], %[so] offen offset:2048 sc1\nbuffer_load_dwordx4 v[92:95], %[wa], %[rs], %[so] offen offset:3072 sc1\ns_barrier\ns_mov_b32 %[s1], 0x100000\n"
        ".LC1w%=:\nds_read_b32 %[pv], %[sy] offset:0\ns_waitcnt lgkmcnt(0)\nv_readfirstlane_b32 %[s0], %[pv]\ns_nop 0\ns_cmp_ge_i32 %[s0], 6\ns_cbranch_scc1 .LC1g%=\ns_sleep 1\n"
        "s_sub_u32 %[s1], %[s1], 1\ns_cbranch_scc0 .LC1w%=\n.LC1g%=:\nds_read_b64_tr_b16 v[24:25], %[xa] offset:3168\nds_read_b64_tr_b16 v[26:27], %[xa] offset:3680\n"
        "ds_read_b64_tr_b16 v[28:29], %[xa] offset:4224\nds_read_b64_tr_b16 v[30:31], %[xa] offset:4736\nds_read_b64_tr_b16 v[12:13], %[xa] offset:0\nds_read_b64_tr_b16 v[14:15], %[xa] offset:512\n"
        "ds_read_b64_tr_b16 v[16:17], %[xa] offset:1056\nds_read_b64_tr_b16 v[18:19], %[xa] offset:1568\ns_waitcnt vmcnt(12) lgkmcnt(6)\nv_mfma_f32_16x16x32_bf16 v[96:99], v[32:35], v[24:27], 0\n"
        "ds_read_b64_tr_b16 v[20:21], %[xa] offset:2112\nv_mfma_f32_16x16x32_bf16 v[116:119], v[36:39], v[24:27], 0\nds_read_b64_tr_b16 v[22:23], %[xa] offset:2624\n"
        "v_mfma_f32_16x16x32_bf16 v[136:139], v[40:43], v[24:27], 0\nds_read_b32 %[pv], %[sy] offset:4\nv_mfma_f32_16x16x32_bf16 v[156:159], v[44:47], v[24:27], 0\n"
        "ds_read_b64_tr_b16 v[24:25], %[xa] offset:8448\ns_waitcnt lgkmcnt(8)\nv_mfma_f32_16x16x32_bf16 v[100:103], v[32:35], v[28:31], 0\nds_read_b64_tr_b16 v[26:27], %[xa] offset:8960\n"
        "v_mfma_f32_16x16x32_bf16 v[120:123], v[36:39], v[28:31], 0\nv_mfma_f32_16x16x32_bf16 v[140:143], v[40:43], v[28:31], 0\nv_mfma_f32_16x16x32_bf16 v[160:163], v[44:47], v[28:31], 0\n"
        "ds_read_b64_tr_b16 v[28:29], %[xa] offset:9504\ns_waitcnt lgkmcnt(8)\nv_mfma_f32_16x16x32_bf16 v[104:107], v[36:39], v[12:15], 0\nds_read_b64_tr_b16 v[30:31], %[xa] offset:10016\n"
        "v_mfma_f32_16x16x32_bf16 v[124:127], v[40:43], v[12:15], 0\nv_mfma_f32_16x16x32_bf16 v[144:147], v[44:47], v[12:15], 0\nds_read_b64_tr_b16 v[12:13], %[xa] offset:5280\ns_waitcnt lgkmcnt(8)\n"
        "v_mfma_f32_16x16x32_bf16 v[108:111], v[36:39], v[16:19], 0\nds_read_b64_tr_b16 v[14:15], %[xa] offset:5792\nv_mfma_f32_16x16x32_bf16 v[128:131], v[40:43], v[16:19], 0\n"
        "v_mfma_f32_16x16x32_bf16 v[148:151], v[44:47], v[16:19], 0\nds_read_b64_tr_b16 v[16:17], %[xa] offset:6336\ns_waitcnt lgkmcnt(8)\nv_mfma_f32_16x16x32_bf16 v[112:115], v[36:39], v[20:23], 0\n"
        "ds_read_b64_tr_b16 v[18:19], %[xa] offset:6848\nv_mfma_f32_16x16x32_bf16 v[132:135], v[40:43], v[20:23], 0\nv_mfma_f32_16x16x32_bf16 v[152:155], v[44:47], v[20:23], 0\n.LC1l%=:\n"
        "s_waitcnt vmcnt(8) lgkmcnt(6)\nv_mfma_f32_16x16x32_bf16 v[96:99], v[48:51], v[24:27], v[96:99]\ns_addk_i32 %[so], 15360\nv_readfirstlane_b32 %[s0], %[pv]\n"
        "v_mfma_f32_16x16x32_bf16 v[116:119], v[52:55], v[24:27], v[116:119]\ns_cmp_ge_i32 %[s0], 6\nds_read_b64_tr_b16 v[20:21], %[xa] offset:7392\n"
        "v_mfma_f32_16x16x32_bf16 v[136:139], v[56:59], v[24:27], v[136:139]\ns_cbranch_scc0 .LC1s0_%=\n.LC1r0_%=:\nds_read_b64_tr_b16 v[22:23], %[xa] offset:7904\n"
        "v_mfma_f32_16x16x32_bf16 v[156:159], v[60:63], v[24:27], v[156:159]\nds_read_b64_tr_b16 v[24:25], %[xa] offset:13728\ns_waitcnt lgkmcnt(7)\n"
        "v_mfma_f32_16x16x32_bf16 v[100:103], v[48:51], v[28:31], v[100:103]\nds_read_b64_tr_b16 v[26:27], %[xa] offset:14240\nbuffer_load_dwordx4 v[32:35], %[wa], %[rs], %[so] offen offset:0 sc1\n"
        "v_mfma_f32_16x16x32_bf16 v[120:123], v[52:55], v[28:31], v[120:123]\nbuffer_load_dwordx4 v[36:39], %[wa], %[rs], %[so] offen offset:1024 sc1\n"
        "v_mfma_f32_16x16x32_bf16 v[140:143], v[56:59], v[28:31], v[140:143]\nv_mfma_f32_16x16x32_bf16 v[160:163], v[60:63], v[28:31], v[160:163]\nds_read_b64_tr_b16 v[28:29], %[xa] offset:14784\n"
        "s_waitcnt lgkmcnt(7)\nv_mfma_f32_16x16x32_bf16 v[104:107], v[52:55], v[12:15], v[104:107]\nds_read_b64_tr_b16 v[30:31], %[xa] offset:15296\n"
        "v_mfma_f32_16x16x32_bf16 v[124:127], v[56:59], v[12:15], v[124:127]\nbuffer_load_dwordx4 v[40:43], %[wa], %[rs], %[so] offen offset:2048 sc1\n"
        "v_mfma_f32_16x16x32_bf16 v[144:147], v[60:63], v[12:15], v[144:147]\nds_read_b64_tr_b16 v[12:13], %[xa] offset:10560\ns_waitcnt lgkmcnt(7)\n"
        "v_mfma_f32_16x16x32_bf16 v[108:111], v[52:55], v[16:19], v[108:111]\nds_read_b64_tr_b16 v[14:15], %[xa] offset:11072\nv_mfma_f32_16x16x32_bf16 v[128:131], v[56:59], v[16:19], v[128:131]\n"
        "buffer_load_dwordx4 v[44:47], %[wa], %[rs], %[so] offen offset:3072 sc1\nv_mfma_f32_16x16x32_bf16 v[148:151], v[60:63], v[16:19], v[148:151]\nds_read_b64_tr_b16 v[16:17], %[xa] offset:11616\n"
        "s_waitcnt lgkmcnt(7)\nv_mfma_f32_16x16x32_bf16 v[112:115], v[52:55], v[20:23], v[112:115]\nv_add_u32 %[sy], 4, %[sy]\nds_read_b64_tr_b16 v[18:19], %[xa] offset:12128\n"
        "v_mfma_f32_16x16x32_bf16 v[132:135], v[56:59], v[20:23], v[132:135]\nds_write_b32 %[pg], %[sy]\nv_mfma_f32_16x16x32_bf16 v[152:155], v[60:63], v[20:23], v[152:155]\n"
        "s_waitcnt vmcnt(8) lgkmcnt(7)\nv_mfma_f32_16x16x32_bf16 v[96:99], v[64:67], v[24:27], v[96:99]\ns_addk_i32 %[so], 15360\nds_read_b64_tr_b16 v[20:21], %[xa] offset:12672\n"
        "v_mfma_f32_16x16x32_bf16 v[116:119], v[68:71], v[24:27], v[116:119]\nds_read_b64_tr_b16 v[22:23], %[xa] offset:13184\nv_mfma_f32_16x16x32_bf16 v[136:139], v[72:75], v[24:27], v[136:139]\n"
        "ds_read_b32 %[pv], %[sy] offset:4\nbuffer_load_dwordx4 v[48:51], %[wa], %[rs], %[so] offen offset:0 sc1\nv_mfma_f32_16x16x32_bf16 v[156:159], v[76:79], v[24:27], v[156:159]\n"
        "ds_read_b64_tr_b16 v[24:25], %[xa] offset:19008\ns_waitcnt lgkmcnt(9)\nv_mfma_f32_16x16x32_bf16 v[100:103], v[64:67], v[28:31], v[100:103]\nds_read_b64_tr_b16 v[26:27], %[xa] offset:19520\n"
        "v_mfma_f32_16x16x32_bf16 v[120:123], v[68:71], v[28:31], v[120:123]\nbuffer_load_dwordx4 v[52:55], %[wa], %[rs], %[so] offen offset:1024 sc1\n"
        "v_mfma_f32_16x16x32_bf16 v[140:143], v[72:75], v[28:31], v[140:143]\nv_mfma_f32_16x16x32_bf16 v[160:163], v[76:79], v[28:31], v[160:163]\nds_read_b64_tr_b16 v[28:29], %[xa] offset:20064\n"
        "s_waitcnt lgkmcnt(9)\nv_mfma_f32_16x16x32_bf16 v[104:107], v[68:71], v[12:15], v[104:107]\nds_read_b64_tr_b16 v[30:31], %[xa] offset:20576\n"
        "v_mfma_f32_16x16x32_bf16 v[124:127], v[72:75], v[12:15], v[124:127]\nbuffer_load_dwordx4 v[56:59], %[wa], %[rs], %[so] offen offset:2048 sc1\n"
        "v_mfma_f32_16x16x32_bf16 v[144:147], v[76:79], v[12:15], v[144:147]\nds_read_b64_tr_b16 v[12:13], %[xa] offset:15840\ns_waitcnt lgkmcnt(9)\n"
        "v_mfma_f32_16x16x32_bf16 v[108:111], v[68:71], v[16:19], v[108:111]\nds_read_b64_tr_b16 v[14:15], %[xa] offset:16352\nv_mfma_f32_16x16x32_bf16 v[128:131], v[72:75], v[16:19], v[128:131]\n"
        "buffer_load_dwordx4 v[60:63], %[wa], %[rs], %[so] offen offset:3072 sc1\nv_mfma_f32_16x16x32_bf16 v[148:151], v[76:79], v[16:19], v[148:151]\nds_read_b64_tr_b16 v[16:17], %[xa] offset:16896\n"
        "s_waitcnt lgkmcnt(8)\nv_mfma_f32_16x16x32_bf16 v[112:115], v[68:71], v[20:23], v[112:115]\nds_read_b64_tr_b16 v[18:19], %[xa] offset:17408\n"
        "v_mfma_f32_16x16x32_bf16 v[132:135], v[72:75], v[20:23], v[132:135]\nv_mfma_f32_16x16x32_bf16 v[152:155], v[76:79], v[20:23], v[152:155]\ns_waitcnt vmcnt(8) lgkmcnt(6)\n"
        "v_mfma_f32_16x16x32_bf16 v[96:99], v[80:83], v[24:27], v[96:99]\ns_addk_i32 %[so], 15360\nv_readfirstlane_b32 %[s0], %[pv]\n"
        "v_mfma_f32_16x16x32_bf16 v[116:119], v[84:87], v[24:27], v[116:119]\ns_cmp_ge_i32 %[s0], 6\nds_read_b64_tr_b16 v[20:21], %[xa] offset:17952\n"
        "v_mfma_f32_16x16x32_bf16 v[136:139], v[88:91], v[24:27], v[136:139]\ns_cbranch_scc0 .LC1s1_%=\n.LC1r1_%=:\nds_read_b64_tr_b16 v[22:23], %[xa] offset:18464\n"
        "v_mfma_f32_16x16x32_bf16 v[156:159], v[92:95], v[24:27], v[156:159]\nds_read_b64_tr_b16 v[24:25], %[xa] offset:24288\ns_waitcnt lgkmcnt(7)\n"
        "v_mfma_f32_16x16x32_bf16 v[100:103], v[80:83], v[28:31], v[100:103]\nds_read_b64_tr_b16 v[26:27], %[xa] offset:24800\nbuffer_load_dwordx4 v[64:67], %[wa], %[rs], %[so] offen offset:0 sc1\n"
        "v_mfma_f32_16x16x32_bf16 v[120:123], v[84:87], v[28:31], v[120:123]\nbuffer_load_dwordx4 v[68:71], %[wa], %[rs], %[so] offen offset:1024 sc1\n"
        "v_mfma_f32_16x16x32_bf16 v[140:143], v[88:91], v[28:31], v[140:143]\nv_mfma_f32_16x16x32_bf16 v[160:163], v[92:95], v[28:31], v[160:163]\nds_read_b64_tr_b16 v[28:29], %[xa] offset:25344\n"
        "s_waitcnt lgkmcnt(7)\nv_mfma_f32_16x16x32_bf16 v[104:107], v[84:87], v[12:15], v[104:107]\nds_read_b64_tr_b16 v[30:31], %[xa] offset:25856\n"
        "v_mfma_f32_16x16x32_bf16 v[124:127], v[88:91], v[12:15], v[124:127]\nbuffer_load_dwordx4 v[72:75], %[wa], %[rs], %[so] offen offset:2048 sc1\n"
        "v_mfma_f32_16x16x32_bf16 v[144:147], v[92:95], v[12:15], v[144:147]\nds_read_b64_tr_b16 v[12:13], %[xa] offset:21120\ns_waitcnt lgkmcnt(7)\n"
        "v_mfma_f32_16x16x32_bf16 v[108:111], v[84:87], v[16:19], v[108:111]\nds_read_b64_tr_b16 v[14:15], %[xa] offset:21632\nv_mfma_f32_16x16x32_bf16 v[128:131], v[88:91], v[16:19], v[128:131]\n"
        "buffer_load_dwordx4 v[76:79], %[wa], %[rs], %[so] offen offset:3072 sc1\nv_mfma_f32_16x16x32_bf16 v[148:151], v[92:95], v[16:19], v[148:151]\nds_read_b64_tr_b16 v[16:17], %[xa] offset:22176\n"
        "s_waitcnt lgkmcnt(7)\nv_mfma_f32_16x16x32_bf16 v[112:115], v[84:87], v[20:23], v[112:115]\nv_add_u32 %[sy], 4, %[sy]\nds_read_b64_tr_b16 v[18:19], %[xa] offset:22688\n"
        "v_mfma_f32_16x16x32_bf16 v[132:135], v[88:91], v[20:23], v[132:135]\nds_write_b32 %[pg], %[sy]\nv_mfma_f32_16x16x32_bf16 v[152:155], v[92:95], v[20:23], v[152:155]\n"
        "s_waitcnt vmcnt(8) lgkmcnt(7)\nv_mfma_f32_16x16x32_bf16 v[96:99], v[32:35], v[24:27], v[96:99]\ns_addk_i32 %[so], 15360\nds_read_b64_tr_b16 v[20:21], %[xa] offset:23232\n"
        "v_mfma_f32_16x16x32_bf16 v[116:119], v[36:39], v[24:27], v[116:119]\nds_read_b64_tr_b16 v[22:23], %[xa] offset:23744\nv_mfma_f32_16x16x32_bf16 v[136:139], v[40:43], v[24:27], v[136:139]\n"
        "ds_read_b32 %[pv], %[sy] offset:4\nbuffer_load_dwordx4 v[80:83], %[wa], %[rs], %[so] offen offset:0 sc1\nv_mfma_f32_16x16x32_bf16 v[156:159], v[44:47], v[24:27], v[156:159]\n"
        "ds_read_b64_tr_b16 v[24:25], %[xa] offset:29568\ns_waitcnt lgkmcnt(9)\nv_mfma_f32_16x16x32_bf16 v[100:103], v[32:35], v[28:31], v[100:103]\nds_read_b64_tr_b16 v[26:27], %[xa] offset:30080\n"
        "v_mfma_f32_16x16x32_bf16 v[120:123], v[36:39], v[28:31], v[120:123]\nbuffer_load_dwordx4 v[84:87], %[wa], %[rs], %[so] offen offset:1024 sc1\n"
        "v_mfma_f32_16x16x32_bf16 v[140:143], v[40:43], v[28:31], v[140:143]\nv_mfma_f32_16x16x32_bf16 v[160:163], v[44:47], v[28:31], v[160:163]\nds_read_b64_tr_b16 v[28:29], %[xa] offset:30624\n"
        "s_waitcnt lgkmcnt(9)\nv_mfma_f32_16x16x32_bf16 v[104:107], v[36:39], v[12:15], v[104:107]\nds_read_b64_tr_b16 v[30:31], %[xa] offset:31136\n"
        "v_mfma_f32_16x16x32_bf16 v[124:127], v[40:43], v[12:15], v[124:127]\nbuffer_load_dwordx4 v[88:91], %[wa], %[rs], %[so] offen offset:2048 sc1\n"
        "v_mfma_f32_16x16x32_bf16 v[144:147], v[44:47], v[12:15], v[144:147]\nds_read_b64_tr_b16 v[12:13], %[xa] offset:26400\ns_waitcnt lgkmcnt(9)\n"
        "v_mfma_f32_16x16x32_bf16 v[108:111], v[36:39], v[16:19], v[108:111]\nds_read_b64_tr_b16 v[14:15], %[xa] offset:26912\nv_mfma_f32_16x16x32_bf16 v[128:131], v[40:43], v[16:19], v[128:131]\n"
        "buffer_load_dwordx4 v[92:95], %[wa], %[rs], %[so] offen offset:3072 sc1\nv_mfma_f32_16x16x32_bf16 v[148:151], v[44:47], v[16:19], v[148:151]\nds_read_b64_tr_b16 v[16:17], %[xa] offset:27456\n"
        "s_waitcnt lgkmcnt(8)\nv_mfma_f32_16x16x32_bf16 v[112:115], v[36:39], v[20:23], v[112:115]\nds_read_b64_tr_b16 v[18:19], %[xa] offset:27968\n"
        "v_mfma_f32_16x16x32_bf16 v[132:135], v[40:43], v[20:23], v[132:135]\ns_cmp_lg_u32 %[so], 291840\nv_mfma_f32_16x16x32_bf16 v[152:155], v[44:47], v[20:23], v[152:155]\n"
        "v_add_u32 %[xa], 21120, %[xa]\ns_cbranch_scc1 .LC1l%=\ns_waitcnt vmcnt(8) lgkmcnt(6)\nv_mfma_f32_16x16x32_bf16 v[96:99], v[48:51], v[24:27], v[96:99]\nv_readfirstlane_b32 %[s0], %[pv]\n"
        "ds_read_b64_tr_b16 v[20:21], %[xa] offset:7392\nv_mfma_f32_16x16x32_bf16 v[116:119], v[52:55], v[24:27], v[116:119]\ns_cmp_ge_i32 %[s0], 6\nds_read_b64_tr_b16 v[22:23], %[xa] offset:7904\n"
        "v_mfma_f32_16x16x32_bf16 v[136:139], v[56:59], v[24:27], v[136:139]\ns_cbranch_scc0 .LC1s2_%=\n.LC1r2_%=:\nv_mfma_f32_16x16x32_bf16 v[156:159], v[60:63], v[24:27], v[156:159]\n"
        "ds_read_b64_tr_b16 v[24:25], %[xa] offset:13728\ns_waitcnt lgkmcnt(7)\nv_mfma_f32_16x16x32_bf16 v[100:103], v[48:51], v[28:31], v[100:103]\nds_read_b64_tr_b16 v[26:27], %[xa] offset:14240\n"
        "v_mfma_f32_16x16x32_bf16 v[120:123], v[52:55], v[28:31], v[120:123]\nv_mfma_f32_16x16x32_bf16 v[140:143], v[56:59], v[28:31], v[140:143]\n"
        "v_mfma_f32_16x16x32_bf16 v[160:163], v[60:63], v[28:31], v[160:163]\nds_read_b64_tr_b16 v[28:29], %[xa] offset:14784\ns_waitcnt lgkmcnt(7)\n"
        "v_mfma_f32_16x16x32_bf16 v[104:107], v[52:55], v[12:15], v[104:107]\nds_read_b64_tr_b16 v[30:31], %[xa] offset:15296\nv_mfma_f32_16x16x32_bf16 v[124:127], v[56:59], v[12:15], v[124:127]\n"
        "v_mfma_f32_16x16x32_bf16 v[144:147], v[60:63], v[12:15], v[144:147]\nds_read_b64_tr_b16 v[12:13], %[xa] offset:10560\ns_waitcnt lgkmcnt(7)\n"
        "v_mfma_f32_16x16x32_bf16 v[108:111], v[52:55], v[16:19], v[108:111]\nds_read_b64_tr_b16 v[14:15], %[xa] offset:11072\nv_mfma_f32_16x16x32_bf16 v[128:131], v[56:59], v[16:19], v[128:131]\n"
        "v_mfma_f32_16x16x32_bf16 v[148:151], v[60:63], v[16:19], v[148:151]\nds_read_b64_tr_b16 v[16:17], %[xa] offset:11616\ns_waitcnt lgkmcnt(7)\n"
        "v_mfma_f32_16x16x32_bf16 v[112:115], v[52:55], v[20:23], v[112:115]\nv_add_u32 %[sy], 4, %[sy]\nds_read_b64_tr_b16 v[18:19], %[xa] offset:12128\n"
        "v_mfma_f32_16x16x32_bf16 v[132:135], v[56:59], v[20:23], v[132:135]\nv_mfma_f32_16x16x32_bf16 v[152:155], v[60:63], v[20:23], v[152:155]\ns_waitcnt vmcnt(4) lgkmcnt(6)\n"
        "v_mfma_f32_16x16x32_bf16 v[96:99], v[64:67], v[24:27], v[96:99]\nds_read_b64_tr_b16 v[20:21], %[xa] offset:12672\nv_mfma_f32_16x16x32_bf16 v[116:119], v[68:71], v[24:27], v[116:119]\n"
        "ds_read_b64_tr_b16 v[22:23], %[xa] offset:13184\nv_mfma_f32_16x16x32_bf16 v[136:139], v[72:75], v[24:27], v[136:139]\nds_read_b32 %[pv], %[sy] offset:4\n"
        "v_mfma_f32_16x16x32_bf16 v[156:159], v[76:79], v[24:27], v[156:159]\nds_read_b64_tr_b16 v[24:25], %[xa] offset:19008\ns_waitcnt lgkmcnt(8)\n"
        "v_mfma_f32_16x16x32_bf16 v[100:103], v[64:67], v[28:31], v[100:103]\nds_read_b64_tr_b16 v[26:27], %[xa] offset:19520\nv_mfma_f32_16x16x32_bf16 v[120:123], v[68:71], v[28:31], v[120:123]\n"
        "v_mfma_f32_16x16x32_bf16 v[140:143], v[72:75], v[28:31], v[140:143]\nv_mfma_f32_16x16x32_bf16 v[160:163], v[76:79], v[28:31], v[160:163]\nds_read_b64_tr_b16 v[28:29], %[xa] offset:20064\n"
        "s_waitcnt lgkmcnt(8)\nv_mfma_f32_16x16x32_bf16 v[104:107], v[68:71], v[12:15], v[104:107]\nds_read_b64_tr_b16 v[30:31], %[xa] offset:20576\n"
        "v_mfma_f32_16x16x32_bf16 v[124:127], v[72:75], v[12:15], v[124:127]\nv_mfma_f32_16x16x32_bf16 v[144:147], v[76:79], v[12:15], v[144:147]\nds_read_b64_tr_b16 v[12:13], %[xa] offset:15840\n"
        "s_waitcnt lgkmcnt(8)\nv_mfma_f32_16x16x32_bf16 v[108:111], v[68:71], v[16:19], v[108:111]\nds_read_b64_tr_b16 v[14:15], %[xa] offset:16352\n"
        "v_mfma_f32_16x16x32_bf16 v[128:131], v[72:75], v[16:19], v[128:131]\nv_mfma_f32_16x16x32_bf16 v[148:151], v[76:79], v[16:19], v[148:151]\nds_read_b64_tr_b16 v[16:17], %[xa] offset:16896\n"
        "s_waitcnt lgkmcnt(8)\nv_mfma_f32_16x16x32_bf16 v[112:115], v[68:71], v[20:23], v[112:115]\nds_read_b64_tr_b16 v[18:19], %[xa] offset:17408\n"
        "v_mfma_f32_16x16x32_bf16 v[132:135], v[72:75], v[20:23], v[132:135]\nv_mfma_f32_16x16x32_bf16 v[152:155], v[76:79], v[20:23], v[152:155]\ns_waitcnt vmcnt(0) lgkmcnt(6)\n"
        "v_mfma_f32_16x16x32_bf16 v[96:99], v[80:83], v[24:27], v[96:99]\nv_readfirstlane_b32 %[s0], %[pv]\nds_read_b64_tr_b16 v[20:21], %[xa] offset:17952\n"
        "v_mfma_f32_16x16x32_bf16 v[116:119], v[84:87], v[24:27], v[116:119]\nds_read_b64_tr_b16 v[22:23], %[xa] offset:18464\nv_mfma_f32_16x16x32_bf16 v[136:139], v[88:91], v[24:27], v[136:139]\n"
        "s_cmp_ge_i32 %[s0], %[nst]\nv_mfma_f32_16x16x32_bf16 v[156:159], v[92:95], v[24:27], v[156:159]\ns_cbranch_scc0 .LC1t%=\n.LC1u%=:\ns_waitcnt lgkmcnt(6)\n"
        "v_mfma_f32_16x16x32_bf16 v[100:103], v[80:83], v[28:31], v[100:103]\nv_add_u32 %[sy], 4, %[sy]\nds_read_b128 v[24:27], %[mra] offset:768\n"
        "v_mfma_f32_16x16x32_bf16 v[120:123], v[84:87], v[28:31], v[120:123]\nv_mfma_f32_16x16x32_bf16 v[140:143], v[88:91], v[28:31], v[140:143]\n"
        "v_mfma_f32_16x16x32_bf16 v[160:163], v[92:95], v[28:31], v[160:163]\nds_read_b128 v[32:35], %[cwa] offset:448\ns_waitcnt lgkmcnt(6)\n"
        "v_mfma_f32_16x16x32_bf16 v[104:107], v[84:87], v[12:15], v[104:107]\nds_read_b128 v[36:39], %[cwa] offset:256\nds_read_b128 v[28:31], %[mra] offset:1024\n"
        "v_mfma_f32_16x16x32_bf16 v[124:127], v[88:91], v[12:15], v[124:127]\nds_read_b128 v[40:43], %[cwa] offset:832\nv_mfma_f32_16x16x32_bf16 v[144:147], v[92:95], v[12:15], v[144:147]\n"
        "ds_read_b128 v[44:47], %[cwa] offset:896\ns_waitcnt lgkmcnt(8)\nv_mfma_f32_16x16x32_bf16 v[108:111], v[84:87], v[16:19], v[108:111]\nv_add_u32 %[sy], 4, %[sy]\n"
        "ds_read_b128 v[12:15], %[mra] offset:0\nv_mfma_f32_16x16x32_bf16 v[128:131], v[88:91], v[16:19], v[128:131]\nv_mfma_f32_16x16x32_bf16 v[148:151], v[92:95], v[16:19], v[148:151]\n"
        "ds_read_b128 v[16:19], %[mra] offset:256\ns_waitcnt lgkmcnt(8)\nv_mfma_f32_16x16x32_bf16 v[112:115], v[84:87], v[20:23], v[112:115]\n"
        "v_mfma_f32_16x16x32_bf16 v[132:135], v[88:91], v[20:23], v[132:135]\nv_mfma_f32_16x16x32_bf16 v[152:155], v[92:95], v[20:23], v[152:155]\nds_read_b128 v[20:23], %[mra] offset:512\n"
        "s_setprio %[pfs]\ns_waitcnt lgkmcnt(1)\n.long 0xd3b04060, 0x1d823120\n.long 0xd3b04062, 0x1d8a3122\n.long 0xd3b04074, 0x1dd23124\n.long 0xd3b04076, 0x1dda3126\n.long 0xd3b14060, 0x18023560\n"
        ".long 0xd3b14062, 0x18023562\n.long 0xd3b04064, 0x1d923920\n.long 0xd3b04066, 0x1d9a3922\n.long 0xd3b14074, 0x18023574\n.long 0xd3b14076, 0x18023576\nv_cvt_pk_bf16_f32 v96, v96, v97\n"
        "v_cvt_pk_bf16_f32 v97, v98, v99\n.long 0xd3b04078, 0x1de23924\n.long 0xd3b0407a, 0x1dea3926\n.long 0xd3b14064, 0x18023d64\n.long 0xd3b14066, 0x18023d66\nv_cvt_pk_bf16_f32 v116, v116, v117\n"
        "v_cvt_pk_bf16_f32 v117, v118, v119\nds_write_b64 %[sta], v[96:97] offset:24032\n.long 0xd3b04068, 0x1da21924\n.long 0xd3b0406a, 0x1daa1926\n.long 0xd3b14078, 0x18023d78\n"
        ".long 0xd3b1407a, 0x18023d7a\nv_cvt_pk_bf16_f32 v100, v100, v101\nv_cvt_pk_bf16_f32 v101, v102, v103\nds_write_b64 %[sta], v[116:117] offset:23936\n.long 0xd3b0406c, 0x1db22124\n"
        ".long 0xd3b0406e, 0x1dba2126\n.long 0xd3b14068, 0x18021d68\n.long 0xd3b1406a, 0x18021d6a\nv_cvt_pk_bf16_f32 v120, v120, v121\nv_cvt_pk_bf16_f32 v121, v122, v123\n"
        "ds_write_b64 %[sta], v[100:101] offset:31968\ns_waitcnt lgkmcnt(3)\n.long 0xd3b04070, 0x1dc22924\n.long 0xd3b04072, 0x1dca2926\n.long 0xd3b1406c, 0x1802256c\n.long 0xd3b1406e, 0x1802256e\n"
        "v_cvt_pk_bf16_f32 v104, v104, v105\nv_cvt_pk_bf16_f32 v105, v106, v107\nds_write_b64 %[sta], v[120:121] offset:31872\n.long 0xd3b14070, 0x18022d70\n.long 0xd3b14072, 0x18022d72\n"
        "v_cvt_pk_bf16_f32 v108, v108, v109\nv_cvt_pk_bf16_f32 v109, v110, v111\nds_write_b64 %[sta], v[104:105] offset:128\nv_cvt_pk_bf16_f32 v112, v112, v113\nv_cvt_pk_bf16_f32 v113, v114, v115\n"
        "ds_write_b64 %[sta], v[108:109] offset:8064\nds_write_b64 %[sta], v[112:113] offset:16000\nds_write_b32 %[pg], %[sy]\nv_add_u32 %[sy], 4, %[sy]\n.long 0xd3b04088, 0x1e223128\n"
        ".long 0xd3b0408a, 0x1e2a312a\n.long 0xd3b0409c, 0x1e72312c\n.long 0xd3b0409e, 0x1e7a312e\n.long 0xd3b14088, 0x18023588\n.long 0xd3b1408a, 0x1802358a\n.long 0xd3b0408c, 0x1e323928\n"
        ".long 0xd3b0408e, 0x1e3a392a\n.long 0xd3b1409c, 0x1802359c\n.long 0xd3b1409e, 0x1802359e\nv_cvt_pk_bf16_f32 v136, v136, v137\nv_cvt_pk_bf16_f32 v137, v138, v139\n.long 0xd3b040a0, 0x1e82392c\n"
        ".long 0xd3b040a2, 0x1e8a392e\n.long 0xd3b1408c, 0x18023d8c\n.long 0xd3b1408e, 0x18023d8e\nv_cvt_pk_bf16_f32 v156, v156, v157\nv_cvt_pk_bf16_f32 v157, v158, v159\n"
        "ds_write_b64 %[sta], v[136:137] offset:24224\n.long 0xd3b0407c, 0x1df21928\n.long 0xd3b0407e, 0x1dfa192a\n.long 0xd3b140a0, 0x18023da0\n.long 0xd3b140a2, 0x18023da2\n"
        "v_cvt_pk_bf16_f32 v140, v140, v141\nv_cvt_pk_bf16_f32 v141, v142, v143\nds_write_b64 %[sta], v[156:157] offset:24256\n.long 0xd3b04090, 0x1e42192c\n.long 0xd3b04092, 0x1e4a192e\n"
        ".long 0xd3b1407c, 0x18021d7c\n.long 0xd3b1407e, 0x18021d7e\nv_cvt_pk_bf16_f32 v160, v160, v161\nv_cvt_pk_bf16_f32 v161, v162, v163\nds_write_b64 %[sta], v[140:141] offset:32160\n"
        ".long 0xd3b04080, 0x1e022128\n.long 0xd3b04082, 0x1e0a212a\n.long 0xd3b14090, 0x18021d90\n.long 0xd3b14092, 0x18021d92\nv_cvt_pk_bf16_f32 v124, v124, v125\nv_cvt_pk_bf16_f32 v125, v126, v127\n"
        "ds_write_b64 %[sta], v[160:161] offset:32192\n.long 0xd3b04094, 0x1e52212c\n.long 0xd3b04096, 0x1e5a212e\n.long 0xd3b14080, 0x18022580\n.long 0xd3b14082, 0x18022582\n"
        "v_cvt_pk_bf16_f32 v144, v144, v145\nv_cvt_pk_bf16_f32 v145, v146, v147\nds_write_b64 %[sta], v[124:125] offset:416\n.long 0xd3b04084, 0x1e122928\n.long 0xd3b04086, 0x1e1a292a\n"
        ".long 0xd3b14094, 0x18022594\n.long 0xd3b14096, 0x18022596\nv_cvt_pk_bf16_f32 v128, v128, v129\nv_cvt_pk_bf16_f32 v129, v130, v131\nds_write_b64 %[sta], v[144:145] offset:448\n"
        ".long 0xd3b04098, 0x1e62292c\n.long 0xd3b0409a, 0x1e6a292e\n.long 0xd3b14084, 0x18022d84\n.long 0xd3b14086, 0x18022d86\nv_cvt_pk_bf16_f32 v148, v148, v149\nv_cvt_pk_bf16_f32 v149, v150, v151\n"
        "ds_write_b64 %[sta], v[128:129] offset:8352\n.long 0xd3b14098, 0x18022d98\n.long 0xd3b1409a, 0x18022d9a\nv_cvt_pk_bf16_f32 v132, v132, v133\nv_cvt_pk_bf16_f32 v133, v134, v135\n"
        "ds_write_b64 %[sta], v[148:149] offset:8384\nv_cvt_pk_bf16_f32 v152, v152, v153\nv_cvt_pk_bf16_f32 v153, v154, v155\nds_write_b64 %[sta], v[132:133] offset:16288\n"
        "ds_write_b64 %[sta], v[152:153] offset:16320\nds_write_b32 %[pg], %[sy]\ns_branch .LE%=\n.LC1s0_%=:\ns_mov_b32 %[s1], 0x100000\n.LC1p0_%=:\nds_read_b32 %[pv], %[sy] offset:4\n"
        "s_waitcnt lgkmcnt(0)\nv_readfirstlane_b32 %[s0], %[pv]\ns_nop 0\ns_cmp_ge_i32 %[s0], 6\ns_cbranch_scc1 .LC1r0_%=\ns_sleep 1\ns_sub_u32 %[s1], %[s1], 1\ns_cbranch_scc0 .LC1p0_%=\n"
        "s_branch .LC1r0_%=\n.LC1s1_%=:\ns_mov_b32 %[s1], 0x100000\n.LC1p1_%=:\nds_read_b32 %[pv], %[sy] offset:4\ns_waitcnt lgkmcnt(0)\nv_readfirstlane_b32 %[s0], %[pv]\ns_nop 0\n"
        "s_cmp_ge_i32 %[s0], 6\ns_cbranch_scc1 .LC1r1_%=\ns_sleep 1\ns_sub_u32 %[s1], %[s1], 1\ns_cbranch_scc0 .LC1p1_%=\ns_branch .LC1r1_%=\n.LC1s2_%=:\ns_mov_b32 %[s1], 0x100000\n.LC1p2_%=:\n"
        "ds_read_b32 %[pv], %[sy] offset:4\ns_waitcnt lgkmcnt(0)\nv_readfirstlane_b32 %[s0], %[pv]\ns_nop 0\ns_cmp_ge_i32 %[s0], 6\ns_cbranch_scc1 .LC1r2_%=\ns_sleep 1\ns_sub_u32 %[s1], %[s1], 1\n"
        "s_cbranch_scc0 .LC1p2_%=\ns_branch .LC1r2_%=\n.LC1t%=:\ns_mov_b32 %[s1], 0x100000\n.LC1q%=:\nds_read_b32 %[pv], %[sy] offset:4\ns_waitcnt lgkmcnt(0)\nv_readfirstlane_b32 %[s0], %[pv]\n"
        "s_nop 0\ns_cmp_ge_i32 %[s0], %[nst]\ns_cbranch_scc1 .LC1u%=\ns_sleep 1\ns_sub_u32 %[s1], %[s1], 1\ns_cbranch_scc0 .LC1q%=\ns_branch .LC1u%=\n.LE%=:\n"
        : [pv] "=&v"(pv), [xa] "+v"(xa), [sy] "+v"(sy), [so] "=&s"(so), [s0] "=&s"(s0_), [s1] "=&s"(s1_)
        : [wa] "v"(wa), [rs] "s"(wrsrc), [pg] "v"(pga), [cwa] "v"(cwa), [mra] "v"(mra), [sta] "v"(sta), [w] "s"(w), [ow] "i"(SY_W - SY_X), [pfs] "i"(PFS), [nst] "i"(NST1)
        : "memory", "scc", "v12", "v13", "v14", "v15", "v16", "v17", "v18", "v19", "v20", "v21", "v22", "v23", "v24", "v25", "v26", "v27", "v28", "v29", "v30", "v31", "v32", "v33", "v34", "v35", "v36", "v37", "v38", "v39", "v40", "v41", "v42", "v43", "v44", "v45", "v46", "v47", "v48", "v49", "v50", "v51", "v52", "v53", "v54", "v55", "v56", "v57", "v58", "v59", "v60", "v61", "v62", "v63", "v64", "v65", "v66", "v67", "v68", "v69", "v70", "v71", "v72", "v73", "v74", "v75", "v76", "v77", "v78", "v79", "v80", "v81", "v82", "v83", "v84", "v85", "v86", "v87", "v88", "v89", "v90", "v91", "v92", "v93", "v94", "v95", "v96", "v97", "v98", "v99", "v100", "v101", "v102", "v103", "v104", "v105", "v106", "v107", "v108", "v109", "v110", "v111", "v112", "v113", "v114", "v115", "v116", "v117", "v118", "v119", "v120", "v121", "v122", "v123", "v124", "v125", "v126", "v127", "v128", "v129", "v130", "v131", "v132", "v133", "v134", "v135", "v136", "v137", "v138", "v139", "v140", "v141", "v142", "v143", "v144", "v145", "v146", "v147", "v148", "v149", "v150", "v151", "v152", "v153", "v154", "v155", "v156", "v157", "v158", "v159", "v160", "v161", "v162", "v163", "v164", "v165", "v166", "v167");
    }
    }
    ST(5);
    LANE(lane);
    const int t = 64 * w + lane;
    if (!layer && w == 3) {
      int spins = 0;
      _Pragma("unroll 1")
      while (true) {
        asm volatile("" ::: "memory");
        u32 d = 0;
#pragma unroll
        for (int i = 0; i < 2; ++i) {
          const u32x4 f = __builtin_amdgcn_raw_buffer_load_b128(frs, lane * 16, FL_M2 + i * 1024, 16);
          d |= (f[0] ^ tg0) | (f[1] ^ tg1) | (f[2] ^ tg0) | (f[3] ^ tg1);
        }
        if (__builtin_amdgcn_ballot_w64(d != 0) == 0 || ++spins > GCAP) break;
        __builtin_amdgcn_s_sleep(8);
      }
      VMCNT(0);
      ST(8);
    }
    if (layer && b == 0 && w == 1) {
      if (lane == 0) __builtin_amdgcn_raw_buffer_store_b64((u32x2){tg0, tg1}, frs, 0, 0, 16);
    }
    ST_FLUSH(kid);
    return;
  }
  const int j = w - 4, grp = j >> 2, mt2 = j & 3, kh = grp, qt = mt2, tt = t - 256;
  u32 l16 = lane * 16;
  int xso = g == (l15 >> 2) ? (rb * 8 + hg) * 1024 + mt2 * 256 + l15 * 16 : -1;
  const u32 tq1 = tg0 + tg1;
  if (layer) {
    const __amdgpu_buffer_rsrc_t xrs = __builtin_amdgcn_make_buffer_rsrc((void*)(xt + (size_t)rb * 51200), 0, 102400, 0x00020000);
    if (grp) __builtin_amdgcn_s_barrier();
    int gp[5], gh[3];
#pragma unroll
    for (int i = 0; i < 5; ++i) { const int p = i < 4 ? 4 * (i >> 1) + (i & 1) + 2 * grp : 8 + grp; gp[i] = PAIR(p, rot) * 10240; }
    const int ho = (mt2 & 1) * 5120 + (rb ? 4096 : 0);
    gh[0] = (mt2 & 2 ? gp[1] : gp[0]) + ho; gh[1] = (mt2 & 2 ? gp[3] : gp[2]) + ho; gh[2] = mt2 & 2 ? 102400 : gp[4] + ho;
    const int ck = (lane & 7) ^ ((lane >> 3) & 6);
    u32 vm0 = l16 + mt2 * 1024, vm1 = vm0 + 5120;
    u32 lm0 = LDSA(At) + grp * 21120 + (1 + (ck >> 1)) * 1056 + (8 * mt2 + (lane >> 3)) * 32 + (ck & 1) * 16, lm1 = lm0 + 84480 - grp * 10560;
    u32 lh0 = LDSA(At) + j * 5280 + l16, lh1 = lh0 + 84480 - grp * 10560, sy0 = sya + 8 * grp, sy1 = sya + 32 + 4 * grp, syh = sy0 + 2 * (mt2 & 2), tm;
    u32 xsb = LDSA(At) + (mt2 + 1) * 1056 + (4 * g + ((lane >> 2) & 3)) * 32 + 8 * (lane & 3), xsa = xsb + (12 + (hg & 1)) * 5280;
    xsb += xs0 * 5280;
    const int ns = hg;
    const u64 m1 = 0xaaaaaaaaaaaaaaaaull, m2 = 0xccccccccccccccccull;
    int cap, s0_, s1_;
    __builtin_amdgcn_s_setprio(PTX);
    if (grp) __builtin_amdgcn_s_sleep(XSLEEP1); else __builtin_amdgcn_s_sleep(XSLEEP);
      asm volatile(
        "s_mov_b32 %[cap], 0x10000\nbuffer_load_dwordx4 v[132:135], %[vm0], %[xrs], %[gp0] offen sc1\nbuffer_load_dwordx4 v[136:139], %[vm1], %[xrs], %[gp0] offen sc1\n"
        "buffer_load_dwordx4 v[140:143], %[l16], %[xrs], %[gh0] offen sc1\nbuffer_load_dwordx4 v[144:147], %[vm0], %[xrs], %[gp1] offen sc1\ns_cmp_lt_u32 %[j], 4\ns_cbranch_scc0 .LP0_%=\ns_barrier\n"
        ".LP0_%=:\ns_waitcnt vmcnt(3)\nv_max3_u32 %[tm], v132, v133, v134\nv_max_u32 %[tm], %[tm], v135\nv_cmp_eq_u32 vcc, -1, %[tm]\ns_cbranch_vccnz .LR0_%=\n.LK0_%=:\n"
        "ds_write_b128 %[lm0], v[132:135] offset:0\nbuffer_load_dwordx4 v[148:151], %[vm1], %[xrs], %[gp1] offen sc1\n.LP1_%=:\ns_waitcnt vmcnt(3)\nv_max3_u32 %[tm], v136, v137, v138\n"
        "v_max_u32 %[tm], %[tm], v139\nv_cmp_eq_u32 vcc, -1, %[tm]\ns_cbranch_vccnz .LR1_%=\n.LK1_%=:\nds_write_b128 %[lm0], v[136:139] offset:5280\ns_mov_b64 exec, 1\n"
        "ds_add_u32 %[sy0], %[one] offset:%[ox]+0\ns_mov_b64 exec, -1\nbuffer_load_dwordx4 v[132:135], %[vm0], %[xrs], %[gp2] offen sc1\n.LP2_%=:\ns_waitcnt vmcnt(3)\n"
        "v_max3_u32 %[tm], v140, v141, v142\nv_max_u32 %[tm], %[tm], v143\nv_cmp_eq_u32 vcc, -1, %[tm]\ns_cbranch_vccnz .LR2_%=\n.LK2_%=:\nds_write_b128 %[lh0], v[140:143] offset:0\ns_mov_b64 exec, 1\n"
        "ds_add_u32 %[syh], %[one] offset:%[ox]+0\ns_mov_b64 exec, -1\nbuffer_load_dwordx4 v[136:139], %[vm1], %[xrs], %[gp2] offen sc1\n.LP3_%=:\ns_waitcnt vmcnt(3)\n"
        "v_max3_u32 %[tm], v144, v145, v146\nv_max_u32 %[tm], %[tm], v147\nv_cmp_eq_u32 vcc, -1, %[tm]\ns_cbranch_vccnz .LR3_%=\n.LK3_%=:\nds_write_b128 %[lm0], v[144:147] offset:10560\n"
        "buffer_load_dwordx4 v[140:143], %[l16], %[xrs], %[gh1] offen sc1\n.LP4_%=:\ns_waitcnt vmcnt(3)\nv_max3_u32 %[tm], v148, v149, v150\nv_max_u32 %[tm], %[tm], v151\nv_cmp_eq_u32 vcc, -1, %[tm]\n"
        "s_cbranch_vccnz .LR4_%=\n.LK4_%=:\nds_write_b128 %[lm0], v[148:151] offset:15840\ns_mov_b64 exec, 1\nds_add_u32 %[sy0], %[one] offset:%[ox]+4\ns_mov_b64 exec, -1\n"
        "buffer_load_dwordx4 v[144:147], %[vm0], %[xrs], %[gp3] offen sc1\n.LP5_%=:\ns_waitcnt vmcnt(3)\nv_max3_u32 %[tm], v132, v133, v134\nv_max_u32 %[tm], %[tm], v135\nv_cmp_eq_u32 vcc, -1, %[tm]\n"
        "s_cbranch_vccnz .LR5_%=\n.LK5_%=:\nds_write_b128 %[lm0], v[132:135] offset:42240\nbuffer_load_dwordx4 v[148:151], %[vm1], %[xrs], %[gp3] offen sc1\n.LP6_%=:\ns_waitcnt vmcnt(3)\n"
        "v_max3_u32 %[tm], v136, v137, v138\nv_max_u32 %[tm], %[tm], v139\nv_cmp_eq_u32 vcc, -1, %[tm]\ns_cbranch_vccnz .LR6_%=\n.LK6_%=:\nds_write_b128 %[lm0], v[136:139] offset:47520\n"
        "s_mov_b64 exec, 1\nds_add_u32 %[sy0], %[one] offset:%[ox]+16\ns_mov_b64 exec, -1\nbuffer_load_dwordx4 v[132:135], %[vm0], %[xrs], %[gp4] offen sc1\n.LP7_%=:\ns_waitcnt vmcnt(3)\n"
        "v_max3_u32 %[tm], v140, v141, v142\nv_max_u32 %[tm], %[tm], v143\nv_cmp_eq_u32 vcc, -1, %[tm]\ns_cbranch_vccnz .LR7_%=\n.LK7_%=:\nds_write_b128 %[lh0], v[140:143] offset:42240\n"
        "s_mov_b64 exec, 1\nds_add_u32 %[syh], %[one] offset:%[ox]+16\ns_mov_b64 exec, -1\nbuffer_load_dwordx4 v[136:139], %[vm1], %[xrs], %[gp4] offen sc1\n.LP8_%=:\ns_waitcnt vmcnt(3)\n"
        "v_max3_u32 %[tm], v144, v145, v146\nv_max_u32 %[tm], %[tm], v147\nv_cmp_eq_u32 vcc, -1, %[tm]\ns_cbranch_vccnz .LR8_%=\n.LK8_%=:\nds_write_b128 %[lm0], v[144:147] offset:52800\n"
        "buffer_load_dwordx4 v[140:143], %[l16], %[xrs], %[gh2] offen sc1\ns_cmp_lt_u32 %[j], 4\ns_cbranch_scc1 .LT%=\ns_mov_b32 %[s1], 0x100000\n.LU%=:\n"
        "ds_read_b128 v[160:163], %[sy0] offset:%[ox]-8\nds_read_b128 v[164:167], %[sy0] offset:%[ox]+8\nds_read_b64_tr_b16 v[152:153], %[xsa]\nds_read_b64_tr_b16 v[154:155], %[xsa] offset:512\n"
        "ds_read_b64_tr_b16 v[156:157], %[xsb]\nds_read_b64_tr_b16 v[158:159], %[xsb] offset:512\ns_waitcnt lgkmcnt(4)\nv_min3_u32 v160, v160, v161, v162\nv_min3_u32 v164, v164, v165, v166\n"
        "v_min3_u32 v160, v160, v163, v164\ns_nop 0\nv_readfirstlane_b32 %[s0], v160\ns_waitcnt lgkmcnt(0)\ns_cmp_ge_i32 %[s0], 6\ns_cbranch_scc1 .LV%=\ns_sleep 1\ns_sub_u32 %[s1], %[s1], 1\n"
        "s_cbranch_scc0 .LU%=\n.LV%=:\nv_mfma_f32_16x16x32_bf16 v[160:163], %[ones], v[152:155], 0\nv_mfma_f32_16x16x32_bf16 v[164:167], v[152:155], v[152:155], 0\n"
        "ds_read_b64_tr_b16 v[152:153], %[xsb] offset:5280\nds_read_b64_tr_b16 v[154:155], %[xsb] offset:5792\ns_bitcmp1_b32 %[ns], 0\n"
        "v_mfma_f32_16x16x32_bf16 v[160:163], %[ones], v[156:159], v[160:163]\nv_mfma_f32_16x16x32_bf16 v[164:167], v[156:159], v[156:159], v[164:167]\ns_waitcnt lgkmcnt(0)\ns_cbranch_scc1 .LW%=\n"
        "v_mfma_f32_16x16x32_bf16 v[160:163], %[ones], v[152:155], v[160:163]\nv_mfma_f32_16x16x32_bf16 v[164:167], v[152:155], v[152:155], v[164:167]\n.LW%=:\ns_nop 7\n"
        "v_cndmask_b32 v164, v164, v165, %[m1]\nv_cndmask_b32 v166, v166, v167, %[m1]\nv_mov_b32 v161, %[tg0]\nv_mov_b32 v163, %[tq1]\nv_cndmask_b32 v162, v164, v166, %[m2]\n"
        "buffer_store_dwordx4 v[160:163], %[xso], %[srs], 0 offen sc1\n.LT%=:\n.LP9_%=:\ns_waitcnt vmcnt(3)\nv_max3_u32 %[tm], v148, v149, v150\nv_max_u32 %[tm], %[tm], v151\n"
        "v_cmp_eq_u32 vcc, -1, %[tm]\ns_cbranch_vccnz .LR9_%=\n.LK9_%=:\nds_write_b128 %[lm0], v[148:151] offset:58080\ns_mov_b64 exec, 1\nds_add_u32 %[sy0], %[one] offset:%[ox]+20\n"
        "s_mov_b64 exec, -1\n.LP10_%=:\ns_waitcnt vmcnt(2)\nv_max3_u32 %[tm], v132, v133, v134\nv_max_u32 %[tm], %[tm], v135\nv_cmp_eq_u32 vcc, -1, %[tm]\ns_cbranch_vccnz .LR10_%=\n.LK10_%=:\n"
        "ds_write_b128 %[lm1], v[132:135] offset:0\n.LP11_%=:\ns_waitcnt vmcnt(1)\nv_max3_u32 %[tm], v136, v137, v138\nv_max_u32 %[tm], %[tm], v139\nv_cmp_eq_u32 vcc, -1, %[tm]\n"
        "s_cbranch_vccnz .LR11_%=\n.LK11_%=:\nds_write_b128 %[lm1], v[136:139] offset:5280\ns_mov_b64 exec, 1\nds_add_u32 %[sy1], %[one] offset:%[ox]+0\ns_mov_b64 exec, -1\n.LP12_%=:\n"
        "s_waitcnt vmcnt(0)\nv_max3_u32 %[tm], v140, v141, v142\nv_max_u32 %[tm], %[tm], v143\nv_cmp_eq_u32 vcc, -1, %[tm]\ns_cbranch_vccnz .LR12_%=\n.LK12_%=:\ns_bitcmp1_b32 %[j], 1\n"
        "s_cbranch_scc1 .LE%=\nds_write_b128 %[lh1], v[140:143] offset:0\ns_mov_b64 exec, 1\nds_add_u32 %[sy1], %[one] offset:%[ox]\ns_mov_b64 exec, -1\ns_branch .LE%=\n.LR0_%=:\n"
        "s_sub_u32 %[cap], %[cap], 1\ns_cbranch_scc0 .LQ0_%=\ns_mov_b32 %[cap], 0\ns_branch .LK0_%=\n.LQ0_%=:\ns_sleep %[xp]\nbuffer_load_dwordx4 v[132:135], %[vm0], %[xrs], %[gp0] offen sc1\n"
        "buffer_load_dwordx4 v[136:139], %[vm1], %[xrs], %[gp0] offen sc1\nbuffer_load_dwordx4 v[140:143], %[l16], %[xrs], %[gh0] offen sc1\n"
        "buffer_load_dwordx4 v[144:147], %[vm0], %[xrs], %[gp1] offen sc1\ns_branch .LP0_%=\n.LR1_%=:\ns_sub_u32 %[cap], %[cap], 1\ns_cbranch_scc0 .LQ1_%=\ns_mov_b32 %[cap], 0\ns_branch .LK1_%=\n"
        ".LQ1_%=:\ns_sleep %[xp]\nbuffer_load_dwordx4 v[136:139], %[vm1], %[xrs], %[gp0] offen sc1\nbuffer_load_dwordx4 v[140:143], %[l16], %[xrs], %[gh0] offen sc1\n"
        "buffer_load_dwordx4 v[144:147], %[vm0], %[xrs], %[gp1] offen sc1\nbuffer_load_dwordx4 v[148:151], %[vm1], %[xrs], %[gp1] offen sc1\ns_branch .LP1_%=\n.LR2_%=:\ns_sub_u32 %[cap], %[cap], 1\n"
        "s_cbranch_scc0 .LQ2_%=\ns_mov_b32 %[cap], 0\ns_branch .LK2_%=\n.LQ2_%=:\ns_sleep %[xp]\nbuffer_load_dwordx4 v[140:143], %[l16], %[xrs], %[gh0] offen sc1\n"
        "buffer_load_dwordx4 v[144:147], %[vm0], %[xrs], %[gp1] offen sc1\nbuffer_load_dwordx4 v[148:151], %[vm1], %[xrs], %[gp1] offen sc1\n"
        "buffer_load_dwordx4 v[132:135], %[vm0], %[xrs], %[gp2] offen sc1\ns_branch .LP2_%=\n.LR3_%=:\ns_sub_u32 %[cap], %[cap], 1\ns_cbranch_scc0 .LQ3_%=\ns_mov_b32 %[cap], 0\ns_branch .LK3_%=\n"
        ".LQ3_%=:\ns_sleep %[xp]\nbuffer_load_dwordx4 v[144:147], %[vm0], %[xrs], %[gp1] offen sc1\nbuffer_load_dwordx4 v[148:151], %[vm1], %[xrs], %[gp1] offen sc1\n"
        "buffer_load_dwordx4 v[132:135], %[vm0], %[xrs], %[gp2] offen sc1\nbuffer_load_dwordx4 v[136:139], %[vm1], %[xrs], %[gp2] offen sc1\ns_branch .LP3_%=\n.LR4_%=:\ns_sub_u32 %[cap], %[cap], 1\n"
        "s_cbranch_scc0 .LQ4_%=\ns_mov_b32 %[cap], 0\ns_branch .LK4_%=\n.LQ4_%=:\ns_sleep %[xp]\nbuffer_load_dwordx4 v[148:151], %[vm1], %[xrs], %[gp1] offen sc1\n"
        "buffer_load_dwordx4 v[132:135], %[vm0], %[xrs], %[gp2] offen sc1\nbuffer_load_dwordx4 v[136:139], %[vm1], %[xrs], %[gp2] offen sc1\n"
        "buffer_load_dwordx4 v[140:143], %[l16], %[xrs], %[gh1] offen sc1\ns_branch .LP4_%=\n.LR5_%=:\ns_sub_u32 %[cap], %[cap], 1\ns_cbranch_scc0 .LQ5_%=\ns_mov_b32 %[cap], 0\ns_branch .LK5_%=\n"
        ".LQ5_%=:\ns_sleep %[xp]\nbuffer_load_dwordx4 v[132:135], %[vm0], %[xrs], %[gp2] offen sc1\nbuffer_load_dwordx4 v[136:139], %[vm1], %[xrs], %[gp2] offen sc1\n"
        "buffer_load_dwordx4 v[140:143], %[l16], %[xrs], %[gh1] offen sc1\nbuffer_load_dwordx4 v[144:147], %[vm0], %[xrs], %[gp3] offen sc1\ns_branch .LP5_%=\n.LR6_%=:\ns_sub_u32 %[cap], %[cap], 1\n"
        "s_cbranch_scc0 .LQ6_%=\ns_mov_b32 %[cap], 0\ns_branch .LK6_%=\n.LQ6_%=:\ns_sleep %[xp]\nbuffer_load_dwordx4 v[136:139], %[vm1], %[xrs], %[gp2] offen sc1\n"
        "buffer_load_dwordx4 v[140:143], %[l16], %[xrs], %[gh1] offen sc1\nbuffer_load_dwordx4 v[144:147], %[vm0], %[xrs], %[gp3] offen sc1\n"
        "buffer_load_dwordx4 v[148:151], %[vm1], %[xrs], %[gp3] offen sc1\ns_branch .LP6_%=\n.LR7_%=:\ns_sub_u32 %[cap], %[cap], 1\ns_cbranch_scc0 .LQ7_%=\ns_mov_b32 %[cap], 0\ns_branch .LK7_%=\n"
        ".LQ7_%=:\ns_sleep %[xp]\nbuffer_load_dwordx4 v[140:143], %[l16], %[xrs], %[gh1] offen sc1\nbuffer_load_dwordx4 v[144:147], %[vm0], %[xrs], %[gp3] offen sc1\n"
        "buffer_load_dwordx4 v[148:151], %[vm1], %[xrs], %[gp3] offen sc1\nbuffer_load_dwordx4 v[132:135], %[vm0], %[xrs], %[gp4] offen sc1\ns_branch .LP7_%=\n.LR8_%=:\ns_sub_u32 %[cap], %[cap], 1\n"
        "s_cbranch_scc0 .LQ8_%=\ns_mov_b32 %[cap], 0\ns_branch .LK8_%=\n.LQ8_%=:\ns_sleep %[xp]\nbuffer_load_dwordx4 v[144:147], %[vm0], %[xrs], %[gp3] offen sc1\n"
        "buffer_load_dwordx4 v[148:151], %[vm1], %[xrs], %[gp3] offen sc1\nbuffer_load_dwordx4 v[132:135], %[vm0], %[xrs], %[gp4] offen sc1\n"
        "buffer_load_dwordx4 v[136:139], %[vm1], %[xrs], %[gp4] offen sc1\ns_branch .LP8_%=\n.LR9_%=:\ns_sub_u32 %[cap], %[cap], 1\ns_cbranch_scc0 .LQ9_%=\ns_mov_b32 %[cap], 0\ns_branch .LK9_%=\n"
        ".LQ9_%=:\ns_sleep %[xp]\nbuffer_load_dwordx4 v[148:151], %[vm1], %[xrs], %[gp3] offen sc1\nbuffer_load_dwordx4 v[132:135], %[vm0], %[xrs], %[gp4] offen sc1\n"
        "buffer_load_dwordx4 v[136:139], %[vm1], %[xrs], %[gp4] offen sc1\nbuffer_load_dwordx4 v[140:143], %[l16], %[xrs], %[gh2] offen sc1\ns_branch .LP9_%=\n.LR10_%=:\ns_sub_u32 %[cap], %[cap], 1\n"
        "s_cbranch_scc0 .LQ10_%=\ns_mov_b32 %[cap], 0\ns_branch .LK10_%=\n.LQ10_%=:\ns_sleep %[xp]\nbuffer_load_dwordx4 v[132:135], %[vm0], %[xrs], %[gp4] offen sc1\n"
        "buffer_load_dwordx4 v[136:139], %[vm1], %[xrs], %[gp4] offen sc1\nbuffer_load_dwordx4 v[140:143], %[l16], %[xrs], %[gh2] offen sc1\ns_branch .LP10_%=\n.LR11_%=:\ns_sub_u32 %[cap], %[cap], 1\n"
        "s_cbranch_scc0 .LQ11_%=\ns_mov_b32 %[cap], 0\ns_branch .LK11_%=\n.LQ11_%=:\ns_sleep %[xp]\nbuffer_load_dwordx4 v[136:139], %[vm1], %[xrs], %[gp4] offen sc1\n"
        "buffer_load_dwordx4 v[140:143], %[l16], %[xrs], %[gh2] offen sc1\ns_branch .LP11_%=\n.LR12_%=:\ns_sub_u32 %[cap], %[cap], 1\ns_cbranch_scc0 .LQ12_%=\ns_mov_b32 %[cap], 0\ns_branch .LK12_%=\n"
        ".LQ12_%=:\ns_sleep %[xp]\nbuffer_load_dwordx4 v[140:143], %[l16], %[xrs], %[gh2] offen sc1\ns_branch .LP12_%=\n.LE%=:\n"
        : [tm] "=&v"(tm), [cap] "=&s"(cap), [s0] "=&s"(s0_), [s1] "=&s"(s1_)
        : [l16] "v"(l16), [vm0] "v"(vm0), [vm1] "v"(vm1), [xrs] "s"(xrs), [gp0] "s"(gp[0]), [gp1] "s"(gp[1]), [gp2] "s"(gp[2]), [gp3] "s"(gp[3]), [gp4] "s"(gp[4]), [gh0] "s"(gh[0]), [gh1] "s"(gh[1]), [gh2] "s"(gh[2]), [lm0] "v"(lm0), [lm1] "v"(lm1), [lh0] "v"(lh0), [lh1] "v"(lh1), [sy0] "v"(sy0), [sy1] "v"(sy1), [syh] "v"(syh), [xsb] "v"(xsb), [one] "v"(one), [j] "s"(j), [xsa] "v"(xsa), [ns] "s"(ns), [ones] "v"(onesf), [m1] "s"(m1), [m2] "s"(m2), [xso] "v"(xso), [srs] "s"(xsrs), [tg0] "s"(tg0), [tq1] "s"(tq1), [ox] "i"(SY_X), [xp] "i"(XPOLL)
        : "memory", "scc", "vcc", "v132", "v133", "v134", "v135", "v136", "v137", "v138", "v139", "v140", "v141", "v142", "v143", "v144", "v145", "v146", "v147", "v148", "v149", "v150", "v151", "v152", "v153", "v154", "v155", "v156", "v157", "v158", "v159", "v160", "v161", "v162", "v163", "v164", "v165", "v166", "v167");
    RETRY(8, 0x10000 - cap);
    ST(3);
  } else {
    ST(5);
#define MARKS \
    if (w == MKW) { \
      const __amdgpu_buffer_rsrc_t mrs = __builtin_amdgcn_make_buffer_rsrc(obuf, 0, HO_BYTES, 0x00020000); \
      const __amdgpu_buffer_rsrc_t irs = __builtin_amdgcn_make_buffer_rsrc(ws + WS_IM1 + b * 12800, 0, 12800, 0x00020000); \
      __builtin_amdgcn_raw_buffer_store_b128(MARK16, mrs, lane < 16 ? (lane >> 3) * 4096 + (lane & 7) * 16 : -1, HO_FS + hg * 8192 + s0 * 2, 17); \
      _Pragma("unroll 1") \
      for (int i = 0; i < 10; ++i) { \
        __builtin_amdgcn_raw_buffer_store_b128(MARK16, mrs, (lane >> 3) * (XP * 2) + (lane & 7) * 16, HO_X1 + (hg * 80 + 8 * i) * (XP * 2) + s0 * 2, 17); \
        __builtin_amdgcn_s_sleep(MSL1); \
      } \
      _Pragma("unroll 1") \
      for (int i = 0; i < 13; ++i) { \
        __builtin_amdgcn_raw_buffer_store_b128(MARK16, irs, lane * 16, i * 1024, 17); \
        __builtin_amdgcn_s_sleep(MSL2); \
      } \
      VMCNT(0); \
      if (lane == 0) __builtin_amdgcn_raw_buffer_store_b64((u32x2){tg0, tg1}, frs, 0, FL_M2 + b * 8, 16); \
      ST(1); \
    } \

    if (MKPOS == 0) MARKS
    lds_poll<SY_X, 2>(sya, 3);
    if (j == 0 ? MKPOS : j == 4 ? !MKPOS : j < 4) {
      const u32 xa = LDSA(At) + (2 * mt2 + 2 + (l15 >> 3)) * 1024 + (l15 & 7) * 128 + ((g ^ (l15 & 6)) << 4);
      bf16x8 fv[3];
      asm volatile("ds_read_b128 %0, %3 offset:61440\n\tds_read_b128 %1, %4\n\tds_read_b128 %2, %5\n\ts_waitcnt lgkmcnt(0)" : "=&v"(fv[0]), "=&v"(fv[1]), "=&v"(fv[2])
                   : "v"(xa ^ (64 * (hg & 1))), "v"((xa ^ (64 * (xs0 & 1))) + (xs0 >> 1) * 10240), "v"((xa ^ (64 * (~xs0 & 1))) + ((xs0 + 1) >> 1) * 10240) : "memory");
      f32x4 s1 = {0.f, 0.f, 0.f, 0.f}, s2 = s1;
#pragma unroll
      for (int q = 0; q < 3; ++q) {
        if (q < 2 || !(hg & 1)) {
          s1 = __builtin_amdgcn_mfma_f32_16x16x32_bf16(onesf, fv[q], s1, 0, 0, 0);
          s2 = __builtin_amdgcn_mfma_f32_16x16x32_bf16(fv[q], fv[q], s2, 0, 0, 0);
        }
      }
      const float d2 = (l15 & 2) ? ((l15 & 1) ? s2[3] : s2[2]) : ((l15 & 1) ? s2[1] : s2[0]);
      __builtin_amdgcn_raw_buffer_store_b128((u32x4){__float_as_uint(s1[0]), tg0, __float_as_uint(d2), tq1}, xsrs, xso, 0, 16);
    }
  }
  __builtin_amdgcn_s_setprio(PTW);
  if (!layer && MKPOS == 1) MARKS
  if (layer) lds_inc<SY_PART>(sya, one);
  const __amdgpu_buffer_rsrc_t orsrc = __builtin_amdgcn_make_buffer_rsrc(obuf + (size_t)rb * 40960, 0, 81920, 0x00020000);
  u32 qka = LDSA(St) + ((16 * qt + l15) * X1_P + 160 * grp + 8 * g) * 2;
  u32 vta = LDSA(St) + ((16 * qt + 4 * g + ((lane >> 2) & 3)) * X1_P + 160 * grp + 40 + 4 * (lane & 3)) * 2;
  const bool par = l15 & 1;
  const int kk0 = 4 * g + (l15 & 1), kk1 = kk0 + 2;
  const bool c0 = kk0 >= l15 + 2, c1 = kk1 >= l15 + 2;
  const u32 e0 = par ? 0x01000c0cu : 0x0c0c0100u, e1 = par ? 0x03020c0cu : 0x0c0c0302u;
  u32 sA0 = c0 ? e0 : 0x0c0c0c0cu, sB0 = c0 ? 0x0c0c0c0cu : e0;
  u32 sA1 = c1 ? e1 : 0x0c0c0c0cu, sB1 = c1 ? 0x0c0c0c0cu : e1;
  u32 oha = g * 10240 + l15 * 16, qa[4];
#pragma unroll
  for (int i = 0; i < 4; ++i) { qa[i] = g >= 3 - (i & 1) ? sya + SY_Z : qka + (32 * X1_P + 160 - 240 * grp) + 40 * (i >> 1) + 8 * (i & 1); PIN(qa[i]); }
  const float nb = s0 + qt ? 0.f : -1e30f;
  f32x4 bias = {nb, nb, nb, nb};
  int ohs = g ? -1 : l16;
  u64 c0m = __builtin_amdgcn_ballot_w64(c0), c1m = __builtin_amdgcn_ballot_w64(c1);
  int so0 = (hg * 4 + qt) * 2560 + grp * 1024, so1 = (hg * 4 + qt) * 2560 + 2048 + grp * 256;
  float nc = -0.22360679774997896f * 1.4426950408889634f;
  asm volatile("" : "+s"(c0m), "+s"(c1m), "+s"(so0), "+s"(so1), "+s"(nc));
  PIN(qka); PIN(vta); PIN(sA0); PIN(sA1); PIN(sB0); PIN(sB1); PIN(l16); PIN(oha); PIN(sya); PIN(one); PIN(bias); PIN(ohs);
  u32 rw = LDSA(smem) + A_R1 + j * 3072 + l16, fw = sya + SY_RED + 4 * j, f0 = 1 - kh;
  u32 xo_ = LDSA(smem + X1T_OFF) + (32 * kh + l15) * 144 + 32 * mt2 + 8 * g;
  u32 xi_ = LDSA(smem + X1T_OFF) + (tt >> 3) * 144 + (tt & 7) * 16;
  const __amdgpu_buffer_rsrc_t x1rs = __builtin_amdgcn_make_buffer_rsrc(xtout + (size_t)hg * 80 * XP + s0, 0, 80 * XP * 2, 0x00020000);
  int xg_ = (tt >> 3) * (XP * 2) + (tt & 7) * 16;
  const __amdgpu_buffer_rsrc_t srsrc = __builtin_amdgcn_make_buffer_rsrc(stats + (size_t)s0 * 16, 0, 8192, 0x00020000);
  u32 rr = LDSA(smem) + A_R1 + (j ^ 4) * 3072 + l16 - 1024 * kh;
  int so_ = (l15 & 3) ? -1 : (lane >> 2) * 8;
  u32 bb = LDSA(At) + kh * 25600 + l16, fr = fw ^ 16, wd = LDSA(At) + j * 1024 + l16, ra[3];
  {
    const int rr_ = 4 * (g & 1) + ((lane >> 2) & 3), pr_ = 4 * (l15 >> 3) + (l15 & 3) + 4 * (l15 & 4);
    const u32 ro = LDSA(At) + (layer ? (mt2 + 1) * 1056 + pr_ * 32 + 8 * g : (2 * mt2 + 2 + (g >> 1)) * 1024 + rr_ * 128 + ((rr_ & 6) << 4) + (lane & 3) * 8);
#pragma unroll
    for (int i = 0; i < 3; ++i) {
      const int kc = hg * 80 + 16 * (2 * kh + i), kr = 64 * PAIR(kc >> 6, -rot) + (kc & 63);
      ra[i] = layer ? ro + (kr >> 5) * 5280 + (kr & 16) * 16 : (ro ^ ((kr & 48) * 2)) + (kr >> 6) * 10240;
      if (i == 0 && kh) ra[i] = sya + SY_Z;
      PIN(ra[i]);
    }
  }
  PIN(rw); PIN(fw); PIN(f0); PIN(xo_); PIN(xi_); PIN(xg_); PIN(rr); PIN(so_); PIN(bb); PIN(fr); PIN(wd);
  STX(8);
  STX(10);
    if (!layer) {
    if (MKPOS == 2) MARKS
      if (w == CWW) {
        const __amdgpu_buffer_rsrc_t crs = __builtin_amdgcn_make_buffer_rsrc(ws + WS_CW + hg * 20480, 0, (EXP & 4) ? 0 : 20480, 0x00020000);
        pg_poll<4>(sya, LDSA(smem) + SY_OFF + SY_X + CWAT);
        __builtin_amdgcn_s_setprio(PCW);
        f32x4 cv[20];
#pragma unroll
        for (int i = 0; i < 20; ++i) cv[i] = __builtin_bit_cast(f32x4, __builtin_amdgcn_raw_buffer_load_b128(crs, min(lane, 59) * 16, i * 1024, 16));
#pragma unroll
        for (int i = 0; i < 10; ++i) cv[i] += cv[i + 10];
        const f32x4 cs = ((cv[0] + cv[1]) + (cv[2] + cv[3])) + ((cv[4] + cv[5]) + (cv[6] + cv[7])) + (cv[8] + cv[9]);
        asm volatile("ds_write_b128 %0, %1" :: "v"(LDSA(smem) + CW_OFF + lane * 16), "v"(cs) : "memory");
        lds_inc<SY_STAT>(sya, one);
        __builtin_amdgcn_s_setprio(PTW);
        ST(1);
      }
      if (w == 9) {
        const int fb = lane < 8 ? (b & 62) + (lane & 1) + 64 * (lane >> 1) : 256 + 16 * ((lane >> 1) & 7) + 2 * hg + (lane & 1);
        pg_poll<8>(sya, LDSA(smem) + SY_OFF + SY_X + MKAT);
        int spins = 0;
        _Pragma("unroll 1")
        while (true) {
          asm volatile("" ::: "memory");
          const u32x2 f = __builtin_amdgcn_raw_buffer_load_b64(frs, lane < 24 ? fb * 64 : b * 64, FL_M, 16);
          if (__builtin_amdgcn_ballot_w64(f[0] != tg0 || f[1] != tg1) == 0 || ++spins > GCAP) break;
          __builtin_amdgcn_s_sleep(8);
        }
        RETRY(15, spins);
        lds_inc<SY_STAT>(sya, one);
        lds_inc<SY_MK>(sya, one);
        ST(1);
      }
    }
    if (j == XJ0 || j == (layer ? XJ1 : XJ2)) {
      const int xo = j == XJ0 ? rb * 8192 + l16 : (rb ? rb - 1 : 0) * 8192 + 768 + l15 * 16;
      pg_poll<4>(sya, LDSA(smem) + SY_OFF + 256 * layer + SY_X + (layer ? XSAT : XSAT0));
      __builtin_amdgcn_s_sleep(layer ? XSSL : 0);
      __builtin_amdgcn_s_setprio(3);
      u32x4 pv[8];
      int spins = 0;
      _Pragma("unroll 1")
      while (true) {
        asm volatile("" ::: "memory");
        u32 m = 0;
#pragma unroll
        for (int i = 0; i < 8; ++i) pv[i] = __builtin_amdgcn_raw_buffer_load_b128(xsrs, xo, i * 1024, 16);
#pragma unroll
        for (int i = 0; i < 8; ++i) m |= (pv[i][1] ^ tg0) | (pv[i][3] ^ tq1);
        if (__builtin_amdgcn_ballot_w64(m != 0) == 0 || ++spins > GCAP) break;
        __builtin_amdgcn_s_sleep(XSRT);
      }
      RETRY(15, spins);
      float sv[2];
#pragma unroll
      for (int i = 0; i < 2; ++i)
        sv[i] = ((__uint_as_float(pv[0][2 * i]) + __uint_as_float(pv[1][2 * i])) + (__uint_as_float(pv[2][2 * i]) + __uint_as_float(pv[3][2 * i]))) +
                ((__uint_as_float(pv[4][2 * i]) + __uint_as_float(pv[5][2 * i])) + (__uint_as_float(pv[6][2 * i]) + __uint_as_float(pv[7][2 * i])));
      asm volatile("ds_write_b128 %0, %1" :: "v"(LDSA(smem) + A_R2 + (j == XJ0 ? 256 + l16 : l15 * 16)), "v"(ln_fin(sv[0], sv[1])) : "memory");
      lds_inc<SY_STAT>(sya, one);
      __builtin_amdgcn_s_setprio(PTW);
      ST(1);
    }
  u32x2 rv[3];
  if (layer) {
    lds_poll<SY_PART, 2>(sya, 8);
    asm volatile("ds_read_b64 %0, %3\n\tds_read_b64 %1, %4\n\tds_read_b64 %2, %5\n\ts_waitcnt lgkmcnt(0)" : "=&v"(rv[0]), "=&v"(rv[1]), "=&v"(rv[2]) : "v"(ra[0]), "v"(ra[1]), "v"(ra[2]) : "memory");
  } else
    asm volatile("ds_read_b64_tr_b16 %0, %3\n\tds_read_b64_tr_b16 %1, %4\n\tds_read_b64_tr_b16 %2, %5\n\ts_waitcnt lgkmcnt(0)" : "=&v"(rv[0]), "=&v"(rv[1]), "=&v"(rv[2]) : "v"(ra[0]), "v"(ra[1]), "v"(ra[2]) : "memory");
  lds_inc<SY_RV>(sya, one);
  STX(9);
  u32x4 wv[2][7];
  {
    const __amdgpu_buffer_rsrc_t wos = __builtin_amdgcn_make_buffer_rsrc((void*)(wot + (size_t)hg * 51200 + j * 512), 0, 102400 - j * 1024, 0x00020000);
    if (!layer) lds_poll<SY_MK, 2>(sya, 1);
    pg_poll<2>(sya, LDSA(smem) + SY_OFF + 256 * layer + SY_X + WOAT);
    __builtin_amdgcn_s_setprio(PTO);
    STX(11);
#pragma unroll
    for (int p = 0; p < 2; ++p)
#pragma unroll
      for (int i = 0; i < 7; ++i) wv[p][i] = __builtin_amdgcn_raw_buffer_load_b128(wos, l16, (i == 6 && j >= 2) ? 102400 : p * 51200 + i * 8192, 16);
    lds_poll<SY_RV, 1>(sya, 8);
#pragma unroll
    for (int i = 0; i < 7; ++i) if (i < 6 || j < 2) asm volatile("ds_write_b128 %0, %1 offset:%2" :: "v"(wd), "v"(wv[0][i]), "i"(i * 8192) : "memory");
    lds_inc<SY_WO>(sya, one);
    wd += WO1_OFF;
    PIN(wd);
  }
  if (grp) { pg_poll<2>(sya, LDSA(smem) + SY_OFF + 256 * layer + SY_X + 44); __builtin_amdgcn_s_setprio(PTA1); } else __builtin_amdgcn_s_setprio(PTA);
  STX(12);
  pg_poll<APS>(sya, LDSA(smem) + SY_OFF + 256 * layer + SY_X + 44 + 4 * grp);
  if (layer) ST(5);
      asm volatile(
        "ds_read_b64 v[30:31], %[q0]\nds_read_b64 v[32:33], %[q1]\nds_read_b64 v[38:39], %[ka] offset:0\nds_read_b64 v[40:41], %[ka] offset:8\nds_read_b64 v[42:43], %[ka] offset:7936\n"
        "ds_read_b64 v[44:45], %[ka] offset:7944\nds_read_b64 v[34:35], %[q2]\nds_read_b64 v[36:37], %[q3]\nds_read_b64 v[46:47], %[ka] offset:40\nds_read_b64 v[48:49], %[ka] offset:48\n"
        "ds_read_b64 v[50:51], %[ka] offset:7976\nds_read_b64 v[52:53], %[ka] offset:7984\nds_read_b64_tr_b16 v[54:55], %[va] offset:0\nds_read_b64_tr_b16 v[56:57], %[va] offset:7936\n"
        "ds_read_b64_tr_b16 v[58:59], %[va] offset:32\nds_read_b64_tr_b16 v[60:61], %[va] offset:7968\nds_read_b64_tr_b16 v[62:63], %[va] offset:40\nds_read_b64_tr_b16 v[64:65], %[va] offset:7976\n"
        "ds_read_b64_tr_b16 v[66:67], %[va] offset:72\nds_read_b64_tr_b16 v[68:69], %[va] offset:8008\ns_waitcnt lgkmcnt(14)\nv_mfma_f32_16x16x32_bf16 v[70:73], v[38:41], v[30:33], %[bias]\n"
        "v_mfma_f32_16x16x32_bf16 v[74:77], v[42:45], v[30:33], 0\ns_waitcnt lgkmcnt(8)\nv_mfma_f32_16x16x32_bf16 v[78:81], v[46:49], v[34:37], %[bias]\n"
        "v_mfma_f32_16x16x32_bf16 v[82:85], v[50:53], v[34:37], 0\ns_nop 4\nv_cndmask_b32 v70, v70, v71, %[par]\nv_cndmask_b32 v72, v72, v73, %[par]\nv_cndmask_b32 v74, v74, v75, %[par]\n"
        "v_cndmask_b32 v76, v76, v77, %[par]\nv_cndmask_b32 v30, v74, v70, %[c0]\nv_cndmask_b32 v31, v76, v72, %[c1]\nv_max_f32 v34, v30, v31\nv_cndmask_b32 v78, v78, v79, %[par]\n"
        "v_cndmask_b32 v80, v80, v81, %[par]\nv_cndmask_b32 v82, v82, v83, %[par]\nv_cndmask_b32 v84, v84, v85, %[par]\nv_cndmask_b32 v32, v82, v78, %[c0]\nv_cndmask_b32 v33, v84, v80, %[c1]\n"
        "v_max_f32 v35, v32, v33\ns_nop 1\nv_permlane16_swap_b32 v34, v35\nv_max_f32 v34, v34, v35\nv_mov_b32 v35, v34\ns_nop 1\nv_permlane32_swap_b32 v34, v35\nv_max_f32 v34, v34, v35\n"
        "v_mul_f32 v35, %[nc], v34\nv_mul_f32 v34, %[nc], v34\ns_nop 1\nv_permlane16_swap_b32 v34, v35\nv_fma_f32 v30, -v30, %[nc], v34\nv_fma_f32 v31, -v31, %[nc], v34\n"
        "v_fma_f32 v32, -v32, %[nc], v35\nv_fma_f32 v33, -v33, %[nc], v35\nv_exp_f32 v30, v30\nv_exp_f32 v31, v31\nv_exp_f32 v32, v32\nv_exp_f32 v33, v33\nv_add_f32 v34, v30, v31\n"
        "v_add_f32 v35, v32, v33\ns_nop 1\nv_permlane16_swap_b32 v34, v35\nv_add_f32 v34, v34, v35\nv_mov_b32 v35, v34\ns_nop 1\nv_permlane32_swap_b32 v34, v35\nv_add_f32 v34, v34, v35\n"
        "v_rcp_f32 v34, v34\ns_nop 0\nv_mov_b32 v35, v34\ns_nop 1\nv_permlane16_swap_b32 v34, v35\nv_mul_f32 v30, v30, v34\nv_mul_f32 v31, v31, v34\nv_mul_f32 v32, v32, v35\nv_mul_f32 v33, v33, v35\n"
        "v_cvt_pk_bf16_f32 v30, v30, v31\nv_cvt_pk_bf16_f32 v32, v32, v33\nv_perm_b32 v38, v30, v30, %[sa0]\nv_perm_b32 v39, v30, v30, %[sa1]\nv_perm_b32 v40, v30, v30, %[sb0]\n"
        "v_perm_b32 v41, v30, v30, %[sb1]\nv_perm_b32 v42, v32, v32, %[sa0]\nv_perm_b32 v43, v32, v32, %[sa1]\nv_perm_b32 v44, v32, v32, %[sb0]\nv_perm_b32 v45, v32, v32, %[sb1]\ns_waitcnt lgkmcnt(0)\n"
        "v_mfma_f32_16x16x32_bf16 v[70:73], v[54:57], v[38:41], 0\nv_mfma_f32_16x16x32_bf16 v[78:81], v[62:65], v[42:45], 0\nv_mfma_f32_16x16x32_bf16 v[74:77], v[58:61], v[38:41], 0\n"
        "v_mfma_f32_16x16x32_bf16 v[82:85], v[66:69], v[42:45], 0\ns_nop 4\nv_cvt_pk_bf16_f32 v46, v70, v71\nv_cvt_pk_bf16_f32 v47, v72, v73\nv_cvt_pk_bf16_f32 v48, v78, v79\n"
        "v_cvt_pk_bf16_f32 v49, v80, v81\nv_min_u32 v46, -2, v46\nv_min_u32 v47, -2, v47\nv_min_u32 v48, -2, v48\nv_min_u32 v49, -2, v49\n"
        "buffer_store_dwordx4 v[46:49], %[l16], %[ors], %[so0] offen sc1\nv_cvt_pk_bf16_f32 v50, v74, v75\nv_cvt_pk_bf16_f32 v51, v76, v77\nv_cvt_pk_bf16_f32 v52, v82, v83\n"
        "v_cvt_pk_bf16_f32 v53, v84, v85\nv_min_u32 v50, -2, v50\nv_min_u32 v51, -2, v51\nv_min_u32 v52, -2, v52\nv_min_u32 v53, -2, v53\n"
        "buffer_store_dwordx4 v[50:53], %[oh], %[ors], %[so1] offen sc1\ns_nop 1\n"
        :
        : [q0] "v"(qa[0]), [q1] "v"(qa[1]), [q2] "v"(qa[2]), [q3] "v"(qa[3]), [ka] "v"(qka), [va] "v"(vta), [bias] "v"(bias), [par] "s"(0xaaaaaaaaaaaaaaaaull), [c0] "s"(c0m), [c1] "s"(c1m), [nc] "s"(nc), [sa0] "v"(sA0), [sa1] "v"(sA1), [sb0] "v"(sB0), [sb1] "v"(sB1), [l16] "v"(l16), [oh] "v"(ohs), [ors] "s"(orsrc), [so0] "s"(so0), [so1] "s"(so1)
        : "memory", "scc", "v30", "v31", "v32", "v33", "v34", "v35", "v36", "v37", "v38", "v39", "v40", "v41", "v42", "v43", "v44", "v45", "v46", "v47", "v48", "v49", "v50", "v51", "v52", "v53", "v54", "v55", "v56", "v57", "v58", "v59", "v60", "v61", "v62", "v63", "v64", "v65", "v66", "v67", "v68", "v69", "v70", "v71", "v72", "v73", "v74", "v75", "v76", "v77", "v78", "v79", "v80", "v81", "v82", "v83", "v84", "v85");
  ST(6);
  {
#pragma unroll
    for (int i = 0; i < 7; ++i) if (i < 6 || j < 2) asm volatile("ds_write_b128 %0, %1 offset:%2" :: "v"(wd), "v"(wv[1][i]), "i"(i * 8192) : "memory");
    lds_inc<SY_WO + 4>(sya, one);
  }
  {
    const int ob = (16 * kh + mt2) * 2560;
    u32 tm;
    int cap, s0_, s1_, rt = 0;
      asm volatile(
        "s_mov_b32 %[cap], 0x10000\ns_cmp_eq_u32 %[kh], 0\ns_cbranch_scc1 .LS0_%=\ns_sleep %[sl1]\nbuffer_load_dwordx4 v[28:31], %[l16], %[ors], %[o0] offen offset:0 sc1\n"
        "buffer_load_dwordx4 v[32:35], %[l16], %[ors], %[o1] offen offset:0 sc1\nbuffer_load_dwordx4 v[36:39], %[l16], %[ors], %[o2] offen offset:0 sc1\n"
        "buffer_load_dwordx4 v[40:43], %[l16], %[ors], %[o3] offen offset:0 sc1\nbuffer_load_dwordx4 v[44:47], %[oha], %[ors], %[o4] offen offset:0 sc1\n.LS0_%=:\ns_mov_b32 %[s1], 0x100000\n.LWa%=:\n"
        "ds_read_b32 %[tm], %[sy] offset:%[ow1]-4\ns_waitcnt lgkmcnt(0)\nv_readfirstlane_b32 %[s0], %[tm]\ns_nop 0\ns_cmp_ge_i32 %[s0], %[nwo]\ns_cbranch_scc1 .LXa%=\ns_sleep 1\n"
        "s_sub_u32 %[s1], %[s1], 1\ns_cbranch_scc0 .LWa%=\n.LXa%=:\nds_read_b128 v[68:71], %[bb] offset:0\nds_read_b128 v[72:75], %[bb] offset:1024\nds_read_b128 v[76:79], %[bb] offset:2048\n"
        "ds_read_b128 v[80:83], %[bb] offset:3072\nds_read_b128 v[84:87], %[bb] offset:4096\nds_read_b128 v[88:91], %[bb] offset:5120\nds_read_b128 v[92:95], %[bb] offset:6144\n"
        "ds_read_b128 v[96:99], %[bb] offset:7168\nds_read_b128 v[100:103], %[bb] offset:8192\nds_read_b128 v[104:107], %[bb] offset:9216\nds_read_b128 v[108:111], %[bb] offset:10240\n"
        "ds_read_b128 v[112:115], %[bb] offset:11264\nds_read_b128 v[116:119], %[bb] offset:12288\nds_read_b128 v[120:123], %[bb] offset:13312\nds_read_b128 v[124:127], %[bb] offset:14336\n"
        "ds_read_b128 v[128:131], %[bb] offset:15360\nds_read_b128 v[132:135], %[bb] offset:16384\nds_read_b128 v[136:139], %[bb] offset:17408\nds_read_b128 v[140:143], %[bb] offset:18432\n"
        "ds_read_b128 v[144:147], %[bb] offset:19456\nds_read_b128 v[148:151], %[bb] offset:20480\nds_read_b128 v[152:155], %[bb] offset:21504\nds_read_b128 v[156:159], %[bb] offset:22528\n"
        "ds_read_b128 v[160:163], %[bb] offset:23552\nds_read_b128 v[164:167], %[bb] offset:24576\ns_mov_b64 exec, 1\nds_add_u32 %[sy], %[one] offset:%[ofr]\ns_mov_b64 exec, -1\n"
        "v_add_u32 %[bb], %[wo1], %[bb]\nv_lshlrev_b32 v48, 16, v22\nv_and_b32 v49, 0xffff0000, v22\nv_lshlrev_b32 v50, 16, v23\nv_and_b32 v51, 0xffff0000, v23\nv_lshlrev_b32 v52, 16, v24\n"
        "v_and_b32 v53, 0xffff0000, v24\nv_lshlrev_b32 v54, 16, v25\nv_and_b32 v55, 0xffff0000, v25\nv_lshlrev_b32 v56, 16, v26\nv_and_b32 v57, 0xffff0000, v26\nv_lshlrev_b32 v58, 16, v27\n"
        "v_and_b32 v59, 0xffff0000, v27\ns_cmp_eq_u32 %[kh], 0\ns_cbranch_scc0 .LS1_%=\ns_sleep %[sl0]\nbuffer_load_dwordx4 v[28:31], %[l16], %[ors], %[o0] offen offset:0 sc1\n"
        "buffer_load_dwordx4 v[32:35], %[l16], %[ors], %[o1] offen offset:0 sc1\nbuffer_load_dwordx4 v[36:39], %[l16], %[ors], %[o2] offen offset:0 sc1\n"
        "buffer_load_dwordx4 v[40:43], %[l16], %[ors], %[o3] offen offset:0 sc1\nbuffer_load_dwordx4 v[44:47], %[oha], %[ors], %[o4] offen offset:0 sc1\n.LS1_%=:\ns_mov_b32 %[s1], 0x100000\n.LWb%=:\n"
        "ds_read_b32 %[tm], %[sy] offset:%[ow1]\ns_waitcnt lgkmcnt(0)\nv_readfirstlane_b32 %[s0], %[tm]\ns_nop 0\ns_cmp_ge_i32 %[s0], %[nwo]\ns_cbranch_scc1 .LXb%=\ns_sleep 1\n"
        "s_sub_u32 %[s1], %[s1], 1\ns_cbranch_scc0 .LWb%=\n.LXb%=:\n.LC0_%=:\ns_waitcnt vmcnt(4)\nv_max_u32 %[tm], v28, v29\nv_max3_u32 %[tm], %[tm], v30, v31\ns_waitcnt vmcnt(3)\n"
        "v_max3_u32 %[tm], %[tm], v32, v33\nv_max3_u32 %[tm], %[tm], v34, v35\ns_waitcnt vmcnt(2)\nv_max3_u32 %[tm], %[tm], v36, v37\nv_max3_u32 %[tm], %[tm], v38, v39\ns_waitcnt vmcnt(1)\n"
        "v_max3_u32 %[tm], %[tm], v40, v41\nv_max3_u32 %[tm], %[tm], v42, v43\ns_waitcnt vmcnt(0)\nv_max3_u32 %[tm], %[tm], v44, v45\nv_max3_u32 %[tm], %[tm], v46, v47\nv_cmp_eq_u32 vcc, -1, %[tm]\n"
        "s_cbranch_vccnz .LR0_%=\n.LK0_%=:\nv_mfma_f32_16x16x32_bf16 v[68:71], v[28:31], v[68:71], 0\nv_mfma_f32_16x16x32_bf16 v[72:75], v[28:31], v[72:75], 0\n"
        "v_mfma_f32_16x16x32_bf16 v[76:79], v[28:31], v[76:79], v[48:51]\nv_mfma_f32_16x16x32_bf16 v[80:83], v[28:31], v[80:83], v[52:55]\n"
        "v_mfma_f32_16x16x32_bf16 v[84:87], v[28:31], v[84:87], v[56:59]\nbuffer_load_dwordx4 v[48:51], %[l16], %[ors], %[o0] offen offset:1024 sc1\n"
        "buffer_load_dwordx4 v[52:55], %[l16], %[ors], %[o1] offen offset:1024 sc1\nbuffer_load_dwordx4 v[56:59], %[l16], %[ors], %[o2] offen offset:1024 sc1\n"
        "buffer_load_dwordx4 v[60:63], %[l16], %[ors], %[o3] offen offset:1024 sc1\nbuffer_load_dwordx4 v[64:67], %[oha], %[ors], %[o4] offen offset:256 sc1\n"
        "v_mfma_f32_16x16x32_bf16 v[68:71], v[32:35], v[88:91], v[68:71]\nds_read_b128 v[88:91], %[bb] offset:0\nv_mfma_f32_16x16x32_bf16 v[72:75], v[32:35], v[92:95], v[72:75]\n"
        "ds_read_b128 v[92:95], %[bb] offset:5120\nv_mfma_f32_16x16x32_bf16 v[76:79], v[32:35], v[96:99], v[76:79]\nds_read_b128 v[96:99], %[bb] offset:10240\n"
        "v_mfma_f32_16x16x32_bf16 v[80:83], v[32:35], v[100:103], v[80:83]\nds_read_b128 v[100:103], %[bb] offset:15360\nv_mfma_f32_16x16x32_bf16 v[84:87], v[32:35], v[104:107], v[84:87]\n"
        "ds_read_b128 v[104:107], %[bb] offset:20480\nv_mfma_f32_16x16x32_bf16 v[68:71], v[36:39], v[108:111], v[68:71]\nds_read_b128 v[108:111], %[bb] offset:1024\n"
        "v_mfma_f32_16x16x32_bf16 v[72:75], v[36:39], v[112:115], v[72:75]\nds_read_b128 v[112:115], %[bb] offset:6144\nv_mfma_f32_16x16x32_bf16 v[76:79], v[36:39], v[116:119], v[76:79]\n"
        "ds_read_b128 v[116:119], %[bb] offset:11264\nv_mfma_f32_16x16x32_bf16 v[80:83], v[36:39], v[120:123], v[80:83]\nds_read_b128 v[120:123], %[bb] offset:16384\n"
        "v_mfma_f32_16x16x32_bf16 v[84:87], v[36:39], v[124:127], v[84:87]\nds_read_b128 v[124:127], %[bb] offset:21504\nv_mfma_f32_16x16x32_bf16 v[68:71], v[40:43], v[128:131], v[68:71]\n"
        "ds_read_b128 v[128:131], %[bb] offset:2048\nv_mfma_f32_16x16x32_bf16 v[72:75], v[40:43], v[132:135], v[72:75]\nds_read_b128 v[132:135], %[bb] offset:7168\n"
        "v_mfma_f32_16x16x32_bf16 v[76:79], v[40:43], v[136:139], v[76:79]\nds_read_b128 v[136:139], %[bb] offset:12288\nv_mfma_f32_16x16x32_bf16 v[80:83], v[40:43], v[140:143], v[80:83]\n"
        "ds_read_b128 v[140:143], %[bb] offset:17408\nv_mfma_f32_16x16x32_bf16 v[84:87], v[40:43], v[144:147], v[84:87]\nds_read_b128 v[144:147], %[bb] offset:22528\n"
        "v_mfma_f32_16x16x32_bf16 v[68:71], v[44:47], v[148:151], v[68:71]\nds_read_b128 v[148:151], %[bb] offset:3072\nv_mfma_f32_16x16x32_bf16 v[72:75], v[44:47], v[152:155], v[72:75]\n"
        "ds_read_b128 v[152:155], %[bb] offset:8192\nv_mfma_f32_16x16x32_bf16 v[76:79], v[44:47], v[156:159], v[76:79]\nds_read_b128 v[156:159], %[bb] offset:13312\n"
        "v_mfma_f32_16x16x32_bf16 v[80:83], v[44:47], v[160:163], v[80:83]\nds_read_b128 v[160:163], %[bb] offset:18432\nv_mfma_f32_16x16x32_bf16 v[84:87], v[44:47], v[164:167], v[84:87]\n"
        "ds_read_b128 v[164:167], %[bb] offset:23552\nds_read_b128 v[28:31], %[bb] offset:4096\nds_read_b128 v[32:35], %[bb] offset:9216\nds_read_b128 v[36:39], %[bb] offset:14336\n"
        "ds_read_b128 v[40:43], %[bb] offset:19456\nds_read_b128 v[44:47], %[bb] offset:24576\ns_mov_b32 %[s1], 0x100000\n.LWc%=:\nds_read_b32 %[tm], %[sy] offset:%[ofr]\ns_waitcnt lgkmcnt(0)\n"
        "v_readfirstlane_b32 %[s0], %[tm]\ns_nop 0\ns_cmp_ge_i32 %[s0], 8\ns_cbranch_scc1 .LXc%=\ns_sleep 1\ns_sub_u32 %[s1], %[s1], 1\ns_cbranch_scc0 .LWc%=\n.LXc%=:\n.LC1_%=:\ns_waitcnt vmcnt(4)\n"
        "v_max_u32 %[tm], v48, v49\nv_max3_u32 %[tm], %[tm], v50, v51\ns_waitcnt vmcnt(3)\nv_max3_u32 %[tm], %[tm], v52, v53\nv_max3_u32 %[tm], %[tm], v54, v55\ns_waitcnt vmcnt(2)\n"
        "v_max3_u32 %[tm], %[tm], v56, v57\nv_max3_u32 %[tm], %[tm], v58, v59\ns_waitcnt vmcnt(1)\nv_max3_u32 %[tm], %[tm], v60, v61\nv_max3_u32 %[tm], %[tm], v62, v63\ns_waitcnt vmcnt(0)\n"
        "v_max3_u32 %[tm], %[tm], v64, v65\nv_max3_u32 %[tm], %[tm], v66, v67\nv_cmp_eq_u32 vcc, -1, %[tm]\ns_cbranch_vccnz .LR1_%=\n.LK1_%=:\n"
        "v_mfma_f32_16x16x32_bf16 v[68:71], v[48:51], v[88:91], v[68:71]\nv_mfma_f32_16x16x32_bf16 v[68:71], v[52:55], v[92:95], v[68:71]\n"
        "v_mfma_f32_16x16x32_bf16 v[68:71], v[56:59], v[96:99], v[68:71]\nv_mfma_f32_16x16x32_bf16 v[68:71], v[60:63], v[100:103], v[68:71]\n"
        "v_mfma_f32_16x16x32_bf16 v[68:71], v[64:67], v[104:107], v[68:71]\nv_mfma_f32_16x16x32_bf16 v[72:75], v[48:51], v[108:111], v[72:75]\n"
        "v_mfma_f32_16x16x32_bf16 v[72:75], v[52:55], v[112:115], v[72:75]\nv_mfma_f32_16x16x32_bf16 v[72:75], v[56:59], v[116:119], v[72:75]\nds_write_b128 %[rw], v[68:71]\n"
        "v_mfma_f32_16x16x32_bf16 v[72:75], v[60:63], v[120:123], v[72:75]\nv_mfma_f32_16x16x32_bf16 v[72:75], v[64:67], v[124:127], v[72:75]\n"
        "v_mfma_f32_16x16x32_bf16 v[76:79], v[48:51], v[128:131], v[76:79]\nv_mfma_f32_16x16x32_bf16 v[76:79], v[52:55], v[132:135], v[76:79]\n"
        "v_mfma_f32_16x16x32_bf16 v[76:79], v[56:59], v[136:139], v[76:79]\nds_write_b128 %[rw], v[72:75] offset:1024\nv_mfma_f32_16x16x32_bf16 v[76:79], v[60:63], v[140:143], v[76:79]\n"
        "ds_write_b32 %[fw], %[f0]\nv_mfma_f32_16x16x32_bf16 v[76:79], v[64:67], v[144:147], v[76:79]\nv_mfma_f32_16x16x32_bf16 v[80:83], v[48:51], v[148:151], v[80:83]\n"
        "v_mfma_f32_16x16x32_bf16 v[80:83], v[52:55], v[152:155], v[80:83]\nv_mfma_f32_16x16x32_bf16 v[80:83], v[56:59], v[156:159], v[80:83]\nds_write_b128 %[rw], v[76:79] offset:2048\n"
        "v_mfma_f32_16x16x32_bf16 v[80:83], v[60:63], v[160:163], v[80:83]\nds_write_b32 %[fw], %[one]\nv_mfma_f32_16x16x32_bf16 v[80:83], v[64:67], v[164:167], v[80:83]\n"
        "v_mfma_f32_16x16x32_bf16 v[84:87], v[48:51], v[28:31], v[84:87]\nv_mfma_f32_16x16x32_bf16 v[84:87], v[52:55], v[32:35], v[84:87]\n"
        "v_mfma_f32_16x16x32_bf16 v[84:87], v[56:59], v[36:39], v[84:87]\nv_mfma_f32_16x16x32_bf16 v[84:87], v[60:63], v[40:43], v[84:87]\n"
        "v_mfma_f32_16x16x32_bf16 v[84:87], v[64:67], v[44:47], v[84:87]\ns_mov_b32 %[s1], 0x100000\n.LY%=:\nds_read_b32 %[tm], %[fr]\nds_read_b128 v[96:99], %[rr] offset:2048\n"
        "ds_read_b128 v[92:95], %[rr] offset:1024\nds_read_b128 v[88:91], %[rr] offset:0\ns_waitcnt lgkmcnt(3)\nv_readfirstlane_b32 %[s0], %[tm]\ns_waitcnt lgkmcnt(0)\ns_cmp_lg_u32 %[s0], 0\n"
        "s_cbranch_scc1 .LZ%=\ns_sub_u32 %[s1], %[s1], 1\ns_cbranch_scc0 .LY%=\n.LZ%=:\nv_add_f32 v80, v80, v92\nv_add_f32 v84, v84, v96\nv_add_f32 v81, v81, v93\nv_add_f32 v85, v85, v97\n"
        "v_add_f32 v82, v82, v94\nv_add_f32 v86, v86, v98\nv_add_f32 v83, v83, v95\nv_add_f32 v87, v87, v99\nv_cvt_pk_bf16_f32 v92, v80, v81\nv_cvt_pk_bf16_f32 v96, v84, v85\n"
        "v_cvt_pk_bf16_f32 v93, v82, v83\nv_cvt_pk_bf16_f32 v97, v86, v87\nv_min_u32 v92, -2, v92\nv_min_u32 v96, -2, v96\nv_min_u32 v93, -2, v93\nv_min_u32 v97, -2, v97\n"
        "ds_write_b64 %[xo], v[92:93] offset:2304\nds_write_b64 %[xo], v[96:97] offset:4608\ns_cmp_eq_u32 %[kh], 0\nv_add_f32 v100, v80, v84\nv_add_f32 v101, v81, v85\nv_add_f32 v102, v82, v86\n"
        "v_add_f32 v103, v83, v87\nv_mul_f32 v104, v84, v84\nv_mul_f32 v105, v85, v85\nv_mul_f32 v106, v86, v86\nv_mul_f32 v107, v87, v87\nv_fmac_f32 v104, v80, v80\nv_fmac_f32 v105, v81, v81\n"
        "v_fmac_f32 v106, v82, v82\nv_fmac_f32 v107, v83, v83\ns_cbranch_scc0 .LV%=\nv_add_f32 v76, v76, v88\nv_add_f32 v77, v77, v89\nv_add_f32 v78, v78, v90\nv_add_f32 v79, v79, v91\n"
        "v_cvt_pk_bf16_f32 v88, v76, v77\nv_cvt_pk_bf16_f32 v89, v78, v79\nv_min_u32 v88, -2, v88\nv_min_u32 v89, -2, v89\nds_write_b64 %[xo], v[88:89] offset:0\nv_add_f32 v100, v100, v76\n"
        "v_add_f32 v101, v101, v77\nv_add_f32 v102, v102, v78\nv_add_f32 v103, v103, v79\nv_fmac_f32 v104, v76, v76\nv_fmac_f32 v105, v77, v77\nv_fmac_f32 v106, v78, v78\nv_fmac_f32 v107, v79, v79\n"
        ".LV%=:\ns_mov_b64 exec, 1\nds_add_u32 %[sy], %[one] offset:%[ox1]\ns_mov_b64 exec, -1\nv_add_f32_dpp v100, v100, v100 row_ror:8 row_mask:0xf bank_mask:0x3\n"
        "v_add_f32_dpp v101, v101, v101 row_ror:8 row_mask:0xf bank_mask:0x3\nv_add_f32_dpp v104, v104, v104 row_ror:8 row_mask:0xf bank_mask:0x3\n"
        "v_add_f32_dpp v105, v105, v105 row_ror:8 row_mask:0xf bank_mask:0x3\nv_add_f32_dpp v100, v102, v102 row_ror:8 row_mask:0xf bank_mask:0xc\n"
        "v_add_f32_dpp v101, v103, v103 row_ror:8 row_mask:0xf bank_mask:0xc\nv_add_f32_dpp v104, v106, v106 row_ror:8 row_mask:0xf bank_mask:0xc\n"
        "v_add_f32_dpp v105, v107, v107 row_ror:8 row_mask:0xf bank_mask:0xc\nv_add_f32_dpp v102, v100, v100 row_half_mirror row_mask:0xf bank_mask:0x5\n"
        "v_add_f32_dpp v103, v104, v104 row_half_mirror row_mask:0xf bank_mask:0x5\ns_nop 0\nv_add_f32_dpp v102, v101, v101 row_half_mirror row_mask:0xf bank_mask:0xa\n"
        "v_add_f32_dpp v103, v105, v105 row_half_mirror row_mask:0xf bank_mask:0xa\ns_nop 0\nv_add_f32_dpp v102, v102, v102 quad_perm:[1,0,3,2] row_mask:0xf bank_mask:0xf\n"
        "v_add_f32_dpp v103, v103, v103 quad_perm:[1,0,3,2] row_mask:0xf bank_mask:0xf\ns_nop 0\nv_add_f32_dpp v102, v102, v102 quad_perm:[2,3,0,1] row_mask:0xf bank_mask:0xf\n"
        "v_add_f32_dpp v103, v103, v103 quad_perm:[2,3,0,1] row_mask:0xf bank_mask:0xf\nv_min_u32 v102, -2, v102\nv_min_u32 v103, -2, v103\n"
        "buffer_store_dwordx2 v[102:103], %[sv], %[srs], %[ss] offen sc1\ns_branch .LE%=\n.LR0_%=:\ns_sub_u32 %[cap], %[cap], 1\ns_cbranch_scc0 .LQ0_%=\ns_mov_b32 %[cap], 0\ns_branch .LK0_%=\n"
        ".LQ0_%=:\ns_sleep 2\nbuffer_load_dwordx4 v[28:31], %[l16], %[ors], %[o0] offen offset:0 sc1\nbuffer_load_dwordx4 v[32:35], %[l16], %[ors], %[o1] offen offset:0 sc1\n"
        "buffer_load_dwordx4 v[36:39], %[l16], %[ors], %[o2] offen offset:0 sc1\nbuffer_load_dwordx4 v[40:43], %[l16], %[ors], %[o3] offen offset:0 sc1\n"
        "buffer_load_dwordx4 v[44:47], %[oha], %[ors], %[o4] offen offset:0 sc1\ns_branch .LC0_%=\n.LR1_%=:\ns_sub_u32 %[cap], %[cap], 1\ns_cbranch_scc0 .LQ1_%=\ns_mov_b32 %[cap], 0\ns_branch .LK1_%=\n"
        ".LQ1_%=:\ns_sleep 2\nbuffer_load_dwordx4 v[48:51], %[l16], %[ors], %[o0] offen offset:1024 sc1\nbuffer_load_dwordx4 v[52:55], %[l16], %[ors], %[o1] offen offset:1024 sc1\n"
        "buffer_load_dwordx4 v[56:59], %[l16], %[ors], %[o2] offen offset:1024 sc1\nbuffer_load_dwordx4 v[60:63], %[l16], %[ors], %[o3] offen offset:1024 sc1\n"
        "buffer_load_dwordx4 v[64:67], %[oha], %[ors], %[o4] offen offset:256 sc1\ns_branch .LC1_%=\n.LE%=:\n"
        : [tm] "=&v"(tm), [bb] "+{v19}"(bb), [cap] "=&s"(cap), [s0] "=&s"(s0_), [s1] "=&s"(s1_), "+{v20}"(xi_), "+{v21}"(xg_)
        : "{v[22:23]}"(rv[0]), "{v[24:25]}"(rv[1]), "{v[26:27]}"(rv[2]), [l16] "{v8}"(l16), [oha] "{v9}"(oha), [ors] "s"(orsrc), [o0] "s"(ob + 0), [o1] "s"(ob + 10240), [o2] "s"(ob + 20480), [o3] "s"(ob + 30720), [o4] "s"(ob + 2048), [sy] "{v10}"(sya), [one] "{v11}"(one), [rw] "{v12}"(rw), [fw] "{v13}"(fw), [f0] "{v14}"(f0), [rr] "{v15}"(rr), [fr] "{v18}"(fr), [xo] "{v16}"(xo_), [sv] "{v17}"(so_), [srs] "s"(srsrc), [ss] "s"(mt2 * 128 + (2 * hg + kh) * 512), [kh] "s"(kh), [wo1] "s"(WO1_OFF), [ofr] "i"(SY_FREE), [ox1] "i"(SY_X1), [ow1] "i"(SY_WO + 4), [nwo] "i"(8), [sl0] "i"(OSLEEP), [sl1] "i"(OSLEEP1)
        : "memory", "scc", "vcc", "v28", "v29", "v30", "v31", "v32", "v33", "v34", "v35", "v36", "v37", "v38", "v39", "v40", "v41", "v42", "v43", "v44", "v45", "v46", "v47", "v48", "v49", "v50", "v51", "v52", "v53", "v54", "v55", "v56", "v57", "v58", "v59", "v60", "v61", "v62", "v63", "v64", "v65", "v66", "v67", "v68", "v69", "v70", "v71", "v72", "v73", "v74", "v75", "v76", "v77", "v78", "v79", "v80", "v81", "v82", "v83", "v84", "v85", "v86", "v87", "v88", "v89", "v90", "v91", "v92", "v93", "v94", "v95", "v96", "v97", "v98", "v99", "v100", "v101", "v102", "v103", "v104", "v105", "v106", "v107", "v108", "v109", "v110", "v111", "v112", "v113", "v114", "v115", "v116", "v117", "v118", "v119", "v120", "v121", "v122", "v123", "v124", "v125", "v126", "v127", "v128", "v129", "v130", "v131", "v132", "v133", "v134", "v135", "v136", "v137", "v138", "v139", "v140", "v141", "v142", "v143", "v144", "v145", "v146", "v147", "v148", "v149", "v150", "v151", "v152", "v153", "v154", "v155", "v156", "v157", "v158", "v159", "v160", "v161", "v162", "v163", "v164", "v165", "v166", "v167");
    RETRY(8 * layer + 1 + grp, 0x10000 - cap);
    RETRY(8 * layer + 3 + grp, rt >> 16);
  }
  lds_inc<SY_FIN>(sya, one);
  lds_poll<SY_X1, 0>(sya, 8);
  {
    typedef __attribute__((address_space(3))) const u32x4* lp4;
    __builtin_amdgcn_raw_buffer_store_b128(*(lp4)(size_t)xi_, x1rs, xg_, 0, 17);
    if (j < 2) __builtin_amdgcn_raw_buffer_store_b128(*(lp4)(size_t)(xi_ + 64 * 144), x1rs, xg_, 64 * XP * 2, 17);
  }
  STY(13);
  {
    LANE(lane);
    const int t = 64 * w + lane;
    ST_FLUSH(kid);
  }
}

#define BAUX 17
#define PC8 (gq == 0)
#define PC4 (gq == 0)
#define UPAD (9 * 144)
#define UF_N (UPAD + 16 * 144)
#define KY0 1058
#define KC_N 1120
#define KZ0 130
#define K8_N 192
#define Z4_P 264
#define CH_EL (UF_N + 4 * KC_N + 2 * K8_N)
#define AUX_B 25600
#define B_CNT (3 * CH_EL * 2 + 3 * AUX_B)

__device__ __forceinline__ void kc_store(u16* kd, const u32* d) {
  u32 sw[5], mx[5];
#pragma unroll
  for (int i = 0; i < 5; ++i) { sw[i] = __builtin_amdgcn_alignbit(d[i], d[i], 16); mx[i] = __builtin_amdgcn_perm(d[i], d[i + 1], 0x07060100); }
  *(uint4*)kd = make_uint4(mx[4], mx[3], mx[2], mx[1]);
  *(uint4*)(kd + KC_N) = make_uint4(sw[4], sw[3], sw[2], sw[1]);
  *(uint4*)(kd + 2 * KC_N) = make_uint4(mx[3], mx[2], mx[1], mx[0]);
  *(uint4*)(kd + 3 * KC_N) = make_uint4(sw[3], sw[2], sw[1], sw[0]);
}
#define DUPLO_(x) __builtin_amdgcn_perm(x, x, 0x01000100)
#define DUPHI_(x) __builtin_amdgcn_perm(x, x, 0x03020302)

#define FSLEEP 18
#define TSLEEP 24
#define OAUX 0
#define FPOLL 4
__device__ __forceinline__ void b_tables(unsigned char* smem, const int ch, const int lane) {
  const u16* kc = (const u16*)(smem + KQ_OFF) + ch * 1536;
  u16* Uf = (u16*)smem + ch * CH_EL;
  u16* KC = Uf + UF_N;
  const u16* p = kc + (lane < 32 ? 8 * lane : 256 + 4 * (lane - 32));
  const uint2 a0 = *(const uint2*)p, a1 = *(const uint2*)(p + 4), a2 = *(const uint2*)(p + 8);
  const u32 b0 = *(const u32*)&kc[384 + 2 * lane], b1 = *(const u32*)&kc[386 + 2 * lane];
  const uint2 eh = *(const uint2*)(kc + (lane < 3 ? 0 : lane == 3 ? 384 : 512));
  const u32 c0 = *(const u32*)&kc[638 - 2 * lane], c1 = *(const u32*)&kc[640 - 2 * lane];
  u32 dh[6];
  {
    const bool r1 = lane < 32;
    dh[0] = r1 ? a0.x : DUPLO_(a0.x);
    dh[1] = r1 ? a0.y : __builtin_amdgcn_perm(a0.y, a0.x, 0x05040302);
    dh[2] = r1 ? a1.x : DUPLO_(a0.y);
    dh[3] = r1 ? a1.y : __builtin_amdgcn_perm(a1.x, a0.y, 0x05040302);
    const u32 nx = r1 ? a2.x : a1.x;
    dh[4] = lane < 31 ? nx : DUPLO_(nx);
    dh[5] = lane < 31 ? a2.y : (lane == 63 ? nx : nx >> 16);
    kc_store(KC + 8 * (131 - lane), dh);
  }
  {
    u16* R = KC + 128;
#pragma unroll
    for (int c = 0; c < 4; ++c) if (lane < 36) *(uint4*)&R[c * KC_N + 8 * lane] = make_uint4(0, 0, 0, 0);
    asm volatile("" ::: "memory");
#pragma unroll
    for (int c = 0; c < 4; ++c) { R[c * (KC_N - 1) + 95 - lane] = (u16)b1; R[c * (KC_N - 1) + 239 - lane] = (u16)(b0 >> 16); }
  }
  {
    u32* K8 = (u32*)(KC + 4 * KC_N);
    const u32 wh = __builtin_amdgcn_perm(c0, c1, 0x07060100);
    K8[lane + 1] = wh;
    K8[K8_N / 2 + lane] = wh;
    K8[lane < 32 ? (lane ? 64 + lane : 0) : 128 + lane] = 0u;
  }
  *(uint4*)&Uf[8 * lane] = make_uint4(0, 0, 0, 0);
  *(uint4*)&Uf[8 * lane + 512] = make_uint4(0, 0, 0, 0);
  *(uint4*)&Uf[8 * lane + UPAD - 512] = make_uint4(0, 0, 0, 0);
  if (lane < 23) {
    const bool e0 = lane == 2, e1 = lane == 3, e2 = lane == 19;
    dh[0] = e1 ? DUPLO_(eh.x) : 0u; dh[1] = e1 ? eh.x & 0xffffu : e2 ? eh.x << 16 : 0u; dh[2] = e2 ? eh.x & 0xffffu : 0u; dh[3] = 0u; dh[4] = e0 ? eh.x : 0u; dh[5] = e0 ? eh.y : 0u;
    kc_store(KC + 8 * (131 - (lane < 3 ? lane - 3 : lane < 11 ? 61 + lane : 109 + lane)), dh);
  }
}
#define FSH 16
#define FS0 14
#define FS1 10
#define BPS 0
#define PBU 1
#define PBU2 1
#define PBUF 1
#define PBL1 2
#define FSLEEP0 FSLEEP
#define PBL 2
#define PBH 3
#define PBE 3
template <bool FULL, int layer>
__device__ __forceinline__ void b_conv(unsigned char* smem, const int b, const int w, const int gq, const int wq, const int cG, const u16* xtin, float2* stats, u32* fstat,
                                       u16* xtout, u32x4* mark
                                       ) {
  constexpr int NB = FULL ? 2 : 1;
  LANE(lane);
  const int t = 64 * w + lane, n = lane & 15, g = lane >> 4, ck = 64 * wq + lane;
  const int v = (wq + gq) & 3;
  const int p = FULL ? (v & 1) : (b & 1);
  const int r0 = FULL ? (v & 2) : v;
  const u32 Uf = LDSA(smem) + gq * (CH_EL * 2);
  const u32 aux = LDSA(smem) + 50944 + gq * AUX_B;
  const u32 sya = LDSA(smem) + SY_OFF + 256 * layer;
  u32 cna = sya + 80 + 16 * gq, one = 1u;
  const int ct = FULL ? v : ((v == 1 || v == 2) ? 1 : 0), q0 = FULL ? 0 : (v == 2 ? 2 : 0);
  u32 ub = Uf + 2 * (UPAD + 144 * (n - r0 - NB) + 8 * g + 16 * p - 32);
  u32 ubx = ub + ((g >= 2 || p) ? 32 : 0);
  const int tx = (FULL ? 256 * ct + 16 * n : 512 * ct + 32 * n + 16 * p) - 8 + 8 * g;
  u32 ux = Uf + 2 * (UPAD + tx + 16 * (tx >> 7));
  const int h = n + g - 3;
  u32 p1 = Uf + 2 * (UPAD + 144 * wq + 8 * h - (h < 0 ? 16 : 0));
  const int ct4 = FULL ? (v < 2 ? 32 * v : 16 * v + 32) : 32 * min(v, 2), ao = FULL ? n : 4 * (n >> 1) + 2 * p + (n & 1), zs = 31 - ao + 8 * (g & 1);
  const u32 Zb = Uf + 2 * UF_N;
  const int zro = 4 * (n & 3) + ((n >> 1) & 2);
  u32 zw = Zb + 2 * ((zro + (n >> 3)) * Z4_P + 16 * wq + 4 * g);
  u32 zr = Zb + 2 * ((zro + (g >> 1)) * Z4_P + 96 * (n >> 3) + ct4 + 8 * (g & 1));
  u32 k4a = Zb + 2 * ((zs & 3) * KC_N + 128 + 144 * (g >> 1) + (zs & ~3));
  u32 mw = Zb + 3072 * (g & 1) + 16 * (FULL ? 96 * (g >> 1) + ct4 + n : 48 * (g >> 1) + ct4 / 2 + n);
  const u32 xb = aux + 16 * (17 * n + (n & 8));
  u32 xd = xb + 128 * (g >> 1) + 16 * wq + 8 * (g & 1), xr = xb + 128 * (g & 1) + 32 * q0 + 16 * (g >> 1);
  u32 uw_ = Uf + 2 * (UPAD + 8 * ck + 16 * (ck >> 4));
  const int eck = FULL ? ck : (4 * (ck >> 1) + 2 * p + (ck & 1)) & 255;
  u32 ue = Uf + 2 * (UPAD + 8 * eck + 16 * (eck >> 4));
  u32 exw = aux + 9216 + (FULL ? (v >> 1) * 8192 + (g & 1) * 4096 + n * 256 + (((2 * p + (g >> 1)) ^ (n & 7)) << 4) : v * 4096 + (g & 1) * 2048 + n * 128 + (((g >> 1) ^ (n & 7)) << 4));
  u32 exr = aux + 9216 + (FULL ? (ck ^ ((ck >> 4) & 7)) : ((ck & 127) ^ ((ck >> 3) & 7))) * 16;
  u32 frw = aux + 4608 + (FULL ? (g & 1) * 2304 + (g >> 1) * 1152 : (v >= 2 ? 2304 : 0) + (g & 1) * 1152 + (g >> 1) * 640) + ct * 256 + n * 16;
  u32 frr = aux + 4608 + (FULL ? (ck & 1) * 1152 + (ck & 126) * 8 : (ck & 1) * 640 + (ck & 62) * 8);
  u32 mr_ = Zb + 16 * (FULL ? (ck >= 64 ? ck - 64 : ck) : ((ck & 127) >= 32 ? (ck & 127) - 32 : ck & 127));
  float fm4 = eck >= 64 ? 1.f : 0.f;
  int od, oh;
  if (layer) { od = (((eck >> 3) * 640 + cG) * 64 + 8 * (eck & 7)) * 2; oh = -1; }
  else {
    const int dst = (eck >> 3) * 51200 + (cG >> 5) * 2560;
    const int pr = 4 * ((cG >> 3) & 3) + (cG & 3) + 4 * (cG & 4);
    od = (dst + pr * 64 + 8 * ((eck & 7) ^ (pr & 6))) * 2;
    oh = ((eck & 7) >= 6 && eck < 248) ? (dst + 51200 + 2048 + pr * 16 + 8 * (eck & 1)) * 2 : -1;
  }
  const int y0 = KY0 - 16 - n + 8 * g - 128 * r0;
  u32 kpa = Uf + 2 * (UF_N + (y0 & 3) * KC_N + (y0 & ~3)) - 256 * NB, kxa = !FULL && v == 2 ? Uf : kpa + 256 * (NB + r0) - 2032;
  const int z0 = KZ0 - 2 - (FULL ? 2 * n : 4 * n + 2 * p) + 8 * g - (FULL ? 32 : 64) * ct + 32 * q0;
  u32 k8a = Uf + 2 * (UF_N + 4 * KC_N + ((z0 >> 1) & 1) * K8_N + (z0 & ~3));
  u32 bna = LDSA(smem) + BND_OFF + 16 * lane, kfa = LDSA(smem) + KQ_OFF + gq * 3072 + 1536;
  int c16 = ck * 16;
  const __amdgpu_buffer_rsrc_t xrs = __builtin_amdgcn_make_buffer_rsrc((void*)(xtin + (size_t)cG * XP), 0, XP * 2, 0x00020000);
  const __amdgpu_buffer_rsrc_t frs = __builtin_amdgcn_make_buffer_rsrc((void*)(fstat + (b & 7) * 2048), 0, 8192, 0x00020000);
  const __amdgpu_buffer_rsrc_t ors = __builtin_amdgcn_make_buffer_rsrc(xtout, 0, 32 * 102400, 0x00020000);
  PIN(ub); PIN(ubx); PIN(p1); PIN(xd); PIN(xr); PIN(uw_); PIN(exw); PIN(exr); PIN(frw); PIN(frr); PIN(od);
  PIN(ux); PIN(zw); PIN(zr); PIN(mw); PIN(mr_); PIN(k4a); PIN(kxa);
  int xo = eck * 16;
  if (!FULL) { PIN(ue); PIN(fm4); PIN(xo); }
  if (!layer) PIN(oh);
  PIN(cna); PIN(one); PIN(kpa); PIN(k8a); PIN(bna); PIN(kfa); PIN(c16);
  ST(6);
  if (!FULL && wq == 3) {
    const int xcd = b & 7, idx = b >> 3;
    const int s0 = 64 * (8 * (xcd >> 1) + (idx & 7)), hg = 4 * (xcd & 1) + (idx >> 3);
    const __amdgpu_buffer_rsrc_t srsrc = __builtin_amdgcn_make_buffer_rsrc(stats + (size_t)s0 * 16, 0, 8192, 0x00020000);
    const __amdgpu_buffer_rsrc_t wrs = __builtin_amdgcn_make_buffer_rsrc(fstat + hg * 2048 + s0 / 2, 0, 4096 + 128, 0x00020000);
    int so = lane * 16;
    int wo_ = (lane >> 5) * 4096 + (lane & 31) * 4;
    PIN(so); PIN(wo_);
    lds_poll<SY_FIN, 2>(sya, 8);
    __builtin_amdgcn_s_sleep(SSLEEP);
    f32x4 sm;
    int spins = 0;
    _Pragma("unroll 1")
    while (true) {
      asm volatile("" ::: "memory");
      u32x4 pv[8];
#pragma unroll
      for (int i = 0; i < 8; ++i) pv[i] = __builtin_amdgcn_raw_buffer_load_b128(srsrc, so, i * 1024, 16);
      u32 m = 0;
      sm = __builtin_bit_cast(f32x4, pv[0]);
#pragma unroll
      for (int i = 0; i < 8; ++i) {
        m = umax3(umax3(m, pv[i][0], pv[i][1]), pv[i][2], pv[i][3]);
        if (i) sm += __builtin_bit_cast(f32x4, pv[i]);
      }
      if (__builtin_amdgcn_ballot_w64(m == 0xffffffffu) == 0 || ++spins > GCAP) break;
      __builtin_amdgcn_s_sleep(4);
    }
    RETRY(8 * layer + 5, spins);
    ST(7);
    const auto q0_ = __builtin_amdgcn_permlane32_swap(__float_as_uint(sm[0]), __float_as_uint(sm[2]), false, false);
    const auto q1_ = __builtin_amdgcn_permlane32_swap(__float_as_uint(sm[1]), __float_as_uint(sm[3]), false, false);
    const float mean = (__uint_as_float(q0_[0]) + __uint_as_float(q0_[1])) * (1.0f / 640.0f);
    const float var = fmaxf(fmaf(-mean, mean, (__uint_as_float(q1_[0]) + __uint_as_float(q1_[1])) * (1.0f / 640.0f)), 0.f);
    const u32 d = pack2(__builtin_amdgcn_sqrtf(rbf(rbf(var) + 1e-5f)) * (1.0f / 256.0f), 0.f), mb = __float_as_uint(mean);
    const u32 o = ((max((mb & 0x7fffffffu) + 0x8000u, d) - d) & 0xffff0000u) | d | (mb & 0x80000000u);
    __builtin_amdgcn_raw_buffer_store_b32(min(o, 0xfffffffeu), wrs, wo_, 0, 17);
    lds_inc<SY_FS>(sya, one);
    ST(8);
    __builtin_amdgcn_s_setprio(PBU);
  }
  f32x2 kd;
  lds_poll<12, 1>(cna, 1);
  u32x4 kt[18];
  if (FULL) {
      asm volatile(
        "ds_read_b64 v[132:133], %[kpa] offset:512\nds_read_b64 v[134:135], %[kpa] offset:520\nds_read_b64 v[136:137], %[kpa] offset:448\nds_read_b64 v[138:139], %[kpa] offset:456\n"
        "ds_read_b64 v[140:141], %[kpa] offset:384\nds_read_b64 v[142:143], %[kpa] offset:392\nds_read_b64 v[144:145], %[kpa] offset:320\nds_read_b64 v[146:147], %[kpa] offset:328\n"
        "ds_read_b64 v[148:149], %[kpa] offset:256\nds_read_b64 v[150:151], %[kpa] offset:264\nds_read_b64 v[152:153], %[kpa] offset:192\nds_read_b64 v[154:155], %[kpa] offset:200\n"
        "ds_read_b64 v[156:157], %[kpa] offset:128\nds_read_b64 v[158:159], %[kpa] offset:136\nds_read_b64 v[160:161], %[kpa] offset:64\nds_read_b64 v[162:163], %[kpa] offset:72\n"
        "ds_read_b64 v[164:165], %[kpa] offset:0\nds_read_b64 v[166:167], %[kpa] offset:8\nds_read_b64 v[128:129], %[kxa] offset:0\nds_read_b64 v[130:131], %[kxa] offset:8\n"
        "ds_read_b64 v[108:109], %[k4a] offset:0\nds_read_b64 v[110:111], %[k4a] offset:8\nds_read_b64 v[112:113], %[k4a] offset:32\nds_read_b64 v[114:115], %[k4a] offset:40\n"
        "ds_read_b64 v[116:117], %[k4a] offset:64\nds_read_b64 v[118:119], %[k4a] offset:72\nds_read_b64 v[120:121], %[k4a] offset:96\nds_read_b64 v[122:123], %[k4a] offset:104\n"
        "ds_read_b64 v[124:125], %[k4a] offset:128\nds_read_b64 v[126:127], %[k4a] offset:136\nds_read_b128 v[96:99], %[bna] offset:0\nds_read_b128 v[100:103], %[bna] offset:1024\n"
        "ds_read_b128 v[104:107], %[bna] offset:2048\nds_read_b64 %[kd], %[kfa]\ns_waitcnt lgkmcnt(0)\n"
        : "=&{v[96:99]}"(kt[0]), "=&{v[100:103]}"(kt[1]), "=&{v[104:107]}"(kt[2]), "=&{v[108:111]}"(kt[3]), "=&{v[112:115]}"(kt[4]), "=&{v[116:119]}"(kt[5]), "=&{v[120:123]}"(kt[6]), "=&{v[124:127]}"(kt[7]), "=&{v[128:131]}"(kt[8]), "=&{v[132:135]}"(kt[9]), "=&{v[136:139]}"(kt[10]), "=&{v[140:143]}"(kt[11]), "=&{v[144:147]}"(kt[12]), "=&{v[148:151]}"(kt[13]), "=&{v[152:155]}"(kt[14]), "=&{v[156:159]}"(kt[15]), "=&{v[160:163]}"(kt[16]), "=&{v[164:167]}"(kt[17]), [kd] "=&v"(kd)
        : [kpa] "v"(kpa), [kxa] "v"(kxa), [k4a] "v"(k4a), [bna] "v"(bna), [kfa] "v"(kfa)
        : "memory", "scc", "vcc");
  } else {
      asm volatile(
        "ds_read_b64 v[132:133], %[kpa] offset:256\nds_read_b64 v[134:135], %[kpa] offset:264\nds_read_b64 v[136:137], %[kpa] offset:192\nds_read_b64 v[138:139], %[kpa] offset:200\n"
        "ds_read_b64 v[140:141], %[kpa] offset:128\nds_read_b64 v[142:143], %[kpa] offset:136\nds_read_b64 v[144:145], %[kpa] offset:64\nds_read_b64 v[146:147], %[kpa] offset:72\n"
        "ds_read_b64 v[148:149], %[kpa] offset:0\nds_read_b64 v[150:151], %[kpa] offset:8\nds_read_b64 v[128:129], %[kxa] offset:0\nds_read_b64 v[130:131], %[kxa] offset:8\n"
        "ds_read_b64 v[104:105], %[k4a] offset:0\nds_read_b64 v[106:107], %[k4a] offset:8\nds_read_b64 v[108:109], %[k4a] offset:32\nds_read_b64 v[110:111], %[k4a] offset:40\n"
        "ds_read_b64 v[112:113], %[k4a] offset:64\nds_read_b64 v[114:115], %[k4a] offset:72\nds_read_b64 v[116:117], %[k4a] offset:96\nds_read_b64 v[118:119], %[k4a] offset:104\n"
        "ds_read_b64 v[120:121], %[k4a] offset:128\nds_read_b64 v[122:123], %[k4a] offset:136\nds_read_b64 v[124:125], %[k4a] offset:160\nds_read_b64 v[126:127], %[k4a] offset:168\n"
        "ds_read_b128 v[92:95], %[bna] offset:0\nds_read_b128 v[96:99], %[bna] offset:1024\nds_read_b128 v[100:103], %[bna] offset:2048\nds_read_b64 %[kd], %[kfa]\ns_waitcnt lgkmcnt(0)\n"
        : "=&{v[92:95]}"(kt[0]), "=&{v[96:99]}"(kt[1]), "=&{v[100:103]}"(kt[2]), "=&{v[104:107]}"(kt[3]), "=&{v[108:111]}"(kt[4]), "=&{v[112:115]}"(kt[5]), "=&{v[116:119]}"(kt[6]), "=&{v[120:123]}"(kt[7]), "=&{v[124:127]}"(kt[8]), "=&{v[128:131]}"(kt[9]), "=&{v[132:135]}"(kt[10]), "=&{v[136:139]}"(kt[11]), "=&{v[140:143]}"(kt[12]), "=&{v[144:147]}"(kt[13]), "=&{v[148:151]}"(kt[14]), [kd] "=&v"(kd)
        : [kpa] "v"(kpa), [kxa] "v"(kxa), [k4a] "v"(k4a), [bna] "v"(bna), [kfa] "v"(kfa)
        : "memory", "scc", "vcc");
  }
  lds_inc<12>(cna, one);
  lds_poll<12, 2>(cna, 5);
  asm volatile("ds_write_b64 %0, %1" :: "v"(zw), "v"((u32x2){0u, 0u}) : "memory");
  ST(1);
  lds_poll<SY_FS, 2>(sya, 1);
  u32x4 x4, f0, f1;
  {
    int spins = 0;
    __builtin_amdgcn_s_setprio(FULL ? PBUF : PBU2);
    if (!FULL) __builtin_amdgcn_s_sleep(FSH); else if (!gq) __builtin_amdgcn_s_sleep(FS0); else __builtin_amdgcn_s_sleep(FS1);
    _Pragma("unroll 1")
    while (true) {
      asm volatile("" ::: "memory");
      f0 = __builtin_amdgcn_raw_buffer_load_b128(frs, c16, 0, 16);
      f1 = __builtin_amdgcn_raw_buffer_load_b128(frs, c16, 4096, 16);
      x4 = __builtin_amdgcn_raw_buffer_load_b128(xrs, c16, 0, 16);
      const u32 m = umax3(umax3(umax3(f0[0], f0[1], f0[2]), f0[3], f1[0]), umax3(umax3(f1[1], f1[2], f1[3]), x4[0], x4[1]), max(x4[2], x4[3]));
      if (__builtin_amdgcn_ballot_w64(m == 0xffffffffu) == 0 || ++spins > GCAP) break;
      __builtin_amdgcn_s_sleep(FPOLL);
    }
    RETRY(8 * layer + 6, spins);
  }
  ST(2);
  float bs[8];
  {
    float uf[8];
#pragma unroll
    for (int e = 0; e < 8; ++e) {
      const u32 f = (e & 1) ? f1[e >> 1] : f0[e >> 1];
      bs[e] = (e & 1) ? hi_f(x4[e >> 1]) : lo_f(x4[e >> 1]);
      uf[e] = (bs[e] - __uint_as_float(f)) * __builtin_amdgcn_rcpf(lo_f(f));
    }
    const u32x4 uv = {packh(uf[0], uf[1]), packh(uf[2], uf[3]), packh(uf[4], uf[5]), packh(uf[6], uf[7])};
    asm volatile("ds_write_b128 %0, %1" :: "v"(uw_), "v"(uv) : "memory");
    lds_inc<0>(cna, one);
      if (!layer) {
        const __amdgpu_buffer_rsrc_t mrs = __builtin_amdgcn_make_buffer_rsrc(mark + b * 2032, 0, 2032 * 16, 0x00020000);
#pragma unroll
        for (int i = 0; i < 3; ++i) __builtin_amdgcn_raw_buffer_store_b128(MARK16, mrs, t * 16, i * 12288, 17);
      }
    if (FULL) {
#pragma unroll
      for (int e = 0; e < 8; ++e) { bs[e] = fmaf(uf[e], kd[1], bs[e]); PIN(bs[e]); }
    }
  }
  if (FULL && PBL != PBL1 && gq) __builtin_amdgcn_s_setprio(PBL1); else __builtin_amdgcn_s_setprio(FULL ? PBL : PBH);
  lds_poll<0, BPS>(cna, 4);
  ST(3);
  {
    u32 pv; int s0_, s1_;
    f32x4 bs0 = {bs[0], bs[1], bs[2], bs[3]}, bs1 = {bs[4], bs[5], bs[6], bs[7]};
    if (FULL && layer) {
      asm volatile(
        "ds_read_b128 v[52:55], %[p1] offset:0\nds_read_b128 v[56:59], %[p1] offset:1152\nds_read_b128 v[60:63], %[p1] offset:2304\nds_read_b128 v[72:75], %[ub] offset:800\n"
        "ds_read_b128 v[76:79], %[ub] offset:736\nds_read_b128 v[80:83], %[ub] offset:672\nds_read_b128 v[84:87], %[ubx] offset:576\nds_read_b128 v[88:91], %[ub] offset:512\n"
        "ds_read_b128 v[92:95], %[ub] offset:448\nv_xor_b32 v14, 64, %[exw]\ns_waitcnt lgkmcnt(8)\nv_mfma_f32_16x16x32_f16 v[64:67], v[52:55], v[96:99], 0\n"
        "v_mfma_f32_16x16x32_f16 v[68:71], v[52:55], v[100:103], 0\nv_mfma_f32_16x16x32_f16 v[52:55], v[52:55], v[104:107], 0\ns_waitcnt lgkmcnt(7)\n"
        "v_mfma_f32_16x16x32_f16 v[96:99], v[56:59], v[96:99], 0\nv_mfma_f32_16x16x32_f16 v[100:103], v[56:59], v[100:103], 0\nv_mfma_f32_16x16x32_f16 v[56:59], v[56:59], v[104:107], 0\n"
        "v_cvt_pk_f16_f32 v52, v52, v53\nv_cvt_pk_f16_f32 v53, v54, v55\ns_waitcnt lgkmcnt(6)\nv_mfma_f32_16x16x32_f16 v[60:63], v[60:63], v[104:107], 0\nv_cvt_pk_f16_f32 v64, v64, v69\n"
        "v_cvt_pk_f16_f32 v65, v66, v71\ns_waitcnt lgkmcnt(5)\nv_mfma_f32_16x16x32_f16 v[48:51], v[132:135], v[72:75], 0\nds_write_b64 %[zw], v[52:53] offset:128\n"
        "ds_write_b64 %[xd], v[64:65] offset:0\ns_waitcnt lgkmcnt(6)\nv_mfma_f32_16x16x32_f16 v[44:47], v[132:135], v[76:79], 0\nds_read_b128 v[72:75], %[ub] offset:384\n"
        "v_cvt_pk_f16_f32 v56, v56, v57\ns_waitcnt lgkmcnt(6)\nv_mfma_f32_16x16x32_f16 v[40:43], v[132:135], v[80:83], 0\nv_cvt_pk_f16_f32 v57, v58, v59\nv_cvt_pk_f16_f32 v96, v96, v101\n"
        "s_waitcnt lgkmcnt(5)\nv_mfma_f32_16x16x32_f16 v[36:39], v[132:135], v[84:87], 0\nv_cvt_pk_f16_f32 v97, v98, v103\nds_write_b64 %[zw], v[56:57] offset:256\n"
        "v_mfma_f32_16x16x32_f16 v[48:51], v[136:139], v[76:79], v[48:51]\nds_write_b64 %[xd], v[96:97] offset:64\nv_cvt_pk_f16_f32 v60, v60, v61\n"
        "v_mfma_f32_16x16x32_f16 v[44:47], v[136:139], v[80:83], v[44:47]\nv_cvt_pk_f16_f32 v61, v62, v63\nds_write_b64 %[zw], v[60:61] offset:384\n"
        "v_mfma_f32_16x16x32_f16 v[40:43], v[136:139], v[84:87], v[40:43]\ns_mov_b64 exec, 1\nds_add_u32 %[cna], %[one] offset:4\ns_mov_b64 exec, -1\ns_waitcnt lgkmcnt(8)\n"
        "v_mfma_f32_16x16x32_f16 v[36:39], v[136:139], v[88:91], v[36:39]\nds_read_b128 v[76:79], %[ubx] offset:288\nv_mfma_f32_16x16x32_f16 v[48:51], v[140:143], v[80:83], v[48:51]\n"
        "ds_read_b128 v[80:83], %[ub] offset:224\nds_read_b128 v[136:139], %[ux]\nv_mfma_f32_16x16x32_f16 v[44:47], v[140:143], v[84:87], v[44:47]\n"
        "v_mfma_f32_16x16x32_f16 v[40:43], v[140:143], v[88:91], v[40:43]\ns_waitcnt lgkmcnt(10)\nv_mfma_f32_16x16x32_f16 v[36:39], v[140:143], v[92:95], v[36:39]\n"
        "v_mfma_f32_16x16x32_f16 v[48:51], v[144:147], v[84:87], v[48:51]\nds_read_b128 v[84:87], %[ub] offset:160\nv_mfma_f32_16x16x32_f16 v[44:47], v[144:147], v[88:91], v[44:47]\n"
        "v_mfma_f32_16x16x32_f16 v[40:43], v[144:147], v[92:95], v[40:43]\ns_waitcnt lgkmcnt(8)\nv_mfma_f32_16x16x32_f16 v[36:39], v[144:147], v[72:75], v[36:39]\n"
        "v_mfma_f32_16x16x32_f16 v[48:51], v[148:151], v[88:91], v[48:51]\nds_read_b128 v[88:91], %[ub] offset:96\nds_read_b32 %[pv], %[cna] offset:4\n"
        "v_mfma_f32_16x16x32_f16 v[44:47], v[148:151], v[92:95], v[44:47]\nds_read_b64 v[140:141], %[k8a] offset:0\nds_read_b64 v[142:143], %[k8a] offset:8\n"
        "v_mfma_f32_16x16x32_f16 v[40:43], v[148:151], v[72:75], v[40:43]\nds_read_b64 v[144:145], %[k8a] offset:64\nds_read_b64 v[146:147], %[k8a] offset:72\ns_waitcnt lgkmcnt(9)\n"
        "v_mfma_f32_16x16x32_f16 v[36:39], v[148:151], v[76:79], v[36:39]\nv_mfma_f32_16x16x32_f16 v[48:51], v[152:155], v[92:95], v[48:51]\ns_waitcnt lgkmcnt(4)\nv_readfirstlane_b32 %[s0], %[pv]\n"
        "s_nop 0\nv_mfma_f32_16x16x32_f16 v[44:47], v[152:155], v[72:75], v[44:47]\ns_cmp_ge_i32 %[s0], 4\ns_cbranch_scc0 .LG%=\n.LH%=:\n"
        "v_mfma_f32_16x16x32_f16 v[40:43], v[152:155], v[76:79], v[40:43]\nds_read_b128 v[100:103], %[xr] offset:0\nds_read_b128 v[104:107], %[xr] offset:32\n"
        "v_mfma_f32_16x16x32_f16 v[36:39], v[152:155], v[80:83], v[36:39]\nds_read_b128 v[52:55], %[zr] offset:0\nds_read_b128 v[56:59], %[zr] offset:32\n"
        "v_mfma_f32_16x16x32_f16 v[48:51], v[156:159], v[72:75], v[48:51]\nds_read_b128 v[60:63], %[zr] offset:64\nds_read_b128 v[64:67], %[zr] offset:96\n"
        "v_mfma_f32_16x16x32_f16 v[44:47], v[156:159], v[76:79], v[44:47]\nds_read_b128 v[68:71], %[zr] offset:128\nv_mfma_f32_16x16x32_f16 v[40:43], v[156:159], v[80:83], v[40:43]\n"
        "v_mfma_f32_16x16x32_f16 v[36:39], v[156:159], v[84:87], v[36:39]\nv_mfma_f32_16x16x32_f16 v[48:51], v[160:163], v[76:79], v[48:51]\n"
        "v_mfma_f32_16x16x32_f16 v[44:47], v[160:163], v[80:83], v[44:47]\nv_mfma_f32_16x16x32_f16 v[40:43], v[160:163], v[84:87], v[40:43]\n"
        "v_mfma_f32_16x16x32_f16 v[36:39], v[160:163], v[88:91], v[36:39]\ns_cmp_lt_u32 %[v], 2\ns_cbranch_scc0 .LT%=\nds_read_b128 v[96:99], %[zr] offset:160\ns_mov_b64 exec, 1\n"
        "ds_add_u32 %[cna], %[one] offset:12\ns_mov_b64 exec, -1\nv_mfma_f32_16x16x32_f16 v[72:75], v[128:131], v[136:139], 0\ns_waitcnt lgkmcnt(8)\n"
        "v_mfma_f32_16x16x32_f16 v[72:75], v[100:103], v[140:143], v[72:75]\ns_waitcnt lgkmcnt(7)\nv_mfma_f32_16x16x32_f16 v[72:75], v[104:107], v[144:147], v[72:75]\n"
        "ds_write_b128 v14, v[48:51] offset:128\nds_write_b128 %[exw], v[44:47] offset:128\ns_waitcnt lgkmcnt(8)\nv_mfma_f32_16x16x32_f16 v[76:79], v[52:55], v[108:111], 0\n"
        "ds_write_b128 v14, v[40:43] offset:0\nds_write_b128 %[exw], v[36:39] offset:0\ns_waitcnt lgkmcnt(9)\nv_mfma_f32_16x16x32_f16 v[76:79], v[56:59], v[112:115], v[76:79]\n"
        "ds_read_b32 %[pv], %[cna] offset:12\ns_waitcnt lgkmcnt(9)\nv_mfma_f32_16x16x32_f16 v[76:79], v[60:63], v[116:119], v[76:79]\ns_waitcnt lgkmcnt(8)\n"
        "v_mfma_f32_16x16x32_f16 v[76:79], v[64:67], v[120:123], v[76:79]\nds_write_b128 %[frw], v[72:75]\ns_waitcnt lgkmcnt(8)\nv_mfma_f32_16x16x32_f16 v[76:79], v[68:71], v[124:127], v[76:79]\n"
        "v_mfma_f32_16x16x32_f16 v[80:83], v[56:59], v[108:111], 0\ns_waitcnt lgkmcnt(1)\nv_readfirstlane_b32 %[s0], %[pv]\ns_nop 0\nv_mfma_f32_16x16x32_f16 v[80:83], v[60:63], v[112:115], v[80:83]\n"
        "s_cmp_ge_i32 %[s0], 9\ns_cbranch_scc0 .LJa%=\n.LKa%=:\nv_mfma_f32_16x16x32_f16 v[80:83], v[64:67], v[116:119], v[80:83]\nv_mfma_f32_16x16x32_f16 v[80:83], v[68:71], v[120:123], v[80:83]\n"
        "ds_write_b128 %[mw], v[76:79]\nv_mfma_f32_16x16x32_f16 v[80:83], v[96:99], v[124:127], v[80:83]\ns_nop 7\nds_write_b128 %[mw], v[80:83] offset:256\ns_branch .LE%=\n.LT%=:\n"
        "ds_read_b128 v[92:95], %[ubx] offset:0\ns_mov_b64 exec, 1\nds_add_u32 %[cna], %[one] offset:12\ns_mov_b64 exec, -1\nds_read_b128 v[132:135], %[xr] offset:64\n"
        "ds_read_b128 v[96:99], %[xr] offset:96\nds_read_b64 v[148:149], %[k8a] offset:128\nds_read_b64 v[150:151], %[k8a] offset:136\nds_read_b64 v[152:153], %[k8a] offset:192\n"
        "ds_read_b64 v[154:155], %[k8a] offset:200\nv_mfma_f32_16x16x32_f16 v[48:51], v[164:167], v[80:83], v[48:51]\nv_mfma_f32_16x16x32_f16 v[44:47], v[164:167], v[84:87], v[44:47]\n"
        "v_mfma_f32_16x16x32_f16 v[40:43], v[164:167], v[88:91], v[40:43]\nds_read_b32 %[pv], %[cna] offset:12\ns_waitcnt lgkmcnt(13)\nv_mfma_f32_16x16x32_f16 v[76:79], v[52:55], v[108:111], 0\n"
        "s_waitcnt lgkmcnt(12)\nv_mfma_f32_16x16x32_f16 v[76:79], v[56:59], v[112:115], v[76:79]\ns_waitcnt lgkmcnt(11)\nv_mfma_f32_16x16x32_f16 v[76:79], v[60:63], v[116:119], v[76:79]\n"
        "s_waitcnt lgkmcnt(10)\nv_mfma_f32_16x16x32_f16 v[76:79], v[64:67], v[120:123], v[76:79]\ns_waitcnt lgkmcnt(0)\nv_readfirstlane_b32 %[s0], %[pv]\ns_nop 0\n"
        "v_mfma_f32_16x16x32_f16 v[76:79], v[68:71], v[124:127], v[76:79]\ns_cmp_ge_i32 %[s0], 9\ns_cbranch_scc0 .LJb%=\n.LKb%=:\nv_mfma_f32_16x16x32_f16 v[36:39], v[164:167], v[92:95], v[36:39]\n"
        "ds_write_b128 v14, v[48:51] offset:128\nds_write_b128 %[exw], v[44:47] offset:128\nv_mfma_f32_16x16x32_f16 v[72:75], v[128:131], v[136:139], 0\nds_write_b128 v14, v[40:43] offset:0\n"
        "v_mfma_f32_16x16x32_f16 v[72:75], v[100:103], v[140:143], v[72:75]\nv_mfma_f32_16x16x32_f16 v[72:75], v[104:107], v[144:147], v[72:75]\nds_write_b128 %[mw], v[76:79]\n"
        "v_mfma_f32_16x16x32_f16 v[72:75], v[132:135], v[148:151], v[72:75]\nds_write_b128 %[exw], v[36:39] offset:0\nv_mfma_f32_16x16x32_f16 v[72:75], v[96:99], v[152:155], v[72:75]\ns_nop 7\n"
        "ds_write_b128 %[frw], v[72:75]\ns_branch .LE%=\n.LG%=:\ns_mov_b32 %[s1], 0x100000\n.LGx%=:\nds_read_b32 %[pv], %[cna] offset:4\ns_waitcnt lgkmcnt(0)\nv_readfirstlane_b32 %[s0], %[pv]\n"
        "s_nop 0\ns_cmp_ge_i32 %[s0], 4\ns_cbranch_scc1 .LH%=\ns_sub_u32 %[s1], %[s1], 1\ns_cbranch_scc0 .LGx%=\ns_branch .LH%=\n.LJa%=:\ns_mov_b32 %[s1], 0x100000\n.LJax%=:\n"
        "ds_read_b32 %[pv], %[cna] offset:12\ns_waitcnt lgkmcnt(0)\nv_readfirstlane_b32 %[s0], %[pv]\ns_nop 0\ns_cmp_ge_i32 %[s0], 9\ns_cbranch_scc1 .LKa%=\ns_sub_u32 %[s1], %[s1], 1\n"
        "s_cbranch_scc0 .LJax%=\ns_branch .LKa%=\n.LJb%=:\ns_mov_b32 %[s1], 0x100000\n.LJbx%=:\nds_read_b32 %[pv], %[cna] offset:12\ns_waitcnt lgkmcnt(0)\nv_readfirstlane_b32 %[s0], %[pv]\ns_nop 0\n"
        "s_cmp_ge_i32 %[s0], 9\ns_cbranch_scc1 .LKb%=\ns_sub_u32 %[s1], %[s1], 1\ns_cbranch_scc0 .LJbx%=\ns_branch .LKb%=\n.LE%=:\ns_mov_b64 exec, 1\nds_add_u32 %[cna], %[one] offset:8\n"
        "s_mov_b64 exec, -1\ns_setprio %[pbe]\ns_mov_b32 %[s1], 0x100000\n.LP%=:\nds_read_b32 %[pv], %[cna] offset:8\ns_waitcnt lgkmcnt(0)\nv_readfirstlane_b32 %[s0], %[pv]\ns_nop 0\n"
        "s_cmp_ge_i32 %[s0], 4\ns_cbranch_scc1 .LR%=\ns_sub_u32 %[s1], %[s1], 1\ns_cbranch_scc0 .LP%=\n.LR%=:\nds_read_b128 v[96:99], %[exr] offset:0\nds_read_b128 v[100:103], %[exr] offset:4096\n"
        "ds_read_b128 v[104:107], %[exr] offset:8192\nds_read_b128 v[108:111], %[exr] offset:12288\ns_cmp_eq_u32 %[wq], 0\ns_cbranch_scc1 .LQ%=\nds_read_b128 v[112:115], %[mr] offset:0\n"
        "ds_read_b128 v[116:119], %[mr] offset:3072\ns_cmp_lt_u32 %[wq], 2\ns_cbranch_scc1 .LQ%=\nds_read_b128 v[120:123], %[frr] offset:0\nds_read_b128 v[124:127], %[frr] offset:2304\n.LQ%=:\n"
        "s_waitcnt lgkmcnt(0)\nv_add_f32 v96, v96, v104\nv_add_f32 v97, v97, v105\nv_add_f32 v98, v98, v106\nv_add_f32 v99, v99, v107\nv_add_f32 v100, v100, v108\nv_add_f32 v101, v101, v109\n"
        "v_add_f32 v102, v102, v110\nv_add_f32 v103, v103, v111\ns_cmp_eq_u32 %[wq], 0\ns_cbranch_scc1 .LS%=\nv_add_f32 v96, v96, v112\nv_add_f32 v97, v97, v113\nv_add_f32 v98, v98, v114\n"
        "v_add_f32 v99, v99, v115\nv_add_f32 v100, v100, v116\nv_add_f32 v101, v101, v117\nv_add_f32 v102, v102, v118\nv_add_f32 v103, v103, v119\ns_cmp_eq_u32 %[wq], 1\ns_cbranch_scc1 .LS%=\n"
        "v_add_f32 v96, v96, v120\nv_add_f32 v97, v97, v121\nv_add_f32 v98, v98, v122\nv_add_f32 v99, v99, v123\nv_add_f32 v100, v100, v124\nv_add_f32 v101, v101, v125\nv_add_f32 v102, v102, v126\n"
        "v_add_f32 v103, v103, v127\n.LS%=:\nv_fmac_f32 v26, v96, %[kf]\nv_fmac_f32 v27, v97, %[kf]\nv_fmac_f32 v28, v98, %[kf]\nv_fmac_f32 v29, v99, %[kf]\nv_fmac_f32 v30, v100, %[kf]\n"
        "v_fmac_f32 v31, v101, %[kf]\nv_fmac_f32 v32, v102, %[kf]\nv_fmac_f32 v33, v103, %[kf]\nv_cvt_pk_bf16_f32 v104, v26, v27\nv_cvt_pk_bf16_f32 v105, v28, v29\nv_cvt_pk_bf16_f32 v106, v30, v31\n"
        "v_cvt_pk_bf16_f32 v107, v32, v33\nv_min_u32 v104, -2, v104\nv_min_u32 v105, -2, v105\nv_min_u32 v106, -2, v106\nv_min_u32 v107, -2, v107\n"
        "buffer_store_dwordx4 v[104:107], %[od], %[ors], 0 offen sc0 sc1\ns_nop 1\n.LZ%=:\n"
        : "+{v[96:99]}"(kt[0]), "+{v[100:103]}"(kt[1]), "+{v[104:107]}"(kt[2]), "+{v[108:111]}"(kt[3]), "+{v[112:115]}"(kt[4]), "+{v[116:119]}"(kt[5]), "+{v[120:123]}"(kt[6]), "+{v[124:127]}"(kt[7]), "+{v[128:131]}"(kt[8]), "+{v[132:135]}"(kt[9]), "+{v[136:139]}"(kt[10]), "+{v[140:143]}"(kt[11]), "+{v[144:147]}"(kt[12]), "+{v[148:151]}"(kt[13]), "+{v[152:155]}"(kt[14]), "+{v[156:159]}"(kt[15]), "+{v[160:163]}"(kt[16]), "+{v[164:167]}"(kt[17]), [pv] "=&{v16}"(pv), [s0] "=&s"(s0_), [s1] "=&s"(s1_), "+{v[26:29]}"(bs0), "+{v[30:33]}"(bs1)
        : [ub] "{v1}"(ub), [ubx] "{v2}"(ubx), [p1] "{v3}"(p1), [zw] "{v4}"(zw), [xd] "{v5}"(xd), [zr] "{v6}"(zr), [xr] "{v7}"(xr), [ux] "{v8}"(ux), [mw] "{v9}"(mw), [frw] "{v10}"(frw), [exw] "{v11}"(exw), [cna] "{v12}"(cna), [one] "{v13}"(one), [k8a] "{v23}"(k8a), [exr] "{v17}"(exr), [frr] "{v18}"(frr), [mr] "{v19}"(mr_), [od] "{v20}"(od), [kf] "{v21}"(kd[0]), [v] "s"(v), [wq] "s"(wq), [ors] "s"(ors), [pbe] "i"(PBE)
        : "memory", "scc", "v14", "v15", "v36", "v37", "v38", "v39", "v40", "v41", "v42", "v43", "v44", "v45", "v46", "v47", "v48", "v49", "v50", "v51", "v52", "v53", "v54", "v55", "v56", "v57", "v58", "v59", "v60", "v61", "v62", "v63", "v64", "v65", "v66", "v67", "v68", "v69", "v70", "v71", "v72", "v73", "v74", "v75", "v76", "v77", "v78", "v79", "v80", "v81", "v82", "v83", "v84", "v85", "v86", "v87", "v88", "v89", "v90", "v91", "v92", "v93", "v94", "v95");
    } else if (FULL) {
      asm volatile(
        "ds_read_b128 v[52:55], %[p1] offset:0\nds_read_b128 v[56:59], %[p1] offset:1152\nds_read_b128 v[60:63], %[p1] offset:2304\nds_read_b128 v[72:75], %[ub] offset:800\n"
        "ds_read_b128 v[76:79], %[ub] offset:736\nds_read_b128 v[80:83], %[ub] offset:672\nds_read_b128 v[84:87], %[ubx] offset:576\nds_read_b128 v[88:91], %[ub] offset:512\n"
        "ds_read_b128 v[92:95], %[ub] offset:448\nv_xor_b32 v14, 64, %[exw]\ns_waitcnt lgkmcnt(8)\nv_mfma_f32_16x16x32_f16 v[64:67], v[52:55], v[96:99], 0\n"
        "v_mfma_f32_16x16x32_f16 v[68:71], v[52:55], v[100:103], 0\nv_mfma_f32_16x16x32_f16 v[52:55], v[52:55], v[104:107], 0\ns_waitcnt lgkmcnt(7)\n"
        "v_mfma_f32_16x16x32_f16 v[96:99], v[56:59], v[96:99], 0\nv_mfma_f32_16x16x32_f16 v[100:103], v[56:59], v[100:103], 0\nv_mfma_f32_16x16x32_f16 v[56:59], v[56:59], v[104:107], 0\n"
        "v_cvt_pk_f16_f32 v52, v52, v53\nv_cvt_pk_f16_f32 v53, v54, v55\ns_waitcnt lgkmcnt(6)\nv_mfma_f32_16x16x32_f16 v[60:63], v[60:63], v[104:107], 0\nv_cvt_pk_f16_f32 v64, v64, v69\n"
        "v_cvt_pk_f16_f32 v65, v66, v71\ns_waitcnt lgkmcnt(5)\nv_mfma_f32_16x16x32_f16 v[48:51], v[132:135], v[72:75], 0\nds_write_b64 %[zw], v[52:53] offset:128\n"
        "ds_write_b64 %[xd], v[64:65] offset:0\ns_waitcnt lgkmcnt(6)\nv_mfma_f32_16x16x32_f16 v[44:47], v[132:135], v[76:79], 0\nds_read_b128 v[72:75], %[ub] offset:384\n"
        "v_cvt_pk_f16_f32 v56, v56, v57\ns_waitcnt lgkmcnt(6)\nv_mfma_f32_16x16x32_f16 v[40:43], v[132:135], v[80:83], 0\nv_cvt_pk_f16_f32 v57, v58, v59\nv_cvt_pk_f16_f32 v96, v96, v101\n"
        "s_waitcnt lgkmcnt(5)\nv_mfma_f32_16x16x32_f16 v[36:39], v[132:135], v[84:87], 0\nv_cvt_pk_f16_f32 v97, v98, v103\nds_write_b64 %[zw], v[56:57] offset:256\n"
        "v_mfma_f32_16x16x32_f16 v[48:51], v[136:139], v[76:79], v[48:51]\nds_write_b64 %[xd], v[96:97] offset:64\nv_cvt_pk_f16_f32 v60, v60, v61\n"
        "v_mfma_f32_16x16x32_f16 v[44:47], v[136:139], v[80:83], v[44:47]\nv_cvt_pk_f16_f32 v61, v62, v63\nds_write_b64 %[zw], v[60:61] offset:384\n"
        "v_mfma_f32_16x16x32_f16 v[40:43], v[136:139], v[84:87], v[40:43]\ns_mov_b64 exec, 1\nds_add_u32 %[cna], %[one] offset:4\ns_mov_b64 exec, -1\ns_waitcnt lgkmcnt(8)\n"
        "v_mfma_f32_16x16x32_f16 v[36:39], v[136:139], v[88:91], v[36:39]\nds_read_b128 v[76:79], %[ubx] offset:288\nv_mfma_f32_16x16x32_f16 v[48:51], v[140:143], v[80:83], v[48:51]\n"
        "ds_read_b128 v[80:83], %[ub] offset:224\nds_read_b128 v[136:139], %[ux]\nv_mfma_f32_16x16x32_f16 v[44:47], v[140:143], v[84:87], v[44:47]\n"
        "v_mfma_f32_16x16x32_f16 v[40:43], v[140:143], v[88:91], v[40:43]\ns_waitcnt lgkmcnt(10)\nv_mfma_f32_16x16x32_f16 v[36:39], v[140:143], v[92:95], v[36:39]\n"
        "v_mfma_f32_16x16x32_f16 v[48:51], v[144:147], v[84:87], v[48:51]\nds_read_b128 v[84:87], %[ub] offset:160\nv_mfma_f32_16x16x32_f16 v[44:47], v[144:147], v[88:91], v[44:47]\n"
        "v_mfma_f32_16x16x32_f16 v[40:43], v[144:147], v[92:95], v[40:43]\ns_waitcnt lgkmcnt(8)\nv_mfma_f32_16x16x32_f16 v[36:39], v[144:147], v[72:75], v[36:39]\n"
        "v_mfma_f32_16x16x32_f16 v[48:51], v[148:151], v[88:91], v[48:51]\nds_read_b128 v[88:91], %[ub] offset:96\nds_read_b32 %[pv], %[cna] offset:4\n"
        "v_mfma_f32_16x16x32_f16 v[44:47], v[148:151], v[92:95], v[44:47]\nds_read_b64 v[140:141], %[k8a] offset:0\nds_read_b64 v[142:143], %[k8a] offset:8\n"
        "v_mfma_f32_16x16x32_f16 v[40:43], v[148:151], v[72:75], v[40:43]\nds_read_b64 v[144:145], %[k8a] offset:64\nds_read_b64 v[146:147], %[k8a] offset:72\ns_waitcnt lgkmcnt(9)\n"
        "v_mfma_f32_16x16x32_f16 v[36:39], v[148:151], v[76:79], v[36:39]\nv_mfma_f32_16x16x32_f16 v[48:51], v[152:155], v[92:95], v[48:51]\ns_waitcnt lgkmcnt(4)\nv_readfirstlane_b32 %[s0], %[pv]\n"
        "s_nop 0\nv_mfma_f32_16x16x32_f16 v[44:47], v[152:155], v[72:75], v[44:47]\ns_cmp_ge_i32 %[s0], 4\ns_cbranch_scc0 .LG%=\n.LH%=:\n"
        "v_mfma_f32_16x16x32_f16 v[40:43], v[152:155], v[76:79], v[40:43]\nds_read_b128 v[100:103], %[xr] offset:0\nds_read_b128 v[104:107], %[xr] offset:32\n"
        "v_mfma_f32_16x16x32_f16 v[36:39], v[152:155], v[80:83], v[36:39]\nds_read_b128 v[52:55], %[zr] offset:0\nds_read_b128 v[56:59], %[zr] offset:32\n"
        "v_mfma_f32_16x16x32_f16 v[48:51], v[156:159], v[72:75], v[48:51]\nds_read_b128 v[60:63], %[zr] offset:64\nds_read_b128 v[64:67], %[zr] offset:96\n"
        "v_mfma_f32_16x16x32_f16 v[44:47], v[156:159], v[76:79], v[44:47]\nds_read_b128 v[68:71], %[zr] offset:128\nv_mfma_f32_16x16x32_f16 v[40:43], v[156:159], v[80:83], v[40:43]\n"
        "v_mfma_f32_16x16x32_f16 v[36:39], v[156:159], v[84:87], v[36:39]\nv_mfma_f32_16x16x32_f16 v[48:51], v[160:163], v[76:79], v[48:51]\n"
        "v_mfma_f32_16x16x32_f16 v[44:47], v[160:163], v[80:83], v[44:47]\nv_mfma_f32_16x16x32_f16 v[40:43], v[160:163], v[84:87], v[40:43]\n"
        "v_mfma_f32_16x16x32_f16 v[36:39], v[160:163], v[88:91], v[36:39]\ns_cmp_lt_u32 %[v], 2\ns_cbranch_scc0 .LT%=\nds_read_b128 v[96:99], %[zr] offset:160\ns_mov_b64 exec, 1\n"
        "ds_add_u32 %[cna], %[one] offset:12\ns_mov_b64 exec, -1\nv_mfma_f32_16x16x32_f16 v[72:75], v[128:131], v[136:139], 0\ns_waitcnt lgkmcnt(8)\n"
        "v_mfma_f32_16x16x32_f16 v[72:75], v[100:103], v[140:143], v[72:75]\ns_waitcnt lgkmcnt(7)\nv_mfma_f32_16x16x32_f16 v[72:75], v[104:107], v[144:147], v[72:75]\n"
        "ds_write_b128 v14, v[48:51] offset:128\nds_write_b128 %[exw], v[44:47] offset:128\ns_waitcnt lgkmcnt(8)\nv_mfma_f32_16x16x32_f16 v[76:79], v[52:55], v[108:111], 0\n"
        "ds_write_b128 v14, v[40:43] offset:0\nds_write_b128 %[exw], v[36:39] offset:0\ns_waitcnt lgkmcnt(9)\nv_mfma_f32_16x16x32_f16 v[76:79], v[56:59], v[112:115], v[76:79]\n"
        "ds_read_b32 %[pv], %[cna] offset:12\ns_waitcnt lgkmcnt(9)\nv_mfma_f32_16x16x32_f16 v[76:79], v[60:63], v[116:119], v[76:79]\ns_waitcnt lgkmcnt(8)\n"
        "v_mfma_f32_16x16x32_f16 v[76:79], v[64:67], v[120:123], v[76:79]\nds_write_b128 %[frw], v[72:75]\ns_waitcnt lgkmcnt(8)\nv_mfma_f32_16x16x32_f16 v[76:79], v[68:71], v[124:127], v[76:79]\n"
        "v_mfma_f32_16x16x32_f16 v[80:83], v[56:59], v[108:111], 0\ns_waitcnt lgkmcnt(1)\nv_readfirstlane_b32 %[s0], %[pv]\ns_nop 0\nv_mfma_f32_16x16x32_f16 v[80:83], v[60:63], v[112:115], v[80:83]\n"
        "s_cmp_ge_i32 %[s0], 9\ns_cbranch_scc0 .LJa%=\n.LKa%=:\nv_mfma_f32_16x16x32_f16 v[80:83], v[64:67], v[116:119], v[80:83]\nv_mfma_f32_16x16x32_f16 v[80:83], v[68:71], v[120:123], v[80:83]\n"
        "ds_write_b128 %[mw], v[76:79]\nv_mfma_f32_16x16x32_f16 v[80:83], v[96:99], v[124:127], v[80:83]\ns_nop 7\nds_write_b128 %[mw], v[80:83] offset:256\ns_branch .LE%=\n.LT%=:\n"
        "ds_read_b128 v[92:95], %[ubx] offset:0\ns_mov_b64 exec, 1\nds_add_u32 %[cna], %[one] offset:12\ns_mov_b64 exec, -1\nds_read_b128 v[132:135], %[xr] offset:64\n"
        "ds_read_b128 v[96:99], %[xr] offset:96\nds_read_b64 v[148:149], %[k8a] offset:128\nds_read_b64 v[150:151], %[k8a] offset:136\nds_read_b64 v[152:153], %[k8a] offset:192\n"
        "ds_read_b64 v[154:155], %[k8a] offset:200\nv_mfma_f32_16x16x32_f16 v[48:51], v[164:167], v[80:83], v[48:51]\nv_mfma_f32_16x16x32_f16 v[44:47], v[164:167], v[84:87], v[44:47]\n"
        "v_mfma_f32_16x16x32_f16 v[40:43], v[164:167], v[88:91], v[40:43]\nds_read_b32 %[pv], %[cna] offset:12\ns_waitcnt lgkmcnt(13)\nv_mfma_f32_16x16x32_f16 v[76:79], v[52:55], v[108:111], 0\n"
        "s_waitcnt lgkmcnt(12)\nv_mfma_f32_16x16x32_f16 v[76:79], v[56:59], v[112:115], v[76:79]\ns_waitcnt lgkmcnt(11)\nv_mfma_f32_16x16x32_f16 v[76:79], v[60:63], v[116:119], v[76:79]\n"
        "s_waitcnt lgkmcnt(10)\nv_mfma_f32_16x16x32_f16 v[76:79], v[64:67], v[120:123], v[76:79]\ns_waitcnt lgkmcnt(0)\nv_readfirstlane_b32 %[s0], %[pv]\ns_nop 0\n"
        "v_mfma_f32_16x16x32_f16 v[76:79], v[68:71], v[124:127], v[76:79]\ns_cmp_ge_i32 %[s0], 9\ns_cbranch_scc0 .LJb%=\n.LKb%=:\nv_mfma_f32_16x16x32_f16 v[36:39], v[164:167], v[92:95], v[36:39]\n"
        "ds_write_b128 v14, v[48:51] offset:128\nds_write_b128 %[exw], v[44:47] offset:128\nv_mfma_f32_16x16x32_f16 v[72:75], v[128:131], v[136:139], 0\nds_write_b128 v14, v[40:43] offset:0\n"
        "v_mfma_f32_16x16x32_f16 v[72:75], v[100:103], v[140:143], v[72:75]\nv_mfma_f32_16x16x32_f16 v[72:75], v[104:107], v[144:147], v[72:75]\nds_write_b128 %[mw], v[76:79]\n"
        "v_mfma_f32_16x16x32_f16 v[72:75], v[132:135], v[148:151], v[72:75]\nds_write_b128 %[exw], v[36:39] offset:0\nv_mfma_f32_16x16x32_f16 v[72:75], v[96:99], v[152:155], v[72:75]\ns_nop 7\n"
        "ds_write_b128 %[frw], v[72:75]\ns_branch .LE%=\n.LG%=:\ns_mov_b32 %[s1], 0x100000\n.LGx%=:\nds_read_b32 %[pv], %[cna] offset:4\ns_waitcnt lgkmcnt(0)\nv_readfirstlane_b32 %[s0], %[pv]\n"
        "s_nop 0\ns_cmp_ge_i32 %[s0], 4\ns_cbranch_scc1 .LH%=\ns_sub_u32 %[s1], %[s1], 1\ns_cbranch_scc0 .LGx%=\ns_branch .LH%=\n.LJa%=:\ns_mov_b32 %[s1], 0x100000\n.LJax%=:\n"
        "ds_read_b32 %[pv], %[cna] offset:12\ns_waitcnt lgkmcnt(0)\nv_readfirstlane_b32 %[s0], %[pv]\ns_nop 0\ns_cmp_ge_i32 %[s0], 9\ns_cbranch_scc1 .LKa%=\ns_sub_u32 %[s1], %[s1], 1\n"
        "s_cbranch_scc0 .LJax%=\ns_branch .LKa%=\n.LJb%=:\ns_mov_b32 %[s1], 0x100000\n.LJbx%=:\nds_read_b32 %[pv], %[cna] offset:12\ns_waitcnt lgkmcnt(0)\nv_readfirstlane_b32 %[s0], %[pv]\ns_nop 0\n"
        "s_cmp_ge_i32 %[s0], 9\ns_cbranch_scc1 .LKb%=\ns_sub_u32 %[s1], %[s1], 1\ns_cbranch_scc0 .LJbx%=\ns_branch .LKb%=\n.LE%=:\ns_waitcnt vmcnt(0)\ns_mov_b64 exec, 1\n"
        "ds_add_u32 %[cna], %[one] offset:8\ns_mov_b64 exec, -1\ns_setprio %[pbe]\ns_mov_b32 %[s1], 0x100000\n.LP%=:\nds_read_b32 %[pv], %[cna] offset:8\ns_waitcnt lgkmcnt(0)\n"
        "v_readfirstlane_b32 %[s0], %[pv]\ns_nop 0\ns_cmp_ge_i32 %[s0], 4\ns_cbranch_scc1 .LR%=\ns_sub_u32 %[s1], %[s1], 1\ns_cbranch_scc0 .LP%=\n.LR%=:\nds_read_b128 v[96:99], %[exr] offset:0\n"
        "ds_read_b128 v[100:103], %[exr] offset:4096\nds_read_b128 v[104:107], %[exr] offset:8192\nds_read_b128 v[108:111], %[exr] offset:12288\ns_cmp_eq_u32 %[wq], 0\ns_cbranch_scc1 .LQ%=\n"
        "ds_read_b128 v[112:115], %[mr] offset:0\nds_read_b128 v[116:119], %[mr] offset:3072\ns_cmp_lt_u32 %[wq], 2\ns_cbranch_scc1 .LQ%=\nds_read_b128 v[120:123], %[frr] offset:0\n"
        "ds_read_b128 v[124:127], %[frr] offset:2304\n.LQ%=:\ns_waitcnt lgkmcnt(0)\nv_add_f32 v96, v96, v104\nv_add_f32 v97, v97, v105\nv_add_f32 v98, v98, v106\nv_add_f32 v99, v99, v107\n"
        "v_add_f32 v100, v100, v108\nv_add_f32 v101, v101, v109\nv_add_f32 v102, v102, v110\nv_add_f32 v103, v103, v111\ns_cmp_eq_u32 %[wq], 0\ns_cbranch_scc1 .LS%=\nv_add_f32 v96, v96, v112\n"
        "v_add_f32 v97, v97, v113\nv_add_f32 v98, v98, v114\nv_add_f32 v99, v99, v115\nv_add_f32 v100, v100, v116\nv_add_f32 v101, v101, v117\nv_add_f32 v102, v102, v118\nv_add_f32 v103, v103, v119\n"
        "s_cmp_eq_u32 %[wq], 1\ns_cbranch_scc1 .LS%=\nv_add_f32 v96, v96, v120\nv_add_f32 v97, v97, v121\nv_add_f32 v98, v98, v122\nv_add_f32 v99, v99, v123\nv_add_f32 v100, v100, v124\n"
        "v_add_f32 v101, v101, v125\nv_add_f32 v102, v102, v126\nv_add_f32 v103, v103, v127\n.LS%=:\nv_fmac_f32 v26, v96, %[kf]\nv_fmac_f32 v27, v97, %[kf]\nv_fmac_f32 v28, v98, %[kf]\n"
        "v_fmac_f32 v29, v99, %[kf]\nv_fmac_f32 v30, v100, %[kf]\nv_fmac_f32 v31, v101, %[kf]\nv_fmac_f32 v32, v102, %[kf]\nv_fmac_f32 v33, v103, %[kf]\nv_cvt_pk_bf16_f32 v104, v26, v27\n"
        "v_cvt_pk_bf16_f32 v105, v28, v29\nv_cvt_pk_bf16_f32 v106, v30, v31\nv_cvt_pk_bf16_f32 v107, v32, v33\nv_min_u32 v104, -2, v104\nv_min_u32 v105, -2, v105\nv_min_u32 v106, -2, v106\n"
        "v_min_u32 v107, -2, v107\nbuffer_store_dwordx4 v[104:107], %[od], %[ors], 0 offen sc0 sc1\nbuffer_store_dwordx4 v[104:107], %[oh], %[ors], 0 offen sc0 sc1\ns_nop 1\n.LZ%=:\n"
        : "+{v[96:99]}"(kt[0]), "+{v[100:103]}"(kt[1]), "+{v[104:107]}"(kt[2]), "+{v[108:111]}"(kt[3]), "+{v[112:115]}"(kt[4]), "+{v[116:119]}"(kt[5]), "+{v[120:123]}"(kt[6]), "+{v[124:127]}"(kt[7]), "+{v[128:131]}"(kt[8]), "+{v[132:135]}"(kt[9]), "+{v[136:139]}"(kt[10]), "+{v[140:143]}"(kt[11]), "+{v[144:147]}"(kt[12]), "+{v[148:151]}"(kt[13]), "+{v[152:155]}"(kt[14]), "+{v[156:159]}"(kt[15]), "+{v[160:163]}"(kt[16]), "+{v[164:167]}"(kt[17]), [pv] "=&{v16}"(pv), [s0] "=&s"(s0_), [s1] "=&s"(s1_), "+{v[26:29]}"(bs0), "+{v[30:33]}"(bs1)
        : [ub] "{v1}"(ub), [ubx] "{v2}"(ubx), [p1] "{v3}"(p1), [zw] "{v4}"(zw), [xd] "{v5}"(xd), [zr] "{v6}"(zr), [xr] "{v7}"(xr), [ux] "{v8}"(ux), [mw] "{v9}"(mw), [frw] "{v10}"(frw), [exw] "{v11}"(exw), [cna] "{v12}"(cna), [one] "{v13}"(one), [k8a] "{v23}"(k8a), [exr] "{v17}"(exr), [frr] "{v18}"(frr), [mr] "{v19}"(mr_), [od] "{v20}"(od), [kf] "{v21}"(kd[0]), [oh] "{v22}"(oh), [v] "s"(v), [wq] "s"(wq), [ors] "s"(ors), [pbe] "i"(PBE)
        : "memory", "scc", "v14", "v15", "v36", "v37", "v38", "v39", "v40", "v41", "v42", "v43", "v44", "v45", "v46", "v47", "v48", "v49", "v50", "v51", "v52", "v53", "v54", "v55", "v56", "v57", "v58", "v59", "v60", "v61", "v62", "v63", "v64", "v65", "v66", "v67", "v68", "v69", "v70", "v71", "v72", "v73", "v74", "v75", "v76", "v77", "v78", "v79", "v80", "v81", "v82", "v83", "v84", "v85", "v86", "v87", "v88", "v89", "v90", "v91", "v92", "v93", "v94", "v95");
    } else if (layer) {
      asm volatile(
        "ds_read_b128 v[48:51], %[p1] offset:0\nds_read_b128 v[52:55], %[p1] offset:1152\nds_read_b128 v[56:59], %[p1] offset:2304\nds_read_b128 v[68:71], %[ub] offset:512\n"
        "ds_read_b128 v[72:75], %[ub] offset:448\nds_read_b128 v[76:79], %[ub] offset:384\nds_read_b128 v[80:83], %[ubx] offset:288\nds_read_b128 v[84:87], %[ub] offset:224\n"
        "ds_read_b128 v[88:91], %[ub] offset:160\nbuffer_load_dwordx4 v[152:155], %[xo], %[xrs], 0 offen sc1\nv_xor_b32 v14, 32, %[exw]\nv_xor_b32 v15, 64, %[exw]\nv_xor_b32 v28, 96, %[exw]\n"
        "s_waitcnt lgkmcnt(8)\nv_mfma_f32_16x16x32_f16 v[60:63], v[48:51], v[92:95], 0\nv_mfma_f32_16x16x32_f16 v[64:67], v[48:51], v[96:99], 0\n"
        "v_mfma_f32_16x16x32_f16 v[48:51], v[48:51], v[100:103], 0\ns_waitcnt lgkmcnt(7)\nv_mfma_f32_16x16x32_f16 v[92:95], v[52:55], v[92:95], 0\n"
        "v_mfma_f32_16x16x32_f16 v[96:99], v[52:55], v[96:99], 0\nv_mfma_f32_16x16x32_f16 v[52:55], v[52:55], v[100:103], 0\nv_cvt_pk_f16_f32 v48, v48, v49\nv_cvt_pk_f16_f32 v49, v50, v51\n"
        "s_waitcnt lgkmcnt(6)\nv_mfma_f32_16x16x32_f16 v[56:59], v[56:59], v[100:103], 0\nv_cvt_pk_f16_f32 v60, v60, v65\nv_cvt_pk_f16_f32 v61, v62, v67\ns_waitcnt lgkmcnt(5)\n"
        "v_mfma_f32_16x16x32_f16 v[44:47], v[132:135], v[68:71], 0\nds_write_b64 %[zw], v[48:49] offset:128\nds_write_b64 %[xd], v[60:61] offset:0\ns_waitcnt lgkmcnt(6)\n"
        "v_mfma_f32_16x16x32_f16 v[40:43], v[132:135], v[72:75], 0\nds_read_b128 v[68:71], %[ub] offset:96\nv_cvt_pk_f16_f32 v52, v52, v53\ns_waitcnt lgkmcnt(6)\n"
        "v_mfma_f32_16x16x32_f16 v[36:39], v[132:135], v[76:79], 0\nv_cvt_pk_f16_f32 v53, v54, v55\nv_cvt_pk_f16_f32 v92, v92, v97\ns_waitcnt lgkmcnt(5)\n"
        "v_mfma_f32_16x16x32_f16 v[32:35], v[132:135], v[80:83], 0\nv_cvt_pk_f16_f32 v93, v94, v99\nds_write_b64 %[zw], v[52:53] offset:256\n"
        "v_mfma_f32_16x16x32_f16 v[44:47], v[136:139], v[72:75], v[44:47]\nds_write_b64 %[xd], v[92:93] offset:64\nv_cvt_pk_f16_f32 v56, v56, v57\n"
        "v_mfma_f32_16x16x32_f16 v[40:43], v[136:139], v[76:79], v[40:43]\nv_cvt_pk_f16_f32 v57, v58, v59\nds_write_b64 %[zw], v[56:57] offset:384\n"
        "v_mfma_f32_16x16x32_f16 v[36:39], v[136:139], v[80:83], v[36:39]\ns_mov_b64 exec, 1\nds_add_u32 %[cna], %[one] offset:4\ns_mov_b64 exec, -1\ns_waitcnt lgkmcnt(8)\n"
        "v_mfma_f32_16x16x32_f16 v[32:35], v[136:139], v[84:87], v[32:35]\nv_mfma_f32_16x16x32_f16 v[44:47], v[140:143], v[76:79], v[44:47]\n"
        "v_mfma_f32_16x16x32_f16 v[40:43], v[140:143], v[80:83], v[40:43]\nv_mfma_f32_16x16x32_f16 v[36:39], v[140:143], v[84:87], v[36:39]\ns_waitcnt lgkmcnt(7)\n"
        "v_mfma_f32_16x16x32_f16 v[32:35], v[140:143], v[88:91], v[32:35]\nv_mfma_f32_16x16x32_f16 v[44:47], v[144:147], v[80:83], v[44:47]\n"
        "v_mfma_f32_16x16x32_f16 v[40:43], v[144:147], v[84:87], v[40:43]\nds_read_b32 %[pv], %[cna] offset:4\nv_mfma_f32_16x16x32_f16 v[36:39], v[144:147], v[88:91], v[36:39]\ns_waitcnt lgkmcnt(5)\n"
        "v_mfma_f32_16x16x32_f16 v[32:35], v[144:147], v[68:71], v[32:35]\nds_read_b128 v[156:159], %[ue]\ns_cmp_eq_u32 %[v], 3\ns_cbranch_scc1 .LT%=\nds_read_b128 v[136:139], %[ux]\n"
        "ds_read_b64 v[140:141], %[k8a] offset:0\nds_read_b64 v[142:143], %[k8a] offset:8\nds_read_b64 v[144:145], %[k8a] offset:64\nds_read_b64 v[146:147], %[k8a] offset:72\ns_waitcnt lgkmcnt(6)\n"
        "v_readfirstlane_b32 %[s0], %[pv]\ns_nop 0\ns_cmp_ge_i32 %[s0], 4\ns_cbranch_scc0 .LG%=\n.LH%=:\nds_read_b128 v[96:99], %[xr] offset:0\nds_read_b128 v[100:103], %[xr] offset:32\n"
        "ds_read_b128 v[48:51], %[zr] offset:0\nds_read_b128 v[52:55], %[zr] offset:32\nds_read_b128 v[56:59], %[zr] offset:64\nds_read_b128 v[60:63], %[zr] offset:96\n"
        "ds_read_b128 v[64:67], %[zr] offset:128\nds_read_b128 v[92:95], %[zr] offset:160\ns_mov_b64 exec, 1\nds_add_u32 %[cna], %[one] offset:12\ns_mov_b64 exec, -1\nds_write_b128 v28, v[44:47]\n"
        "ds_write_b128 v15, v[40:43]\nds_write_b128 v14, v[36:39]\nds_write_b128 %[exw], v[32:35]\ns_waitcnt lgkmcnt(15)\nv_mfma_f32_16x16x32_f16 v[132:135], v[128:131], v[136:139], 0\n"
        "s_waitcnt lgkmcnt(12)\nv_mfma_f32_16x16x32_f16 v[132:135], v[96:99], v[140:143], v[132:135]\ns_waitcnt lgkmcnt(11)\nv_mfma_f32_16x16x32_f16 v[132:135], v[100:103], v[144:147], v[132:135]\n"
        "ds_read_b32 %[pv], %[cna] offset:12\ns_waitcnt lgkmcnt(11)\nv_mfma_f32_16x16x32_f16 v[76:79], v[48:51], v[104:107], 0\ns_waitcnt lgkmcnt(10)\n"
        "v_mfma_f32_16x16x32_f16 v[76:79], v[52:55], v[108:111], v[76:79]\ns_waitcnt lgkmcnt(9)\nv_mfma_f32_16x16x32_f16 v[76:79], v[56:59], v[112:115], v[76:79]\ns_waitcnt lgkmcnt(8)\n"
        "v_mfma_f32_16x16x32_f16 v[76:79], v[60:63], v[116:119], v[76:79]\ns_waitcnt lgkmcnt(0)\nv_readfirstlane_b32 %[s0], %[pv]\ns_nop 0\n"
        "v_mfma_f32_16x16x32_f16 v[76:79], v[64:67], v[120:123], v[76:79]\nds_write_b128 %[frw], v[132:135]\ns_cmp_ge_i32 %[s0], 9\nv_mfma_f32_16x16x32_f16 v[76:79], v[92:95], v[124:127], v[76:79]\n"
        "s_cbranch_scc0 .LJa%=\n.LKa%=:\ns_nop 7\nds_write_b128 %[mw], v[76:79]\ns_branch .LE%=\n.LT%=:\nds_read_b128 v[72:75], %[ubx] offset:0\ns_mov_b64 exec, 1\nds_add_u32 %[cna], %[one] offset:12\n"
        "s_mov_b64 exec, -1\nv_mov_b32 v132, 0\nv_mov_b32 v133, 0\nv_mov_b32 v134, 0\nv_mov_b32 v135, 0\nv_mfma_f32_16x16x32_f16 v[44:47], v[148:151], v[84:87], v[44:47]\n"
        "ds_write_b128 %[frw], v[132:135]\nv_mfma_f32_16x16x32_f16 v[40:43], v[148:151], v[88:91], v[40:43]\nv_mfma_f32_16x16x32_f16 v[36:39], v[148:151], v[68:71], v[36:39]\ns_waitcnt lgkmcnt(2)\n"
        "v_mfma_f32_16x16x32_f16 v[32:35], v[148:151], v[72:75], v[32:35]\ns_nop 7\nds_write_b128 v28, v[44:47]\nds_write_b128 v15, v[40:43]\nds_write_b128 v14, v[36:39]\n"
        "ds_write_b128 %[exw], v[32:35]\ns_branch .LE%=\n.LG%=:\ns_mov_b32 %[s1], 0x100000\n.LGx%=:\nds_read_b32 %[pv], %[cna] offset:4\ns_waitcnt lgkmcnt(0)\nv_readfirstlane_b32 %[s0], %[pv]\n"
        "s_nop 0\ns_cmp_ge_i32 %[s0], 4\ns_cbranch_scc1 .LH%=\ns_sub_u32 %[s1], %[s1], 1\ns_cbranch_scc0 .LGx%=\ns_branch .LH%=\n.LJa%=:\ns_mov_b32 %[s1], 0x100000\n.LJax%=:\n"
        "ds_read_b32 %[pv], %[cna] offset:12\ns_waitcnt lgkmcnt(0)\nv_readfirstlane_b32 %[s0], %[pv]\ns_nop 0\ns_cmp_ge_i32 %[s0], 9\ns_cbranch_scc1 .LKa%=\ns_sub_u32 %[s1], %[s1], 1\n"
        "s_cbranch_scc0 .LJax%=\ns_branch .LKa%=\n.LE%=:\ns_waitcnt vmcnt(0)\ns_mov_b64 exec, 1\nds_add_u32 %[cna], %[one] offset:8\ns_mov_b64 exec, -1\ns_setprio %[pbe]\ns_cmp_lt_u32 %[wq], 2\n"
        "s_cbranch_scc0 .LZ%=\ns_waitcnt lgkmcnt(6)\nv_lshlrev_b32 v160, 16, v152\nv_and_b32 v161, 0xffff0000, v152\nv_cvt_f32_f16 v152, v156\nv_lshrrev_b32 v156, 16, v156\n"
        "v_lshlrev_b32 v162, 16, v153\nv_and_b32 v163, 0xffff0000, v153\nv_cvt_f32_f16 v153, v157\nv_lshrrev_b32 v157, 16, v157\nv_lshlrev_b32 v164, 16, v154\nv_and_b32 v165, 0xffff0000, v154\n"
        "v_cvt_f32_f16 v154, v158\nv_lshrrev_b32 v158, 16, v158\nv_lshlrev_b32 v166, 16, v155\nv_and_b32 v167, 0xffff0000, v155\nv_cvt_f32_f16 v155, v159\nv_lshrrev_b32 v159, 16, v159\n"
        "v_cvt_f32_f16 v156, v156\nv_fmac_f32 v160, v152, %[kd1]\nv_cvt_f32_f16 v157, v157\nv_fmac_f32 v162, v153, %[kd1]\nv_cvt_f32_f16 v158, v158\nv_fmac_f32 v164, v154, %[kd1]\n"
        "v_cvt_f32_f16 v159, v159\nv_fmac_f32 v166, v155, %[kd1]\nv_fmac_f32 v161, v156, %[kd1]\nv_fmac_f32 v163, v157, %[kd1]\nv_fmac_f32 v165, v158, %[kd1]\nv_fmac_f32 v167, v159, %[kd1]\n"
        "s_mov_b32 %[s1], 0x100000\n.LP%=:\nds_read_b32 %[pv], %[cna] offset:8\ns_waitcnt lgkmcnt(0)\nv_readfirstlane_b32 %[s0], %[pv]\ns_nop 0\ns_cmp_ge_i32 %[s0], 4\ns_cbranch_scc1 .LR%=\n"
        "s_sub_u32 %[s1], %[s1], 1\ns_cbranch_scc0 .LP%=\n.LR%=:\nds_read_b128 v[96:99], %[exr] offset:0\nds_read_b128 v[100:103], %[exr] offset:2048\nds_read_b128 v[104:107], %[exr] offset:4096\n"
        "ds_read_b128 v[108:111], %[exr] offset:6144\nds_read_b128 v[112:115], %[exr] offset:8192\nds_read_b128 v[116:119], %[exr] offset:10240\nds_read_b128 v[120:123], %[exr] offset:12288\n"
        "ds_read_b128 v[124:127], %[exr] offset:14336\nds_read_b128 v[128:131], %[mr] offset:0\nds_read_b128 v[132:135], %[mr] offset:3072\ns_cmp_lt_u32 %[wq], 1\ns_cbranch_scc1 .LQ%=\n"
        "ds_read_b128 v[136:139], %[frr] offset:0\nds_read_b128 v[140:143], %[frr] offset:1152\nds_read_b128 v[144:147], %[frr] offset:2304\nds_read_b128 v[148:151], %[frr] offset:3456\n.LQ%=:\n"
        "s_waitcnt lgkmcnt(0)\nv_add_f32 v96, v96, v104\nv_add_f32 v97, v97, v105\nv_add_f32 v98, v98, v106\nv_add_f32 v99, v99, v107\nv_add_f32 v100, v100, v108\nv_add_f32 v101, v101, v109\n"
        "v_add_f32 v102, v102, v110\nv_add_f32 v103, v103, v111\nv_add_f32 v112, v112, v120\nv_add_f32 v113, v113, v121\nv_add_f32 v114, v114, v122\nv_add_f32 v115, v115, v123\n"
        "v_add_f32 v116, v116, v124\nv_add_f32 v117, v117, v125\nv_add_f32 v118, v118, v126\nv_add_f32 v119, v119, v127\nv_fmac_f32 v96, v128, %[fm4]\nv_fmac_f32 v97, v129, %[fm4]\n"
        "v_fmac_f32 v98, v130, %[fm4]\nv_fmac_f32 v99, v131, %[fm4]\nv_fmac_f32 v100, v132, %[fm4]\nv_fmac_f32 v101, v133, %[fm4]\nv_fmac_f32 v102, v134, %[fm4]\nv_fmac_f32 v103, v135, %[fm4]\n"
        "s_cmp_eq_u32 %[wq], 0\nv_add_f32 v96, v96, v112\nv_add_f32 v97, v97, v113\nv_add_f32 v98, v98, v114\nv_add_f32 v99, v99, v115\nv_add_f32 v100, v100, v116\nv_add_f32 v101, v101, v117\n"
        "v_add_f32 v102, v102, v118\nv_add_f32 v103, v103, v119\ns_cbranch_scc1 .LS%=\nv_add_f32 v136, v136, v144\nv_add_f32 v137, v137, v145\nv_add_f32 v138, v138, v146\nv_add_f32 v139, v139, v147\n"
        "v_add_f32 v140, v140, v148\nv_add_f32 v141, v141, v149\nv_add_f32 v142, v142, v150\nv_add_f32 v143, v143, v151\nv_add_f32 v96, v96, v136\nv_add_f32 v97, v97, v137\nv_add_f32 v98, v98, v138\n"
        "v_add_f32 v99, v99, v139\nv_add_f32 v100, v100, v140\nv_add_f32 v101, v101, v141\nv_add_f32 v102, v102, v142\nv_add_f32 v103, v103, v143\n.LS%=:\nv_fmac_f32 v160, v96, %[kf]\n"
        "v_fmac_f32 v161, v97, %[kf]\nv_fmac_f32 v162, v98, %[kf]\nv_fmac_f32 v163, v99, %[kf]\nv_fmac_f32 v164, v100, %[kf]\nv_fmac_f32 v165, v101, %[kf]\nv_fmac_f32 v166, v102, %[kf]\n"
        "v_fmac_f32 v167, v103, %[kf]\nv_cvt_pk_bf16_f32 v104, v160, v161\nv_cvt_pk_bf16_f32 v105, v162, v163\nv_cvt_pk_bf16_f32 v106, v164, v165\nv_cvt_pk_bf16_f32 v107, v166, v167\n"
        "v_min_u32 v104, -2, v104\nv_min_u32 v105, -2, v105\nv_min_u32 v106, -2, v106\nv_min_u32 v107, -2, v107\nbuffer_store_dwordx4 v[104:107], %[od], %[ors], 0 offen sc0 sc1\ns_nop 1\n.LZ%=:\n"
        : "+{v[92:95]}"(kt[0]), "+{v[96:99]}"(kt[1]), "+{v[100:103]}"(kt[2]), "+{v[104:107]}"(kt[3]), "+{v[108:111]}"(kt[4]), "+{v[112:115]}"(kt[5]), "+{v[116:119]}"(kt[6]), "+{v[120:123]}"(kt[7]), "+{v[124:127]}"(kt[8]), "+{v[128:131]}"(kt[9]), "+{v[132:135]}"(kt[10]), "+{v[136:139]}"(kt[11]), "+{v[140:143]}"(kt[12]), "+{v[144:147]}"(kt[13]), "+{v[148:151]}"(kt[14]), [pv] "=&{v16}"(pv), [s0] "=&s"(s0_), [s1] "=&s"(s1_)
        : [ub] "{v1}"(ub), [ubx] "{v2}"(ubx), [p1] "{v3}"(p1), [zw] "{v4}"(zw), [xd] "{v5}"(xd), [zr] "{v6}"(zr), [xr] "{v7}"(xr), [ux] "{v8}"(ux), [mw] "{v9}"(mw), [frw] "{v10}"(frw), [exw] "{v11}"(exw), [cna] "{v12}"(cna), [one] "{v13}"(one), [k8a] "{v23}"(k8a), [exr] "{v17}"(exr), [frr] "{v18}"(frr), [mr] "{v19}"(mr_), [od] "{v20}"(od), [kf] "{v21}"(kd[0]), [ue] "{v24}"(ue), [fm4] "{v25}"(fm4), [xo] "{v26}"(xo), [kd1] "{v27}"(kd[1]), [xrs] "s"(xrs), [v] "s"(v), [wq] "s"(wq), [ors] "s"(ors), [pbe] "i"(PBE)
        : "memory", "scc", "v14", "v15", "v28", "v32", "v33", "v34", "v35", "v36", "v37", "v38", "v39", "v40", "v41", "v42", "v43", "v44", "v45", "v46", "v47", "v48", "v49", "v50", "v51", "v52", "v53", "v54", "v55", "v56", "v57", "v58", "v59", "v60", "v61", "v62", "v63", "v64", "v65", "v66", "v67", "v68", "v69", "v70", "v71", "v72", "v73", "v74", "v75", "v76", "v77", "v78", "v79", "v80", "v81", "v82", "v83", "v84", "v85", "v86", "v87", "v88", "v89", "v90", "v91", "v152", "v153", "v154", "v155", "v156", "v157", "v158", "v159", "v160", "v161", "v162", "v163", "v164", "v165", "v166", "v167");
    } else {
      asm volatile(
        "ds_read_b128 v[48:51], %[p1] offset:0\nds_read_b128 v[52:55], %[p1] offset:1152\nds_read_b128 v[56:59], %[p1] offset:2304\nds_read_b128 v[68:71], %[ub] offset:512\n"
        "ds_read_b128 v[72:75], %[ub] offset:448\nds_read_b128 v[76:79], %[ub] offset:384\nds_read_b128 v[80:83], %[ubx] offset:288\nds_read_b128 v[84:87], %[ub] offset:224\n"
        "ds_read_b128 v[88:91], %[ub] offset:160\nbuffer_load_dwordx4 v[152:155], %[xo], %[xrs], 0 offen sc1\nv_xor_b32 v14, 32, %[exw]\nv_xor_b32 v15, 64, %[exw]\nv_xor_b32 v28, 96, %[exw]\n"
        "s_waitcnt lgkmcnt(8)\nv_mfma_f32_16x16x32_f16 v[60:63], v[48:51], v[92:95], 0\nv_mfma_f32_16x16x32_f16 v[64:67], v[48:51], v[96:99], 0\n"
        "v_mfma_f32_16x16x32_f16 v[48:51], v[48:51], v[100:103], 0\ns_waitcnt lgkmcnt(7)\nv_mfma_f32_16x16x32_f16 v[92:95], v[52:55], v[92:95], 0\n"
        "v_mfma_f32_16x16x32_f16 v[96:99], v[52:55], v[96:99], 0\nv_mfma_f32_16x16x32_f16 v[52:55], v[52:55], v[100:103], 0\nv_cvt_pk_f16_f32 v48, v48, v49\nv_cvt_pk_f16_f32 v49, v50, v51\n"
        "s_waitcnt lgkmcnt(6)\nv_mfma_f32_16x16x32_f16 v[56:59], v[56:59], v[100:103], 0\nv_cvt_pk_f16_f32 v60, v60, v65\nv_cvt_pk_f16_f32 v61, v62, v67\ns_waitcnt lgkmcnt(5)\n"
        "v_mfma_f32_16x16x32_f16 v[44:47], v[132:135], v[68:71], 0\nds_write_b64 %[zw], v[48:49] offset:128\nds_write_b64 %[xd], v[60:61] offset:0\ns_waitcnt lgkmcnt(6)\n"
        "v_mfma_f32_16x16x32_f16 v[40:43], v[132:135], v[72:75], 0\nds_read_b128 v[68:71], %[ub] offset:96\nv_cvt_pk_f16_f32 v52, v52, v53\ns_waitcnt lgkmcnt(6)\n"
        "v_mfma_f32_16x16x32_f16 v[36:39], v[132:135], v[76:79], 0\nv_cvt_pk_f16_f32 v53, v54, v55\nv_cvt_pk_f16_f32 v92, v92, v97\ns_waitcnt lgkmcnt(5)\n"
        "v_mfma_f32_16x16x32_f16 v[32:35], v[132:135], v[80:83], 0\nv_cvt_pk_f16_f32 v93, v94, v99\nds_write_b64 %[zw], v[52:53] offset:256\n"
        "v_mfma_f32_16x16x32_f16 v[44:47], v[136:139], v[72:75], v[44:47]\nds_write_b64 %[xd], v[92:93] offset:64\nv_cvt_pk_f16_f32 v56, v56, v57\n"
        "v_mfma_f32_16x16x32_f16 v[40:43], v[136:139], v[76:79], v[40:43]\nv_cvt_pk_f16_f32 v57, v58, v59\nds_write_b64 %[zw], v[56:57] offset:384\n"
        "v_mfma_f32_16x16x32_f16 v[36:39], v[136:139], v[80:83], v[36:39]\ns_mov_b64 exec, 1\nds_add_u32 %[cna], %[one] offset:4\ns_mov_b64 exec, -1\ns_waitcnt lgkmcnt(8)\n"
        "v_mfma_f32_16x16x32_f16 v[32:35], v[136:139], v[84:87], v[32:35]\nv_mfma_f32_16x16x32_f16 v[44:47], v[140:143], v[76:79], v[44:47]\n"
        "v_mfma_f32_16x16x32_f16 v[40:43], v[140:143], v[80:83], v[40:43]\nv_mfma_f32_16x16x32_f16 v[36:39], v[140:143], v[84:87], v[36:39]\ns_waitcnt lgkmcnt(7)\n"
        "v_mfma_f32_16x16x32_f16 v[32:35], v[140:143], v[88:91], v[32:35]\nv_mfma_f32_16x16x32_f16 v[44:47], v[144:147], v[80:83], v[44:47]\n"
        "v_mfma_f32_16x16x32_f16 v[40:43], v[144:147], v[84:87], v[40:43]\nds_read_b32 %[pv], %[cna] offset:4\nv_mfma_f32_16x16x32_f16 v[36:39], v[144:147], v[88:91], v[36:39]\ns_waitcnt lgkmcnt(5)\n"
        "v_mfma_f32_16x16x32_f16 v[32:35], v[144:147], v[68:71], v[32:35]\nds_read_b128 v[156:159], %[ue]\ns_cmp_eq_u32 %[v], 3\ns_cbranch_scc1 .LT%=\nds_read_b128 v[136:139], %[ux]\n"
        "ds_read_b64 v[140:141], %[k8a] offset:0\nds_read_b64 v[142:143], %[k8a] offset:8\nds_read_b64 v[144:145], %[k8a] offset:64\nds_read_b64 v[146:147], %[k8a] offset:72\ns_waitcnt lgkmcnt(6)\n"
        "v_readfirstlane_b32 %[s0], %[pv]\ns_nop 0\ns_cmp_ge_i32 %[s0], 4\ns_cbranch_scc0 .LG%=\n.LH%=:\nds_read_b128 v[96:99], %[xr] offset:0\nds_read_b128 v[100:103], %[xr] offset:32\n"
        "ds_read_b128 v[48:51], %[zr] offset:0\nds_read_b128 v[52:55], %[zr] offset:32\nds_read_b128 v[56:59], %[zr] offset:64\nds_read_b128 v[60:63], %[zr] offset:96\n"
        "ds_read_b128 v[64:67], %[zr] offset:128\nds_read_b128 v[92:95], %[zr] offset:160\ns_mov_b64 exec, 1\nds_add_u32 %[cna], %[one] offset:12\ns_mov_b64 exec, -1\nds_write_b128 v28, v[44:47]\n"
        "ds_write_b128 v15, v[40:43]\nds_write_b128 v14, v[36:39]\nds_write_b128 %[exw], v[32:35]\ns_waitcnt lgkmcnt(15)\nv_mfma_f32_16x16x32_f16 v[132:135], v[128:131], v[136:139], 0\n"
        "s_waitcnt lgkmcnt(12)\nv_mfma_f32_16x16x32_f16 v[132:135], v[96:99], v[140:143], v[132:135]\ns_waitcnt lgkmcnt(11)\nv_mfma_f32_16x16x32_f16 v[132:135], v[100:103], v[144:147], v[132:135]\n"
        "ds_read_b32 %[pv], %[cna] offset:12\ns_waitcnt lgkmcnt(11)\nv_mfma_f32_16x16x32_f16 v[76:79], v[48:51], v[104:107], 0\ns_waitcnt lgkmcnt(10)\n"
        "v_mfma_f32_16x16x32_f16 v[76:79], v[52:55], v[108:111], v[76:79]\ns_waitcnt lgkmcnt(9)\nv_mfma_f32_16x16x32_f16 v[76:79], v[56:59], v[112:115], v[76:79]\ns_waitcnt lgkmcnt(8)\n"
        "v_mfma_f32_16x16x32_f16 v[76:79], v[60:63], v[116:119], v[76:79]\ns_waitcnt lgkmcnt(0)\nv_readfirstlane_b32 %[s0], %[pv]\ns_nop 0\n"
        "v_mfma_f32_16x16x32_f16 v[76:79], v[64:67], v[120:123], v[76:79]\nds_write_b128 %[frw], v[132:135]\ns_cmp_ge_i32 %[s0], 9\nv_mfma_f32_16x16x32_f16 v[76:79], v[92:95], v[124:127], v[76:79]\n"
        "s_cbranch_scc0 .LJa%=\n.LKa%=:\ns_nop 7\nds_write_b128 %[mw], v[76:79]\ns_branch .LE%=\n.LT%=:\nds_read_b128 v[72:75], %[ubx] offset:0\ns_mov_b64 exec, 1\nds_add_u32 %[cna], %[one] offset:12\n"
        "s_mov_b64 exec, -1\nv_mov_b32 v132, 0\nv_mov_b32 v133, 0\nv_mov_b32 v134, 0\nv_mov_b32 v135, 0\nv_mfma_f32_16x16x32_f16 v[44:47], v[148:151], v[84:87], v[44:47]\n"
        "ds_write_b128 %[frw], v[132:135]\nv_mfma_f32_16x16x32_f16 v[40:43], v[148:151], v[88:91], v[40:43]\nv_mfma_f32_16x16x32_f16 v[36:39], v[148:151], v[68:71], v[36:39]\ns_waitcnt lgkmcnt(2)\n"
        "v_mfma_f32_16x16x32_f16 v[32:35], v[148:151], v[72:75], v[32:35]\ns_nop 7\nds_write_b128 v28, v[44:47]\nds_write_b128 v15, v[40:43]\nds_write_b128 v14, v[36:39]\n"
        "ds_write_b128 %[exw], v[32:35]\ns_branch .LE%=\n.LG%=:\ns_mov_b32 %[s1], 0x100000\n.LGx%=:\nds_read_b32 %[pv], %[cna] offset:4\ns_waitcnt lgkmcnt(0)\nv_readfirstlane_b32 %[s0], %[pv]\n"
        "s_nop 0\ns_cmp_ge_i32 %[s0], 4\ns_cbranch_scc1 .LH%=\ns_sub_u32 %[s1], %[s1], 1\ns_cbranch_scc0 .LGx%=\ns_branch .LH%=\n.LJa%=:\ns_mov_b32 %[s1], 0x100000\n.LJax%=:\n"
        "ds_read_b32 %[pv], %[cna] offset:12\ns_waitcnt lgkmcnt(0)\nv_readfirstlane_b32 %[s0], %[pv]\ns_nop 0\ns_cmp_ge_i32 %[s0], 9\ns_cbranch_scc1 .LKa%=\ns_sub_u32 %[s1], %[s1], 1\n"
        "s_cbranch_scc0 .LJax%=\ns_branch .LKa%=\n.LE%=:\ns_waitcnt vmcnt(0)\ns_mov_b64 exec, 1\nds_add_u32 %[cna], %[one] offset:8\ns_mov_b64 exec, -1\ns_setprio %[pbe]\ns_cmp_lt_u32 %[wq], 2\n"
        "s_cbranch_scc0 .LZ%=\ns_waitcnt lgkmcnt(6)\nv_lshlrev_b32 v160, 16, v152\nv_and_b32 v161, 0xffff0000, v152\nv_cvt_f32_f16 v152, v156\nv_lshrrev_b32 v156, 16, v156\n"
        "v_lshlrev_b32 v162, 16, v153\nv_and_b32 v163, 0xffff0000, v153\nv_cvt_f32_f16 v153, v157\nv_lshrrev_b32 v157, 16, v157\nv_lshlrev_b32 v164, 16, v154\nv_and_b32 v165, 0xffff0000, v154\n"
        "v_cvt_f32_f16 v154, v158\nv_lshrrev_b32 v158, 16, v158\nv_lshlrev_b32 v166, 16, v155\nv_and_b32 v167, 0xffff0000, v155\nv_cvt_f32_f16 v155, v159\nv_lshrrev_b32 v159, 16, v159\n"
        "v_cvt_f32_f16 v156, v156\nv_fmac_f32 v160, v152, %[kd1]\nv_cvt_f32_f16 v157, v157\nv_fmac_f32 v162, v153, %[kd1]\nv_cvt_f32_f16 v158, v158\nv_fmac_f32 v164, v154, %[kd1]\n"
        "v_cvt_f32_f16 v159, v159\nv_fmac_f32 v166, v155, %[kd1]\nv_fmac_f32 v161, v156, %[kd1]\nv_fmac_f32 v163, v157, %[kd1]\nv_fmac_f32 v165, v158, %[kd1]\nv_fmac_f32 v167, v159, %[kd1]\n"
        "s_mov_b32 %[s1], 0x100000\n.LP%=:\nds_read_b32 %[pv], %[cna] offset:8\ns_waitcnt lgkmcnt(0)\nv_readfirstlane_b32 %[s0], %[pv]\ns_nop 0\ns_cmp_ge_i32 %[s0], 4\ns_cbranch_scc1 .LR%=\n"
        "s_sub_u32 %[s1], %[s1], 1\ns_cbranch_scc0 .LP%=\n.LR%=:\nds_read_b128 v[96:99], %[exr] offset:0\nds_read_b128 v[100:103], %[exr] offset:2048\nds_read_b128 v[104:107], %[exr] offset:4096\n"
        "ds_read_b128 v[108:111], %[exr] offset:6144\nds_read_b128 v[112:115], %[exr] offset:8192\nds_read_b128 v[116:119], %[exr] offset:10240\nds_read_b128 v[120:123], %[exr] offset:12288\n"
        "ds_read_b128 v[124:127], %[exr] offset:14336\nds_read_b128 v[128:131], %[mr] offset:0\nds_read_b128 v[132:135], %[mr] offset:3072\ns_cmp_lt_u32 %[wq], 1\ns_cbranch_scc1 .LQ%=\n"
        "ds_read_b128 v[136:139], %[frr] offset:0\nds_read_b128 v[140:143], %[frr] offset:1152\nds_read_b128 v[144:147], %[frr] offset:2304\nds_read_b128 v[148:151], %[frr] offset:3456\n.LQ%=:\n"
        "s_waitcnt lgkmcnt(0)\nv_add_f32 v96, v96, v104\nv_add_f32 v97, v97, v105\nv_add_f32 v98, v98, v106\nv_add_f32 v99, v99, v107\nv_add_f32 v100, v100, v108\nv_add_f32 v101, v101, v109\n"
        "v_add_f32 v102, v102, v110\nv_add_f32 v103, v103, v111\nv_add_f32 v112, v112, v120\nv_add_f32 v113, v113, v121\nv_add_f32 v114, v114, v122\nv_add_f32 v115, v115, v123\n"
        "v_add_f32 v116, v116, v124\nv_add_f32 v117, v117, v125\nv_add_f32 v118, v118, v126\nv_add_f32 v119, v119, v127\nv_fmac_f32 v96, v128, %[fm4]\nv_fmac_f32 v97, v129, %[fm4]\n"
        "v_fmac_f32 v98, v130, %[fm4]\nv_fmac_f32 v99, v131, %[fm4]\nv_fmac_f32 v100, v132, %[fm4]\nv_fmac_f32 v101, v133, %[fm4]\nv_fmac_f32 v102, v134, %[fm4]\nv_fmac_f32 v103, v135, %[fm4]\n"
        "s_cmp_eq_u32 %[wq], 0\nv_add_f32 v96, v96, v112\nv_add_f32 v97, v97, v113\nv_add_f32 v98, v98, v114\nv_add_f32 v99, v99, v115\nv_add_f32 v100, v100, v116\nv_add_f32 v101, v101, v117\n"
        "v_add_f32 v102, v102, v118\nv_add_f32 v103, v103, v119\ns_cbranch_scc1 .LS%=\nv_add_f32 v136, v136, v144\nv_add_f32 v137, v137, v145\nv_add_f32 v138, v138, v146\nv_add_f32 v139, v139, v147\n"
        "v_add_f32 v140, v140, v148\nv_add_f32 v141, v141, v149\nv_add_f32 v142, v142, v150\nv_add_f32 v143, v143, v151\nv_add_f32 v96, v96, v136\nv_add_f32 v97, v97, v137\nv_add_f32 v98, v98, v138\n"
        "v_add_f32 v99, v99, v139\nv_add_f32 v100, v100, v140\nv_add_f32 v101, v101, v141\nv_add_f32 v102, v102, v142\nv_add_f32 v103, v103, v143\n.LS%=:\nv_fmac_f32 v160, v96, %[kf]\n"
        "v_fmac_f32 v161, v97, %[kf]\nv_fmac_f32 v162, v98, %[kf]\nv_fmac_f32 v163, v99, %[kf]\nv_fmac_f32 v164, v100, %[kf]\nv_fmac_f32 v165, v101, %[kf]\nv_fmac_f32 v166, v102, %[kf]\n"
        "v_fmac_f32 v167, v103, %[kf]\nv_cvt_pk_bf16_f32 v104, v160, v161\nv_cvt_pk_bf16_f32 v105, v162, v163\nv_cvt_pk_bf16_f32 v106, v164, v165\nv_cvt_pk_bf16_f32 v107, v166, v167\n"
        "v_min_u32 v104, -2, v104\nv_min_u32 v105, -2, v105\nv_min_u32 v106, -2, v106\nv_min_u32 v107, -2, v107\nbuffer_store_dwordx4 v[104:107], %[od], %[ors], 0 offen sc0 sc1\n"
        "buffer_store_dwordx4 v[104:107], %[oh], %[ors], 0 offen sc0 sc1\ns_nop 1\n.LZ%=:\n"
        : "+{v[92:95]}"(kt[0]), "+{v[96:99]}"(kt[1]), "+{v[100:103]}"(kt[2]), "+{v[104:107]}"(kt[3]), "+{v[108:111]}"(kt[4]), "+{v[112:115]}"(kt[5]), "+{v[116:119]}"(kt[6]), "+{v[120:123]}"(kt[7]), "+{v[124:127]}"(kt[8]), "+{v[128:131]}"(kt[9]), "+{v[132:135]}"(kt[10]), "+{v[136:139]}"(kt[11]), "+{v[140:143]}"(kt[12]), "+{v[144:147]}"(kt[13]), "+{v[148:151]}"(kt[14]), [pv] "=&{v16}"(pv), [s0] "=&s"(s0_), [s1] "=&s"(s1_)
        : [ub] "{v1}"(ub), [ubx] "{v2}"(ubx), [p1] "{v3}"(p1), [zw] "{v4}"(zw), [xd] "{v5}"(xd), [zr] "{v6}"(zr), [xr] "{v7}"(xr), [ux] "{v8}"(ux), [mw] "{v9}"(mw), [frw] "{v10}"(frw), [exw] "{v11}"(exw), [cna] "{v12}"(cna), [one] "{v13}"(one), [k8a] "{v23}"(k8a), [exr] "{v17}"(exr), [frr] "{v18}"(frr), [mr] "{v19}"(mr_), [od] "{v20}"(od), [kf] "{v21}"(kd[0]), [oh] "{v22}"(oh), [ue] "{v24}"(ue), [fm4] "{v25}"(fm4), [xo] "{v26}"(xo), [kd1] "{v27}"(kd[1]), [xrs] "s"(xrs), [v] "s"(v), [wq] "s"(wq), [ors] "s"(ors), [pbe] "i"(PBE)
        : "memory", "scc", "v14", "v15", "v28", "v32", "v33", "v34", "v35", "v36", "v37", "v38", "v39", "v40", "v41", "v42", "v43", "v44", "v45", "v46", "v47", "v48", "v49", "v50", "v51", "v52", "v53", "v54", "v55", "v56", "v57", "v58", "v59", "v60", "v61", "v62", "v63", "v64", "v65", "v66", "v67", "v68", "v69", "v70", "v71", "v72", "v73", "v74", "v75", "v76", "v77", "v78", "v79", "v80", "v81", "v82", "v83", "v84", "v85", "v86", "v87", "v88", "v89", "v90", "v91", "v152", "v153", "v154", "v155", "v156", "v157", "v158", "v159", "v160", "v161", "v162", "v163", "v164", "v165", "v166", "v167");
    }
  }
}

template <int layer>
__device__ __forceinline__ void b_phase(unsigned char* smem, const int b, const int t, const u16* xtin, float2* stats, u32* fstat,
                                        u16* xtout, u32x4* mark) {
  const int kid = layer ? 3 : 5;
  const int lane = t & 63, w = __builtin_amdgcn_readfirstlane(t >> 6);
  const int gq = w < 4 ? 2 : (w >> 2) - 1, wq = w & 3;
  const int cG = CHAN(b, gq);
  ST_DECL
  if (w != 3) __builtin_amdgcn_s_setprio(PBU); else __builtin_amdgcn_s_setprio(3);
  if (w < 3) {
    const u32 sya = LDSA(smem) + SY_OFF + 256 * layer;
    lds_poll<SY_FREE, 2>(sya, 8);
    b_tables(smem, w, lane);
    lds_inc<80 + 12>(sya + 16 * w, 1u);
    ST(9);
  }
#define TSARG
  if (gq < 2) b_conv<true, layer>(smem, b, w, gq, wq, cG, xtin, stats, fstat, xtout, mark TSARG);
  else b_conv<false, layer>(smem, b, w, gq, wq, cG, xtin, stats, fstat, xtout, mark TSARG);
  ST(5);
  {
    LANE(lane);
    const int t = 64 * w + lane;
    ST_FLUSH(kid);
  }
}

struct MainArgs { const u16 *x, *wqkv, *wo; unsigned char* ws; u16* out; };
__global__ __launch_bounds__(768, 3) void k_main(MainArgs ma, KgenArgs ka) {
  __shared__ __attribute__((aligned(16))) unsigned char smem[A_LDS + 64];
  const int b_ = BIDX;
  u32x4 sink;
  int sb = b_, t0 = TIDX;
  asm volatile(
      "s_cmp_lt_u32 %1, 16\n\t"
      "s_cbranch_scc0 1f\n\t"
      "v_cmp_gt_u32 vcc, 0x80, %2\n\t"
      "s_and_b64 exec, exec, vcc\n\t"
      "s_getpc_b64 s[40:41]\n\t"
      "s_add_u32 s40, s40, _Z6k_main8MainArgs8KgenArgs@rel32@lo+4\n\t"
      "s_addc_u32 s41, s41, _Z6k_main8MainArgs8KgenArgs@rel32@hi+12\n\t"
      "s_lshl_b32 s42, %1, 11\n\t"
      "s_and_b32 s42, s42, 0x4000\n\t"
      "v_lshl_add_u32 %3, %2, 7, s42\n\t"
      "s_mov_b32 s42, kmain_tail-_Z6k_main8MainArgs8KgenArgs\n\t"
      "global_load_dword %0, %3, s[40:41]\n\t"
      "v_add_u32 %3, 0x8000, %3\n\t"
      "v_min_u32 %4, s42, %3\n\t"
      "global_load_dword %4, %4, s[40:41]\n\t"
      "s_mov_b64 exec, -1\n\t"
      "1:"
      : "=&v"(sink[0]), "+s"(sb), "+v"(t0), "=&v"(sink[1]), "=&v"(sink[2]) :: "scc", "vcc", "s40", "s41", "s42", "memory");
  unsigned char* const ws_ = ma.ws;
  const int w64 = __builtin_amdgcn_readfirstlane(t0) & ~63;
#define GETT(t) asm volatile("v_mbcnt_lo_u32_b32 %0, -1, %1\n\tv_mbcnt_hi_u32_b32 %0, -1, %0" : "=v"(t) : "s"(w64))
#define FRESH int t, b = b_; unsigned char* ws = ws_; GETT(t); asm volatile("" : "+s"(b), "+s"(ws));
#define LAYER(L) { \
    { FRESH unsigned char* ho = ws + WS_O + L * HO_BYTES; \
      a_phase<L>(smem, b, t, L ? (const u16*)(ws + WS_IM1) : ma.x, ma.wqkv, ma.wo, ws, (u16*)ho, (u16*)(ho + HO_X1), (float2*)(ho + HO_ST), ka, sink, tg0, tg1); } \
    { FRESH unsigned char* ho = ws + WS_O + L * HO_BYTES; \
      b_phase<L>(smem, b, t, (u16*)(ho + HO_X1), (float2*)(ho + HO_ST), (u32*)(ho + HO_FS), (u16*)(ws + (L ? WS_FIN : WS_IM1)), (u32x4*)(ws + WS_O + HO_BYTES)); } }
  u32 tg0, tg1;
  LAYER(0)
  LAYER(1)
  {
    FRESH
    const int lane = t & 63, w = __builtin_amdgcn_readfirstlane(t >> 6);
    ST_DECL
    const int i7 = (b >> 3) & 7;
    if (w == 2 || w == 3 || (w == 1 && i7 < 4)) {
      const int rb = 4 * (b & 7) + (b >> 6), cg = i7 + (w == 2 ? 12 : w == 3 ? 4 : 0);
      const __amdgpu_buffer_rsrc_t rs = __builtin_amdgcn_make_buffer_rsrc(ws + WS_FIN + (size_t)(rb * 640 + cg * 32) * 128, 0, 4096, 0x00020000);
      const __amdgpu_buffer_rsrc_t os = __builtin_amdgcn_make_buffer_rsrc(ma.out + (size_t)rb * 64 * D_ + cg * 32, 0, 63 * D_ * 2 + 64, 0x00020000);
      u32 sw = LDSA(smem) + X1T_OFF + (w - 1) * 4608, sr = sw + (8 * (lane >> 4) + ((lane >> 2) & 3)) * 144 + 8 * (lane & 3);
      sw += (lane >> 3) * 144 + (lane & 7) * 16;
      int l16 = lane * 16, oo = (lane & 15) * (D_ * 2) + (lane >> 4) * 16;
      PIN(sw); PIN(sr); PIN(l16); PIN(oo);
      __builtin_amdgcn_s_setprio(3);
      if (w != 1) lds_poll<88, 2>(LDSA(smem) + SY_OFF + 256 + (w == 2 ? 16 : 0), 4);
      if (w == 1) __builtin_amdgcn_s_sleep(TSLEEP - 8); else __builtin_amdgcn_s_sleep(TSLEEP);
      u32x4 v[4];
      int spins = 0;
      _Pragma("unroll 1")
      while (true) {
        asm volatile("" ::: "memory");
        u32 m = 0;
#pragma unroll
        for (int i = 0; i < 4; ++i) v[i] = __builtin_amdgcn_raw_buffer_load_b128(rs, l16, i * 1024, 16);
#pragma unroll
        for (int i = 0; i < 4; ++i) {
          asm volatile("ds_write_b128 %0, %1 offset:%2" :: "v"(sw), "v"(v[i]), "i"(i * 1152) : "memory");
          m = umax3(umax3(m, v[i][0], v[i][1]), v[i][2], v[i][3]);
        }
        if (__builtin_amdgcn_ballot_w64(m == 0xffffffffu) == 0 || ++spins > GCAP) break;
        __builtin_amdgcn_s_sleep(FPOLL);
      }
      RETRY(7, spins);
      ST(1);
      s16x4 f[8];
      asm volatile("ds_read_b64_tr_b16 %0, %8\n\tds_read_b64_tr_b16 %1, %8 offset:576\n\tds_read_b64_tr_b16 %2, %8 offset:32\n\tds_read_b64_tr_b16 %3, %8 offset:608\n\t"
                   "ds_read_b64_tr_b16 %4, %8 offset:64\n\tds_read_b64_tr_b16 %5, %8 offset:640\n\tds_read_b64_tr_b16 %6, %8 offset:96\n\tds_read_b64_tr_b16 %7, %8 offset:672\n\ts_waitcnt lgkmcnt(0)"
                   : "=&v"(f[0]), "=&v"(f[1]), "=&v"(f[2]), "=&v"(f[3]), "=&v"(f[4]), "=&v"(f[5]), "=&v"(f[6]), "=&v"(f[7]) : "v"(sr) : "memory");
#pragma unroll
      for (int i = 0; i < 4; ++i)
        __builtin_amdgcn_raw_buffer_store_b128(__builtin_bit_cast(u32x4, __builtin_shufflevector(f[2 * i], f[2 * i + 1], 0, 1, 2, 3, 4, 5, 6, 7)), os, oo, i * 16 * D_ * 2, OAUX);
    }
    ST(2);
    ST_FLUSH(4);
  }
}
extern "C" __device__ __attribute__((used, noinline)) void kmain_tail() {}
extern "C" void kernel_launch(void* const* d_in, const int* in_sizes, int n_in, void* d_out, int out_size,
                              void* d_ws, size_t ws_size, hipStream_t stream) {
  unsigned char* ws = (unsigned char*)d_ws;
  KgenArgs ka;
  ka.logA = (const u16*)d_in[4]; ka.Aim = (const u16*)d_in[5]; ka.Cre = (const u16*)d_in[6]; ka.Cim = (const u16*)d_in[7];
  ka.logdt = (const u16*)d_in[8]; ka.Dskip = (const u16*)d_in[9];
  MainArgs ma;
  ma.x = (const u16*)d_in[0];
  ma.wqkv = (const u16*)d_in[1]; ma.wo = (const u16*)d_in[2];
  ma.ws = ws; ma.out = (u16*)d_out;
  k_main<<<256, 768, 0, stream>>>(ma, ka);
}
#if defined(__HIP_DEVICE_COMPILE__) && !defined(ALLOW_PK)
#pragma clang attribute pop
#endif
```

```cpp
#if defined(__HIP_DEVICE_COMPILE__) && !defined(ALLOW_PK)
#pragma clang attribute push(__attribute__((target("no-packed-fp32-ops"))), apply_to = function)
#endif
#include <hip/hip_runtime.h>
#include <hip/hip_bf16.h>
#include <stdint.h>

#define BIDX ((int)__builtin_amdgcn_workgroup_id_x())
#define TIDX ((int)__builtin_amdgcn_workitem_id_x())
typedef unsigned short u16;
typedef unsigned int u32;
typedef unsigned long long u64;
typedef __attribute__((ext_vector_type(8))) short bf16x8;
typedef __attribute__((ext_vector_type(4))) short s16x4;
typedef __attribute__((ext_vector_type(4))) float f32x4;
typedef __attribute__((ext_vector_type(2))) float f32x2;
typedef __attribute__((ext_vector_type(2))) __bf16 bf2v;
typedef __attribute__((address_space(3))) s16x4* lds_s16x4_p;
typedef __attribute__((ext_vector_type(8))) u32 u32x8;
typedef __attribute__((ext_vector_type(4))) u32 u32x4;
typedef __attribute__((ext_vector_type(2))) u32 u32x2;
typedef __attribute__((address_space(1))) u32 gu32;
typedef __attribute__((address_space(1))) u64 gu64;

#define S_ 2048
#define XP 2112
#define D_ 640
#define NST 192
#define CHAN(b, q) ((q) == 2 ? (b) >> 1 : 128 + 256 * (q) + (b))

#define WS_WQ  0
#define WS_WOT (WS_WQ + 1920 * 640 * 2)
#define WS_CW  (WS_WOT + 640 * 640 * 2)
#define WS_KP  (WS_CW + 4 * 2048 * 4)
#define WS_IM0 (WS_KP + 640 * 768 * 4)
#define WS_IM1 (WS_IM0 + 32 * 102400)
#define HO_ST (2048 * 640 * 2)
#define HO_X1 (HO_ST + 2048 * 128)
#define HO_FS (HO_X1 + 640 * XP * 2)
#define HO_BYTES (256 * 1392 * 16)
#define WS_O   (WS_IM1 + 32 * 102400)
#define WS_FIN (WS_O + 2 * HO_BYTES)
#define WS_FLG (WS_FIN + 2048 * 640 * 2)
#define FL_W 64
#define FL_M (FL_W + 480 * 64)
#define FL_M2 (FL_M + 384 * 64)
#define FL_END (FL_M2 + 256 * 8)
#define WS_END (WS_FLG + FL_END)

#define RETRY(i, n)
#define ST_DECL
#define ST(i)
#define ST_FLUSH(kid)

__device__ __forceinline__ float bf2f(u16 x) { return __uint_as_float(((u32)x) << 16); }
__device__ __forceinline__ u32 pack2(float a, float b) {
  f32x2 v = {a, b};
  bf2v r = __builtin_convertvector(v, bf2v);
  return __builtin_bit_cast(u32, r);
}
__device__ __forceinline__ float lo_f(u32 w) { return __uint_as_float(w << 16); }
__device__ __forceinline__ float hi_f(u32 w) { return __uint_as_float(w & 0xffff0000u); }
__device__ __forceinline__ float rbf(float f) { return lo_f(pack2(f, f)); }
__device__ __forceinline__ bf16x8 as_frag(uint4 v) {
  union { uint4 u; bf16x8 f; } c; c.u = v; return c.f;
}
__device__ __forceinline__ float2 cmul(float2 a, float2 b) {
  return make_float2(a.x * b.x - a.y * b.y, a.x * b.y + a.y * b.x);
}
__device__ __forceinline__ float2 cexp_scaled(float zr, float zi, float e) {
  float ar = zr * e, ai = zi * e;
  float m = __expf(ar);
  const float INV2PI_HI = 0.15915493667125702f;
  const float INV2PI_LO = 6.4206382432985265e-09f;
  float t = ai * INV2PI_HI;
  float err = fmaf(ai, INV2PI_HI, -t);
  float r = t - rintf(t);
  r = r + (err + ai * INV2PI_LO);
  float s = __builtin_amdgcn_sinf(r), c = __builtin_amdgcn_cosf(r);
  return make_float2(m * c, m * s);
}
__device__ __forceinline__ void cexp2(f32x2 zr, f32x2 zi, float e, f32x2& re, f32x2& im) {
  f32x2 ar = zr * e, ai = zi * e;
  f32x2 m = {__expf(ar.x), __expf(ar.y)};
  const float INV2PI_HI = 0.15915493667125702f;
  const float INV2PI_LO = 6.4206382432985265e-09f;
  f32x2 t = ai * INV2PI_HI;
  f32x2 err = {fmaf(ai.x, INV2PI_HI, -t.x), fmaf(ai.y, INV2PI_HI, -t.y)};
  f32x2 r = t - (f32x2){rintf(t.x), rintf(t.y)};
  r = r + (err + ai * INV2PI_LO);
  re = m * (f32x2){__builtin_amdgcn_cosf(r.x), __builtin_amdgcn_cosf(r.y)};
  im = m * (f32x2){__builtin_amdgcn_sinf(r.x), __builtin_amdgcn_sinf(r.y)};
}
__device__ __forceinline__ float dpp_ror8(float x) {
  return __builtin_bit_cast(float, __builtin_amdgcn_update_dpp(0, __builtin_bit_cast(int, x), 0x128, 0xf, 0xf, false));
}
__device__ __forceinline__ float BCAST0(float x) {
  return __builtin_bit_cast(float, __builtin_amdgcn_update_dpp(0, __builtin_bit_cast(int, x), 0x150, 0xf, 0xf, false));
}
__device__ __forceinline__ float rowsum16(float x) {
  x += __builtin_bit_cast(float, __builtin_amdgcn_update_dpp(0, __builtin_bit_cast(int, x), 0xB1, 0xf, 0xf, false));
  x += __builtin_bit_cast(float, __builtin_amdgcn_update_dpp(0, __builtin_bit_cast(int, x), 0x4E, 0xf, 0xf, false));
  x += __builtin_bit_cast(float, __builtin_amdgcn_update_dpp(0, __builtin_bit_cast(int, x), 0x141, 0xf, 0xf, false));
  x += __builtin_bit_cast(float, __builtin_amdgcn_update_dpp(0, __builtin_bit_cast(int, x), 0x140, 0xf, 0xf, false));
  return x;
}
__device__ __forceinline__ float dpp_xor1(float x) {
  return __builtin_bit_cast(float, __builtin_amdgcn_update_dpp(0, __builtin_bit_cast(int, x), 0xB1, 0xf, 0xf, false));
}
__device__ __forceinline__ bf16x8 tr_frag(const u16* p0, const u16* p1) {
  s16x4 a = __builtin_amdgcn_ds_read_tr16_b64_v4i16((lds_s16x4_p)p0);
  s16x4 b = __builtin_amdgcn_ds_read_tr16_b64_v4i16((lds_s16x4_p)p1);
  return __builtin_shufflevector(a, b, 0, 1, 2, 3, 4, 5, 6, 7);
}

#define X1_P 248
__device__ __forceinline__ float dot2bf(u32 a, u32 b, float c) {
  return __builtin_amdgcn_fdot2_f32_bf16(__builtin_bit_cast(bf2v, a), __builtin_bit_cast(bf2v, b), c, false);
}
__device__ __forceinline__ float xsum16_32(float x) {
  auto s = __builtin_amdgcn_permlane16_swap(__float_as_uint(x), __float_as_uint(x), false, false);
  x = __uint_as_float(s[0]) + __uint_as_float(s[1]);
  auto t = __builtin_amdgcn_permlane32_swap(__float_as_uint(x), __float_as_uint(x), false, false);
  return __uint_as_float(t[0]) + __uint_as_float(t[1]);
}
__device__ __forceinline__ float xmax16_32(float x) {
  auto s = __builtin_amdgcn_permlane16_swap(__float_as_uint(x), __float_as_uint(x), false, false);
  x = fmaxf(__uint_as_float(s[0]), __uint_as_float(s[1]));
  auto t = __builtin_amdgcn_permlane32_swap(__float_as_uint(x), __float_as_uint(x), false, false);
  return fmaxf(__uint_as_float(t[0]), __uint_as_float(t[1]));
}

#define WT " sc0 sc1"
template <class T> __device__ __forceinline__ void st16(void* p, T v) {
  asm volatile("global_store_dwordx4 %0, %1, off" WT "\n\ts_nop 1" :: "v"(p), "v"(v) : "memory");
}
template <class T> __device__ __forceinline__ void st8(void* p, T v) {
  asm volatile("global_store_dwordx2 %0, %1, off" WT :: "v"(p), "v"(v) : "memory");
}

typedef __attribute__((ext_vector_type(8))) _Float16 f16x8;
typedef __attribute__((ext_vector_type(2))) _Float16 f16x2;
__device__ __forceinline__ u32 packh(float a, float b) {
  f32x2 v = {a, b};
  return __builtin_bit_cast(u32, __builtin_convertvector(v, f16x2));
}
__device__ __forceinline__ float lo_h(u32 w) { return (float)__builtin_bit_cast(f16x2, w).x; }
__device__ __forceinline__ float hi_h(u32 w) { return (float)__builtin_bit_cast(f16x2, w).y; }
__device__ __forceinline__ f16x8 as_hfrag(u32 a, u32 b, u32 c, u32 d) { return __builtin_bit_cast(f16x8, (u32x4){a, b, c, d}); }
__device__ __forceinline__ float wave_max(float x) {
  x = fmaxf(x, __builtin_bit_cast(float, __builtin_amdgcn_update_dpp(0, __builtin_bit_cast(int, x), 0xB1, 0xf, 0xf, false)));
  x = fmaxf(x, __builtin_bit_cast(float, __builtin_amdgcn_update_dpp(0, __builtin_bit_cast(int, x), 0x4E, 0xf, 0xf, false)));
  x = fmaxf(x, __builtin_bit_cast(float, __builtin_amdgcn_update_dpp(0, __builtin_bit_cast(int, x), 0x141, 0xf, 0xf, false)));
  x = fmaxf(x, __builtin_bit_cast(float, __builtin_amdgcn_update_dpp(0, __builtin_bit_cast(int, x), 0x140, 0xf, 0xf, false)));
  return xmax16_32(x);
}
template <int T> __device__ __forceinline__ void pow2_scale(float m, float& s, float& inv) {
  int e = (int)(__float_as_uint(m) >> 23);
  e = min(max(e, T + 2), 250);
  s = __uint_as_float((u32)(254 + T - e) << 23);
  inv = __uint_as_float((u32)(e - T) << 23);
}
__device__ __forceinline__ void cexpr(float zl, float e, float zh, float ep, float& re, float& im) {
  const float m = __builtin_amdgcn_exp2f(zl * e);
  const float r = zh * ep;
  re = m * __builtin_amdgcn_cosf(r);
  im = m * __builtin_amdgcn_sinf(r);
}
struct KgenArgs { const u16 *logA, *Aim, *Cre, *Cim, *logdt, *Dskip; };
#define LDSA(p) ((u32)(size_t)(__attribute__((address_space(3))) void*)(p))
#define PIN(x) asm volatile("" : "+v"(x))
template <int OFF> __device__ __forceinline__ void lds_inc(u32 a, u32 one) {
  asm volatile("s_mov_b64 exec, 1\n\tds_add_u32 %0, %1 offset:%2\n\ts_mov_b64 exec, -1" :: "v"(a), "v"(one), "i"(OFF) : "memory");
}
template <int OFF, int SLEEP> __device__ __forceinline__ void lds_poll(u32 a, int target) {
  u32 v; int s, cnt;
  asm volatile(
      "s_mov_b32 %2, 0x100000\n\t"
      "1:\n\t"
      "ds_read_b32 %0, %3 offset:%5\n\t"
      "s_waitcnt lgkmcnt(0)\n\t"
      "v_readfirstlane_b32 %1, %0\n\t"
      "s_nop 0\n\t"
      "s_cmp_ge_i32 %1, %4\n\t"
      "s_cbranch_scc1 2f\n\t"
      "s_sleep %6\n\t"
      "s_sub_u32 %2, %2, 1\n\t"
      "s_cbranch_scc0 1b\n\t"
      "2:\n\t"
      : "=&v"(v), "=&s"(s), "=&s"(cnt) : "v"(a), "s"(target), "i"(OFF), "i"(SLEEP) : "memory", "scc");
}
template <int OFF, int SLEEP> __device__ __forceinline__ void lds_poll2(u32 a, int target, u32& v) {
  int s, cnt;
  asm volatile(
      "v_readfirstlane_b32 %1, %0\n\t"
      "s_mov_b32 %2, 0x100000\n\t"
      "s_cmp_ge_i32 %1, %4\n\t"
      "s_cbranch_scc1 2f\n\t"
      "1:\n\t"
      "ds_read_b32 %0, %3 offset:%5\n\t"
      "s_waitcnt lgkmcnt(0)\n\t"
      "v_readfirstlane_b32 %1, %0\n\t"
      "s_nop 0\n\t"
      "s_cmp_ge_i32 %1, %4\n\t"
      "s_cbranch_scc1 2f\n\t"
      "s_sleep %6\n\t"
      "s_sub_u32 %2, %2, 1\n\t"
      "s_cbranch_scc0 1b\n\t"
      "2:\n\t"
      : "+v"(v), "=&s"(s), "=&s"(cnt) : "v"(a), "s"(target), "i"(OFF), "i"(SLEEP) : "memory", "scc");
}
#define SY_PART 0
#define SY_FREE 4
#define SY_WO 8
#define SY_FIN 24
#define SY_X1 32
#define SY_FS 36
#define SY_WI 40
#define SY_KP 44
#define SY_MK 48
#define SY_RV 52
#define SY_TI 56
#define SY_PG 64
#define SY_X 128
#define SY_STAT 168
#define SY_W 172
#define SY_RED 192
#define SY_Z 224
template <int SLEEP> __device__ __forceinline__ void pg_poll(u32 a, u32 target) {
  int cnt = 1 << 20;
  _Pragma("unroll 1")
  while (true) {
    u32x4 v;
    asm volatile("ds_read_b128 %0, %1 offset:%2\n\ts_waitcnt lgkmcnt(0)" : "=v"(v) : "v"(a), "i"(SY_PG) : "memory");
    if ((u32)__builtin_amdgcn_readfirstlane(min(min(v[0], v[1]), min(v[2], v[3]))) >= target || --cnt < 0) break;
    if (SLEEP) __builtin_amdgcn_s_sleep(SLEEP);
  }
}
__device__ __forceinline__ void kgen_units(int lane, const float* Kd, int nk, f32x4 (&acc)[3]) {
  const int i = lane & 15, kg = lane >> 4;
  const float eE = 8.f * (float)i;
  const int r_ = i & 3;
  const float e0 = (float)((i < 8 ? 2 * r_ : r_ == 0 ? 512 : r_ == 1 ? 516 : 512 * r_) + 32 * (i & 4));
  const float e0p = i < 8 ? e0 : 0.25f * e0;
  typedef __attribute__((address_space(3))) const f32x4* lf4;
  typedef __attribute__((address_space(3))) const u32x4* lu4;
  u32 pp = LDSA(Kd) + 16 * kg, pc = pp + (i < 8 ? 0 : 256);
#pragma unroll 1
  for (int ks = 0; ks < nk; ++ks) {
    PIN(pp); PIN(pc);
    const f32x4 zl = *(lf4)(size_t)pp, zs = ((lf4)(size_t)pc)[16], zh = ((lf4)(size_t)pp)[16];
    const f32x4 lc = ((lf4)(size_t)pp)[48], ph = ((lf4)(size_t)pp)[64];
    const u32x4 ma[2] = {((lu4)(size_t)pc)[80], ((lu4)(size_t)pp)[112]};
    pp += 64; pc += 64;
    float r[4], q[4], gr[4], gi[4];
#pragma unroll
    for (int e = 0; e < 4; ++e) cexpr(zl[e], e0, zs[e], e0p, gr[e], gi[e]);
#pragma unroll
    for (int e = 0; e < 4; ++e) {
      const float m = __builtin_amdgcn_exp2f(fmaf(zl[e], eE, lc[e])), th = fmaf(zh[e], -eE, ph[e]);
      r[e] = m * __builtin_amdgcn_cosf(th);
      q[e] = m * __builtin_amdgcn_sinf(th);
    }
    const f16x8 eh = as_hfrag(packh(r[0], r[1]), packh(r[2], r[3]), packh(q[0], q[1]), packh(q[2], q[3]));
    const u32 g[4] = {packh(gr[0], gr[1]), packh(gr[2], gr[3]), packh(gi[0], gi[1]), packh(gi[2], gi[3])};
    acc[0] = __builtin_amdgcn_mfma_f32_16x16x32_f16(as_hfrag(g[0], g[1], g[2], g[3]), eh, acc[0], 0, 0, 0);
#pragma unroll
    for (int m = 0; m < 2; ++m) {
      u32 d[4];
#pragma unroll
      for (int e = 0; e < 2; ++e) {
        const f16x2 xr = __builtin_bit_cast(f16x2, g[e]), xi = __builtin_bit_cast(f16x2, g[2 + e]);
        const u32 ur = ma[m][e], ui = ma[m][2 + e];
        const f16x2 mr = __builtin_bit_cast(f16x2, ur), mi = __builtin_bit_cast(f16x2, ui);
        d[e] = __builtin_bit_cast(u32, __builtin_elementwise_fma(xr, mr, -(xi * mi)));
        d[2 + e] = __builtin_bit_cast(u32, __builtin_elementwise_fma(xi, mr, xr * mi));
      }
      acc[m + 1] = __builtin_amdgcn_mfma_f32_16x16x32_f16(as_hfrag(d[0], d[1], d[2], d[3]), eh, acc[m + 1], 0, 0, 0);
    }
  }
}
template <int NQ> struct KgenIn { u16 la[NQ], ai[NQ], cr[NQ], ci[NQ], ld, ds; };
template <int NQ, int MODE>
__device__ __forceinline__ void kgen_load(int c, int ks0, int nk, int lane, const KgenArgs& ka, KgenIn<NQ>& in) {
  in.ld = ka.logdt[c];
#pragma unroll
  for (int q = 0; q < NQ; ++q) {
    const int n = 64 * q + lane, gi = c * NST + 16 * ks0 + (n < 16 * nk ? n : 0);
    in.la[q] = ka.logA[gi]; in.ai[q] = ka.Aim[gi]; in.cr[q] = ka.Cre[gi]; in.ci[q] = ka.Cim[gi];
  }
  if (MODE == 1) in.ds = ka.Dskip[c];
}
#define ATC0 1.591508832e-01f
#define ATC1 -5.292989150e-02f
#define ATC2 3.075435195e-02f
#define ATC3 -1.841891266e-02f
#define ATC4 8.268183977e-03f
#define ATC5 -1.824840286e-03f
#define KU 4
__device__ __forceinline__ float kgen_wave(int lane, const KgenIn<1>& in, float* Kd, f32x4 (&acc)[3]) {
#pragma unroll
  for (int m = 0; m < 3; ++m) acc[m] = (f32x4){0.f, 0.f, 0.f, 0.f};
  float cs, csi;
  const u32 za[1] = {in.la[0] | ((u32)in.ai[0] << 16)}, ca[1] = {in.cr[0] | ((u32)in.ci[0] << 16)};
  const float dt = rbf(__expf(bf2f(in.ld)));
  pow2_scale<10>(wave_max(fmaxf(fabsf(bf2f(in.cr[0])), fabsf(bf2f(in.ci[0])))), cs, csi);
  {
    const float zl = (-rbf(__expf(lo_f(za[0]))) * dt) * 1.4426950408889634f, zi = hi_f(za[0]) * dt;
    const float zh = zi * 0.15915494309189535f;
    const float z1 = zh - rintf(zh);
    float ar, ai, br, bi;
    cexpr(zl, 1.0f, z1, 1.0f, ar, ai);
    cexpr(zl, 256.0f, 256.0f * zh - rintf(256.0f * zh), 1.0f, br, bi);
    Kd[lane] = zl;
    Kd[64 + lane] = z1;
    Kd[128 + lane] = 4.0f * zh - rintf(4.0f * zh);
    const float cr = lo_f(ca[0]) * cs, ci = hi_f(ca[0]) * cs, ax = fabsf(cr), ay = fabsf(ci);
    const float tq = fminf(ax, ay) * __builtin_amdgcn_rcpf(fmaxf(fmaxf(ax, ay), 1e-30f)), t2 = tq * tq;
    float an = tq * fmaf(t2, fmaf(t2, fmaf(t2, fmaf(t2, fmaf(t2, ATC5, ATC4), ATC3), ATC2), ATC1), ATC0);
    an = ay > ax ? 0.25f - an : an;
    an = cr < 0.f ? 0.5f - an : an;
    Kd[192 + lane] = 0.5f * __builtin_amdgcn_logf(fmaf(cr, cr, ci * ci));
    Kd[256 + lane] = ci < 0.f ? an : -an;
    u16* kh = (u16*)(Kd + 320) + 8 * (lane >> 2) + (lane & 3);
    const u32 h0 = packh(ar, ai), h1 = packh(br * br - bi * bi, 2.0f * br * bi), h2 = packh(br, bi);
    kh[0] = (u16)h0; kh[4] = (u16)(h0 >> 16);
    kh[128] = (u16)h1; kh[132] = (u16)(h1 >> 16);
    kh[256] = (u16)h2; kh[260] = (u16)(h2 >> 16);
    asm volatile("s_waitcnt lgkmcnt(0)" ::: "memory");
    kgen_units(lane, Kd, KU, acc);
    asm volatile("" ::: "memory");
  }
#pragma unroll
  for (int m = 0; m < 3; ++m) acc[m] *= csi;
  return dt;
}
__device__ __forceinline__ void kgen_fin(int lane, float dt, float dsk, float* Kd, f32x4 (&acc)[3]) {
  const int i = lane & 15, kg = lane >> 4;
  float km = 0.f, ks_, ksi;
#pragma unroll
  for (int m = 0; m < 3; ++m)
#pragma unroll
    for (int r = 0; r < 4; ++r) km = fmaxf(km, fabsf(acc[m][r]));
  pow2_scale<14>(wave_max(km), ks_, ksi);
#pragma unroll
  for (int m = 0; m < 3; ++m) acc[m] *= ks_;
  u16* kq = (u16*)Kd;
  const u32 t0 = packh(acc[2][0], acc[2][1]), t1 = packh(acc[2][2], acc[2][3]);
  if (kg < 2) {
    *(u32x4*)&kq[128 * kg + 8 * i] = (u32x4){packh(acc[0][0], acc[1][0]), packh(acc[0][1], acc[1][1]), packh(acc[0][2], acc[1][2]), packh(acc[0][3], acc[1][3])};
    *(u32x2*)&kq[256 + 64 * kg + 4 * i] = (u32x2){t0, t1};
  } else {
    const u32 s1 = packh(acc[0][2], acc[0][3]);
    *(u32*)&kq[320 + 32 * kg + 2 * i] = packh(acc[0][0], acc[0][1]);
    *(u32*)&kq[384 + 32 * kg + 2 * i] = t0;
    u16* kr = kq + 480 + lane;
    kr[0] = (u16)s1; kr[64] = (u16)(s1 >> 16); kr[32] = (u16)t1; kr[96] = (u16)(t1 >> 16);
    if (lane == 32) kq[640] = (u16)packh(acc[1][3], 0.f);
  }
  if (lane == 0) { Kd[384] = (dt * (2.0f / 256.0f)) * ksi; Kd[385] = dsk * (1.0f / 256.0f); }
}

#define A_R1 105600
#define A_R2 (A_R1 + 80 * X1_P * 2)
#define SY_OFF (A_R2 + 1280)
#define CW_OFF (SY_OFF + 512)
#define KQ_OFF (CW_OFF + 1024)
#define BND_OFF (KQ_OFF + 3 * 3072)
#define WO1_OFF 52800
#define A_LDS (BND_OFF + 4096)
#define VMCNT(n) asm volatile("s_waitcnt vmcnt(" #n ")" ::: "memory")
template <int N> __device__ __forceinline__ void vmwait() { asm volatile("s_waitcnt vmcnt(%0)" :: "i"(N) : "memory"); }
__device__ __forceinline__ void vmwaitn(int n) {
  switch (n) {
    case 0: VMCNT(0); break; case 5: VMCNT(5); break; case 10: VMCNT(10); break; case 15: VMCNT(15); break; default: VMCNT(20); break;
  }
}
__device__ __forceinline__ u32 umax3(u32 a, u32 b, u32 c) { return max(max(a, b), c); }
#define XDEP 2
#define RS0 4
#define RS1 5
#define OSLEEP1 0
#define SSLEEP 11
#define OSLEEP 14
#define PG1B 0
#define PG1 2
#define PG2 2
#define XSLEEP 12
#define WOMODE 2
#define PG1P 3
#define XPOLL 4
#define XEARLY 1
#define WPW 3
#define WSLEEP0 0
#define GCAP (1 << 16)
#define X1T_OFF 130176
#define MARK16 ((u32x4){0xffffffffu, 0xffffffffu, 0xffffffffu, 0xffffffffu})
#define WSLEEP 2
#define IMPOS 2
#define XMPOS 1
#define IMSL 0
#define MSLEEP 32
#define STX(i)
#define STY(i) ST(i)
#define LANE(x) int x; asm volatile("v_mbcnt_lo_u32_b32 %0, -1, 0\n\tv_mbcnt_hi_u32_b32 %0, -1, %0" : "=v"(x))
#define NST0 7
#define NST1 10
#define PF 2
#define PTX 1
#define PTW 1
#define PTA 2
#define WOAT 32
#define FPOS 2
#define PFK 0
#define WARM 0
#define WOSL 0
#define EXP 0
#define XSSL0 24
#define XE 2
#define MSL0 1
#define MSL1 2
#define MSL2 2
#define P0E 2
#define P1F 1
#define MKW 4
#define MKAT 12
#define DQ 5
#define XSAT 20
#define CWAT 4
#define OSLEEP2 0
#define CWW 6
#define PFS 3
#define PTA1 2
#define APS 1
#define OSLEEP3 8
#define PFD 3
#define XSLEEP1 6
#define XJ0 2
#define XJ1 3
#define XSAT0 12
#define PTO 1
#define MKPOS 1
#define PCW 3
#define XSRT 8
#define XJ2 XJ1
#define WOAT0 28
#define OSLEEPB 14
#define XE3 0
#define GREM 0
#define MPW 9
#define XSSL 5
__device__ __forceinline__ f32x4 ln_fin(float s, float ss) {
  const float mean = s * (1.0f / 640.0f);
  const float var = fmaxf(ss * (1.0f / 640.0f) - mean * mean, 0.f);
  const float m = -rbf(mean), r = __builtin_amdgcn_rcpf(rbf(__builtin_amdgcn_sqrtf(rbf(rbf(var) + 1e-5f))));
  return (f32x4){m, m, r, r};
}
template <int layer>
__device__ __forceinline__ void a_phase(unsigned char* smem, const int b, const int t, const u16* xt, const u16* wqkv, const u16* wo, unsigned char* ws,
                                        u16* obuf, u16* xtout, float2* stats, const KgenArgs& ka, u32x4& sink, u32& tg0, u32& tg1) {
  u16* wq = (u16*)(ws + WS_WQ);
  u16* wot = (u16*)(ws + WS_WOT);
  const __amdgpu_buffer_rsrc_t frs = __builtin_amdgcn_make_buffer_rsrc(ws + WS_FLG, 0, FL_END, 0x00020000);
  const int kid = layer ? 2 : 1;
  u16* At = (u16*)smem;
  u16* St = (u16*)(smem + A_R1);
  const int xcd = b & 7, idx = b >> 3;
  const int rb = 8 * (xcd >> 1) + (idx & 7), hg = 4 * (xcd & 1) + (idx >> 3), s0 = rb * 64;
  const int rot = hg >> 1, xs0 = 3 * rot + 2 * (hg & 1);
#define PAIR(p, r) ((p) < 6 ? (p) : 6 + (((p) - 6 + (r)) & 3))
  const int lane = t & 63, w = __builtin_amdgcn_readfirstlane(t >> 6);
  const int l15 = lane & 15, g = lane >> 4;
  u32 sya = LDSA(smem) + SY_OFF + 256 * layer, one = 1u;
  const bf16x8 onesf = as_frag(make_uint4(0x3f803f80u, 0x3f803f80u, 0x3f803f80u, 0x3f803f80u));
  u32x2 tag;
  ST_DECL

  const __amdgpu_buffer_rsrc_t xsrs = __builtin_amdgcn_make_buffer_rsrc(ws + WS_IM0 + layer * 262144, 0, 262144, 0x00020000);
  if (!layer) {
#define XDMA0(P) { const int pc = PAIR(P, rot); \
      const u16* sp = xt + (size_t)(s0 - 16 + (lane >> 3)) * D_ + 8 * ((lane & 7) ^ ((lane >> 3) & 6)) + pc * 64; \
      _Pragma("unroll") for (int i = 0; i < 10; ++i) \
        __builtin_amdgcn_global_load_lds((const u32*)(sp + 8 * i * D_ + (i < 2 && !rb ? 16 * D_ : 0)), (u32*)(At + ((P) * 10 + i) * 512), 16, 0, 0); }
#define WPOLL { \
        __builtin_amdgcn_s_sleep(WSLEEP0); \
        int spins = 0; \
        _Pragma("unroll 1") \
        while (true) { \
          asm volatile("" ::: "memory"); \
          const u32x2 f = __builtin_amdgcn_raw_buffer_load_b64(frs, min(lane, 59) * 64, FL_W + hg * 3840, 16); \
          if (__builtin_amdgcn_ballot_w64(f[0] != tg0 || f[1] != tg1) == 0 || ++spins > GCAP) break; \
          __builtin_amdgcn_s_sleep(WSLEEP); \
        } \
        RETRY(0, spins); \
        lds_inc<SY_W>(sya, one); \
      }
    if (__builtin_expect(w < 2, 1)) {
      const int r6 = (lane * 205) >> 11, c10 = lane - 10 * r6;
      const int q = (lane >> 2) & 3, p4 = lane & 3;
      __builtin_amdgcn_s_setprio(3);
      const bool tw = b >= 16;
      const int j = 2 * b - 32 + w, jq = j / 3, part = j - 3 * jq, jk = jq >> 3, jh = jq & 7;
      if (__builtin_expect(tw, 1)) {
        const u16* src = wqkv + (size_t)(32 * jk + r6) * 1920 + part * 640 + jh * 80 + 8 * c10;
        u16* tl = St + 13952 + 2560 * w;
        if (lane < 60) {
#pragma unroll
          for (int i = 0; i < 5; ++i) __builtin_amdgcn_global_load_lds((const u32*)(src + i * 11520), (u32*)(tl + i * 480), 16, 0, 0);
          if (lane < 20) __builtin_amdgcn_global_load_lds((const u32*)(src + 5 * 11520), (u32*)(tl + 5 * 480), 16, 0, 0);
        }
      }
      asm volatile("buffer_load_dwordx2 %0, off, %1, 0 sc1" : "=v"(tag) : "s"(frs) : "memory");
      __builtin_amdgcn_s_barrier();
      const int pb = l15 + (part ? 40 * part - 40 : 80), pd = part ? 120 : 0;
      int vo[5];
#pragma unroll
      for (int ct = 0; ct < 5; ++ct) {
        const int col = pb + 16 * ct + (16 * ct + l15 >= 40 ? pd : 0);
        vo[ct] = (int)((0xeda6273b51c9840ull >> (4 * (col >> 4))) & 15) * 1024 + (g * 16 + (col & 15)) * 16;
        PIN(vo[ct]);
      }
      int co = lane * 4;
      u32 ta = LDSA(St + 13952 + 2560 * w) + (8 * g + q) * 160 + 8 * p4;
      PIN(ta); PIN(co);
      asm volatile("s_waitcnt vmcnt(0)" : "+v"(tag), "+v"(sink) :: "memory");
      tag[0] += 1u;
      if (__builtin_expect(tw, 1)) {
        const int kp = 2 * PAIR(jk >> 1, -(jh >> 1)) + (jk & 1);
        const __amdgpu_buffer_rsrc_t trs = __builtin_amdgcn_make_buffer_rsrc((void*)(wq + (size_t)jh * 153600 + kp * 7680), 0, 15360, 0x00020000);
        const __amdgpu_buffer_rsrc_t crs = __builtin_amdgcn_make_buffer_rsrc(ws + WS_CW + ((jh * 20 + jk) * 3 + part) * 384, 0, 384, 0x00020000);
        s16x4 fl[5], fh[5];
        asm volatile(
            "ds_read_b64_tr_b16 %0, %10\n\t"
            "ds_read_b64_tr_b16 %5, %10 offset:640\n\t"
            "ds_read_b64_tr_b16 %1, %10 offset:32\n\t"
            "ds_read_b64_tr_b16 %6, %10 offset:672\n\t"
            "ds_read_b64_tr_b16 %2, %10 offset:64\n\t"
            "ds_read_b64_tr_b16 %7, %10 offset:704\n\t"
            "ds_read_b64_tr_b16 %3, %10 offset:96\n\t"
            "ds_read_b64_tr_b16 %8, %10 offset:736\n\t"
            "ds_read_b64_tr_b16 %4, %10 offset:128\n\t"
            "ds_read_b64_tr_b16 %9, %10 offset:768\n\t"
            "s_waitcnt lgkmcnt(0)"
            : "=&v"(fl[0]), "=&v"(fl[1]), "=&v"(fl[2]), "=&v"(fl[3]), "=&v"(fl[4]),
              "=&v"(fh[0]), "=&v"(fh[1]), "=&v"(fh[2]), "=&v"(fh[3]), "=&v"(fh[4])
            : "v"(ta) : "memory");
        ST(1);
        float cs[5];
#pragma unroll
        for (int ct = 0; ct < 5; ++ct) {
          const bf16x8 fr = __builtin_shufflevector(fl[ct], fh[ct], 0, 1, 2, 3, 4, 5, 6, 7);
          asm volatile("buffer_store_dwordx4 %0, %1, %2, 0 offen sc1\n\ts_nop 1" :: "v"(fr), "v"(vo[ct]), "s"(trs) : "memory");
          cs[ct] = __builtin_amdgcn_mfma_f32_16x16x32_bf16(onesf, fr, (f32x4){0.f, 0.f, 0.f, 0.f}, 0, 0, 0)[0];
        }
        const float c03 = g < 2 ? (g ? cs[1] : cs[0]) : (g == 2 ? cs[2] : cs[3]), c4 = g ? cs[0] : cs[4];
        asm volatile("buffer_store_dword %0, %1, %2, 0 offen sc1\n\tbuffer_store_dword %3, %1, %2, 0 offen offset:256 sc1" :: "v"(c03), "v"(co), "s"(crs), "v"(c4) : "memory");
      }
      lds_inc<SY_TI>(sya, one);
      tg0 = __builtin_amdgcn_readfirstlane(tag[0]); tg1 = __builtin_amdgcn_readfirstlane(tag[1]);
      ST(3);
      if (FPOS) XDMA0(2 + w)
      if (tw) {
        if (FPOS) VMCNT(10); else VMCNT(0);
        if (lane == 0) asm volatile("buffer_store_dwordx2 %0, off, %1, %2 sc1" :: "v"(tag), "s"(frs), "s"(FL_W + (jh * 60 + jk * 3 + part) * 64) : "memory");
      }
      ST(7);
      if (!FPOS) XDMA0(2 + w)
      XDMA0(5 + w)
      XDMA0(8 + w)
      if (w == 0) {
        if (WPW == 0) WPOLL
        VMCNT(0);
        lds_inc<SY_X>(sya, one);
      } else {
        const __amdgpu_buffer_rsrc_t mrs = __builtin_amdgcn_make_buffer_rsrc(obuf, 0, HO_BYTES, 0x00020000);
        _Pragma("unroll 1")
        for (int i = 0; i < 11; ++i) {
          __builtin_amdgcn_raw_buffer_store_b128(MARK16, mrs, lane * 16, i < 10 ? (rb * 8 + hg) * 10240 + i * 1024 : HO_ST + rb * 8192 + hg * 1024, 17);
          __builtin_amdgcn_s_sleep(MSL0);
        }
        VMCNT(11);
        lds_inc<SY_X>(sya, one);
        ST(6);
        VMCNT(0);
        if (lane == 0) asm volatile("buffer_store_dwordx2 %0, off, %1, %2 sc1" :: "v"(tag), "s"(frs), "s"(FL_M + b * 64) : "memory");
      }
      ST(9);
    } else if (w == 2) {
      const int r6 = (lane * 205) >> 11, c10 = lane - 10 * r6, q = (lane >> 2) & 3, p4 = lane & 3;
      const bool to = b < 128;
      __builtin_amdgcn_s_setprio(3);
      asm volatile("buffer_load_dwordx2 %0, off, %1, 0 sc1" : "=v"(tag) : "s"(frs) : "memory");
      XDMA0(0)
      __builtin_amdgcn_s_barrier();
      XDMA0(1)
      if (to) {
        const u16* src = wo + (size_t)(80 * (b >> 4) + 40 * (b & 1) + r6) * D_ + ((b >> 1) & 7) * 80 + 8 * c10;
        u16* tl = (u16*)(smem + KQ_OFF);
        if (lane < 60) {
#pragma unroll
          for (int i = 0; i < 6; ++i) __builtin_amdgcn_global_load_lds((const u32*)(src + i * 6 * D_), (u32*)(tl + i * 480), 16, 0, 0);
          if (lane < 40) __builtin_amdgcn_global_load_lds((const u32*)(src + 36 * D_), (u32*)(tl + 6 * 480), 16, 0, 0);
        }
      }
      if (XE3) XDMA0(4)
      lds_poll<SY_TI, 1>(sya, 2);
      ST(1);
      if (!XE3) XDMA0(4)
      XDMA0(7)
      asm volatile("s_waitcnt vmcnt(20)" : "+v"(tag) :: "memory");
      tag[0] += 1u;
      tg0 = __builtin_amdgcn_readfirstlane(tag[0]); tg1 = __builtin_amdgcn_readfirstlane(tag[1]);
      if (to) {
        const int s = b >> 4;
        const __amdgpu_buffer_rsrc_t trs = __builtin_amdgcn_make_buffer_rsrc((void*)(wot + (size_t)((b >> 1) & 7) * 51200 + ((b & 1) * 2 + (s >> 2)) * 12800), 0, 25600, 0x00020000);
        const u32 t0 = LDSA(smem) + KQ_OFF + (4 * g + q) * 160 + 8 * p4, t1 = LDSA(smem) + KQ_OFF + (16 + q) * 160 + 8 * p4;
        const int o0 = lane * 16, o1 = g ? -1 : lane * 16;
#pragma unroll
        for (int nt = 0; nt < 5; ++nt) {
          s16x4 f[4];
          const int np = (s < 4 ? (nt + 2) % 5 : nt) * 1024;
          asm volatile("ds_read_b64_tr_b16 %0, %4 offset:%6\n\tds_read_b64_tr_b16 %1, %4 offset:%6+3200\n\tds_read_b64_tr_b16 %2, %5 offset:%6\n\tds_read_b64_tr_b16 %3, %5 offset:%6+3200\n\ts_waitcnt lgkmcnt(0)"
                       : "=&v"(f[0]), "=&v"(f[1]), "=&v"(f[2]), "=&v"(f[3]) : "v"(t0), "v"(t1), "i"(32 * nt) : "memory");
          asm volatile("buffer_store_dwordx4 %0, %2, %4, %5 offen sc1\n\tbuffer_store_dwordx4 %1, %3, %4, %6 offen sc1\n\ts_nop 1"
                       :: "v"(__builtin_shufflevector(f[0], f[1], 0, 1, 2, 3, 4, 5, 6, 7)), "v"(__builtin_shufflevector(f[2], f[3], 0, 1, 2, 3, 4, 5, 6, 7)),
                          "v"(o0), "v"(o1), "s"(trs), "s"((s & 3) * 5120 + np), "s"(20480 + (s & 3) * 256 + np) : "memory");
        }
      }
      ST(3);
      if (to) VMCNT(10); else VMCNT(0);
      lds_inc<SY_X>(sya, one);
      ST(6);
      __builtin_amdgcn_s_waitcnt(0x0070);
      if (to && lane == 0) asm volatile("buffer_store_dwordx2 %0, off, %1, %2 sc1" :: "v"(tag), "s"(frs), "s"(FL_M + (256 + b) * 64) : "memory");
      ST(9);
    } else {
      const int ar = GREM ? (w == 11 ? 5 : w >= 8 ? w - 2 : w - 3) : w - 3, ci = ar % 3;
      KgenIn<1> kin;
      tag = __builtin_amdgcn_raw_buffer_load_b64(frs, 0, 0, 16);
      kgen_load<1, 1>(CHAN(b, ci), 4 * (ar / 3), 4, lane, ka, kin);
      asm volatile("" ::: "memory");
      if (w == 4) asm volatile("ds_write_b64 %0, %1\n\ts_waitcnt lgkmcnt(0)" :: "v"(sya + 8 * lane), "v"((u32x2){0u, 0u}) : "memory");
      __builtin_amdgcn_s_barrier();
      if (w == 8) {
        bf16x8 be, bo, b5, b3;
#pragma unroll
        for (int e = 0; e < 8; ++e) {
          const int d = 8 * g + e - l15, d4 = (g >> 1) == (l15 >> 3) ? d - (l15 & 8) : -1;
          be[e] = (d >= 4 && d <= 12) ? (short)0x3c00 : (short)0;
          bo[e] = (d >= 5 && d <= 11) ? (short)0x3c00 : (short)0;
          b5[e] = (d4 >= 2 && d4 <= 6) ? (short)0x3c00 : (short)0;
          b3[e] = (d4 >= 3 && d4 <= 5) ? (short)0x3c00 : (short)0;
        }
        asm volatile("ds_write_b128 %0, %1\n\tds_write_b128 %0, %2 offset:1024\n\tds_write_b128 %0, %3 offset:2048\n\tds_write_b128 %0, %4 offset:3072"
                     :: "v"(LDSA(smem) + BND_OFF + 16 * lane), "v"(be), "v"(bo), "v"(b5), "v"(b3) : "memory");
      }
      if (w == 3) __builtin_amdgcn_s_setprio(2); else __builtin_amdgcn_s_setprio(1);
      tg0 = __builtin_amdgcn_readfirstlane(tag[0]) + 1u; tg1 = __builtin_amdgcn_readfirstlane(tag[1]);
      f32x4 kacc[3];
      f32x4* ka_ = (f32x4*)(St + ar * 1536);
      const float dt = kgen_wave(lane, kin, (float*)ka_, kacc);
      if (w == 3) ST(6); else ST(4);
      if (ar < 6) {
#pragma unroll
        for (int m = 0; m < 3; ++m) ka_[64 * m + lane] = kacc[m];
        lds_inc<SY_KP>(sya, one);
      } else {
        const f32x4* pa = (const f32x4*)St + ci * 192 + lane;
        lds_poll<SY_KP, 2>(sya, 6);
#pragma unroll
        for (int m = 0; m < 3; ++m) { kacc[m] += pa[64 * m] + pa[576 + 64 * m]; PIN(kacc[m]); }
        lds_inc<SY_STAT>(sya, one);
        lds_poll<SY_X, 2>(sya, 3);
        kgen_fin(lane, dt, bf2f(kin.ds), (float*)(smem + KQ_OFF + ci * 3072), kacc);
      }
      __builtin_amdgcn_s_waitcnt(0x0070);
    }
  }
  if (w < 4) {
    {
    LANE(lane);
    const int l15 = lane & 15, g = lane >> 4;
    const __amdgpu_buffer_rsrc_t wrsrc = __builtin_amdgcn_make_buffer_rsrc((void*)(wq + (size_t)hg * 153600), 0, 307200, 0x00020000);
    const int tb = w == 1 ? 1 : 0;
    u32 wa = (w == 3 ? 11 : 4 * w) * 1024 + lane * 16;
    u32 xa = LDSA(At) + (layer ? (4 * g + ((lane >> 2) & 3)) * 32 + 8 * (lane & 3) : (l15 >> 3) * 1024 + (l15 & 7) * 128 + ((g ^ (l15 & 6)) << 4)), xb = xa ^ 64;
    u32 sy = sya + SY_X, pga = sya + SY_PG + 4 * w, cwa = LDSA(smem) + CW_OFF + tb * 64 + g * 16, mra = LDSA(smem) + A_R2 + l15 * 16;
    u32 sta = LDSA(St) + (l15 * X1_P + tb * 16 + 4 * g) * 2, pv;
    int so, s0_, s1_;
    if (!layer && WPW && w == WPW) {
      __builtin_amdgcn_s_setprio(3);
      PIN(wa); PIN(xa); PIN(xb); PIN(sy); PIN(pga); PIN(cwa); PIN(mra); PIN(sta);
      WPOLL
      ST(10);
    } else __builtin_amdgcn_s_setprio(PF);
    if (!layer) {
      asm volatile(
        "s_cmp_lt_u32 %[w], 2\ns_cbranch_scc0 .LB%=\ns_mov_b32 %[s1], 0x100000\n.LA0w%=:\nds_read_b32 %[pv], %[sy] offset:%[ow]\ns_waitcnt lgkmcnt(0)\nv_readfirstlane_b32 %[s0], %[pv]\ns_nop 0\n"
        "s_cmp_ge_i32 %[s0], 1\ns_cbranch_scc1 .LA0h%=\ns_sleep 1\ns_sub_u32 %[s1], %[s1], 1\ns_cbranch_scc0 .LA0w%=\n.LA0h%=:\ns_mov_b32 %[so], 0\n"
        "buffer_load_dwordx4 v[32:35], %[wa], %[rs], %[so] offen offset:0 sc1\nbuffer_load_dwordx4 v[36:39], %[wa], %[rs], %[so] offen offset:1024 sc1\n"
        "buffer_load_dwordx4 v[40:43], %[wa], %[rs], %[so] offen offset:2048 sc1\nbuffer_load_dwordx4 v[44:47], %[wa], %[rs], %[so] offen offset:3072 sc1\ns_mov_b32 %[so], 15360\n"
        "buffer_load_dwordx4 v[48:51], %[wa], %[rs], %[so] offen offset:0 sc1\nbuffer_load_dwordx4 v[52:55], %[wa], %[rs], %[so] offen offset:1024 sc1\n"
        "buffer_load_dwordx4 v[56:59], %[wa], %[rs], %[so] offen offset:2048 sc1\nbuffer_load_dwordx4 v[60:63], %[wa], %[rs], %[so] offen offset:3072 sc1\ns_mov_b32 %[so], 30720\n"
        "buffer_load_dwordx4 v[64:67], %[wa], %[rs], %[so] offen offset:0 sc1\nbuffer_load_dwordx4 v[68:71], %[wa], %[rs], %[so] offen offset:1024 sc1\n"
        "buffer_load_dwordx4 v[72:75], %[wa], %[rs], %[so] offen offset:2048 sc1\nbuffer_load_dwordx4 v[76:79], %[wa], %[rs], %[so] offen offset:3072 sc1\ns_mov_b32 %[so], 46080\n"
        "buffer_load_dwordx4 v[80:83], %[wa], %[rs], %[so] offen offset:0 sc1\nbuffer_load_dwordx4 v[84:87], %[wa], %[rs], %[so] offen offset:1024 sc1\n"
        "buffer_load_dwordx4 v[88:91], %[wa], %[rs], %[so] offen offset:2048 sc1\nbuffer_load_dwordx4 v[92:95], %[wa], %[rs], %[so] offen offset:3072 sc1\ns_setprio %[pf]\ns_mov_b32 %[s1], 0x100000\n"
        ".LA0v%=:\nds_read_b32 %[pv], %[sy] offset:0\ns_waitcnt lgkmcnt(0)\nv_readfirstlane_b32 %[s0], %[pv]\ns_nop 0\ns_cmp_ge_i32 %[s0], 3\ns_cbranch_scc1 .LA0g%=\ns_sleep 1\n"
        "s_sub_u32 %[s1], %[s1], 1\ns_cbranch_scc0 .LA0v%=\n.LA0g%=:\nds_read_b128 v[16:19], %[xa] offset:2048\nds_read_b128 v[20:23], %[xa] offset:4096\nds_read_b128 v[12:15], %[xa] offset:0\n"
        "ds_read_b128 v[24:27], %[xa] offset:6144\ns_waitcnt vmcnt(12) lgkmcnt(3)\nv_mfma_f32_16x16x32_bf16 v[100:103], v[32:35], v[16:19], 0\nds_read_b128 v[28:31], %[xa] offset:8192\n"
        "v_mfma_f32_16x16x32_bf16 v[116:119], v[36:39], v[16:19], 0\nv_mfma_f32_16x16x32_bf16 v[136:139], v[40:43], v[16:19], 0\nv_mfma_f32_16x16x32_bf16 v[152:155], v[44:47], v[16:19], 0\n"
        "ds_read_b128 v[16:19], %[xb] offset:2048\ns_waitcnt lgkmcnt(4)\nv_mfma_f32_16x16x32_bf16 v[104:107], v[32:35], v[20:23], 0\nv_mfma_f32_16x16x32_bf16 v[120:123], v[36:39], v[20:23], 0\n"
        "v_mfma_f32_16x16x32_bf16 v[140:143], v[40:43], v[20:23], 0\nv_mfma_f32_16x16x32_bf16 v[156:159], v[44:47], v[20:23], 0\nds_read_b128 v[20:23], %[xb] offset:4096\ns_waitcnt lgkmcnt(4)\n"
        "v_mfma_f32_16x16x32_bf16 v[96:99], v[32:35], v[12:15], 0\nv_mfma_f32_16x16x32_bf16 v[132:135], v[40:43], v[12:15], 0\nds_read_b128 v[12:15], %[xb] offset:0\ns_waitcnt lgkmcnt(4)\n"
        "v_mfma_f32_16x16x32_bf16 v[108:111], v[32:35], v[24:27], 0\nv_mfma_f32_16x16x32_bf16 v[124:127], v[36:39], v[24:27], 0\nv_mfma_f32_16x16x32_bf16 v[144:147], v[40:43], v[24:27], 0\n"
        "v_mfma_f32_16x16x32_bf16 v[160:163], v[44:47], v[24:27], 0\nds_read_b128 v[24:27], %[xb] offset:6144\ns_waitcnt lgkmcnt(4)\nv_mfma_f32_16x16x32_bf16 v[112:115], v[32:35], v[28:31], 0\n"
        "v_mfma_f32_16x16x32_bf16 v[128:131], v[36:39], v[28:31], 0\nv_mfma_f32_16x16x32_bf16 v[148:151], v[40:43], v[28:31], 0\nv_mfma_f32_16x16x32_bf16 v[164:167], v[44:47], v[28:31], 0\n.LA0l%=:\n"
        "s_waitcnt vmcnt(8) lgkmcnt(3)\nv_mfma_f32_16x16x32_bf16 v[100:103], v[48:51], v[16:19], v[100:103]\ns_addk_i32 %[so], 15360\nds_read_b128 v[28:31], %[xb] offset:8192\n"
        "v_mfma_f32_16x16x32_bf16 v[116:119], v[52:55], v[16:19], v[116:119]\nv_mfma_f32_16x16x32_bf16 v[136:139], v[56:59], v[16:19], v[136:139]\n"
        "buffer_load_dwordx4 v[32:35], %[wa], %[rs], %[so] offen offset:0 sc1\nv_mfma_f32_16x16x32_bf16 v[152:155], v[60:63], v[16:19], v[152:155]\nds_read_b128 v[16:19], %[xa] offset:12288\n"
        "s_waitcnt lgkmcnt(4)\nv_mfma_f32_16x16x32_bf16 v[104:107], v[48:51], v[20:23], v[104:107]\nv_mfma_f32_16x16x32_bf16 v[120:123], v[52:55], v[20:23], v[120:123]\n"
        "buffer_load_dwordx4 v[36:39], %[wa], %[rs], %[so] offen offset:1024 sc1\nv_mfma_f32_16x16x32_bf16 v[140:143], v[56:59], v[20:23], v[140:143]\n"
        "v_mfma_f32_16x16x32_bf16 v[156:159], v[60:63], v[20:23], v[156:159]\nds_read_b128 v[20:23], %[xa] offset:14336\ns_waitcnt lgkmcnt(4)\n"
        "v_mfma_f32_16x16x32_bf16 v[96:99], v[48:51], v[12:15], v[96:99]\nv_mfma_f32_16x16x32_bf16 v[132:135], v[56:59], v[12:15], v[132:135]\nds_read_b128 v[12:15], %[xa] offset:10240\n"
        "s_waitcnt lgkmcnt(4)\nv_mfma_f32_16x16x32_bf16 v[108:111], v[48:51], v[24:27], v[108:111]\nbuffer_load_dwordx4 v[40:43], %[wa], %[rs], %[so] offen offset:2048 sc1\n"
        "v_mfma_f32_16x16x32_bf16 v[124:127], v[52:55], v[24:27], v[124:127]\nv_mfma_f32_16x16x32_bf16 v[144:147], v[56:59], v[24:27], v[144:147]\n"
        "buffer_load_dwordx4 v[44:47], %[wa], %[rs], %[so] offen offset:3072 sc1\nv_mfma_f32_16x16x32_bf16 v[160:163], v[60:63], v[24:27], v[160:163]\nds_read_b128 v[24:27], %[xa] offset:16384\n"
        "s_waitcnt lgkmcnt(4)\nv_mfma_f32_16x16x32_bf16 v[112:115], v[48:51], v[28:31], v[112:115]\nv_mfma_f32_16x16x32_bf16 v[128:131], v[52:55], v[28:31], v[128:131]\nv_add_u32 %[sy], 4, %[sy]\n"
        "v_mfma_f32_16x16x32_bf16 v[148:151], v[56:59], v[28:31], v[148:151]\nds_write_b32 %[pg], %[sy]\nv_mfma_f32_16x16x32_bf16 v[164:167], v[60:63], v[28:31], v[164:167]\n"
        "s_waitcnt vmcnt(8) lgkmcnt(4)\nv_mfma_f32_16x16x32_bf16 v[100:103], v[64:67], v[16:19], v[100:103]\ns_addk_i32 %[so], 15360\nds_read_b128 v[28:31], %[xa] offset:18432\n"
        "v_mfma_f32_16x16x32_bf16 v[116:119], v[68:71], v[16:19], v[116:119]\nv_mfma_f32_16x16x32_bf16 v[136:139], v[72:75], v[16:19], v[136:139]\n"
        "buffer_load_dwordx4 v[48:51], %[wa], %[rs], %[so] offen offset:0 sc1\nv_mfma_f32_16x16x32_bf16 v[152:155], v[76:79], v[16:19], v[152:155]\nds_read_b128 v[16:19], %[xb] offset:12288\n"
        "s_waitcnt lgkmcnt(5)\nv_mfma_f32_16x16x32_bf16 v[104:107], v[64:67], v[20:23], v[104:107]\nv_mfma_f32_16x16x32_bf16 v[120:123], v[68:71], v[20:23], v[120:123]\n"
        "buffer_load_dwordx4 v[52:55], %[wa], %[rs], %[so] offen offset:1024 sc1\nv_mfma_f32_16x16x32_bf16 v[140:143], v[72:75], v[20:23], v[140:143]\n"
        "v_mfma_f32_16x16x32_bf16 v[156:159], v[76:79], v[20:23], v[156:159]\nds_read_b128 v[20:23], %[xb] offset:14336\ns_waitcnt lgkmcnt(5)\n"
        "v_mfma_f32_16x16x32_bf16 v[96:99], v[64:67], v[12:15], v[96:99]\nv_mfma_f32_16x16x32_bf16 v[132:135], v[72:75], v[12:15], v[132:135]\nds_read_b128 v[12:15], %[xb] offset:10240\n"
        "s_waitcnt lgkmcnt(5)\nv_mfma_f32_16x16x32_bf16 v[108:111], v[64:67], v[24:27], v[108:111]\nbuffer_load_dwordx4 v[56:59], %[wa], %[rs], %[so] offen offset:2048 sc1\n"
        "v_mfma_f32_16x16x32_bf16 v[124:127], v[68:71], v[24:27], v[124:127]\nv_mfma_f32_16x16x32_bf16 v[144:147], v[72:75], v[24:27], v[144:147]\n"
        "buffer_load_dwordx4 v[60:63], %[wa], %[rs], %[so] offen offset:3072 sc1\nv_mfma_f32_16x16x32_bf16 v[160:163], v[76:79], v[24:27], v[160:163]\nds_read_b128 v[24:27], %[xb] offset:16384\n"
        "s_waitcnt lgkmcnt(4)\nv_mfma_f32_16x16x32_bf16 v[112:115], v[64:67], v[28:31], v[112:115]\nv_mfma_f32_16x16x32_bf16 v[128:131], v[68:71], v[28:31], v[128:131]\n"
        "v_mfma_f32_16x16x32_bf16 v[148:151], v[72:75], v[28:31], v[148:151]\nv_mfma_f32_16x16x32_bf16 v[164:167], v[76:79], v[28:31], v[164:167]\ns_waitcnt vmcnt(8) lgkmcnt(3)\n"
        "v_mfma_f32_16x16x32_bf16 v[100:103], v[80:83], v[16:19], v[100:103]\ns_addk_i32 %[so], 15360\nds_read_b128 v[28:31], %[xb] offset:18432\n"
        "v_mfma_f32_16x16x32_bf16 v[116:119], v[84:87], v[16:19], v[116:119]\nv_mfma_f32_16x16x32_bf16 v[136:139], v[88:91], v[16:19], v[136:139]\n"
        "buffer_load_dwordx4 v[64:67], %[wa], %[rs], %[so] offen offset:0 sc1\nv_mfma_f32_16x16x32_bf16 v[152:155], v[92:95], v[16:19], v[152:155]\nds_read_b128 v[16:19], %[xa] offset:22528\n"
        "s_waitcnt lgkmcnt(4)\nv_mfma_f32_16x16x32_bf16 v[104:107], v[80:83], v[20:23], v[104:107]\nv_mfma_f32_16x16x32_bf16 v[120:123], v[84:87], v[20:23], v[120:123]\n"
        "buffer_load_dwordx4 v[68:71], %[wa], %[rs], %[so] offen offset:1024 sc1\nv_mfma_f32_16x16x32_bf16 v[140:143], v[88:91], v[20:23], v[140:143]\n"
        "v_mfma_f32_16x16x32_bf16 v[156:159], v[92:95], v[20:23], v[156:159]\nds_read_b128 v[20:23], %[xa] offset:24576\ns_waitcnt lgkmcnt(4)\n"
        "v_mfma_f32_16x16x32_bf16 v[96:99], v[80:83], v[12:15], v[96:99]\nv_mfma_f32_16x16x32_bf16 v[132:135], v[88:91], v[12:15], v[132:135]\nds_read_b128 v[12:15], %[xa] offset:20480\n"
        "s_waitcnt lgkmcnt(4)\nv_mfma_f32_16x16x32_bf16 v[108:111], v[80:83], v[24:27], v[108:111]\nbuffer_load_dwordx4 v[72:75], %[wa], %[rs], %[so] offen offset:2048 sc1\n"
        "v_mfma_f32_16x16x32_bf16 v[124:127], v[84:87], v[24:27], v[124:127]\nv_mfma_f32_16x16x32_bf16 v[144:147], v[88:91], v[24:27], v[144:147]\n"
        "buffer_load_dwordx4 v[76:79], %[wa], %[rs], %[so] offen offset:3072 sc1\nv_mfma_f32_16x16x32_bf16 v[160:163], v[92:95], v[24:27], v[160:163]\nds_read_b128 v[24:27], %[xa] offset:26624\n"
        "s_waitcnt lgkmcnt(4)\nv_mfma_f32_16x16x32_bf16 v[112:115], v[80:83], v[28:31], v[112:115]\nv_mfma_f32_16x16x32_bf16 v[128:131], v[84:87], v[28:31], v[128:131]\nv_add_u32 %[sy], 4, %[sy]\n"
        "v_mfma_f32_16x16x32_bf16 v[148:151], v[88:91], v[28:31], v[148:151]\nds_write_b32 %[pg], %[sy]\nv_mfma_f32_16x16x32_bf16 v[164:167], v[92:95], v[28:31], v[164:167]\n"
        "s_waitcnt vmcnt(8) lgkmcnt(4)\nv_mfma_f32_16x16x32_bf16 v[100:103], v[32:35], v[16:19], v[100:103]\ns_addk_i32 %[so], 15360\nds_read_b128 v[28:31], %[xa] offset:28672\n"
        "v_mfma_f32_16x16x32_bf16 v[116:119], v[36:39], v[16:19], v[116:119]\nv_mfma_f32_16x16x32_bf16 v[136:139], v[40:43], v[16:19], v[136:139]\n"
        "buffer_load_dwordx4 v[80:83], %[wa], %[rs], %[so] offen offset:0 sc1\nv_mfma_f32_16x16x32_bf16 v[152:155], v[44:47], v[16:19], v[152:155]\nds_read_b128 v[16:19], %[xb] offset:22528\n"
        "s_waitcnt lgkmcnt(5)\nv_mfma_f32_16x16x32_bf16 v[104:107], v[32:35], v[20:23], v[104:107]\nv_mfma_f32_16x16x32_bf16 v[120:123], v[36:39], v[20:23], v[120:123]\n"
        "buffer_load_dwordx4 v[84:87], %[wa], %[rs], %[so] offen offset:1024 sc1\nv_mfma_f32_16x16x32_bf16 v[140:143], v[40:43], v[20:23], v[140:143]\n"
        "v_mfma_f32_16x16x32_bf16 v[156:159], v[44:47], v[20:23], v[156:159]\nds_read_b128 v[20:23], %[xb] offset:24576\ns_waitcnt lgkmcnt(5)\n"
        "v_mfma_f32_16x16x32_bf16 v[96:99], v[32:35], v[12:15], v[96:99]\nv_mfma_f32_16x16x32_bf16 v[132:135], v[40:43], v[12:15], v[132:135]\nds_read_b128 v[12:15], %[xb] offset:20480\n"
        "s_waitcnt lgkmcnt(5)\nv_mfma_f32_16x16x32_bf16 v[108:111], v[32:35], v[24:27], v[108:111]\nbuffer_load_dwordx4 v[88:91], %[wa], %[rs], %[so] offen offset:2048 sc1\n"
        "v_mfma_f32_16x16x32_bf16 v[124:127], v[36:39], v[24:27], v[124:127]\nv_mfma_f32_16x16x32_bf16 v[144:147], v[40:43], v[24:27], v[144:147]\n"
        "buffer_load_dwordx4 v[92:95], %[wa], %[rs], %[so] offen offset:3072 sc1\nv_mfma_f32_16x16x32_bf16 v[160:163], v[44:47], v[24:27], v[160:163]\nds_read_b128 v[24:27], %[xb] offset:26624\n"
        "s_waitcnt lgkmcnt(4)\nv_mfma_f32_16x16x32_bf16 v[112:115], v[32:35], v[28:31], v[112:115]\nv_mfma_f32_16x16x32_bf16 v[128:131], v[36:39], v[28:31], v[128:131]\n"
        "v_mfma_f32_16x16x32_bf16 v[148:151], v[40:43], v[28:31], v[148:151]\ns_cmp_lg_u32 %[so], 291840\nv_mfma_f32_16x16x32_bf16 v[164:167], v[44:47], v[28:31], v[164:167]\n"
        "v_add_u32 %[xa], 20480, %[xa]\nv_add_u32 %[xb], 20480, %[xb]\ns_cbranch_scc1 .LA0l%=\ns_waitcnt vmcnt(8) lgkmcnt(3)\nv_mfma_f32_16x16x32_bf16 v[100:103], v[48:51], v[16:19], v[100:103]\n"
        "ds_read_b128 v[28:31], %[xb] offset:8192\nv_mfma_f32_16x16x32_bf16 v[116:119], v[52:55], v[16:19], v[116:119]\nv_mfma_f32_16x16x32_bf16 v[136:139], v[56:59], v[16:19], v[136:139]\n"
        "v_mfma_f32_16x16x32_bf16 v[152:155], v[60:63], v[16:19], v[152:155]\nds_read_b128 v[16:19], %[xa] offset:12288\ns_waitcnt lgkmcnt(4)\n"
        "v_mfma_f32_16x16x32_bf16 v[104:107], v[48:51], v[20:23], v[104:107]\nv_mfma_f32_16x16x32_bf16 v[120:123], v[52:55], v[20:23], v[120:123]\n"
        "v_mfma_f32_16x16x32_bf16 v[140:143], v[56:59], v[20:23], v[140:143]\nv_mfma_f32_16x16x32_bf16 v[156:159], v[60:63], v[20:23], v[156:159]\nds_read_b128 v[20:23], %[xa] offset:14336\n"
        "s_waitcnt lgkmcnt(4)\nv_mfma_f32_16x16x32_bf16 v[96:99], v[48:51], v[12:15], v[96:99]\nv_mfma_f32_16x16x32_bf16 v[132:135], v[56:59], v[12:15], v[132:135]\n"
        "ds_read_b128 v[12:15], %[xa] offset:10240\ns_waitcnt lgkmcnt(4)\nv_mfma_f32_16x16x32_bf16 v[108:111], v[48:51], v[24:27], v[108:111]\n"
        "v_mfma_f32_16x16x32_bf16 v[124:127], v[52:55], v[24:27], v[124:127]\nv_mfma_f32_16x16x32_bf16 v[144:147], v[56:59], v[24:27], v[144:147]\n"
        "v_mfma_f32_16x16x32_bf16 v[160:163], v[60:63], v[24:27], v[160:163]\nds_read_b128 v[24:27], %[xa] offset:16384\ns_waitcnt lgkmcnt(4)\n"
        "v_mfma_f32_16x16x32_bf16 v[112:115], v[48:51], v[28:31], v[112:115]\nv_mfma_f32_16x16x32_bf16 v[128:131], v[52:55], v[28:31], v[128:131]\nv_add_u32 %[sy], 4, %[sy]\n"
        "v_mfma_f32_16x16x32_bf16 v[148:151], v[56:59], v[28:31], v[148:151]\nv_mfma_f32_16x16x32_bf16 v[164:167], v[60:63], v[28:31], v[164:167]\ns_waitcnt vmcnt(4) lgkmcnt(3)\n"
        "v_mfma_f32_16x16x32_bf16 v[100:103], v[64:67], v[16:19], v[100:103]\nds_read_b128 v[28:31], %[xa] offset:18432\nv_mfma_f32_16x16x32_bf16 v[116:119], v[68:71], v[16:19], v[116:119]\n"
        "v_mfma_f32_16x16x32_bf16 v[136:139], v[72:75], v[16:19], v[136:139]\nds_read_b32 %[pv], %[sy] offset:4\nv_mfma_f32_16x16x32_bf16 v[152:155], v[76:79], v[16:19], v[152:155]\n"
        "ds_read_b128 v[16:19], %[xb] offset:12288\ns_waitcnt lgkmcnt(5)\nv_mfma_f32_16x16x32_bf16 v[104:107], v[64:67], v[20:23], v[104:107]\n"
        "v_mfma_f32_16x16x32_bf16 v[120:123], v[68:71], v[20:23], v[120:123]\nv_mfma_f32_16x16x32_bf16 v[140:143], v[72:75], v[20:23], v[140:143]\n"
        "v_mfma_f32_16x16x32_bf16 v[156:159], v[76:79], v[20:23], v[156:159]\nds_read_b128 v[20:23], %[xb] offset:14336\ns_waitcnt lgkmcnt(5)\n"
        "v_mfma_f32_16x16x32_bf16 v[96:99], v[64:67], v[12:15], v[96:99]\nv_mfma_f32_16x16x32_bf16 v[132:135], v[72:75], v[12:15], v[132:135]\nds_read_b128 v[12:15], %[xb] offset:10240\n"
        "s_waitcnt lgkmcnt(5)\nv_mfma_f32_16x16x32_bf16 v[108:111], v[64:67], v[24:27], v[108:111]\nv_mfma_f32_16x16x32_bf16 v[124:127], v[68:71], v[24:27], v[124:127]\n"
        "v_mfma_f32_16x16x32_bf16 v[144:147], v[72:75], v[24:27], v[144:147]\nv_mfma_f32_16x16x32_bf16 v[160:163], v[76:79], v[24:27], v[160:163]\nds_read_b128 v[24:27], %[xb] offset:16384\n"
        "s_waitcnt lgkmcnt(5)\nv_mfma_f32_16x16x32_bf16 v[112:115], v[64:67], v[28:31], v[112:115]\nv_mfma_f32_16x16x32_bf16 v[128:131], v[68:71], v[28:31], v[128:131]\n"
        "v_mfma_f32_16x16x32_bf16 v[148:151], v[72:75], v[28:31], v[148:151]\nv_mfma_f32_16x16x32_bf16 v[164:167], v[76:79], v[28:31], v[164:167]\ns_waitcnt vmcnt(0) lgkmcnt(3)\n"
        "v_mfma_f32_16x16x32_bf16 v[100:103], v[80:83], v[16:19], v[100:103]\nv_readfirstlane_b32 %[s0], %[pv]\nds_read_b128 v[28:31], %[xb] offset:18432\n"
        "v_mfma_f32_16x16x32_bf16 v[116:119], v[84:87], v[16:19], v[116:119]\nv_mfma_f32_16x16x32_bf16 v[136:139], v[88:91], v[16:19], v[136:139]\ns_cmp_ge_i32 %[s0], %[nst]\n"
        "v_mfma_f32_16x16x32_bf16 v[152:155], v[92:95], v[16:19], v[152:155]\ns_cbranch_scc0 .LA0t%=\n.LA0u%=:\ns_waitcnt lgkmcnt(3)\n"
        "v_mfma_f32_16x16x32_bf16 v[104:107], v[80:83], v[20:23], v[104:107]\nv_add_u32 %[sy], 4, %[sy]\nds_read_b128 v[16:19], %[mra] offset:256\n"
        "v_mfma_f32_16x16x32_bf16 v[120:123], v[84:87], v[20:23], v[120:123]\nv_mfma_f32_16x16x32_bf16 v[140:143], v[88:91], v[20:23], v[140:143]\n"
        "v_mfma_f32_16x16x32_bf16 v[156:159], v[92:95], v[20:23], v[156:159]\nds_read_b128 v[32:35], %[cwa] offset:0\ns_waitcnt lgkmcnt(4)\n"
        "v_mfma_f32_16x16x32_bf16 v[96:99], v[80:83], v[12:15], v[96:99]\nds_read_b128 v[36:39], %[cwa] offset:320\nds_read_b128 v[20:23], %[mra] offset:512\n"
        "v_mfma_f32_16x16x32_bf16 v[132:135], v[88:91], v[12:15], v[132:135]\nds_read_b128 v[40:43], %[cwa] offset:640\ns_waitcnt lgkmcnt(6)\n"
        "v_mfma_f32_16x16x32_bf16 v[108:111], v[80:83], v[24:27], v[108:111]\nds_read_b128 v[44:47], %[cwa] offset:512\nds_read_b128 v[12:15], %[mra] offset:0\n"
        "v_mfma_f32_16x16x32_bf16 v[124:127], v[84:87], v[24:27], v[124:127]\nv_add_u32 %[sy], 4, %[sy]\nv_mfma_f32_16x16x32_bf16 v[144:147], v[88:91], v[24:27], v[144:147]\n"
        "v_mfma_f32_16x16x32_bf16 v[160:163], v[92:95], v[24:27], v[160:163]\nds_read_b128 v[24:27], %[mra] offset:768\ns_waitcnt lgkmcnt(8)\n"
        "v_mfma_f32_16x16x32_bf16 v[112:115], v[80:83], v[28:31], v[112:115]\nv_mfma_f32_16x16x32_bf16 v[128:131], v[84:87], v[28:31], v[128:131]\n"
        "v_mfma_f32_16x16x32_bf16 v[148:151], v[88:91], v[28:31], v[148:151]\nv_mfma_f32_16x16x32_bf16 v[164:167], v[92:95], v[28:31], v[164:167]\nds_read_b128 v[28:31], %[mra] offset:1024\n"
        "s_setprio %[pfs]\ns_waitcnt lgkmcnt(1)\n.long 0xd3b04064, 0x1d922120\n.long 0xd3b04066, 0x1d9a2122\n.long 0xd3b04074, 0x1dd22124\n.long 0xd3b04076, 0x1dda2126\n.long 0xd3b14064, 0x18022564\n"
        ".long 0xd3b14066, 0x18022566\n.long 0xd3b04068, 0x1da22920\n.long 0xd3b0406a, 0x1daa2922\n.long 0xd3b14074, 0x18022574\n.long 0xd3b14076, 0x18022576\nv_cvt_pk_bf16_f32 v100, v100, v101\n"
        "v_cvt_pk_bf16_f32 v101, v102, v103\n.long 0xd3b04078, 0x1de22924\n.long 0xd3b0407a, 0x1dea2926\n.long 0xd3b14068, 0x18022d68\n.long 0xd3b1406a, 0x18022d6a\nv_cvt_pk_bf16_f32 v116, v116, v117\n"
        "v_cvt_pk_bf16_f32 v117, v118, v119\nds_write_b64 %[sta], v[100:101] offset:7936\n.long 0xd3b04060, 0x1d821920\n.long 0xd3b04062, 0x1d8a1922\n.long 0xd3b14078, 0x18022d78\n"
        ".long 0xd3b1407a, 0x18022d7a\nv_cvt_pk_bf16_f32 v104, v104, v105\nv_cvt_pk_bf16_f32 v105, v106, v107\nds_write_b64 %[sta], v[116:117] offset:8096\n.long 0xd3b0406c, 0x1db23120\n"
        ".long 0xd3b0406e, 0x1dba3122\n.long 0xd3b14060, 0x18021d60\n.long 0xd3b14062, 0x18021d62\nv_cvt_pk_bf16_f32 v120, v120, v121\nv_cvt_pk_bf16_f32 v121, v122, v123\n"
        "ds_write_b64 %[sta], v[104:105] offset:15872\n.long 0xd3b0407c, 0x1df23124\n.long 0xd3b0407e, 0x1dfa3126\n.long 0xd3b1406c, 0x1802356c\n.long 0xd3b1406e, 0x1802356e\n"
        "v_cvt_pk_bf16_f32 v96, v96, v97\nv_cvt_pk_bf16_f32 v97, v98, v99\nds_write_b64 %[sta], v[120:121] offset:16032\ns_waitcnt lgkmcnt(4)\n.long 0xd3b04070, 0x1dc23920\n"
        ".long 0xd3b04072, 0x1dca3922\n.long 0xd3b1407c, 0x1802357c\n.long 0xd3b1407e, 0x1802357e\nv_cvt_pk_bf16_f32 v108, v108, v109\nv_cvt_pk_bf16_f32 v109, v110, v111\n"
        "ds_write_b64 %[sta], v[96:97] offset:0\n.long 0xd3b04080, 0x1e023924\n.long 0xd3b04082, 0x1e0a3926\n.long 0xd3b14070, 0x18023d70\n.long 0xd3b14072, 0x18023d72\n"
        "v_cvt_pk_bf16_f32 v124, v124, v125\nv_cvt_pk_bf16_f32 v125, v126, v127\nds_write_b64 %[sta], v[108:109] offset:23808\n.long 0xd3b14080, 0x18023d80\n.long 0xd3b14082, 0x18023d82\n"
        "v_cvt_pk_bf16_f32 v112, v112, v113\nv_cvt_pk_bf16_f32 v113, v114, v115\nds_write_b64 %[sta], v[124:125] offset:23968\nv_cvt_pk_bf16_f32 v128, v128, v129\nv_cvt_pk_bf16_f32 v129, v130, v131\n"
        "ds_write_b64 %[sta], v[112:113] offset:31744\nds_write_b64 %[sta], v[128:129] offset:31904\nds_write_b32 %[pg], %[sy]\nv_add_u32 %[sy], 4, %[sy]\n.long 0xd3b04088, 0x1e222128\n"
        ".long 0xd3b0408a, 0x1e2a212a\n.long 0xd3b04098, 0x1e62212c\n.long 0xd3b0409a, 0x1e6a212e\n.long 0xd3b14088, 0x18022588\n.long 0xd3b1408a, 0x1802258a\n.long 0xd3b0408c, 0x1e322928\n"
        ".long 0xd3b0408e, 0x1e3a292a\n.long 0xd3b14098, 0x18022598\n.long 0xd3b1409a, 0x1802259a\nv_cvt_pk_bf16_f32 v136, v136, v137\nv_cvt_pk_bf16_f32 v137, v138, v139\n.long 0xd3b0409c, 0x1e72292c\n"
        ".long 0xd3b0409e, 0x1e7a292e\n.long 0xd3b1408c, 0x18022d8c\n.long 0xd3b1408e, 0x18022d8e\nv_cvt_pk_bf16_f32 v152, v152, v153\nv_cvt_pk_bf16_f32 v153, v154, v155\n"
        "ds_write_b64 %[sta], v[136:137] offset:8256\n.long 0xd3b04084, 0x1e121928\n.long 0xd3b04086, 0x1e1a192a\n.long 0xd3b1409c, 0x18022d9c\n.long 0xd3b1409e, 0x18022d9e\n"
        "v_cvt_pk_bf16_f32 v140, v140, v141\nv_cvt_pk_bf16_f32 v141, v142, v143\nds_write_b64 %[sta], v[152:153] offset:8192\n.long 0xd3b04090, 0x1e423128\n.long 0xd3b04092, 0x1e4a312a\n"
        ".long 0xd3b14084, 0x18021d84\n.long 0xd3b14086, 0x18021d86\nv_cvt_pk_bf16_f32 v156, v156, v157\nv_cvt_pk_bf16_f32 v157, v158, v159\nds_write_b64 %[sta], v[140:141] offset:16192\n"
        ".long 0xd3b040a0, 0x1e82312c\n.long 0xd3b040a2, 0x1e8a312e\n.long 0xd3b14090, 0x18023590\n.long 0xd3b14092, 0x18023592\nv_cvt_pk_bf16_f32 v132, v132, v133\nv_cvt_pk_bf16_f32 v133, v134, v135\n"
        "ds_write_b64 %[sta], v[156:157] offset:16128\n.long 0xd3b04094, 0x1e523928\n.long 0xd3b04096, 0x1e5a392a\n.long 0xd3b140a0, 0x180235a0\n.long 0xd3b140a2, 0x180235a2\n"
        "v_cvt_pk_bf16_f32 v144, v144, v145\nv_cvt_pk_bf16_f32 v145, v146, v147\nds_write_b64 %[sta], v[132:133] offset:320\n.long 0xd3b040a4, 0x1e92392c\n.long 0xd3b040a6, 0x1e9a392e\n"
        ".long 0xd3b14094, 0x18023d94\n.long 0xd3b14096, 0x18023d96\nv_cvt_pk_bf16_f32 v160, v160, v161\nv_cvt_pk_bf16_f32 v161, v162, v163\nds_write_b64 %[sta], v[144:145] offset:24128\n"
        ".long 0xd3b140a4, 0x18023da4\n.long 0xd3b140a6, 0x18023da6\nv_cvt_pk_bf16_f32 v148, v148, v149\nv_cvt_pk_bf16_f32 v149, v150, v151\nds_write_b64 %[sta], v[160:161] offset:24064\n"
        "v_cvt_pk_bf16_f32 v164, v164, v165\nv_cvt_pk_bf16_f32 v165, v166, v167\nds_write_b64 %[sta], v[148:149] offset:32064\nds_write_b64 %[sta], v[164:165] offset:32000\nds_write_b32 %[pg], %[sy]\n"
        "s_branch .LE%=\n.LA0t%=:\ns_mov_b32 %[s1], 0x100000\n.LA0q%=:\nds_read_b32 %[pv], %[sy] offset:4\ns_waitcnt lgkmcnt(0)\nv_readfirstlane_b32 %[s0], %[pv]\ns_nop 0\ns_cmp_ge_i32 %[s0], %[nst]\n"
        "s_cbranch_scc1 .LA0u%=\ns_sleep 1\ns_sub_u32 %[s1], %[s1], 1\ns_cbranch_scc0 .LA0q%=\ns_branch .LA0u%=\n.LB%=:\ns_cmp_eq_u32 %[w], 2\ns_cbranch_scc0 .LC%=\ns_mov_b32 %[s1], 0x100000\n"
        ".LB0w%=:\nds_read_b32 %[pv], %[sy] offset:%[ow]\ns_waitcnt lgkmcnt(0)\nv_readfirstlane_b32 %[s0], %[pv]\ns_nop 0\ns_cmp_ge_i32 %[s0], 1\ns_cbranch_scc1 .LB0h%=\ns_sleep 1\n"
        "s_sub_u32 %[s1], %[s1], 1\ns_cbranch_scc0 .LB0w%=\n.LB0h%=:\ns_mov_b32 %[so], 0\nbuffer_load_dwordx4 v[32:35], %[wa], %[rs], %[so] offen offset:3072 sc1\n"
        "buffer_load_dwordx4 v[36:39], %[wa], %[rs], %[so] offen offset:0 sc1\nbuffer_load_dwordx4 v[40:43], %[wa], %[rs], %[so] offen offset:1024 sc1\n"
        "buffer_load_dwordx4 v[44:47], %[wa], %[rs], %[so] offen offset:2048 sc1\ns_mov_b32 %[so], 15360\nbuffer_load_dwordx4 v[48:51], %[wa], %[rs], %[so] offen offset:3072 sc1\n"
        "buffer_load_dwordx4 v[52:55], %[wa], %[rs], %[so] offen offset:0 sc1\nbuffer_load_dwordx4 v[56:59], %[wa], %[rs], %[so] offen offset:1024 sc1\n"
        "buffer_load_dwordx4 v[60:63], %[wa], %[rs], %[so] offen offset:2048 sc1\ns_mov_b32 %[so], 30720\nbuffer_load_dwordx4 v[64:67], %[wa], %[rs], %[so] offen offset:3072 sc1\n"
        "buffer_load_dwordx4 v[68:71], %[wa], %[rs], %[so] offen offset:0 sc1\nbuffer_load_dwordx4 v[72:75], %[wa], %[rs], %[so] offen offset:1024 sc1\n"
        "buffer_load_dwordx4 v[76:79], %[wa], %[rs], %[so] offen offset:2048 sc1\ns_mov_b32 %[so], 46080\nbuffer_load_dwordx4 v[80:83], %[wa], %[rs], %[so] offen offset:3072 sc1\n"
        "buffer_load_dwordx4 v[84:87], %[wa], %[rs], %[so] offen offset:0 sc1\nbuffer_load_dwordx4 v[88:91], %[wa], %[rs], %[so] offen offset:1024 sc1\n"
        "buffer_load_dwordx4 v[92:95], %[wa], %[rs], %[so] offen offset:2048 sc1\ns_setprio %[pf]\ns_mov_b32 %[s1], 0x100000\n.LB0v%=:\nds_read_b32 %[pv], %[sy] offset:0\ns_waitcnt lgkmcnt(0)\n"
        "v_readfirstlane_b32 %[s0], %[pv]\ns_nop 0\ns_cmp_ge_i32 %[s0], 3\ns_cbranch_scc1 .LB0g%=\ns_sleep 1\ns_sub_u32 %[s1], %[s1], 1\ns_cbranch_scc0 .LB0v%=\n.LB0g%=:\n"
        "ds_read_b128 v[16:19], %[xa] offset:2048\nds_read_b128 v[20:23], %[xa] offset:4096\nds_read_b128 v[12:15], %[xa] offset:0\nds_read_b128 v[24:27], %[xa] offset:6144\n"
        "s_waitcnt vmcnt(12) lgkmcnt(3)\nv_mfma_f32_16x16x32_bf16 v[96:99], v[32:35], v[16:19], 0\nds_read_b128 v[28:31], %[xa] offset:8192\nv_mfma_f32_16x16x32_bf16 v[108:111], v[36:39], v[16:19], 0\n"
        "v_mfma_f32_16x16x32_bf16 v[128:131], v[40:43], v[16:19], 0\nv_mfma_f32_16x16x32_bf16 v[148:151], v[44:47], v[16:19], 0\nds_read_b128 v[16:19], %[xb] offset:2048\ns_waitcnt lgkmcnt(4)\n"
        "v_mfma_f32_16x16x32_bf16 v[100:103], v[32:35], v[20:23], 0\nv_mfma_f32_16x16x32_bf16 v[112:115], v[36:39], v[20:23], 0\nv_mfma_f32_16x16x32_bf16 v[132:135], v[40:43], v[20:23], 0\n"
        "v_mfma_f32_16x16x32_bf16 v[152:155], v[44:47], v[20:23], 0\nds_read_b128 v[20:23], %[xb] offset:4096\ns_waitcnt lgkmcnt(4)\nv_mfma_f32_16x16x32_bf16 v[104:107], v[36:39], v[12:15], 0\n"
        "v_mfma_f32_16x16x32_bf16 v[124:127], v[40:43], v[12:15], 0\nv_mfma_f32_16x16x32_bf16 v[144:147], v[44:47], v[12:15], 0\nds_read_b128 v[12:15], %[xb] offset:0\ns_waitcnt lgkmcnt(4)\n"
        "v_mfma_f32_16x16x32_bf16 v[116:119], v[36:39], v[24:27], 0\nv_mfma_f32_16x16x32_bf16 v[136:139], v[40:43], v[24:27], 0\nv_mfma_f32_16x16x32_bf16 v[156:159], v[44:47], v[24:27], 0\n"
        "ds_read_b128 v[24:27], %[xb] offset:6144\ns_waitcnt lgkmcnt(4)\nv_mfma_f32_16x16x32_bf16 v[120:123], v[36:39], v[28:31], 0\nv_mfma_f32_16x16x32_bf16 v[140:143], v[40:43], v[28:31], 0\n"
        "v_mfma_f32_16x16x32_bf16 v[160:163], v[44:47], v[28:31], 0\n.LB0l%=:\ns_waitcnt vmcnt(8) lgkmcnt(3)\nv_mfma_f32_16x16x32_bf16 v[96:99], v[48:51], v[16:19], v[96:99]\ns_addk_i32 %[so], 15360\n"
        "ds_read_b128 v[28:31], %[xb] offset:8192\nv_mfma_f32_16x16x32_bf16 v[108:111], v[52:55], v[16:19], v[108:111]\nv_mfma_f32_16x16x32_bf16 v[128:131], v[56:59], v[16:19], v[128:131]\n"
        "buffer_load_dwordx4 v[32:35], %[wa], %[rs], %[so] offen offset:3072 sc1\nv_mfma_f32_16x16x32_bf16 v[148:151], v[60:63], v[16:19], v[148:151]\nds_read_b128 v[16:19], %[xa] offset:12288\n"
        "s_waitcnt lgkmcnt(4)\nv_mfma_f32_16x16x32_bf16 v[100:103], v[48:51], v[20:23], v[100:103]\nv_mfma_f32_16x16x32_bf16 v[112:115], v[52:55], v[20:23], v[112:115]\n"
        "buffer_load_dwordx4 v[36:39], %[wa], %[rs], %[so] offen offset:0 sc1\nv_mfma_f32_16x16x32_bf16 v[132:135], v[56:59], v[20:23], v[132:135]\n"
        "v_mfma_f32_16x16x32_bf16 v[152:155], v[60:63], v[20:23], v[152:155]\nds_read_b128 v[20:23], %[xa] offset:14336\ns_waitcnt lgkmcnt(4)\n"
        "v_mfma_f32_16x16x32_bf16 v[104:107], v[52:55], v[12:15], v[104:107]\nv_mfma_f32_16x16x32_bf16 v[124:127], v[56:59], v[12:15], v[124:127]\n"
        "buffer_load_dwordx4 v[40:43], %[wa], %[rs], %[so] offen offset:1024 sc1\nv_mfma_f32_16x16x32_bf16 v[144:147], v[60:63], v[12:15], v[144:147]\nds_read_b128 v[12:15], %[xa] offset:10240\n"
        "s_waitcnt lgkmcnt(4)\nv_mfma_f32_16x16x32_bf16 v[116:119], v[52:55], v[24:27], v[116:119]\nv_mfma_f32_16x16x32_bf16 v[136:139], v[56:59], v[24:27], v[136:139]\n"
        "buffer_load_dwordx4 v[44:47], %[wa], %[rs], %[so] offen offset:2048 sc1\nv_mfma_f32_16x16x32_bf16 v[156:159], v[60:63], v[24:27], v[156:159]\nds_read_b128 v[24:27], %[xa] offset:16384\n"
        "s_waitcnt lgkmcnt(4)\nv_mfma_f32_16x16x32_bf16 v[120:123], v[52:55], v[28:31], v[120:123]\nv_add_u32 %[sy], 4, %[sy]\nv_mfma_f32_16x16x32_bf16 v[140:143], v[56:59], v[28:31], v[140:143]\n"
        "ds_write_b32 %[pg], %[sy]\nv_mfma_f32_16x16x32_bf16 v[160:163], v[60:63], v[28:31], v[160:163]\ns_waitcnt vmcnt(8) lgkmcnt(4)\nv_mfma_f32_16x16x32_bf16 v[96:99], v[64:67], v[16:19], v[96:99]\n"
        "s_addk_i32 %[so], 15360\nds_read_b128 v[28:31], %[xa] offset:18432\nv_mfma_f32_16x16x32_bf16 v[108:111], v[68:71], v[16:19], v[108:111]\n"
        "v_mfma_f32_16x16x32_bf16 v[128:131], v[72:75], v[16:19], v[128:131]\nbuffer_load_dwordx4 v[48:51], %[wa], %[rs], %[so] offen offset:3072 sc1\n"
        "v_mfma_f32_16x16x32_bf16 v[148:151], v[76:79], v[16:19], v[148:151]\nds_read_b128 v[16:19], %[xb] offset:12288\ns_waitcnt lgkmcnt(5)\n"
        "v_mfma_f32_16x16x32_bf16 v[100:103], v[64:67], v[20:23], v[100:103]\nv_mfma_f32_16x16x32_bf16 v[112:115], v[68:71], v[20:23], v[112:115]\n"
        "buffer_load_dwordx4 v[52:55], %[wa], %[rs], %[so] offen offset:0 sc1\nv_mfma_f32_16x16x32_bf16 v[132:135], v[72:75], v[20:23], v[132:135]\n"
        "v_mfma_f32_16x16x32_bf16 v[152:155], v[76:79], v[20:23], v[152:155]\nds_read_b128 v[20:23], %[xb] offset:14336\ns_waitcnt lgkmcnt(5)\n"
        "v_mfma_f32_16x16x32_bf16 v[104:107], v[68:71], v[12:15], v[104:107]\nv_mfma_f32_16x16x32_bf16 v[124:127], v[72:75], v[12:15], v[124:127]\n"
        "buffer_load_dwordx4 v[56:59], %[wa], %[rs], %[so] offen offset:1024 sc1\nv_mfma_f32_16x16x32_bf16 v[144:147], v[76:79], v[12:15], v[144:147]\nds_read_b128 v[12:15], %[xb] offset:10240\n"
        "s_waitcnt lgkmcnt(5)\nv_mfma_f32_16x16x32_bf16 v[116:119], v[68:71], v[24:27], v[116:119]\nv_mfma_f32_16x16x32_bf16 v[136:139], v[72:75], v[24:27], v[136:139]\n"
        "buffer_load_dwordx4 v[60:63], %[wa], %[rs], %[so] offen offset:2048 sc1\nv_mfma_f32_16x16x32_bf16 v[156:159], v[76:79], v[24:27], v[156:159]\nds_read_b128 v[24:27], %[xb] offset:16384\n"
        "s_waitcnt lgkmcnt(4)\nv_mfma_f32_16x16x32_bf16 v[120:123], v[68:71], v[28:31], v[120:123]\nv_mfma_f32_16x16x32_bf16 v[140:143], v[72:75], v[28:31], v[140:143]\n"
        "v_mfma_f32_16x16x32_bf16 v[160:163], v[76:79], v[28:31], v[160:163]\ns_waitcnt vmcnt(8) lgkmcnt(3)\nv_mfma_f32_16x16x32_bf16 v[96:99], v[80:83], v[16:19], v[96:99]\ns_addk_i32 %[so], 15360\n"
        "ds_read_b128 v[28:31], %[xb] offset:18432\nv_mfma_f32_16x16x32_bf16 v[108:111], v[84:87], v[16:19], v[108:111]\nv_mfma_f32_16x16x32_bf16 v[128:131], v[88:91], v[16:19], v[128:131]\n"
        "buffer_load_dwordx4 v[64:67], %[wa], %[rs], %[so] offen offset:3072 sc1\nv_mfma_f32_16x16x32_bf16 v[148:151], v[92:95], v[16:19], v[148:151]\nds_read_b128 v[16:19], %[xa] offset:22528\n"
        "s_waitcnt lgkmcnt(4)\nv_mfma_f32_16x16x32_bf16 v[100:103], v[80:83], v[20:23], v[100:103]\nv_mfma_f32_16x16x32_bf16 v[112:115], v[84:87], v[20:23], v[112:115]\n"
        "buffer_load_dwordx4 v[68:71], %[wa], %[rs], %[so] offen offset:0 sc1\nv_mfma_f32_16x16x32_bf16 v[132:135], v[88:91], v[20:23], v[132:135]\n"
        "v_mfma_f32_16x16x32_bf16 v[152:155], v[92:95], v[20:23], v[152:155]\nds_read_b128 v[20:23], %[xa] offset:24576\ns_waitcnt lgkmcnt(4)\n"
        "v_mfma_f32_16x16x32_bf16 v[104:107], v[84:87], v[12:15], v[104:107]\nv_mfma_f32_16x16x32_bf16 v[124:127], v[88:91], v[12:15], v[124:127]\n"
        "buffer_load_dwordx4 v[72:75], %[wa], %[rs], %[so] offen offset:1024 sc1\nv_mfma_f32_16x16x32_bf16 v[144:147], v[92:95], v[12:15], v[144:147]\nds_read_b128 v[12:15], %[xa] offset:20480\n"
        "s_waitcnt lgkmcnt(4)\nv_mfma_f32_16x16x32_bf16 v[116:119], v[84:87], v[24:27], v[116:119]\nv_mfma_f32_16x16x32_bf16 v[136:139], v[88:91], v[24:27], v[136:139]\n"
        "buffer_load_dwordx4 v[76:79], %[wa], %[rs], %[so] offen offset:2048 sc1\nv_mfma_f32_16x16x32_bf16 v[156:159], v[92:95], v[24:27], v[156:159]\nds_read_b128 v[24:27], %[xa] offset:26624\n"
        "s_waitcnt lgkmcnt(4)\nv_mfma_f32_16x16x32_bf16 v[120:123], v[84:87], v[28:31], v[120:123]\nv_add_u32 %[sy], 4, %[sy]\nv_mfma_f32_16x16x32_bf16 v[140:143], v[88:91], v[28:31], v[140:143]\n"
        "ds_write_b32 %[pg], %[sy]\nv_mfma_f32_16x16x32_bf16 v[160:163], v[92:95], v[28:31], v[160:163]\ns_waitcnt vmcnt(8) lgkmcnt(4)\nv_mfma_f32_16x16x32_bf16 v[96:99], v[32:35], v[16:19], v[96:99]\n"
        "s_addk_i32 %[so], 15360\nds_read_b128 v[28:31], %[xa] offset:28672\nv_mfma_f32_16x16x32_bf16 v[108:111], v[36:39], v[16:19], v[108:111]\n"
        "v_mfma_f32_16x16x32_bf16 v[128:131], v[40:43], v[16:19], v[128:131]\nbuffer_load_dwordx4 v[80:83], %[wa], %[rs], %[so] offen offset:3072 sc1\n"
        "v_mfma_f32_16x16x32_bf16 v[148:151], v[44:47], v[16:19], v[148:151]\nds_read_b128 v[16:19], %[xb] offset:22528\ns_waitcnt lgkmcnt(5)\n"
        "v_mfma_f32_16x16x32_bf16 v[100:103], v[32:35], v[20:23], v[100:103]\nv_mfma_f32_16x16x32_bf16 v[112:115], v[36:39], v[20:23], v[112:115]\n"
        "buffer_load_dwordx4 v[84:87], %[wa], %[rs], %[so] offen offset:0 sc1\nv_mfma_f32_16x16x32_bf16 v[132:135], v[40:43], v[20:23], v[132:135]\n"
        "v_mfma_f32_16x16x32_bf16 v[152:155], v[44:47], v[20:23], v[152:155]\nds_read_b128 v[20:23], %[xb] offset:24576\ns_waitcnt lgkmcnt(5)\n"
        "v_mfma_f32_16x16x32_bf16 v[104:107], v[36:39], v[12:15], v[104:107]\nv_mfma_f32_16x16x32_bf16 v[124:127], v[40:43], v[12:15], v[124:127]\n"
        "buffer_load_dwordx4 v[88:91], %[wa], %[rs], %[so] offen offset:1024 sc1\nv_mfma_f32_16x16x32_bf16 v[144:147], v[44:47], v[12:15], v[144:147]\nds_read_b128 v[12:15], %[xb] offset:20480\n"
        "s_waitcnt lgkmcnt(5)\nv_mfma_f32_16x16x32_bf16 v[116:119], v[36:39], v[24:27], v[116:119]\nv_mfma_f32_16x16x32_bf16 v[136:139], v[40:43], v[24:27], v[136:139]\n"
        "buffer_load_dwordx4 v[92:95], %[wa], %[rs], %[so] offen offset:2048 sc1\nv_mfma_f32_16x16x32_bf16 v[156:159], v[44:47], v[24:27], v[156:159]\nds_read_b128 v[24:27], %[xb] offset:26624\n"
        "s_waitcnt lgkmcnt(4)\nv_mfma_f32_16x16x32_bf16 v[120:123], v[36:39], v[28:31], v[120:123]\nv_mfma_f32_16x16x32_bf16 v[140:143], v[40:43], v[28:31], v[140:143]\ns_cmp_lg_u32 %[so], 291840\n"
        "v_mfma_f32_16x16x32_bf16 v[160:163], v[44:47], v[28:31], v[160:163]\nv_add_u32 %[xa], 20480, %[xa]\nv_add_u32 %[xb], 20480, %[xb]\ns_cbranch_scc1 .LB0l%=\ns_waitcnt vmcnt(8) lgkmcnt(3)\n"
        "v_mfma_f32_16x16x32_bf16 v[96:99], v[48:51], v[16:19], v[96:99]\nds_read_b128 v[28:31], %[xb] offset:8192\nv_mfma_f32_16x16x32_bf16 v[108:111], v[52:55], v[16:19], v[108:111]\n"
        "v_mfma_f32_16x16x32_bf16 v[128:131], v[56:59], v[16:19], v[128:131]\nv_mfma_f32_16x16x32_bf16 v[148:151], v[60:63], v[16:19], v[148:151]\nds_read_b128 v[16:19], %[xa] offset:12288\n"
        "s_waitcnt lgkmcnt(4)\nv_mfma_f32_16x16x32_bf16 v[100:103], v[48:51], v[20:23], v[100:103]\nv_mfma_f32_16x16x32_bf16 v[112:115], v[52:55], v[20:23], v[112:115]\n"
        "v_mfma_f32_16x16x32_bf16 v[132:135], v[56:59], v[20:23], v[132:135]\nv_mfma_f32_16x16x32_bf16 v[152:155], v[60:63], v[20:23], v[152:155]\nds_read_b128 v[20:23], %[xa] offset:14336\n"
        "s_waitcnt lgkmcnt(4)\nv_mfma_f32_16x16x32_bf16 v[104:107], v[52:55], v[12:15], v[104:107]\nv_mfma_f32_16x16x32_bf16 v[124:127], v[56:59], v[12:15], v[124:127]\n"
        "v_mfma_f32_16x16x32_bf16 v[144:147], v[60:63], v[12:15], v[144:147]\nds_read_b128 v[12:15], %[xa] offset:10240\ns_waitcnt lgkmcnt(4)\n"
        "v_mfma_f32_16x16x32_bf16 v[116:119], v[52:55], v[24:27], v[116:119]\nv_mfma_f32_16x16x32_bf16 v[136:139], v[56:59], v[24:27], v[136:139]\n"
        "v_mfma_f32_16x16x32_bf16 v[156:159], v[60:63], v[24:27], v[156:159]\nds_read_b128 v[24:27], %[xa] offset:16384\ns_waitcnt lgkmcnt(4)\n"
        "v_mfma_f32_16x16x32_bf16 v[120:123], v[52:55], v[28:31], v[120:123]\nv_add_u32 %[sy], 4, %[sy]\nv_mfma_f32_16x16x32_bf16 v[140:143], v[56:59], v[28:31], v[140:143]\n"
        "v_mfma_f32_16x16x32_bf16 v[160:163], v[60:63], v[28:31], v[160:163]\ns_waitcnt vmcnt(4) lgkmcnt(3)\nv_mfma_f32_16x16x32_bf16 v[96:99], v[64:67], v[16:19], v[96:99]\n"
        "ds_read_b128 v[28:31], %[xa] offset:18432\nv_mfma_f32_16x16x32_bf16 v[108:111], v[68:71], v[16:19], v[108:111]\nv_mfma_f32_16x16x32_bf16 v[128:131], v[72:75], v[16:19], v[128:131]\n"
        "ds_read_b32 %[pv], %[sy] offset:4\nv_mfma_f32_16x16x32_bf16 v[148:151], v[76:79], v[16:19], v[148:151]\nds_read_b128 v[16:19], %[xb] offset:12288\ns_waitcnt lgkmcnt(5)\n"
        "v_mfma_f32_16x16x32_bf16 v[100:103], v[64:67], v[20:23], v[100:103]\nv_mfma_f32_16x16x32_bf16 v[112:115], v[68:71], v[20:23], v[112:115]\n"
        "v_mfma_f32_16x16x32_bf16 v[132:135], v[72:75], v[20:23], v[132:135]\nv_mfma_f32_16x16x32_bf16 v[152:155], v[76:79], v[20:23], v[152:155]\nds_read_b128 v[20:23], %[xb] offset:14336\n"
        "s_waitcnt lgkmcnt(5)\nv_mfma_f32_16x16x32_bf16 v[104:107], v[68:71], v[12:15], v[104:107]\nv_mfma_f32_16x16x32_bf16 v[124:127], v[72:75], v[12:15], v[124:127]\n"
        "v_mfma_f32_16x16x32_bf16 v[144:147], v[76:79], v[12:15], v[144:147]\nds_read_b128 v[12:15], %[xb] offset:10240\ns_waitcnt lgkmcnt(5)\n"
        "v_mfma_f32_16x16x32_bf16 v[116:119], v[68:71], v[24:27], v[116:119]\nv_mfma_f32_16x16x32_bf16 v[136:139], v[72:75], v[24:27], v[136:139]\n"
        "v_mfma_f32_16x16x32_bf16 v[156:159], v[76:79], v[24:27], v[156:159]\nds_read_b128 v[24:27], %[xb] offset:16384\ns_waitcnt lgkmcnt(5)\n"
        "v_mfma_f32_16x16x32_bf16 v[120:123], v[68:71], v[28:31], v[120:123]\nv_mfma_f32_16x16x32_bf16 v[140:143], v[72:75], v[28:31], v[140:143]\n"
        "v_mfma_f32_16x16x32_bf16 v[160:163], v[76:79], v[28:31], v[160:163]\ns_waitcnt vmcnt(0) lgkmcnt(3)\nv_mfma_f32_16x16x32_bf16 v[96:99], v[80:83], v[16:19], v[96:99]\n"
        "v_readfirstlane_b32 %[s0], %[pv]\nds_read_b128 v[28:31], %[xb] offset:18432\nv_mfma_f32_16x16x32_bf16 v[108:111], v[84:87], v[16:19], v[108:111]\n"
        "v_mfma_f32_16x16x32_bf16 v[128:131], v[88:91], v[16:19], v[128:131]\ns_cmp_ge_i32 %[s0], %[nst]\nv_mfma_f32_16x16x32_bf16 v[148:151], v[92:95], v[16:19], v[148:151]\ns_cbranch_scc0 .LB0t%=\n"
        ".LB0u%=:\ns_waitcnt lgkmcnt(3)\nv_mfma_f32_16x16x32_bf16 v[100:103], v[80:83], v[20:23], v[100:103]\nv_add_u32 %[sy], 4, %[sy]\nds_read_b128 v[16:19], %[mra] offset:256\n"
        "v_mfma_f32_16x16x32_bf16 v[112:115], v[84:87], v[20:23], v[112:115]\nv_mfma_f32_16x16x32_bf16 v[132:135], v[88:91], v[20:23], v[132:135]\n"
        "v_mfma_f32_16x16x32_bf16 v[152:155], v[92:95], v[20:23], v[152:155]\nds_read_b128 v[32:35], %[cwa] offset:448\ns_waitcnt lgkmcnt(4)\n"
        "v_mfma_f32_16x16x32_bf16 v[104:107], v[84:87], v[12:15], v[104:107]\nds_read_b128 v[36:39], %[cwa] offset:128\nds_read_b128 v[20:23], %[mra] offset:512\n"
        "v_mfma_f32_16x16x32_bf16 v[124:127], v[88:91], v[12:15], v[124:127]\nds_read_b128 v[40:43], %[cwa] offset:192\nv_mfma_f32_16x16x32_bf16 v[144:147], v[92:95], v[12:15], v[144:147]\n"
        "ds_read_b128 v[44:47], %[cwa] offset:768\ns_waitcnt lgkmcnt(7)\nv_mfma_f32_16x16x32_bf16 v[116:119], v[84:87], v[24:27], v[116:119]\nv_add_u32 %[sy], 4, %[sy]\n"
        "ds_read_b128 v[12:15], %[mra] offset:0\nv_mfma_f32_16x16x32_bf16 v[136:139], v[88:91], v[24:27], v[136:139]\nv_mfma_f32_16x16x32_bf16 v[156:159], v[92:95], v[24:27], v[156:159]\n"
        "ds_read_b128 v[24:27], %[mra] offset:768\ns_waitcnt lgkmcnt(8)\nv_mfma_f32_16x16x32_bf16 v[120:123], v[84:87], v[28:31], v[120:123]\n"
        "v_mfma_f32_16x16x32_bf16 v[140:143], v[88:91], v[28:31], v[140:143]\nv_mfma_f32_16x16x32_bf16 v[160:163], v[92:95], v[28:31], v[160:163]\nds_read_b128 v[28:31], %[mra] offset:1024\n"
        "s_setprio %[pfs]\ns_waitcnt lgkmcnt(1)\n.long 0xd3b04060, 0x1d822120\n.long 0xd3b04062, 0x1d8a2122\n.long 0xd3b0406c, 0x1db22124\n.long 0xd3b0406e, 0x1dba2126\n.long 0xd3b14060, 0x18022560\n"
        ".long 0xd3b14062, 0x18022562\n.long 0xd3b04080, 0x1e022128\n.long 0xd3b04082, 0x1e0a212a\n.long 0xd3b1406c, 0x1802256c\n.long 0xd3b1406e, 0x1802256e\nv_cvt_pk_bf16_f32 v96, v96, v97\n"
        "v_cvt_pk_bf16_f32 v97, v98, v99\n.long 0xd3b04064, 0x1d922920\n.long 0xd3b04066, 0x1d9a2922\n.long 0xd3b14080, 0x18022580\n.long 0xd3b14082, 0x18022582\nv_cvt_pk_bf16_f32 v108, v108, v109\n"
        "v_cvt_pk_bf16_f32 v109, v110, v111\nds_write_b64 %[sta], v[96:97] offset:8160\n.long 0xd3b04070, 0x1dc22924\n.long 0xd3b04072, 0x1dca2926\n.long 0xd3b14064, 0x18022d64\n"
        ".long 0xd3b14066, 0x18022d66\nv_cvt_pk_bf16_f32 v128, v128, v129\nv_cvt_pk_bf16_f32 v129, v130, v131\nds_write_b64 %[sta], v[108:109] offset:8000\n.long 0xd3b04084, 0x1e122928\n"
        ".long 0xd3b04086, 0x1e1a292a\n.long 0xd3b14070, 0x18022d70\n.long 0xd3b14072, 0x18022d72\nv_cvt_pk_bf16_f32 v100, v100, v101\nv_cvt_pk_bf16_f32 v101, v102, v103\n"
        "ds_write_b64 %[sta], v[128:129] offset:8032\n.long 0xd3b04068, 0x1da21924\n.long 0xd3b0406a, 0x1daa1926\n.long 0xd3b14084, 0x18022d84\n.long 0xd3b14086, 0x18022d86\n"
        "v_cvt_pk_bf16_f32 v112, v112, v113\nv_cvt_pk_bf16_f32 v113, v114, v115\nds_write_b64 %[sta], v[100:101] offset:16096\n.long 0xd3b0407c, 0x1df21928\n.long 0xd3b0407e, 0x1dfa192a\n"
        ".long 0xd3b14068, 0x18021d68\n.long 0xd3b1406a, 0x18021d6a\nv_cvt_pk_bf16_f32 v132, v132, v133\nv_cvt_pk_bf16_f32 v133, v134, v135\nds_write_b64 %[sta], v[112:113] offset:15936\n"
        ".long 0xd3b04074, 0x1dd23124\n.long 0xd3b04076, 0x1dda3126\n.long 0xd3b1407c, 0x18021d7c\n.long 0xd3b1407e, 0x18021d7e\nv_cvt_pk_bf16_f32 v104, v104, v105\nv_cvt_pk_bf16_f32 v105, v106, v107\n"
        "ds_write_b64 %[sta], v[132:133] offset:15968\n.long 0xd3b04088, 0x1e223128\n.long 0xd3b0408a, 0x1e2a312a\n.long 0xd3b14074, 0x18023574\n.long 0xd3b14076, 0x18023576\n"
        "v_cvt_pk_bf16_f32 v124, v124, v125\nv_cvt_pk_bf16_f32 v125, v126, v127\nds_write_b64 %[sta], v[104:105] offset:64\ns_waitcnt lgkmcnt(7)\n.long 0xd3b04078, 0x1de23924\n"
        ".long 0xd3b0407a, 0x1dea3926\n.long 0xd3b14088, 0x18023588\n.long 0xd3b1408a, 0x1802358a\nv_cvt_pk_bf16_f32 v116, v116, v117\nv_cvt_pk_bf16_f32 v117, v118, v119\n"
        "ds_write_b64 %[sta], v[124:125] offset:96\n.long 0xd3b0408c, 0x1e323928\n.long 0xd3b0408e, 0x1e3a392a\n.long 0xd3b14078, 0x18023d78\n.long 0xd3b1407a, 0x18023d7a\n"
        "v_cvt_pk_bf16_f32 v136, v136, v137\nv_cvt_pk_bf16_f32 v137, v138, v139\nds_write_b64 %[sta], v[116:117] offset:23872\n.long 0xd3b1408c, 0x18023d8c\n.long 0xd3b1408e, 0x18023d8e\n"
        "v_cvt_pk_bf16_f32 v120, v120, v121\nv_cvt_pk_bf16_f32 v121, v122, v123\nds_write_b64 %[sta], v[136:137] offset:23904\nv_cvt_pk_bf16_f32 v140, v140, v141\nv_cvt_pk_bf16_f32 v141, v142, v143\n"
        "ds_write_b64 %[sta], v[120:121] offset:31808\nds_write_b64 %[sta], v[140:141] offset:31840\nds_write_b32 %[pg], %[sy]\nv_add_u32 %[sy], 4, %[sy]\n.long 0xd3b04094, 0x1e52212c\n"
        ".long 0xd3b04096, 0x1e5a212e\n.long 0xd3b04098, 0x1e62292c\n.long 0xd3b0409a, 0x1e6a292e\n.long 0xd3b14094, 0x18022594\n.long 0xd3b14096, 0x18022596\n.long 0xd3b04090, 0x1e42192c\n"
        ".long 0xd3b04092, 0x1e4a192e\n.long 0xd3b14098, 0x18022d98\n.long 0xd3b1409a, 0x18022d9a\nv_cvt_pk_bf16_f32 v148, v148, v149\nv_cvt_pk_bf16_f32 v149, v150, v151\n.long 0xd3b0409c, 0x1e72312c\n"
        ".long 0xd3b0409e, 0x1e7a312e\n.long 0xd3b14090, 0x18021d90\n.long 0xd3b14092, 0x18021d92\nv_cvt_pk_bf16_f32 v152, v152, v153\nv_cvt_pk_bf16_f32 v153, v154, v155\n"
        "ds_write_b64 %[sta], v[148:149] offset:8320\n.long 0xd3b040a0, 0x1e82392c\n.long 0xd3b040a2, 0x1e8a392e\n.long 0xd3b1409c, 0x1802359c\n.long 0xd3b1409e, 0x1802359e\n"
        "v_cvt_pk_bf16_f32 v144, v144, v145\nv_cvt_pk_bf16_f32 v145, v146, v147\nds_write_b64 %[sta], v[152:153] offset:16256\n.long 0xd3b140a0, 0x18023da0\n.long 0xd3b140a2, 0x18023da2\n"
        "v_cvt_pk_bf16_f32 v156, v156, v157\nv_cvt_pk_bf16_f32 v157, v158, v159\nds_write_b64 %[sta], v[144:145] offset:384\nv_cvt_pk_bf16_f32 v160, v160, v161\nv_cvt_pk_bf16_f32 v161, v162, v163\n"
        "ds_write_b64 %[sta], v[156:157] offset:24192\nds_write_b64 %[sta], v[160:161] offset:32128\nds_write_b32 %[pg], %[sy]\ns_branch .LE%=\n.LB0t%=:\ns_mov_b32 %[s1], 0x100000\n.LB0q%=:\n"
        "ds_read_b32 %[pv], %[sy] offset:4\ns_waitcnt lgkmcnt(0)\nv_readfirstlane_b32 %[s0], %[pv]\ns_nop 0\ns_cmp_ge_i32 %[s0], %[nst]\ns_cbranch_scc1 .LB0u%=\ns_sleep 1\ns_sub_u32 %[s1], %[s1], 1\n"
        "s_cbranch_scc0 .LB0q%=\ns_branch .LB0u%=\n.LC%=:\ns_mov_b32 %[s1], 0x100000\n.LC0w%=:\nds_read_b32 %[pv], %[sy] offset:%[ow]\ns_waitcnt lgkmcnt(0)\nv_readfirstlane_b32 %[s0], %[pv]\ns_nop 0\n"
        "s_cmp_ge_i32 %[s0], 1\ns_cbranch_scc1 .LC0h%=\ns_sleep 1\ns_sub_u32 %[s1], %[s1], 1\ns_cbranch_scc0 .LC0w%=\n.LC0h%=:\ns_mov_b32 %[so], 0\n"
        "buffer_load_dwordx4 v[32:35], %[wa], %[rs], %[so] offen offset:0 sc1\nbuffer_load_dwordx4 v[36:39], %[wa], %[rs], %[so] offen offset:1024 sc1\n"
        "buffer_load_dwordx4 v[40:43], %[wa], %[rs], %[so] offen offset:2048 sc1\nbuffer_load_dwordx4 v[44:47], %[wa], %[rs], %[so] offen offset:3072 sc1\ns_mov_b32 %[so], 15360\n"
        "buffer_load_dwordx4 v[48:51], %[wa], %[rs], %[so] offen offset:0 sc1\nbuffer_load_dwordx4 v[52:55], %[wa], %[rs], %[so] offen offset:1024 sc1\n"
        "buffer_load_dwordx4 v[56:59], %[wa], %[rs], %[so] offen offset:2048 sc1\nbuffer_load_dwordx4 v[60:63], %[wa], %[rs], %[so] offen offset:3072 sc1\ns_mov_b32 %[so], 30720\n"
        "buffer_load_dwordx4 v[64:67], %[wa], %[rs], %[so] offen offset:0 sc1\nbuffer_load_dwordx4 v[68:71], %[wa], %[rs], %[so] offen offset:1024 sc1\n"
        "buffer_load_dwordx4 v[72:75], %[wa], %[rs], %[so] offen offset:2048 sc1\nbuffer_load_dwordx4 v[76:79], %[wa], %[rs], %[so] offen offset:3072 sc1\ns_mov_b32 %[so], 46080\n"
        "buffer_load_dwordx4 v[80:83], %[wa], %[rs], %[so] offen offset:0 sc1\nbuffer_load_dwordx4 v[84:87], %[wa], %[rs], %[so] offen offset:1024 sc1\n"
        "buffer_load_dwordx4 v[88:91], %[wa], %[rs], %[so] offen offset:2048 sc1\nbuffer_load_dwordx4 v[92:95], %[wa], %[rs], %[so] offen offset:3072 sc1\ns_setprio %[pf]\ns_mov_b32 %[s1], 0x100000\n"
        ".LC0v%=:\nds_read_b32 %[pv], %[sy] offset:0\ns_waitcnt lgkmcnt(0)\nv_readfirstlane_b32 %[s0], %[pv]\ns_nop 0\ns_cmp_ge_i32 %[s0], 3\ns_cbranch_scc1 .LC0g%=\ns_sleep 1\n"
        "s_sub_u32 %[s1], %[s1], 1\ns_cbranch_scc0 .LC0v%=\n.LC0g%=:\nds_read_b128 v[24:27], %[xa] offset:6144\nds_read_b128 v[28:31], %[xa] offset:8192\nds_read_b128 v[12:15], %[xa] offset:0\n"
        "ds_read_b128 v[16:19], %[xa] offset:2048\ns_waitcnt vmcnt(12) lgkmcnt(3)\nv_mfma_f32_16x16x32_bf16 v[96:99], v[32:35], v[24:27], 0\nds_read_b128 v[20:23], %[xa] offset:4096\n"
        "v_mfma_f32_16x16x32_bf16 v[116:119], v[36:39], v[24:27], 0\nv_mfma_f32_16x16x32_bf16 v[136:139], v[40:43], v[24:27], 0\nv_mfma_f32_16x16x32_bf16 v[156:159], v[44:47], v[24:27], 0\n"
        "ds_read_b128 v[24:27], %[xb] offset:6144\ns_waitcnt lgkmcnt(4)\nv_mfma_f32_16x16x32_bf16 v[100:103], v[32:35], v[28:31], 0\nv_mfma_f32_16x16x32_bf16 v[120:123], v[36:39], v[28:31], 0\n"
        "v_mfma_f32_16x16x32_bf16 v[140:143], v[40:43], v[28:31], 0\nv_mfma_f32_16x16x32_bf16 v[160:163], v[44:47], v[28:31], 0\nds_read_b128 v[28:31], %[xb] offset:8192\ns_waitcnt lgkmcnt(4)\n"
        "v_mfma_f32_16x16x32_bf16 v[104:107], v[36:39], v[12:15], 0\nv_mfma_f32_16x16x32_bf16 v[124:127], v[40:43], v[12:15], 0\nv_mfma_f32_16x16x32_bf16 v[144:147], v[44:47], v[12:15], 0\n"
        "ds_read_b128 v[12:15], %[xb] offset:0\ns_waitcnt lgkmcnt(4)\nv_mfma_f32_16x16x32_bf16 v[108:111], v[36:39], v[16:19], 0\nv_mfma_f32_16x16x32_bf16 v[128:131], v[40:43], v[16:19], 0\n"
        "v_mfma_f32_16x16x32_bf16 v[148:151], v[44:47], v[16:19], 0\nds_read_b128 v[16:19], %[xb] offset:2048\ns_waitcnt lgkmcnt(4)\nv_mfma_f32_16x16x32_bf16 v[112:115], v[36:39], v[20:23], 0\n"
        "v_mfma_f32_16x16x32_bf16 v[132:135], v[40:43], v[20:23], 0\nv_mfma_f32_16x16x32_bf16 v[152:155], v[44:47], v[20:23], 0\n.LC0l%=:\ns_waitcnt vmcnt(8) lgkmcnt(3)\n"
        "v_mfma_f32_16x16x32_bf16 v[96:99], v[48:51], v[24:27], v[96:99]\ns_addk_i32 %[so], 15360\nds_read_b128 v[20:23], %[xb] offset:4096\n"
        "v_mfma_f32_16x16x32_bf16 v[116:119], v[52:55], v[24:27], v[116:119]\nv_mfma_f32_16x16x32_bf16 v[136:139], v[56:59], v[24:27], v[136:139]\n"
        "buffer_load_dwordx4 v[32:35], %[wa], %[rs], %[so] offen offset:0 sc1\nv_mfma_f32_16x16x32_bf16 v[156:159], v[60:63], v[24:27], v[156:159]\nds_read_b128 v[24:27], %[xa] offset:16384\n"
        "s_waitcnt lgkmcnt(4)\nv_mfma_f32_16x16x32_bf16 v[100:103], v[48:51], v[28:31], v[100:103]\nv_mfma_f32_16x16x32_bf16 v[120:123], v[52:55], v[28:31], v[120:123]\n"
        "buffer_load_dwordx4 v[36:39], %[wa], %[rs], %[so] offen offset:1024 sc1\nv_mfma_f32_16x16x32_bf16 v[140:143], v[56:59], v[28:31], v[140:143]\n"
        "v_mfma_f32_16x16x32_bf16 v[160:163], v[60:63], v[28:31], v[160:163]\nds_read_b128 v[28:31], %[xa] offset:18432\ns_waitcnt lgkmcnt(4)\n"
        "v_mfma_f32_16x16x32_bf16 v[104:107], v[52:55], v[12:15], v[104:107]\nv_mfma_f32_16x16x32_bf16 v[124:127], v[56:59], v[12:15], v[124:127]\n"
        "buffer_load_dwordx4 v[40:43], %[wa], %[rs], %[so] offen offset:2048 sc1\nv_mfma_f32_16x16x32_bf16 v[144:147], v[60:63], v[12:15], v[144:147]\nds_read_b128 v[12:15], %[xa] offset:10240\n"
        "s_waitcnt lgkmcnt(4)\nv_mfma_f32_16x16x32_bf16 v[108:111], v[52:55], v[16:19], v[108:111]\nv_mfma_f32_16x16x32_bf16 v[128:131], v[56:59], v[16:19], v[128:131]\n"
        "buffer_load_dwordx4 v[44:47], %[wa], %[rs], %[so] offen offset:3072 sc1\nv_mfma_f32_16x16x32_bf16 v[148:151], v[60:63], v[16:19], v[148:151]\nds_read_b128 v[16:19], %[xa] offset:12288\n"
        "s_waitcnt lgkmcnt(4)\nv_mfma_f32_16x16x32_bf16 v[112:115], v[52:55], v[20:23], v[112:115]\nv_add_u32 %[sy], 4, %[sy]\nv_mfma_f32_16x16x32_bf16 v[132:135], v[56:59], v[20:23], v[132:135]\n"
        "ds_write_b32 %[pg], %[sy]\nv_mfma_f32_16x16x32_bf16 v[152:155], v[60:63], v[20:23], v[152:155]\ns_waitcnt vmcnt(8) lgkmcnt(4)\nv_mfma_f32_16x16x32_bf16 v[96:99], v[64:67], v[24:27], v[96:99]\n"
        "s_addk_i32 %[so], 15360\nds_read_b128 v[20:23], %[xa] offset:14336\nv_mfma_f32_16x16x32_bf16 v[116:119], v[68:71], v[24:27], v[116:119]\n"
        "v_mfma_f32_16x16x32_bf16 v[136:139], v[72:75], v[24:27], v[136:139]\nbuffer_load_dwordx4 v[48:51], %[wa], %[rs], %[so] offen offset:0 sc1\n"
        "v_mfma_f32_16x16x32_bf16 v[156:159], v[76:79], v[24:27], v[156:159]\nds_read_b128 v[24:27], %[xb] offset:16384\ns_waitcnt lgkmcnt(5)\n"
        "v_mfma_f32_16x16x32_bf16 v[100:103], v[64:67], v[28:31], v[100:103]\nv_mfma_f32_16x16x32_bf16 v[120:123], v[68:71], v[28:31], v[120:123]\n"
        "buffer_load_dwordx4 v[52:55], %[wa], %[rs], %[so] offen offset:1024 sc1\nv_mfma_f32_16x16x32_bf16 v[140:143], v[72:75], v[28:31], v[140:143]\n"
        "v_mfma_f32_16x16x32_bf16 v[160:163], v[76:79], v[28:31], v[160:163]\nds_read_b128 v[28:31], %[xb] offset:18432\ns_waitcnt lgkmcnt(5)\n"
        "v_mfma_f32_16x16x32_bf16 v[104:107], v[68:71], v[12:15], v[104:107]\nv_mfma_f32_16x16x32_bf16 v[124:127], v[72:75], v[12:15], v[124:127]\n"
        "buffer_load_dwordx4 v[56:59], %[wa], %[rs], %[so] offen offset:2048 sc1\nv_mfma_f32_16x16x32_bf16 v[144:147], v[76:79], v[12:15], v[144:147]\nds_read_b128 v[12:15], %[xb] offset:10240\n"
        "s_waitcnt lgkmcnt(5)\nv_mfma_f32_16x16x32_bf16 v[108:111], v[68:71], v[16:19], v[108:111]\nv_mfma_f32_16x16x32_bf16 v[128:131], v[72:75], v[16:19], v[128:131]\n"
        "buffer_load_dwordx4 v[60:63], %[wa], %[rs], %[so] offen offset:3072 sc1\nv_mfma_f32_16x16x32_bf16 v[148:151], v[76:79], v[16:19], v[148:151]\nds_read_b128 v[16:19], %[xb] offset:12288\n"
        "s_waitcnt lgkmcnt(4)\nv_mfma_f32_16x16x32_bf16 v[112:115], v[68:71], v[20:23], v[112:115]\nv_mfma_f32_16x16x32_bf16 v[132:135], v[72:75], v[20:23], v[132:135]\n"
        "v_mfma_f32_16x16x32_bf16 v[152:155], v[76:79], v[20:23], v[152:155]\ns_waitcnt vmcnt(8) lgkmcnt(3)\nv_mfma_f32_16x16x32_bf16 v[96:99], v[80:83], v[24:27], v[96:99]\ns_addk_i32 %[so], 15360\n"
        "ds_read_b128 v[20:23], %[xb] offset:14336\nv_mfma_f32_16x16x32_bf16 v[116:119], v[84:87], v[24:27], v[116:119]\nv_mfma_f32_16x16x32_bf16 v[136:139], v[88:91], v[24:27], v[136:139]\n"
        "buffer_load_dwordx4 v[64:67], %[wa], %[rs], %[so] offen offset:0 sc1\nv_mfma_f32_16x16x32_bf16 v[156:159], v[92:95], v[24:27], v[156:159]\nds_read_b128 v[24:27], %[xa] offset:26624\n"
        "s_waitcnt lgkmcnt(4)\nv_mfma_f32_16x16x32_bf16 v[100:103], v[80:83], v[28:31], v[100:103]\nv_mfma_f32_16x16x32_bf16 v[120:123], v[84:87], v[28:31], v[120:123]\n"
        "buffer_load_dwordx4 v[68:71], %[wa], %[rs], %[so] offen offset:1024 sc1\nv_mfma_f32_16x16x32_bf16 v[140:143], v[88:91], v[28:31], v[140:143]\n"
        "v_mfma_f32_16x16x32_bf16 v[160:163], v[92:95], v[28:31], v[160:163]\nds_read_b128 v[28:31], %[xa] offset:28672\ns_waitcnt lgkmcnt(4)\n"
        "v_mfma_f32_16x16x32_bf16 v[104:107], v[84:87], v[12:15], v[104:107]\nv_mfma_f32_16x16x32_bf16 v[124:127], v[88:91], v[12:15], v[124:127]\n"
        "buffer_load_dwordx4 v[72:75], %[wa], %[rs], %[so] offen offset:2048 sc1\nv_mfma_f32_16x16x32_bf16 v[144:147], v[92:95], v[12:15], v[144:147]\nds_read_b128 v[12:15], %[xa] offset:20480\n"
        "s_waitcnt lgkmcnt(4)\nv_mfma_f32_16x16x32_bf16 v[108:111], v[84:87], v[16:19], v[108:111]\nv_mfma_f32_16x16x32_bf16 v[128:131], v[88:91], v[16:19], v[128:131]\n"
        "buffer_load_dwordx4 v[76:79], %[wa], %[rs], %[so] offen offset:3072 sc1\nv_mfma_f32_16x16x32_bf16 v[148:151], v[92:95], v[16:19], v[148:151]\nds_read_b128 v[16:19], %[xa] offset:22528\n"
        "s_waitcnt lgkmcnt(4)\nv_mfma_f32_16x16x32_bf16 v[112:115], v[84:87], v[20:23], v[112:115]\nv_add_u32 %[sy], 4, %[sy]\nv_mfma_f32_16x16x32_bf16 v[132:135], v[88:91], v[20:23], v[132:135]\n"
        "ds_write_b32 %[pg], %[sy]\nv_mfma_f32_16x16x32_bf16 v[152:155], v[92:95], v[20:23], v[152:155]\ns_waitcnt vmcnt(8) lgkmcnt(4)\nv_mfma_f32_16x16x32_bf16 v[96:99], v[32:35], v[24:27], v[96:99]\n"
        "s_addk_i32 %[so], 15360\nds_read_b128 v[20:23], %[xa] offset:24576\nv_mfma_f32_16x16x32_bf16 v[116:119], v[36:39], v[24:27], v[116:119]\n"
        "v_mfma_f32_16x16x32_bf16 v[136:139], v[40:43], v[24:27], v[136:139]\nbuffer_load_dwordx4 v[80:83], %[wa], %[rs], %[so] offen offset:0 sc1\n"
        "v_mfma_f32_16x16x32_bf16 v[156:159], v[44:47], v[24:27], v[156:159]\nds_read_b128 v[24:27], %[xb] offset:26624\ns_waitcnt lgkmcnt(5)\n"
        "v_mfma_f32_16x16x32_bf16 v[100:103], v[32:35], v[28:31], v[100:103]\nv_mfma_f32_16x16x32_bf16 v[120:123], v[36:39], v[28:31], v[120:123]\n"
        "buffer_load_dwordx4 v[84:87], %[wa], %[rs], %[so] offen offset:1024 sc1\nv_mfma_f32_16x16x32_bf16 v[140:143], v[40:43], v[28:31], v[140:143]\n"
        "v_mfma_f32_16x16x32_bf16 v[160:163], v[44:47], v[28:31], v[160:163]\nds_read_b128 v[28:31], %[xb] offset:28672\ns_waitcnt lgkmcnt(5)\n"
        "v_mfma_f32_16x16x32_bf16 v[104:107], v[36:39], v[12:15], v[104:107]\nv_mfma_f32_16x16x32_bf16 v[124:127], v[40:43], v[12:15], v[124:127]\n"
        "buffer_load_dwordx4 v[88:91], %[wa], %[rs], %[so] offen offset:2048 sc1\nv_mfma_f32_16x16x32_bf16 v[144:147], v[44:47], v[12:15], v[144:147]\nds_read_b128 v[12:15], %[xb] offset:20480\n"
        "s_waitcnt lgkmcnt(5)\nv_mfma_f32_16x16x32_bf16 v[108:111], v[36:39], v[16:19], v[108:111]\nv_mfma_f32_16x16x32_bf16 v[128:131], v[40:43], v[16:19], v[128:131]\n"
        "buffer_load_dwordx4 v[92:95], %[wa], %[rs], %[so] offen offset:3072 sc1\nv_mfma_f32_16x16x32_bf16 v[148:151], v[44:47], v[16:19], v[148:151]\nds_read_b128 v[16:19], %[xb] offset:22528\n"
        "s_waitcnt lgkmcnt(4)\nv_mfma_f32_16x16x32_bf16 v[112:115], v[36:39], v[20:23], v[112:115]\nv_mfma_f32_16x16x32_bf16 v[132:135], v[40:43], v[20:23], v[132:135]\ns_cmp_lg_u32 %[so], 291840\n"
        "v_mfma_f32_16x16x32_bf16 v[152:155], v[44:47], v[20:23], v[152:155]\nv_add_u32 %[xa], 20480, %[xa]\nv_add_u32 %[xb], 20480, %[xb]\ns_cbranch_scc1 .LC0l%=\ns_waitcnt vmcnt(8) lgkmcnt(3)\n"
        "v_mfma_f32_16x16x32_bf16 v[96:99], v[48:51], v[24:27], v[96:99]\nds_read_b128 v[20:23], %[xb] offset:4096\nv_mfma_f32_16x16x32_bf16 v[116:119], v[52:55], v[24:27], v[116:119]\n"
        "v_mfma_f32_16x16x32_bf16 v[136:139], v[56:59], v[24:27], v[136:139]\nv_mfma_f32_16x16x32_bf16 v[156:159], v[60:63], v[24:27], v[156:159]\nds_read_b128 v[24:27], %[xa] offset:16384\n"
        "s_waitcnt lgkmcnt(4)\nv_mfma_f32_16x16x32_bf16 v[100:103], v[48:51], v[28:31], v[100:103]\nv_mfma_f32_16x16x32_bf16 v[120:123], v[52:55], v[28:31], v[120:123]\n"
        "v_mfma_f32_16x16x32_bf16 v[140:143], v[56:59], v[28:31], v[140:143]\nv_mfma_f32_16x16x32_bf16 v[160:163], v[60:63], v[28:31], v[160:163]\nds_read_b128 v[28:31], %[xa] offset:18432\n"
        "s_waitcnt lgkmcnt(4)\nv_mfma_f32_16x16x32_bf16 v[104:107], v[52:55], v[12:15], v[104:107]\nv_mfma_f32_16x16x32_bf16 v[124:127], v[56:59], v[12:15], v[124:127]\n"
        "v_mfma_f32_16x16x32_bf16 v[144:147], v[60:63], v[12:15], v[144:147]\nds_read_b128 v[12:15], %[xa] offset:10240\ns_waitcnt lgkmcnt(4)\n"
        "v_mfma_f32_16x16x32_bf16 v[108:111], v[52:55], v[16:19], v[108:111]\nv_mfma_f32_16x16x32_bf16 v[128:131], v[56:59], v[16:19], v[128:131]\n"
        "v_mfma_f32_16x16x32_bf16 v[148:151], v[60:63], v[16:19], v[148:151]\nds_read_b128 v[16:19], %[xa] offset:12288\ns_waitcnt lgkmcnt(4)\n"
        "v_mfma_f32_16x16x32_bf16 v[112:115], v[52:55], v[20:23], v[112:115]\nv_add_u32 %[sy], 4, %[sy]\nv_mfma_f32_16x16x32_bf16 v[132:135], v[56:59], v[20:23], v[132:135]\n"
        "v_mfma_f32_16x16x32_bf16 v[152:155], v[60:63], v[20:23], v[152:155]\ns_waitcnt vmcnt(4) lgkmcnt(3)\nv_mfma_f32_16x16x32_bf16 v[96:99], v[64:67], v[24:27], v[96:99]\n"
        "ds_read_b128 v[20:23], %[xa] offset:14336\nv_mfma_f32_16x16x32_bf16 v[116:119], v[68:71], v[24:27], v[116:119]\nv_mfma_f32_16x16x32_bf16 v[136:139], v[72:75], v[24:27], v[136:139]\n"
        "ds_read_b32 %[pv], %[sy] offset:4\nv_mfma_f32_16x16x32_bf16 v[156:159], v[76:79], v[24:27], v[156:159]\nds_read_b128 v[24:27], %[xb] offset:16384\ns_waitcnt lgkmcnt(5)\n"
        "v_mfma_f32_16x16x32_bf16 v[100:103], v[64:67], v[28:31], v[100:103]\nv_mfma_f32_16x16x32_bf16 v[120:123], v[68:71], v[28:31], v[120:123]\n"
        "v_mfma_f32_16x16x32_bf16 v[140:143], v[72:75], v[28:31], v[140:143]\nv_mfma_f32_16x16x32_bf16 v[160:163], v[76:79], v[28:31], v[160:163]\nds_read_b128 v[28:31], %[xb] offset:18432\n"
        "s_waitcnt lgkmcnt(5)\nv_mfma_f32_16x16x32_bf16 v[104:107], v[68:71], v[12:15], v[104:107]\nv_mfma_f32_16x16x32_bf16 v[124:127], v[72:75], v[12:15], v[124:127]\n"
        "v_mfma_f32_16x16x32_bf16 v[144:147], v[76:79], v[12:15], v[144:147]\nds_read_b128 v[12:15], %[xb] offset:10240\ns_waitcnt lgkmcnt(5)\n"
        "v_mfma_f32_16x16x32_bf16 v[108:111], v[68:71], v[16:19], v[108:111]\nv_mfma_f32_16x16x32_bf16 v[128:131], v[72:75], v[16:19], v[128:131]\n"
        "v_mfma_f32_16x16x32_bf16 v[148:151], v[76:79], v[16:19], v[148:151]\nds_read_b128 v[16:19], %[xb] offset:12288\ns_waitcnt lgkmcnt(5)\n"
        "v_mfma_f32_16x16x32_bf16 v[112:115], v[68:71], v[20:23], v[112:115]\nv_mfma_f32_16x16x32_bf16 v[132:135], v[72:75], v[20:23], v[132:135]\n"
        "v_mfma_f32_16x16x32_bf16 v[152:155], v[76:79], v[20:23], v[152:155]\ns_waitcnt vmcnt(0) lgkmcnt(3)\nv_mfma_f32_16x16x32_bf16 v[96:99], v[80:83], v[24:27], v[96:99]\n"
        "v_readfirstlane_b32 %[s0], %[pv]\nds_read_b128 v[20:23], %[xb] offset:14336\nv_mfma_f32_16x16x32_bf16 v[116:119], v[84:87], v[24:27], v[116:119]\n"
        "v_mfma_f32_16x16x32_bf16 v[136:139], v[88:91], v[24:27], v[136:139]\ns_cmp_ge_i32 %[s0], %[nst]\nv_mfma_f32_16x16x32_bf16 v[156:159], v[92:95], v[24:27], v[156:159]\ns_cbranch_scc0 .LC0t%=\n"
        ".LC0u%=:\ns_waitcnt lgkmcnt(3)\nv_mfma_f32_16x16x32_bf16 v[100:103], v[80:83], v[28:31], v[100:103]\nv_add_u32 %[sy], 4, %[sy]\nds_read_b128 v[24:27], %[mra] offset:768\n"
        "v_mfma_f32_16x16x32_bf16 v[120:123], v[84:87], v[28:31], v[120:123]\nv_mfma_f32_16x16x32_bf16 v[140:143], v[88:91], v[28:31], v[140:143]\n"
        "v_mfma_f32_16x16x32_bf16 v[160:163], v[92:95], v[28:31], v[160:163]\nds_read_b128 v[32:35], %[cwa] offset:448\ns_waitcnt lgkmcnt(4)\n"
        "v_mfma_f32_16x16x32_bf16 v[104:107], v[84:87], v[12:15], v[104:107]\nds_read_b128 v[36:39], %[cwa] offset:256\nds_read_b128 v[28:31], %[mra] offset:1024\n"
        "v_mfma_f32_16x16x32_bf16 v[124:127], v[88:91], v[12:15], v[124:127]\nds_read_b128 v[40:43], %[cwa] offset:832\nv_mfma_f32_16x16x32_bf16 v[144:147], v[92:95], v[12:15], v[144:147]\n"
        "ds_read_b128 v[44:47], %[cwa] offset:896\ns_waitcnt lgkmcnt(7)\nv_mfma_f32_16x16x32_bf16 v[108:111], v[84:87], v[16:19], v[108:111]\nv_add_u32 %[sy], 4, %[sy]\n"
        "ds_read_b128 v[12:15], %[mra] offset:0\nv_mfma_f32_16x16x32_bf16 v[128:131], v[88:91], v[16:19], v[128:131]\nv_mfma_f32_16x16x32_bf16 v[148:151], v[92:95], v[16:19], v[148:151]\n"
        "ds_read_b128 v[16:19], %[mra] offset:256\ns_waitcnt lgkmcnt(8)\nv_mfma_f32_16x16x32_bf16 v[112:115], v[84:87], v[20:23], v[112:115]\n"
        "v_mfma_f32_16x16x32_bf16 v[132:135], v[88:91], v[20:23], v[132:135]\nv_mfma_f32_16x16x32_bf16 v[152:155], v[92:95], v[20:23], v[152:155]\nds_read_b128 v[20:23], %[mra] offset:512\n"
        "s_setprio %[pfs]\ns_waitcnt lgkmcnt(1)\n.long 0xd3b04060, 0x1d823120\n.long 0xd3b04062, 0x1d8a3122\n.long 0xd3b04074, 0x1dd23124\n.long 0xd3b04076, 0x1dda3126\n.long 0xd3b14060, 0x18023560\n"
        ".long 0xd3b14062, 0x18023562\n.long 0xd3b04064, 0x1d923920\n.long 0xd3b04066, 0x1d9a3922\n.long 0xd3b14074, 0x18023574\n.long 0xd3b14076, 0x18023576\nv_cvt_pk_bf16_f32 v96, v96, v97\n"
        "v_cvt_pk_bf16_f32 v97, v98, v99\n.long 0xd3b04078, 0x1de23924\n.long 0xd3b0407a, 0x1dea3926\n.long 0xd3b14064, 0x18023d64\n.long 0xd3b14066, 0x18023d66\nv_cvt_pk_bf16_f32 v116, v116, v117\n"
        "v_cvt_pk_bf16_f32 v117, v118, v119\nds_write_b64 %[sta], v[96:97] offset:24032\n.long 0xd3b04068, 0x1da21924\n.long 0xd3b0406a, 0x1daa1926\n.long 0xd3b14078, 0x18023d78\n"
        ".long 0xd3b1407a, 0x18023d7a\nv_cvt_pk_bf16_f32 v100, v100, v101\nv_cvt_pk_bf16_f32 v101, v102, v103\nds_write_b64 %[sta], v[116:117] offset:23936\n.long 0xd3b0406c, 0x1db22124\n"
        ".long 0xd3b0406e, 0x1dba2126\n.long 0xd3b14068, 0x18021d68\n.long 0xd3b1406a, 0x18021d6a\nv_cvt_pk_bf16_f32 v120, v120, v121\nv_cvt_pk_bf16_f32 v121, v122, v123\n"
        "ds_write_b64 %[sta], v[100:101] offset:31968\ns_waitcnt lgkmcnt(3)\n.long 0xd3b04070, 0x1dc22924\n.long 0xd3b04072, 0x1dca2926\n.long 0xd3b1406c, 0x1802256c\n.long 0xd3b1406e, 0x1802256e\n"
        "v_cvt_pk_bf16_f32 v104, v104, v105\nv_cvt_pk_bf16_f32 v105, v106, v107\nds_write_b64 %[sta], v[120:121] offset:31872\n.long 0xd3b14070, 0x18022d70\n.long 0xd3b14072, 0x18022d72\n"
        "v_cvt_pk_bf16_f32 v108, v108, v109\nv_cvt_pk_bf16_f32 v109, v110, v111\nds_write_b64 %[sta], v[104:105] offset:128\nv_cvt_pk_bf16_f32 v112, v112, v113\nv_cvt_pk_bf16_f32 v113, v114, v115\n"
        "ds_write_b64 %[sta], v[108:109] offset:8064\nds_write_b64 %[sta], v[112:113] offset:16000\nds_write_b32 %[pg], %[sy]\nv_add_u32 %[sy], 4, %[sy]\n.long 0xd3b04088, 0x1e223128\n"
        ".long 0xd3b0408a, 0x1e2a312a\n.long 0xd3b0409c, 0x1e72312c\n.long 0xd3b0409e, 0x1e7a312e\n.long 0xd3b14088, 0x18023588\n.long 0xd3b1408a, 0x1802358a\n.long 0xd3b0408c, 0x1e323928\n"
        ".long 0xd3b0408e, 0x1e3a392a\n.long 0xd3b1409c, 0x1802359c\n.long 0xd3b1409e, 0x1802359e\nv_cvt_pk_bf16_f32 v136, v136, v137\nv_cvt_pk_bf16_f32 v137, v138, v139\n.long 0xd3b040a0, 0x1e82392c\n"
        ".long 0xd3b040a2, 0x1e8a392e\n.long 0xd3b1408c, 0x18023d8c\n.long 0xd3b1408e, 0x18023d8e\nv_cvt_pk_bf16_f32 v156, v156, v157\nv_cvt_pk_bf16_f32 v157, v158, v159\n"
        "ds_write_b64 %[sta], v[136:137] offset:24224\n.long 0xd3b0407c, 0x1df21928\n.long 0xd3b0407e, 0x1dfa192a\n.long 0xd3b140a0, 0x18023da0\n.long 0xd3b140a2, 0x18023da2\n"
        "v_cvt_pk_bf16_f32 v140, v140, v141\nv_cvt_pk_bf16_f32 v141, v142, v143\nds_write_b64 %[sta], v[156:157] offset:24256\n.long 0xd3b04090, 0x1e42192c\n.long 0xd3b04092, 0x1e4a192e\n"
        ".long 0xd3b1407c, 0x18021d7c\n.long 0xd3b1407e, 0x18021d7e\nv_cvt_pk_bf16_f32 v160, v160, v161\nv_cvt_pk_bf16_f32 v161, v162, v163\nds_write_b64 %[sta], v[140:141] offset:32160\n"
        ".long 0xd3b04080, 0x1e022128\n.long 0xd3b04082, 0x1e0a212a\n.long 0xd3b14090, 0x18021d90\n.long 0xd3b14092, 0x18021d92\nv_cvt_pk_bf16_f32 v124, v124, v125\nv_cvt_pk_bf16_f32 v125, v126, v127\n"
        "ds_write_b64 %[sta], v[160:161] offset:32192\n.long 0xd3b04094, 0x1e52212c\n.long 0xd3b04096, 0x1e5a212e\n.long 0xd3b14080, 0x18022580\n.long 0xd3b14082, 0x18022582\n"
        "v_cvt_pk_bf16_f32 v144, v144, v145\nv_cvt_pk_bf16_f32 v145, v146, v147\nds_write_b64 %[sta], v[124:125] offset:416\n.long 0xd3b04084, 0x1e122928\n.long 0xd3b04086, 0x1e1a292a\n"
        ".long 0xd3b14094, 0x18022594\n.long 0xd3b14096, 0x18022596\nv_cvt_pk_bf16_f32 v128, v128, v129\nv_cvt_pk_bf16_f32 v129, v130, v131\nds_write_b64 %[sta], v[144:145] offset:448\n"
        ".long 0xd3b04098, 0x1e62292c\n.long 0xd3b0409a, 0x1e6a292e\n.long 0xd3b14084, 0x18022d84\n.long 0xd3b14086, 0x18022d86\nv_cvt_pk_bf16_f32 v148, v148, v149\nv_cvt_pk_bf16_f32 v149, v150, v151\n"
        "ds_write_b64 %[sta], v[128:129] offset:8352\n.long 0xd3b14098, 0x18022d98\n.long 0xd3b1409a, 0x18022d9a\nv_cvt_pk_bf16_f32 v132, v132, v133\nv_cvt_pk_bf16_f32 v133, v134, v135\n"
        "ds_write_b64 %[sta], v[148:149] offset:8384\nv_cvt_pk_bf16_f32 v152, v152, v153\nv_cvt_pk_bf16_f32 v153, v154, v155\nds_write_b64 %[sta], v[132:133] offset:16288\n"
        "ds_write_b64 %[sta], v[152:153] offset:16320\nds_write_b32 %[pg], %[sy]\ns_branch .LE%=\n.LC0t%=:\ns_mov_b32 %[s1], 0x100000\n.LC0q%=:\nds_read_b32 %[pv], %[sy] offset:4\n"
        "s_waitcnt lgkmcnt(0)\nv_readfirstlane_b32 %[s0], %[pv]\ns_nop 0\ns_cmp_ge_i32 %[s0], %[nst]\ns_cbranch_scc1 .LC0u%=\ns_sleep 1\ns_sub_u32 %[s1], %[s1], 1\ns_cbranch_scc0 .LC0q%=\n"
        "s_branch .LC0u%=\n.LE%=:\n"
        : [pv] "=&v"(pv), [xa] "+v"(xa), [xb] "+v"(xb), [sy] "+v"(sy), [so] "=&s"(so), [s0] "=&s"(s0_), [s1] "=&s"(s1_)
        : [wa] "v"(wa), [rs] "s"(wrsrc), [pg] "v"(pga), [cwa] "v"(cwa), [mra] "v"(mra), [sta] "v"(sta), [w] "s"(w), [ow] "i"(SY_W - SY_X), [pfs] "i"(PFS), [pf] "i"(PF), [nst] "i"(NST0)
        : "memory", "scc", "v12", "v13", "v14", "v15", "v16", "v17", "v18", "v19", "v20", "v21", "v22", "v23", "v24", "v25", "v26", "v27", "v28", "v29", "v30", "v31", "v32", "v33", "v34", "v35", "v36", "v37", "v38", "v39", "v40", "v41", "v42", "v43", "v44", "v45", "v46", "v47", "v48", "v49", "v50", "v51", "v52", "v53", "v54", "v55", "v56", "v57", "v58", "v59", "v60", "v61", "v62", "v63", "v64", "v65", "v66", "v67", "v68", "v69", "v70", "v71", "v72", "v73", "v74", "v75", "v76", "v77", "v78", "v79", "v80", "v81", "v82", "v83", "v84", "v85", "v86", "v87", "v88", "v89", "v90", "v91", "v92", "v93", "v94", "v95", "v96", "v97", "v98", "v99", "v100", "v101", "v102", "v103", "v104", "v105", "v106", "v107", "v108", "v109", "v110", "v111", "v112", "v113", "v114", "v115", "v116", "v117", "v118", "v119", "v120", "v121", "v122", "v123", "v124", "v125", "v126", "v127", "v128", "v129", "v130", "v131", "v132", "v133", "v134", "v135", "v136", "v137", "v138", "v139", "v140", "v141", "v142", "v143", "v144", "v145", "v146", "v147", "v148", "v149", "v150", "v151", "v152", "v153", "v154", "v155", "v156", "v157", "v158", "v159", "v160", "v161", "v162", "v163", "v164", "v165", "v166", "v167");
    } else {
      asm volatile(
        "s_cmp_lt_u32 %[w], 2\ns_cbranch_scc0 .LB%=\ns_mov_b32 %[so], 0\nbuffer_load_dwordx4 v[32:35], %[wa], %[rs], %[so] offen offset:0 sc1\n"
        "buffer_load_dwordx4 v[36:39], %[wa], %[rs], %[so] offen offset:1024 sc1\nbuffer_load_dwordx4 v[40:43], %[wa], %[rs], %[so] offen offset:2048 sc1\n"
        "buffer_load_dwordx4 v[44:47], %[wa], %[rs], %[so] offen offset:3072 sc1\ns_mov_b32 %[so], 15360\nbuffer_load_dwordx4 v[48:51], %[wa], %[rs], %[so] offen offset:0 sc1\n"
        "buffer_load_dwordx4 v[52:55], %[wa], %[rs], %[so] offen offset:1024 sc1\nbuffer_load_dwordx4 v[56:59], %[wa], %[rs], %[so] offen offset:2048 sc1\n"
        "buffer_load_dwordx4 v[60:63], %[wa], %[rs], %[so] offen offset:3072 sc1\ns_mov_b32 %[so], 30720\nbuffer_load_dwordx4 v[64:67], %[wa], %[rs], %[so] offen offset:0 sc1\n"
        "buffer_load_dwordx4 v[68:71], %[wa], %[rs], %[so] offen offset:1024 sc1\nbuffer_load_dwordx4 v[72:75], %[wa], %[rs], %[so] offen offset:2048 sc1\n"
        "buffer_load_dwordx4 v[76:79], %[wa], %[rs], %[so] offen offset:3072 sc1\ns_mov_b32 %[so], 46080\nbuffer_load_dwordx4 v[80:83], %[wa], %[rs], %[so] offen offset:0 sc1\n"
        "buffer_load_dwordx4 v[84:87], %[wa], %[rs], %[so] offen offset:1024 sc1\nbuffer_load_dwordx4 v[88:91], %[wa], %[rs], %[so] offen offset:2048 sc1\n"
        "buffer_load_dwordx4 v[92:95], %[wa], %[rs], %[so] offen offset:3072 sc1\ns_barrier\ns_mov_b32 %[s1], 0x100000\n.LA1w%=:\nds_read_b32 %[pv], %[sy] offset:0\ns_waitcnt lgkmcnt(0)\n"
        "v_readfirstlane_b32 %[s0], %[pv]\ns_nop 0\ns_cmp_ge_i32 %[s0], 6\ns_cbranch_scc1 .LA1g%=\ns_sleep 1\ns_sub_u32 %[s1], %[s1], 1\ns_cbranch_scc0 .LA1w%=\n.LA1g%=:\n"
        "ds_read_b64_tr_b16 v[16:17], %[xa] offset:1056\nds_read_b64_tr_b16 v[18:19], %[xa] offset:1568\nds_read_b64_tr_b16 v[20:21], %[xa] offset:2112\nds_read_b64_tr_b16 v[22:23], %[xa] offset:2624\n"
        "ds_read_b64_tr_b16 v[12:13], %[xa] offset:0\nds_read_b64_tr_b16 v[14:15], %[xa] offset:512\nds_read_b64_tr_b16 v[24:25], %[xa] offset:3168\nds_read_b64_tr_b16 v[26:27], %[xa] offset:3680\n"
        "s_waitcnt vmcnt(12) lgkmcnt(6)\nv_mfma_f32_16x16x32_bf16 v[100:103], v[32:35], v[16:19], 0\nds_read_b64_tr_b16 v[28:29], %[xa] offset:4224\n"
        "v_mfma_f32_16x16x32_bf16 v[116:119], v[36:39], v[16:19], 0\nds_read_b64_tr_b16 v[30:31], %[xa] offset:4736\nv_mfma_f32_16x16x32_bf16 v[136:139], v[40:43], v[16:19], 0\n"
        "ds_read_b32 %[pv], %[sy] offset:4\nv_mfma_f32_16x16x32_bf16 v[152:155], v[44:47], v[16:19], 0\nds_read_b64_tr_b16 v[16:17], %[xa] offset:6336\ns_waitcnt lgkmcnt(8)\n"
        "v_mfma_f32_16x16x32_bf16 v[104:107], v[32:35], v[20:23], 0\nds_read_b64_tr_b16 v[18:19], %[xa] offset:6848\nv_mfma_f32_16x16x32_bf16 v[120:123], v[36:39], v[20:23], 0\n"
        "v_mfma_f32_16x16x32_bf16 v[140:143], v[40:43], v[20:23], 0\nv_mfma_f32_16x16x32_bf16 v[156:159], v[44:47], v[20:23], 0\nds_read_b64_tr_b16 v[20:21], %[xa] offset:7392\ns_waitcnt lgkmcnt(8)\n"
        "v_mfma_f32_16x16x32_bf16 v[96:99], v[32:35], v[12:15], 0\nds_read_b64_tr_b16 v[22:23], %[xa] offset:7904\nv_mfma_f32_16x16x32_bf16 v[132:135], v[40:43], v[12:15], 0\n"
        "ds_read_b64_tr_b16 v[12:13], %[xa] offset:5280\ns_waitcnt lgkmcnt(8)\nv_mfma_f32_16x16x32_bf16 v[108:111], v[32:35], v[24:27], 0\nds_read_b64_tr_b16 v[14:15], %[xa] offset:5792\n"
        "v_mfma_f32_16x16x32_bf16 v[124:127], v[36:39], v[24:27], 0\nv_mfma_f32_16x16x32_bf16 v[144:147], v[40:43], v[24:27], 0\nv_mfma_f32_16x16x32_bf16 v[160:163], v[44:47], v[24:27], 0\n"
        "ds_read_b64_tr_b16 v[24:25], %[xa] offset:8448\ns_waitcnt lgkmcnt(8)\nv_mfma_f32_16x16x32_bf16 v[112:115], v[32:35], v[28:31], 0\nds_read_b64_tr_b16 v[26:27], %[xa] offset:8960\n"
        "v_mfma_f32_16x16x32_bf16 v[128:131], v[36:39], v[28:31], 0\nv_mfma_f32_16x16x32_bf16 v[148:151], v[40:43], v[28:31], 0\nv_mfma_f32_16x16x32_bf16 v[164:167], v[44:47], v[28:31], 0\n.LA1l%=:\n"
        "s_waitcnt vmcnt(8) lgkmcnt(6)\nv_mfma_f32_16x16x32_bf16 v[100:103], v[48:51], v[16:19], v[100:103]\ns_addk_i32 %[so], 15360\nv_readfirstlane_b32 %[s0], %[pv]\n"
        "v_mfma_f32_16x16x32_bf16 v[116:119], v[52:55], v[16:19], v[116:119]\ns_cmp_ge_i32 %[s0], 6\nds_read_b64_tr_b16 v[28:29], %[xa] offset:9504\n"
        "v_mfma_f32_16x16x32_bf16 v[136:139], v[56:59], v[16:19], v[136:139]\ns_cbranch_scc0 .LA1s0_%=\n.LA1r0_%=:\nds_read_b64_tr_b16 v[30:31], %[xa] offset:10016\n"
        "v_mfma_f32_16x16x32_bf16 v[152:155], v[60:63], v[16:19], v[152:155]\nds_read_b64_tr_b16 v[16:17], %[xa] offset:11616\ns_waitcnt lgkmcnt(7)\n"
        "v_mfma_f32_16x16x32_bf16 v[104:107], v[48:51], v[20:23], v[104:107]\nds_read_b64_tr_b16 v[18:19], %[xa] offset:12128\nbuffer_load_dwordx4 v[32:35], %[wa], %[rs], %[so] offen offset:0 sc1\n"
        "v_mfma_f32_16x16x32_bf16 v[120:123], v[52:55], v[20:23], v[120:123]\nbuffer_load_dwordx4 v[36:39], %[wa], %[rs], %[so] offen offset:1024 sc1\n"
        "v_mfma_f32_16x16x32_bf16 v[140:143], v[56:59], v[20:23], v[140:143]\nv_mfma_f32_16x16x32_bf16 v[156:159], v[60:63], v[20:23], v[156:159]\nds_read_b64_tr_b16 v[20:21], %[xa] offset:12672\n"
        "s_waitcnt lgkmcnt(7)\nv_mfma_f32_16x16x32_bf16 v[96:99], v[48:51], v[12:15], v[96:99]\nds_read_b64_tr_b16 v[22:23], %[xa] offset:13184\n"
        "v_mfma_f32_16x16x32_bf16 v[132:135], v[56:59], v[12:15], v[132:135]\nds_read_b64_tr_b16 v[12:13], %[xa] offset:10560\ns_waitcnt lgkmcnt(7)\n"
        "v_mfma_f32_16x16x32_bf16 v[108:111], v[48:51], v[24:27], v[108:111]\nds_read_b64_tr_b16 v[14:15], %[xa] offset:11072\nv_mfma_f32_16x16x32_bf16 v[124:127], v[52:55], v[24:27], v[124:127]\n"
        "buffer_load_dwordx4 v[40:43], %[wa], %[rs], %[so] offen offset:2048 sc1\nv_mfma_f32_16x16x32_bf16 v[144:147], v[56:59], v[24:27], v[144:147]\n"
        "buffer_load_dwordx4 v[44:47], %[wa], %[rs], %[so] offen offset:3072 sc1\nv_mfma_f32_16x16x32_bf16 v[160:163], v[60:63], v[24:27], v[160:163]\nds_read_b64_tr_b16 v[24:25], %[xa] offset:13728\n"
        "s_waitcnt lgkmcnt(7)\nv_mfma_f32_16x16x32_bf16 v[112:115], v[48:51], v[28:31], v[112:115]\nds_read_b64_tr_b16 v[26:27], %[xa] offset:14240\n"
        "v_mfma_f32_16x16x32_bf16 v[128:131], v[52:55], v[28:31], v[128:131]\nv_add_u32 %[sy], 4, %[sy]\nv_mfma_f32_16x16x32_bf16 v[148:151], v[56:59], v[28:31], v[148:151]\nds_write_b32 %[pg], %[sy]\n"
        "v_mfma_f32_16x16x32_bf16 v[164:167], v[60:63], v[28:31], v[164:167]\ns_waitcnt vmcnt(8) lgkmcnt(7)\nv_mfma_f32_16x16x32_bf16 v[100:103], v[64:67], v[16:19], v[100:103]\n"
        "s_addk_i32 %[so], 15360\nds_read_b64_tr_b16 v[28:29], %[xa] offset:14784\nv_mfma_f32_16x16x32_bf16 v[116:119], v[68:71], v[16:19], v[116:119]\nds_read_b64_tr_b16 v[30:31], %[xa] offset:15296\n"
        "v_mfma_f32_16x16x32_bf16 v[136:139], v[72:75], v[16:19], v[136:139]\nds_read_b32 %[pv], %[sy] offset:4\nbuffer_load_dwordx4 v[48:51], %[wa], %[rs], %[so] offen offset:0 sc1\n"
        "v_mfma_f32_16x16x32_bf16 v[152:155], v[76:79], v[16:19], v[152:155]\nds_read_b64_tr_b16 v[16:17], %[xa] offset:16896\ns_waitcnt lgkmcnt(9)\n"
        "v_mfma_f32_16x16x32_bf16 v[104:107], v[64:67], v[20:23], v[104:107]\nds_read_b64_tr_b16 v[18:19], %[xa] offset:17408\nv_mfma_f32_16x16x32_bf16 v[120:123], v[68:71], v[20:23], v[120:123]\n"
        "buffer_load_dwordx4 v[52:55], %[wa], %[rs], %[so] offen offset:1024 sc1\nv_mfma_f32_16x16x32_bf16 v[140:143], v[72:75], v[20:23], v[140:143]\n"
        "v_mfma_f32_16x16x32_bf16 v[156:159], v[76:79], v[20:23], v[156:159]\nds_read_b64_tr_b16 v[20:21], %[xa] offset:17952\ns_waitcnt lgkmcnt(9)\n"
        "v_mfma_f32_16x16x32_bf16 v[96:99], v[64:67], v[12:15], v[96:99]\nds_read_b64_tr_b16 v[22:23], %[xa] offset:18464\nv_mfma_f32_16x16x32_bf16 v[132:135], v[72:75], v[12:15], v[132:135]\n"
        "ds_read_b64_tr_b16 v[12:13], %[xa] offset:15840\ns_waitcnt lgkmcnt(9)\nv_mfma_f32_16x16x32_bf16 v[108:111], v[64:67], v[24:27], v[108:111]\nds_read_b64_tr_b16 v[14:15], %[xa] offset:16352\n"
        "v_mfma_f32_16x16x32_bf16 v[124:127], v[68:71], v[24:27], v[124:127]\nbuffer_load_dwordx4 v[56:59], %[wa], %[rs], %[so] offen offset:2048 sc1\n"
        "v_mfma_f32_16x16x32_bf16 v[144:147], v[72:75], v[24:27], v[144:147]\nbuffer_load_dwordx4 v[60:63], %[wa], %[rs], %[so] offen offset:3072 sc1\n"
        "v_mfma_f32_16x16x32_bf16 v[160:163], v[76:79], v[24:27], v[160:163]\nds_read_b64_tr_b16 v[24:25], %[xa] offset:19008\ns_waitcnt lgkmcnt(8)\n"
        "v_mfma_f32_16x16x32_bf16 v[112:115], v[64:67], v[28:31], v[112:115]\nds_read_b64_tr_b16 v[26:27], %[xa] offset:19520\nv_mfma_f32_16x16x32_bf16 v[128:131], v[68:71], v[28:31], v[128:131]\n"
        "v_mfma_f32_16x16x32_bf16 v[148:151], v[72:75], v[28:31], v[148:151]\nv_mfma_f32_16x16x32_bf16 v[164:167], v[76:79], v[28:31], v[164:167]\ns_waitcnt vmcnt(8) lgkmcnt(6)\n"
        "v_mfma_f32_16x16x32_bf16 v[100:103], v[80:83], v[16:19], v[100:103]\ns_addk_i32 %[so], 15360\nv_readfirstlane_b32 %[s0], %[pv]\n"
        "v_mfma_f32_16x16x32_bf16 v[116:119], v[84:87], v[16:19], v[116:119]\ns_cmp_ge_i32 %[s0], 6\nds_read_b64_tr_b16 v[28:29], %[xa] offset:20064\n"
        "v_mfma_f32_16x16x32_bf16 v[136:139], v[88:91], v[16:19], v[136:139]\ns_cbranch_scc0 .LA1s1_%=\n.LA1r1_%=:\nds_read_b64_tr_b16 v[30:31], %[xa] offset:20576\n"
        "v_mfma_f32_16x16x32_bf16 v[152:155], v[92:95], v[16:19], v[152:155]\nds_read_b64_tr_b16 v[16:17], %[xa] offset:22176\ns_waitcnt lgkmcnt(7)\n"
        "v_mfma_f32_16x16x32_bf16 v[104:107], v[80:83], v[20:23], v[104:107]\nds_read_b64_tr_b16 v[18:19], %[xa] offset:22688\nbuffer_load_dwordx4 v[64:67], %[wa], %[rs], %[so] offen offset:0 sc1\n"
        "v_mfma_f32_16x16x32_bf16 v[120:123], v[84:87], v[20:23], v[120:123]\nbuffer_load_dwordx4 v[68:71], %[wa], %[rs], %[so] offen offset:1024 sc1\n"
        "v_mfma_f32_16x16x32_bf16 v[140:143], v[88:91], v[20:23], v[140:143]\nv_mfma_f32_16x16x32_bf16 v[156:159], v[92:95], v[20:23], v[156:159]\nds_read_b64_tr_b16 v[20:21], %[xa] offset:23232\n"
        "s_waitcnt lgkmcnt(7)\nv_mfma_f32_16x16x32_bf16 v[96:99], v[80:83], v[12:15], v[96:99]\nds_read_b64_tr_b16 v[22:23], %[xa] offset:23744\n"
        "v_mfma_f32_16x16x32_bf16 v[132:135], v[88:91], v[12:15], v[132:135]\nds_read_b64_tr_b16 v[12:13], %[xa] offset:21120\ns_waitcnt lgkmcnt(7)\n"
        "v_mfma_f32_16x16x32_bf16 v[108:111], v[80:83], v[24:27], v[108:111]\nds_read_b64_tr_b16 v[14:15], %[xa] offset:21632\nv_mfma_f32_16x16x32_bf16 v[124:127], v[84:87], v[24:27], v[124:127]\n"
        "buffer_load_dwordx4 v[72:75], %[wa], %[rs], %[so] offen offset:2048 sc1\nv_mfma_f32_16x16x32_bf16 v[144:147], v[88:91], v[24:27], v[144:147]\n"
        "buffer_load_dwordx4 v[76:79], %[wa], %[rs], %[so] offen offset:3072 sc1\nv_mfma_f32_16x16x32_bf16 v[160:163], v[92:95], v[24:27], v[160:163]\nds_read_b64_tr_b16 v[24:25], %[xa] offset:24288\n"
        "s_waitcnt lgkmcnt(7)\nv_mfma_f32_16x16x32_bf16 v[112:115], v[80:83], v[28:31], v[112:115]\nds_read_b64_tr_b16 v[26:27], %[xa] offset:24800\n"
        "v_mfma_f32_16x16x32_bf16 v[128:131], v[84:87], v[28:31], v[128:131]\nv_add_u32 %[sy], 4, %[sy]\nv_mfma_f32_16x16x32_bf16 v[148:151], v[88:91], v[28:31], v[148:151]\nds_write_b32 %[pg], %[sy]\n"
        "v_mfma_f32_16x16x32_bf16 v[164:167], v[92:95], v[28:31], v[164:167]\ns_waitcnt vmcnt(8) lgkmcnt(7)\nv_mfma_f32_16x16x32_bf16 v[100:103], v[32:35], v[16:19], v[100:103]\n"
        "s_addk_i32 %[so], 15360\nds_read_b64_tr_b16 v[28:29], %[xa] offset:25344\nv_mfma_f32_16x16x32_bf16 v[116:119], v[36:39], v[16:19], v[116:119]\nds_read_b64_tr_b16 v[30:31], %[xa] offset:25856\n"
        "v_mfma_f32_16x16x32_bf16 v[136:139], v[40:43], v[16:19], v[136:139]\nds_read_b32 %[pv], %[sy] offset:4\nbuffer_load_dwordx4 v[80:83], %[wa], %[rs], %[so] offen offset:0 sc1\n"
        "v_mfma_f32_16x16x32_bf16 v[152:155], v[44:47], v[16:19], v[152:155]\nds_read_b64_tr_b16 v[16:17], %[xa] offset:27456\ns_waitcnt lgkmcnt(9)\n"
        "v_mfma_f32_16x16x32_bf16 v[104:107], v[32:35], v[20:23], v[104:107]\nds_read_b64_tr_b16 v[18:19], %[xa] offset:27968\nv_mfma_f32_16x16x32_bf16 v[120:123], v[36:39], v[20:23], v[120:123]\n"
        "buffer_load_dwordx4 v[84:87], %[wa], %[rs], %[so] offen offset:1024 sc1\nv_mfma_f32_16x16x32_bf16 v[140:143], v[40:43], v[20:23], v[140:143]\n"
        "v_mfma_f32_16x16x32_bf16 v[156:159], v[44:47], v[20:23], v[156:159]\nds_read_b64_tr_b16 v[20:21], %[xa] offset:28512\ns_waitcnt lgkmcnt(9)\n"
        "v_mfma_f32_16x16x32_bf16 v[96:99], v[32:35], v[12:15], v[96:99]\nds_read_b64_tr_b16 v[22:23], %[xa] offset:29024\nv_mfma_f32_16x16x32_bf16 v[132:135], v[40:43], v[12:15], v[132:135]\n"
        "ds_read_b64_tr_b16 v[12:13], %[xa] offset:26400\ns_waitcnt lgkmcnt(9)\nv_mfma_f32_16x16x32_bf16 v[108:111], v[32:35], v[24:27], v[108:111]\nds_read_b64_tr_b16 v[14:15], %[xa] offset:26912\n"
        "v_mfma_f32_16x16x32_bf16 v[124:127], v[36:39], v[24:27], v[124:127]\nbuffer_load_dwordx4 v[88:91], %[wa], %[rs], %[so] offen offset:2048 sc1\n"
        "v_mfma_f32_16x16x32_bf16 v[144:147], v[40:43], v[24:27], v[144:147]\nbuffer_load_dwordx4 v[92:95], %[wa], %[rs], %[so] offen offset:3072 sc1\n"
        "v_mfma_f32_16x16x32_bf16 v[160:163], v[44:47], v[24:27], v[160:163]\nds_read_b64_tr_b16 v[24:25], %[xa] offset:29568\ns_waitcnt lgkmcnt(8)\n"
        "v_mfma_f32_16x16x32_bf16 v[112:115], v[32:35], v[28:31], v[112:115]\nds_read_b64_tr_b16 v[26:27], %[xa] offset:30080\nv_mfma_f32_16x16x32_bf16 v[128:131], v[36:39], v[28:31], v[128:131]\n"
        "v_mfma_f32_16x16x32_bf16 v[148:151], v[40:43], v[28:31], v[148:151]\ns_cmp_lg_u32 %[so], 291840\nv_mfma_f32_16x16x32_bf16 v[164:167], v[44:47], v[28:31], v[164:167]\n"
        "v_add_u32 %[xa], 21120, %[xa]\ns_cbranch_scc1 .LA1l%=\ns_waitcnt vmcnt(8) lgkmcnt(6)\nv_mfma_f32_16x16x32_bf16 v[100:103], v[48:51], v[16:19], v[100:103]\nv_readfirstlane_b32 %[s0], %[pv]\n"
        "ds_read_b64_tr_b16 v[28:29], %[xa] offset:9504\nv_mfma_f32_16x16x32_bf16 v[116:119], v[52:55], v[16:19], v[116:119]\ns_cmp_ge_i32 %[s0], 6\nds_read_b64_tr_b16 v[30:31], %[xa] offset:10016\n"
        "v_mfma_f32_16x16x32_bf16 v[136:139], v[56:59], v[16:19], v[136:139]\ns_cbranch_scc0 .LA1s2_%=\n.LA1r2_%=:\nv_mfma_f32_16x16x32_bf16 v[152:155], v[60:63], v[16:19], v[152:155]\n"
        "ds_read_b64_tr_b16 v[16:17], %[xa] offset:11616\ns_waitcnt lgkmcnt(7)\nv_mfma_f32_16x16x32_bf16 v[104:107], v[48:51], v[20:23], v[104:107]\nds_read_b64_tr_b16 v[18:19], %[xa] offset:12128\n"
        "v_mfma_f32_16x16x32_bf16 v[120:123], v[52:55], v[20:23], v[120:123]\nv_mfma_f32_16x16x32_bf16 v[140:143], v[56:59], v[20:23], v[140:143]\n"
        "v_mfma_f32_16x16x32_bf16 v[156:159], v[60:63], v[20:23], v[156:159]\nds_read_b64_tr_b16 v[20:21], %[xa] offset:12672\ns_waitcnt lgkmcnt(7)\n"
        "v_mfma_f32_16x16x32_bf16 v[96:99], v[48:51], v[12:15], v[96:99]\nds_read_b64_tr_b16 v[22:23], %[xa] offset:13184\nv_mfma_f32_16x16x32_bf16 v[132:135], v[56:59], v[12:15], v[132:135]\n"
        "ds_read_b64_tr_b16 v[12:13], %[xa] offset:10560\ns_waitcnt lgkmcnt(7)\nv_mfma_f32_16x16x32_bf16 v[108:111], v[48:51], v[24:27], v[108:111]\nds_read_b64_tr_b16 v[14:15], %[xa] offset:11072\n"
        "v_mfma_f32_16x16x32_bf16 v[124:127], v[52:55], v[24:27], v[124:127]\nv_mfma_f32_16x16x32_bf16 v[144:147], v[56:59], v[24:27], v[144:147]\n"
        "v_mfma_f32_16x16x32_bf16 v[160:163], v[60:63], v[24:27], v[160:163]\nds_read_b64_tr_b16 v[24:25], %[xa] offset:13728\ns_waitcnt lgkmcnt(7)\n"
        "v_mfma_f32_16x16x32_bf16 v[112:115], v[48:51], v[28:31], v[112:115]\nds_read_b64_tr_b16 v[26:27], %[xa] offset:14240\nv_mfma_f32_16x16x32_bf16 v[128:131], v[52:55], v[28:31], v[128:131]\n"
        "v_add_u32 %[sy], 4, %[sy]\nv_mfma_f32_16x16x32_bf16 v[148:151], v[56:59], v[28:31], v[148:151]\nv_mfma_f32_16x16x32_bf16 v[164:167], v[60:63], v[28:31], v[164:167]\n"
        "s_waitcnt vmcnt(4) lgkmcnt(6)\nv_mfma_f32_16x16x32_bf16 v[100:103], v[64:67], v[16:19], v[100:103]\nds_read_b64_tr_b16 v[28:29], %[xa] offset:14784\n"
        "v_mfma_f32_16x16x32_bf16 v[116:119], v[68:71], v[16:19], v[116:119]\nds_read_b64_tr_b16 v[30:31], %[xa] offset:15296\nv_mfma_f32_16x16x32_bf16 v[136:139], v[72:75], v[16:19], v[136:139]\n"
        "ds_read_b32 %[pv], %[sy] offset:4\nv_mfma_f32_16x16x32_bf16 v[152:155], v[76:79], v[16:19], v[152:155]\nds_read_b64_tr_b16 v[16:17], %[xa] offset:16896\ns_waitcnt lgkmcnt(8)\n"
        "v_mfma_f32_16x16x32_bf16 v[104:107], v[64:67], v[20:23], v[104:107]\nds_read_b64_tr_b16 v[18:19], %[xa] offset:17408\nv_mfma_f32_16x16x32_bf16 v[120:123], v[68:71], v[20:23], v[120:123]\n"
        "v_mfma_f32_16x16x32_bf16 v[140:143], v[72:75], v[20:23], v[140:143]\nv_mfma_f32_16x16x32_bf16 v[156:159], v[76:79], v[20:23], v[156:159]\nds_read_b64_tr_b16 v[20:21], %[xa] offset:17952\n"
        "s_waitcnt lgkmcnt(8)\nv_mfma_f32_16x16x32_bf16 v[96:99], v[64:67], v[12:15], v[96:99]\nds_read_b64_tr_b16 v[22:23], %[xa] offset:18464\n"
        "v_mfma_f32_16x16x32_bf16 v[132:135], v[72:75], v[12:15], v[132:135]\nds_read_b64_tr_b16 v[12:13], %[xa] offset:15840\ns_waitcnt lgkmcnt(8)\n"
        "v_mfma_f32_16x16x32_bf16 v[108:111], v[64:67], v[24:27], v[108:111]\nds_read_b64_tr_b16 v[14:15], %[xa] offset:16352\nv_mfma_f32_16x16x32_bf16 v[124:127], v[68:71], v[24:27], v[124:127]\n"
        "v_mfma_f32_16x16x32_bf16 v[144:147], v[72:75], v[24:27], v[144:147]\nv_mfma_f32_16x16x32_bf16 v[160:163], v[76:79], v[24:27], v[160:163]\nds_read_b64_tr_b16 v[24:25], %[xa] offset:19008\n"
        "s_waitcnt lgkmcnt(8)\nv_mfma_f32_16x16x32_bf16 v[112:115], v[64:67], v[28:31], v[112:115]\nds_read_b64_tr_b16 v[26:27], %[xa] offset:19520\n"
        "v_mfma_f32_16x16x32_bf16 v[128:131], v[68:71], v[28:31], v[128:131]\nv_mfma_f32_16x16x32_bf16 v[148:151], v[72:75], v[28:31], v[148:151]\n"
        "v_mfma_f32_16x16x32_bf16 v[164:167], v[76:79], v[28:31], v[164:167]\ns_waitcnt vmcnt(0) lgkmcnt(6)\nv_mfma_f32_16x16x32_bf16 v[100:103], v[80:83], v[16:19], v[100:103]\n"
        "v_readfirstlane_b32 %[s0], %[pv]\nds_read_b64_tr_b16 v[28:29], %[xa] offset:20064\nv_mfma_f32_16x16x32_bf16 v[116:119], v[84:87], v[16:19], v[116:119]\n"
        "ds_read_b64_tr_b16 v[30:31], %[xa] offset:20576\nv_mfma_f32_16x16x32_bf16 v[136:139], v[88:91], v[16:19], v[136:139]\ns_cmp_ge_i32 %[s0], %[nst]\n"
        "v_mfma_f32_16x16x32_bf16 v[152:155], v[92:95], v[16:19], v[152:155]\ns_cbranch_scc0 .LA1t%=\n.LA1u%=:\ns_waitcnt lgkmcnt(6)\n"
        "v_mfma_f32_16x16x32_bf16 v[104:107], v[80:83], v[20:23], v[104:107]\nv_add_u32 %[sy], 4, %[sy]\nds_read_b128 v[16:19], %[mra] offset:256\n"
        "v_mfma_f32_16x16x32_bf16 v[120:123], v[84:87], v[20:23], v[120:123]\nv_mfma_f32_16x16x32_bf16 v[140:143], v[88:91], v[20:23], v[140:143]\n"
        "v_mfma_f32_16x16x32_bf16 v[156:159], v[92:95], v[20:23], v[156:159]\nds_read_b128 v[32:35], %[cwa] offset:0\ns_waitcnt lgkmcnt(6)\n"
        "v_mfma_f32_16x16x32_bf16 v[96:99], v[80:83], v[12:15], v[96:99]\nds_read_b128 v[36:39], %[cwa] offset:320\nds_read_b128 v[20:23], %[mra] offset:512\n"
        "v_mfma_f32_16x16x32_bf16 v[132:135], v[88:91], v[12:15], v[132:135]\nds_read_b128 v[40:43], %[cwa] offset:640\ns_waitcnt lgkmcnt(7)\n"
        "v_mfma_f32_16x16x32_bf16 v[108:111], v[80:83], v[24:27], v[108:111]\nds_read_b128 v[44:47], %[cwa] offset:512\nds_read_b128 v[12:15], %[mra] offset:0\n"
        "v_mfma_f32_16x16x32_bf16 v[124:127], v[84:87], v[24:27], v[124:127]\nv_add_u32 %[sy], 4, %[sy]\nv_mfma_f32_16x16x32_bf16 v[144:147], v[88:91], v[24:27], v[144:147]\n"
        "v_mfma_f32_16x16x32_bf16 v[160:163], v[92:95], v[24:27], v[160:163]\nds_read_b128 v[24:27], %[mra] offset:768\ns_waitcnt lgkmcnt(8)\n"
        "v_mfma_f32_16x16x32_bf16 v[112:115], v[80:83], v[28:31], v[112:115]\nv_mfma_f32_16x16x32_bf16 v[128:131], v[84:87], v[28:31], v[128:131]\n"
        "v_mfma_f32_16x16x32_bf16 v[148:151], v[88:91], v[28:31], v[148:151]\nv_mfma_f32_16x16x32_bf16 v[164:167], v[92:95], v[28:31], v[164:167]\nds_read_b128 v[28:31], %[mra] offset:1024\n"
        "s_setprio %[pfs]\ns_waitcnt lgkmcnt(1)\n.long 0xd3b04064, 0x1d922120\n.long 0xd3b04066, 0x1d9a2122\n.long 0xd3b04074, 0x1dd22124\n.long 0xd3b04076, 0x1dda2126\n.long 0xd3b14064, 0x18022564\n"
        ".long 0xd3b14066, 0x18022566\n.long 0xd3b04068, 0x1da22920\n.long 0xd3b0406a, 0x1daa2922\n.long 0xd3b14074, 0x18022574\n.long 0xd3b14076, 0x18022576\nv_cvt_pk_bf16_f32 v100, v100, v101\n"
        "v_cvt_pk_bf16_f32 v101, v102, v103\n.long 0xd3b04078, 0x1de22924\n.long 0xd3b0407a, 0x1dea2926\n.long 0xd3b14068, 0x18022d68\n.long 0xd3b1406a, 0x18022d6a\nv_cvt_pk_bf16_f32 v116, v116, v117\n"
        "v_cvt_pk_bf16_f32 v117, v118, v119\nds_write_b64 %[sta], v[100:101] offset:7936\n.long 0xd3b04060, 0x1d821920\n.long 0xd3b04062, 0x1d8a1922\n.long 0xd3b14078, 0x18022d78\n"
        ".long 0xd3b1407a, 0x18022d7a\nv_cvt_pk_bf16_f32 v104, v104, v105\nv_cvt_pk_bf16_f32 v105, v106, v107\nds_write_b64 %[sta], v[116:117] offset:8096\n.long 0xd3b0406c, 0x1db23120\n"
        ".long 0xd3b0406e, 0x1dba3122\n.long 0xd3b14060, 0x18021d60\n.long 0xd3b14062, 0x18021d62\nv_cvt_pk_bf16_f32 v120, v120, v121\nv_cvt_pk_bf16_f32 v121, v122, v123\n"
        "ds_write_b64 %[sta], v[104:105] offset:15872\n.long 0xd3b0407c, 0x1df23124\n.long 0xd3b0407e, 0x1dfa3126\n.long 0xd3b1406c, 0x1802356c\n.long 0xd3b1406e, 0x1802356e\n"
        "v_cvt_pk_bf16_f32 v96, v96, v97\nv_cvt_pk_bf16_f32 v97, v98, v99\nds_write_b64 %[sta], v[120:121] offset:16032\ns_waitcnt lgkmcnt(4)\n.long 0xd3b04070, 0x1dc23920\n"
        ".long 0xd3b04072, 0x1dca3922\n.long 0xd3b1407c, 0x1802357c\n.long 0xd3b1407e, 0x1802357e\nv_cvt_pk_bf16_f32 v108, v108, v109\nv_cvt_pk_bf16_f32 v109, v110, v111\n"
        "ds_write_b64 %[sta], v[96:97] offset:0\n.long 0xd3b04080, 0x1e023924\n.long 0xd3b04082, 0x1e0a3926\n.long 0xd3b14070, 0x18023d70\n.long 0xd3b14072, 0x18023d72\n"
        "v_cvt_pk_bf16_f32 v124, v124, v125\nv_cvt_pk_bf16_f32 v125, v126, v127\nds_write_b64 %[sta], v[108:109] offset:23808\n.long 0xd3b14080, 0x18023d80\n.long 0xd3b14082, 0x18023d82\n"
        "v_cvt_pk_bf16_f32 v112, v112, v113\nv_cvt_pk_bf16_f32 v113, v114, v115\nds_write_b64 %[sta], v[124:125] offset:23968\nv_cvt_pk_bf16_f32 v128, v128, v129\nv_cvt_pk_bf16_f32 v129, v130, v131\n"
        "ds_write_b64 %[sta], v[112:113] offset:31744\nds_write_b64 %[sta], v[128:129] offset:31904\nds_write_b32 %[pg], %[sy]\nv_add_u32 %[sy], 4, %[sy]\n.long 0xd3b04088, 0x1e222128\n"
        ".long 0xd3b0408a, 0x1e2a212a\n.long 0xd3b04098, 0x1e62212c\n.long 0xd3b0409a, 0x1e6a212e\n.long 0xd3b14088, 0x18022588\n.long 0xd3b1408a, 0x1802258a\n.long 0xd3b0408c, 0x1e322928\n"
        ".long 0xd3b0408e, 0x1e3a292a\n.long 0xd3b14098, 0x18022598\n.long 0xd3b1409a, 0x1802259a\nv_cvt_pk_bf16_f32 v136, v136, v137\nv_cvt_pk_bf16_f32 v137, v138, v139\n.long 0xd3b0409c, 0x1e72292c\n"
        ".long 0xd3b0409e, 0x1e7a292e\n.long 0xd3b1408c, 0x18022d8c\n.long 0xd3b1408e, 0x18022d8e\nv_cvt_pk_bf16_f32 v152, v152, v153\nv_cvt_pk_bf16_f32 v153, v154, v155\n"
        "ds_write_b64 %[sta], v[136:137] offset:8256\n.long 0xd3b04084, 0x1e121928\n.long 0xd3b04086, 0x1e1a192a\n.long 0xd3b1409c, 0x18022d9c\n.long 0xd3b1409e, 0x18022d9e\n"
        "v_cvt_pk_bf16_f32 v140, v140, v141\nv_cvt_pk_bf16_f32 v141, v142, v143\nds_write_b64 %[sta], v[152:153] offset:8192\n.long 0xd3b04090, 0x1e423128\n.long 0xd3b04092, 0x1e4a312a\n"
        ".long 0xd3b14084, 0x18021d84\n.long 0xd3b14086, 0x18021d86\nv_cvt_pk_bf16_f32 v156, v156, v157\nv_cvt_pk_bf16_f32 v157, v158, v159\nds_write_b64 %[sta], v[140:141] offset:16192\n"
        ".long 0xd3b040a0, 0x1e82312c\n.long 0xd3b040a2, 0x1e8a312e\n.long 0xd3b14090, 0x18023590\n.long 0xd3b14092, 0x18023592\nv_cvt_pk_bf16_f32 v132, v132, v133\nv_cvt_pk_bf16_f32 v133, v134, v135\n"
        "ds_write_b64 %[sta], v[156:157] offset:16128\n.long 0xd3b04094, 0x1e523928\n.long 0xd3b04096, 0x1e5a392a\n.long 0xd3b140a0, 0x180235a0\n.long 0xd3b140a2, 0x180235a2\n"
        "v_cvt_pk_bf16_f32 v144, v144, v145\nv_cvt_pk_bf16_f32 v145, v146, v147\nds_write_b64 %[sta], v[132:133] offset:320\n.long 0xd3b040a4, 0x1e92392c\n.long 0xd3b040a6, 0x1e9a392e\n"
        ".long 0xd3b14094, 0x18023d94\n.long 0xd3b14096, 0x18023d96\nv_cvt_pk_bf16_f32 v160, v160, v161\nv_cvt_pk_bf16_f32 v161, v162, v163\nds_write_b64 %[sta], v[144:145] offset:24128\n"
        ".long 0xd3b140a4, 0x18023da4\n.long 0xd3b140a6, 0x18023da6\nv_cvt_pk_bf16_f32 v148, v148, v149\nv_cvt_pk_bf16_f32 v149, v150, v151\nds_write_b64 %[sta], v[160:161] offset:24064\n"
        "v_cvt_pk_bf16_f32 v164, v164, v165\nv_cvt_pk_bf16_f32 v165, v166, v167\nds_write_b64 %[sta], v[148:149] offset:32064\nds_write_b64 %[sta], v[164:165] offset:32000\nds_write_b32 %[pg], %[sy]\n"
        "s_branch .LE%=\n.LA1s0_%=:\ns_mov_b32 %[s1], 0x100000\n.LA1p0_%=:\nds_read_b32 %[pv], %[sy] offset:4\ns_waitcnt lgkmcnt(0)\nv_readfirstlane_b32 %[s0], %[pv]\ns_nop 0\ns_cmp_ge_i32 %[s0], 6\n"
        "s_cbranch_scc1 .LA1r0_%=\ns_sleep 1\ns_sub_u32 %[s1], %[s1], 1\ns_cbranch_scc0 .LA1p0_%=\ns_branch .LA1r0_%=\n.LA1s1_%=:\ns_mov_b32 %[s1], 0x100000\n.LA1p1_%=:\n"
        "ds_read_b32 %[pv], %[sy] offset:4\ns_waitcnt lgkmcnt(0)\nv_readfirstlane_b32 %[s0], %[pv]\ns_nop 0\ns_cmp_ge_i32 %[s0], 6\ns_cbranch_scc1 .LA1r1_%=\ns_sleep 1\ns_sub_u32 %[s1], %[s1], 1\n"
        "s_cbranch_scc0 .LA1p1_%=\ns_branch .LA1r1_%=\n.LA1s2_%=:\ns_mov_b32 %[s1], 0x100000\n.LA1p2_%=:\nds_read_b32 %[pv], %[sy] offset:4\ns_waitcnt lgkmcnt(0)\nv_readfirstlane_b32 %[s0], %[pv]\n"
        "s_nop 0\ns_cmp_ge_i32 %[s0], 6\ns_cbranch_scc1 .LA1r2_%=\ns_sleep 1\ns_sub_u32 %[s1], %[s1], 1\ns_cbranch_scc0 .LA1p2_%=\ns_branch .LA1r2_%=\n.LA1t%=:\ns_mov_b32 %[s1], 0x100000\n.LA1q%=:\n"
        "ds_read_b32 %[pv], %[sy] offset:4\ns_waitcnt lgkmcnt(0)\nv_readfirstlane_b32 %[s0], %[pv]\ns_nop 0\ns_cmp_ge_i32 %[s0], %[nst]\ns_cbranch_scc1 .LA1u%=\ns_sleep 1\ns_sub_u32 %[s1], %[s1], 1\n"
        "s_cbranch_scc0 .LA1q%=\ns_branch .LA1u%=\n.LB%=:\ns_cmp_eq_u32 %[w], 2\ns_cbranch_scc0 .LC%=\ns_mov_b32 %[so], 0\nbuffer_load_dwordx4 v[32:35], %[wa], %[rs], %[so] offen offset:3072 sc1\n"
        "buffer_load_dwordx4 v[36:39], %[wa], %[rs], %[so] offen offset:0 sc1\nbuffer_load_dwordx4 v[40:43], %[wa], %[rs], %[so] offen offset:1024 sc1\n"
        "buffer_load_dwordx4 v[44:47], %[wa], %[rs], %[so] offen offset:2048 sc1\ns_mov_b32 %[so], 15360\nbuffer_load_dwordx4 v[48:51], %[wa], %[rs], %[so] offen offset:3072 sc1\n"
        "buffer_load_dwordx4 v[52:55], %[wa], %[rs], %[so] offen offset:0 sc1\nbuffer_load_dwordx4 v[56:59], %[wa], %[rs], %[so] offen offset:1024 sc1\n"
        "buffer_load_dwordx4 v[60:63], %[wa], %[rs], %[so] offen offset:2048 sc1\ns_mov_b32 %[so], 30720\nbuffer_load_dwordx4 v[64:67], %[wa], %[rs], %[so] offen offset:3072 sc1\n"
        "buffer_load_dwordx4 v[68:71], %[wa], %[rs], %[so] offen offset:0 sc1\nbuffer_load_dwordx4 v[72:75], %[wa], %[rs], %[so] offen offset:1024 sc1\n"
        "buffer_load_dwordx4 v[76:79], %[wa], %[rs], %[so] offen offset:2048 sc1\ns_mov_b32 %[so], 46080\nbuffer_load_dwordx4 v[80:83], %[wa], %[rs], %[so] offen offset:3072 sc1\n"
        "buffer_load_dwordx4 v[84:87], %[wa], %[rs], %[so] offen offset:0 sc1\nbuffer_load_dwordx4 v[88:91], %[wa], %[rs], %[so] offen offset:1024 sc1\n"
        "buffer_load_dwordx4 v[92:95], %[wa], %[rs], %[so] offen offset:2048 sc1\ns_barrier\ns_mov_b32 %[s1], 0x100000\n.LB1w%=:\nds_read_b32 %[pv], %[sy] offset:0\ns_waitcnt lgkmcnt(0)\n"
        "v_readfirstlane_b32 %[s0], %[pv]\ns_nop 0\ns_cmp_ge_i32 %[s0], 6\ns_cbranch_scc1 .LB1g%=\ns_sleep 1\ns_sub_u32 %[s1], %[s1], 1\ns_cbranch_scc0 .LB1w%=\n.LB1g%=:\n"
        "ds_read_b64_tr_b16 v[16:17], %[xa] offset:1056\nds_read_b64_tr_b16 v[18:19], %[xa] offset:1568\nds_read_b64_tr_b16 v[20:21], %[xa] offset:2112\nds_read_b64_tr_b16 v[22:23], %[xa] offset:2624\n"
        "ds_read_b64_tr_b16 v[12:13], %[xa] offset:0\nds_read_b64_tr_b16 v[14:15], %[xa] offset:512\nds_read_b64_tr_b16 v[24:25], %[xa] offset:3168\nds_read_b64_tr_b16 v[26:27], %[xa] offset:3680\n"
        "s_waitcnt vmcnt(12) lgkmcnt(6)\nv_mfma_f32_16x16x32_bf16 v[96:99], v[32:35], v[16:19], 0\nds_read_b64_tr_b16 v[28:29], %[xa] offset:4224\n"
        "v_mfma_f32_16x16x32_bf16 v[108:111], v[36:39], v[16:19], 0\nds_read_b64_tr_b16 v[30:31], %[xa] offset:4736\nv_mfma_f32_16x16x32_bf16 v[128:131], v[40:43], v[16:19], 0\n"
        "ds_read_b32 %[pv], %[sy] offset:4\nv_mfma_f32_16x16x32_bf16 v[148:151], v[44:47], v[16:19], 0\nds_read_b64_tr_b16 v[16:17], %[xa] offset:6336\ns_waitcnt lgkmcnt(8)\n"
        "v_mfma_f32_16x16x32_bf16 v[100:103], v[32:35], v[20:23], 0\nds_read_b64_tr_b16 v[18:19], %[xa] offset:6848\nv_mfma_f32_16x16x32_bf16 v[112:115], v[36:39], v[20:23], 0\n"
        "v_mfma_f32_16x16x32_bf16 v[132:135], v[40:43], v[20:23], 0\nv_mfma_f32_16x16x32_bf16 v[152:155], v[44:47], v[20:23], 0\nds_read_b64_tr_b16 v[20:21], %[xa] offset:7392\ns_waitcnt lgkmcnt(8)\n"
        "v_mfma_f32_16x16x32_bf16 v[104:107], v[36:39], v[12:15], 0\nds_read_b64_tr_b16 v[22:23], %[xa] offset:7904\nv_mfma_f32_16x16x32_bf16 v[124:127], v[40:43], v[12:15], 0\n"
        "v_mfma_f32_16x16x32_bf16 v[144:147], v[44:47], v[12:15], 0\nds_read_b64_tr_b16 v[12:13], %[xa] offset:5280\ns_waitcnt lgkmcnt(8)\nv_mfma_f32_16x16x32_bf16 v[116:119], v[36:39], v[24:27], 0\n"
        "ds_read_b64_tr_b16 v[14:15], %[xa] offset:5792\nv_mfma_f32_16x16x32_bf16 v[136:139], v[40:43], v[24:27], 0\nv_mfma_f32_16x16x32_bf16 v[156:159], v[44:47], v[24:27], 0\n"
        "ds_read_b64_tr_b16 v[24:25], %[xa] offset:8448\ns_waitcnt lgkmcnt(8)\nv_mfma_f32_16x16x32_bf16 v[120:123], v[36:39], v[28:31], 0\nds_read_b64_tr_b16 v[26:27], %[xa] offset:8960\n"
        "v_mfma_f32_16x16x32_bf16 v[140:143], v[40:43], v[28:31], 0\nv_mfma_f32_16x16x32_bf16 v[160:163], v[44:47], v[28:31], 0\n.LB1l%=:\ns_waitcnt vmcnt(8) lgkmcnt(6)\n"
        "v_mfma_f32_16x16x32_bf16 v[96:99], v[48:51], v[16:19], v[96:99]\ns_addk_i32 %[so], 15360\nv_readfirstlane_b32 %[s0], %[pv]\n"
        "v_mfma_f32_16x16x32_bf16 v[108:111], v[52:55], v[16:19], v[108:111]\ns_cmp_ge_i32 %[s0], 6\nds_read_b64_tr_b16 v[28:29], %[xa] offset:9504\n"
        "v_mfma_f32_16x16x32_bf16 v[128:131], v[56:59], v[16:19], v[128:131]\ns_cbranch_scc0 .LB1s0_%=\n.LB1r0_%=:\nds_read_b64_tr_b16 v[30:31], %[xa] offset:10016\n"
        "v_mfma_f32_16x16x32_bf16 v[148:151], v[60:63], v[16:19], v[148:151]\nds_read_b64_tr_b16 v[16:17], %[xa] offset:11616\ns_waitcnt lgkmcnt(7)\n"
        "v_mfma_f32_16x16x32_bf16 v[100:103], v[48:51], v[20:23], v[100:103]\nds_read_b64_tr_b16 v[18:19], %[xa] offset:12128\nbuffer_load_dwordx4 v[32:35], %[wa], %[rs], %[so] offen offset:3072 sc1\n"
        "v_mfma_f32_16x16x32_bf16 v[112:115], v[52:55], v[20:23], v[112:115]\nbuffer_load_dwordx4 v[36:39], %[wa], %[rs], %[so] offen offset:0 sc1\n"
        "v_mfma_f32_16x16x32_bf16 v[132:135], v[56:59], v[20:23], v[132:135]\nv_mfma_f32_16x16x32_bf16 v[152:155], v[60:63], v[20:23], v[152:155]\nds_read_b64_tr_b16 v[20:21], %[xa] offset:12672\n"
        "s_waitcnt lgkmcnt(7)\nv_mfma_f32_16x16x32_bf16 v[104:107], v[52:55], v[12:15], v[104:107]\nds_read_b64_tr_b16 v[22:23], %[xa] offset:13184\n"
        "v_mfma_f32_16x16x32_bf16 v[124:127], v[56:59], v[12:15], v[124:127]\nbuffer_load_dwordx4 v[40:43], %[wa], %[rs], %[so] offen offset:1024 sc1\n"
        "v_mfma_f32_16x16x32_bf16 v[144:147], v[60:63], v[12:15], v[144:147]\nds_read_b64_tr_b16 v[12:13], %[xa] offset:10560\ns_waitcnt lgkmcnt(7)\n"
        "v_mfma_f32_16x16x32_bf16 v[116:119], v[52:55], v[24:27], v[116:119]\nds_read_b64_tr_b16 v[14:15], %[xa] offset:11072\nv_mfma_f32_16x16x32_bf16 v[136:139], v[56:59], v[24:27], v[136:139]\n"
        "buffer_load_dwordx4 v[44:47], %[wa], %[rs], %[so] offen offset:2048 sc1\nv_mfma_f32_16x16x32_bf16 v[156:159], v[60:63], v[24:27], v[156:159]\nds_read_b64_tr_b16 v[24:25], %[xa] offset:13728\n"
        "s_waitcnt lgkmcnt(7)\nv_mfma_f32_16x16x32_bf16 v[120:123], v[52:55], v[28:31], v[120:123]\nv_add_u32 %[sy], 4, %[sy]\nds_read_b64_tr_b16 v[26:27], %[xa] offset:14240\n"
        "v_mfma_f32_16x16x32_bf16 v[140:143], v[56:59], v[28:31], v[140:143]\nds_write_b32 %[pg], %[sy]\nv_mfma_f32_16x16x32_bf16 v[160:163], v[60:63], v[28:31], v[160:163]\n"
        "s_waitcnt vmcnt(8) lgkmcnt(7)\nv_mfma_f32_16x16x32_bf16 v[96:99], v[64:67], v[16:19], v[96:99]\ns_addk_i32 %[so], 15360\nds_read_b64_tr_b16 v[28:29], %[xa] offset:14784\n"
        "v_mfma_f32_16x16x32_bf16 v[108:111], v[68:71], v[16:19], v[108:111]\nds_read_b64_tr_b16 v[30:31], %[xa] offset:15296\nv_mfma_f32_16x16x32_bf16 v[128:131], v[72:75], v[16:19], v[128:131]\n"
        "ds_read_b32 %[pv], %[sy] offset:4\nbuffer_load_dwordx4 v[48:51], %[wa], %[rs], %[so] offen offset:3072 sc1\nv_mfma_f32_16x16x32_bf16 v[148:151], v[76:79], v[16:19], v[148:151]\n"
        "ds_read_b64_tr_b16 v[16:17], %[xa] offset:16896\ns_waitcnt lgkmcnt(9)\nv_mfma_f32_16x16x32_bf16 v[100:103], v[64:67], v[20:23], v[100:103]\nds_read_b64_tr_b16 v[18:19], %[xa] offset:17408\n"
        "v_mfma_f32_16x16x32_bf16 v[112:115], v[68:71], v[20:23], v[112:115]\nbuffer_load_dwordx4 v[52:55], %[wa], %[rs], %[so] offen offset:0 sc1\n"
        "v_mfma_f32_16x16x32_bf16 v[132:135], v[72:75], v[20:23], v[132:135]\nv_mfma_f32_16x16x32_bf16 v[152:155], v[76:79], v[20:23], v[152:155]\nds_read_b64_tr_b16 v[20:21], %[xa] offset:17952\n"
        "s_waitcnt lgkmcnt(9)\nv_mfma_f32_16x16x32_bf16 v[104:107], v[68:71], v[12:15], v[104:107]\nds_read_b64_tr_b16 v[22:23], %[xa] offset:18464\n"
        "v_mfma_f32_16x16x32_bf16 v[124:127], v[72:75], v[12:15], v[124:127]\nbuffer_load_dwordx4 v[56:59], %[wa], %[rs], %[so] offen offset:1024 sc1\n"
        "v_mfma_f32_16x16x32_bf16 v[144:147], v[76:79], v[12:15], v[144:147]\nds_read_b64_tr_b16 v[12:13], %[xa] offset:15840\ns_waitcnt lgkmcnt(9)\n"
        "v_mfma_f32_16x16x32_bf16 v[116:119], v[68:71], v[24:27], v[116:119]\nds_read_b64_tr_b16 v[14:15], %[xa] offset:16352\nv_mfma_f32_16x16x32_bf16 v[136:139], v[72:75], v[24:27], v[136:139]\n"
        "buffer_load_dwordx4 v[60:63], %[wa], %[rs], %[so] offen offset:2048 sc1\nv_mfma_f32_16x16x32_bf16 v[156:159], v[76:79], v[24:27], v[156:159]\nds_read_b64_tr_b16 v[24:25], %[xa] offset:19008\n"
        "s_waitcnt lgkmcnt(8)\nv_mfma_f32_16x16x32_bf16 v[120:123], v[68:71], v[28:31], v[120:123]\nds_read_b64_tr_b16 v[26:27], %[xa] offset:19520\n"
        "v_mfma_f32_16x16x32_bf16 v[140:143], v[72:75], v[28:31], v[140:143]\nv_mfma_f32_16x16x32_bf16 v[160:163], v[76:79], v[28:31], v[160:163]\ns_waitcnt vmcnt(8) lgkmcnt(6)\n"
        "v_mfma_f32_16x16x32_bf16 v[96:99], v[80:83], v[16:19], v[96:99]\ns_addk_i32 %[so], 15360\nv_readfirstlane_b32 %[s0], %[pv]\n"
        "v_mfma_f32_16x16x32_bf16 v[108:111], v[84:87], v[16:19], v[108:111]\ns_cmp_ge_i32 %[s0], 6\nds_read_b64_tr_b16 v[28:29], %[xa] offset:20064\n"
        "v_mfma_f32_16x16x32_bf16 v[128:131], v[88:91], v[16:19], v[128:131]\ns_cbranch_scc0 .LB1s1_%=\n.LB1r1_%=:\nds_read_b64_tr_b16 v[30:31], %[xa] offset:20576\n"
        "v_mfma_f32_16x16x32_bf16 v[148:151], v[92:95], v[16:19], v[148:151]\nds_read_b64_tr_b16 v[16:17], %[xa] offset:22176\ns_waitcnt lgkmcnt(7)\n"
        "v_mfma_f32_16x16x32_bf16 v[100:103], v[80:83], v[20:23], v[100:103]\nds_read_b64_tr_b16 v[18:19], %[xa] offset:22688\nbuffer_load_dwordx4 v[64:67], %[wa], %[rs], %[so] offen offset:3072 sc1\n"
        "v_mfma_f32_16x16x32_bf16 v[112:115], v[84:87], v[20:23], v[112:115]\nbuffer_load_dwordx4 v[68:71], %[wa], %[rs], %[so] offen offset:0 sc1\n"
        "v_mfma_f32_16x16x32_bf16 v[132:135], v[88:91], v[20:23], v[132:135]\nv_mfma_f32_16x16x32_bf16 v[152:155], v[92:95], v[20:23], v[152:155]\nds_read_b64_tr_b16 v[20:21], %[xa] offset:23232\n"
        "s_waitcnt lgkmcnt(7)\nv_mfma_f32_16x16x32_bf16 v[104:107], v[84:87], v[12:15], v[104:107]\nds_read_b64_tr_b16 v[22:23], %[xa] offset:23744\n"
        "v_mfma_f32_16x16x32_bf16 v[124:127], v[88:91], v[12:15], v[124:127]\nbuffer_load_dwordx4 v[72:75], %[wa], %[rs], %[so] offen offset:1024 sc1\n"
        "v_mfma_f32_16x16x32_bf16 v[144:147], v[92:95], v[12:15], v[144:147]\nds_read_b64_tr_b16 v[12:13], %[xa] offset:21120\ns_waitcnt lgkmcnt(7)\n"
        "v_mfma_f32_16x16x32_bf16 v[116:119], v[84:87], v[24:27], v[116:119]\nds_read_b64_tr_b16 v[14:15], %[xa] offset:21632\nv_mfma_f32_16x16x32_bf16 v[136:139], v[88:91], v[24:27], v[136:139]\n"
        "buffer_load_dwordx4 v[76:79], %[wa], %[rs], %[so] offen offset:2048 sc1\nv_mfma_f32_16x16x32_bf16 v[156:159], v[92:95], v[24:27], v[156:159]\nds_read_b64_tr_b16 v[24:25], %[xa] offset:24288\n"
        "s_waitcnt lgkmcnt(7)\nv_mfma_f32_16x16x32_bf16 v[120:123], v[84:87], v[28:31], v[120:123]\nv_add_u32 %[sy], 4, %[sy]\nds_read_b64_tr_b16 v[26:27], %[xa] offset:24800\n"
        "v_mfma_f32_16x16x32_bf16 v[140:143], v[88:91], v[28:31], v[140:143]\nds_write_b32 %[pg], %[sy]\nv_mfma_f32_16x16x32_bf16 v[160:163], v[92:95], v[28:31], v[160:163]\n"
        "s_waitcnt vmcnt(8) lgkmcnt(7)\nv_mfma_f32_16x16x32_bf16 v[96:99], v[32:35], v[16:19], v[96:99]\ns_addk_i32 %[so], 15360\nds_read_b64_tr_b16 v[28:29], %[xa] offset:25344\n"
        "v_mfma_f32_16x16x32_bf16 v[108:111], v[36:39], v[16:19], v[108:111]\nds_read_b64_tr_b16 v[30:31], %[xa] offset:25856\nv_mfma_f32_16x16x32_bf16 v[128:131], v[40:43], v[16:19], v[128:131]\n"
        "ds_read_b32 %[pv], %[sy] offset:4\nbuffer_load_dwordx4 v[80:83], %[wa], %[rs], %[so] offen offset:3072 sc1\nv_mfma_f32_16x16x32_bf16 v[148:151], v[44:47], v[16:19], v[148:151]\n"
        "ds_read_b64_tr_b16 v[16:17], %[xa] offset:27456\ns_waitcnt lgkmcnt(9)\nv_mfma_f32_16x16x32_bf16 v[100:103], v[32:35], v[20:23], v[100:103]\nds_read_b64_tr_b16 v[18:19], %[xa] offset:27968\n"
        "v_mfma_f32_16x16x32_bf16 v[112:115], v[36:39], v[20:23], v[112:115]\nbuffer_load_dwordx4 v[84:87], %[wa], %[rs], %[so] offen offset:0 sc1\n"
        "v_mfma_f32_16x16x32_bf16 v[132:135], v[40:43], v[20:23], v[132:135]\nv_mfma_f32_16x16x32_bf16 v[152:155], v[44:47], v[20:23], v[152:155]\nds_read_b64_tr_b16 v[20:21], %[xa] offset:28512\n"
        "s_waitcnt lgkmcnt(9)\nv_mfma_f32_16x16x32_bf16 v[104:107], v[36:39], v[12:15], v[104:107]\nds_read_b64_tr_b16 v[22:23], %[xa] offset:29024\n"
        "v_mfma_f32_16x16x32_bf16 v[124:127], v[40:43], v[12:15], v[124:127]\nbuffer_load_dwordx4 v[88:91], %[wa], %[rs], %[so] offen offset:1024 sc1\n"
        "v_mfma_f32_16x16x32_bf16 v[144:147], v[44:47], v[12:15], v[144:147]\nds_read_b64_tr_b16 v[12:13], %[xa] offset:26400\ns_waitcnt lgkmcnt(9)\n"
        "v_mfma_f32_16x16x32_bf16 v[116:119], v[36:39], v[24:27], v[116:119]\nds_read_b64_tr_b16 v[14:15], %[xa] offset:26912\nv_mfma_f32_16x16x32_bf16 v[136:139], v[40:43], v[24:27], v[136:139]\n"
        "buffer_load_dwordx4 v[92:95], %[wa], %[rs], %[so] offen offset:2048 sc1\nv_mfma_f32_16x16x32_bf16 v[156:159], v[44:47], v[24:27], v[156:159]\nds_read_b64_tr_b16 v[24:25], %[xa] offset:29568\n"
        "s_waitcnt lgkmcnt(8)\nv_mfma_f32_16x16x32_bf16 v[120:123], v[36:39], v[28:31], v[120:123]\nds_read_b64_tr_b16 v[26:27], %[xa] offset:30080\n"
        "v_mfma_f32_16x16x32_bf16 v[140:143], v[40:43], v[28:31], v[140:143]\ns_cmp_lg_u32 %[so], 291840\nv_mfma_f32_16x16x32_bf16 v[160:163], v[44:47], v[28:31], v[160:163]\n"
        "v_add_u32 %[xa], 21120, %[xa]\ns_cbranch_scc1 .LB1l%=\ns_waitcnt vmcnt(8) lgkmcnt(6)\nv_mfma_f32_16x16x32_bf16 v[96:99], v[48:51], v[16:19], v[96:99]\nv_readfirstlane_b32 %[s0], %[pv]\n"
        "ds_read_b64_tr_b16 v[28:29], %[xa] offset:9504\nv_mfma_f32_16x16x32_bf16 v[108:111], v[52:55], v[16:19], v[108:111]\ns_cmp_ge_i32 %[s0], 6\nds_read_b64_tr_b16 v[30:31], %[xa] offset:10016\n"
        "v_mfma_f32_16x16x32_bf16 v[128:131], v[56:59], v[16:19], v[128:131]\ns_cbranch_scc0 .LB1s2_%=\n.LB1r2_%=:\nv_mfma_f32_16x16x32_bf16 v[148:151], v[60:63], v[16:19], v[148:151]\n"
        "ds_read_b64_tr_b16 v[16:17], %[xa] offset:11616\ns_waitcnt lgkmcnt(7)\nv_mfma_f32_16x16x32_bf16 v[100:103], v[48:51], v[20:23], v[100:103]\nds_read_b64_tr_b16 v[18:19], %[xa] offset:12128\n"
        "v_mfma_f32_16x16x32_bf16 v[112:115], v[52:55], v[20:23], v[112:115]\nv_mfma_f32_16x16x32_bf16 v[132:135], v[56:59], v[20:23], v[132:135]\n"
        "v_mfma_f32_16x16x32_bf16 v[152:155], v[60:63], v[20:23], v[152:155]\nds_read_b64_tr_b16 v[20:21], %[xa] offset:12672\ns_waitcnt lgkmcnt(7)\n"
        "v_mfma_f32_16x16x32_bf16 v[104:107], v[52:55], v[12:15], v[104:107]\nds_read_b64_tr_b16 v[22:23], %[xa] offset:13184\nv_mfma_f32_16x16x32_bf16 v[124:127], v[56:59], v[12:15], v[124:127]\n"
        "v_mfma_f32_16x16x32_bf16 v[144:147], v[60:63], v[12:15], v[144:147]\nds_read_b64_tr_b16 v[12:13], %[xa] offset:10560\ns_waitcnt lgkmcnt(7)\n"
        "v_mfma_f32_16x16x32_bf16 v[116:119], v[52:55], v[24:27], v[116:119]\nds_read_b64_tr_b16 v[14:15], %[xa] offset:11072\nv_mfma_f32_16x16x32_bf16 v[136:139], v[56:59], v[24:27], v[136:139]\n"
        "v_mfma_f32_16x16x32_bf16 v[156:159], v[60:63], v[24:27], v[156:159]\nds_read_b64_tr_b16 v[24:25], %[xa] offset:13728\ns_waitcnt lgkmcnt(7)\n"
        "v_mfma_f32_16x16x32_bf16 v[120:123], v[52:55], v[28:31], v[120:123]\nv_add_u32 %[sy], 4, %[sy]\nds_read_b64_tr_b16 v[26:27], %[xa] offset:14240\n"
        "v_mfma_f32_16x16x32_bf16 v[140:143], v[56:59], v[28:31], v[140:143]\nv_mfma_f32_16x16x32_bf16 v[160:163], v[60:63], v[28:31], v[160:163]\ns_waitcnt vmcnt(4) lgkmcnt(6)\n"
        "v_mfma_f32_16x16x32_bf16 v[96:99], v[64:67], v[16:19], v[96:99]\nds_read_b64_tr_b16 v[28:29], %[xa] offset:14784\nv_mfma_f32_16x16x32_bf16 v[108:111], v[68:71], v[16:19], v[108:111]\n"
        "ds_read_b64_tr_b16 v[30:31], %[xa] offset:15296\nv_mfma_f32_16x16x32_bf16 v[128:131], v[72:75], v[16:19], v[128:131]\nds_read_b32 %[pv], %[sy] offset:4\n"
        "v_mfma_f32_16x16x32_bf16 v[148:151], v[76:79], v[16:19], v[148:151]\nds_read_b64_tr_b16 v[16:17], %[xa] offset:16896\ns_waitcnt lgkmcnt(8)\n"
        "v_mfma_f32_16x16x32_bf16 v[100:103], v[64:67], v[20:23], v[100:103]\nds_read_b64_tr_b16 v[18:19], %[xa] offset:17408\nv_mfma_f32_16x16x32_bf16 v[112:115], v[68:71], v[20:23], v[112:115]\n"
        "v_mfma_f32_16x16x32_bf16 v[132:135], v[72:75], v[20:23], v[132:135]\nv_mfma_f32_16x16x32_bf16 v[152:155], v[76:79], v[20:23], v[152:155]\nds_read_b64_tr_b16 v[20:21], %[xa] offset:17952\n"
        "s_waitcnt lgkmcnt(8)\nv_mfma_f32_16x16x32_bf16 v[104:107], v[68:71], v[12:15], v[104:107]\nds_read_b64_tr_b16 v[22:23], %[xa] offset:18464\n"
        "v_mfma_f32_16x16x32_bf16 v[124:127], v[72:75], v[12:15], v[124:127]\nv_mfma_f32_16x16x32_bf16 v[144:147], v[76:79], v[12:15], v[144:147]\nds_read_b64_tr_b16 v[12:13], %[xa] offset:15840\n"
        "s_waitcnt lgkmcnt(8)\nv_mfma_f32_16x16x32_bf16 v[116:119], v[68:71], v[24:27], v[116:119]\nds_read_b64_tr_b16 v[14:15], %[xa] offset:16352\n"
        "v_mfma_f32_16x16x32_bf16 v[136:139], v[72:75], v[24:27], v[136:139]\nv_mfma_f32_16x16x32_bf16 v[156:159], v[76:79], v[24:27], v[156:159]\nds_read_b64_tr_b16 v[24:25], %[xa] offset:19008\n"
        "s_waitcnt lgkmcnt(8)\nv_mfma_f32_16x16x32_bf16 v[120:123], v[68:71], v[28:31], v[120:123]\nds_read_b64_tr_b16 v[26:27], %[xa] offset:19520\n"
        "v_mfma_f32_16x16x32_bf16 v[140:143], v[72:75], v[28:31], v[140:143]\nv_mfma_f32_16x16x32_bf16 v[160:163], v[76:79], v[28:31], v[160:163]\ns_waitcnt vmcnt(0) lgkmcnt(6)\n"
        "v_mfma_f32_16x16x32_bf16 v[96:99], v[80:83], v[16:19], v[96:99]\nv_readfirstlane_b32 %[s0], %[pv]\nds_read_b64_tr_b16 v[28:29], %[xa] offset:20064\n"
        "v_mfma_f32_16x16x32_bf16 v[108:111], v[84:87], v[16:19], v[108:111]\nds_read_b64_tr_b16 v[30:31], %[xa] offset:20576\nv_mfma_f32_16x16x32_bf16 v[128:131], v[88:91], v[16:19], v[128:131]\n"
        "s_cmp_ge_i32 %[s0], %[nst]\nv_mfma_f32_16x16x32_bf16 v[148:151], v[92:95], v[16:19], v[148:151]\ns_cbranch_scc0 .LB1t%=\n.LB1u%=:\ns_waitcnt lgkmcnt(6)\n"
        "v_mfma_f32_16x16x32_bf16 v[100:103], v[80:83], v[20:23], v[100:103]\nv_add_u32 %[sy], 4, %[sy]\nds_read_b128 v[16:19], %[mra] offset:256\n"
        "v_mfma_f32_16x16x32_bf16 v[112:115], v[84:87], v[20:23], v[112:115]\nv_mfma_f32_16x16x32_bf16 v[132:135], v[88:91], v[20:23], v[132:135]\n"
        "v_mfma_f32_16x16x32_bf16 v[152:155], v[92:95], v[20:23], v[152:155]\nds_read_b128 v[32:35], %[cwa] offset:448\ns_waitcnt lgkmcnt(6)\n"
        "v_mfma_f32_16x16x32_bf16 v[104:107], v[84:87], v[12:15], v[104:107]\nds_read_b128 v[36:39], %[cwa] offset:128\nds_read_b128 v[20:23], %[mra] offset:512\n"
        "v_mfma_f32_16x16x32_bf16 v[124:127], v[88:91], v[12:15], v[124:127]\nds_read_b128 v[40:43], %[cwa] offset:192\nv_mfma_f32_16x16x32_bf16 v[144:147], v[92:95], v[12:15], v[144:147]\n"
        "ds_read_b128 v[44:47], %[cwa] offset:768\ns_waitcnt lgkmcnt(8)\nv_mfma_f32_16x16x32_bf16 v[116:119], v[84:87], v[24:27], v[116:119]\nv_add_u32 %[sy], 4, %[sy]\n"
        "ds_read_b128 v[12:15], %[mra] offset:0\nv_mfma_f32_16x16x32_bf16 v[136:139], v[88:91], v[24:27], v[136:139]\nv_mfma_f32_16x16x32_bf16 v[156:159], v[92:95], v[24:27], v[156:159]\n"
        "ds_read_b128 v[24:27], %[mra] offset:768\ns_waitcnt lgkmcnt(8)\nv_mfma_f32_16x16x32_bf16 v[120:123], v[84:87], v[28:31], v[120:123]\n"
        "v_mfma_f32_16x16x32_bf16 v[140:143], v[88:91], v[28:31], v[140:143]\nv_mfma_f32_16x16x32_bf16 v[160:163], v[92:95], v[28:31], v[160:163]\nds_read_b128 v[28:31], %[mra] offset:1024\n"
        "s_setprio %[pfs]\ns_waitcnt lgkmcnt(1)\n.long 0xd3b04060, 0x1d822120\n.long 0xd3b04062, 0x1d8a2122\n.long 0xd3b0406c, 0x1db22124\n.long 0xd3b0406e, 0x1dba2126\n.long 0xd3b14060, 0x18022560\n"
        ".long 0xd3b14062, 0x18022562\n.long 0xd3b04080, 0x1e022128\n.long 0xd3b04082, 0x1e0a212a\n.long 0xd3b1406c, 0x1802256c\n.long 0xd3b1406e, 0x1802256e\nv_cvt_pk_bf16_f32 v96, v96, v97\n"
        "v_cvt_pk_bf16_f32 v97, v98, v99\n.long 0xd3b04064, 0x1d922920\n.long 0xd3b04066, 0x1d9a2922\n.long 0xd3b14080, 0x18022580\n.long 0xd3b14082, 0x18022582\nv_cvt_pk_bf16_f32 v108, v108, v109\n"
        "v_cvt_pk_bf16_f32 v109, v110, v111\nds_write_b64 %[sta], v[96:97] offset:8160\n.long 0xd3b04070, 0x1dc22924\n.long 0xd3b04072, 0x1dca2926\n.long 0xd3b14064, 0x18022d64\n"
        ".long 0xd3b14066, 0x18022d66\nv_cvt_pk_bf16_f32 v128, v128, v129\nv_cvt_pk_bf16_f32 v129, v130, v131\nds_write_b64 %[sta], v[108:109] offset:8000\n.long 0xd3b04084, 0x1e122928\n"
        ".long 0xd3b04086, 0x1e1a292a\n.long 0xd3b14070, 0x18022d70\n.long 0xd3b14072, 0x18022d72\nv_cvt_pk_bf16_f32 v100, v100, v101\nv_cvt_pk_bf16_f32 v101, v102, v103\n"
        "ds_write_b64 %[sta], v[128:129] offset:8032\n.long 0xd3b04068, 0x1da21924\n.long 0xd3b0406a, 0x1daa1926\n.long 0xd3b14084, 0x18022d84\n.long 0xd3b14086, 0x18022d86\n"
        "v_cvt_pk_bf16_f32 v112, v112, v113\nv_cvt_pk_bf16_f32 v113, v114, v115\nds_write_b64 %[sta], v[100:101] offset:16096\n.long 0xd3b0407c, 0x1df21928\n.long 0xd3b0407e, 0x1dfa192a\n"
        ".long 0xd3b14068, 0x18021d68\n.long 0xd3b1406a, 0x18021d6a\nv_cvt_pk_bf16_f32 v132, v132, v133\nv_cvt_pk_bf16_f32 v133, v134, v135\nds_write_b64 %[sta], v[112:113] offset:15936\n"
        ".long 0xd3b04074, 0x1dd23124\n.long 0xd3b04076, 0x1dda3126\n.long 0xd3b1407c, 0x18021d7c\n.long 0xd3b1407e, 0x18021d7e\nv_cvt_pk_bf16_f32 v104, v104, v105\nv_cvt_pk_bf16_f32 v105, v106, v107\n"
        "ds_write_b64 %[sta], v[132:133] offset:15968\n.long 0xd3b04088, 0x1e223128\n.long 0xd3b0408a, 0x1e2a312a\n.long 0xd3b14074, 0x18023574\n.long 0xd3b14076, 0x18023576\n"
        "v_cvt_pk_bf16_f32 v124, v124, v125\nv_cvt_pk_bf16_f32 v125, v126, v127\nds_write_b64 %[sta], v[104:105] offset:64\ns_waitcnt lgkmcnt(7)\n.long 0xd3b04078, 0x1de23924\n"
        ".long 0xd3b0407a, 0x1dea3926\n.long 0xd3b14088, 0x18023588\n.long 0xd3b1408a, 0x1802358a\nv_cvt_pk_bf16_f32 v116, v116, v117\nv_cvt_pk_bf16_f32 v117, v118, v119\n"
        "ds_write_b64 %[sta], v[124:125] offset:96\n.long 0xd3b0408c, 0x1e323928\n.long 0xd3b0408e, 0x1e3a392a\n.long 0xd3b14078, 0x18023d78\n.long 0xd3b1407a, 0x18023d7a\n"
        "v_cvt_pk_bf16_f32 v136, v136, v137\nv_cvt_pk_bf16_f32 v137, v138, v139\nds_write_b64 %[sta], v[116:117] offset:23872\n.long 0xd3b1408c, 0x18023d8c\n.long 0xd3b1408e, 0x18023d8e\n"
        "v_cvt_pk_bf16_f32 v120, v120, v121\nv_cvt_pk_bf16_f32 v121, v122, v123\nds_write_b64 %[sta], v[136:137] offset:23904\nv_cvt_pk_bf16_f32 v140, v140, v141\nv_cvt_pk_bf16_f32 v141, v142, v143\n"
        "ds_write_b64 %[sta], v[120:121] offset:31808\nds_write_b64 %[sta], v[140:141] offset:31840\nds_write_b32 %[pg], %[sy]\nv_add_u32 %[sy], 4, %[sy]\n.long 0xd3b04094, 0x1e52212c\n"
        ".long 0xd3b04096, 0x1e5a212e\n.long 0xd3b04098, 0x1e62292c\n.long 0xd3b0409a, 0x1e6a292e\n.long 0xd3b14094, 0x18022594\n.long 0xd3b14096, 0x18022596\n.long 0xd3b04090, 0x1e42192c\n"
        ".long 0xd3b04092, 0x1e4a192e\n.long 0xd3b14098, 0x18022d98\n.long 0xd3b1409a, 0x18022d9a\nv_cvt_pk_bf16_f32 v148, v148, v149\nv_cvt_pk_bf16_f32 v149, v150, v151\n.long 0xd3b0409c, 0x1e72312c\n"
        ".long 0xd3b0409e, 0x1e7a312e\n.long 0xd3b14090, 0x18021d90\n.long 0xd3b14092, 0x18021d92\nv_cvt_pk_bf16_f32 v152, v152, v153\nv_cvt_pk_bf16_f32 v153, v154, v155\n"
        "ds_write_b64 %[sta], v[148:149] offset:8320\n.long 0xd3b040a0, 0x1e82392c\n.long 0xd3b040a2, 0x1e8a392e\n.long 0xd3b1409c, 0x1802359c\n.long 0xd3b1409e, 0x1802359e\n"
        "v_cvt_pk_bf16_f32 v144, v144, v145\nv_cvt_pk_bf16_f32 v145, v146, v147\nds_write_b64 %[sta], v[152:153] offset:16256\n.long 0xd3b140a0, 0x18023da0\n.long 0xd3b140a2, 0x18023da2\n"
        "v_cvt_pk_bf16_f32 v156, v156, v157\nv_cvt_pk_bf16_f32 v157, v158, v159\nds_write_b64 %[sta], v[144:145] offset:384\nv_cvt_pk_bf16_f32 v160, v160, v161\nv_cvt_pk_bf16_f32 v161, v162, v163\n"
        "ds_write_b64 %[sta], v[156:157] offset:24192\nds_write_b64 %[sta], v[160:161] offset:32128\nds_write_b32 %[pg], %[sy]\ns_branch .LE%=\n.LB1s0_%=:\ns_mov_b32 %[s1], 0x100000\n.LB1p0_%=:\n"
        "ds_read_b32 %[pv], %[sy] offset:4\ns_waitcnt lgkmcnt(0)\nv_readfirstlane_b32 %[s0], %[pv]\ns_nop 0\ns_cmp_ge_i32 %[s0], 6\ns_cbranch_scc1 .LB1r0_%=\ns_sleep 1\ns_sub_u32 %[s1], %[s1], 1\n"
        "s_cbranch_scc0 .LB1p0_%=\ns_branch .LB1r0_%=\n.LB1s1_%=:\ns_mov_b32 %[s1], 0x100000\n.LB1p1_%=:\nds_read_b32 %[pv], %[sy] offset:4\ns_waitcnt lgkmcnt(0)\nv_readfirstlane_b32 %[s0], %[pv]\n"
        "s_nop 0\ns_cmp_ge_i32 %[s0], 6\ns_cbranch_scc1 .LB1r1_%=\ns_sleep 1\ns_sub_u32 %[s1], %[s1], 1\ns_cbranch_scc0 .LB1p1_%=\ns_branch .LB1r1_%=\n.LB1s2_%=:\ns_mov_b32 %[s1], 0x100000\n"
        ".LB1p2_%=:\nds_read_b32 %[pv], %[sy] offset:4\ns_waitcnt lgkmcnt(0)\nv_readfirstlane_b32 %[s0], %[pv]\ns_nop 0\ns_cmp_ge_i32 %[s0], 6\ns_cbranch_scc1 .LB1r2_%=\ns_sleep 1\n"
        "s_sub_u32 %[s1], %[s1], 1\ns_cbranch_scc0 .LB1p2_%=\ns_branch .LB1r2_%=\n.LB1t%=:\ns_mov_b32 %[s1], 0x100000\n.LB1q%=:\nds_read_b32 %[pv], %[sy] offset:4\ns_waitcnt lgkmcnt(0)\n"
        "v_readfirstlane_b32 %[s0], %[pv]\ns_nop 0\ns_cmp_ge_i32 %[s0], %[nst]\ns_cbranch_scc1 .LB1u%=\ns_sleep 1\ns_sub_u32 %[s1], %[s1], 1\ns_cbranch_scc0 .LB1q%=\ns_branch .LB1u%=\n.LC%=:\n"
        "s_mov_b32 %[so], 0\nbuffer_load_dwordx4 v[32:35], %[wa], %[rs], %[so] offen offset:0 sc1\nbuffer_load_dwordx4 v[36:39], %[wa], %[rs], %[so] offen offset:1024 sc1\n"
        "buffer_load_dwordx4 v[40:43], %[wa], %[rs], %[so] offen offset:2048 sc1\nbuffer_load_dwordx4 v[44:47], %[wa], %[rs], %[so] offen offset:3072 sc1\ns_mov_b32 %[so], 15360\n"
        "buffer_load_dwordx4 v[48:51], %[wa], %[rs], %[so] offen offset:0 sc1\nbuffer_load_dwordx4 v[52:55], %[wa], %[rs], %[so] offen offset:1024 sc1\n"
        "buffer_load_dwordx4 v[56:59], %[wa], %[rs], %[so] offen offset:2048 sc1\nbuffer_load_dwordx4 v[60:63], %[wa], %[rs], %[so] offen offset:3072 sc1\ns_mov_b32 %[so], 30720\n"
        "buffer_load_dwordx4 v[64:67], %[wa], %[rs], %[so] offen offset:0 sc1\nbuffer_load_dwordx4 v[68:71], %[wa], %[rs], %[so] offen offset:1024 sc1\n"
        "buffer_load_dwordx4 v[72:75], %[wa], %[rs], %[so] offen offset:2048 sc1\nbuffer_load_dwordx4 v[76:79], %[wa], %[rs], %[so] offen offset:3072 sc1\ns_mov_b32 %[so], 46080\n"
        "buffer_load_dwordx4 v[80:83], %[wa], %[rs], %[so] offen offset:0 sc1\nbuffer_load_dwordx4 v[84:87], %[wa], %[rs], %[so] offen offset:1024 sc1\n"
        "buffer_load_dwordx4 v[88:91], %[wa], %[rs], %[so] offen offset:2048 sc1\nbuffer_load_dwordx4 v[92:95], %[wa], %[rs], %[so] offen offset:3072 sc1\ns_barrier\ns_mov_b32 %[s1], 0x100000\n"
        ".LC1w%=:\nds_read_b32 %[pv], %[sy] offset:0\ns_waitcnt lgkmcnt(0)\nv_readfirstlane_b32 %[s0], %[pv]\ns_nop 0\ns_cmp_ge_i32 %[s0], 6\ns_cbranch_scc1 .LC1g%=\ns_sleep 1\n"
        "s_sub_u32 %[s1], %[s1], 1\ns_cbranch_scc0 .LC1w%=\n.LC1g%=:\nds_read_b64_tr_b16 v[24:25], %[xa] offset:3168\nds_read_b64_tr_b16 v[26:27], %[xa] offset:3680\n"
        "ds_read_b64_tr_b16 v[28:29], %[xa] offset:4224\nds_read_b64_tr_b16 v[30:31], %[xa] offset:4736\nds_read_b64_tr_b16 v[12:13], %[xa] offset:0\nds_read_b64_tr_b16 v[14:15], %[xa] offset:512\n"
        "ds_read_b64_tr_b16 v[16:17], %[xa] offset:1056\nds_read_b64_tr_b16 v[18:19], %[xa] offset:1568\ns_waitcnt vmcnt(12) lgkmcnt(6)\nv_mfma_f32_16x16x32_bf16 v[96:99], v[32:35], v[24:27], 0\n"
        "ds_read_b64_tr_b16 v[20:21], %[xa] offset:2112\nv_mfma_f32_16x16x32_bf16 v[116:119], v[36:39], v[24:27], 0\nds_read_b64_tr_b16 v[22:23], %[xa] offset:2624\n"
        "v_mfma_f32_16x16x32_bf16 v[136:139], v[40:43], v[24:27], 0\nds_read_b32 %[pv], %[sy] offset:4\nv_mfma_f32_16x16x32_bf16 v[156:159], v[44:47], v[24:27], 0\n"
        "ds_read_b64_tr_b16 v[24:25], %[xa] offset:8448\ns_waitcnt lgkmcnt(8)\nv_mfma_f32_16x16x32_bf16 v[100:103], v[32:35], v[28:31], 0\nds_read_b64_tr_b16 v[26:27], %[xa] offset:8960\n"
        "v_mfma_f32_16x16x32_bf16 v[120:123], v[36:39], v[28:31], 0\nv_mfma_f32_16x16x32_bf16 v[140:143], v[40:43], v[28:31], 0\nv_mfma_f32_16x16x32_bf16 v[160:163], v[44:47], v[28:31], 0\n"
        "ds_read_b64_tr_b16 v[28:29], %[xa] offset:9504\ns_waitcnt lgkmcnt(8)\nv_mfma_f32_16x16x32_bf16 v[104:107], v[36:39], v[12:15], 0\nds_read_b64_tr_b16 v[30:31], %[xa] offset:10016\n"
        "v_mfma_f32_16x16x32_bf16 v[124:127], v[40:43], v[12:15], 0\nv_mfma_f32_16x16x32_bf16 v[144:147], v[44:47], v[12:15], 0\nds_read_b64_tr_b16 v[12:13], %[xa] offset:5280\ns_waitcnt lgkmcnt(8)\n"
        "v_mfma_f32_16x16x32_bf16 v[108:111], v[36:39], v[16:19], 0\nds_read_b64_tr_b16 v[14:15], %[xa] offset:5792\nv_mfma_f32_16x16x32_bf16 v[128:131], v[40:43], v[16:19], 0\n"
        "v_mfma_f32_16x16x32_bf16 v[148:151], v[44:47], v[16:19], 0\nds_read_b64_tr_b16 v[16:17], %[xa] offset:6336\ns_waitcnt lgkmcnt(8)\nv_mfma_f32_16x16x32_bf16 v[112:115], v[36:39], v[20:23], 0\n"
        "ds_read_b64_tr_b16 v[18:19], %[xa] offset:6848\nv_mfma_f32_16x16x32_bf16 v[132:135], v[40:43], v[20:23], 0\nv_mfma_f32_16x16x32_bf16 v[152:155], v[44:47], v[20:23], 0\n.LC1l%=:\n"
        "s_waitcnt vmcnt(8) lgkmcnt(6)\nv_mfma_f32_16x16x32_bf16 v[96:99], v[48:51], v[24:27], v[96:99]\ns_addk_i32 %[so], 15360\nv_readfirstlane_b32 %[s0], %[pv]\n"
        "v_mfma_f32_16x16x32_bf16 v[116:119], v[52:55], v[24:27], v[116:119]\ns_cmp_ge_i32 %[s0], 6\nds_read_b64_tr_b16 v[20:21], %[xa] offset:7392\n"
        "v_mfma_f32_16x16x32_bf16 v[136:139], v[56:59], v[24:27], v[136:139]\ns_cbranch_scc0 .LC1s0_%=\n.LC1r0_%=:\nds_read_b64_tr_b16 v[22:23], %[xa] offset:7904\n"
        "v_mfma_f32_16x16x32_bf16 v[156:159], v[60:63], v[24:27], v[156:159]\nds_read_b64_tr_b16 v[24:25], %[xa] offset:13728\ns_waitcnt lgkmcnt(7)\n"
        "v_mfma_f32_16x16x32_bf16 v[100:103], v[48:51], v[28:31], v[100:103]\nds_read_b64_tr_b16 v[26:27], %[xa] offset:14240\nbuffer_load_dwordx4 v[32:35], %[wa], %[rs], %[so] offen offset:0 sc1\n"
        "v_mfma_f32_16x16x32_bf16 v[120:123], v[52:55], v[28:31], v[120:123]\nbuffer_load_dwordx4 v[36:39], %[wa], %[rs], %[so] offen offset:1024 sc1\n"
        "v_mfma_f32_16x16x32_bf16 v[140:143], v[56:59], v[28:31], v[140:143]\nv_mfma_f32_16x16x32_bf16 v[160:163], v[60:63], v[28:31], v[160:163]\nds_read_b64_tr_b16 v[28:29], %[xa] offset:14784\n"
        "s_waitcnt lgkmcnt(7)\nv_mfma_f32_16x16x32_bf16 v[104:107], v[52:55], v[12:15], v[104:107]\nds_read_b64_tr_b16 v[30:31], %[xa] offset:15296\n"
        "v_mfma_f32_16x16x32_bf16 v[124:127], v[56:59], v[12:15], v[124:127]\nbuffer_load_dwordx4 v[40:43], %[wa], %[rs], %[so] offen offset:2048 sc1\n"
        "v_mfma_f32_16x16x32_bf16 v[144:147], v[60:63], v[12:15], v[144:147]\nds_read_b64_tr_b16 v[12:13], %[xa] offset:10560\ns_waitcnt lgkmcnt(7)\n"
        "v_mfma_f32_16x16x32_bf16 v[108:111], v[52:55], v[16:19], v[108:111]\nds_read_b64_tr_b16 v[14:15], %[xa] offset:11072\nv_mfma_f32_16x16x32_bf16 v[128:131], v[56:59], v[16:19], v[128:131]\n"
        "buffer_load_dwordx4 v[44:47], %[wa], %[rs], %[so] offen offset:3072 sc1\nv_mfma_f32_16x16x32_bf16 v[148:151], v[60:63], v[16:19], v[148:151]\nds_read_b64_tr_b16 v[16:17], %[xa] offset:11616\n"
        "s_waitcnt lgkmcnt(7)\nv_mfma_f32_16x16x32_bf16 v[112:115], v[52:55], v[20:23], v[112:115]\nv_add_u32 %[sy], 4, %[sy]\nds_read_b64_tr_b16 v[18:19], %[xa] offset:12128\n"
        "v_mfma_f32_16x16x32_bf16 v[132:135], v[56:59], v[20:23], v[132:135]\nds_write_b32 %[pg], %[sy]\nv_mfma_f32_16x16x32_bf16 v[152:155], v[60:63], v[20:23], v[152:155]\n"
        "s_waitcnt vmcnt(8) lgkmcnt(7)\nv_mfma_f32_16x16x32_bf16 v[96:99], v[64:67], v[24:27], v[96:99]\ns_addk_i32 %[so], 15360\nds_read_b64_tr_b16 v[20:21], %[xa] offset:12672\n"
        "v_mfma_f32_16x16x32_bf16 v[116:119], v[68:71], v[24:27], v[116:119]\nds_read_b64_tr_b16 v[22:23], %[xa] offset:13184\nv_mfma_f32_16x16x32_bf16 v[136:139], v[72:75], v[24:27], v[136:139]\n"
        "ds_read_b32 %[pv], %[sy] offset:4\nbuffer_load_dwordx4 v[48:51], %[wa], %[rs], %[so] offen offset:0 sc1\nv_mfma_f32_16x16x32_bf16 v[156:159], v[76:79], v[24:27], v[156:159]\n"
        "ds_read_b64_tr_b16 v[24:25], %[xa] offset:19008\ns_waitcnt lgkmcnt(9)\nv_mfma_f32_16x16x32_bf16 v[100:103], v[64:67], v[28:31], v[100:103]\nds_read_b64_tr_b16 v[26:27], %[xa] offset:19520\n"
        "v_mfma_f32_16x16x32_bf16 v[120:123], v[68:71], v[28:31], v[120:123]\nbuffer_load_dwordx4 v[52:55], %[wa], %[rs], %[so] offen offset:1024 sc1\n"
        "v_mfma_f32_16x16x32_bf16 v[140:143], v[72:75], v[28:31], v[140:143]\nv_mfma_f32_16x16x32_bf16 v[160:163], v[76:79], v[28:31], v[160:163]\nds_read_b64_tr_b16 v[28:29], %[xa] offset:20064\n"
        "s_waitcnt lgkmcnt(9)\nv_mfma_f32_16x16x32_bf16 v[104:107], v[68:71], v[12:15], v[104:107]\nds_read_b64_tr_b16 v[30:31], %[xa] offset:20576\n"
        "v_mfma_f32_16x16x32_bf16 v[124:127], v[72:75], v[12:15], v[124:127]\nbuffer_load_dwordx4 v[56:59], %[wa], %[rs], %[so] offen offset:2048 sc1\n"
        "v_mfma_f32_16x16x32_bf16 v[144:147], v[76:79], v[12:15], v[144:147]\nds_read_b64_tr_b16 v[12:13], %[xa] offset:15840\ns_waitcnt lgkmcnt(9)\n"
        "v_mfma_f32_16x16x32_bf16 v[108:111], v[68:71], v[16:19], v[108:111]\nds_read_b64_tr_b16 v[14:15], %[xa] offset:16352\nv_mfma_f32_16x16x32_bf16 v[128:131], v[72:75], v[16:19], v[128:131]\n"
        "buffer_load_dwordx4 v[60:63], %[wa], %[rs], %[so] offen offset:3072 sc1\nv_mfma_f32_16x16x32_bf16 v[148:151], v[76:79], v[16:19], v[148:151]\nds_read_b64_tr_b16 v[16:17], %[xa] offset:16896\n"
        "s_waitcnt lgkmcnt(8)\nv_mfma_f32_16x16x32_bf16 v[112:115], v[68:71], v[20:23], v[112:115]\nds_read_b64_tr_b16 v[18:19], %[xa] offset:17408\n"
        "v_mfma_f32_16x16x32_bf16 v[132:135], v[72:75], v[20:23], v[132:135]\nv_mfma_f32_16x16x32_bf16 v[152:155], v[76:79], v[20:23], v[152:155]\ns_waitcnt vmcnt(8) lgkmcnt(6)\n"
        "v_mfma_f32_16x16x32_bf16 v[96:99], v[80:83], v[24:27], v[96:99]\ns_addk_i32 %[so], 15360\nv_readfirstlane_b32 %[s0], %[pv]\n"
        "v_mfma_f32_16x16x32_bf16 v[116:119], v[84:87], v[24:27], v[116:119]\ns_cmp_ge_i32 %[s0], 6\nds_read_b64_tr_b16 v[20:21], %[xa] offset:17952\n"
        "v_mfma_f32_16x16x32_bf16 v[136:139], v[88:91], v[24:27], v[136:139]\ns_cbranch_scc0 .LC1s1_%=\n.LC1r1_%=:\nds_read_b64_tr_b16 v[22:23], %[xa] offset:18464\n"
        "v_mfma_f32_16x16x32_bf16 v[156:159], v[92:95], v[24:27], v[156:159]\nds_read_b64_tr_b16 v[24:25], %[xa] offset:24288\ns_waitcnt lgkmcnt(7)\n"
        "v_mfma_f32_16x16x32_bf16 v[100:103], v[80:83], v[28:31], v[100:103]\nds_read_b64_tr_b16 v[26:27], %[xa] offset:24800\nbuffer_load_dwordx4 v[64:67], %[wa], %[rs], %[so] offen offset:0 sc1\n"
        "v_mfma_f32_16x16x32_bf16 v[120:123], v[84:87], v[28:31], v[120:123]\nbuffer_load_dwordx4 v[68:71], %[wa], %[rs], %[so] offen offset:1024 sc1\n"
        "v_mfma_f32_16x16x32_bf16 v[140:143], v[88:91], v[28:31], v[140:143]\nv_mfma_f32_16x16x32_bf16 v[160:163], v[92:95], v[28:31], v[160:163]\nds_read_b64_tr_b16 v[28:29], %[xa] offset:25344\n"
        "s_waitcnt lgkmcnt(7)\nv_mfma_f32_16x16x32_bf16 v[104:107], v[84:87], v[12:15], v[104:107]\nds_read_b64_tr_b16 v[30:31], %[xa] offset:25856\n"
        "v_mfma_f32_16x16x32_bf16 v[124:127], v[88:91], v[12:15], v[124:127]\nbuffer_load_dwordx4 v[72:75], %[wa], %[rs], %[so] offen offset:2048 sc1\n"
        "v_mfma_f32_16x16x32_bf16 v[144:147], v[92:95], v[12:15], v[144:147]\nds_read_b64_tr_b16 v[12:13], %[xa] offset:21120\ns_waitcnt lgkmcnt(7)\n"
        "v_mfma_f32_16x16x32_bf16 v[108:111], v[84:87], v[16:19], v[108:111]\nds_read_b64_tr_b16 v[14:15], %[xa] offset:21632\nv_mfma_f32_16x16x32_bf16 v[128:131], v[88:91], v[16:19], v[128:131]\n"
        "buffer_load_dwordx4 v[76:79], %[wa], %[rs], %[so] offen offset:3072 sc1\nv_mfma_f32_16x16x32_bf16 v[148:151], v[92:95], v[16:19], v[148:151]\nds_read_b64_tr_b16 v[16:17], %[xa] offset:22176\n"
        "s_waitcnt lgkmcnt(7)\nv_mfma_f32_16x16x32_bf16 v[112:115], v[84:87], v[20:23], v[112:115]\nv_add_u32 %[sy], 4, %[sy]\nds_read_b64_tr_b16 v[18:19], %[xa] offset:22688\n"
        "v_mfma_f32_16x16x32_bf16 v[132:135], v[88:91], v[20:23], v[132:135]\nds_write_b32 %[pg], %[sy]\nv_mfma_f32_16x16x32_bf16 v[152:155], v[92:95], v[20:23], v[152:155]\n"
        "s_waitcnt vmcnt(8) lgkmcnt(7)\nv_mfma_f32_16x16x32_bf16 v[96:99], v[32:35], v[24:27], v[96:99]\ns_addk_i32 %[so], 15360\nds_read_b64_tr_b16 v[20:21], %[xa] offset:23232\n"
        "v_mfma_f32_16x16x32_bf16 v[116:119], v[36:39], v[24:27], v[116:119]\nds_read_b64_tr_b16 v[22:23], %[xa] offset:23744\nv_mfma_f32_16x16x32_bf16 v[136:139], v[40:43], v[24:27], v[136:139]\n"
        "ds_read_b32 %[pv], %[sy] offset:4\nbuffer_load_dwordx4 v[80:83], %[wa], %[rs], %[so] offen offset:0 sc1\nv_mfma_f32_16x16x32_bf16 v[156:159], v[44:47], v[24:27], v[156:159]\n"
        "ds_read_b64_tr_b16 v[24:25], %[xa] offset:29568\ns_waitcnt lgkmcnt(9)\nv_mfma_f32_16x16x32_bf16 v[100:103], v[32:35], v[28:31], v[100:103]\nds_read_b64_tr_b16 v[26:27], %[xa] offset:30080\n"
        "v_mfma_f32_16x16x32_bf16 v[120:123], v[36:39], v[28:31], v[120:123]\nbuffer_load_dwordx4 v[84:87], %[wa], %[rs], %[so] offen offset:1024 sc1\n"
        "v_mfma_f32_16x16x32_bf16 v[140:143], v[40:43], v[28:31], v[140:143]\nv_mfma_f32_16x16x32_bf16 v[160:163], v[44:47], v[28:31], v[160:163]\nds_read_b64_tr_b16 v[28:29], %[xa] offset:30624\n"
        "s_waitcnt lgkmcnt(9)\nv_mfma_f32_16x16x32_bf16 v[104:107], v[36:39], v[12:15], v[104:107]\nds_read_b64_tr_b16 v[30:31], %[xa] offset:31136\n"
        "v_mfma_f32_16x16x32_bf16 v[124:127], v[40:43], v[12:15], v[124:127]\nbuffer_load_dwordx4 v[88:91], %[wa], %[rs], %[so] offen offset:2048 sc1\n"
        "v_mfma_f32_16x16x32_bf16 v[144:147], v[44:47], v[12:15], v[144:147]\nds_read_b64_tr_b16 v[12:13], %[xa] offset:26400\ns_waitcnt lgkmcnt(9)\n"
        "v_mfma_f32_16x16x32_bf16 v[108:111], v[36:39], v[16:19], v[108:111]\nds_read_b64_tr_b16 v[14:15], %[xa] offset:26912\nv_mfma_f32_16x16x32_bf16 v[128:131], v[40:43], v[16:19], v[128:131]\n"
        "buffer_load_dwordx4 v[92:95], %[wa], %[rs], %[so] offen offset:3072 sc1\nv_mfma_f32_16x16x32_bf16 v[148:151], v[44:47], v[16:19], v[148:151]\nds_read_b64_tr_b16 v[16:17], %[xa] offset:27456\n"
        "s_waitcnt lgkmcnt(8)\nv_mfma_f32_16x16x32_bf16 v[112:115], v[36:39], v[20:23], v[112:115]\nds_read_b64_tr_b16 v[18:19], %[xa] offset:27968\n"
        "v_mfma_f32_16x16x32_bf16 v[132:135], v[40:43], v[20:23], v[132:135]\ns_cmp_lg_u32 %[so], 291840\nv_mfma_f32_16x16x32_bf16 v[152:155], v[44:47], v[20:23], v[152:155]\n"
        "v_add_u32 %[xa], 21120, %[xa]\ns_cbranch_scc1 .LC1l%=\ns_waitcnt vmcnt(8) lgkmcnt(6)\nv_mfma_f32_16x16x32_bf16 v[96:99], v[48:51], v[24:27], v[96:99]\nv_readfirstlane_b32 %[s0], %[pv]\n"
        "ds_read_b64_tr_b16 v[20:21], %[xa] offset:7392\nv_mfma_f32_16x16x32_bf16 v[116:119], v[52:55], v[24:27], v[116:119]\ns_cmp_ge_i32 %[s0], 6\nds_read_b64_tr_b16 v[22:23], %[xa] offset:7904\n"
        "v_mfma_f32_16x16x32_bf16 v[136:139], v[56:59], v[24:27], v[136:139]\ns_cbranch_scc0 .LC1s2_%=\n.LC1r2_%=:\nv_mfma_f32_16x16x32_bf16 v[156:159], v[60:63], v[24:27], v[156:159]\n"
        "ds_read_b64_tr_b16 v[24:25], %[xa] offset:13728\ns_waitcnt lgkmcnt(7)\nv_mfma_f32_16x16x32_bf16 v[100:103], v[48:51], v[28:31], v[100:103]\nds_read_b64_tr_b16 v[26:27], %[xa] offset:14240\n"
        "v_mfma_f32_16x16x32_bf16 v[120:123], v[52:55], v[28:31], v[120:123]\nv_mfma_f32_16x16x32_bf16 v[140:143], v[56:59], v[28:31], v[140:143]\n"
        "v_mfma_f32_16x16x32_bf16 v[160:163], v[60:63], v[28:31], v[160:163]\nds_read_b64_tr_b16 v[28:29], %[xa] offset:14784\ns_waitcnt lgkmcnt(7)\n"
        "v_mfma_f32_16x16x32_bf16 v[104:107], v[52:55], v[12:15], v[104:107]\nds_read_b64_tr_b16 v[30:31], %[xa] offset:15296\nv_mfma_f32_16x16x32_bf16 v[124:127], v[56:59], v[12:15], v[124:127]\n"
        "v_mfma_f32_16x16x32_bf16 v[144:147], v[60:63], v[12:15], v[144:147]\nds_read_b64_tr_b16 v[12:13], %[xa] offset:10560\ns_waitcnt lgkmcnt(7)\n"
        "v_mfma_f32_16x16x32_bf16 v[108:111], v[52:55], v[16:19], v[108:111]\nds_read_b64_tr_b16 v[14:15], %[xa] offset:11072\nv_mfma_f32_16x16x32_bf16 v[128:131], v[56:59], v[16:19], v[128:131]\n"
        "v_mfma_f32_16x16x32_bf16 v[148:151], v[60:63], v[16:19], v[148:151]\nds_read_b64_tr_b16 v[16:17], %[xa] offset:11616\ns_waitcnt lgkmcnt(7)\n"
        "v_mfma_f32_16x16x32_bf16 v[112:115], v[52:55], v[20:23], v[112:115]\nv_add_u32 %[sy], 4, %[sy]\nds_read_b64_tr_b16 v[18:19], %[xa] offset:12128\n"
        "v_mfma_f32_16x16x32_bf16 v[132:135], v[56:59], v[20:23], v[132:135]\nv_mfma_f32_16x16x32_bf16 v[152:155], v[60:63], v[20:23], v[152:155]\ns_waitcnt vmcnt(4) lgkmcnt(6)\n"
        "v_mfma_f32_16x16x32_bf16 v[96:99], v[64:67], v[24:27], v[96:99]\nds_read_b64_tr_b16 v[20:21], %[xa] offset:12672\nv_mfma_f32_16x16x32_bf16 v[116:119], v[68:71], v[24:27], v[116:119]\n"
        "ds_read_b64_tr_b16 v[22:23], %[xa] offset:13184\nv_mfma_f32_16x16x32_bf16 v[136:139], v[72:75], v[24:27], v[136:139]\nds_read_b32 %[pv], %[sy] offset:4\n"
        "v_mfma_f32_16x16x32_bf16 v[156:159], v[76:79], v[24:27], v[156:159]\nds_read_b64_tr_b16 v[24:25], %[xa] offset:19008\ns_waitcnt lgkmcnt(8)\n"
        "v_mfma_f32_16x16x32_bf16 v[100:103], v[64:67], v[28:31], v[100:103]\nds_read_b64_tr_b16 v[26:27], %[xa] offset:19520\nv_mfma_f32_16x16x32_bf16 v[120:123], v[68:71], v[28:31], v[120:123]\n"
        "v_mfma_f32_16x16x32_bf16 v[140:143], v[72:75], v[28:31], v[140:143]\nv_mfma_f32_16x16x32_bf16 v[160:163], v[76:79], v[28:31], v[160:163]\nds_read_b64_tr_b16 v[28:29], %[xa] offset:20064\n"
        "s_waitcnt lgkmcnt(8)\nv_mfma_f32_16x16x32_bf16 v[104:107], v[68:71], v[12:15], v[104:107]\nds_read_b64_tr_b16 v[30:31], %[xa] offset:20576\n"
        "v_mfma_f32_16x16x32_bf16 v[124:127], v[72:75], v[12:15], v[124:127]\nv_mfma_f32_16x16x32_bf16 v[144:147], v[76:79], v[12:15], v[144:147]\nds_read_b64_tr_b16 v[12:13], %[xa] offset:15840\n"
        "s_waitcnt lgkmcnt(8)\nv_mfma_f32_16x16x32_bf16 v[108:111], v[68:71], v[16:19], v[108:111]\nds_read_b64_tr_b16 v[14:15], %[xa] offset:16352\n"
        "v_mfma_f32_16x16x32_bf16 v[128:131], v[72:75], v[16:19], v[128:131]\nv_mfma_f32_16x16x32_bf16 v[148:151], v[76:79], v[16:19], v[148:151]\nds_read_b64_tr_b16 v[16:17], %[xa] offset:16896\n"
        "s_waitcnt lgkmcnt(8)\nv_mfma_f32_16x16x32_bf16 v[112:115], v[68:71], v[20:23], v[112:115]\nds_read_b64_tr_b16 v[18:19], %[xa] offset:17408\n"
        "v_mfma_f32_16x16x32_bf16 v[132:135], v[72:75], v[20:23], v[132:135]\nv_mfma_f32_16x16x32_bf16 v[152:155], v[76:79], v[20:23], v[152:155]\ns_waitcnt vmcnt(0) lgkmcnt(6)\n"
        "v_mfma_f32_16x16x32_bf16 v[96:99], v[80:83], v[24:27], v[96:99]\nv_readfirstlane_b32 %[s0], %[pv]\nds_read_b64_tr_b16 v[20:21], %[xa] offset:17952\n"
        "v_mfma_f32_16x16x32_bf16 v[116:119], v[84:87], v[24:27], v[116:119]\nds_read_b64_tr_b16 v[22:23], %[xa] offset:18464\nv_mfma_f32_16x16x32_bf16 v[136:139], v[88:91], v[24:27], v[136:139]\n"
        "s_cmp_ge_i32 %[s0], %[nst]\nv_mfma_f32_16x16x32_bf16 v[156:159], v[92:95], v[24:27], v[156:159]\ns_cbranch_scc0 .LC1t%=\n.LC1u%=:\ns_waitcnt lgkmcnt(6)\n"
        "v_mfma_f32_16x16x32_bf16 v[100:103], v[80:83], v[28:31], v[100:103]\nv_add_u32 %[sy], 4, %[sy]\nds_read_b128 v[24:27], %[mra] offset:768\n"
        "v_mfma_f32_16x16x32_bf16 v[120:123], v[84:87], v[28:31], v[120:123]\nv_mfma_f32_16x16x32_bf16 v[140:143], v[88:91], v[28:31], v[140:143]\n"
        "v_mfma_f32_16x16x32_bf16 v[160:163], v[92:95], v[28:31], v[160:163]\nds_read_b128 v[32:35], %[cwa] offset:448\ns_waitcnt lgkmcnt(6)\n"
        "v_mfma_f32_16x16x32_bf16 v[104:107], v[84:87], v[12:15], v[104:107]\nds_read_b128 v[36:39], %[cwa] offset:256\nds_read_b128 v[28:31], %[mra] offset:1024\n"
        "v_mfma_f32_16x16x32_bf16 v[124:127], v[88:91], v[12:15], v[124:127]\nds_read_b128 v[40:43], %[cwa] offset:832\nv_mfma_f32_16x16x32_bf16 v[144:147], v[92:95], v[12:15], v[144:147]\n"
        "ds_read_b128 v[44:47], %[cwa] offset:896\ns_waitcnt lgkmcnt(8)\nv_mfma_f32_16x16x32_bf16 v[108:111], v[84:87], v[16:19], v[108:111]\nv_add_u32 %[sy], 4, %[sy]\n"
        "ds_read_b128 v[12:15], %[mra] offset:0\nv_mfma_f32_16x16x32_bf16 v[128:131], v[88:91], v[16:19], v[128:131]\nv_mfma_f32_16x16x32_bf16 v[148:151], v[92:95], v[16:19], v[148:151]\n"
        "ds_read_b128 v[16:19], %[mra] offset:256\ns_waitcnt lgkmcnt(8)\nv_mfma_f32_16x16x32_bf16 v[112:115], v[84:87], v[20:23], v[112:115]\n"
        "v_mfma_f32_16x16x32_bf16 v[132:135], v[88:91], v[20:23], v[132:135]\nv_mfma_f32_16x16x32_bf16 v[152:155], v[92:95], v[20:23], v[152:155]\nds_read_b128 v[20:23], %[mra] offset:512\n"
        "s_setprio %[pfs]\ns_waitcnt lgkmcnt(1)\n.long 0xd3b04060, 0x1d823120\n.long 0xd3b04062, 0x1d8a3122\n.long 0xd3b04074, 0x1dd23124\n.long 0xd3b04076, 0x1dda3126\n.long 0xd3b14060, 0x18023560\n"
        ".long 0xd3b14062, 0x18023562\n.long 0xd3b04064, 0x1d923920\n.long 0xd3b04066, 0x1d9a3922\n.long 0xd3b14074, 0x18023574\n.long 0xd3b14076, 0x18023576\nv_cvt_pk_bf16_f32 v96, v96, v97\n"
        "v_cvt_pk_bf16_f32 v97, v98, v99\n.long 0xd3b04078, 0x1de23924\n.long 0xd3b0407a, 0x1dea3926\n.long 0xd3b14064, 0x18023d64\n.long 0xd3b14066, 0x18023d66\nv_cvt_pk_bf16_f32 v116, v116, v117\n"
        "v_cvt_pk_bf16_f32 v117, v118, v119\nds_write_b64 %[sta], v[96:97] offset:24032\n.long 0xd3b04068, 0x1da21924\n.long 0xd3b0406a, 0x1daa1926\n.long 0xd3b14078, 0x18023d78\n"
        ".long 0xd3b1407a, 0x18023d7a\nv_cvt_pk_bf16_f32 v100, v100, v101\nv_cvt_pk_bf16_f32 v101, v102, v103\nds_write_b64 %[sta], v[116:117] offset:23936\n.long 0xd3b0406c, 0x1db22124\n"
        ".long 0xd3b0406e, 0x1dba2126\n.long 0xd3b14068, 0x18021d68\n.long 0xd3b1406a, 0x18021d6a\nv_cvt_pk_bf16_f32 v120, v120, v121\nv_cvt_pk_bf16_f32 v121, v122, v123\n"
        "ds_write_b64 %[sta], v[100:101] offset:31968\ns_waitcnt lgkmcnt(3)\n.long 0xd3b04070, 0x1dc22924\n.long 0xd3b04072, 0x1dca2926\n.long 0xd3b1406c, 0x1802256c\n.long 0xd3b1406e, 0x1802256e\n"
        "v_cvt_pk_bf16_f32 v104, v104, v105\nv_cvt_pk_bf16_f32 v105, v106, v107\nds_write_b64 %[sta], v[120:121] offset:31872\n.long 0xd3b14070, 0x18022d70\n.long 0xd3b14072, 0x18022d72\n"
        "v_cvt_pk_bf16_f32 v108, v108, v109\nv_cvt_pk_bf16_f32 v109, v110, v111\nds_write_b64 %[sta], v[104:105] offset:128\nv_cvt_pk_bf16_f32 v112, v112, v113\nv_cvt_pk_bf16_f32 v113, v114, v115\n"
        "ds_write_b64 %[sta], v[108:109] offset:8064\nds_write_b64 %[sta], v[112:113] offset:16000\nds_write_b32 %[pg], %[sy]\nv_add_u32 %[sy], 4, %[sy]\n.long 0xd3b04088, 0x1e223128\n"
        ".long 0xd3b0408a, 0x1e2a312a\n.long 0xd3b0409c, 0x1e72312c\n.long 0xd3b0409e, 0x1e7a312e\n.long 0xd3b14088, 0x18023588\n.long 0xd3b1408a, 0x1802358a\n.long 0xd3b0408c, 0x1e323928\n"
        ".long 0xd3b0408e, 0x1e3a392a\n.long 0xd3b1409c, 0x1802359c\n.long 0xd3b1409e, 0x1802359e\nv_cvt_pk_bf16_f32 v136, v136, v137\nv_cvt_pk_bf16_f32 v137, v138, v139\n.long 0xd3b040a0, 0x1e82392c\n"
        ".long 0xd3b040a2, 0x1e8a392e\n.long 0xd3b1408c, 0x18023d8c\n.long 0xd3b1408e, 0x18023d8e\nv_cvt_pk_bf16_f32 v156, v156, v157\nv_cvt_pk_bf16_f32 v157, v158, v159\n"
        "ds_write_b64 %[sta], v[136:137] offset:24224\n.long 0xd3b0407c, 0x1df21928\n.long 0xd3b0407e, 0x1dfa192a\n.long 0xd3b140a0, 0x18023da0\n.long 0xd3b140a2, 0x18023da2\n"
        "v_cvt_pk_bf16_f32 v140, v140, v141\nv_cvt_pk_bf16_f32 v141, v142, v143\nds_write_b64 %[sta], v[156:157] offset:24256\n.long 0xd3b04090, 0x1e42192c\n.long 0xd3b04092, 0x1e4a192e\n"
        ".long 0xd3b1407c, 0x18021d7c\n.long 0xd3b1407e, 0x18021d7e\nv_cvt_pk_bf16_f32 v160, v160, v161\nv_cvt_pk_bf16_f32 v161, v162, v163\nds_write_b64 %[sta], v[140:141] offset:32160\n"
        ".long 0xd3b04080, 0x1e022128\n.long 0xd3b04082, 0x1e0a212a\n.long 0xd3b14090, 0x18021d90\n.long 0xd3b14092, 0x18021d92\nv_cvt_pk_bf16_f32 v124, v124, v125\nv_cvt_pk_bf16_f32 v125, v126, v127\n"
        "ds_write_b64 %[sta], v[160:161] offset:32192\n.long 0xd3b04094, 0x1e52212c\n.long 0xd3b04096, 0x1e5a212e\n.long 0xd3b14080, 0x18022580\n.long 0xd3b14082, 0x18022582\n"
        "v_cvt_pk_bf16_f32 v144, v144, v145\nv_cvt_pk_bf16_f32 v145, v146, v147\nds_write_b64 %[sta], v[124:125] offset:416\n.long 0xd3b04084, 0x1e122928\n.long 0xd3b04086, 0x1e1a292a\n"
        ".long 0xd3b14094, 0x18022594\n.long 0xd3b14096, 0x18022596\nv_cvt_pk_bf16_f32 v128, v128, v129\nv_cvt_pk_bf16_f32 v129, v130, v131\nds_write_b64 %[sta], v[144:145] offset:448\n"
        ".long 0xd3b04098, 0x1e62292c\n.long 0xd3b0409a, 0x1e6a292e\n.long 0xd3b14084, 0x18022d84\n.long 0xd3b14086, 0x18022d86\nv_cvt_pk_bf16_f32 v148, v148, v149\nv_cvt_pk_bf16_f32 v149, v150, v151\n"
        "ds_write_b64 %[sta], v[128:129] offset:8352\n.long 0xd3b14098, 0x18022d98\n.long 0xd3b1409a, 0x18022d9a\nv_cvt_pk_bf16_f32 v132, v132, v133\nv_cvt_pk_bf16_f32 v133, v134, v135\n"
        "ds_write_b64 %[sta], v[148:149] offset:8384\nv_cvt_pk_bf16_f32 v152, v152, v153\nv_cvt_pk_bf16_f32 v153, v154, v155\nds_write_b64 %[sta], v[132:133] offset:16288\n"
        "ds_write_b64 %[sta], v[152:153] offset:16320\nds_write_b32 %[pg], %[sy]\ns_branch .LE%=\n.LC1s0_%=:\ns_mov_b32 %[s1], 0x100000\n.LC1p0_%=:\nds_read_b32 %[pv], %[sy] offset:4\n"
        "s_waitcnt lgkmcnt(0)\nv_readfirstlane_b32 %[s0], %[pv]\ns_nop 0\ns_cmp_ge_i32 %[s0], 6\ns_cbranch_scc1 .LC1r0_%=\ns_sleep 1\ns_sub_u32 %[s1], %[s1], 1\ns_cbranch_scc0 .LC1p0_%=\n"
        "s_branch .LC1r0_%=\n.LC1s1_%=:\ns_mov_b32 %[s1], 0x100000\n.LC1p1_%=:\nds_read_b32 %[pv], %[sy] offset:4\ns_waitcnt lgkmcnt(0)\nv_readfirstlane_b32 %[s0], %[pv]\ns_nop 0\n"
        "s_cmp_ge_i32 %[s0], 6\ns_cbranch_scc1 .LC1r1_%=\ns_sleep 1\ns_sub_u32 %[s1], %[s1], 1\ns_cbranch_scc0 .LC1p1_%=\ns_branch .LC1r1_%=\n.LC1s2_%=:\ns_mov_b32 %[s1], 0x100000\n.LC1p2_%=:\n"
        "ds_read_b32 %[pv], %[sy] offset:4\ns_waitcnt lgkmcnt(0)\nv_readfirstlane_b32 %[s0], %[pv]\ns_nop 0\ns_cmp_ge_i32 %[s0], 6\ns_cbranch_scc1 .LC1r2_%=\ns_sleep 1\ns_sub_u32 %[s1], %[s1], 1\n"
        "s_cbranch_scc0 .LC1p2_%=\ns_branch .LC1r2_%=\n.LC1t%=:\ns_mov_b32 %[s1], 0x100000\n.LC1q%=:\nds_read_b32 %[pv], %[sy] offset:4\ns_waitcnt lgkmcnt(0)\nv_readfirstlane_b32 %[s0], %[pv]\n"
        "s_nop 0\ns_cmp_ge_i32 %[s0], %[nst]\ns_cbranch_scc1 .LC1u%=\ns_sleep 1\ns_sub_u32 %[s1], %[s1], 1\ns_cbranch_scc0 .LC1q%=\ns_branch .LC1u%=\n.LE%=:\n"
        : [pv] "=&v"(pv), [xa] "+v"(xa), [sy] "+v"(sy), [so] "=&s"(so), [s0] "=&s"(s0_), [s1] "=&s"(s1_)
        : [wa] "v"(wa), [rs] "s"(wrsrc), [pg] "v"(pga), [cwa] "v"(cwa), [mra] "v"(mra), [sta] "v"(sta), [w] "s"(w), [ow] "i"(SY_W - SY_X), [pfs] "i"(PFS), [pf] "i"(PF), [nst] "i"(NST1)
        : "memory", "scc", "v12", "v13", "v14", "v15", "v16", "v17", "v18", "v19", "v20", "v21", "v22", "v23", "v24", "v25", "v26", "v27", "v28", "v29", "v30", "v31", "v32", "v33", "v34", "v35", "v36", "v37", "v38", "v39", "v40", "v41", "v42", "v43", "v44", "v45", "v46", "v47", "v48", "v49", "v50", "v51", "v52", "v53", "v54", "v55", "v56", "v57", "v58", "v59", "v60", "v61", "v62", "v63", "v64", "v65", "v66", "v67", "v68", "v69", "v70", "v71", "v72", "v73", "v74", "v75", "v76", "v77", "v78", "v79", "v80", "v81", "v82", "v83", "v84", "v85", "v86", "v87", "v88", "v89", "v90", "v91", "v92", "v93", "v94", "v95", "v96", "v97", "v98", "v99", "v100", "v101", "v102", "v103", "v104", "v105", "v106", "v107", "v108", "v109", "v110", "v111", "v112", "v113", "v114", "v115", "v116", "v117", "v118", "v119", "v120", "v121", "v122", "v123", "v124", "v125", "v126", "v127", "v128", "v129", "v130", "v131", "v132", "v133", "v134", "v135", "v136", "v137", "v138", "v139", "v140", "v141", "v142", "v143", "v144", "v145", "v146", "v147", "v148", "v149", "v150", "v151", "v152", "v153", "v154", "v155", "v156", "v157", "v158", "v159", "v160", "v161", "v162", "v163", "v164", "v165", "v166", "v167");
    }
    }
    ST(5);
    LANE(lane);
    const int t = 64 * w + lane;
    if (!layer && w == 3) {
      int spins = 0;
      _Pragma("unroll 1")
      while (true) {
        asm volatile("" ::: "memory");
        u32 d = 0;
#pragma unroll
        for (int i = 0; i < 2; ++i) {
          const u32x4 f = __builtin_amdgcn_raw_buffer_load_b128(frs, lane * 16, FL_M2 + i * 1024, 16);
          d |= (f[0] ^ tg0) | (f[1] ^ tg1) | (f[2] ^ tg0) | (f[3] ^ tg1);
        }
        if (__builtin_amdgcn_ballot_w64(d != 0) == 0 || ++spins > GCAP) break;
        __builtin_amdgcn_s_sleep(8);
      }
      VMCNT(0);
      ST(8);
    }
    if (layer && b == 0 && w == 1) {
      if (lane == 0) __builtin_amdgcn_raw_buffer_store_b64((u32x2){tg0, tg1}, frs, 0, 0, 16);
    }
    ST_FLUSH(kid);
    return;
  }
  const int j = w - 4, grp = j >> 2, mt2 = j & 3, kh = grp, qt = mt2, tt = t - 256;
  u32 l16 = lane * 16;
  int xso = g == (l15 >> 2) ? (rb * 8 + hg) * 1024 + mt2 * 256 + l15 * 16 : -1;
  const u32 tq1 = tg0 + tg1;
  if (layer) {
    const __amdgpu_buffer_rsrc_t xrs = __builtin_amdgcn_make_buffer_rsrc((void*)(xt + (size_t)rb * 51200), 0, 102400, 0x00020000);
    if (grp) __builtin_amdgcn_s_barrier();
    int gp[5], gh[3];
#pragma unroll
    for (int i = 0; i < 5; ++i) { const int p = i < 4 ? 4 * (i >> 1) + (i & 1) + 2 * grp : 8 + grp; gp[i] = PAIR(p, rot) * 10240; }
    const int ho = (mt2 & 1) * 5120 + (rb ? 4096 : 0);
    gh[0] = (mt2 & 2 ? gp[1] : gp[0]) + ho; gh[1] = (mt2 & 2 ? gp[3] : gp[2]) + ho; gh[2] = mt2 & 2 ? 102400 : gp[4] + ho;
    const int ck = (lane & 7) ^ ((lane >> 3) & 6);
    u32 vm0 = l16 + mt2 * 1024, vm1 = vm0 + 5120;
    u32 lm0 = LDSA(At) + grp * 21120 + (1 + (ck >> 1)) * 1056 + (8 * mt2 + (lane >> 3)) * 32 + (ck & 1) * 16, lm1 = lm0 + 84480 - grp * 10560;
    u32 lh0 = LDSA(At) + j * 5280 + l16, lh1 = lh0 + 84480 - grp * 10560, sy0 = sya + 8 * grp, sy1 = sya + 32 + 4 * grp, syh = sy0 + 2 * (mt2 & 2), tm;
    u32 xsb = LDSA(At) + (mt2 + 1) * 1056 + (4 * g + ((lane >> 2) & 3)) * 32 + 8 * (lane & 3), xsa = xsb + (12 + (hg & 1)) * 5280;
    xsb += xs0 * 5280;
    const int ns = hg;
    const u64 m1 = 0xaaaaaaaaaaaaaaaaull, m2 = 0xccccccccccccccccull;
    int cap, s0_, s1_;
    __builtin_amdgcn_s_setprio(PTX);
    if (grp) __builtin_amdgcn_s_sleep(XSLEEP1); else __builtin_amdgcn_s_sleep(XSLEEP);
      asm volatile(
        "s_mov_b32 %[cap], 0x10000\nbuffer_load_dwordx4 v[132:135], %[vm0], %[xrs], %[gp0] offen sc1\nbuffer_load_dwordx4 v[136:139], %[vm1], %[xrs], %[gp0] offen sc1\n"
        "buffer_load_dwordx4 v[140:143], %[l16], %[xrs], %[gh0] offen sc1\nbuffer_load_dwordx4 v[144:147], %[vm0], %[xrs], %[gp1] offen sc1\ns_cmp_lt_u32 %[j], 4\ns_cbranch_scc0 .LP0_%=\ns_barrier\n"
        ".LP0_%=:\ns_waitcnt vmcnt(3)\nv_max3_u32 %[tm], v132, v133, v134\nv_max_u32 %[tm], %[tm], v135\nv_cmp_eq_u32 vcc, -1, %[tm]\ns_cbranch_vccnz .LR0_%=\n.LK0_%=:\n"
        "ds_write_b128 %[lm0], v[132:135] offset:0\nbuffer_load_dwordx4 v[148:151], %[vm1], %[xrs], %[gp1] offen sc1\n.LP1_%=:\ns_waitcnt vmcnt(3)\nv_max3_u32 %[tm], v136, v137, v138\n"
        "v_max_u32 %[tm], %[tm], v139\nv_cmp_eq_u32 vcc, -1, %[tm]\ns_cbranch_vccnz .LR1_%=\n.LK1_%=:\nds_write_b128 %[lm0], v[136:139] offset:5280\ns_mov_b64 exec, 1\n"
        "ds_add_u32 %[sy0], %[one] offset:%[ox]+0\ns_mov_b64 exec, -1\nbuffer_load_dwordx4 v[132:135], %[vm0], %[xrs], %[gp2] offen sc1\n.LP2_%=:\ns_waitcnt vmcnt(3)\n"
        "v_max3_u32 %[tm], v140, v141, v142\nv_max_u32 %[tm], %[tm], v143\nv_cmp_eq_u32 vcc, -1, %[tm]\ns_cbranch_vccnz .LR2_%=\n.LK2_%=:\nds_write_b128 %[lh0], v[140:143] offset:0\ns_mov_b64 exec, 1\n"
        "ds_add_u32 %[syh], %[one] offset:%[ox]+0\ns_mov_b64 exec, -1\nbuffer_load_dwordx4 v[136:139], %[vm1], %[xrs], %[gp2] offen sc1\n.LP3_%=:\ns_waitcnt vmcnt(3)\n"
        "v_max3_u32 %[tm], v144, v145, v146\nv_max_u32 %[tm], %[tm], v147\nv_cmp_eq_u32 vcc, -1, %[tm]\ns_cbranch_vccnz .LR3_%=\n.LK3_%=:\nds_write_b128 %[lm0], v[144:147] offset:10560\n"
        "buffer_load_dwordx4 v[140:143], %[l16], %[xrs], %[gh1] offen sc1\n.LP4_%=:\ns_waitcnt vmcnt(3)\nv_max3_u32 %[tm], v148, v149, v150\nv_max_u32 %[tm], %[tm], v151\nv_cmp_eq_u32 vcc, -1, %[tm]\n"
        "s_cbranch_vccnz .LR4_%=\n.LK4_%=:\nds_write_b128 %[lm0], v[148:151] offset:15840\ns_mov_b64 exec, 1\nds_add_u32 %[sy0], %[one] offset:%[ox]+4\ns_mov_b64 exec, -1\n"
        "buffer_load_dwordx4 v[144:147], %[vm0], %[xrs], %[gp3] offen sc1\n.LP5_%=:\ns_waitcnt vmcnt(3)\nv_max3_u32 %[tm], v132, v133, v134\nv_max_u32 %[tm], %[tm], v135\nv_cmp_eq_u32 vcc, -1, %[tm]\n"
        "s_cbranch_vccnz .LR5_%=\n.LK5_%=:\nds_write_b128 %[lm0], v[132:135] offset:42240\nbuffer_load_dwordx4 v[148:151], %[vm1], %[xrs], %[gp3] offen sc1\n.LP6_%=:\ns_waitcnt vmcnt(3)\n"
        "v_max3_u32 %[tm], v136, v137, v138\nv_max_u32 %[tm], %[tm], v139\nv_cmp_eq_u32 vcc, -1, %[tm]\ns_cbranch_vccnz .LR6_%=\n.LK6_%=:\nds_write_b128 %[lm0], v[136:139] offset:47520\n"
        "s_mov_b64 exec, 1\nds_add_u32 %[sy0], %[one] offset:%[ox]+16\ns_mov_b64 exec, -1\nbuffer_load_dwordx4 v[132:135], %[vm0], %[xrs], %[gp4] offen sc1\n.LP7_%=:\ns_waitcnt vmcnt(3)\n"
        "v_max3_u32 %[tm], v140, v141, v142\nv_max_u32 %[tm], %[tm], v143\nv_cmp_eq_u32 vcc, -1, %[tm]\ns_cbranch_vccnz .LR7_%=\n.LK7_%=:\nds_write_b128 %[lh0], v[140:143] offset:42240\n"
        "s_mov_b64 exec, 1\nds_add_u32 %[syh], %[one] offset:%[ox]+16\ns_mov_b64 exec, -1\nbuffer_load_dwordx4 v[136:139], %[vm1], %[xrs], %[gp4] offen sc1\n.LP8_%=:\ns_waitcnt vmcnt(3)\n"
        "v_max3_u32 %[tm], v144, v145, v146\nv_max_u32 %[tm], %[tm], v147\nv_cmp_eq_u32 vcc, -1, %[tm]\ns_cbranch_vccnz .LR8_%=\n.LK8_%=:\nds_write_b128 %[lm0], v[144:147] offset:52800\n"
        "buffer_load_dwordx4 v[140:143], %[l16], %[xrs], %[gh2] offen sc1\ns_cmp_lt_u32 %[j], 4\ns_cbranch_scc1 .LT%=\ns_mov_b32 %[s1], 0x100000\n.LU%=:\n"
        "ds_read_b128 v[160:163], %[sy0] offset:%[ox]-8\nds_read_b128 v[164:167], %[sy0] offset:%[ox]+8\nds_read_b64_tr_b16 v[152:153], %[xsa]\nds_read_b64_tr_b16 v[154:155], %[xsa] offset:512\n"
        "ds_read_b64_tr_b16 v[156:157], %[xsb]\nds_read_b64_tr_b16 v[158:159], %[xsb] offset:512\ns_waitcnt lgkmcnt(4)\nv_min3_u32 v160, v160, v161, v162\nv_min3_u32 v164, v164, v165, v166\n"
        "v_min3_u32 v160, v160, v163, v164\ns_nop 0\nv_readfirstlane_b32 %[s0], v160\ns_waitcnt lgkmcnt(0)\ns_cmp_ge_i32 %[s0], 6\ns_cbranch_scc1 .LV%=\ns_sleep 1\ns_sub_u32 %[s1], %[s1], 1\n"
        "s_cbranch_scc0 .LU%=\n.LV%=:\nv_mfma_f32_16x16x32_bf16 v[160:163], %[ones], v[152:155], 0\nv_mfma_f32_16x16x32_bf16 v[164:167], v[152:155], v[152:155], 0\n"
        "ds_read_b64_tr_b16 v[152:153], %[xsb] offset:5280\nds_read_b64_tr_b16 v[154:155], %[xsb] offset:5792\ns_bitcmp1_b32 %[ns], 0\n"
        "v_mfma_f32_16x16x32_bf16 v[160:163], %[ones], v[156:159], v[160:163]\nv_mfma_f32_16x16x32_bf16 v[164:167], v[156:159], v[156:159], v[164:167]\ns_waitcnt lgkmcnt(0)\ns_cbranch_scc1 .LW%=\n"
        "v_mfma_f32_16x16x32_bf16 v[160:163], %[ones], v[152:155], v[160:163]\nv_mfma_f32_16x16x32_bf16 v[164:167], v[152:155], v[152:155], v[164:167]\n.LW%=:\ns_nop 7\n"
        "v_cndmask_b32 v164, v164, v165, %[m1]\nv_cndmask_b32 v166, v166, v167, %[m1]\nv_mov_b32 v161, %[tg0]\nv_mov_b32 v163, %[tq1]\nv_cndmask_b32 v162, v164, v166, %[m2]\n"
        "buffer_store_dwordx4 v[160:163], %[xso], %[srs], 0 offen sc1\n.LT%=:\n.LP9_%=:\ns_waitcnt vmcnt(3)\nv_max3_u32 %[tm], v148, v149, v150\nv_max_u32 %[tm], %[tm], v151\n"
        "v_cmp_eq_u32 vcc, -1, %[tm]\ns_cbranch_vccnz .LR9_%=\n.LK9_%=:\nds_write_b128 %[lm0], v[148:151] offset:58080\ns_mov_b64 exec, 1\nds_add_u32 %[sy0], %[one] offset:%[ox]+20\n"
        "s_mov_b64 exec, -1\n.LP10_%=:\ns_waitcnt vmcnt(2)\nv_max3_u32 %[tm], v132, v133, v134\nv_max_u32 %[tm], %[tm], v135\nv_cmp_eq_u32 vcc, -1, %[tm]\ns_cbranch_vccnz .LR10_%=\n.LK10_%=:\n"
        "ds_write_b128 %[lm1], v[132:135] offset:0\n.LP11_%=:\ns_waitcnt vmcnt(1)\nv_max3_u32 %[tm], v136, v137, v138\nv_max_u32 %[tm], %[tm], v139\nv_cmp_eq_u32 vcc, -1, %[tm]\n"
        "s_cbranch_vccnz .LR11_%=\n.LK11_%=:\nds_write_b128 %[lm1], v[136:139] offset:5280\ns_mov_b64 exec, 1\nds_add_u32 %[sy1], %[one] offset:%[ox]+0\ns_mov_b64 exec, -1\n.LP12_%=:\n"
        "s_waitcnt vmcnt(0)\nv_max3_u32 %[tm], v140, v141, v142\nv_max_u32 %[tm], %[tm], v143\nv_cmp_eq_u32 vcc, -1, %[tm]\ns_cbranch_vccnz .LR12_%=\n.LK12_%=:\ns_bitcmp1_b32 %[j], 1\n"
        "s_cbranch_scc1 .LE%=\nds_write_b128 %[lh1], v[140:143] offset:0\ns_mov_b64 exec, 1\nds_add_u32 %[sy1], %[one] offset:%[ox]\ns_mov_b64 exec, -1\ns_branch .LE%=\n.LR0_%=:\n"
        "s_sub_u32 %[cap], %[cap], 1\ns_cbranch_scc0 .LQ0_%=\ns_mov_b32 %[cap], 0\ns_branch .LK0_%=\n.LQ0_%=:\ns_sleep %[xp]\nbuffer_load_dwordx4 v[132:135], %[vm0], %[xrs], %[gp0] offen sc1\n"
        "buffer_load_dwordx4 v[136:139], %[vm1], %[xrs], %[gp0] offen sc1\nbuffer_load_dwordx4 v[140:143], %[l16], %[xrs], %[gh0] offen sc1\n"
        "buffer_load_dwordx4 v[144:147], %[vm0], %[xrs], %[gp1] offen sc1\ns_branch .LP0_%=\n.LR1_%=:\ns_sub_u32 %[cap], %[cap], 1\ns_cbranch_scc0 .LQ1_%=\ns_mov_b32 %[cap], 0\ns_branch .LK1_%=\n"
        ".LQ1_%=:\ns_sleep %[xp]\nbuffer_load_dwordx4 v[136:139], %[vm1], %[xrs], %[gp0] offen sc1\nbuffer_load_dwordx4 v[140:143], %[l16], %[xrs], %[gh0] offen sc1\n"
        "buffer_load_dwordx4 v[144:147], %[vm0], %[xrs], %[gp1] offen sc1\nbuffer_load_dwordx4 v[148:151], %[vm1], %[xrs], %[gp1] offen sc1\ns_branch .LP1_%=\n.LR2_%=:\ns_sub_u32 %[cap], %[cap], 1\n"
        "s_cbranch_scc0 .LQ2_%=\ns_mov_b32 %[cap], 0\ns_branch .LK2_%=\n.LQ2_%=:\ns_sleep %[xp]\nbuffer_load_dwordx4 v[140:143], %[l16], %[xrs], %[gh0] offen sc1\n"
        "buffer_load_dwordx4 v[144:147], %[vm0], %[xrs], %[gp1] offen sc1\nbuffer_load_dwordx4 v[148:151], %[vm1], %[xrs], %[gp1] offen sc1\n"
        "buffer_load_dwordx4 v[132:135], %[vm0], %[xrs], %[gp2] offen sc1\ns_branch .LP2_%=\n.LR3_%=:\ns_sub_u32 %[cap], %[cap], 1\ns_cbranch_scc0 .LQ3_%=\ns_mov_b32 %[cap], 0\ns_branch .LK3_%=\n"
        ".LQ3_%=:\ns_sleep %[xp]\nbuffer_load_dwordx4 v[144:147], %[vm0], %[xrs], %[gp1] offen sc1\nbuffer_load_dwordx4 v[148:151], %[vm1], %[xrs], %[gp1] offen sc1\n"
        "buffer_load_dwordx4 v[132:135], %[vm0], %[xrs], %[gp2] offen sc1\nbuffer_load_dwordx4 v[136:139], %[vm1], %[xrs], %[gp2] offen sc1\ns_branch .LP3_%=\n.LR4_%=:\ns_sub_u32 %[cap], %[cap], 1\n"
        "s_cbranch_scc0 .LQ4_%=\ns_mov_b32 %[cap], 0\ns_branch .LK4_%=\n.LQ4_%=:\ns_sleep %[xp]\nbuffer_load_dwordx4 v[148:151], %[vm1], %[xrs], %[gp1] offen sc1\n"
        "buffer_load_dwordx4 v[132:135], %[vm0], %[xrs], %[gp2] offen sc1\nbuffer_load_dwordx4 v[136:139], %[vm1], %[xrs], %[gp2] offen sc1\n"
        "buffer_load_dwordx4 v[140:143], %[l16], %[xrs], %[gh1] offen sc1\ns_branch .LP4_%=\n.LR5_%=:\ns_sub_u32 %[cap], %[cap], 1\ns_cbranch_scc0 .LQ5_%=\ns_mov_b32 %[cap], 0\ns_branch .LK5_%=\n"
        ".LQ5_%=:\ns_sleep %[xp]\nbuffer_load_dwordx4 v[132:135], %[vm0], %[xrs], %[gp2] offen sc1\nbuffer_load_dwordx4 v[136:139], %[vm1], %[xrs], %[gp2] offen sc1\n"
        "buffer_load_dwordx4 v[140:143], %[l16], %[xrs], %[gh1] offen sc1\nbuffer_load_dwordx4 v[144:147], %[vm0], %[xrs], %[gp3] offen sc1\ns_branch .LP5_%=\n.LR6_%=:\ns_sub_u32 %[cap], %[cap], 1\n"
        "s_cbranch_scc0 .LQ6_%=\ns_mov_b32 %[cap], 0\ns_branch .LK6_%=\n.LQ6_%=:\ns_sleep %[xp]\nbuffer_load_dwordx4 v[136:139], %[vm1], %[xrs], %[gp2] offen sc1\n"
        "buffer_load_dwordx4 v[140:143], %[l16], %[xrs], %[gh1] offen sc1\nbuffer_load_dwordx4 v[144:147], %[vm0], %[xrs], %[gp3] offen sc1\n"
        "buffer_load_dwordx4 v[148:151], %[vm1], %[xrs], %[gp3] offen sc1\ns_branch .LP6_%=\n.LR7_%=:\ns_sub_u32 %[cap], %[cap], 1\ns_cbranch_scc0 .LQ7_%=\ns_mov_b32 %[cap], 0\ns_branch .LK7_%=\n"
        ".LQ7_%=:\ns_sleep %[xp]\nbuffer_load_dwordx4 v[140:143], %[l16], %[xrs], %[gh1] offen sc1\nbuffer_load_dwordx4 v[144:147], %[vm0], %[xrs], %[gp3] offen sc1\n"
        "buffer_load_dwordx4 v[148:151], %[vm1], %[xrs], %[gp3] offen sc1\nbuffer_load_dwordx4 v[132:135], %[vm0], %[xrs], %[gp4] offen sc1\ns_branch .LP7_%=\n.LR8_%=:\ns_sub_u32 %[cap], %[cap], 1\n"
        "s_cbranch_scc0 .LQ8_%=\ns_mov_b32 %[cap], 0\ns_branch .LK8_%=\n.LQ8_%=:\ns_sleep %[xp]\nbuffer_load_dwordx4 v[144:147], %[vm0], %[xrs], %[gp3] offen sc1\n"
        "buffer_load_dwordx4 v[148:151], %[vm1], %[xrs], %[gp3] offen sc1\nbuffer_load_dwordx4 v[132:135], %[vm0], %[xrs], %[gp4] offen sc1\n"
        "buffer_load_dwordx4 v[136:139], %[vm1], %[xrs], %[gp4] offen sc1\ns_branch .LP8_%=\n.LR9_%=:\ns_sub_u32 %[cap], %[cap], 1\ns_cbranch_scc0 .LQ9_%=\ns_mov_b32 %[cap], 0\ns_branch .LK9_%=\n"
        ".LQ9_%=:\ns_sleep %[xp]\nbuffer_load_dwordx4 v[148:151], %[vm1], %[xrs], %[gp3] offen sc1\nbuffer_load_dwordx4 v[132:135], %[vm0], %[xrs], %[gp4] offen sc1\n"
        "buffer_load_dwordx4 v[136:139], %[vm1], %[xrs], %[gp4] offen sc1\nbuffer_load_dwordx4 v[140:143], %[l16], %[xrs], %[gh2] offen sc1\ns_branch .LP9_%=\n.LR10_%=:\ns_sub_u32 %[cap], %[cap], 1\n"
        "s_cbranch_scc0 .LQ10_%=\ns_mov_b32 %[cap], 0\ns_branch .LK10_%=\n.LQ10_%=:\ns_sleep %[xp]\nbuffer_load_dwordx4 v[132:135], %[vm0], %[xrs], %[gp4] offen sc1\n"
        "buffer_load_dwordx4 v[136:139], %[vm1], %[xrs], %[gp4] offen sc1\nbuffer_load_dwordx4 v[140:143], %[l16], %[xrs], %[gh2] offen sc1\ns_branch .LP10_%=\n.LR11_%=:\ns_sub_u32 %[cap], %[cap], 1\n"
        "s_cbranch_scc0 .LQ11_%=\ns_mov_b32 %[cap], 0\ns_branch .LK11_%=\n.LQ11_%=:\ns_sleep %[xp]\nbuffer_load_dwordx4 v[136:139], %[vm1], %[xrs], %[gp4] offen sc1\n"
        "buffer_load_dwordx4 v[140:143], %[l16], %[xrs], %[gh2] offen sc1\ns_branch .LP11_%=\n.LR12_%=:\ns_sub_u32 %[cap], %[cap], 1\ns_cbranch_scc0 .LQ12_%=\ns_mov_b32 %[cap], 0\ns_branch .LK12_%=\n"
        ".LQ12_%=:\ns_sleep %[xp]\nbuffer_load_dwordx4 v[140:143], %[l16], %[xrs], %[gh2] offen sc1\ns_branch .LP12_%=\n.LE%=:\n"
        : [tm] "=&v"(tm), [cap] "=&s"(cap), [s0] "=&s"(s0_), [s1] "=&s"(s1_)
        : [l16] "v"(l16), [vm0] "v"(vm0), [vm1] "v"(vm1), [xrs] "s"(xrs), [gp0] "s"(gp[0]), [gp1] "s"(gp[1]), [gp2] "s"(gp[2]), [gp3] "s"(gp[3]), [gp4] "s"(gp[4]), [gh0] "s"(gh[0]), [gh1] "s"(gh[1]), [gh2] "s"(gh[2]), [lm0] "v"(lm0), [lm1] "v"(lm1), [lh0] "v"(lh0), [lh1] "v"(lh1), [sy0] "v"(sy0), [sy1] "v"(sy1), [syh] "v"(syh), [xsb] "v"(xsb), [one] "v"(one), [j] "s"(j), [xsa] "v"(xsa), [ns] "s"(ns), [ones] "v"(onesf), [m1] "s"(m1), [m2] "s"(m2), [xso] "v"(xso), [srs] "s"(xsrs), [tg0] "s"(tg0), [tq1] "s"(tq1), [ox] "i"(SY_X), [xp] "i"(XPOLL)
        : "memory", "scc", "vcc", "v132", "v133", "v134", "v135", "v136", "v137", "v138", "v139", "v140", "v141", "v142", "v143", "v144", "v145", "v146", "v147", "v148", "v149", "v150", "v151", "v152", "v153", "v154", "v155", "v156", "v157", "v158", "v159", "v160", "v161", "v162", "v163", "v164", "v165", "v166", "v167");
    RETRY(8, 0x10000 - cap);
    ST(3);
  } else {
    ST(5);
#define MARKS \
    if (w == MKW) { \
      const __amdgpu_buffer_rsrc_t mrs = __builtin_amdgcn_make_buffer_rsrc(obuf, 0, HO_BYTES, 0x00020000); \
      const __amdgpu_buffer_rsrc_t irs = __builtin_amdgcn_make_buffer_rsrc(ws + WS_IM1 + b * 12800, 0, 12800, 0x00020000); \
      __builtin_amdgcn_raw_buffer_store_b128(MARK16, mrs, lane < 16 ? (lane >> 3) * 4096 + (lane & 7) * 16 : -1, HO_FS + hg * 8192 + s0 * 2, 17); \
      _Pragma("unroll 1") \
      for (int i = 0; i < 10; ++i) { \
        __builtin_amdgcn_raw_buffer_store_b128(MARK16, mrs, (lane >> 3) * (XP * 2) + (lane & 7) * 16, HO_X1 + (hg * 80 + 8 * i) * (XP * 2) + s0 * 2, 17); \
        __builtin_amdgcn_s_sleep(MSL1); \
      } \
      _Pragma("unroll 1") \
      for (int i = 0; i < 13; ++i) { \
        __builtin_amdgcn_raw_buffer_store_b128(MARK16, irs, lane * 16, i * 1024, 17); \
        __builtin_amdgcn_s_sleep(MSL2); \
      } \
      VMCNT(0); \
      if (lane == 0) __builtin_amdgcn_raw_buffer_store_b64((u32x2){tg0, tg1}, frs, 0, FL_M2 + b * 8, 16); \
      ST(1); \
    } \

    if (MKPOS == 0) MARKS
    lds_poll<SY_X, 2>(sya, 3);
    if (j == 0 ? MKPOS : j == 4 ? !MKPOS : j < 4) {
      const u32 xa = LDSA(At) + (2 * mt2 + 2 + (l15 >> 3)) * 1024 + (l15 & 7) * 128 + ((g ^ (l15 & 6)) << 4);
      bf16x8 fv[3];
      asm volatile("ds_read_b128 %0, %3 offset:61440\n\tds_read_b128 %1, %4\n\tds_read_b128 %2, %5\n\ts_waitcnt lgkmcnt(0)" : "=&v"(fv[0]), "=&v"(fv[1]), "=&v"(fv[2])
                   : "v"(xa ^ (64 * (hg & 1))), "v"((xa ^ (64 * (xs0 & 1))) + (xs0 >> 1) * 10240), "v"((xa ^ (64 * (~xs0 & 1))) + ((xs0 + 1) >> 1) * 10240) : "memory");
      f32x4 s1 = {0.f, 0.f, 0.f, 0.f}, s2 = s1;
#pragma unroll
      for (int q = 0; q < 3; ++q) {
        if (q < 2 || !(hg & 1)) {
          s1 = __builtin_amdgcn_mfma_f32_16x16x32_bf16(onesf, fv[q], s1, 0, 0, 0);
          s2 = __builtin_amdgcn_mfma_f32_16x16x32_bf16(fv[q], fv[q], s2, 0, 0, 0);
        }
      }
      const float d2 = (l15 & 2) ? ((l15 & 1) ? s2[3] : s2[2]) : ((l15 & 1) ? s2[1] : s2[0]);
      __builtin_amdgcn_raw_buffer_store_b128((u32x4){__float_as_uint(s1[0]), tg0, __float_as_uint(d2), tq1}, xsrs, xso, 0, 16);
    }
  }
  __builtin_amdgcn_s_setprio(PTW);
  if (!layer && MKPOS == 1) MARKS
  u32x4 wv[2][7];
  if (layer) {
    lds_inc<SY_PART>(sya, one);
    asm volatile("ds_read_b128 %0, %5\n\tds_read_b128 %1, %5 offset:8192\n\tds_read_b128 %2, %5 offset:16384\n\tds_read_b128 %3, %5 offset:24576\n\tds_read_b128 %4, %5 offset:32768\n\ts_waitcnt lgkmcnt(0)"
                 : "=&v"(wv[0][0]), "=&v"(wv[0][1]), "=&v"(wv[0][2]), "=&v"(wv[0][3]), "=&v"(wv[0][4]) : "v"(LDSA(St) + j * 1024 + l16) : "memory");
    lds_inc<SY_STAT>(sya, one);
  }
  const __amdgpu_buffer_rsrc_t orsrc = __builtin_amdgcn_make_buffer_rsrc(obuf + (size_t)rb * 40960, 0, 81920, 0x00020000);
  u32 qka = LDSA(St) + ((16 * qt + l15) * X1_P + 160 * grp + 8 * g) * 2;
  u32 vta = LDSA(St) + ((16 * qt + 4 * g + ((lane >> 2) & 3)) * X1_P + 160 * grp + 40 + 4 * (lane & 3)) * 2;
  const bool par = l15 & 1;
  const int kk0 = 4 * g + (l15 & 1), kk1 = kk0 + 2;
  const bool c0 = kk0 >= l15 + 2, c1 = kk1 >= l15 + 2;
  const u32 e0 = par ? 0x01000c0cu : 0x0c0c0100u, e1 = par ? 0x03020c0cu : 0x0c0c0302u;
  u32 sA0 = c0 ? e0 : 0x0c0c0c0cu, sB0 = c0 ? 0x0c0c0c0cu : e0;
  u32 sA1 = c1 ? e1 : 0x0c0c0c0cu, sB1 = c1 ? 0x0c0c0c0cu : e1;
  u32 oha = g * 10240 + l15 * 16, qa[4];
#pragma unroll
  for (int i = 0; i < 4; ++i) { qa[i] = g >= 3 - (i & 1) ? sya + SY_Z : qka + (32 * X1_P + 160 - 240 * grp) + 40 * (i >> 1) + 8 * (i & 1); PIN(qa[i]); }
  const float nb = s0 + qt ? 0.f : -1e30f;
  f32x4 bias = {nb, nb, nb, nb};
  int ohs = g ? -1 : l16;
  u64 c0m = __builtin_amdgcn_ballot_w64(c0), c1m = __builtin_amdgcn_ballot_w64(c1);
  int so0 = (hg * 4 + qt) * 2560 + grp * 1024, so1 = (hg * 4 + qt) * 2560 + 2048 + grp * 256;
  float nc = -0.22360679774997896f * 1.4426950408889634f;
  asm volatile("" : "+s"(c0m), "+s"(c1m), "+s"(so0), "+s"(so1), "+s"(nc));
  PIN(qka); PIN(vta); PIN(sA0); PIN(sA1); PIN(sB0); PIN(sB1); PIN(l16); PIN(oha); PIN(sya); PIN(one); PIN(bias); PIN(ohs);
  u32 rw = LDSA(smem) + A_R1 + j * 3072 + l16, fw = sya + SY_RED + 4 * j, f0 = 1 - kh;
  u32 xo_ = LDSA(smem + X1T_OFF) + (32 * kh + l15) * 144 + 32 * mt2 + 8 * g;
  u32 xi_ = LDSA(smem + X1T_OFF) + (tt >> 3) * 144 + (tt & 7) * 16;
  const __amdgpu_buffer_rsrc_t x1rs = __builtin_amdgcn_make_buffer_rsrc(xtout + (size_t)hg * 80 * XP + s0, 0, 80 * XP * 2, 0x00020000);
  int xg_ = (tt >> 3) * (XP * 2) + (tt & 7) * 16;
  const __amdgpu_buffer_rsrc_t srsrc = __builtin_amdgcn_make_buffer_rsrc(stats + (size_t)s0 * 16, 0, 8192, 0x00020000);
  u32 rr = LDSA(smem) + A_R1 + (j ^ 4) * 3072 + l16 - 1024 * kh;
  int so_ = (l15 & 3) ? -1 : (lane >> 2) * 8;
  u32 bb = LDSA(At) + kh * 25600 + l16, fr = fw ^ 16, wd = LDSA(At) + j * 1024 + l16, ra[3];
  {
    const int rr_ = 4 * (g & 1) + ((lane >> 2) & 3), pr_ = 4 * (l15 >> 3) + (l15 & 3) + 4 * (l15 & 4);
    const u32 ro = LDSA(At) + (layer ? (mt2 + 1) * 1056 + pr_ * 32 + 8 * g : (2 * mt2 + 2 + (g >> 1)) * 1024 + rr_ * 128 + ((rr_ & 6) << 4) + (lane & 3) * 8);
#pragma unroll
    for (int i = 0; i < 3; ++i) {
      const int kc = hg * 80 + 16 * (2 * kh + i), kr = 64 * PAIR(kc >> 6, -rot) + (kc & 63);
      ra[i] = layer ? ro + (kr >> 5) * 5280 + (kr & 16) * 16 : (ro ^ ((kr & 48) * 2)) + (kr >> 6) * 10240;
      if (i == 0 && kh) ra[i] = sya + SY_Z;
      PIN(ra[i]);
    }
  }
  PIN(rw); PIN(fw); PIN(f0); PIN(xo_); PIN(xi_); PIN(xg_); PIN(rr); PIN(so_); PIN(bb); PIN(fr); PIN(wd);
  STX(8);
  STX(10);
    if (!layer) {
    if (MKPOS == 2) MARKS
      if (w == CWW) {
        const __amdgpu_buffer_rsrc_t crs = __builtin_amdgcn_make_buffer_rsrc(ws + WS_CW + hg * 23040, 0, 23040, 0x00020000);
        const int cp = min(lane, 59) / 20, cc = min(lane, 59) - 20 * cp;
        pg_poll<4>(sya, LDSA(smem) + SY_OFF + SY_X + CWAT);
        __builtin_amdgcn_s_setprio(PCW);
        f32x4 cv[20];
#pragma unroll
        for (int i = 0; i < 20; ++i) cv[i] = __builtin_bit_cast(f32x4, __builtin_amdgcn_raw_buffer_load_b128(crs, cp * 384 + cc * 16, i * 1152, 16));
#pragma unroll
        for (int i = 0; i < 10; ++i) cv[i] += cv[i + 10];
        const f32x4 cs = ((cv[0] + cv[1]) + (cv[2] + cv[3])) + ((cv[4] + cv[5]) + (cv[6] + cv[7])) + (cv[8] + cv[9]);
        asm volatile("ds_write_b128 %0, %1" :: "v"(LDSA(smem) + CW_OFF + (cc + (cp ? 10 * cp - 10 + (cc >= 10 ? 30 : 0) : 20)) * 16), "v"(cs) : "memory");
        lds_inc<SY_STAT>(sya, one);
        __builtin_amdgcn_s_setprio(PTW);
        ST(1);
      }
      if (w == MPW) {
        const int fb = lane < 8 ? (b & 62) + (lane & 1) + 64 * (lane >> 1) : 256 + 16 * ((lane >> 1) & 7) + 2 * hg + (lane & 1);
        pg_poll<8>(sya, LDSA(smem) + SY_OFF + SY_X + MKAT);
        int spins = 0;
        _Pragma("unroll 1")
        while (true) {
          asm volatile("" ::: "memory");
          const u32x2 f = __builtin_amdgcn_raw_buffer_load_b64(frs, lane < 24 ? fb * 64 : b * 64, FL_M, 16);
          if (__builtin_amdgcn_ballot_w64(f[0] != tg0 || f[1] != tg1) == 0 || ++spins > GCAP) break;
          __builtin_amdgcn_s_sleep(8);
        }
        RETRY(15, spins);
        lds_inc<SY_STAT>(sya, one);
        lds_inc<SY_MK>(sya, one);
        ST(1);
      }
    }
    if (j == XJ0 || j == (layer ? XJ1 : XJ2)) {
      const int xo = j == XJ0 ? rb * 8192 + l16 : (rb ? rb - 1 : 0) * 8192 + 768 + l15 * 16;
      pg_poll<4>(sya, LDSA(smem) + SY_OFF + 256 * layer + SY_X + (layer ? XSAT : XSAT0));
      __builtin_amdgcn_s_sleep(layer ? XSSL : 0);
      __builtin_amdgcn_s_setprio(3);
      u32x4 pv[8];
      int spins = 0;
      _Pragma("unroll 1")
      while (true) {
        asm volatile("" ::: "memory");
        u32 m = 0;
#pragma unroll
        for (int i = 0; i < 8; ++i) pv[i] = __builtin_amdgcn_raw_buffer_load_b128(xsrs, xo, i * 1024, 16);
#pragma unroll
        for (int i = 0; i < 8; ++i) m |= (pv[i][1] ^ tg0) | (pv[i][3] ^ tq1);
        if (__builtin_amdgcn_ballot_w64(m != 0) == 0 || ++spins > GCAP) break;
        __builtin_amdgcn_s_sleep(XSRT);
      }
      RETRY(15, spins);
      float sv[2];
#pragma unroll
      for (int i = 0; i < 2; ++i)
        sv[i] = ((__uint_as_float(pv[0][2 * i]) + __uint_as_float(pv[1][2 * i])) + (__uint_as_float(pv[2][2 * i]) + __uint_as_float(pv[3][2 * i]))) +
                ((__uint_as_float(pv[4][2 * i]) + __uint_as_float(pv[5][2 * i])) + (__uint_as_float(pv[6][2 * i]) + __uint_as_float(pv[7][2 * i])));
      asm volatile("ds_write_b128 %0, %1" :: "v"(LDSA(smem) + A_R2 + (j == XJ0 ? 256 + l16 : l15 * 16)), "v"(ln_fin(sv[0], sv[1])) : "memory");
      lds_inc<SY_STAT>(sya, one);
      __builtin_amdgcn_s_setprio(PTW);
      ST(1);
    }
  u32x2 rv[3];
  if (layer) {
    lds_poll<SY_PART, 2>(sya, 8);
    asm volatile("ds_read_b64 %0, %3\n\tds_read_b64 %1, %4\n\tds_read_b64 %2, %5\n\ts_waitcnt lgkmcnt(0)" : "=&v"(rv[0]), "=&v"(rv[1]), "=&v"(rv[2]) : "v"(ra[0]), "v"(ra[1]), "v"(ra[2]) : "memory");
  } else
    asm volatile("ds_read_b64_tr_b16 %0, %3\n\tds_read_b64_tr_b16 %1, %4\n\tds_read_b64_tr_b16 %2, %5\n\ts_waitcnt lgkmcnt(0)" : "=&v"(rv[0]), "=&v"(rv[1]), "=&v"(rv[2]) : "v"(ra[0]), "v"(ra[1]), "v"(ra[2]) : "memory");
  lds_inc<SY_RV>(sya, one);
  STX(9);
  {
    const __amdgpu_buffer_rsrc_t wos = __builtin_amdgcn_make_buffer_rsrc((void*)(wot + (size_t)hg * 51200 + j * 512), 0, 102400 - j * 1024, 0x00020000);
    if (!layer) lds_poll<SY_MK, 2>(sya, 1);
    pg_poll<2>(sya, LDSA(smem) + SY_OFF + 256 * layer + SY_X + (layer ? WOAT : WOAT0));
    __builtin_amdgcn_s_setprio(PTO);
    STX(11);
#pragma unroll
    for (int p = 0; p < 2; ++p)
#pragma unroll
      for (int i = 0; i < 7; ++i) if (!layer || p || i > 4 || (i == 4 && j >= 6)) wv[p][i] = __builtin_amdgcn_raw_buffer_load_b128(wos, l16, (i == 6 && j >= 2) ? 102400 : p * 51200 + i * 8192, 16);
    lds_poll<SY_RV, 1>(sya, 8);
#pragma unroll
    for (int i = 0; i < 7; ++i) if (i < 6 || j < 2) asm volatile("ds_write_b128 %0, %1 offset:%2" :: "v"(wd), "v"(wv[0][i]), "i"(i * 8192) : "memory");
    lds_inc<SY_WO>(sya, one);
    wd += WO1_OFF;
    PIN(wd);
  }
  if (grp) { pg_poll<2>(sya, LDSA(smem) + SY_OFF + 256 * layer + SY_X + 44); __builtin_amdgcn_s_setprio(PTA1); } else __builtin_amdgcn_s_setprio(PTA);
  STX(12);
  pg_poll<APS>(sya, LDSA(smem) + SY_OFF + 256 * layer + SY_X + 44 + 4 * grp);
  if (layer) ST(5);
      asm volatile(
        "ds_read_b64 v[30:31], %[q0]\nds_read_b64 v[32:33], %[q1]\nds_read_b64 v[38:39], %[ka] offset:0\nds_read_b64 v[40:41], %[ka] offset:8\nds_read_b64 v[42:43], %[ka] offset:7936\n"
        "ds_read_b64 v[44:45], %[ka] offset:7944\nds_read_b64 v[34:35], %[q2]\nds_read_b64 v[36:37], %[q3]\nds_read_b64 v[46:47], %[ka] offset:40\nds_read_b64 v[48:49], %[ka] offset:48\n"
        "ds_read_b64 v[50:51], %[ka] offset:7976\nds_read_b64 v[52:53], %[ka] offset:7984\nds_read_b64_tr_b16 v[54:55], %[va] offset:0\nds_read_b64_tr_b16 v[56:57], %[va] offset:7936\n"
        "ds_read_b64_tr_b16 v[58:59], %[va] offset:32\nds_read_b64_tr_b16 v[60:61], %[va] offset:7968\nds_read_b64_tr_b16 v[62:63], %[va] offset:40\nds_read_b64_tr_b16 v[64:65], %[va] offset:7976\n"
        "ds_read_b64_tr_b16 v[66:67], %[va] offset:72\nds_read_b64_tr_b16 v[68:69], %[va] offset:8008\ns_waitcnt lgkmcnt(14)\nv_mfma_f32_16x16x32_bf16 v[70:73], v[38:41], v[30:33], %[bias]\n"
        "v_mfma_f32_16x16x32_bf16 v[74:77], v[42:45], v[30:33], 0\ns_waitcnt lgkmcnt(8)\nv_mfma_f32_16x16x32_bf16 v[78:81], v[46:49], v[34:37], %[bias]\n"
        "v_mfma_f32_16x16x32_bf16 v[82:85], v[50:53], v[34:37], 0\ns_nop 4\nv_cndmask_b32 v70, v70, v71, %[par]\nv_cndmask_b32 v72, v72, v73, %[par]\nv_cndmask_b32 v74, v74, v75, %[par]\n"
        "v_cndmask_b32 v76, v76, v77, %[par]\nv_cndmask_b32 v30, v74, v70, %[c0]\nv_cndmask_b32 v31, v76, v72, %[c1]\nv_max_f32 v34, v30, v31\nv_cndmask_b32 v78, v78, v79, %[par]\n"
        "v_cndmask_b32 v80, v80, v81, %[par]\nv_cndmask_b32 v82, v82, v83, %[par]\nv_cndmask_b32 v84, v84, v85, %[par]\nv_cndmask_b32 v32, v82, v78, %[c0]\nv_cndmask_b32 v33, v84, v80, %[c1]\n"
        "v_max_f32 v35, v32, v33\ns_nop 1\nv_permlane16_swap_b32 v34, v35\nv_max_f32 v34, v34, v35\nv_mov_b32 v35, v34\ns_nop 1\nv_permlane32_swap_b32 v34, v35\nv_max_f32 v34, v34, v35\n"
        "v_mul_f32 v35, %[nc], v34\nv_mul_f32 v34, %[nc], v34\ns_nop 1\nv_permlane16_swap_b32 v34, v35\nv_fma_f32 v30, -v30, %[nc], v34\nv_fma_f32 v31, -v31, %[nc], v34\n"
        "v_fma_f32 v32, -v32, %[nc], v35\nv_fma_f32 v33, -v33, %[nc], v35\nv_exp_f32 v30, v30\nv_exp_f32 v31, v31\nv_exp_f32 v32, v32\nv_exp_f32 v33, v33\nv_add_f32 v34, v30, v31\n"
        "v_add_f32 v35, v32, v33\ns_nop 1\nv_permlane16_swap_b32 v34, v35\nv_add_f32 v34, v34, v35\nv_mov_b32 v35, v34\ns_nop 1\nv_permlane32_swap_b32 v34, v35\nv_add_f32 v34, v34, v35\n"
        "v_rcp_f32 v34, v34\ns_nop 0\nv_mov_b32 v35, v34\ns_nop 1\nv_permlane16_swap_b32 v34, v35\nv_mul_f32 v30, v30, v34\nv_mul_f32 v31, v31, v34\nv_mul_f32 v32, v32, v35\nv_mul_f32 v33, v33, v35\n"
        "v_cvt_pk_bf16_f32 v30, v30, v31\nv_cvt_pk_bf16_f32 v32, v32, v33\nv_perm_b32 v38, v30, v30, %[sa0]\nv_perm_b32 v39, v30, v30, %[sa1]\nv_perm_b32 v40, v30, v30, %[sb0]\n"
        "v_perm_b32 v41, v30, v30, %[sb1]\nv_perm_b32 v42, v32, v32, %[sa0]\nv_perm_b32 v43, v32, v32, %[sa1]\nv_perm_b32 v44, v32, v32, %[sb0]\nv_perm_b32 v45, v32, v32, %[sb1]\ns_waitcnt lgkmcnt(0)\n"
        "v_mfma_f32_16x16x32_bf16 v[70:73], v[54:57], v[38:41], 0\nv_mfma_f32_16x16x32_bf16 v[78:81], v[62:65], v[42:45], 0\nv_mfma_f32_16x16x32_bf16 v[74:77], v[58:61], v[38:41], 0\n"
        "v_mfma_f32_16x16x32_bf16 v[82:85], v[66:69], v[42:45], 0\ns_nop 4\nv_cvt_pk_bf16_f32 v46, v70, v71\nv_cvt_pk_bf16_f32 v47, v72, v73\nv_cvt_pk_bf16_f32 v48, v78, v79\n"
        "v_cvt_pk_bf16_f32 v49, v80, v81\nv_min_u32 v46, -2, v46\nv_min_u32 v47, -2, v47\nv_min_u32 v48, -2, v48\nv_min_u32 v49, -2, v49\n"
        "buffer_store_dwordx4 v[46:49], %[l16], %[ors], %[so0] offen sc1\nv_cvt_pk_bf16_f32 v50, v74, v75\nv_cvt_pk_bf16_f32 v51, v76, v77\nv_cvt_pk_bf16_f32 v52, v82, v83\n"
        "v_cvt_pk_bf16_f32 v53, v84, v85\nv_min_u32 v50, -2, v50\nv_min_u32 v51, -2, v51\nv_min_u32 v52, -2, v52\nv_min_u32 v53, -2, v53\n"
        "buffer_store_dwordx4 v[50:53], %[oh], %[ors], %[so1] offen sc1\ns_nop 1\n"
        :
        : [q0] "v"(qa[0]), [q1] "v"(qa[1]), [q2] "v"(qa[2]), [q3] "v"(qa[3]), [ka] "v"(qka), [va] "v"(vta), [bias] "v"(bias), [par] "s"(0xaaaaaaaaaaaaaaaaull), [c0] "s"(c0m), [c1] "s"(c1m), [nc] "s"(nc), [sa0] "v"(sA0), [sa1] "v"(sA1), [sb0] "v"(sB0), [sb1] "v"(sB1), [l16] "v"(l16), [oh] "v"(ohs), [ors] "s"(orsrc), [so0] "s"(so0), [so1] "s"(so1)
        : "memory", "scc", "v30", "v31", "v32", "v33", "v34", "v35", "v36", "v37", "v38", "v39", "v40", "v41", "v42", "v43", "v44", "v45", "v46", "v47", "v48", "v49", "v50", "v51", "v52", "v53", "v54", "v55", "v56", "v57", "v58", "v59", "v60", "v61", "v62", "v63", "v64", "v65", "v66", "v67", "v68", "v69", "v70", "v71", "v72", "v73", "v74", "v75", "v76", "v77", "v78", "v79", "v80", "v81", "v82", "v83", "v84", "v85");
  ST(6);
  {
#pragma unroll
    for (int i = 0; i < 7; ++i) if (i < 6 || j < 2) asm volatile("ds_write_b128 %0, %1 offset:%2" :: "v"(wd), "v"(wv[1][i]), "i"(i * 8192) : "memory");
    lds_inc<SY_WO + 4>(sya, one);
  }
  {
    const int ob = (16 * kh + mt2) * 2560;
    u32 tm;
    int cap, s0_, s1_, rt = 0;
      asm volatile(
        "s_mov_b32 %[cap], 0x10000\ns_cmp_eq_u32 %[kh], 0\ns_cbranch_scc1 .LS0_%=\ns_sleep %[sl1]\nbuffer_load_dwordx4 v[28:31], %[l16], %[ors], %[o0] offen offset:0 sc1\n"
        "buffer_load_dwordx4 v[32:35], %[l16], %[ors], %[o1] offen offset:0 sc1\nbuffer_load_dwordx4 v[36:39], %[l16], %[ors], %[o2] offen offset:0 sc1\n"
        "buffer_load_dwordx4 v[40:43], %[l16], %[ors], %[o3] offen offset:0 sc1\nbuffer_load_dwordx4 v[44:47], %[oha], %[ors], %[o4] offen offset:0 sc1\n.LS0_%=:\ns_mov_b32 %[s1], 0x100000\n.LWa%=:\n"
        "ds_read_b32 %[tm], %[sy] offset:%[ow1]-4\ns_waitcnt lgkmcnt(0)\nv_readfirstlane_b32 %[s0], %[tm]\ns_nop 0\ns_cmp_ge_i32 %[s0], %[nwo]\ns_cbranch_scc1 .LXa%=\ns_sleep 1\n"
        "s_sub_u32 %[s1], %[s1], 1\ns_cbranch_scc0 .LWa%=\n.LXa%=:\nds_read_b128 v[68:71], %[bb] offset:0\nds_read_b128 v[72:75], %[bb] offset:1024\nds_read_b128 v[76:79], %[bb] offset:2048\n"
        "ds_read_b128 v[80:83], %[bb] offset:3072\nds_read_b128 v[84:87], %[bb] offset:4096\nds_read_b128 v[88:91], %[bb] offset:5120\nds_read_b128 v[92:95], %[bb] offset:6144\n"
        "ds_read_b128 v[96:99], %[bb] offset:7168\nds_read_b128 v[100:103], %[bb] offset:8192\nds_read_b128 v[104:107], %[bb] offset:9216\nds_read_b128 v[108:111], %[bb] offset:10240\n"
        "ds_read_b128 v[112:115], %[bb] offset:11264\nds_read_b128 v[116:119], %[bb] offset:12288\nds_read_b128 v[120:123], %[bb] offset:13312\nds_read_b128 v[124:127], %[bb] offset:14336\n"
        "ds_read_b128 v[128:131], %[bb] offset:15360\nds_read_b128 v[132:135], %[bb] offset:16384\nds_read_b128 v[136:139], %[bb] offset:17408\nds_read_b128 v[140:143], %[bb] offset:18432\n"
        "ds_read_b128 v[144:147], %[bb] offset:19456\nds_read_b128 v[148:151], %[bb] offset:20480\nds_read_b128 v[152:155], %[bb] offset:21504\nds_read_b128 v[156:159], %[bb] offset:22528\n"
        "ds_read_b128 v[160:163], %[bb] offset:23552\nds_read_b128 v[164:167], %[bb] offset:24576\ns_mov_b64 exec, 1\nds_add_u32 %[sy], %[one] offset:%[ofr]\ns_mov_b64 exec, -1\n"
        "v_add_u32 %[bb], %[wo1], %[bb]\nv_lshlrev_b32 v48, 16, v22\nv_and_b32 v49, 0xffff0000, v22\nv_lshlrev_b32 v50, 16, v23\nv_and_b32 v51, 0xffff0000, v23\nv_lshlrev_b32 v52, 16, v24\n"
        "v_and_b32 v53, 0xffff0000, v24\nv_lshlrev_b32 v54, 16, v25\nv_and_b32 v55, 0xffff0000, v25\nv_lshlrev_b32 v56, 16, v26\nv_and_b32 v57, 0xffff0000, v26\nv_lshlrev_b32 v58, 16, v27\n"
        "v_and_b32 v59, 0xffff0000, v27\ns_cmp_eq_u32 %[kh], 0\ns_cbranch_scc0 .LS1_%=\ns_sleep %[sl0]\nbuffer_load_dwordx4 v[28:31], %[l16], %[ors], %[o0] offen offset:0 sc1\n"
        "buffer_load_dwordx4 v[32:35], %[l16], %[ors], %[o1] offen offset:0 sc1\nbuffer_load_dwordx4 v[36:39], %[l16], %[ors], %[o2] offen offset:0 sc1\n"
        "buffer_load_dwordx4 v[40:43], %[l16], %[ors], %[o3] offen offset:0 sc1\nbuffer_load_dwordx4 v[44:47], %[oha], %[ors], %[o4] offen offset:0 sc1\n.LS1_%=:\ns_mov_b32 %[s1], 0x100000\n.LWb%=:\n"
        "ds_read_b32 %[tm], %[sy] offset:%[ow1]\ns_waitcnt lgkmcnt(0)\nv_readfirstlane_b32 %[s0], %[tm]\ns_nop 0\ns_cmp_ge_i32 %[s0], %[nwo]\ns_cbranch_scc1 .LXb%=\ns_sleep 1\n"
        "s_sub_u32 %[s1], %[s1], 1\ns_cbranch_scc0 .LWb%=\n.LXb%=:\n.LC0_%=:\ns_waitcnt vmcnt(4)\nv_max_u32 %[tm], v28, v29\nv_max3_u32 %[tm], %[tm], v30, v31\ns_waitcnt vmcnt(3)\n"
        "v_max3_u32 %[tm], %[tm], v32, v33\nv_max3_u32 %[tm], %[tm], v34, v35\ns_waitcnt vmcnt(2)\nv_max3_u32 %[tm], %[tm], v36, v37\nv_max3_u32 %[tm], %[tm], v38, v39\ns_waitcnt vmcnt(1)\n"
        "v_max3_u32 %[tm], %[tm], v40, v41\nv_max3_u32 %[tm], %[tm], v42, v43\ns_waitcnt vmcnt(0)\nv_max3_u32 %[tm], %[tm], v44, v45\nv_max3_u32 %[tm], %[tm], v46, v47\nv_cmp_eq_u32 vcc, -1, %[tm]\n"
        "s_cbranch_vccnz .LR0_%=\n.LK0_%=:\nv_mfma_f32_16x16x32_bf16 v[68:71], v[28:31], v[68:71], 0\nv_mfma_f32_16x16x32_bf16 v[72:75], v[28:31], v[72:75], 0\n"
        "v_mfma_f32_16x16x32_bf16 v[76:79], v[28:31], v[76:79], v[48:51]\nv_mfma_f32_16x16x32_bf16 v[80:83], v[28:31], v[80:83], v[52:55]\n"
        "v_mfma_f32_16x16x32_bf16 v[84:87], v[28:31], v[84:87], v[56:59]\nbuffer_load_dwordx4 v[48:51], %[l16], %[ors], %[o0] offen offset:1024 sc1\n"
        "buffer_load_dwordx4 v[52:55], %[l16], %[ors], %[o1] offen offset:1024 sc1\nbuffer_load_dwordx4 v[56:59], %[l16], %[ors], %[o2] offen offset:1024 sc1\n"
        "buffer_load_dwordx4 v[60:63], %[l16], %[ors], %[o3] offen offset:1024 sc1\nbuffer_load_dwordx4 v[64:67], %[oha], %[ors], %[o4] offen offset:256 sc1\n"
        "v_mfma_f32_16x16x32_bf16 v[68:71], v[32:35], v[88:91], v[68:71]\nds_read_b128 v[88:91], %[bb] offset:0\nv_mfma_f32_16x16x32_bf16 v[72:75], v[32:35], v[92:95], v[72:75]\n"
        "ds_read_b128 v[92:95], %[bb] offset:5120\nv_mfma_f32_16x16x32_bf16 v[76:79], v[32:35], v[96:99], v[76:79]\nds_read_b128 v[96:99], %[bb] offset:10240\n"
        "v_mfma_f32_16x16x32_bf16 v[80:83], v[32:35], v[100:103], v[80:83]\nds_read_b128 v[100:103], %[bb] offset:15360\nv_mfma_f32_16x16x32_bf16 v[84:87], v[32:35], v[104:107], v[84:87]\n"
        "ds_read_b128 v[104:107], %[bb] offset:20480\nv_mfma_f32_16x16x32_bf16 v[68:71], v[36:39], v[108:111], v[68:71]\nds_read_b128 v[108:111], %[bb] offset:1024\n"
        "v_mfma_f32_16x16x32_bf16 v[72:75], v[36:39], v[112:115], v[72:75]\nds_read_b128 v[112:115], %[bb] offset:6144\nv_mfma_f32_16x16x32_bf16 v[76:79], v[36:39], v[116:119], v[76:79]\n"
        "ds_read_b128 v[116:119], %[bb] offset:11264\nv_mfma_f32_16x16x32_bf16 v[80:83], v[36:39], v[120:123], v[80:83]\nds_read_b128 v[120:123], %[bb] offset:16384\n"
        "v_mfma_f32_16x16x32_bf16 v[84:87], v[36:39], v[124:127], v[84:87]\nds_read_b128 v[124:127], %[bb] offset:21504\nv_mfma_f32_16x16x32_bf16 v[68:71], v[40:43], v[128:131], v[68:71]\n"
        "ds_read_b128 v[128:131], %[bb] offset:2048\nv_mfma_f32_16x16x32_bf16 v[72:75], v[40:43], v[132:135], v[72:75]\nds_read_b128 v[132:135], %[bb] offset:7168\n"
        "v_mfma_f32_16x16x32_bf16 v[76:79], v[40:43], v[136:139], v[76:79]\nds_read_b128 v[136:139], %[bb] offset:12288\nv_mfma_f32_16x16x32_bf16 v[80:83], v[40:43], v[140:143], v[80:83]\n"
        "ds_read_b128 v[140:143], %[bb] offset:17408\nv_mfma_f32_16x16x32_bf16 v[84:87], v[40:43], v[144:147], v[84:87]\nds_read_b128 v[144:147], %[bb] offset:22528\n"
        "v_mfma_f32_16x16x32_bf16 v[68:71], v[44:47], v[148:151], v[68:71]\nds_read_b128 v[148:151], %[bb] offset:3072\nv_mfma_f32_16x16x32_bf16 v[72:75], v[44:47], v[152:155], v[72:75]\n"
        "ds_read_b128 v[152:155], %[bb] offset:8192\nv_mfma_f32_16x16x32_bf16 v[76:79], v[44:47], v[156:159], v[76:79]\nds_read_b128 v[156:159], %[bb] offset:13312\n"
        "v_mfma_f32_16x16x32_bf16 v[80:83], v[44:47], v[160:163], v[80:83]\nds_read_b128 v[160:163], %[bb] offset:18432\nv_mfma_f32_16x16x32_bf16 v[84:87], v[44:47], v[164:167], v[84:87]\n"
        "ds_read_b128 v[164:167], %[bb] offset:23552\nds_read_b128 v[28:31], %[bb] offset:4096\nds_read_b128 v[32:35], %[bb] offset:9216\nds_read_b128 v[36:39], %[bb] offset:14336\n"
        "ds_read_b128 v[40:43], %[bb] offset:19456\nds_read_b128 v[44:47], %[bb] offset:24576\ns_mov_b32 %[s1], 0x100000\n.LWc%=:\nds_read_b32 %[tm], %[sy] offset:%[ofr]\ns_waitcnt lgkmcnt(0)\n"
        "v_readfirstlane_b32 %[s0], %[tm]\ns_nop 0\ns_cmp_ge_i32 %[s0], 8\ns_cbranch_scc1 .LXc%=\ns_sleep 1\ns_sub_u32 %[s1], %[s1], 1\ns_cbranch_scc0 .LWc%=\n.LXc%=:\n.LC1_%=:\ns_waitcnt vmcnt(4)\n"
        "v_max_u32 %[tm], v48, v49\nv_max3_u32 %[tm], %[tm], v50, v51\ns_waitcnt vmcnt(3)\nv_max3_u32 %[tm], %[tm], v52, v53\nv_max3_u32 %[tm], %[tm], v54, v55\ns_waitcnt vmcnt(2)\n"
        "v_max3_u32 %[tm], %[tm], v56, v57\nv_max3_u32 %[tm], %[tm], v58, v59\ns_waitcnt vmcnt(1)\nv_max3_u32 %[tm], %[tm], v60, v61\nv_max3_u32 %[tm], %[tm], v62, v63\ns_waitcnt vmcnt(0)\n"
        "v_max3_u32 %[tm], %[tm], v64, v65\nv_max3_u32 %[tm], %[tm], v66, v67\nv_cmp_eq_u32 vcc, -1, %[tm]\ns_cbranch_vccnz .LR1_%=\n.LK1_%=:\n"
        "v_mfma_f32_16x16x32_bf16 v[68:71], v[48:51], v[88:91], v[68:71]\nv_mfma_f32_16x16x32_bf16 v[68:71], v[52:55], v[92:95], v[68:71]\n"
        "v_mfma_f32_16x16x32_bf16 v[68:71], v[56:59], v[96:99], v[68:71]\nv_mfma_f32_16x16x32_bf16 v[68:71], v[60:63], v[100:103], v[68:71]\n"
        "v_mfma_f32_16x16x32_bf16 v[68:71], v[64:67], v[104:107], v[68:71]\nv_mfma_f32_16x16x32_bf16 v[72:75], v[48:51], v[108:111], v[72:75]\n"
        "v_mfma_f32_16x16x32_bf16 v[72:75], v[52:55], v[112:115], v[72:75]\nv_mfma_f32_16x16x32_bf16 v[72:75], v[56:59], v[116:119], v[72:75]\nds_write_b128 %[rw], v[68:71]\n"
        "v_mfma_f32_16x16x32_bf16 v[72:75], v[60:63], v[120:123], v[72:75]\nv_mfma_f32_16x16x32_bf16 v[72:75], v[64:67], v[124:127], v[72:75]\n"
        "v_mfma_f32_16x16x32_bf16 v[76:79], v[48:51], v[128:131], v[76:79]\nv_mfma_f32_16x16x32_bf16 v[76:79], v[52:55], v[132:135], v[76:79]\n"
        "v_mfma_f32_16x16x32_bf16 v[76:79], v[56:59], v[136:139], v[76:79]\nds_write_b128 %[rw], v[72:75] offset:1024\nv_mfma_f32_16x16x32_bf16 v[76:79], v[60:63], v[140:143], v[76:79]\n"
        "ds_write_b32 %[fw], %[f0]\nv_mfma_f32_16x16x32_bf16 v[76:79], v[64:67], v[144:147], v[76:79]\nv_mfma_f32_16x16x32_bf16 v[80:83], v[48:51], v[148:151], v[80:83]\n"
        "v_mfma_f32_16x16x32_bf16 v[80:83], v[52:55], v[152:155], v[80:83]\nv_mfma_f32_16x16x32_bf16 v[80:83], v[56:59], v[156:159], v[80:83]\nds_write_b128 %[rw], v[76:79] offset:2048\n"
        "v_mfma_f32_16x16x32_bf16 v[80:83], v[60:63], v[160:163], v[80:83]\nds_write_b32 %[fw], %[one]\nv_mfma_f32_16x16x32_bf16 v[80:83], v[64:67], v[164:167], v[80:83]\n"
        "v_mfma_f32_16x16x32_bf16 v[84:87], v[48:51], v[28:31], v[84:87]\nv_mfma_f32_16x16x32_bf16 v[84:87], v[52:55], v[32:35], v[84:87]\n"
        "v_mfma_f32_16x16x32_bf16 v[84:87], v[56:59], v[36:39], v[84:87]\nv_mfma_f32_16x16x32_bf16 v[84:87], v[60:63], v[40:43], v[84:87]\n"
        "v_mfma_f32_16x16x32_bf16 v[84:87], v[64:67], v[44:47], v[84:87]\ns_mov_b32 %[s1], 0x100000\n.LY%=:\nds_read_b32 %[tm], %[fr]\nds_read_b128 v[96:99], %[rr] offset:2048\n"
        "ds_read_b128 v[92:95], %[rr] offset:1024\nds_read_b128 v[88:91], %[rr] offset:0\ns_waitcnt lgkmcnt(3)\nv_readfirstlane_b32 %[s0], %[tm]\ns_waitcnt lgkmcnt(0)\ns_cmp_lg_u32 %[s0], 0\n"
        "s_cbranch_scc1 .LZ%=\ns_sub_u32 %[s1], %[s1], 1\ns_cbranch_scc0 .LY%=\n.LZ%=:\nv_add_f32 v80, v80, v92\nv_add_f32 v84, v84, v96\nv_add_f32 v81, v81, v93\nv_add_f32 v85, v85, v97\n"
        "v_add_f32 v82, v82, v94\nv_add_f32 v86, v86, v98\nv_add_f32 v83, v83, v95\nv_add_f32 v87, v87, v99\nv_cvt_pk_bf16_f32 v92, v80, v81\nv_cvt_pk_bf16_f32 v96, v84, v85\n"
        "v_cvt_pk_bf16_f32 v93, v82, v83\nv_cvt_pk_bf16_f32 v97, v86, v87\nv_min_u32 v92, -2, v92\nv_min_u32 v96, -2, v96\nv_min_u32 v93, -2, v93\nv_min_u32 v97, -2, v97\n"
        "ds_write_b64 %[xo], v[92:93] offset:2304\nds_write_b64 %[xo], v[96:97] offset:4608\ns_cmp_eq_u32 %[kh], 0\nv_add_f32 v100, v80, v84\nv_add_f32 v101, v81, v85\nv_add_f32 v102, v82, v86\n"
        "v_add_f32 v103, v83, v87\nv_mul_f32 v104, v84, v84\nv_mul_f32 v105, v85, v85\nv_mul_f32 v106, v86, v86\nv_mul_f32 v107, v87, v87\nv_fmac_f32 v104, v80, v80\nv_fmac_f32 v105, v81, v81\n"
        "v_fmac_f32 v106, v82, v82\nv_fmac_f32 v107, v83, v83\ns_cbranch_scc0 .LV%=\nv_add_f32 v76, v76, v88\nv_add_f32 v77, v77, v89\nv_add_f32 v78, v78, v90\nv_add_f32 v79, v79, v91\n"
        "v_cvt_pk_bf16_f32 v88, v76, v77\nv_cvt_pk_bf16_f32 v89, v78, v79\nv_min_u32 v88, -2, v88\nv_min_u32 v89, -2, v89\nds_write_b64 %[xo], v[88:89] offset:0\nv_add_f32 v100, v100, v76\n"
        "v_add_f32 v101, v101, v77\nv_add_f32 v102, v102, v78\nv_add_f32 v103, v103, v79\nv_fmac_f32 v104, v76, v76\nv_fmac_f32 v105, v77, v77\nv_fmac_f32 v106, v78, v78\nv_fmac_f32 v107, v79, v79\n"
        ".LV%=:\ns_mov_b64 exec, 1\nds_add_u32 %[sy], %[one] offset:%[ox1]\ns_mov_b64 exec, -1\nv_add_f32_dpp v100, v100, v100 row_ror:8 row_mask:0xf bank_mask:0x3\n"
        "v_add_f32_dpp v101, v101, v101 row_ror:8 row_mask:0xf bank_mask:0x3\nv_add_f32_dpp v104, v104, v104 row_ror:8 row_mask:0xf bank_mask:0x3\n"
        "v_add_f32_dpp v105, v105, v105 row_ror:8 row_mask:0xf bank_mask:0x3\nv_add_f32_dpp v100, v102, v102 row_ror:8 row_mask:0xf bank_mask:0xc\n"
        "v_add_f32_dpp v101, v103, v103 row_ror:8 row_mask:0xf bank_mask:0xc\nv_add_f32_dpp v104, v106, v106 row_ror:8 row_mask:0xf bank_mask:0xc\n"
        "v_add_f32_dpp v105, v107, v107 row_ror:8 row_mask:0xf bank_mask:0xc\nv_add_f32_dpp v102, v100, v100 row_half_mirror row_mask:0xf bank_mask:0x5\n"
        "v_add_f32_dpp v103, v104, v104 row_half_mirror row_mask:0xf bank_mask:0x5\ns_nop 0\nv_add_f32_dpp v102, v101, v101 row_half_mirror row_mask:0xf bank_mask:0xa\n"
        "v_add_f32_dpp v103, v105, v105 row_half_mirror row_mask:0xf bank_mask:0xa\ns_nop 0\nv_add_f32_dpp v102, v102, v102 quad_perm:[1,0,3,2] row_mask:0xf bank_mask:0xf\n"
        "v_add_f32_dpp v103, v103, v103 quad_perm:[1,0,3,2] row_mask:0xf bank_mask:0xf\ns_nop 0\nv_add_f32_dpp v102, v102, v102 quad_perm:[2,3,0,1] row_mask:0xf bank_mask:0xf\n"
        "v_add_f32_dpp v103, v103, v103 quad_perm:[2,3,0,1] row_mask:0xf bank_mask:0xf\nv_min_u32 v102, -2, v102\nv_min_u32 v103, -2, v103\n"
        "buffer_store_dwordx2 v[102:103], %[sv], %[srs], %[ss] offen sc1\ns_branch .LE%=\n.LR0_%=:\ns_sub_u32 %[cap], %[cap], 1\ns_cbranch_scc0 .LQ0_%=\ns_mov_b32 %[cap], 0\ns_branch .LK0_%=\n"
        ".LQ0_%=:\ns_sleep 2\nbuffer_load_dwordx4 v[28:31], %[l16], %[ors], %[o0] offen offset:0 sc1\nbuffer_load_dwordx4 v[32:35], %[l16], %[ors], %[o1] offen offset:0 sc1\n"
        "buffer_load_dwordx4 v[36:39], %[l16], %[ors], %[o2] offen offset:0 sc1\nbuffer_load_dwordx4 v[40:43], %[l16], %[ors], %[o3] offen offset:0 sc1\n"
        "buffer_load_dwordx4 v[44:47], %[oha], %[ors], %[o4] offen offset:0 sc1\ns_branch .LC0_%=\n.LR1_%=:\ns_sub_u32 %[cap], %[cap], 1\ns_cbranch_scc0 .LQ1_%=\ns_mov_b32 %[cap], 0\ns_branch .LK1_%=\n"
        ".LQ1_%=:\ns_sleep 2\nbuffer_load_dwordx4 v[48:51], %[l16], %[ors], %[o0] offen offset:1024 sc1\nbuffer_load_dwordx4 v[52:55], %[l16], %[ors], %[o1] offen offset:1024 sc1\n"
        "buffer_load_dwordx4 v[56:59], %[l16], %[ors], %[o2] offen offset:1024 sc1\nbuffer_load_dwordx4 v[60:63], %[l16], %[ors], %[o3] offen offset:1024 sc1\n"
        "buffer_load_dwordx4 v[64:67], %[oha], %[ors], %[o4] offen offset:256 sc1\ns_branch .LC1_%=\n.LE%=:\n"
        : [tm] "=&v"(tm), [bb] "+{v19}"(bb), [cap] "=&s"(cap), [s0] "=&s"(s0_), [s1] "=&s"(s1_), "+{v20}"(xi_), "+{v21}"(xg_)
        : "{v[22:23]}"(rv[0]), "{v[24:25]}"(rv[1]), "{v[26:27]}"(rv[2]), [l16] "{v8}"(l16), [oha] "{v9}"(oha), [ors] "s"(orsrc), [o0] "s"(ob + 0), [o1] "s"(ob + 10240), [o2] "s"(ob + 20480), [o3] "s"(ob + 30720), [o4] "s"(ob + 2048), [sy] "{v10}"(sya), [one] "{v11}"(one), [rw] "{v12}"(rw), [fw] "{v13}"(fw), [f0] "{v14}"(f0), [rr] "{v15}"(rr), [fr] "{v18}"(fr), [xo] "{v16}"(xo_), [sv] "{v17}"(so_), [srs] "s"(srsrc), [ss] "s"(mt2 * 128 + (2 * hg + kh) * 512), [kh] "s"(kh), [wo1] "s"(WO1_OFF), [ofr] "i"(SY_FREE), [ox1] "i"(SY_X1), [ow1] "i"(SY_WO + 4), [nwo] "i"(8), [sl0] "i"(layer ? OSLEEPB : OSLEEP), [sl1] "i"(OSLEEP1)
        : "memory", "scc", "vcc", "v28", "v29", "v30", "v31", "v32", "v33", "v34", "v35", "v36", "v37", "v38", "v39", "v40", "v41", "v42", "v43", "v44", "v45", "v46", "v47", "v48", "v49", "v50", "v51", "v52", "v53", "v54", "v55", "v56", "v57", "v58", "v59", "v60", "v61", "v62", "v63", "v64", "v65", "v66", "v67", "v68", "v69", "v70", "v71", "v72", "v73", "v74", "v75", "v76", "v77", "v78", "v79", "v80", "v81", "v82", "v83", "v84", "v85", "v86", "v87", "v88", "v89", "v90", "v91", "v92", "v93", "v94", "v95", "v96", "v97", "v98", "v99", "v100", "v101", "v102", "v103", "v104", "v105", "v106", "v107", "v108", "v109", "v110", "v111", "v112", "v113", "v114", "v115", "v116", "v117", "v118", "v119", "v120", "v121", "v122", "v123", "v124", "v125", "v126", "v127", "v128", "v129", "v130", "v131", "v132", "v133", "v134", "v135", "v136", "v137", "v138", "v139", "v140", "v141", "v142", "v143", "v144", "v145", "v146", "v147", "v148", "v149", "v150", "v151", "v152", "v153", "v154", "v155", "v156", "v157", "v158", "v159", "v160", "v161", "v162", "v163", "v164", "v165", "v166", "v167");
    RETRY(8 * layer + 1 + grp, 0x10000 - cap);
    RETRY(8 * layer + 3 + grp, rt >> 16);
  }
  lds_inc<SY_FIN>(sya, one);
  lds_poll<SY_X1, 0>(sya, 8);
  {
    typedef __attribute__((address_space(3))) const u32x4* lp4;
    __builtin_amdgcn_raw_buffer_store_b128(*(lp4)(size_t)xi_, x1rs, xg_, 0, 17);
    if (j < 2) __builtin_amdgcn_raw_buffer_store_b128(*(lp4)(size_t)(xi_ + 64 * 144), x1rs, xg_, 64 * XP * 2, 17);
  }
  if (!layer) lds_inc<SY_WI>(sya, one);
  STY(13);
  {
    LANE(lane);
    const int t = 64 * w + lane;
    ST_FLUSH(kid);
  }
}

#define BAUX 17
#define PC8 (gq == 0)
#define PC4 (gq == 0)
#define UPAD (5 * 144)
#define UF_N (UPAD + 16 * 144)
#define KY0 1058
#define KC_N 1120
#define KZ0 130
#define K8_N 192
#define CH_EL (UF_N + 4 * KC_N + 2 * K8_N)
#define AUX_B 18944

__device__ __forceinline__ void kc_store(u16* kd, const u32* d) {
  u32 sw[5], mx[5];
#pragma unroll
  for (int i = 0; i < 5; ++i) { sw[i] = __builtin_amdgcn_alignbit(d[i], d[i], 16); mx[i] = __builtin_amdgcn_perm(d[i], d[i + 1], 0x07060100); }
  *(uint4*)kd = make_uint4(mx[4], mx[3], mx[2], mx[1]);
  *(uint4*)(kd + KC_N) = make_uint4(sw[4], sw[3], sw[2], sw[1]);
  *(uint4*)(kd + 2 * KC_N) = make_uint4(mx[3], mx[2], mx[1], mx[0]);
  *(uint4*)(kd + 3 * KC_N) = make_uint4(sw[3], sw[2], sw[1], sw[0]);
}
#define DUPLO_(x) __builtin_amdgcn_perm(x, x, 0x01000100)
#define DUPHI_(x) __builtin_amdgcn_perm(x, x, 0x03020302)

#define FSLEEP 18
#define TSLEEP 24
#define OAUX 0
#define FPOLL 4
__device__ __forceinline__ void b_tables(unsigned char* smem, const int ch, const int lane) {
  const u16* kc = (const u16*)(smem + KQ_OFF) + ch * 1536;
  u16* Uf = (u16*)smem + ch * CH_EL;
  u16* KC = Uf + UF_N;
  const u16* p = kc + (lane < 32 ? 8 * lane : 256 + 4 * (lane - 32));
  const uint2 a0 = *(const uint2*)p, a1 = *(const uint2*)(p + 4), a2 = *(const uint2*)(p + 8);
  const u32 b0 = *(const u32*)&kc[384 + 2 * lane], b1 = *(const u32*)&kc[386 + 2 * lane];
  const uint2 eh = *(const uint2*)(kc + (lane < 3 ? 0 : lane == 3 ? 384 : 512));
  const u32 c0 = *(const u32*)&kc[638 - 2 * lane], c1 = *(const u32*)&kc[640 - 2 * lane];
  u32 dh[6];
  {
    const bool r1 = lane < 32;
    dh[0] = r1 ? a0.x : DUPLO_(a0.x);
    dh[1] = r1 ? a0.y : __builtin_amdgcn_perm(a0.y, a0.x, 0x05040302);
    dh[2] = r1 ? a1.x : DUPLO_(a0.y);
    dh[3] = r1 ? a1.y : __builtin_amdgcn_perm(a1.x, a0.y, 0x05040302);
    const u32 nx = r1 ? a2.x : a1.x;
    dh[4] = lane < 31 ? nx : DUPLO_(nx);
    dh[5] = lane < 31 ? a2.y : (lane == 63 ? nx : nx >> 16);
    kc_store(KC + 8 * (131 - lane), dh);
  }
  {
    u16* R = KC + 128;
#pragma unroll
    for (int c = 0; c < 4; ++c) if (lane < 36) *(uint4*)&R[c * KC_N + 8 * lane] = make_uint4(0, 0, 0, 0);
    asm volatile("" ::: "memory");
#pragma unroll
    for (int c = 0; c < 4; ++c) { R[c * (KC_N - 1) + 95 - lane] = (u16)b1; R[c * (KC_N - 1) + 239 - lane] = (u16)(b0 >> 16); }
  }
  {
    u32* K8 = (u32*)(KC + 4 * KC_N);
    const u32 wh = __builtin_amdgcn_perm(c0, c1, 0x07060100);
    K8[lane + 1] = wh;
    K8[K8_N / 2 + lane] = wh;
    K8[lane < 32 ? (lane ? 64 + lane : 0) : 128 + lane] = 0u;
  }
  *(uint4*)&Uf[8 * lane] = make_uint4(0, 0, 0, 0);
  *(uint4*)&Uf[8 * lane + UPAD - 512] = make_uint4(0, 0, 0, 0);
  if (lane < 23) {
    const bool e0 = lane == 2, e1 = lane == 3, e2 = lane == 19;
    dh[0] = e1 ? DUPLO_(eh.x) : 0u; dh[1] = e1 ? eh.x & 0xffffu : e2 ? eh.x << 16 : 0u; dh[2] = e2 ? eh.x & 0xffffu : 0u; dh[3] = 0u; dh[4] = e0 ? eh.x : 0u; dh[5] = e0 ? eh.y : 0u;
    kc_store(KC + 8 * (131 - (lane < 3 ? lane - 3 : lane < 11 ? 61 + lane : 109 + lane)), dh);
  }
}
#define FSH 20
#define FS0 19
#define FS1 18
#define FSX 4
#define FSH0 12
#define BPS 0
#define PBU 1
#define PBU2 1
#define PBUF 1
#define PBL1 2
#define FSLEEP0 FSLEEP
#define PBL 2
#define PBH 3
#define PBE 3
template <bool FULL, int layer>
__device__ __forceinline__ void b_conv(unsigned char* smem, const int b, const int w, const int gq, const int wq, const int cG, const u16* xtin, float2* stats, u32* fstat,
                                       u16* xtout, u32x4* mark, const u16* wot
                                       ) {
  LANE(lane);
  const int t = 64 * w + lane, n = lane & 15, g = lane >> 4, ck = 64 * wq + lane;
  const int v = FULL ? (wq + 2 * gq) & 3 : (0x1023 >> (4 * wq)) & 3;
  const int p = FULL ? (v & 1) : (b & 1);
  const u32 Uf = LDSA(smem) + gq * (CH_EL * 2), Kb = Uf + 2 * UF_N;
  const u32 aux = LDSA(smem) + 47360 + gq * AUX_B;
  const u32 sya = LDSA(smem) + SY_OFF + 256 * layer;
  u32 cna = sya + 80 + 16 * gq, one = 1u;
  u32 ra, rb, wa, ka, kb = Kb;
  if (v < 2) {
    const int r1 = FULL ? 4 : 2 + 2 * v, y0 = KY0 - 16 - n + 8 * g - 128 * r1;
    ra = Uf + 2 * (UPAD + 144 * (n - r1) + 8 * g + 16 * p - 32);
    rb = ra + ((g >= 2 || p) ? 32 : 0);
    wa = aux + (FULL ? (g & 1) * 4096 + n * 256 + (((2 * p + (g >> 1)) ^ (n & 7)) << 4) : v * 4096 + (g & 1) * 2048 + n * 128 + (((g >> 1) ^ (n & 7)) << 4));
    ka = Kb + 2 * ((y0 & 3) * KC_N + (y0 & ~3));
  } else if (v == 2) {
    const int h = n + (g & 1) - 1, zs = 31 - (FULL ? n : 4 * (n >> 1) + 2 * p + (n & 1)) + 4 * g;
    ra = Uf + 2 * (UPAD + 864 * (g >> 1) - 576 + 8 * h - (h < 0 ? 16 : 0));
    rb = ra - (h == 0 ? 40 : 8);
    wa = aux + 12800 + 3072 * (g & 1) + 16 * ((FULL ? 96 : 48) * (g >> 1) + n);
    ka = Kb + 2 * ((zs & 3) * KC_N + 128 + (zs & ~3));
  } else {
    const int h = n + g - 3, tx = (FULL ? 16 * n : 32 * n + 16 * p) - 8 + 8 * g;
    const int z0 = KZ0 - 2 - (FULL ? 2 * n + 96 : 4 * n + 2 * p + 64) + 4 * g, yx = KY0 - 1032 - n + 8 * g;
    ra = Uf + 2 * (UPAD + 8 * h - (h < 0 ? 16 : 0));
    rb = Uf + 2 * (UPAD + tx + 16 * (tx >> 7));
    wa = aux + 8192 + (FULL ? (g & 1) * 2304 + (g >> 1) * 1152 : (g & 1) * 1152 + (g >> 1) * 640) + n * 16;
    ka = Kb + 2 * (4 * KC_N + ((z0 >> 1) & 1) * K8_N + (z0 & ~3));
    kb = Kb + 2 * ((yx & 3) * KC_N + (yx & ~3));
  }
  u32 uw_ = Uf + 2 * (UPAD + 8 * ck + 16 * (ck >> 4));
  const int eck = FULL ? ck : (4 * (ck >> 1) + 2 * p + (ck & 1)) & 255;
  u32 ue = Uf + 2 * (UPAD + 8 * eck + 16 * (eck >> 4));
  u32 exr = aux + (FULL ? (ck ^ ((ck >> 4) & 7)) : ((ck & 127) ^ ((ck >> 3) & 7))) * 16;
  u32 frr = aux + 8192 + (FULL ? (ck & 1) * 1152 + (ck & 126) * 8 : (ck & 1) * 640 + (ck & 62) * 8);
  u32 mr_ = aux + 12800 + 16 * (FULL ? ck - 64 : ck >= 32 ? ck - 32 : ck);
  int od, oh;
  if (layer) { od = (((eck >> 3) * 640 + cG) * 64 + 8 * (eck & 7)) * 2; oh = -1; }
  else {
    const int dst = (eck >> 3) * 51200 + (cG >> 5) * 2560;
    const int pr = 4 * ((cG >> 3) & 3) + (cG & 3) + 4 * (cG & 4);
    od = (dst + pr * 64 + 8 * ((eck & 7) ^ (pr & 6))) * 2;
    oh = ((eck & 7) >= 6 && eck < 248) ? (dst + 51200 + 2048 + pr * 16 + 8 * (eck & 1)) * 2 : -1;
  }
  u32 bna = LDSA(smem) + BND_OFF + (v == 2 ? 2048 : 0) + 16 * lane, kfa = LDSA(smem) + KQ_OFF + gq * 3072 + 1536;
  int c16 = ck * 16;
  const __amdgpu_buffer_rsrc_t xrs = __builtin_amdgcn_make_buffer_rsrc((void*)(xtin + (size_t)cG * XP), 0, XP * 2, 0x00020000);
  const __amdgpu_buffer_rsrc_t frs = __builtin_amdgcn_make_buffer_rsrc((void*)(fstat + (b & 7) * 2048), 0, 8192, 0x00020000);
  const __amdgpu_buffer_rsrc_t ors = __builtin_amdgcn_make_buffer_rsrc(xtout, 0, 32 * 102400, 0x00020000);
  PIN(ra); PIN(rb); PIN(wa); PIN(uw_); PIN(exr); PIN(frr); PIN(mr_); PIN(od);
  int xo = eck * 16;
  if (!FULL) { PIN(ue); PIN(xo); }
  const __amdgpu_buffer_rsrc_t mrs = __builtin_amdgcn_make_buffer_rsrc(mark + b * 2032, 0, 2032 * 16, 0x00020000);
  int mo = t * 16;
  if (!layer) { PIN(oh); PIN(mo); }
  PIN(cna); PIN(one); PIN(ka); PIN(kb); PIN(bna); PIN(kfa); PIN(c16);
  ST(6);
  if (!FULL && wq == 3) {
    const int xcd = b & 7, idx = b >> 3;
    const int s0 = 64 * (8 * (xcd >> 1) + (idx & 7)), hg = 4 * (xcd & 1) + (idx >> 3);
    const __amdgpu_buffer_rsrc_t srsrc = __builtin_amdgcn_make_buffer_rsrc(stats + (size_t)s0 * 16, 0, 8192, 0x00020000);
    const __amdgpu_buffer_rsrc_t wrs = __builtin_amdgcn_make_buffer_rsrc(fstat + hg * 2048 + s0 / 2, 0, 4096 + 128, 0x00020000);
    int so = lane * 16;
    int wo_ = (lane >> 5) * 4096 + (lane & 31) * 4;
    PIN(so); PIN(wo_);
    lds_poll<SY_FIN, 2>(sya, 8);
    __builtin_amdgcn_s_sleep(SSLEEP);
    f32x4 sm;
    int spins = 0;
    _Pragma("unroll 1")
    while (true) {
      asm volatile("" ::: "memory");
      u32x4 pv[8];
#pragma unroll
      for (int i = 0; i < 8; ++i) pv[i] = __builtin_amdgcn_raw_buffer_load_b128(srsrc, so, i * 1024, 16);
      u32 m = 0;
      sm = __builtin_bit_cast(f32x4, pv[0]);
#pragma unroll
      for (int i = 0; i < 8; ++i) {
        m = umax3(umax3(m, pv[i][0], pv[i][1]), pv[i][2], pv[i][3]);
        if (i) sm += __builtin_bit_cast(f32x4, pv[i]);
      }
      if (__builtin_amdgcn_ballot_w64(m == 0xffffffffu) == 0 || ++spins > GCAP) break;
      __builtin_amdgcn_s_sleep(4);
    }
    RETRY(8 * layer + 5, spins);
    ST(7);
    const auto q0_ = __builtin_amdgcn_permlane32_swap(__float_as_uint(sm[0]), __float_as_uint(sm[2]), false, false);
    const auto q1_ = __builtin_amdgcn_permlane32_swap(__float_as_uint(sm[1]), __float_as_uint(sm[3]), false, false);
    const float mean = (__uint_as_float(q0_[0]) + __uint_as_float(q0_[1])) * (1.0f / 640.0f);
    const float var = fmaxf(fmaf(-mean, mean, (__uint_as_float(q1_[0]) + __uint_as_float(q1_[1])) * (1.0f / 640.0f)), 0.f);
    const float r = 256.0f * __builtin_amdgcn_rcpf(rbf(__builtin_amdgcn_sqrtf(rbf(rbf(var) + 1e-5f))));
    const u32 d = pack2(-mean * r, 0.f);
    const u32 o = ((max(__float_as_uint(r) + 0x8000u, d) - d) & 0xffff0000u) | d;
    __builtin_amdgcn_raw_buffer_store_b32(min(o, 0xfffffffeu), wrs, wo_, 0, 17);
    lds_inc<SY_FS>(sya, one);
    ST(8);
    __builtin_amdgcn_s_setprio(PBU);
  }
  f32x2 kd;
  lds_poll<12, 1>(cna, 1);
  u32x4 kt[18];
  if (FULL) {
      asm volatile(
        "ds_read_b64 %[kd], %[kfa]\ns_cmp_lt_u32 %[v], 2\ns_cbranch_scc0 .LA%=\nds_read_b64 v[100:101], %[ka] offset:1024\nds_read_b64 v[102:103], %[ka] offset:1032\n"
        "ds_read_b64 v[104:105], %[ka] offset:960\nds_read_b64 v[106:107], %[ka] offset:968\nds_read_b64 v[108:109], %[ka] offset:896\nds_read_b64 v[110:111], %[ka] offset:904\n"
        "ds_read_b64 v[112:113], %[ka] offset:832\nds_read_b64 v[114:115], %[ka] offset:840\nds_read_b64 v[116:117], %[ka] offset:768\nds_read_b64 v[118:119], %[ka] offset:776\n"
        "ds_read_b64 v[120:121], %[ka] offset:704\nds_read_b64 v[122:123], %[ka] offset:712\nds_read_b64 v[124:125], %[ka] offset:640\nds_read_b64 v[126:127], %[ka] offset:648\n"
        "ds_read_b64 v[128:129], %[ka] offset:576\nds_read_b64 v[130:131], %[ka] offset:584\nds_read_b64 v[132:133], %[ka] offset:512\nds_read_b64 v[134:135], %[ka] offset:520\n"
        "ds_read_b64 v[136:137], %[ka] offset:448\nds_read_b64 v[138:139], %[ka] offset:456\nds_read_b64 v[140:141], %[ka] offset:384\nds_read_b64 v[142:143], %[ka] offset:392\n"
        "ds_read_b64 v[144:145], %[ka] offset:320\nds_read_b64 v[146:147], %[ka] offset:328\nds_read_b64 v[148:149], %[ka] offset:256\nds_read_b64 v[150:151], %[ka] offset:264\n"
        "ds_read_b64 v[152:153], %[ka] offset:192\nds_read_b64 v[154:155], %[ka] offset:200\nds_read_b64 v[156:157], %[ka] offset:128\nds_read_b64 v[158:159], %[ka] offset:136\n"
        "ds_read_b64 v[160:161], %[ka] offset:64\nds_read_b64 v[162:163], %[ka] offset:72\nds_read_b64 v[164:165], %[ka] offset:0\nds_read_b64 v[166:167], %[ka] offset:8\ns_branch .LE%=\n.LA%=:\n"
        "ds_read_b128 v[100:103], %[bna]\nds_read_b128 v[104:107], %[bna] offset:1024\ns_cmp_eq_u32 %[v], 2\ns_cbranch_scc0 .LB%=\nds_read_b64 v[108:109], %[ka] offset:0\n"
        "ds_read_b64 v[110:111], %[ka] offset:288\nds_read_b64 v[112:113], %[ka] offset:32\nds_read_b64 v[114:115], %[ka] offset:320\nds_read_b64 v[116:117], %[ka] offset:64\n"
        "ds_read_b64 v[118:119], %[ka] offset:352\nds_read_b64 v[120:121], %[ka] offset:96\nds_read_b64 v[122:123], %[ka] offset:384\nds_read_b64 v[124:125], %[ka] offset:128\n"
        "ds_read_b64 v[126:127], %[ka] offset:416\ns_branch .LE%=\n.LB%=:\nds_read_b64 v[108:109], %[ka] offset:192\nds_read_b64 v[110:111], %[ka] offset:224\nds_read_b64 v[112:113], %[ka] offset:128\n"
        "ds_read_b64 v[114:115], %[ka] offset:160\nds_read_b64 v[116:117], %[ka] offset:64\nds_read_b64 v[118:119], %[ka] offset:96\nds_read_b64 v[120:121], %[ka] offset:0\n"
        "ds_read_b64 v[122:123], %[ka] offset:32\nds_read_b64 v[124:125], %[kb] offset:0\nds_read_b64 v[126:127], %[kb] offset:8\n.LE%=:\ns_waitcnt lgkmcnt(0)\n"
        : "=&{v[100:103]}"(kt[0]), "=&{v[104:107]}"(kt[1]), "=&{v[108:111]}"(kt[2]), "=&{v[112:115]}"(kt[3]), "=&{v[116:119]}"(kt[4]), "=&{v[120:123]}"(kt[5]), "=&{v[124:127]}"(kt[6]), "=&{v[128:131]}"(kt[7]), "=&{v[132:135]}"(kt[8]), "=&{v[136:139]}"(kt[9]), "=&{v[140:143]}"(kt[10]), "=&{v[144:147]}"(kt[11]), "=&{v[148:151]}"(kt[12]), "=&{v[152:155]}"(kt[13]), "=&{v[156:159]}"(kt[14]), "=&{v[160:163]}"(kt[15]), "=&{v[164:167]}"(kt[16]), [kd] "=&v"(kd)
        : [ka] "v"(ka), [kb] "v"(kb), [bna] "v"(bna), [kfa] "v"(kfa), [v] "s"(v)
        : "memory", "scc", "vcc");
  } else {
      asm volatile(
        "ds_read_b64 %[kd], %[kfa]\ns_cmp_lt_u32 %[v], 2\ns_cbranch_scc0 .LA%=\nds_read_b64 v[100:101], %[ka] offset:512\nds_read_b64 v[102:103], %[ka] offset:520\n"
        "ds_read_b64 v[104:105], %[ka] offset:448\nds_read_b64 v[106:107], %[ka] offset:456\nds_read_b64 v[108:109], %[ka] offset:384\nds_read_b64 v[110:111], %[ka] offset:392\n"
        "ds_read_b64 v[112:113], %[ka] offset:320\nds_read_b64 v[114:115], %[ka] offset:328\nds_read_b64 v[116:117], %[ka] offset:256\nds_read_b64 v[118:119], %[ka] offset:264\n"
        "ds_read_b64 v[120:121], %[ka] offset:192\nds_read_b64 v[122:123], %[ka] offset:200\nds_read_b64 v[124:125], %[ka] offset:128\nds_read_b64 v[126:127], %[ka] offset:136\n"
        "ds_read_b64 v[128:129], %[ka] offset:64\nds_read_b64 v[130:131], %[ka] offset:72\nds_read_b64 v[132:133], %[ka] offset:0\nds_read_b64 v[134:135], %[ka] offset:8\ns_branch .LE%=\n.LA%=:\n"
        "ds_read_b128 v[100:103], %[bna]\nds_read_b128 v[104:107], %[bna] offset:1024\ns_cmp_eq_u32 %[v], 2\ns_cbranch_scc0 .LB%=\nds_read_b64 v[108:109], %[ka] offset:0\n"
        "ds_read_b64 v[110:111], %[ka] offset:288\nds_read_b64 v[112:113], %[ka] offset:32\nds_read_b64 v[114:115], %[ka] offset:320\nds_read_b64 v[116:117], %[ka] offset:64\n"
        "ds_read_b64 v[118:119], %[ka] offset:352\nds_read_b64 v[120:121], %[ka] offset:96\nds_read_b64 v[122:123], %[ka] offset:384\nds_read_b64 v[124:125], %[ka] offset:128\n"
        "ds_read_b64 v[126:127], %[ka] offset:416\nds_read_b64 v[128:129], %[ka] offset:160\nds_read_b64 v[130:131], %[ka] offset:448\ns_branch .LE%=\n.LB%=:\nds_read_b64 v[108:109], %[ka] offset:192\n"
        "ds_read_b64 v[110:111], %[ka] offset:224\nds_read_b64 v[112:113], %[ka] offset:128\nds_read_b64 v[114:115], %[ka] offset:160\nds_read_b64 v[116:117], %[ka] offset:64\n"
        "ds_read_b64 v[118:119], %[ka] offset:96\nds_read_b64 v[120:121], %[ka] offset:0\nds_read_b64 v[122:123], %[ka] offset:32\nds_read_b64 v[124:125], %[kb] offset:0\n"
        "ds_read_b64 v[126:127], %[kb] offset:8\n.LE%=:\ns_waitcnt lgkmcnt(0)\n"
        : "=&{v[100:103]}"(kt[0]), "=&{v[104:107]}"(kt[1]), "=&{v[108:111]}"(kt[2]), "=&{v[112:115]}"(kt[3]), "=&{v[116:119]}"(kt[4]), "=&{v[120:123]}"(kt[5]), "=&{v[124:127]}"(kt[6]), "=&{v[128:131]}"(kt[7]), "=&{v[132:135]}"(kt[8]), [kd] "=&v"(kd)
        : [ka] "v"(ka), [kb] "v"(kb), [bna] "v"(bna), [kfa] "v"(kfa), [v] "s"(v)
        : "memory", "scc", "vcc");
  }
  float kf4 = ck >= 32 ? kd[0] : 0.f;
  if (!FULL) PIN(kf4);
  ST(1);
  lds_poll<SY_FS, 2>(sya, 1);
  if (!FULL && !layer && wq < 3) {
    const u16* sp = wot + (size_t)(4 * (b & 1) + (b >> 6)) * 51200 + wq * 512 + lane * 8;
    lds_poll<SY_WI, 1>(sya, 8);
#pragma unroll
    for (int i = 0; i < 13; ++i) if (i < 12 || wq < 2) __builtin_amdgcn_global_load_lds((const u32*)(sp + i * 1536), (u32*)(smem + A_R1 + (3 * i + wq) * 1024), 16, 0, 16);
  }
  __builtin_amdgcn_s_setprio(FULL ? PBUF : PBU2);
  {
    u32 pv; int s0_, s1_, cap;
    if (FULL && layer) {
      asm volatile(
        "s_mov_b32 %[cap], 0x10000\ns_movk_i32 %[s1], 4096\ns_sleep %[fsx]\nbuffer_load_dwordx4 v[36:39], %[c16], %[xrs], 0 offen sc1\ns_sleep %[fsa]\ns_cmp_eq_u32 %[gq], 0\ns_cbranch_scc0 .LA%=\n"
        "s_sleep %[fsb]\n.LA%=:\nbuffer_load_dwordx4 v[40:43], %[c16], %[frs], 0 offen sc1\nbuffer_load_dwordx4 v[44:47], %[c16], %[frs], %[s1] offen sc1\ns_waitcnt vmcnt(2)\n.LB%=:\n"
        "v_max3_u32 %[pv], v36, v37, v38\nv_max_u32 %[pv], %[pv], v39\nv_cmp_eq_u32 vcc, -1, %[pv]\ns_cbranch_vccnz .LC%=\n.LD%=:\nv_lshlrev_b32 v26, 16, v36\nv_and_b32 v27, 0xffff0000, v36\n"
        "v_lshlrev_b32 v28, 16, v37\nv_and_b32 v29, 0xffff0000, v37\nv_lshlrev_b32 v30, 16, v38\nv_and_b32 v31, 0xffff0000, v38\nv_lshlrev_b32 v32, 16, v39\nv_and_b32 v33, 0xffff0000, v39\n.LF%=:\n"
        "s_waitcnt vmcnt(1)\nv_max3_u32 %[pv], v40, v41, v42\nv_lshlrev_b32 v48, 16, v40\nv_lshlrev_b32 v50, 16, v41\nv_lshlrev_b32 v52, 16, v42\nv_lshlrev_b32 v54, 16, v43\nv_fmac_f32 v48, v26, v40\n"
        "v_fmac_f32 v50, v28, v41\nv_fmac_f32 v52, v30, v42\nv_fmac_f32 v54, v32, v43\ns_waitcnt vmcnt(0)\nv_max3_u32 %[pv], %[pv], v43, v44\nv_max3_u32 %[pv], %[pv], v45, v46\n"
        "v_max_u32 %[pv], %[pv], v47\nv_cmp_eq_u32 vcc, -1, %[pv]\nv_lshlrev_b32 v49, 16, v44\nv_lshlrev_b32 v51, 16, v45\nv_lshlrev_b32 v53, 16, v46\nv_lshlrev_b32 v55, 16, v47\n"
        "s_cbranch_vccnz .LI%=\n.LM%=:\nv_fmac_f32 v49, v27, v44\nv_fmac_f32 v51, v29, v45\nv_fmac_f32 v53, v31, v46\nv_fmac_f32 v55, v33, v47\nv_cvt_pk_f16_f32 v36, v48, v49\n"
        "v_cvt_pk_f16_f32 v37, v50, v51\nv_cvt_pk_f16_f32 v38, v52, v53\nv_cvt_pk_f16_f32 v39, v54, v55\nds_write_b128 %[uw], v[36:39]\ns_mov_b64 exec, 1\nds_add_u32 %[cna], %[one] offset:0\n"
        "s_mov_b64 exec, -1\nv_fmac_f32 v26, v48, %[kd1]\nv_fmac_f32 v27, v49, %[kd1]\nv_fmac_f32 v28, v50, %[kd1]\nv_fmac_f32 v29, v51, %[kd1]\nv_fmac_f32 v30, v52, %[kd1]\n"
        "v_fmac_f32 v31, v53, %[kd1]\nv_fmac_f32 v32, v54, %[kd1]\nv_fmac_f32 v33, v55, %[kd1]\ns_setprio %[pbl]\ns_mov_b32 %[s1], 0x100000\n.LN%=:\nds_read_b32 %[pv], %[cna]\ns_waitcnt lgkmcnt(0)\n"
        "v_readfirstlane_b32 %[s0], %[pv]\ns_nop 0\ns_cmp_ge_i32 %[s0], 4\ns_cbranch_scc1 .LO%=\ns_sub_u32 %[s1], %[s1], 1\ns_cbranch_scc0 .LN%=\n.LO%=:\ns_cmp_lt_u32 %[v], 2\ns_cbranch_scc0 .LT%=\n"
        "ds_read_b128 v[52:55], %[ra] offset:1376\nds_read_b128 v[56:59], %[ra] offset:1312\nds_read_b128 v[60:63], %[ra] offset:1248\nds_read_b128 v[64:67], %[rb] offset:1152\n"
        "ds_read_b128 v[68:71], %[ra] offset:1088\nds_read_b128 v[72:75], %[ra] offset:1024\nds_read_b128 v[76:79], %[ra] offset:960\nds_read_b128 v[80:83], %[rb] offset:864\nv_xor_b32 v14, 64, %[wa]\n"
        "s_waitcnt lgkmcnt(7)\nv_mfma_f32_16x16x32_f16 v[48:51], v[100:103], v[52:55], 0\nds_read_b128 v[52:55], %[ra] offset:800\ns_waitcnt lgkmcnt(7)\n"
        "v_mfma_f32_16x16x32_f16 v[44:47], v[100:103], v[56:59], 0\ns_waitcnt lgkmcnt(6)\nv_mfma_f32_16x16x32_f16 v[40:43], v[100:103], v[60:63], 0\ns_waitcnt lgkmcnt(5)\n"
        "v_mfma_f32_16x16x32_f16 v[36:39], v[100:103], v[64:67], 0\nv_mfma_f32_16x16x32_f16 v[48:51], v[104:107], v[56:59], v[48:51]\nds_read_b128 v[56:59], %[ra] offset:736\n"
        "v_mfma_f32_16x16x32_f16 v[44:47], v[104:107], v[60:63], v[44:47]\nv_mfma_f32_16x16x32_f16 v[40:43], v[104:107], v[64:67], v[40:43]\ns_waitcnt lgkmcnt(5)\n"
        "v_mfma_f32_16x16x32_f16 v[36:39], v[104:107], v[68:71], v[36:39]\nv_mfma_f32_16x16x32_f16 v[48:51], v[108:111], v[60:63], v[48:51]\nds_read_b128 v[60:63], %[ra] offset:672\n"
        "v_mfma_f32_16x16x32_f16 v[44:47], v[108:111], v[64:67], v[44:47]\nv_mfma_f32_16x16x32_f16 v[40:43], v[108:111], v[68:71], v[40:43]\ns_waitcnt lgkmcnt(5)\n"
        "v_mfma_f32_16x16x32_f16 v[36:39], v[108:111], v[72:75], v[36:39]\nv_mfma_f32_16x16x32_f16 v[48:51], v[112:115], v[64:67], v[48:51]\nds_read_b128 v[64:67], %[rb] offset:576\n"
        "v_mfma_f32_16x16x32_f16 v[44:47], v[112:115], v[68:71], v[44:47]\nv_mfma_f32_16x16x32_f16 v[40:43], v[112:115], v[72:75], v[40:43]\ns_waitcnt lgkmcnt(5)\n"
        "v_mfma_f32_16x16x32_f16 v[36:39], v[112:115], v[76:79], v[36:39]\nv_mfma_f32_16x16x32_f16 v[48:51], v[116:119], v[68:71], v[48:51]\nds_read_b128 v[68:71], %[ra] offset:512\n"
        "v_mfma_f32_16x16x32_f16 v[44:47], v[116:119], v[72:75], v[44:47]\nv_mfma_f32_16x16x32_f16 v[40:43], v[116:119], v[76:79], v[40:43]\ns_waitcnt lgkmcnt(5)\n"
        "v_mfma_f32_16x16x32_f16 v[36:39], v[116:119], v[80:83], v[36:39]\nv_mfma_f32_16x16x32_f16 v[48:51], v[120:123], v[72:75], v[48:51]\nds_read_b128 v[72:75], %[ra] offset:448\n"
        "v_mfma_f32_16x16x32_f16 v[44:47], v[120:123], v[76:79], v[44:47]\nv_mfma_f32_16x16x32_f16 v[40:43], v[120:123], v[80:83], v[40:43]\ns_waitcnt lgkmcnt(5)\n"
        "v_mfma_f32_16x16x32_f16 v[36:39], v[120:123], v[52:55], v[36:39]\nv_mfma_f32_16x16x32_f16 v[48:51], v[124:127], v[76:79], v[48:51]\nds_read_b128 v[76:79], %[ra] offset:384\n"
        "v_mfma_f32_16x16x32_f16 v[44:47], v[124:127], v[80:83], v[44:47]\nv_mfma_f32_16x16x32_f16 v[40:43], v[124:127], v[52:55], v[40:43]\ns_waitcnt lgkmcnt(5)\n"
        "v_mfma_f32_16x16x32_f16 v[36:39], v[124:127], v[56:59], v[36:39]\nv_mfma_f32_16x16x32_f16 v[48:51], v[128:131], v[80:83], v[48:51]\nds_read_b128 v[80:83], %[rb] offset:288\n"
        "v_mfma_f32_16x16x32_f16 v[44:47], v[128:131], v[52:55], v[44:47]\nv_mfma_f32_16x16x32_f16 v[40:43], v[128:131], v[56:59], v[40:43]\ns_waitcnt lgkmcnt(5)\n"
        "v_mfma_f32_16x16x32_f16 v[36:39], v[128:131], v[60:63], v[36:39]\nv_mfma_f32_16x16x32_f16 v[48:51], v[132:135], v[52:55], v[48:51]\nds_read_b128 v[52:55], %[ra] offset:224\n"
        "v_mfma_f32_16x16x32_f16 v[44:47], v[132:135], v[56:59], v[44:47]\nv_mfma_f32_16x16x32_f16 v[40:43], v[132:135], v[60:63], v[40:43]\ns_waitcnt lgkmcnt(5)\n"
        "v_mfma_f32_16x16x32_f16 v[36:39], v[132:135], v[64:67], v[36:39]\nv_mfma_f32_16x16x32_f16 v[48:51], v[136:139], v[56:59], v[48:51]\nds_read_b128 v[56:59], %[ra] offset:160\n"
        "v_mfma_f32_16x16x32_f16 v[44:47], v[136:139], v[60:63], v[44:47]\nv_mfma_f32_16x16x32_f16 v[40:43], v[136:139], v[64:67], v[40:43]\ns_waitcnt lgkmcnt(5)\n"
        "v_mfma_f32_16x16x32_f16 v[36:39], v[136:139], v[68:71], v[36:39]\nv_mfma_f32_16x16x32_f16 v[48:51], v[140:143], v[60:63], v[48:51]\nds_read_b128 v[60:63], %[ra] offset:96\n"
        "v_mfma_f32_16x16x32_f16 v[44:47], v[140:143], v[64:67], v[44:47]\nv_mfma_f32_16x16x32_f16 v[40:43], v[140:143], v[68:71], v[40:43]\ns_waitcnt lgkmcnt(5)\n"
        "v_mfma_f32_16x16x32_f16 v[36:39], v[140:143], v[72:75], v[36:39]\nv_mfma_f32_16x16x32_f16 v[48:51], v[144:147], v[64:67], v[48:51]\nds_read_b128 v[64:67], %[rb] offset:0\n"
        "v_mfma_f32_16x16x32_f16 v[44:47], v[144:147], v[68:71], v[44:47]\nv_mfma_f32_16x16x32_f16 v[40:43], v[144:147], v[72:75], v[40:43]\ns_waitcnt lgkmcnt(5)\n"
        "v_mfma_f32_16x16x32_f16 v[36:39], v[144:147], v[76:79], v[36:39]\nv_mfma_f32_16x16x32_f16 v[48:51], v[148:151], v[68:71], v[48:51]\n"
        "v_mfma_f32_16x16x32_f16 v[44:47], v[148:151], v[72:75], v[44:47]\nv_mfma_f32_16x16x32_f16 v[40:43], v[148:151], v[76:79], v[40:43]\ns_waitcnt lgkmcnt(4)\n"
        "v_mfma_f32_16x16x32_f16 v[36:39], v[148:151], v[80:83], v[36:39]\nv_mfma_f32_16x16x32_f16 v[48:51], v[152:155], v[72:75], v[48:51]\n"
        "v_mfma_f32_16x16x32_f16 v[44:47], v[152:155], v[76:79], v[44:47]\nv_mfma_f32_16x16x32_f16 v[40:43], v[152:155], v[80:83], v[40:43]\ns_waitcnt lgkmcnt(3)\n"
        "v_mfma_f32_16x16x32_f16 v[36:39], v[152:155], v[52:55], v[36:39]\nv_mfma_f32_16x16x32_f16 v[48:51], v[156:159], v[76:79], v[48:51]\n"
        "v_mfma_f32_16x16x32_f16 v[44:47], v[156:159], v[80:83], v[44:47]\nv_mfma_f32_16x16x32_f16 v[40:43], v[156:159], v[52:55], v[40:43]\ns_waitcnt lgkmcnt(2)\n"
        "v_mfma_f32_16x16x32_f16 v[36:39], v[156:159], v[56:59], v[36:39]\nv_mfma_f32_16x16x32_f16 v[48:51], v[160:163], v[80:83], v[48:51]\n"
        "v_mfma_f32_16x16x32_f16 v[44:47], v[160:163], v[52:55], v[44:47]\nv_mfma_f32_16x16x32_f16 v[40:43], v[160:163], v[56:59], v[40:43]\ns_waitcnt lgkmcnt(1)\n"
        "v_mfma_f32_16x16x32_f16 v[36:39], v[160:163], v[60:63], v[36:39]\nv_mfma_f32_16x16x32_f16 v[48:51], v[164:167], v[52:55], v[48:51]\n"
        "v_mfma_f32_16x16x32_f16 v[44:47], v[164:167], v[56:59], v[44:47]\nv_mfma_f32_16x16x32_f16 v[40:43], v[164:167], v[60:63], v[40:43]\ns_waitcnt lgkmcnt(0)\n"
        "v_mfma_f32_16x16x32_f16 v[36:39], v[164:167], v[64:67], v[36:39]\ns_nop 4\nds_write_b128 v14, v[48:51] offset:128\nds_write_b128 %[wa], v[44:47] offset:128\n"
        "ds_write_b128 v14, v[40:43] offset:0\nds_write_b128 %[wa], v[36:39] offset:0\ns_branch .LE%=\n.LT%=:\ns_cmp_eq_u32 %[v], 2\ns_cbranch_scc0 .LV%=\nds_read_b64 v[60:61], %[rb] offset:0\n"
        "ds_read_b128 v[62:65], %[ra] offset:0\nds_read_b64 v[66:67], %[rb] offset:288\nds_read_b128 v[68:71], %[ra] offset:288\nds_read_b64 v[72:73], %[rb] offset:576\n"
        "ds_read_b128 v[74:77], %[ra] offset:576\nds_read_b64 v[78:79], %[rb] offset:864\nds_read_b128 v[80:83], %[ra] offset:864\ns_waitcnt lgkmcnt(6)\n"
        "v_mfma_f32_16x16x32_f16 v[84:87], v[60:63], v[100:103], 0\nv_mfma_f32_16x16x32_f16 v[88:91], v[62:65], v[104:107], 0\nds_read_b64 v[60:61], %[rb] offset:1152\n"
        "ds_read_b128 v[62:65], %[ra] offset:1152\ns_waitcnt lgkmcnt(6)\nv_mfma_f32_16x16x32_f16 v[92:95], v[66:69], v[100:103], 0\nv_mfma_f32_16x16x32_f16 v[96:99], v[68:71], v[104:107], 0\n"
        "ds_read_b64 v[66:67], %[rb] offset:1440\nds_read_b128 v[68:71], %[ra] offset:1440\ns_nop 7\nv_cvt_pk_f16_f32 v136, v84, v85\nv_cvt_pk_f16_f32 v137, v86, v87\nv_cvt_pk_f16_f32 v138, v88, v89\n"
        "v_cvt_pk_f16_f32 v139, v90, v91\ns_nop 1\nv_mfma_f32_16x16x32_f16 v[36:39], v[136:139], v[108:111], 0\ns_waitcnt lgkmcnt(6)\nv_mfma_f32_16x16x32_f16 v[84:87], v[72:75], v[100:103], 0\n"
        "v_mfma_f32_16x16x32_f16 v[88:91], v[74:77], v[104:107], 0\nds_read_b64 v[72:73], %[rb] offset:1728\nds_read_b128 v[74:77], %[ra] offset:1728\nv_cvt_pk_f16_f32 v140, v92, v93\n"
        "v_cvt_pk_f16_f32 v141, v94, v95\nv_cvt_pk_f16_f32 v142, v96, v97\nv_cvt_pk_f16_f32 v143, v98, v99\ns_nop 1\nv_mfma_f32_16x16x32_f16 v[36:39], v[140:143], v[112:115], v[36:39]\n"
        "v_mfma_f32_16x16x32_f16 v[40:43], v[140:143], v[108:111], 0\ns_waitcnt lgkmcnt(6)\nv_mfma_f32_16x16x32_f16 v[92:95], v[78:81], v[100:103], 0\nv_cvt_pk_f16_f32 v136, v84, v85\n"
        "v_cvt_pk_f16_f32 v137, v86, v87\nv_mfma_f32_16x16x32_f16 v[96:99], v[80:83], v[104:107], 0\nv_cvt_pk_f16_f32 v138, v88, v89\nv_cvt_pk_f16_f32 v139, v90, v91\ns_nop 1\n"
        "v_mfma_f32_16x16x32_f16 v[36:39], v[136:139], v[116:119], v[36:39]\nds_read_b64 v[78:79], %[rb] offset:2016\nds_read_b128 v[80:83], %[ra] offset:2016\n"
        "v_mfma_f32_16x16x32_f16 v[40:43], v[136:139], v[112:115], v[40:43]\nv_mfma_f32_16x16x32_f16 v[44:47], v[136:139], v[108:111], 0\nv_cvt_pk_f16_f32 v140, v92, v93\n"
        "v_cvt_pk_f16_f32 v141, v94, v95\ns_waitcnt lgkmcnt(6)\nv_mfma_f32_16x16x32_f16 v[84:87], v[60:63], v[100:103], 0\nv_cvt_pk_f16_f32 v142, v96, v97\nv_cvt_pk_f16_f32 v143, v98, v99\n"
        "v_mfma_f32_16x16x32_f16 v[88:91], v[62:65], v[104:107], 0\nds_read_b64 v[60:61], %[rb] offset:2304\nds_read_b128 v[62:65], %[ra] offset:2304\n"
        "v_mfma_f32_16x16x32_f16 v[36:39], v[140:143], v[120:123], v[36:39]\nv_mfma_f32_16x16x32_f16 v[40:43], v[140:143], v[116:119], v[40:43]\n"
        "v_mfma_f32_16x16x32_f16 v[44:47], v[140:143], v[112:115], v[44:47]\nv_mfma_f32_16x16x32_f16 v[48:51], v[140:143], v[108:111], 0\nv_cvt_pk_f16_f32 v136, v84, v85\n"
        "v_cvt_pk_f16_f32 v137, v86, v87\ns_waitcnt lgkmcnt(6)\nv_mfma_f32_16x16x32_f16 v[92:95], v[66:69], v[100:103], 0\nv_cvt_pk_f16_f32 v138, v88, v89\nv_cvt_pk_f16_f32 v139, v90, v91\n"
        "v_mfma_f32_16x16x32_f16 v[96:99], v[68:71], v[104:107], 0\nds_read_b64 v[66:67], %[rb] offset:2592\nds_read_b128 v[68:71], %[ra] offset:2592\n"
        "v_mfma_f32_16x16x32_f16 v[36:39], v[136:139], v[124:127], v[36:39]\nv_mfma_f32_16x16x32_f16 v[40:43], v[136:139], v[120:123], v[40:43]\n"
        "v_mfma_f32_16x16x32_f16 v[44:47], v[136:139], v[116:119], v[44:47]\nv_mfma_f32_16x16x32_f16 v[48:51], v[136:139], v[112:115], v[48:51]\nds_write_b128 %[wa], v[36:39] offset:0\n"
        "v_mfma_f32_16x16x32_f16 v[52:55], v[136:139], v[108:111], 0\nv_cvt_pk_f16_f32 v140, v92, v93\nv_cvt_pk_f16_f32 v141, v94, v95\ns_waitcnt lgkmcnt(7)\n"
        "v_mfma_f32_16x16x32_f16 v[84:87], v[72:75], v[100:103], 0\nv_cvt_pk_f16_f32 v142, v96, v97\nv_cvt_pk_f16_f32 v143, v98, v99\nv_mfma_f32_16x16x32_f16 v[88:91], v[74:77], v[104:107], 0\n"
        "s_nop 0\nv_mfma_f32_16x16x32_f16 v[40:43], v[140:143], v[124:127], v[40:43]\nv_mfma_f32_16x16x32_f16 v[44:47], v[140:143], v[120:123], v[44:47]\n"
        "v_mfma_f32_16x16x32_f16 v[48:51], v[140:143], v[116:119], v[48:51]\nv_mfma_f32_16x16x32_f16 v[52:55], v[140:143], v[112:115], v[52:55]\nds_write_b128 %[wa], v[40:43] offset:256\n"
        "v_mfma_f32_16x16x32_f16 v[56:59], v[140:143], v[108:111], 0\nv_cvt_pk_f16_f32 v136, v84, v85\nv_cvt_pk_f16_f32 v137, v86, v87\ns_waitcnt lgkmcnt(6)\n"
        "v_mfma_f32_16x16x32_f16 v[92:95], v[78:81], v[100:103], 0\nv_cvt_pk_f16_f32 v138, v88, v89\nv_cvt_pk_f16_f32 v139, v90, v91\nv_mfma_f32_16x16x32_f16 v[96:99], v[80:83], v[104:107], 0\n"
        "s_nop 0\nv_mfma_f32_16x16x32_f16 v[44:47], v[136:139], v[124:127], v[44:47]\nv_mfma_f32_16x16x32_f16 v[48:51], v[136:139], v[120:123], v[48:51]\n"
        "v_mfma_f32_16x16x32_f16 v[52:55], v[136:139], v[116:119], v[52:55]\nv_mfma_f32_16x16x32_f16 v[56:59], v[136:139], v[112:115], v[56:59]\nds_write_b128 %[wa], v[44:47] offset:512\n"
        "v_cvt_pk_f16_f32 v140, v92, v93\ns_waitcnt lgkmcnt(5)\nv_mfma_f32_16x16x32_f16 v[84:87], v[60:63], v[100:103], 0\nv_cvt_pk_f16_f32 v141, v94, v95\nv_cvt_pk_f16_f32 v142, v96, v97\n"
        "v_mfma_f32_16x16x32_f16 v[88:91], v[62:65], v[104:107], 0\nv_cvt_pk_f16_f32 v143, v98, v99\ns_nop 1\nv_mfma_f32_16x16x32_f16 v[48:51], v[140:143], v[124:127], v[48:51]\n"
        "v_mfma_f32_16x16x32_f16 v[52:55], v[140:143], v[120:123], v[52:55]\nv_mfma_f32_16x16x32_f16 v[56:59], v[140:143], v[116:119], v[56:59]\nv_cvt_pk_f16_f32 v136, v84, v85\n"
        "v_cvt_pk_f16_f32 v137, v86, v87\ns_waitcnt lgkmcnt(3)\nv_mfma_f32_16x16x32_f16 v[92:95], v[66:69], v[100:103], 0\nv_cvt_pk_f16_f32 v138, v88, v89\nv_cvt_pk_f16_f32 v139, v90, v91\n"
        "v_mfma_f32_16x16x32_f16 v[96:99], v[68:71], v[104:107], 0\nds_write_b128 %[wa], v[48:51] offset:768\nv_mfma_f32_16x16x32_f16 v[52:55], v[136:139], v[124:127], v[52:55]\n"
        "v_mfma_f32_16x16x32_f16 v[56:59], v[136:139], v[120:123], v[56:59]\ns_nop 7\nv_cvt_pk_f16_f32 v140, v92, v93\nv_cvt_pk_f16_f32 v141, v94, v95\nv_cvt_pk_f16_f32 v142, v96, v97\n"
        "v_cvt_pk_f16_f32 v143, v98, v99\ns_nop 1\nv_mfma_f32_16x16x32_f16 v[56:59], v[140:143], v[124:127], v[56:59]\ns_nop 7\nds_write_b128 %[wa], v[52:55] offset:1024\n"
        "ds_write_b128 %[wa], v[56:59] offset:1280\ns_branch .LE%=\n.LV%=:\nds_read_b128 v[52:55], %[ra] offset:0\nds_read_b128 v[56:59], %[ra] offset:288\nds_read_b128 v[60:63], %[ra] offset:576\n"
        "ds_read_b128 v[64:67], %[ra] offset:864\nds_read_b128 v[84:87], %[rb] offset:0\nds_read_b128 v[88:91], %[rb] offset:576\nds_read_b128 v[92:95], %[rb] offset:1152\n"
        "ds_read_b128 v[96:99], %[rb] offset:1728\ns_waitcnt lgkmcnt(7)\nv_mfma_f32_16x16x32_f16 v[68:71], v[52:55], v[100:103], 0\nv_mfma_f32_16x16x32_f16 v[72:75], v[52:55], v[104:107], 0\n"
        "ds_read_b128 v[52:55], %[ra] offset:1152\ns_waitcnt lgkmcnt(7)\nv_mfma_f32_16x16x32_f16 v[76:79], v[56:59], v[100:103], 0\nv_mfma_f32_16x16x32_f16 v[80:83], v[56:59], v[104:107], 0\n"
        "ds_read_b128 v[56:59], %[ra] offset:1440\ns_waitcnt lgkmcnt(5)\nv_mfma_f32_16x16x32_f16 v[36:39], v[124:127], v[84:87], 0\nv_cvt_pk_f16_f32 v136, v68, v73\nv_cvt_pk_f16_f32 v137, v70, v75\n"
        "s_waitcnt lgkmcnt(4)\nv_mfma_f32_16x16x32_f16 v[40:43], v[124:127], v[88:91], 0\ns_waitcnt lgkmcnt(3)\nv_mfma_f32_16x16x32_f16 v[44:47], v[124:127], v[92:95], 0\n"
        "v_cvt_pk_f16_f32 v138, v76, v81\nv_cvt_pk_f16_f32 v139, v78, v83\ns_waitcnt lgkmcnt(2)\nv_mfma_f32_16x16x32_f16 v[48:51], v[124:127], v[96:99], 0\n"
        "v_mfma_f32_16x16x32_f16 v[68:71], v[60:63], v[100:103], 0\nv_mfma_f32_16x16x32_f16 v[72:75], v[60:63], v[104:107], 0\nds_read_b128 v[60:63], %[ra] offset:1728\n"
        "v_mfma_f32_16x16x32_f16 v[36:39], v[136:139], v[108:111], v[36:39]\nv_mfma_f32_16x16x32_f16 v[40:43], v[136:139], v[112:115], v[40:43]\n"
        "v_mfma_f32_16x16x32_f16 v[44:47], v[136:139], v[116:119], v[44:47]\nv_cvt_pk_f16_f32 v140, v68, v73\nv_cvt_pk_f16_f32 v141, v70, v75\n"
        "v_mfma_f32_16x16x32_f16 v[48:51], v[136:139], v[120:123], v[48:51]\nds_write_b128 %[wa], v[36:39] offset:0\nv_mfma_f32_16x16x32_f16 v[76:79], v[64:67], v[100:103], 0\n"
        "v_mfma_f32_16x16x32_f16 v[80:83], v[64:67], v[104:107], 0\nds_read_b128 v[64:67], %[ra] offset:2016\ns_waitcnt lgkmcnt(4)\nv_mfma_f32_16x16x32_f16 v[68:71], v[52:55], v[100:103], 0\n"
        "v_mfma_f32_16x16x32_f16 v[72:75], v[52:55], v[104:107], 0\ns_nop 7\nv_cvt_pk_f16_f32 v142, v76, v81\nv_cvt_pk_f16_f32 v143, v78, v83\ns_nop 1\n"
        "v_mfma_f32_16x16x32_f16 v[40:43], v[140:143], v[108:111], v[40:43]\nv_mfma_f32_16x16x32_f16 v[44:47], v[140:143], v[112:115], v[44:47]\n"
        "v_mfma_f32_16x16x32_f16 v[48:51], v[140:143], v[116:119], v[48:51]\nv_cvt_pk_f16_f32 v136, v68, v73\nv_cvt_pk_f16_f32 v137, v70, v75\ns_waitcnt lgkmcnt(3)\n"
        "v_mfma_f32_16x16x32_f16 v[76:79], v[56:59], v[100:103], 0\nds_write_b128 %[wa], v[40:43] offset:256\nv_mfma_f32_16x16x32_f16 v[80:83], v[56:59], v[104:107], 0\ns_waitcnt lgkmcnt(3)\n"
        "v_mfma_f32_16x16x32_f16 v[68:71], v[60:63], v[100:103], 0\nv_mfma_f32_16x16x32_f16 v[72:75], v[60:63], v[104:107], 0\ns_nop 7\nv_cvt_pk_f16_f32 v138, v76, v81\n"
        "v_cvt_pk_f16_f32 v139, v78, v83\ns_nop 1\nv_mfma_f32_16x16x32_f16 v[44:47], v[136:139], v[108:111], v[44:47]\nv_mfma_f32_16x16x32_f16 v[48:51], v[136:139], v[112:115], v[48:51]\n"
        "s_waitcnt lgkmcnt(1)\nv_mfma_f32_16x16x32_f16 v[76:79], v[64:67], v[100:103], 0\nv_cvt_pk_f16_f32 v140, v68, v73\nv_cvt_pk_f16_f32 v141, v70, v75\n"
        "v_mfma_f32_16x16x32_f16 v[80:83], v[64:67], v[104:107], 0\nds_write_b128 %[wa], v[44:47] offset:512\ns_nop 7\nv_cvt_pk_f16_f32 v142, v76, v81\nv_cvt_pk_f16_f32 v143, v78, v83\ns_nop 1\n"
        "v_mfma_f32_16x16x32_f16 v[48:51], v[140:143], v[108:111], v[48:51]\ns_nop 7\nds_write_b128 %[wa], v[48:51] offset:768\ns_branch .LE%=\n.LC%=:\ns_sub_u32 %[cap], %[cap], 1\n"
        "s_cbranch_scc0 .LCy%=\ns_mov_b32 %[cap], 0\ns_branch .LD%=\n.LCy%=:\nbuffer_load_dwordx4 v[36:39], %[c16], %[xrs], 0 offen sc1\ns_waitcnt vmcnt(0)\ns_branch .LB%=\n.LI%=:\n"
        "s_sub_u32 %[cap], %[cap], 1\ns_cbranch_scc0 .LIy%=\ns_mov_b32 %[cap], 0\ns_branch .LM%=\n.LIy%=:\ns_sleep %[fp]\nbuffer_load_dwordx4 v[40:43], %[c16], %[frs], 0 offen sc1\n"
        "buffer_load_dwordx4 v[44:47], %[c16], %[frs], %[s1] offen sc1\ns_branch .LF%=\n.LE%=:\ns_mov_b64 exec, 1\nds_add_u32 %[cna], %[one] offset:8\ns_mov_b64 exec, -1\ns_setprio %[pbe]\n"
        "s_mov_b32 %[s1], 0x100000\n.LP%=:\nds_read_b32 %[pv], %[cna] offset:8\ns_waitcnt lgkmcnt(0)\nv_readfirstlane_b32 %[s0], %[pv]\ns_nop 0\ns_cmp_ge_i32 %[s0], 4\ns_cbranch_scc1 .LR%=\n"
        "s_sub_u32 %[s1], %[s1], 1\ns_cbranch_scc0 .LP%=\n.LR%=:\ns_cmp_lt_u32 %[wq], 2\ns_cbranch_scc1 .LS3_%=\nds_read_b128 v[36:39], %[exr] offset:0\nds_read_b128 v[40:43], %[exr] offset:4096\n"
        "ds_read_b128 v[44:47], %[mr] offset:0\nds_read_b128 v[48:51], %[mr] offset:3072\nds_read_b128 v[52:55], %[frr] offset:0\nds_read_b128 v[56:59], %[frr] offset:2304\ns_waitcnt lgkmcnt(4)\n"
        "v_fmac_f32 v26, v36, %[kf]\nv_fmac_f32 v27, v37, %[kf]\nv_fmac_f32 v28, v38, %[kf]\nv_fmac_f32 v29, v39, %[kf]\nv_fmac_f32 v30, v40, %[kf]\nv_fmac_f32 v31, v41, %[kf]\n"
        "v_fmac_f32 v32, v42, %[kf]\nv_fmac_f32 v33, v43, %[kf]\ns_waitcnt lgkmcnt(2)\nv_fmac_f32 v26, v44, %[kf]\nv_fmac_f32 v27, v45, %[kf]\nv_fmac_f32 v28, v46, %[kf]\nv_fmac_f32 v29, v47, %[kf]\n"
        "v_fmac_f32 v30, v48, %[kf]\nv_fmac_f32 v31, v49, %[kf]\nv_fmac_f32 v32, v50, %[kf]\nv_fmac_f32 v33, v51, %[kf]\ns_waitcnt lgkmcnt(0)\nv_fmac_f32 v26, v52, %[kf]\nv_fmac_f32 v27, v53, %[kf]\n"
        "v_fmac_f32 v28, v54, %[kf]\nv_fmac_f32 v29, v55, %[kf]\nv_fmac_f32 v30, v56, %[kf]\nv_fmac_f32 v31, v57, %[kf]\nv_fmac_f32 v32, v58, %[kf]\nv_fmac_f32 v33, v59, %[kf]\ns_branch .LS%=\n"
        ".LS3_%=:\ns_cmp_lt_u32 %[wq], 1\ns_cbranch_scc1 .LS2_%=\nds_read_b128 v[36:39], %[exr] offset:0\nds_read_b128 v[40:43], %[exr] offset:4096\nds_read_b128 v[44:47], %[mr] offset:0\n"
        "ds_read_b128 v[48:51], %[mr] offset:3072\ns_waitcnt lgkmcnt(2)\nv_fmac_f32 v26, v36, %[kf]\nv_fmac_f32 v27, v37, %[kf]\nv_fmac_f32 v28, v38, %[kf]\nv_fmac_f32 v29, v39, %[kf]\n"
        "v_fmac_f32 v30, v40, %[kf]\nv_fmac_f32 v31, v41, %[kf]\nv_fmac_f32 v32, v42, %[kf]\nv_fmac_f32 v33, v43, %[kf]\ns_waitcnt lgkmcnt(0)\nv_fmac_f32 v26, v44, %[kf]\nv_fmac_f32 v27, v45, %[kf]\n"
        "v_fmac_f32 v28, v46, %[kf]\nv_fmac_f32 v29, v47, %[kf]\nv_fmac_f32 v30, v48, %[kf]\nv_fmac_f32 v31, v49, %[kf]\nv_fmac_f32 v32, v50, %[kf]\nv_fmac_f32 v33, v51, %[kf]\ns_branch .LS%=\n"
        ".LS2_%=:\nds_read_b128 v[36:39], %[exr] offset:0\nds_read_b128 v[40:43], %[exr] offset:4096\ns_waitcnt lgkmcnt(0)\nv_fmac_f32 v26, v36, %[kf]\nv_fmac_f32 v27, v37, %[kf]\n"
        "v_fmac_f32 v28, v38, %[kf]\nv_fmac_f32 v29, v39, %[kf]\nv_fmac_f32 v30, v40, %[kf]\nv_fmac_f32 v31, v41, %[kf]\nv_fmac_f32 v32, v42, %[kf]\nv_fmac_f32 v33, v43, %[kf]\n.LS%=:\n"
        "v_cvt_pk_bf16_f32 v36, v26, v27\nv_cvt_pk_bf16_f32 v37, v28, v29\nv_cvt_pk_bf16_f32 v38, v30, v31\nv_cvt_pk_bf16_f32 v39, v32, v33\nv_min_u32 v36, -2, v36\nv_min_u32 v37, -2, v37\n"
        "v_min_u32 v38, -2, v38\nv_min_u32 v39, -2, v39\nbuffer_store_dwordx4 v[36:39], %[od], %[ors], 0 offen sc0 sc1\ns_nop 1\n.LZ%=:\n"
        : "+{v[100:103]}"(kt[0]), "+{v[104:107]}"(kt[1]), "+{v[108:111]}"(kt[2]), "+{v[112:115]}"(kt[3]), "+{v[116:119]}"(kt[4]), "+{v[120:123]}"(kt[5]), "+{v[124:127]}"(kt[6]), "+{v[128:131]}"(kt[7]), "+{v[132:135]}"(kt[8]), "+{v[136:139]}"(kt[9]), "+{v[140:143]}"(kt[10]), "+{v[144:147]}"(kt[11]), "+{v[148:151]}"(kt[12]), "+{v[152:155]}"(kt[13]), "+{v[156:159]}"(kt[14]), "+{v[160:163]}"(kt[15]), "+{v[164:167]}"(kt[16]), [pv] "=&{v16}"(pv), [s0] "=&s"(s0_), [s1] "=&s"(s1_), [cap] "=&s"(cap)
        : [ra] "{v1}"(ra), [rb] "{v2}"(rb), [wa] "{v3}"(wa), [cna] "{v4}"(cna), [one] "{v5}"(one), [exr] "{v6}"(exr), [frr] "{v7}"(frr), [mr] "{v8}"(mr_), [od] "{v9}"(od), [kf] "{v10}"(kd[0]), [kd1] "{v11}"(kd[1]), [c16] "{v12}"(c16), [uw] "{v17}"(uw_), [gq] "s"(gq), [fsa] "i"(FS1 - FSX), [fsb] "i"(FS0 - FS1), [pbl] "i"(PBL), [v] "s"(v), [wq] "s"(wq), [ors] "s"(ors), [xrs] "s"(xrs), [frs] "s"(frs), [pbe] "i"(PBE), [fsx] "i"(FSX), [fp] "i"(FPOLL)
        : "memory", "scc", "v13", "v14", "v15", "v26", "v27", "v28", "v29", "v30", "v31", "v32", "v33", "v36", "v37", "v38", "v39", "v40", "v41", "v42", "v43", "v44", "v45", "v46", "v47", "v48", "v49", "v50", "v51", "v52", "v53", "v54", "v55", "v56", "v57", "v58", "v59", "v60", "v61", "v62", "v63", "v64", "v65", "v66", "v67", "v68", "v69", "v70", "v71", "v72", "v73", "v74", "v75", "v76", "v77", "v78", "v79", "v80", "v81", "v82", "v83", "v84", "v85", "v86", "v87", "v88", "v89", "v90", "v91", "v92", "v93", "v94", "v95", "v96", "v97", "v98", "v99", "vcc");
    } else if (FULL) {
      asm volatile(
        "s_mov_b32 %[cap], 0x10000\ns_movk_i32 %[s1], 4096\ns_sleep %[fsx]\nbuffer_load_dwordx4 v[36:39], %[c16], %[xrs], 0 offen sc1\nv_mov_b32 v56, -1\nv_mov_b32 v57, -1\nv_mov_b32 v58, -1\n"
        "v_mov_b32 v59, -1\ns_sleep %[fsa]\ns_cmp_eq_u32 %[gq], 0\ns_cbranch_scc0 .LA%=\ns_sleep %[fsb]\n.LA%=:\nbuffer_load_dwordx4 v[40:43], %[c16], %[frs], 0 offen sc1\n"
        "buffer_load_dwordx4 v[44:47], %[c16], %[frs], %[s1] offen sc1\ns_waitcnt vmcnt(2)\n.LB%=:\nv_max3_u32 %[pv], v36, v37, v38\nv_max_u32 %[pv], %[pv], v39\nv_cmp_eq_u32 vcc, -1, %[pv]\n"
        "s_cbranch_vccnz .LC%=\n.LD%=:\nv_lshlrev_b32 v26, 16, v36\nv_and_b32 v27, 0xffff0000, v36\nv_lshlrev_b32 v28, 16, v37\nv_and_b32 v29, 0xffff0000, v37\nv_lshlrev_b32 v30, 16, v38\n"
        "v_and_b32 v31, 0xffff0000, v38\nv_lshlrev_b32 v32, 16, v39\nv_and_b32 v33, 0xffff0000, v39\n.LF%=:\ns_waitcnt vmcnt(1)\nv_max3_u32 %[pv], v40, v41, v42\nv_lshlrev_b32 v48, 16, v40\n"
        "v_lshlrev_b32 v50, 16, v41\nv_lshlrev_b32 v52, 16, v42\nv_lshlrev_b32 v54, 16, v43\nv_fmac_f32 v48, v26, v40\nv_fmac_f32 v50, v28, v41\nv_fmac_f32 v52, v30, v42\nv_fmac_f32 v54, v32, v43\n"
        "s_waitcnt vmcnt(0)\nv_max3_u32 %[pv], %[pv], v43, v44\nv_max3_u32 %[pv], %[pv], v45, v46\nv_max_u32 %[pv], %[pv], v47\nv_cmp_eq_u32 vcc, -1, %[pv]\nv_lshlrev_b32 v49, 16, v44\n"
        "v_lshlrev_b32 v51, 16, v45\nv_lshlrev_b32 v53, 16, v46\nv_lshlrev_b32 v55, 16, v47\ns_cbranch_vccnz .LI%=\n.LM%=:\nv_fmac_f32 v49, v27, v44\nv_fmac_f32 v51, v29, v45\n"
        "v_fmac_f32 v53, v31, v46\nv_fmac_f32 v55, v33, v47\nv_cvt_pk_f16_f32 v36, v48, v49\nv_cvt_pk_f16_f32 v37, v50, v51\nv_cvt_pk_f16_f32 v38, v52, v53\nv_cvt_pk_f16_f32 v39, v54, v55\n"
        "ds_write_b128 %[uw], v[36:39]\ns_mov_b64 exec, 1\nds_add_u32 %[cna], %[one] offset:0\ns_mov_b64 exec, -1\ns_movk_i32 %[s0], 12288\ns_movk_i32 %[s1], 24576\n"
        "buffer_store_dwordx4 v[56:59], %[mo], %[mrs], 0 offen sc0 sc1\nbuffer_store_dwordx4 v[56:59], %[mo], %[mrs], %[s0] offen sc0 sc1\n"
        "buffer_store_dwordx4 v[56:59], %[mo], %[mrs], %[s1] offen sc0 sc1\nv_fmac_f32 v26, v48, %[kd1]\nv_fmac_f32 v27, v49, %[kd1]\nv_fmac_f32 v28, v50, %[kd1]\nv_fmac_f32 v29, v51, %[kd1]\n"
        "v_fmac_f32 v30, v52, %[kd1]\nv_fmac_f32 v31, v53, %[kd1]\nv_fmac_f32 v32, v54, %[kd1]\nv_fmac_f32 v33, v55, %[kd1]\ns_setprio %[pbl]\ns_mov_b32 %[s1], 0x100000\n.LN%=:\n"
        "ds_read_b32 %[pv], %[cna]\ns_waitcnt lgkmcnt(0)\nv_readfirstlane_b32 %[s0], %[pv]\ns_nop 0\ns_cmp_ge_i32 %[s0], 4\ns_cbranch_scc1 .LO%=\ns_sub_u32 %[s1], %[s1], 1\ns_cbranch_scc0 .LN%=\n"
        ".LO%=:\ns_cmp_lt_u32 %[v], 2\ns_cbranch_scc0 .LT%=\nds_read_b128 v[52:55], %[ra] offset:1376\nds_read_b128 v[56:59], %[ra] offset:1312\nds_read_b128 v[60:63], %[ra] offset:1248\n"
        "ds_read_b128 v[64:67], %[rb] offset:1152\nds_read_b128 v[68:71], %[ra] offset:1088\nds_read_b128 v[72:75], %[ra] offset:1024\nds_read_b128 v[76:79], %[ra] offset:960\n"
        "ds_read_b128 v[80:83], %[rb] offset:864\nv_xor_b32 v14, 64, %[wa]\ns_waitcnt lgkmcnt(7)\nv_mfma_f32_16x16x32_f16 v[48:51], v[100:103], v[52:55], 0\nds_read_b128 v[52:55], %[ra] offset:800\n"
        "s_waitcnt lgkmcnt(7)\nv_mfma_f32_16x16x32_f16 v[44:47], v[100:103], v[56:59], 0\ns_waitcnt lgkmcnt(6)\nv_mfma_f32_16x16x32_f16 v[40:43], v[100:103], v[60:63], 0\ns_waitcnt lgkmcnt(5)\n"
        "v_mfma_f32_16x16x32_f16 v[36:39], v[100:103], v[64:67], 0\nv_mfma_f32_16x16x32_f16 v[48:51], v[104:107], v[56:59], v[48:51]\nds_read_b128 v[56:59], %[ra] offset:736\n"
        "v_mfma_f32_16x16x32_f16 v[44:47], v[104:107], v[60:63], v[44:47]\nv_mfma_f32_16x16x32_f16 v[40:43], v[104:107], v[64:67], v[40:43]\ns_waitcnt lgkmcnt(5)\n"
        "v_mfma_f32_16x16x32_f16 v[36:39], v[104:107], v[68:71], v[36:39]\nv_mfma_f32_16x16x32_f16 v[48:51], v[108:111], v[60:63], v[48:51]\nds_read_b128 v[60:63], %[ra] offset:672\n"
        "v_mfma_f32_16x16x32_f16 v[44:47], v[108:111], v[64:67], v[44:47]\nv_mfma_f32_16x16x32_f16 v[40:43], v[108:111], v[68:71], v[40:43]\ns_waitcnt lgkmcnt(5)\n"
        "v_mfma_f32_16x16x32_f16 v[36:39], v[108:111], v[72:75], v[36:39]\nv_mfma_f32_16x16x32_f16 v[48:51], v[112:115], v[64:67], v[48:51]\nds_read_b128 v[64:67], %[rb] offset:576\n"
        "v_mfma_f32_16x16x32_f16 v[44:47], v[112:115], v[68:71], v[44:47]\nv_mfma_f32_16x16x32_f16 v[40:43], v[112:115], v[72:75], v[40:43]\ns_waitcnt lgkmcnt(5)\n"
        "v_mfma_f32_16x16x32_f16 v[36:39], v[112:115], v[76:79], v[36:39]\nv_mfma_f32_16x16x32_f16 v[48:51], v[116:119], v[68:71], v[48:51]\nds_read_b128 v[68:71], %[ra] offset:512\n"
        "v_mfma_f32_16x16x32_f16 v[44:47], v[116:119], v[72:75], v[44:47]\nv_mfma_f32_16x16x32_f16 v[40:43], v[116:119], v[76:79], v[40:43]\ns_waitcnt lgkmcnt(5)\n"
        "v_mfma_f32_16x16x32_f16 v[36:39], v[116:119], v[80:83], v[36:39]\nv_mfma_f32_16x16x32_f16 v[48:51], v[120:123], v[72:75], v[48:51]\nds_read_b128 v[72:75], %[ra] offset:448\n"
        "v_mfma_f32_16x16x32_f16 v[44:47], v[120:123], v[76:79], v[44:47]\nv_mfma_f32_16x16x32_f16 v[40:43], v[120:123], v[80:83], v[40:43]\ns_waitcnt lgkmcnt(5)\n"
        "v_mfma_f32_16x16x32_f16 v[36:39], v[120:123], v[52:55], v[36:39]\nv_mfma_f32_16x16x32_f16 v[48:51], v[124:127], v[76:79], v[48:51]\nds_read_b128 v[76:79], %[ra] offset:384\n"
        "v_mfma_f32_16x16x32_f16 v[44:47], v[124:127], v[80:83], v[44:47]\nv_mfma_f32_16x16x32_f16 v[40:43], v[124:127], v[52:55], v[40:43]\ns_waitcnt lgkmcnt(5)\n"
        "v_mfma_f32_16x16x32_f16 v[36:39], v[124:127], v[56:59], v[36:39]\nv_mfma_f32_16x16x32_f16 v[48:51], v[128:131], v[80:83], v[48:51]\nds_read_b128 v[80:83], %[rb] offset:288\n"
        "v_mfma_f32_16x16x32_f16 v[44:47], v[128:131], v[52:55], v[44:47]\nv_mfma_f32_16x16x32_f16 v[40:43], v[128:131], v[56:59], v[40:43]\ns_waitcnt lgkmcnt(5)\n"
        "v_mfma_f32_16x16x32_f16 v[36:39], v[128:131], v[60:63], v[36:39]\nv_mfma_f32_16x16x32_f16 v[48:51], v[132:135], v[52:55], v[48:51]\nds_read_b128 v[52:55], %[ra] offset:224\n"
        "v_mfma_f32_16x16x32_f16 v[44:47], v[132:135], v[56:59], v[44:47]\nv_mfma_f32_16x16x32_f16 v[40:43], v[132:135], v[60:63], v[40:43]\ns_waitcnt lgkmcnt(5)\n"
        "v_mfma_f32_16x16x32_f16 v[36:39], v[132:135], v[64:67], v[36:39]\nv_mfma_f32_16x16x32_f16 v[48:51], v[136:139], v[56:59], v[48:51]\nds_read_b128 v[56:59], %[ra] offset:160\n"
        "v_mfma_f32_16x16x32_f16 v[44:47], v[136:139], v[60:63], v[44:47]\nv_mfma_f32_16x16x32_f16 v[40:43], v[136:139], v[64:67], v[40:43]\ns_waitcnt lgkmcnt(5)\n"
        "v_mfma_f32_16x16x32_f16 v[36:39], v[136:139], v[68:71], v[36:39]\nv_mfma_f32_16x16x32_f16 v[48:51], v[140:143], v[60:63], v[48:51]\nds_read_b128 v[60:63], %[ra] offset:96\n"
        "v_mfma_f32_16x16x32_f16 v[44:47], v[140:143], v[64:67], v[44:47]\nv_mfma_f32_16x16x32_f16 v[40:43], v[140:143], v[68:71], v[40:43]\ns_waitcnt lgkmcnt(5)\n"
        "v_mfma_f32_16x16x32_f16 v[36:39], v[140:143], v[72:75], v[36:39]\nv_mfma_f32_16x16x32_f16 v[48:51], v[144:147], v[64:67], v[48:51]\nds_read_b128 v[64:67], %[rb] offset:0\n"
        "v_mfma_f32_16x16x32_f16 v[44:47], v[144:147], v[68:71], v[44:47]\nv_mfma_f32_16x16x32_f16 v[40:43], v[144:147], v[72:75], v[40:43]\ns_waitcnt lgkmcnt(5)\n"
        "v_mfma_f32_16x16x32_f16 v[36:39], v[144:147], v[76:79], v[36:39]\nv_mfma_f32_16x16x32_f16 v[48:51], v[148:151], v[68:71], v[48:51]\n"
        "v_mfma_f32_16x16x32_f16 v[44:47], v[148:151], v[72:75], v[44:47]\nv_mfma_f32_16x16x32_f16 v[40:43], v[148:151], v[76:79], v[40:43]\ns_waitcnt lgkmcnt(4)\n"
        "v_mfma_f32_16x16x32_f16 v[36:39], v[148:151], v[80:83], v[36:39]\nv_mfma_f32_16x16x32_f16 v[48:51], v[152:155], v[72:75], v[48:51]\n"
        "v_mfma_f32_16x16x32_f16 v[44:47], v[152:155], v[76:79], v[44:47]\nv_mfma_f32_16x16x32_f16 v[40:43], v[152:155], v[80:83], v[40:43]\ns_waitcnt lgkmcnt(3)\n"
        "v_mfma_f32_16x16x32_f16 v[36:39], v[152:155], v[52:55], v[36:39]\nv_mfma_f32_16x16x32_f16 v[48:51], v[156:159], v[76:79], v[48:51]\n"
        "v_mfma_f32_16x16x32_f16 v[44:47], v[156:159], v[80:83], v[44:47]\nv_mfma_f32_16x16x32_f16 v[40:43], v[156:159], v[52:55], v[40:43]\ns_waitcnt lgkmcnt(2)\n"
        "v_mfma_f32_16x16x32_f16 v[36:39], v[156:159], v[56:59], v[36:39]\nv_mfma_f32_16x16x32_f16 v[48:51], v[160:163], v[80:83], v[48:51]\n"
        "v_mfma_f32_16x16x32_f16 v[44:47], v[160:163], v[52:55], v[44:47]\nv_mfma_f32_16x16x32_f16 v[40:43], v[160:163], v[56:59], v[40:43]\ns_waitcnt lgkmcnt(1)\n"
        "v_mfma_f32_16x16x32_f16 v[36:39], v[160:163], v[60:63], v[36:39]\nv_mfma_f32_16x16x32_f16 v[48:51], v[164:167], v[52:55], v[48:51]\n"
        "v_mfma_f32_16x16x32_f16 v[44:47], v[164:167], v[56:59], v[44:47]\nv_mfma_f32_16x16x32_f16 v[40:43], v[164:167], v[60:63], v[40:43]\ns_waitcnt lgkmcnt(0)\n"
        "v_mfma_f32_16x16x32_f16 v[36:39], v[164:167], v[64:67], v[36:39]\ns_nop 4\nds_write_b128 v14, v[48:51] offset:128\nds_write_b128 %[wa], v[44:47] offset:128\n"
        "ds_write_b128 v14, v[40:43] offset:0\nds_write_b128 %[wa], v[36:39] offset:0\ns_branch .LE%=\n.LT%=:\ns_cmp_eq_u32 %[v], 2\ns_cbranch_scc0 .LV%=\nds_read_b64 v[60:61], %[rb] offset:0\n"
        "ds_read_b128 v[62:65], %[ra] offset:0\nds_read_b64 v[66:67], %[rb] offset:288\nds_read_b128 v[68:71], %[ra] offset:288\nds_read_b64 v[72:73], %[rb] offset:576\n"
        "ds_read_b128 v[74:77], %[ra] offset:576\nds_read_b64 v[78:79], %[rb] offset:864\nds_read_b128 v[80:83], %[ra] offset:864\ns_waitcnt lgkmcnt(6)\n"
        "v_mfma_f32_16x16x32_f16 v[84:87], v[60:63], v[100:103], 0\nv_mfma_f32_16x16x32_f16 v[88:91], v[62:65], v[104:107], 0\nds_read_b64 v[60:61], %[rb] offset:1152\n"
        "ds_read_b128 v[62:65], %[ra] offset:1152\ns_waitcnt lgkmcnt(6)\nv_mfma_f32_16x16x32_f16 v[92:95], v[66:69], v[100:103], 0\nv_mfma_f32_16x16x32_f16 v[96:99], v[68:71], v[104:107], 0\n"
        "ds_read_b64 v[66:67], %[rb] offset:1440\nds_read_b128 v[68:71], %[ra] offset:1440\ns_nop 7\nv_cvt_pk_f16_f32 v136, v84, v85\nv_cvt_pk_f16_f32 v137, v86, v87\nv_cvt_pk_f16_f32 v138, v88, v89\n"
        "v_cvt_pk_f16_f32 v139, v90, v91\ns_nop 1\nv_mfma_f32_16x16x32_f16 v[36:39], v[136:139], v[108:111], 0\ns_waitcnt lgkmcnt(6)\nv_mfma_f32_16x16x32_f16 v[84:87], v[72:75], v[100:103], 0\n"
        "v_mfma_f32_16x16x32_f16 v[88:91], v[74:77], v[104:107], 0\nds_read_b64 v[72:73], %[rb] offset:1728\nds_read_b128 v[74:77], %[ra] offset:1728\nv_cvt_pk_f16_f32 v140, v92, v93\n"
        "v_cvt_pk_f16_f32 v141, v94, v95\nv_cvt_pk_f16_f32 v142, v96, v97\nv_cvt_pk_f16_f32 v143, v98, v99\ns_nop 1\nv_mfma_f32_16x16x32_f16 v[36:39], v[140:143], v[112:115], v[36:39]\n"
        "v_mfma_f32_16x16x32_f16 v[40:43], v[140:143], v[108:111], 0\ns_waitcnt lgkmcnt(6)\nv_mfma_f32_16x16x32_f16 v[92:95], v[78:81], v[100:103], 0\nv_cvt_pk_f16_f32 v136, v84, v85\n"
        "v_cvt_pk_f16_f32 v137, v86, v87\nv_mfma_f32_16x16x32_f16 v[96:99], v[80:83], v[104:107], 0\nv_cvt_pk_f16_f32 v138, v88, v89\nv_cvt_pk_f16_f32 v139, v90, v91\ns_nop 1\n"
        "v_mfma_f32_16x16x32_f16 v[36:39], v[136:139], v[116:119], v[36:39]\nds_read_b64 v[78:79], %[rb] offset:2016\nds_read_b128 v[80:83], %[ra] offset:2016\n"
        "v_mfma_f32_16x16x32_f16 v[40:43], v[136:139], v[112:115], v[40:43]\nv_mfma_f32_16x16x32_f16 v[44:47], v[136:139], v[108:111], 0\nv_cvt_pk_f16_f32 v140, v92, v93\n"
        "v_cvt_pk_f16_f32 v141, v94, v95\ns_waitcnt lgkmcnt(6)\nv_mfma_f32_16x16x32_f16 v[84:87], v[60:63], v[100:103], 0\nv_cvt_pk_f16_f32 v142, v96, v97\nv_cvt_pk_f16_f32 v143, v98, v99\n"
        "v_mfma_f32_16x16x32_f16 v[88:91], v[62:65], v[104:107], 0\nds_read_b64 v[60:61], %[rb] offset:2304\nds_read_b128 v[62:65], %[ra] offset:2304\n"
        "v_mfma_f32_16x16x32_f16 v[36:39], v[140:143], v[120:123], v[36:39]\nv_mfma_f32_16x16x32_f16 v[40:43], v[140:143], v[116:119], v[40:43]\n"
        "v_mfma_f32_16x16x32_f16 v[44:47], v[140:143], v[112:115], v[44:47]\nv_mfma_f32_16x16x32_f16 v[48:51], v[140:143], v[108:111], 0\nv_cvt_pk_f16_f32 v136, v84, v85\n"
        "v_cvt_pk_f16_f32 v137, v86, v87\ns_waitcnt lgkmcnt(6)\nv_mfma_f32_16x16x32_f16 v[92:95], v[66:69], v[100:103], 0\nv_cvt_pk_f16_f32 v138, v88, v89\nv_cvt_pk_f16_f32 v139, v90, v91\n"
        "v_mfma_f32_16x16x32_f16 v[96:99], v[68:71], v[104:107], 0\nds_read_b64 v[66:67], %[rb] offset:2592\nds_read_b128 v[68:71], %[ra] offset:2592\n"
        "v_mfma_f32_16x16x32_f16 v[36:39], v[136:139], v[124:127], v[36:39]\nv_mfma_f32_16x16x32_f16 v[40:43], v[136:139], v[120:123], v[40:43]\n"
        "v_mfma_f32_16x16x32_f16 v[44:47], v[136:139], v[116:119], v[44:47]\nv_mfma_f32_16x16x32_f16 v[48:51], v[136:139], v[112:115], v[48:51]\nds_write_b128 %[wa], v[36:39] offset:0\n"
        "v_mfma_f32_16x16x32_f16 v[52:55], v[136:139], v[108:111], 0\nv_cvt_pk_f16_f32 v140, v92, v93\nv_cvt_pk_f16_f32 v141, v94, v95\ns_waitcnt lgkmcnt(7)\n"
        "v_mfma_f32_16x16x32_f16 v[84:87], v[72:75], v[100:103], 0\nv_cvt_pk_f16_f32 v142, v96, v97\nv_cvt_pk_f16_f32 v143, v98, v99\nv_mfma_f32_16x16x32_f16 v[88:91], v[74:77], v[104:107], 0\n"
        "s_nop 0\nv_mfma_f32_16x16x32_f16 v[40:43], v[140:143], v[124:127], v[40:43]\nv_mfma_f32_16x16x32_f16 v[44:47], v[140:143], v[120:123], v[44:47]\n"
        "v_mfma_f32_16x16x32_f16 v[48:51], v[140:143], v[116:119], v[48:51]\nv_mfma_f32_16x16x32_f16 v[52:55], v[140:143], v[112:115], v[52:55]\nds_write_b128 %[wa], v[40:43] offset:256\n"
        "v_mfma_f32_16x16x32_f16 v[56:59], v[140:143], v[108:111], 0\nv_cvt_pk_f16_f32 v136, v84, v85\nv_cvt_pk_f16_f32 v137, v86, v87\ns_waitcnt lgkmcnt(6)\n"
        "v_mfma_f32_16x16x32_f16 v[92:95], v[78:81], v[100:103], 0\nv_cvt_pk_f16_f32 v138, v88, v89\nv_cvt_pk_f16_f32 v139, v90, v91\nv_mfma_f32_16x16x32_f16 v[96:99], v[80:83], v[104:107], 0\n"
        "s_nop 0\nv_mfma_f32_16x16x32_f16 v[44:47], v[136:139], v[124:127], v[44:47]\nv_mfma_f32_16x16x32_f16 v[48:51], v[136:139], v[120:123], v[48:51]\n"
        "v_mfma_f32_16x16x32_f16 v[52:55], v[136:139], v[116:119], v[52:55]\nv_mfma_f32_16x16x32_f16 v[56:59], v[136:139], v[112:115], v[56:59]\nds_write_b128 %[wa], v[44:47] offset:512\n"
        "v_cvt_pk_f16_f32 v140, v92, v93\ns_waitcnt lgkmcnt(5)\nv_mfma_f32_16x16x32_f16 v[84:87], v[60:63], v[100:103], 0\nv_cvt_pk_f16_f32 v141, v94, v95\nv_cvt_pk_f16_f32 v142, v96, v97\n"
        "v_mfma_f32_16x16x32_f16 v[88:91], v[62:65], v[104:107], 0\nv_cvt_pk_f16_f32 v143, v98, v99\ns_nop 1\nv_mfma_f32_16x16x32_f16 v[48:51], v[140:143], v[124:127], v[48:51]\n"
        "v_mfma_f32_16x16x32_f16 v[52:55], v[140:143], v[120:123], v[52:55]\nv_mfma_f32_16x16x32_f16 v[56:59], v[140:143], v[116:119], v[56:59]\nv_cvt_pk_f16_f32 v136, v84, v85\n"
        "v_cvt_pk_f16_f32 v137, v86, v87\ns_waitcnt lgkmcnt(3)\nv_mfma_f32_16x16x32_f16 v[92:95], v[66:69], v[100:103], 0\nv_cvt_pk_f16_f32 v138, v88, v89\nv_cvt_pk_f16_f32 v139, v90, v91\n"
        "v_mfma_f32_16x16x32_f16 v[96:99], v[68:71], v[104:107], 0\nds_write_b128 %[wa], v[48:51] offset:768\nv_mfma_f32_16x16x32_f16 v[52:55], v[136:139], v[124:127], v[52:55]\n"
        "v_mfma_f32_16x16x32_f16 v[56:59], v[136:139], v[120:123], v[56:59]\ns_nop 7\nv_cvt_pk_f16_f32 v140, v92, v93\nv_cvt_pk_f16_f32 v141, v94, v95\nv_cvt_pk_f16_f32 v142, v96, v97\n"
        "v_cvt_pk_f16_f32 v143, v98, v99\ns_nop 1\nv_mfma_f32_16x16x32_f16 v[56:59], v[140:143], v[124:127], v[56:59]\ns_nop 7\nds_write_b128 %[wa], v[52:55] offset:1024\n"
        "ds_write_b128 %[wa], v[56:59] offset:1280\ns_branch .LE%=\n.LV%=:\nds_read_b128 v[52:55], %[ra] offset:0\nds_read_b128 v[56:59], %[ra] offset:288\nds_read_b128 v[60:63], %[ra] offset:576\n"
        "ds_read_b128 v[64:67], %[ra] offset:864\nds_read_b128 v[84:87], %[rb] offset:0\nds_read_b128 v[88:91], %[rb] offset:576\nds_read_b128 v[92:95], %[rb] offset:1152\n"
        "ds_read_b128 v[96:99], %[rb] offset:1728\ns_waitcnt lgkmcnt(7)\nv_mfma_f32_16x16x32_f16 v[68:71], v[52:55], v[100:103], 0\nv_mfma_f32_16x16x32_f16 v[72:75], v[52:55], v[104:107], 0\n"
        "ds_read_b128 v[52:55], %[ra] offset:1152\ns_waitcnt lgkmcnt(7)\nv_mfma_f32_16x16x32_f16 v[76:79], v[56:59], v[100:103], 0\nv_mfma_f32_16x16x32_f16 v[80:83], v[56:59], v[104:107], 0\n"
        "ds_read_b128 v[56:59], %[ra] offset:1440\ns_waitcnt lgkmcnt(5)\nv_mfma_f32_16x16x32_f16 v[36:39], v[124:127], v[84:87], 0\nv_cvt_pk_f16_f32 v136, v68, v73\nv_cvt_pk_f16_f32 v137, v70, v75\n"
        "s_waitcnt lgkmcnt(4)\nv_mfma_f32_16x16x32_f16 v[40:43], v[124:127], v[88:91], 0\ns_waitcnt lgkmcnt(3)\nv_mfma_f32_16x16x32_f16 v[44:47], v[124:127], v[92:95], 0\n"
        "v_cvt_pk_f16_f32 v138, v76, v81\nv_cvt_pk_f16_f32 v139, v78, v83\ns_waitcnt lgkmcnt(2)\nv_mfma_f32_16x16x32_f16 v[48:51], v[124:127], v[96:99], 0\n"
        "v_mfma_f32_16x16x32_f16 v[68:71], v[60:63], v[100:103], 0\nv_mfma_f32_16x16x32_f16 v[72:75], v[60:63], v[104:107], 0\nds_read_b128 v[60:63], %[ra] offset:1728\n"
        "v_mfma_f32_16x16x32_f16 v[36:39], v[136:139], v[108:111], v[36:39]\nv_mfma_f32_16x16x32_f16 v[40:43], v[136:139], v[112:115], v[40:43]\n"
        "v_mfma_f32_16x16x32_f16 v[44:47], v[136:139], v[116:119], v[44:47]\nv_cvt_pk_f16_f32 v140, v68, v73\nv_cvt_pk_f16_f32 v141, v70, v75\n"
        "v_mfma_f32_16x16x32_f16 v[48:51], v[136:139], v[120:123], v[48:51]\nds_write_b128 %[wa], v[36:39] offset:0\nv_mfma_f32_16x16x32_f16 v[76:79], v[64:67], v[100:103], 0\n"
        "v_mfma_f32_16x16x32_f16 v[80:83], v[64:67], v[104:107], 0\nds_read_b128 v[64:67], %[ra] offset:2016\ns_waitcnt lgkmcnt(4)\nv_mfma_f32_16x16x32_f16 v[68:71], v[52:55], v[100:103], 0\n"
        "v_mfma_f32_16x16x32_f16 v[72:75], v[52:55], v[104:107], 0\ns_nop 7\nv_cvt_pk_f16_f32 v142, v76, v81\nv_cvt_pk_f16_f32 v143, v78, v83\ns_nop 1\n"
        "v_mfma_f32_16x16x32_f16 v[40:43], v[140:143], v[108:111], v[40:43]\nv_mfma_f32_16x16x32_f16 v[44:47], v[140:143], v[112:115], v[44:47]\n"
        "v_mfma_f32_16x16x32_f16 v[48:51], v[140:143], v[116:119], v[48:51]\nv_cvt_pk_f16_f32 v136, v68, v73\nv_cvt_pk_f16_f32 v137, v70, v75\ns_waitcnt lgkmcnt(3)\n"
        "v_mfma_f32_16x16x32_f16 v[76:79], v[56:59], v[100:103], 0\nds_write_b128 %[wa], v[40:43] offset:256\nv_mfma_f32_16x16x32_f16 v[80:83], v[56:59], v[104:107], 0\ns_waitcnt lgkmcnt(3)\n"
        "v_mfma_f32_16x16x32_f16 v[68:71], v[60:63], v[100:103], 0\nv_mfma_f32_16x16x32_f16 v[72:75], v[60:63], v[104:107], 0\ns_nop 7\nv_cvt_pk_f16_f32 v138, v76, v81\n"
        "v_cvt_pk_f16_f32 v139, v78, v83\ns_nop 1\nv_mfma_f32_16x16x32_f16 v[44:47], v[136:139], v[108:111], v[44:47]\nv_mfma_f32_16x16x32_f16 v[48:51], v[136:139], v[112:115], v[48:51]\n"
        "s_waitcnt lgkmcnt(1)\nv_mfma_f32_16x16x32_f16 v[76:79], v[64:67], v[100:103], 0\nv_cvt_pk_f16_f32 v140, v68, v73\nv_cvt_pk_f16_f32 v141, v70, v75\n"
        "v_mfma_f32_16x16x32_f16 v[80:83], v[64:67], v[104:107], 0\nds_write_b128 %[wa], v[44:47] offset:512\ns_nop 7\nv_cvt_pk_f16_f32 v142, v76, v81\nv_cvt_pk_f16_f32 v143, v78, v83\ns_nop 1\n"
        "v_mfma_f32_16x16x32_f16 v[48:51], v[140:143], v[108:111], v[48:51]\ns_nop 7\nds_write_b128 %[wa], v[48:51] offset:768\ns_branch .LE%=\n.LC%=:\ns_sub_u32 %[cap], %[cap], 1\n"
        "s_cbranch_scc0 .LCy%=\ns_mov_b32 %[cap], 0\ns_branch .LD%=\n.LCy%=:\nbuffer_load_dwordx4 v[36:39], %[c16], %[xrs], 0 offen sc1\ns_waitcnt vmcnt(0)\ns_branch .LB%=\n.LI%=:\n"
        "s_sub_u32 %[cap], %[cap], 1\ns_cbranch_scc0 .LIy%=\ns_mov_b32 %[cap], 0\ns_branch .LM%=\n.LIy%=:\ns_sleep %[fp]\nbuffer_load_dwordx4 v[40:43], %[c16], %[frs], 0 offen sc1\n"
        "buffer_load_dwordx4 v[44:47], %[c16], %[frs], %[s1] offen sc1\ns_branch .LF%=\n.LE%=:\ns_waitcnt vmcnt(0)\ns_mov_b64 exec, 1\nds_add_u32 %[cna], %[one] offset:8\ns_mov_b64 exec, -1\n"
        "s_setprio %[pbe]\ns_mov_b32 %[s1], 0x100000\n.LP%=:\nds_read_b32 %[pv], %[cna] offset:8\ns_waitcnt lgkmcnt(0)\nv_readfirstlane_b32 %[s0], %[pv]\ns_nop 0\ns_cmp_ge_i32 %[s0], 4\n"
        "s_cbranch_scc1 .LR%=\ns_sub_u32 %[s1], %[s1], 1\ns_cbranch_scc0 .LP%=\n.LR%=:\ns_cmp_lt_u32 %[wq], 2\ns_cbranch_scc1 .LS3_%=\nds_read_b128 v[36:39], %[exr] offset:0\n"
        "ds_read_b128 v[40:43], %[exr] offset:4096\nds_read_b128 v[44:47], %[mr] offset:0\nds_read_b128 v[48:51], %[mr] offset:3072\nds_read_b128 v[52:55], %[frr] offset:0\n"
        "ds_read_b128 v[56:59], %[frr] offset:2304\ns_waitcnt lgkmcnt(4)\nv_fmac_f32 v26, v36, %[kf]\nv_fmac_f32 v27, v37, %[kf]\nv_fmac_f32 v28, v38, %[kf]\nv_fmac_f32 v29, v39, %[kf]\n"
        "v_fmac_f32 v30, v40, %[kf]\nv_fmac_f32 v31, v41, %[kf]\nv_fmac_f32 v32, v42, %[kf]\nv_fmac_f32 v33, v43, %[kf]\ns_waitcnt lgkmcnt(2)\nv_fmac_f32 v26, v44, %[kf]\nv_fmac_f32 v27, v45, %[kf]\n"
        "v_fmac_f32 v28, v46, %[kf]\nv_fmac_f32 v29, v47, %[kf]\nv_fmac_f32 v30, v48, %[kf]\nv_fmac_f32 v31, v49, %[kf]\nv_fmac_f32 v32, v50, %[kf]\nv_fmac_f32 v33, v51, %[kf]\ns_waitcnt lgkmcnt(0)\n"
        "v_fmac_f32 v26, v52, %[kf]\nv_fmac_f32 v27, v53, %[kf]\nv_fmac_f32 v28, v54, %[kf]\nv_fmac_f32 v29, v55, %[kf]\nv_fmac_f32 v30, v56, %[kf]\nv_fmac_f32 v31, v57, %[kf]\n"
        "v_fmac_f32 v32, v58, %[kf]\nv_fmac_f32 v33, v59, %[kf]\ns_branch .LS%=\n.LS3_%=:\ns_cmp_lt_u32 %[wq], 1\ns_cbranch_scc1 .LS2_%=\nds_read_b128 v[36:39], %[exr] offset:0\n"
        "ds_read_b128 v[40:43], %[exr] offset:4096\nds_read_b128 v[44:47], %[mr] offset:0\nds_read_b128 v[48:51], %[mr] offset:3072\ns_waitcnt lgkmcnt(2)\nv_fmac_f32 v26, v36, %[kf]\n"
        "v_fmac_f32 v27, v37, %[kf]\nv_fmac_f32 v28, v38, %[kf]\nv_fmac_f32 v29, v39, %[kf]\nv_fmac_f32 v30, v40, %[kf]\nv_fmac_f32 v31, v41, %[kf]\nv_fmac_f32 v32, v42, %[kf]\n"
        "v_fmac_f32 v33, v43, %[kf]\ns_waitcnt lgkmcnt(0)\nv_fmac_f32 v26, v44, %[kf]\nv_fmac_f32 v27, v45, %[kf]\nv_fmac_f32 v28, v46, %[kf]\nv_fmac_f32 v29, v47, %[kf]\nv_fmac_f32 v30, v48, %[kf]\n"
        "v_fmac_f32 v31, v49, %[kf]\nv_fmac_f32 v32, v50, %[kf]\nv_fmac_f32 v33, v51, %[kf]\ns_branch .LS%=\n.LS2_%=:\nds_read_b128 v[36:39], %[exr] offset:0\n"
        "ds_read_b128 v[40:43], %[exr] offset:4096\ns_waitcnt lgkmcnt(0)\nv_fmac_f32 v26, v36, %[kf]\nv_fmac_f32 v27, v37, %[kf]\nv_fmac_f32 v28, v38, %[kf]\nv_fmac_f32 v29, v39, %[kf]\n"
        "v_fmac_f32 v30, v40, %[kf]\nv_fmac_f32 v31, v41, %[kf]\nv_fmac_f32 v32, v42, %[kf]\nv_fmac_f32 v33, v43, %[kf]\n.LS%=:\nv_cvt_pk_bf16_f32 v36, v26, v27\nv_cvt_pk_bf16_f32 v37, v28, v29\n"
        "v_cvt_pk_bf16_f32 v38, v30, v31\nv_cvt_pk_bf16_f32 v39, v32, v33\nv_min_u32 v36, -2, v36\nv_min_u32 v37, -2, v37\nv_min_u32 v38, -2, v38\nv_min_u32 v39, -2, v39\n"
        "buffer_store_dwordx4 v[36:39], %[od], %[ors], 0 offen sc0 sc1\nbuffer_store_dwordx4 v[36:39], %[oh], %[ors], 0 offen sc0 sc1\ns_nop 1\n.LZ%=:\n"
        : "+{v[100:103]}"(kt[0]), "+{v[104:107]}"(kt[1]), "+{v[108:111]}"(kt[2]), "+{v[112:115]}"(kt[3]), "+{v[116:119]}"(kt[4]), "+{v[120:123]}"(kt[5]), "+{v[124:127]}"(kt[6]), "+{v[128:131]}"(kt[7]), "+{v[132:135]}"(kt[8]), "+{v[136:139]}"(kt[9]), "+{v[140:143]}"(kt[10]), "+{v[144:147]}"(kt[11]), "+{v[148:151]}"(kt[12]), "+{v[152:155]}"(kt[13]), "+{v[156:159]}"(kt[14]), "+{v[160:163]}"(kt[15]), "+{v[164:167]}"(kt[16]), [pv] "=&{v16}"(pv), [s0] "=&s"(s0_), [s1] "=&s"(s1_), [cap] "=&s"(cap)
        : [ra] "{v1}"(ra), [rb] "{v2}"(rb), [wa] "{v3}"(wa), [cna] "{v4}"(cna), [one] "{v5}"(one), [exr] "{v6}"(exr), [frr] "{v7}"(frr), [mr] "{v8}"(mr_), [od] "{v9}"(od), [kf] "{v10}"(kd[0]), [kd1] "{v11}"(kd[1]), [c16] "{v12}"(c16), [uw] "{v17}"(uw_), [oh] "{v18}"(oh), [mo] "{v19}"(mo), [mrs] "s"(mrs), [gq] "s"(gq), [fsa] "i"(FS1 - FSX), [fsb] "i"(FS0 - FS1), [pbl] "i"(PBL), [v] "s"(v), [wq] "s"(wq), [ors] "s"(ors), [xrs] "s"(xrs), [frs] "s"(frs), [pbe] "i"(PBE), [fsx] "i"(FSX), [fp] "i"(FPOLL)
        : "memory", "scc", "v13", "v14", "v15", "v26", "v27", "v28", "v29", "v30", "v31", "v32", "v33", "v36", "v37", "v38", "v39", "v40", "v41", "v42", "v43", "v44", "v45", "v46", "v47", "v48", "v49", "v50", "v51", "v52", "v53", "v54", "v55", "v56", "v57", "v58", "v59", "v60", "v61", "v62", "v63", "v64", "v65", "v66", "v67", "v68", "v69", "v70", "v71", "v72", "v73", "v74", "v75", "v76", "v77", "v78", "v79", "v80", "v81", "v82", "v83", "v84", "v85", "v86", "v87", "v88", "v89", "v90", "v91", "v92", "v93", "v94", "v95", "v96", "v97", "v98", "v99", "vcc");
    } else if (layer) {
      asm volatile(
        "s_mov_b32 %[cap], 0x10000\ns_movk_i32 %[s1], 4096\ns_sleep %[fsx]\nbuffer_load_dwordx4 v[36:39], %[c16], %[xrs], 0 offen sc1\ns_sleep %[fsa]\n"
        "buffer_load_dwordx4 v[40:43], %[c16], %[frs], 0 offen sc1\nbuffer_load_dwordx4 v[44:47], %[c16], %[frs], %[s1] offen sc1\ns_waitcnt vmcnt(2)\n.LB%=:\nv_max3_u32 %[pv], v36, v37, v38\n"
        "v_max_u32 %[pv], %[pv], v39\nv_cmp_eq_u32 vcc, -1, %[pv]\ns_cbranch_vccnz .LC%=\n.LD%=:\nv_lshlrev_b32 v26, 16, v36\nv_and_b32 v27, 0xffff0000, v36\nv_lshlrev_b32 v28, 16, v37\n"
        "v_and_b32 v29, 0xffff0000, v37\nv_lshlrev_b32 v30, 16, v38\nv_and_b32 v31, 0xffff0000, v38\nv_lshlrev_b32 v32, 16, v39\nv_and_b32 v33, 0xffff0000, v39\n.LF%=:\ns_waitcnt vmcnt(1)\n"
        "v_max3_u32 %[pv], v40, v41, v42\nv_lshlrev_b32 v48, 16, v40\nv_lshlrev_b32 v50, 16, v41\nv_lshlrev_b32 v52, 16, v42\nv_lshlrev_b32 v54, 16, v43\nv_fmac_f32 v48, v26, v40\n"
        "v_fmac_f32 v50, v28, v41\nv_fmac_f32 v52, v30, v42\nv_fmac_f32 v54, v32, v43\ns_waitcnt vmcnt(0)\nv_max3_u32 %[pv], %[pv], v43, v44\nv_max3_u32 %[pv], %[pv], v45, v46\n"
        "v_max_u32 %[pv], %[pv], v47\nv_cmp_eq_u32 vcc, -1, %[pv]\nv_lshlrev_b32 v49, 16, v44\nv_lshlrev_b32 v51, 16, v45\nv_lshlrev_b32 v53, 16, v46\nv_lshlrev_b32 v55, 16, v47\n"
        "s_cbranch_vccnz .LI%=\n.LM%=:\nv_fmac_f32 v49, v27, v44\nv_fmac_f32 v51, v29, v45\nv_fmac_f32 v53, v31, v46\nv_fmac_f32 v55, v33, v47\nv_cvt_pk_f16_f32 v36, v48, v49\n"
        "v_cvt_pk_f16_f32 v37, v50, v51\nv_cvt_pk_f16_f32 v38, v52, v53\nv_cvt_pk_f16_f32 v39, v54, v55\nds_write_b128 %[uw], v[36:39]\ns_mov_b64 exec, 1\nds_add_u32 %[cna], %[one] offset:0\n"
        "s_mov_b64 exec, -1\ns_setprio %[pbl]\ns_mov_b32 %[s1], 0x100000\n.LN%=:\nds_read_b32 %[pv], %[cna]\ns_waitcnt lgkmcnt(0)\nv_readfirstlane_b32 %[s0], %[pv]\ns_nop 0\ns_cmp_ge_i32 %[s0], 4\n"
        "s_cbranch_scc1 .LO%=\ns_sub_u32 %[s1], %[s1], 1\ns_cbranch_scc0 .LN%=\n.LO%=:\nbuffer_load_dwordx4 v[152:155], %[xo], %[xrs], 0 offen sc1\nds_read_b128 v[156:159], %[ue]\n"
        "s_cmp_lt_u32 %[v], 2\ns_cbranch_scc0 .LT%=\nds_read_b128 v[52:55], %[ra] offset:800\nds_read_b128 v[56:59], %[ra] offset:736\nds_read_b128 v[60:63], %[ra] offset:672\n"
        "ds_read_b128 v[64:67], %[rb] offset:576\nds_read_b128 v[68:71], %[ra] offset:512\nds_read_b128 v[72:75], %[ra] offset:448\nds_read_b128 v[76:79], %[ra] offset:384\n"
        "ds_read_b128 v[80:83], %[rb] offset:288\nv_xor_b32 v14, 32, %[wa]\nv_xor_b32 v15, 64, %[wa]\nv_xor_b32 v13, 96, %[wa]\ns_waitcnt lgkmcnt(7)\n"
        "v_mfma_f32_16x16x32_f16 v[48:51], v[100:103], v[52:55], 0\nds_read_b128 v[52:55], %[ra] offset:224\ns_waitcnt lgkmcnt(7)\nv_mfma_f32_16x16x32_f16 v[44:47], v[100:103], v[56:59], 0\n"
        "s_waitcnt lgkmcnt(6)\nv_mfma_f32_16x16x32_f16 v[40:43], v[100:103], v[60:63], 0\ns_waitcnt lgkmcnt(5)\nv_mfma_f32_16x16x32_f16 v[36:39], v[100:103], v[64:67], 0\n"
        "v_mfma_f32_16x16x32_f16 v[48:51], v[104:107], v[56:59], v[48:51]\nds_read_b128 v[56:59], %[ra] offset:160\nv_mfma_f32_16x16x32_f16 v[44:47], v[104:107], v[60:63], v[44:47]\n"
        "v_mfma_f32_16x16x32_f16 v[40:43], v[104:107], v[64:67], v[40:43]\ns_waitcnt lgkmcnt(5)\nv_mfma_f32_16x16x32_f16 v[36:39], v[104:107], v[68:71], v[36:39]\n"
        "v_mfma_f32_16x16x32_f16 v[48:51], v[108:111], v[60:63], v[48:51]\nds_read_b128 v[60:63], %[ra] offset:96\nv_mfma_f32_16x16x32_f16 v[44:47], v[108:111], v[64:67], v[44:47]\n"
        "v_mfma_f32_16x16x32_f16 v[40:43], v[108:111], v[68:71], v[40:43]\ns_waitcnt lgkmcnt(5)\nv_mfma_f32_16x16x32_f16 v[36:39], v[108:111], v[72:75], v[36:39]\n"
        "v_mfma_f32_16x16x32_f16 v[48:51], v[112:115], v[64:67], v[48:51]\nds_read_b128 v[64:67], %[rb] offset:0\nv_mfma_f32_16x16x32_f16 v[44:47], v[112:115], v[68:71], v[44:47]\n"
        "v_mfma_f32_16x16x32_f16 v[40:43], v[112:115], v[72:75], v[40:43]\ns_waitcnt lgkmcnt(5)\nv_mfma_f32_16x16x32_f16 v[36:39], v[112:115], v[76:79], v[36:39]\n"
        "v_mfma_f32_16x16x32_f16 v[48:51], v[116:119], v[68:71], v[48:51]\nv_mfma_f32_16x16x32_f16 v[44:47], v[116:119], v[72:75], v[44:47]\n"
        "v_mfma_f32_16x16x32_f16 v[40:43], v[116:119], v[76:79], v[40:43]\ns_waitcnt lgkmcnt(4)\nv_mfma_f32_16x16x32_f16 v[36:39], v[116:119], v[80:83], v[36:39]\n"
        "v_mfma_f32_16x16x32_f16 v[48:51], v[120:123], v[72:75], v[48:51]\nv_mfma_f32_16x16x32_f16 v[44:47], v[120:123], v[76:79], v[44:47]\n"
        "v_mfma_f32_16x16x32_f16 v[40:43], v[120:123], v[80:83], v[40:43]\ns_waitcnt lgkmcnt(3)\nv_mfma_f32_16x16x32_f16 v[36:39], v[120:123], v[52:55], v[36:39]\n"
        "v_mfma_f32_16x16x32_f16 v[48:51], v[124:127], v[76:79], v[48:51]\nv_mfma_f32_16x16x32_f16 v[44:47], v[124:127], v[80:83], v[44:47]\n"
        "v_mfma_f32_16x16x32_f16 v[40:43], v[124:127], v[52:55], v[40:43]\ns_waitcnt lgkmcnt(2)\nv_mfma_f32_16x16x32_f16 v[36:39], v[124:127], v[56:59], v[36:39]\n"
        "v_mfma_f32_16x16x32_f16 v[48:51], v[128:131], v[80:83], v[48:51]\nv_mfma_f32_16x16x32_f16 v[44:47], v[128:131], v[52:55], v[44:47]\n"
        "v_mfma_f32_16x16x32_f16 v[40:43], v[128:131], v[56:59], v[40:43]\ns_waitcnt lgkmcnt(1)\nv_mfma_f32_16x16x32_f16 v[36:39], v[128:131], v[60:63], v[36:39]\ns_cmp_eq_u32 %[v], 0\n"
        "s_cbranch_scc1 .LU%=\nv_mfma_f32_16x16x32_f16 v[48:51], v[132:135], v[52:55], v[48:51]\nv_mfma_f32_16x16x32_f16 v[44:47], v[132:135], v[56:59], v[44:47]\n"
        "v_mfma_f32_16x16x32_f16 v[40:43], v[132:135], v[60:63], v[40:43]\ns_waitcnt lgkmcnt(0)\nv_mfma_f32_16x16x32_f16 v[36:39], v[132:135], v[64:67], v[36:39]\n.LU%=:\ns_nop 4\n"
        "ds_write_b128 v13, v[48:51]\nds_write_b128 v15, v[44:47]\nds_write_b128 v14, v[40:43]\nds_write_b128 %[wa], v[36:39]\ns_branch .LE%=\n.LT%=:\ns_cmp_eq_u32 %[v], 2\ns_cbranch_scc0 .LV%=\n"
        "ds_read_b64 v[60:61], %[rb] offset:0\nds_read_b128 v[62:65], %[ra] offset:0\nds_read_b64 v[66:67], %[rb] offset:288\nds_read_b128 v[68:71], %[ra] offset:288\n"
        "ds_read_b64 v[72:73], %[rb] offset:576\nds_read_b128 v[74:77], %[ra] offset:576\nds_read_b64 v[78:79], %[rb] offset:864\nds_read_b128 v[80:83], %[ra] offset:864\ns_waitcnt lgkmcnt(6)\n"
        "v_mfma_f32_16x16x32_f16 v[84:87], v[60:63], v[100:103], 0\nv_mfma_f32_16x16x32_f16 v[88:91], v[62:65], v[104:107], 0\nds_read_b64 v[60:61], %[rb] offset:1152\n"
        "ds_read_b128 v[62:65], %[ra] offset:1152\ns_waitcnt lgkmcnt(6)\nv_mfma_f32_16x16x32_f16 v[92:95], v[66:69], v[100:103], 0\nv_mfma_f32_16x16x32_f16 v[96:99], v[68:71], v[104:107], 0\n"
        "ds_read_b64 v[66:67], %[rb] offset:1440\nds_read_b128 v[68:71], %[ra] offset:1440\ns_nop 7\nv_cvt_pk_f16_f32 v136, v84, v85\nv_cvt_pk_f16_f32 v137, v86, v87\nv_cvt_pk_f16_f32 v138, v88, v89\n"
        "v_cvt_pk_f16_f32 v139, v90, v91\ns_nop 1\nv_mfma_f32_16x16x32_f16 v[36:39], v[136:139], v[108:111], 0\ns_waitcnt lgkmcnt(6)\nv_mfma_f32_16x16x32_f16 v[84:87], v[72:75], v[100:103], 0\n"
        "v_mfma_f32_16x16x32_f16 v[88:91], v[74:77], v[104:107], 0\nds_read_b64 v[72:73], %[rb] offset:1728\nds_read_b128 v[74:77], %[ra] offset:1728\nv_cvt_pk_f16_f32 v140, v92, v93\n"
        "v_cvt_pk_f16_f32 v141, v94, v95\nv_cvt_pk_f16_f32 v142, v96, v97\nv_cvt_pk_f16_f32 v143, v98, v99\ns_nop 1\nv_mfma_f32_16x16x32_f16 v[36:39], v[140:143], v[112:115], v[36:39]\n"
        "s_waitcnt lgkmcnt(6)\nv_mfma_f32_16x16x32_f16 v[92:95], v[78:81], v[100:103], 0\nv_mfma_f32_16x16x32_f16 v[96:99], v[80:83], v[104:107], 0\nds_read_b64 v[78:79], %[rb] offset:2016\n"
        "ds_read_b128 v[80:83], %[ra] offset:2016\nv_cvt_pk_f16_f32 v136, v84, v85\nv_cvt_pk_f16_f32 v137, v86, v87\nv_cvt_pk_f16_f32 v138, v88, v89\nv_cvt_pk_f16_f32 v139, v90, v91\ns_nop 1\n"
        "v_mfma_f32_16x16x32_f16 v[36:39], v[136:139], v[116:119], v[36:39]\nv_mfma_f32_16x16x32_f16 v[40:43], v[136:139], v[108:111], 0\ns_waitcnt lgkmcnt(6)\n"
        "v_mfma_f32_16x16x32_f16 v[84:87], v[60:63], v[100:103], 0\nv_cvt_pk_f16_f32 v140, v92, v93\nv_cvt_pk_f16_f32 v141, v94, v95\nv_mfma_f32_16x16x32_f16 v[88:91], v[62:65], v[104:107], 0\n"
        "v_cvt_pk_f16_f32 v142, v96, v97\nv_cvt_pk_f16_f32 v143, v98, v99\ns_nop 1\nv_mfma_f32_16x16x32_f16 v[36:39], v[140:143], v[120:123], v[36:39]\nds_read_b64 v[60:61], %[rb] offset:2304\n"
        "ds_read_b128 v[62:65], %[ra] offset:2304\nv_mfma_f32_16x16x32_f16 v[40:43], v[140:143], v[112:115], v[40:43]\ns_waitcnt lgkmcnt(6)\nv_mfma_f32_16x16x32_f16 v[92:95], v[66:69], v[100:103], 0\n"
        "v_cvt_pk_f16_f32 v136, v84, v85\nv_cvt_pk_f16_f32 v137, v86, v87\nv_mfma_f32_16x16x32_f16 v[96:99], v[68:71], v[104:107], 0\nv_cvt_pk_f16_f32 v138, v88, v89\nv_cvt_pk_f16_f32 v139, v90, v91\n"
        "s_nop 1\nv_mfma_f32_16x16x32_f16 v[36:39], v[136:139], v[124:127], v[36:39]\nds_read_b64 v[66:67], %[rb] offset:2592\nds_read_b128 v[68:71], %[ra] offset:2592\n"
        "v_mfma_f32_16x16x32_f16 v[40:43], v[136:139], v[116:119], v[40:43]\nv_mfma_f32_16x16x32_f16 v[44:47], v[136:139], v[108:111], 0\nv_cvt_pk_f16_f32 v140, v92, v93\n"
        "v_cvt_pk_f16_f32 v141, v94, v95\ns_waitcnt lgkmcnt(6)\nv_mfma_f32_16x16x32_f16 v[84:87], v[72:75], v[100:103], 0\nv_cvt_pk_f16_f32 v142, v96, v97\nv_cvt_pk_f16_f32 v143, v98, v99\n"
        "v_mfma_f32_16x16x32_f16 v[88:91], v[74:77], v[104:107], 0\ns_nop 0\nv_mfma_f32_16x16x32_f16 v[36:39], v[140:143], v[128:131], v[36:39]\n"
        "v_mfma_f32_16x16x32_f16 v[40:43], v[140:143], v[120:123], v[40:43]\nv_mfma_f32_16x16x32_f16 v[44:47], v[140:143], v[112:115], v[44:47]\nv_cvt_pk_f16_f32 v136, v84, v85\n"
        "v_cvt_pk_f16_f32 v137, v86, v87\ns_waitcnt lgkmcnt(4)\nv_mfma_f32_16x16x32_f16 v[92:95], v[78:81], v[100:103], 0\nv_cvt_pk_f16_f32 v138, v88, v89\nv_cvt_pk_f16_f32 v139, v90, v91\n"
        "v_mfma_f32_16x16x32_f16 v[96:99], v[80:83], v[104:107], 0\nds_write_b128 %[wa], v[36:39] offset:0\nv_mfma_f32_16x16x32_f16 v[40:43], v[136:139], v[124:127], v[40:43]\n"
        "v_mfma_f32_16x16x32_f16 v[44:47], v[136:139], v[116:119], v[44:47]\ns_waitcnt lgkmcnt(3)\nv_mfma_f32_16x16x32_f16 v[84:87], v[60:63], v[100:103], 0\nv_cvt_pk_f16_f32 v140, v92, v93\n"
        "v_cvt_pk_f16_f32 v141, v94, v95\nv_mfma_f32_16x16x32_f16 v[88:91], v[62:65], v[104:107], 0\nv_cvt_pk_f16_f32 v142, v96, v97\nv_cvt_pk_f16_f32 v143, v98, v99\ns_nop 1\n"
        "v_mfma_f32_16x16x32_f16 v[40:43], v[140:143], v[128:131], v[40:43]\nv_mfma_f32_16x16x32_f16 v[44:47], v[140:143], v[120:123], v[44:47]\ns_waitcnt lgkmcnt(1)\n"
        "v_mfma_f32_16x16x32_f16 v[92:95], v[66:69], v[100:103], 0\nv_cvt_pk_f16_f32 v136, v84, v85\nv_cvt_pk_f16_f32 v137, v86, v87\nv_mfma_f32_16x16x32_f16 v[96:99], v[68:71], v[104:107], 0\n"
        "v_cvt_pk_f16_f32 v138, v88, v89\nv_cvt_pk_f16_f32 v139, v90, v91\ns_nop 1\nv_mfma_f32_16x16x32_f16 v[44:47], v[136:139], v[124:127], v[44:47]\nds_write_b128 %[wa], v[40:43] offset:256\n"
        "s_nop 7\nv_cvt_pk_f16_f32 v140, v92, v93\nv_cvt_pk_f16_f32 v141, v94, v95\nv_cvt_pk_f16_f32 v142, v96, v97\nv_cvt_pk_f16_f32 v143, v98, v99\ns_nop 1\n"
        "v_mfma_f32_16x16x32_f16 v[44:47], v[140:143], v[128:131], v[44:47]\ns_nop 7\nds_write_b128 %[wa], v[44:47] offset:512\ns_branch .LE%=\n.LV%=:\nds_read_b128 v[52:55], %[ra] offset:0\n"
        "ds_read_b128 v[56:59], %[ra] offset:288\nds_read_b128 v[60:63], %[ra] offset:576\nds_read_b128 v[64:67], %[ra] offset:864\nds_read_b128 v[84:87], %[rb] offset:0\n"
        "ds_read_b128 v[88:91], %[rb] offset:1152\ns_waitcnt lgkmcnt(5)\nv_mfma_f32_16x16x32_f16 v[68:71], v[52:55], v[100:103], 0\nv_mfma_f32_16x16x32_f16 v[72:75], v[52:55], v[104:107], 0\n"
        "ds_read_b128 v[52:55], %[ra] offset:1152\ns_waitcnt lgkmcnt(5)\nv_mfma_f32_16x16x32_f16 v[76:79], v[56:59], v[100:103], 0\nv_mfma_f32_16x16x32_f16 v[80:83], v[56:59], v[104:107], 0\n"
        "ds_read_b128 v[56:59], %[ra] offset:1440\ns_waitcnt lgkmcnt(3)\nv_mfma_f32_16x16x32_f16 v[36:39], v[124:127], v[84:87], 0\nv_cvt_pk_f16_f32 v136, v68, v73\nv_cvt_pk_f16_f32 v137, v70, v75\n"
        "s_waitcnt lgkmcnt(2)\nv_mfma_f32_16x16x32_f16 v[40:43], v[124:127], v[88:91], 0\nv_mfma_f32_16x16x32_f16 v[68:71], v[60:63], v[100:103], 0\nv_cvt_pk_f16_f32 v138, v76, v81\n"
        "v_cvt_pk_f16_f32 v139, v78, v83\nv_mfma_f32_16x16x32_f16 v[72:75], v[60:63], v[104:107], 0\nds_read_b128 v[60:63], %[ra] offset:1728\n"
        "v_mfma_f32_16x16x32_f16 v[36:39], v[136:139], v[112:115], v[36:39]\nv_mfma_f32_16x16x32_f16 v[40:43], v[136:139], v[120:123], v[40:43]\n"
        "v_mfma_f32_16x16x32_f16 v[76:79], v[64:67], v[100:103], 0\nv_cvt_pk_f16_f32 v140, v68, v73\nv_cvt_pk_f16_f32 v141, v70, v75\nv_mfma_f32_16x16x32_f16 v[80:83], v[64:67], v[104:107], 0\n"
        "ds_read_b128 v[64:67], %[ra] offset:2016\ns_waitcnt lgkmcnt(3)\nv_mfma_f32_16x16x32_f16 v[68:71], v[52:55], v[100:103], 0\nv_mfma_f32_16x16x32_f16 v[72:75], v[52:55], v[104:107], 0\ns_nop 7\n"
        "v_cvt_pk_f16_f32 v142, v76, v81\nv_cvt_pk_f16_f32 v143, v78, v83\ns_nop 1\nv_mfma_f32_16x16x32_f16 v[36:39], v[140:143], v[108:111], v[36:39]\n"
        "v_mfma_f32_16x16x32_f16 v[40:43], v[140:143], v[116:119], v[40:43]\ns_waitcnt lgkmcnt(2)\nv_mfma_f32_16x16x32_f16 v[76:79], v[56:59], v[100:103], 0\nv_cvt_pk_f16_f32 v136, v68, v73\n"
        "v_cvt_pk_f16_f32 v137, v70, v75\nv_mfma_f32_16x16x32_f16 v[80:83], v[56:59], v[104:107], 0\nds_write_b128 %[wa], v[36:39] offset:0\ns_waitcnt lgkmcnt(2)\n"
        "v_mfma_f32_16x16x32_f16 v[68:71], v[60:63], v[100:103], 0\nv_mfma_f32_16x16x32_f16 v[72:75], v[60:63], v[104:107], 0\ns_nop 7\nv_cvt_pk_f16_f32 v138, v76, v81\n"
        "v_cvt_pk_f16_f32 v139, v78, v83\ns_nop 1\nv_mfma_f32_16x16x32_f16 v[40:43], v[136:139], v[112:115], v[40:43]\ns_waitcnt lgkmcnt(1)\nv_mfma_f32_16x16x32_f16 v[76:79], v[64:67], v[100:103], 0\n"
        "v_mfma_f32_16x16x32_f16 v[80:83], v[64:67], v[104:107], 0\nv_cvt_pk_f16_f32 v140, v68, v73\nv_cvt_pk_f16_f32 v141, v70, v75\ns_nop 7\nv_cvt_pk_f16_f32 v142, v76, v81\n"
        "v_cvt_pk_f16_f32 v143, v78, v83\ns_nop 1\nv_mfma_f32_16x16x32_f16 v[40:43], v[140:143], v[108:111], v[40:43]\ns_nop 7\nds_write_b128 %[wa], v[40:43] offset:256\ns_branch .LE%=\n.LC%=:\n"
        "s_sub_u32 %[cap], %[cap], 1\ns_cbranch_scc0 .LCy%=\ns_mov_b32 %[cap], 0\ns_branch .LD%=\n.LCy%=:\nbuffer_load_dwordx4 v[36:39], %[c16], %[xrs], 0 offen sc1\ns_waitcnt vmcnt(0)\n"
        "s_branch .LB%=\n.LI%=:\ns_sub_u32 %[cap], %[cap], 1\ns_cbranch_scc0 .LIy%=\ns_mov_b32 %[cap], 0\ns_branch .LM%=\n.LIy%=:\ns_sleep %[fp]\n"
        "buffer_load_dwordx4 v[40:43], %[c16], %[frs], 0 offen sc1\nbuffer_load_dwordx4 v[44:47], %[c16], %[frs], %[s1] offen sc1\ns_branch .LF%=\n.LE%=:\ns_waitcnt vmcnt(0)\ns_mov_b64 exec, 1\n"
        "ds_add_u32 %[cna], %[one] offset:8\ns_mov_b64 exec, -1\ns_setprio %[pbe]\ns_cmp_lt_u32 %[wq], 2\ns_cbranch_scc0 .LZ%=\nv_lshlrev_b32 v26, 16, v152\nv_and_b32 v27, 0xffff0000, v152\n"
        "v_cvt_f32_f16 v152, v156\nv_lshrrev_b32 v156, 16, v156\nv_lshlrev_b32 v28, 16, v153\nv_and_b32 v29, 0xffff0000, v153\nv_cvt_f32_f16 v153, v157\nv_lshrrev_b32 v157, 16, v157\n"
        "v_lshlrev_b32 v30, 16, v154\nv_and_b32 v31, 0xffff0000, v154\nv_cvt_f32_f16 v154, v158\nv_lshrrev_b32 v158, 16, v158\nv_lshlrev_b32 v32, 16, v155\nv_and_b32 v33, 0xffff0000, v155\n"
        "v_cvt_f32_f16 v155, v159\nv_lshrrev_b32 v159, 16, v159\nv_cvt_f32_f16 v156, v156\nv_fmac_f32 v26, v152, %[kd1]\nv_cvt_f32_f16 v157, v157\nv_fmac_f32 v28, v153, %[kd1]\n"
        "v_cvt_f32_f16 v158, v158\nv_fmac_f32 v30, v154, %[kd1]\nv_cvt_f32_f16 v159, v159\nv_fmac_f32 v32, v155, %[kd1]\nv_fmac_f32 v27, v156, %[kd1]\nv_fmac_f32 v29, v157, %[kd1]\n"
        "v_fmac_f32 v31, v158, %[kd1]\nv_fmac_f32 v33, v159, %[kd1]\ns_mov_b32 %[s1], 0x100000\n.LP%=:\nds_read_b32 %[pv], %[cna] offset:8\ns_waitcnt lgkmcnt(0)\nv_readfirstlane_b32 %[s0], %[pv]\n"
        "s_nop 0\ns_cmp_ge_i32 %[s0], 4\ns_cbranch_scc1 .LR%=\ns_sub_u32 %[s1], %[s1], 1\ns_cbranch_scc0 .LP%=\n.LR%=:\ns_cmp_lt_u32 %[wq], 1\ns_cbranch_scc1 .LS4_%=\n"
        "ds_read_b128 v[36:39], %[exr] offset:0\nds_read_b128 v[40:43], %[exr] offset:2048\nds_read_b128 v[44:47], %[exr] offset:4096\nds_read_b128 v[48:51], %[exr] offset:6144\n"
        "ds_read_b128 v[52:55], %[mr] offset:0\nds_read_b128 v[56:59], %[mr] offset:3072\nds_read_b128 v[60:63], %[frr] offset:0\nds_read_b128 v[64:67], %[frr] offset:1152\ns_waitcnt lgkmcnt(6)\n"
        "v_fmac_f32 v26, v36, %[kf]\nv_fmac_f32 v27, v37, %[kf]\nv_fmac_f32 v28, v38, %[kf]\nv_fmac_f32 v29, v39, %[kf]\nv_fmac_f32 v30, v40, %[kf]\nv_fmac_f32 v31, v41, %[kf]\n"
        "v_fmac_f32 v32, v42, %[kf]\nv_fmac_f32 v33, v43, %[kf]\ns_waitcnt lgkmcnt(4)\nv_fmac_f32 v26, v44, %[kf]\nv_fmac_f32 v27, v45, %[kf]\nv_fmac_f32 v28, v46, %[kf]\nv_fmac_f32 v29, v47, %[kf]\n"
        "v_fmac_f32 v30, v48, %[kf]\nv_fmac_f32 v31, v49, %[kf]\nv_fmac_f32 v32, v50, %[kf]\nv_fmac_f32 v33, v51, %[kf]\ns_waitcnt lgkmcnt(2)\nv_fmac_f32 v26, v52, %[kf4]\nv_fmac_f32 v27, v53, %[kf4]\n"
        "v_fmac_f32 v28, v54, %[kf4]\nv_fmac_f32 v29, v55, %[kf4]\nv_fmac_f32 v30, v56, %[kf4]\nv_fmac_f32 v31, v57, %[kf4]\nv_fmac_f32 v32, v58, %[kf4]\nv_fmac_f32 v33, v59, %[kf4]\n"
        "s_waitcnt lgkmcnt(0)\nv_fmac_f32 v26, v60, %[kf]\nv_fmac_f32 v27, v61, %[kf]\nv_fmac_f32 v28, v62, %[kf]\nv_fmac_f32 v29, v63, %[kf]\nv_fmac_f32 v30, v64, %[kf]\nv_fmac_f32 v31, v65, %[kf]\n"
        "v_fmac_f32 v32, v66, %[kf]\nv_fmac_f32 v33, v67, %[kf]\ns_branch .LS%=\n.LS4_%=:\nds_read_b128 v[36:39], %[exr] offset:0\nds_read_b128 v[40:43], %[exr] offset:2048\n"
        "ds_read_b128 v[44:47], %[exr] offset:4096\nds_read_b128 v[48:51], %[exr] offset:6144\nds_read_b128 v[52:55], %[mr] offset:0\nds_read_b128 v[56:59], %[mr] offset:3072\ns_waitcnt lgkmcnt(4)\n"
        "v_fmac_f32 v26, v36, %[kf]\nv_fmac_f32 v27, v37, %[kf]\nv_fmac_f32 v28, v38, %[kf]\nv_fmac_f32 v29, v39, %[kf]\nv_fmac_f32 v30, v40, %[kf]\nv_fmac_f32 v31, v41, %[kf]\n"
        "v_fmac_f32 v32, v42, %[kf]\nv_fmac_f32 v33, v43, %[kf]\ns_waitcnt lgkmcnt(2)\nv_fmac_f32 v26, v44, %[kf]\nv_fmac_f32 v27, v45, %[kf]\nv_fmac_f32 v28, v46, %[kf]\nv_fmac_f32 v29, v47, %[kf]\n"
        "v_fmac_f32 v30, v48, %[kf]\nv_fmac_f32 v31, v49, %[kf]\nv_fmac_f32 v32, v50, %[kf]\nv_fmac_f32 v33, v51, %[kf]\ns_waitcnt lgkmcnt(0)\nv_fmac_f32 v26, v52, %[kf4]\nv_fmac_f32 v27, v53, %[kf4]\n"
        "v_fmac_f32 v28, v54, %[kf4]\nv_fmac_f32 v29, v55, %[kf4]\nv_fmac_f32 v30, v56, %[kf4]\nv_fmac_f32 v31, v57, %[kf4]\nv_fmac_f32 v32, v58, %[kf4]\nv_fmac_f32 v33, v59, %[kf4]\n.LS%=:\n"
        "v_cvt_pk_bf16_f32 v36, v26, v27\nv_cvt_pk_bf16_f32 v37, v28, v29\nv_cvt_pk_bf16_f32 v38, v30, v31\nv_cvt_pk_bf16_f32 v39, v32, v33\nv_min_u32 v36, -2, v36\nv_min_u32 v37, -2, v37\n"
        "v_min_u32 v38, -2, v38\nv_min_u32 v39, -2, v39\nbuffer_store_dwordx4 v[36:39], %[od], %[ors], 0 offen sc0 sc1\ns_nop 1\n.LZ%=:\n"
        : "+{v[100:103]}"(kt[0]), "+{v[104:107]}"(kt[1]), "+{v[108:111]}"(kt[2]), "+{v[112:115]}"(kt[3]), "+{v[116:119]}"(kt[4]), "+{v[120:123]}"(kt[5]), "+{v[124:127]}"(kt[6]), "+{v[128:131]}"(kt[7]), "+{v[132:135]}"(kt[8]), [pv] "=&{v16}"(pv), [s0] "=&s"(s0_), [s1] "=&s"(s1_), [cap] "=&s"(cap)
        : [ra] "{v1}"(ra), [rb] "{v2}"(rb), [wa] "{v3}"(wa), [cna] "{v4}"(cna), [one] "{v5}"(one), [exr] "{v6}"(exr), [frr] "{v7}"(frr), [mr] "{v8}"(mr_), [od] "{v9}"(od), [kf] "{v10}"(kd[0]), [kd1] "{v11}"(kd[1]), [c16] "{v12}"(c16), [uw] "{v17}"(uw_), [ue] "{v20}"(ue), [kf4] "{v21}"(kf4), [xo] "{v22}"(xo), [fsa] "i"((layer ? FSH : FSH0) - FSX), [pbl] "i"(PBH), [v] "s"(v), [wq] "s"(wq), [ors] "s"(ors), [xrs] "s"(xrs), [frs] "s"(frs), [pbe] "i"(PBE), [fsx] "i"(FSX), [fp] "i"(FPOLL)
        : "memory", "scc", "v13", "v14", "v15", "v26", "v27", "v28", "v29", "v30", "v31", "v32", "v33", "v36", "v37", "v38", "v39", "v40", "v41", "v42", "v43", "v44", "v45", "v46", "v47", "v48", "v49", "v50", "v51", "v52", "v53", "v54", "v55", "v56", "v57", "v58", "v59", "v60", "v61", "v62", "v63", "v64", "v65", "v66", "v67", "v68", "v69", "v70", "v71", "v72", "v73", "v74", "v75", "v76", "v77", "v78", "v79", "v80", "v81", "v82", "v83", "v84", "v85", "v86", "v87", "v88", "v89", "v90", "v91", "v92", "v93", "v94", "v95", "v96", "v97", "v98", "v99", "v136", "v137", "v138", "v139", "v140", "v141", "v142", "v143", "v144", "v145", "v146", "v147", "v148", "v149", "v150", "v151", "v152", "v153", "v154", "v155", "v156", "v157", "v158", "v159", "v160", "v161", "v162", "v163", "v164", "v165", "v166", "v167", "vcc");
    } else {
      asm volatile(
        "s_mov_b32 %[cap], 0x10000\ns_movk_i32 %[s1], 4096\ns_sleep %[fsx]\nbuffer_load_dwordx4 v[36:39], %[c16], %[xrs], 0 offen sc1\nv_mov_b32 v56, -1\nv_mov_b32 v57, -1\nv_mov_b32 v58, -1\n"
        "v_mov_b32 v59, -1\ns_sleep %[fsa]\nbuffer_load_dwordx4 v[40:43], %[c16], %[frs], 0 offen sc1\nbuffer_load_dwordx4 v[44:47], %[c16], %[frs], %[s1] offen sc1\ns_waitcnt vmcnt(2)\n.LB%=:\n"
        "v_max3_u32 %[pv], v36, v37, v38\nv_max_u32 %[pv], %[pv], v39\nv_cmp_eq_u32 vcc, -1, %[pv]\ns_cbranch_vccnz .LC%=\n.LD%=:\nv_lshlrev_b32 v26, 16, v36\nv_and_b32 v27, 0xffff0000, v36\n"
        "v_lshlrev_b32 v28, 16, v37\nv_and_b32 v29, 0xffff0000, v37\nv_lshlrev_b32 v30, 16, v38\nv_and_b32 v31, 0xffff0000, v38\nv_lshlrev_b32 v32, 16, v39\nv_and_b32 v33, 0xffff0000, v39\n.LF%=:\n"
        "s_waitcnt vmcnt(1)\nv_max3_u32 %[pv], v40, v41, v42\nv_lshlrev_b32 v48, 16, v40\nv_lshlrev_b32 v50, 16, v41\nv_lshlrev_b32 v52, 16, v42\nv_lshlrev_b32 v54, 16, v43\nv_fmac_f32 v48, v26, v40\n"
        "v_fmac_f32 v50, v28, v41\nv_fmac_f32 v52, v30, v42\nv_fmac_f32 v54, v32, v43\ns_waitcnt vmcnt(0)\nv_max3_u32 %[pv], %[pv], v43, v44\nv_max3_u32 %[pv], %[pv], v45, v46\n"
        "v_max_u32 %[pv], %[pv], v47\nv_cmp_eq_u32 vcc, -1, %[pv]\nv_lshlrev_b32 v49, 16, v44\nv_lshlrev_b32 v51, 16, v45\nv_lshlrev_b32 v53, 16, v46\nv_lshlrev_b32 v55, 16, v47\n"
        "s_cbranch_vccnz .LI%=\n.LM%=:\nv_fmac_f32 v49, v27, v44\nv_fmac_f32 v51, v29, v45\nv_fmac_f32 v53, v31, v46\nv_fmac_f32 v55, v33, v47\nv_cvt_pk_f16_f32 v36, v48, v49\n"
        "v_cvt_pk_f16_f32 v37, v50, v51\nv_cvt_pk_f16_f32 v38, v52, v53\nv_cvt_pk_f16_f32 v39, v54, v55\nds_write_b128 %[uw], v[36:39]\ns_mov_b64 exec, 1\nds_add_u32 %[cna], %[one] offset:0\n"
        "s_mov_b64 exec, -1\ns_movk_i32 %[s0], 12288\ns_movk_i32 %[s1], 24576\nbuffer_store_dwordx4 v[56:59], %[mo], %[mrs], 0 offen sc0 sc1\n"
        "buffer_store_dwordx4 v[56:59], %[mo], %[mrs], %[s0] offen sc0 sc1\nbuffer_store_dwordx4 v[56:59], %[mo], %[mrs], %[s1] offen sc0 sc1\ns_setprio %[pbl]\ns_mov_b32 %[s1], 0x100000\n.LN%=:\n"
        "ds_read_b32 %[pv], %[cna]\ns_waitcnt lgkmcnt(0)\nv_readfirstlane_b32 %[s0], %[pv]\ns_nop 0\ns_cmp_ge_i32 %[s0], 4\ns_cbranch_scc1 .LO%=\ns_sub_u32 %[s1], %[s1], 1\ns_cbranch_scc0 .LN%=\n"
        ".LO%=:\nbuffer_load_dwordx4 v[152:155], %[xo], %[xrs], 0 offen sc1\nds_read_b128 v[156:159], %[ue]\ns_cmp_lt_u32 %[v], 2\ns_cbranch_scc0 .LT%=\nds_read_b128 v[52:55], %[ra] offset:800\n"
        "ds_read_b128 v[56:59], %[ra] offset:736\nds_read_b128 v[60:63], %[ra] offset:672\nds_read_b128 v[64:67], %[rb] offset:576\nds_read_b128 v[68:71], %[ra] offset:512\n"
        "ds_read_b128 v[72:75], %[ra] offset:448\nds_read_b128 v[76:79], %[ra] offset:384\nds_read_b128 v[80:83], %[rb] offset:288\nv_xor_b32 v14, 32, %[wa]\nv_xor_b32 v15, 64, %[wa]\n"
        "v_xor_b32 v13, 96, %[wa]\ns_waitcnt lgkmcnt(7)\nv_mfma_f32_16x16x32_f16 v[48:51], v[100:103], v[52:55], 0\nds_read_b128 v[52:55], %[ra] offset:224\ns_waitcnt lgkmcnt(7)\n"
        "v_mfma_f32_16x16x32_f16 v[44:47], v[100:103], v[56:59], 0\ns_waitcnt lgkmcnt(6)\nv_mfma_f32_16x16x32_f16 v[40:43], v[100:103], v[60:63], 0\ns_waitcnt lgkmcnt(5)\n"
        "v_mfma_f32_16x16x32_f16 v[36:39], v[100:103], v[64:67], 0\nv_mfma_f32_16x16x32_f16 v[48:51], v[104:107], v[56:59], v[48:51]\nds_read_b128 v[56:59], %[ra] offset:160\n"
        "v_mfma_f32_16x16x32_f16 v[44:47], v[104:107], v[60:63], v[44:47]\nv_mfma_f32_16x16x32_f16 v[40:43], v[104:107], v[64:67], v[40:43]\ns_waitcnt lgkmcnt(5)\n"
        "v_mfma_f32_16x16x32_f16 v[36:39], v[104:107], v[68:71], v[36:39]\nv_mfma_f32_16x16x32_f16 v[48:51], v[108:111], v[60:63], v[48:51]\nds_read_b128 v[60:63], %[ra] offset:96\n"
        "v_mfma_f32_16x16x32_f16 v[44:47], v[108:111], v[64:67], v[44:47]\nv_mfma_f32_16x16x32_f16 v[40:43], v[108:111], v[68:71], v[40:43]\ns_waitcnt lgkmcnt(5)\n"
        "v_mfma_f32_16x16x32_f16 v[36:39], v[108:111], v[72:75], v[36:39]\nv_mfma_f32_16x16x32_f16 v[48:51], v[112:115], v[64:67], v[48:51]\nds_read_b128 v[64:67], %[rb] offset:0\n"
        "v_mfma_f32_16x16x32_f16 v[44:47], v[112:115], v[68:71], v[44:47]\nv_mfma_f32_16x16x32_f16 v[40:43], v[112:115], v[72:75], v[40:43]\ns_waitcnt lgkmcnt(5)\n"
        "v_mfma_f32_16x16x32_f16 v[36:39], v[112:115], v[76:79], v[36:39]\nv_mfma_f32_16x16x32_f16 v[48:51], v[116:119], v[68:71], v[48:51]\n"
        "v_mfma_f32_16x16x32_f16 v[44:47], v[116:119], v[72:75], v[44:47]\nv_mfma_f32_16x16x32_f16 v[40:43], v[116:119], v[76:79], v[40:43]\ns_waitcnt lgkmcnt(4)\n"
        "v_mfma_f32_16x16x32_f16 v[36:39], v[116:119], v[80:83], v[36:39]\nv_mfma_f32_16x16x32_f16 v[48:51], v[120:123], v[72:75], v[48:51]\n"
        "v_mfma_f32_16x16x32_f16 v[44:47], v[120:123], v[76:79], v[44:47]\nv_mfma_f32_16x16x32_f16 v[40:43], v[120:123], v[80:83], v[40:43]\ns_waitcnt lgkmcnt(3)\n"
        "v_mfma_f32_16x16x32_f16 v[36:39], v[120:123], v[52:55], v[36:39]\nv_mfma_f32_16x16x32_f16 v[48:51], v[124:127], v[76:79], v[48:51]\n"
        "v_mfma_f32_16x16x32_f16 v[44:47], v[124:127], v[80:83], v[44:47]\nv_mfma_f32_16x16x32_f16 v[40:43], v[124:127], v[52:55], v[40:43]\ns_waitcnt lgkmcnt(2)\n"
        "v_mfma_f32_16x16x32_f16 v[36:39], v[124:127], v[56:59], v[36:39]\nv_mfma_f32_16x16x32_f16 v[48:51], v[128:131], v[80:83], v[48:51]\n"
        "v_mfma_f32_16x16x32_f16 v[44:47], v[128:131], v[52:55], v[44:47]\nv_mfma_f32_16x16x32_f16 v[40:43], v[128:131], v[56:59], v[40:43]\ns_waitcnt lgkmcnt(1)\n"
        "v_mfma_f32_16x16x32_f16 v[36:39], v[128:131], v[60:63], v[36:39]\ns_cmp_eq_u32 %[v], 0\ns_cbranch_scc1 .LU%=\nv_mfma_f32_16x16x32_f16 v[48:51], v[132:135], v[52:55], v[48:51]\n"
        "v_mfma_f32_16x16x32_f16 v[44:47], v[132:135], v[56:59], v[44:47]\nv_mfma_f32_16x16x32_f16 v[40:43], v[132:135], v[60:63], v[40:43]\ns_waitcnt lgkmcnt(0)\n"
        "v_mfma_f32_16x16x32_f16 v[36:39], v[132:135], v[64:67], v[36:39]\n.LU%=:\ns_nop 4\nds_write_b128 v13, v[48:51]\nds_write_b128 v15, v[44:47]\nds_write_b128 v14, v[40:43]\n"
        "ds_write_b128 %[wa], v[36:39]\ns_branch .LE%=\n.LT%=:\ns_cmp_eq_u32 %[v], 2\ns_cbranch_scc0 .LV%=\nds_read_b64 v[60:61], %[rb] offset:0\nds_read_b128 v[62:65], %[ra] offset:0\n"
        "ds_read_b64 v[66:67], %[rb] offset:288\nds_read_b128 v[68:71], %[ra] offset:288\nds_read_b64 v[72:73], %[rb] offset:576\nds_read_b128 v[74:77], %[ra] offset:576\n"
        "ds_read_b64 v[78:79], %[rb] offset:864\nds_read_b128 v[80:83], %[ra] offset:864\ns_waitcnt lgkmcnt(6)\nv_mfma_f32_16x16x32_f16 v[84:87], v[60:63], v[100:103], 0\n"
        "v_mfma_f32_16x16x32_f16 v[88:91], v[62:65], v[104:107], 0\nds_read_b64 v[60:61], %[rb] offset:1152\nds_read_b128 v[62:65], %[ra] offset:1152\ns_waitcnt lgkmcnt(6)\n"
        "v_mfma_f32_16x16x32_f16 v[92:95], v[66:69], v[100:103], 0\nv_mfma_f32_16x16x32_f16 v[96:99], v[68:71], v[104:107], 0\nds_read_b64 v[66:67], %[rb] offset:1440\n"
        "ds_read_b128 v[68:71], %[ra] offset:1440\ns_nop 7\nv_cvt_pk_f16_f32 v136, v84, v85\nv_cvt_pk_f16_f32 v137, v86, v87\nv_cvt_pk_f16_f32 v138, v88, v89\nv_cvt_pk_f16_f32 v139, v90, v91\ns_nop 1\n"
        "v_mfma_f32_16x16x32_f16 v[36:39], v[136:139], v[108:111], 0\ns_waitcnt lgkmcnt(6)\nv_mfma_f32_16x16x32_f16 v[84:87], v[72:75], v[100:103], 0\n"
        "v_mfma_f32_16x16x32_f16 v[88:91], v[74:77], v[104:107], 0\nds_read_b64 v[72:73], %[rb] offset:1728\nds_read_b128 v[74:77], %[ra] offset:1728\nv_cvt_pk_f16_f32 v140, v92, v93\n"
        "v_cvt_pk_f16_f32 v141, v94, v95\nv_cvt_pk_f16_f32 v142, v96, v97\nv_cvt_pk_f16_f32 v143, v98, v99\ns_nop 1\nv_mfma_f32_16x16x32_f16 v[36:39], v[140:143], v[112:115], v[36:39]\n"
        "s_waitcnt lgkmcnt(6)\nv_mfma_f32_16x16x32_f16 v[92:95], v[78:81], v[100:103], 0\nv_mfma_f32_16x16x32_f16 v[96:99], v[80:83], v[104:107], 0\nds_read_b64 v[78:79], %[rb] offset:2016\n"
        "ds_read_b128 v[80:83], %[ra] offset:2016\nv_cvt_pk_f16_f32 v136, v84, v85\nv_cvt_pk_f16_f32 v137, v86, v87\nv_cvt_pk_f16_f32 v138, v88, v89\nv_cvt_pk_f16_f32 v139, v90, v91\ns_nop 1\n"
        "v_mfma_f32_16x16x32_f16 v[36:39], v[136:139], v[116:119], v[36:39]\nv_mfma_f32_16x16x32_f16 v[40:43], v[136:139], v[108:111], 0\ns_waitcnt lgkmcnt(6)\n"
        "v_mfma_f32_16x16x32_f16 v[84:87], v[60:63], v[100:103], 0\nv_cvt_pk_f16_f32 v140, v92, v93\nv_cvt_pk_f16_f32 v141, v94, v95\nv_mfma_f32_16x16x32_f16 v[88:91], v[62:65], v[104:107], 0\n"
        "v_cvt_pk_f16_f32 v142, v96, v97\nv_cvt_pk_f16_f32 v143, v98, v99\ns_nop 1\nv_mfma_f32_16x16x32_f16 v[36:39], v[140:143], v[120:123], v[36:39]\nds_read_b64 v[60:61], %[rb] offset:2304\n"
        "ds_read_b128 v[62:65], %[ra] offset:2304\nv_mfma_f32_16x16x32_f16 v[40:43], v[140:143], v[112:115], v[40:43]\ns_waitcnt lgkmcnt(6)\nv_mfma_f32_16x16x32_f16 v[92:95], v[66:69], v[100:103], 0\n"
        "v_cvt_pk_f16_f32 v136, v84, v85\nv_cvt_pk_f16_f32 v137, v86, v87\nv_mfma_f32_16x16x32_f16 v[96:99], v[68:71], v[104:107], 0\nv_cvt_pk_f16_f32 v138, v88, v89\nv_cvt_pk_f16_f32 v139, v90, v91\n"
        "s_nop 1\nv_mfma_f32_16x16x32_f16 v[36:39], v[136:139], v[124:127], v[36:39]\nds_read_b64 v[66:67], %[rb] offset:2592\nds_read_b128 v[68:71], %[ra] offset:2592\n"
        "v_mfma_f32_16x16x32_f16 v[40:43], v[136:139], v[116:119], v[40:43]\nv_mfma_f32_16x16x32_f16 v[44:47], v[136:139], v[108:111], 0\nv_cvt_pk_f16_f32 v140, v92, v93\n"
        "v_cvt_pk_f16_f32 v141, v94, v95\ns_waitcnt lgkmcnt(6)\nv_mfma_f32_16x16x32_f16 v[84:87], v[72:75], v[100:103], 0\nv_cvt_pk_f16_f32 v142, v96, v97\nv_cvt_pk_f16_f32 v143, v98, v99\n"
        "v_mfma_f32_16x16x32_f16 v[88:91], v[74:77], v[104:107], 0\ns_nop 0\nv_mfma_f32_16x16x32_f16 v[36:39], v[140:143], v[128:131], v[36:39]\n"
        "v_mfma_f32_16x16x32_f16 v[40:43], v[140:143], v[120:123], v[40:43]\nv_mfma_f32_16x16x32_f16 v[44:47], v[140:143], v[112:115], v[44:47]\nv_cvt_pk_f16_f32 v136, v84, v85\n"
        "v_cvt_pk_f16_f32 v137, v86, v87\ns_waitcnt lgkmcnt(4)\nv_mfma_f32_16x16x32_f16 v[92:95], v[78:81], v[100:103], 0\nv_cvt_pk_f16_f32 v138, v88, v89\nv_cvt_pk_f16_f32 v139, v90, v91\n"
        "v_mfma_f32_16x16x32_f16 v[96:99], v[80:83], v[104:107], 0\nds_write_b128 %[wa], v[36:39] offset:0\nv_mfma_f32_16x16x32_f16 v[40:43], v[136:139], v[124:127], v[40:43]\n"
        "v_mfma_f32_16x16x32_f16 v[44:47], v[136:139], v[116:119], v[44:47]\ns_waitcnt lgkmcnt(3)\nv_mfma_f32_16x16x32_f16 v[84:87], v[60:63], v[100:103], 0\nv_cvt_pk_f16_f32 v140, v92, v93\n"
        "v_cvt_pk_f16_f32 v141, v94, v95\nv_mfma_f32_16x16x32_f16 v[88:91], v[62:65], v[104:107], 0\nv_cvt_pk_f16_f32 v142, v96, v97\nv_cvt_pk_f16_f32 v143, v98, v99\ns_nop 1\n"
        "v_mfma_f32_16x16x32_f16 v[40:43], v[140:143], v[128:131], v[40:43]\nv_mfma_f32_16x16x32_f16 v[44:47], v[140:143], v[120:123], v[44:47]\ns_waitcnt lgkmcnt(1)\n"
        "v_mfma_f32_16x16x32_f16 v[92:95], v[66:69], v[100:103], 0\nv_cvt_pk_f16_f32 v136, v84, v85\nv_cvt_pk_f16_f32 v137, v86, v87\nv_mfma_f32_16x16x32_f16 v[96:99], v[68:71], v[104:107], 0\n"
        "v_cvt_pk_f16_f32 v138, v88, v89\nv_cvt_pk_f16_f32 v139, v90, v91\ns_nop 1\nv_mfma_f32_16x16x32_f16 v[44:47], v[136:139], v[124:127], v[44:47]\nds_write_b128 %[wa], v[40:43] offset:256\n"
        "s_nop 7\nv_cvt_pk_f16_f32 v140, v92, v93\nv_cvt_pk_f16_f32 v141, v94, v95\nv_cvt_pk_f16_f32 v142, v96, v97\nv_cvt_pk_f16_f32 v143, v98, v99\ns_nop 1\n"
        "v_mfma_f32_16x16x32_f16 v[44:47], v[140:143], v[128:131], v[44:47]\ns_nop 7\nds_write_b128 %[wa], v[44:47] offset:512\ns_branch .LE%=\n.LV%=:\nds_read_b128 v[52:55], %[ra] offset:0\n"
        "ds_read_b128 v[56:59], %[ra] offset:288\nds_read_b128 v[60:63], %[ra] offset:576\nds_read_b128 v[64:67], %[ra] offset:864\nds_read_b128 v[84:87], %[rb] offset:0\n"
        "ds_read_b128 v[88:91], %[rb] offset:1152\ns_waitcnt lgkmcnt(5)\nv_mfma_f32_16x16x32_f16 v[68:71], v[52:55], v[100:103], 0\nv_mfma_f32_16x16x32_f16 v[72:75], v[52:55], v[104:107], 0\n"
        "ds_read_b128 v[52:55], %[ra] offset:1152\ns_waitcnt lgkmcnt(5)\nv_mfma_f32_16x16x32_f16 v[76:79], v[56:59], v[100:103], 0\nv_mfma_f32_16x16x32_f16 v[80:83], v[56:59], v[104:107], 0\n"
        "ds_read_b128 v[56:59], %[ra] offset:1440\ns_waitcnt lgkmcnt(3)\nv_mfma_f32_16x16x32_f16 v[36:39], v[124:127], v[84:87], 0\nv_cvt_pk_f16_f32 v136, v68, v73\nv_cvt_pk_f16_f32 v137, v70, v75\n"
        "s_waitcnt lgkmcnt(2)\nv_mfma_f32_16x16x32_f16 v[40:43], v[124:127], v[88:91], 0\nv_mfma_f32_16x16x32_f16 v[68:71], v[60:63], v[100:103], 0\nv_cvt_pk_f16_f32 v138, v76, v81\n"
        "v_cvt_pk_f16_f32 v139, v78, v83\nv_mfma_f32_16x16x32_f16 v[72:75], v[60:63], v[104:107], 0\nds_read_b128 v[60:63], %[ra] offset:1728\n"
        "v_mfma_f32_16x16x32_f16 v[36:39], v[136:139], v[112:115], v[36:39]\nv_mfma_f32_16x16x32_f16 v[40:43], v[136:139], v[120:123], v[40:43]\n"
        "v_mfma_f32_16x16x32_f16 v[76:79], v[64:67], v[100:103], 0\nv_cvt_pk_f16_f32 v140, v68, v73\nv_cvt_pk_f16_f32 v141, v70, v75\nv_mfma_f32_16x16x32_f16 v[80:83], v[64:67], v[104:107], 0\n"
        "ds_read_b128 v[64:67], %[ra] offset:2016\ns_waitcnt lgkmcnt(3)\nv_mfma_f32_16x16x32_f16 v[68:71], v[52:55], v[100:103], 0\nv_mfma_f32_16x16x32_f16 v[72:75], v[52:55], v[104:107], 0\ns_nop 7\n"
        "v_cvt_pk_f16_f32 v142, v76, v81\nv_cvt_pk_f16_f32 v143, v78, v83\ns_nop 1\nv_mfma_f32_16x16x32_f16 v[36:39], v[140:143], v[108:111], v[36:39]\n"
        "v_mfma_f32_16x16x32_f16 v[40:43], v[140:143], v[116:119], v[40:43]\ns_waitcnt lgkmcnt(2)\nv_mfma_f32_16x16x32_f16 v[76:79], v[56:59], v[100:103], 0\nv_cvt_pk_f16_f32 v136, v68, v73\n"
        "v_cvt_pk_f16_f32 v137, v70, v75\nv_mfma_f32_16x16x32_f16 v[80:83], v[56:59], v[104:107], 0\nds_write_b128 %[wa], v[36:39] offset:0\ns_waitcnt lgkmcnt(2)\n"
        "v_mfma_f32_16x16x32_f16 v[68:71], v[60:63], v[100:103], 0\nv_mfma_f32_16x16x32_f16 v[72:75], v[60:63], v[104:107], 0\ns_nop 7\nv_cvt_pk_f16_f32 v138, v76, v81\n"
        "v_cvt_pk_f16_f32 v139, v78, v83\ns_nop 1\nv_mfma_f32_16x16x32_f16 v[40:43], v[136:139], v[112:115], v[40:43]\ns_waitcnt lgkmcnt(1)\nv_mfma_f32_16x16x32_f16 v[76:79], v[64:67], v[100:103], 0\n"
        "v_mfma_f32_16x16x32_f16 v[80:83], v[64:67], v[104:107], 0\nv_cvt_pk_f16_f32 v140, v68, v73\nv_cvt_pk_f16_f32 v141, v70, v75\ns_nop 7\nv_cvt_pk_f16_f32 v142, v76, v81\n"
        "v_cvt_pk_f16_f32 v143, v78, v83\ns_nop 1\nv_mfma_f32_16x16x32_f16 v[40:43], v[140:143], v[108:111], v[40:43]\ns_nop 7\nds_write_b128 %[wa], v[40:43] offset:256\ns_branch .LE%=\n.LC%=:\n"
        "s_sub_u32 %[cap], %[cap], 1\ns_cbranch_scc0 .LCy%=\ns_mov_b32 %[cap], 0\ns_branch .LD%=\n.LCy%=:\nbuffer_load_dwordx4 v[36:39], %[c16], %[xrs], 0 offen sc1\ns_waitcnt vmcnt(0)\n"
        "s_branch .LB%=\n.LI%=:\ns_sub_u32 %[cap], %[cap], 1\ns_cbranch_scc0 .LIy%=\ns_mov_b32 %[cap], 0\ns_branch .LM%=\n.LIy%=:\ns_sleep %[fp]\n"
        "buffer_load_dwordx4 v[40:43], %[c16], %[frs], 0 offen sc1\nbuffer_load_dwordx4 v[44:47], %[c16], %[frs], %[s1] offen sc1\ns_branch .LF%=\n.LE%=:\ns_waitcnt vmcnt(0)\ns_mov_b64 exec, 1\n"
        "ds_add_u32 %[cna], %[one] offset:8\ns_mov_b64 exec, -1\ns_setprio %[pbe]\ns_cmp_lt_u32 %[wq], 2\ns_cbranch_scc0 .LZ%=\nv_lshlrev_b32 v26, 16, v152\nv_and_b32 v27, 0xffff0000, v152\n"
        "v_cvt_f32_f16 v152, v156\nv_lshrrev_b32 v156, 16, v156\nv_lshlrev_b32 v28, 16, v153\nv_and_b32 v29, 0xffff0000, v153\nv_cvt_f32_f16 v153, v157\nv_lshrrev_b32 v157, 16, v157\n"
        "v_lshlrev_b32 v30, 16, v154\nv_and_b32 v31, 0xffff0000, v154\nv_cvt_f32_f16 v154, v158\nv_lshrrev_b32 v158, 16, v158\nv_lshlrev_b32 v32, 16, v155\nv_and_b32 v33, 0xffff0000, v155\n"
        "v_cvt_f32_f16 v155, v159\nv_lshrrev_b32 v159, 16, v159\nv_cvt_f32_f16 v156, v156\nv_fmac_f32 v26, v152, %[kd1]\nv_cvt_f32_f16 v157, v157\nv_fmac_f32 v28, v153, %[kd1]\n"
        "v_cvt_f32_f16 v158, v158\nv_fmac_f32 v30, v154, %[kd1]\nv_cvt_f32_f16 v159, v159\nv_fmac_f32 v32, v155, %[kd1]\nv_fmac_f32 v27, v156, %[kd1]\nv_fmac_f32 v29, v157, %[kd1]\n"
        "v_fmac_f32 v31, v158, %[kd1]\nv_fmac_f32 v33, v159, %[kd1]\ns_mov_b32 %[s1], 0x100000\n.LP%=:\nds_read_b32 %[pv], %[cna] offset:8\ns_waitcnt lgkmcnt(0)\nv_readfirstlane_b32 %[s0], %[pv]\n"
        "s_nop 0\ns_cmp_ge_i32 %[s0], 4\ns_cbranch_scc1 .LR%=\ns_sub_u32 %[s1], %[s1], 1\ns_cbranch_scc0 .LP%=\n.LR%=:\ns_cmp_lt_u32 %[wq], 1\ns_cbranch_scc1 .LS4_%=\n"
        "ds_read_b128 v[36:39], %[exr] offset:0\nds_read_b128 v[40:43], %[exr] offset:2048\nds_read_b128 v[44:47], %[exr] offset:4096\nds_read_b128 v[48:51], %[exr] offset:6144\n"
        "ds_read_b128 v[52:55], %[mr] offset:0\nds_read_b128 v[56:59], %[mr] offset:3072\nds_read_b128 v[60:63], %[frr] offset:0\nds_read_b128 v[64:67], %[frr] offset:1152\ns_waitcnt lgkmcnt(6)\n"
        "v_fmac_f32 v26, v36, %[kf]\nv_fmac_f32 v27, v37, %[kf]\nv_fmac_f32 v28, v38, %[kf]\nv_fmac_f32 v29, v39, %[kf]\nv_fmac_f32 v30, v40, %[kf]\nv_fmac_f32 v31, v41, %[kf]\n"
        "v_fmac_f32 v32, v42, %[kf]\nv_fmac_f32 v33, v43, %[kf]\ns_waitcnt lgkmcnt(4)\nv_fmac_f32 v26, v44, %[kf]\nv_fmac_f32 v27, v45, %[kf]\nv_fmac_f32 v28, v46, %[kf]\nv_fmac_f32 v29, v47, %[kf]\n"
        "v_fmac_f32 v30, v48, %[kf]\nv_fmac_f32 v31, v49, %[kf]\nv_fmac_f32 v32, v50, %[kf]\nv_fmac_f32 v33, v51, %[kf]\ns_waitcnt lgkmcnt(2)\nv_fmac_f32 v26, v52, %[kf4]\nv_fmac_f32 v27, v53, %[kf4]\n"
        "v_fmac_f32 v28, v54, %[kf4]\nv_fmac_f32 v29, v55, %[kf4]\nv_fmac_f32 v30, v56, %[kf4]\nv_fmac_f32 v31, v57, %[kf4]\nv_fmac_f32 v32, v58, %[kf4]\nv_fmac_f32 v33, v59, %[kf4]\n"
        "s_waitcnt lgkmcnt(0)\nv_fmac_f32 v26, v60, %[kf]\nv_fmac_f32 v27, v61, %[kf]\nv_fmac_f32 v28, v62, %[kf]\nv_fmac_f32 v29, v63, %[kf]\nv_fmac_f32 v30, v64, %[kf]\nv_fmac_f32 v31, v65, %[kf]\n"
        "v_fmac_f32 v32, v66, %[kf]\nv_fmac_f32 v33, v67, %[kf]\ns_branch .LS%=\n.LS4_%=:\nds_read_b128 v[36:39], %[exr] offset:0\nds_read_b128 v[40:43], %[exr] offset:2048\n"
        "ds_read_b128 v[44:47], %[exr] offset:4096\nds_read_b128 v[48:51], %[exr] offset:6144\nds_read_b128 v[52:55], %[mr] offset:0\nds_read_b128 v[56:59], %[mr] offset:3072\ns_waitcnt lgkmcnt(4)\n"
        "v_fmac_f32 v26, v36, %[kf]\nv_fmac_f32 v27, v37, %[kf]\nv_fmac_f32 v28, v38, %[kf]\nv_fmac_f32 v29, v39, %[kf]\nv_fmac_f32 v30, v40, %[kf]\nv_fmac_f32 v31, v41, %[kf]\n"
        "v_fmac_f32 v32, v42, %[kf]\nv_fmac_f32 v33, v43, %[kf]\ns_waitcnt lgkmcnt(2)\nv_fmac_f32 v26, v44, %[kf]\nv_fmac_f32 v27, v45, %[kf]\nv_fmac_f32 v28, v46, %[kf]\nv_fmac_f32 v29, v47, %[kf]\n"
        "v_fmac_f32 v30, v48, %[kf]\nv_fmac_f32 v31, v49, %[kf]\nv_fmac_f32 v32, v50, %[kf]\nv_fmac_f32 v33, v51, %[kf]\ns_waitcnt lgkmcnt(0)\nv_fmac_f32 v26, v52, %[kf4]\nv_fmac_f32 v27, v53, %[kf4]\n"
        "v_fmac_f32 v28, v54, %[kf4]\nv_fmac_f32 v29, v55, %[kf4]\nv_fmac_f32 v30, v56, %[kf4]\nv_fmac_f32 v31, v57, %[kf4]\nv_fmac_f32 v32, v58, %[kf4]\nv_fmac_f32 v33, v59, %[kf4]\n.LS%=:\n"
        "v_cvt_pk_bf16_f32 v36, v26, v27\nv_cvt_pk_bf16_f32 v37, v28, v29\nv_cvt_pk_bf16_f32 v38, v30, v31\nv_cvt_pk_bf16_f32 v39, v32, v33\nv_min_u32 v36, -2, v36\nv_min_u32 v37, -2, v37\n"
        "v_min_u32 v38, -2, v38\nv_min_u32 v39, -2, v39\nbuffer_store_dwordx4 v[36:39], %[od], %[ors], 0 offen sc0 sc1\nbuffer_store_dwordx4 v[36:39], %[oh], %[ors], 0 offen sc0 sc1\ns_nop 1\n.LZ%=:\n"
        : "+{v[100:103]}"(kt[0]), "+{v[104:107]}"(kt[1]), "+{v[108:111]}"(kt[2]), "+{v[112:115]}"(kt[3]), "+{v[116:119]}"(kt[4]), "+{v[120:123]}"(kt[5]), "+{v[124:127]}"(kt[6]), "+{v[128:131]}"(kt[7]), "+{v[132:135]}"(kt[8]), [pv] "=&{v16}"(pv), [s0] "=&s"(s0_), [s1] "=&s"(s1_), [cap] "=&s"(cap)
        : [ra] "{v1}"(ra), [rb] "{v2}"(rb), [wa] "{v3}"(wa), [cna] "{v4}"(cna), [one] "{v5}"(one), [exr] "{v6}"(exr), [frr] "{v7}"(frr), [mr] "{v8}"(mr_), [od] "{v9}"(od), [kf] "{v10}"(kd[0]), [kd1] "{v11}"(kd[1]), [c16] "{v12}"(c16), [uw] "{v17}"(uw_), [oh] "{v18}"(oh), [mo] "{v19}"(mo), [mrs] "s"(mrs), [ue] "{v20}"(ue), [kf4] "{v21}"(kf4), [xo] "{v22}"(xo), [fsa] "i"((layer ? FSH : FSH0) - FSX), [pbl] "i"(PBH), [v] "s"(v), [wq] "s"(wq), [ors] "s"(ors), [xrs] "s"(xrs), [frs] "s"(frs), [pbe] "i"(PBE), [fsx] "i"(FSX), [fp] "i"(FPOLL)
        : "memory", "scc", "v13", "v14", "v15", "v26", "v27", "v28", "v29", "v30", "v31", "v32", "v33", "v36", "v37", "v38", "v39", "v40", "v41", "v42", "v43", "v44", "v45", "v46", "v47", "v48", "v49", "v50", "v51", "v52", "v53", "v54", "v55", "v56", "v57", "v58", "v59", "v60", "v61", "v62", "v63", "v64", "v65", "v66", "v67", "v68", "v69", "v70", "v71", "v72", "v73", "v74", "v75", "v76", "v77", "v78", "v79", "v80", "v81", "v82", "v83", "v84", "v85", "v86", "v87", "v88", "v89", "v90", "v91", "v92", "v93", "v94", "v95", "v96", "v97", "v98", "v99", "v136", "v137", "v138", "v139", "v140", "v141", "v142", "v143", "v144", "v145", "v146", "v147", "v148", "v149", "v150", "v151", "v152", "v153", "v154", "v155", "v156", "v157", "v158", "v159", "v160", "v161", "v162", "v163", "v164", "v165", "v166", "v167", "vcc");
    }
    RETRY(8 * layer + 6, 0x10000 - cap);
  }
}

template <int layer>
__device__ __forceinline__ void b_phase(unsigned char* smem, const int b, const int t, const u16* xtin, float2* stats, u32* fstat,
                                        u16* xtout, u32x4* mark, const u16* wot) {
  const int kid = layer ? 3 : 5;
  const int lane = t & 63, w = __builtin_amdgcn_readfirstlane(t >> 6);
  const int gq = w < 4 ? 2 : (w >> 2) - 1, wq = w & 3;
  const int cG = CHAN(b, gq);
  ST_DECL
  if (w != 3) __builtin_amdgcn_s_setprio(PBU); else __builtin_amdgcn_s_setprio(3);
  if (w < 3) {
    const u32 sya = LDSA(smem) + SY_OFF + 256 * layer;
    lds_poll<SY_FREE, 2>(sya, 8);
    b_tables(smem, w, lane);
    lds_inc<80 + 12>(sya + 16 * w, 1u);
    ST(9);
  }
#define TSARG
  if (gq < 2) b_conv<true, layer>(smem, b, w, gq, wq, cG, xtin, stats, fstat, xtout, mark, wot TSARG);
  else b_conv<false, layer>(smem, b, w, gq, wq, cG, xtin, stats, fstat, xtout, mark, wot TSARG);
  ST(5);
  {
    LANE(lane);
    const int t = 64 * w + lane;
    ST_FLUSH(kid);
  }
}

struct MainArgs { const u16 *x, *wqkv, *wo; unsigned char* ws; u16* out; };
__global__ __launch_bounds__(768, 3) void k_main(MainArgs ma, KgenArgs ka) {
  __shared__ __attribute__((aligned(16))) unsigned char smem[A_LDS + 64];
  const int b_ = BIDX;
  u32x4 sink;
  int sb = b_, t0 = TIDX;
  asm volatile(
      "s_cmp_lt_u32 %1, 16\n\t"
      "s_cbranch_scc0 1f\n\t"
      "v_cmp_gt_u32 vcc, 0x80, %2\n\t"
      "s_and_b64 exec, exec, vcc\n\t"
      "s_getpc_b64 s[40:41]\n\t"
      "s_add_u32 s40, s40, _Z6k_main8MainArgs8KgenArgs@rel32@lo+4\n\t"
      "s_addc_u32 s41, s41, _Z6k_main8MainArgs8KgenArgs@rel32@hi+12\n\t"
      "s_lshl_b32 s42, %1, 11\n\t"
      "s_and_b32 s42, s42, 0x4000\n\t"
      "v_lshl_add_u32 %3, %2, 7, s42\n\t"
      "s_mov_b32 s42, kmain_tail-_Z6k_main8MainArgs8KgenArgs\n\t"
      "global_load_dword %0, %3, s[40:41]\n\t"
      "v_add_u32 %3, 0x8000, %3\n\t"
      "v_min_u32 %4, s42, %3\n\t"
      "global_load_dword %4, %4, s[40:41]\n\t"
      "v_add_u32 %3, 0x8000, %3\n\t"
      "v_min_u32 %3, s42, %3\n\t"
      "global_load_dword %3, %3, s[40:41]\n\t"
      "s_mov_b64 exec, -1\n\t"
      "1:"
      : "=&v"(sink[0]), "+s"(sb), "+v"(t0), "=&v"(sink[1]), "=&v"(sink[2]) :: "scc", "vcc", "s40", "s41", "s42", "memory");
  unsigned char* const ws_ = ma.ws;
  const int w64 = __builtin_amdgcn_readfirstlane(t0) & ~63;
#define GETT(t) asm volatile("v_mbcnt_lo_u32_b32 %0, -1, %1\n\tv_mbcnt_hi_u32_b32 %0, -1, %0" : "=v"(t) : "s"(w64))
#define FRESH int t, b = b_; unsigned char* ws = ws_; GETT(t); asm volatile("" : "+s"(b), "+s"(ws));
#define LAYER(L) { \
    { FRESH unsigned char* ho = ws + WS_O + L * HO_BYTES; \
      a_phase<L>(smem, b, t, L ? (const u16*)(ws + WS_IM1) : ma.x, ma.wqkv, ma.wo, ws, (u16*)ho, (u16*)(ho + HO_X1), (float2*)(ho + HO_ST), ka, sink, tg0, tg1); } \
    { FRESH unsigned char* ho = ws + WS_O + L * HO_BYTES; \
      b_phase<L>(smem, b, t, (u16*)(ho + HO_X1), (float2*)(ho + HO_ST), (u32*)(ho + HO_FS), (u16*)(ws + (L ? WS_FIN : WS_IM1)), (u32x4*)(ws + WS_O + HO_BYTES), (const u16*)(ws + WS_WOT)); } }
  u32 tg0, tg1;
  LAYER(0)
  LAYER(1)
  {
    FRESH
    const int lane = t & 63, w = __builtin_amdgcn_readfirstlane(t >> 6);
    ST_DECL
    const int i7 = (b >> 3) & 7;
    if (w == 2 || w == 3 || (w == 1 && i7 < 4)) {
      const int rb = 4 * (b & 7) + (b >> 6), cg = i7 + (w == 2 ? 12 : w == 3 ? 4 : 0);
      const __amdgpu_buffer_rsrc_t rs = __builtin_amdgcn_make_buffer_rsrc(ws + WS_FIN + (size_t)(rb * 640 + cg * 32) * 128, 0, 4096, 0x00020000);
      const __amdgpu_buffer_rsrc_t os = __builtin_amdgcn_make_buffer_rsrc(ma.out + (size_t)rb * 64 * D_ + cg * 32, 0, 63 * D_ * 2 + 64, 0x00020000);
      u32 sw = LDSA(smem) + X1T_OFF + (w - 1) * 4608, sr = sw + (8 * (lane >> 4) + ((lane >> 2) & 3)) * 144 + 8 * (lane & 3);
      sw += (lane >> 3) * 144 + (lane & 7) * 16;
      int l16 = lane * 16, oo = (lane & 15) * (D_ * 2) + (lane >> 4) * 16;
      PIN(sw); PIN(sr); PIN(l16); PIN(oo);
      __builtin_amdgcn_s_setprio(3);
      if (w != 1) lds_poll<88, 2>(LDSA(smem) + SY_OFF + 256 + (w == 2 ? 16 : 0), 4);
      if (w == 1) __builtin_amdgcn_s_sleep(TSLEEP - 8); else __builtin_amdgcn_s_sleep(TSLEEP);
      u32x4 v[4];
      int spins = 0;
      _Pragma("unroll 1")
      while (true) {
        asm volatile("" ::: "memory");
        u32 m = 0;
#pragma unroll
        for (int i = 0; i < 4; ++i) v[i] = __builtin_amdgcn_raw_buffer_load_b128(rs, l16, i * 1024, 16);
#pragma unroll
        for (int i = 0; i < 4; ++i) {
          asm volatile("ds_write_b128 %0, %1 offset:%2" :: "v"(sw), "v"(v[i]), "i"(i * 1152) : "memory");
          m = umax3(umax3(m, v[i][0], v[i][1]), v[i][2], v[i][3]);
        }
        if (__builtin_amdgcn_ballot_w64(m == 0xffffffffu) == 0 || ++spins > GCAP) break;
        __builtin_amdgcn_s_sleep(FPOLL);
      }
      RETRY(7, spins);
      ST(1);
      s16x4 f[8];
      asm volatile("ds_read_b64_tr_b16 %0, %8\n\tds_read_b64_tr_b16 %1, %8 offset:576\n\tds_read_b64_tr_b16 %2, %8 offset:32\n\tds_read_b64_tr_b16 %3, %8 offset:608\n\t"
                   "ds_read_b64_tr_b16 %4, %8 offset:64\n\tds_read_b64_tr_b16 %5, %8 offset:640\n\tds_read_b64_tr_b16 %6, %8 offset:96\n\tds_read_b64_tr_b16 %7, %8 offset:672\n\ts_waitcnt lgkmcnt(0)"
                   : "=&v"(f[0]), "=&v"(f[1]), "=&v"(f[2]), "=&v"(f[3]), "=&v"(f[4]), "=&v"(f[5]), "=&v"(f[6]), "=&v"(f[7]) : "v"(sr) : "memory");
#pragma unroll
      for (int i = 0; i < 4; ++i)
        __builtin_amdgcn_raw_buffer_store_b128(__builtin_bit_cast(u32x4, __builtin_shufflevector(f[2 * i], f[2 * i + 1], 0, 1, 2, 3, 4, 5, 6, 7)), os, oo, i * 16 * D_ * 2, OAUX);
    }
    ST(2);
    ST_FLUSH(4);
  }
}
extern "C" __device__ __attribute__((used, noinline)) void kmain_tail() {}
extern "C" void kernel_launch(void* const* d_in, const int* in_sizes, int n_in, void* d_out, int out_size,
                              void* d_ws, size_t ws_size, hipStream_t stream) {
  unsigned char* ws = (unsigned char*)d_ws;
  KgenArgs ka;
  ka.logA = (const u16*)d_in[4]; ka.Aim = (const u16*)d_in[5]; ka.Cre = (const u16*)d_in[6]; ka.Cim = (const u16*)d_in[7];
  ka.logdt = (const u16*)d_in[8]; ka.Dskip = (const u16*)d_in[9];
  MainArgs ma;
  ma.x = (const u16*)d_in[0];
  ma.wqkv = (const u16*)d_in[1]; ma.wo = (const u16*)d_in[2];
  ma.ws = ws; ma.out = (u16*)d_out;
  k_main<<<256, 768, 0, stream>>>(ma, ka);
}
#if defined(__HIP_DEVICE_COMPILE__) && !defined(ALLOW_PK)
#pragma clang attribute pop
#endif
```

```cpp
#if defined(__HIP_DEVICE_COMPILE__) && !defined(ALLOW_PK)
#pragma clang attribute push(__attribute__((target("no-packed-fp32-ops"))), apply_to = function)
#endif
#include <hip/hip_runtime.h>
#include <hip/hip_bf16.h>
#include <stdint.h>

#define BIDX ((int)__builtin_amdgcn_workgroup_id_x())
#define TIDX ((int)__builtin_amdgcn_workitem_id_x())
typedef unsigned short u16;
typedef unsigned int u32;
typedef unsigned long long u64;
typedef __attribute__((ext_vector_type(8))) short bf16x8;
typedef __attribute__((ext_vector_type(4))) short s16x4;
typedef __attribute__((ext_vector_type(4))) float f32x4;
typedef __attribute__((ext_vector_type(2))) float f32x2;
typedef __attribute__((ext_vector_type(2))) __bf16 bf2v;
typedef __attribute__((address_space(3))) s16x4* lds_s16x4_p;
typedef __attribute__((ext_vector_type(8))) u32 u32x8;
typedef __attribute__((ext_vector_type(4))) u32 u32x4;
typedef __attribute__((ext_vector_type(2))) u32 u32x2;
typedef __attribute__((address_space(1))) u32 gu32;
typedef __attribute__((address_space(1))) u64 gu64;

#define S_ 2048
#define XP 2112
#define D_ 640
#define NST 192
#define CHAN(b, q) ((q) == 2 ? (b) >> 1 : 128 + 256 * (q) + (b))

#define WS_WQ  0
#define WS_WOT (WS_WQ + 1920 * 640 * 2)
#define WS_CW  (WS_WOT + 640 * 640 * 2)
#define WS_KP  (WS_CW + 4 * 2048 * 4)
#define WS_IM0 (WS_KP + 640 * 768 * 4)
#define WS_IM1 (WS_IM0 + 32 * 102400)
#define HO_ST (2048 * 640 * 2)
#define HO_X1 (HO_ST + 2048 * 128)
#define HO_FS (HO_X1 + 640 * XP * 2)
#define HO_BYTES (256 * 1392 * 16)
#define WS_O   (WS_IM1 + 32 * 102400)
#define WS_FIN (WS_O + 2 * HO_BYTES)
#define WS_FLG (WS_FIN + 2048 * 640 * 2)
#define FL_W 128
#define FL_M (FL_W + 480 * 128)
#define FL_M2 (FL_M + 384 * 128)
#define FL_END (FL_M2 + 256 * 8)
#define WS_END (WS_FLG + FL_END)

#define RETRY(i, n)
#define ST_DECL
#define ST(i)
#define ST_FLUSH(kid)

__device__ __forceinline__ float bf2f(u16 x) { return __uint_as_float(((u32)x) << 16); }
__device__ __forceinline__ u32 pack2(float a, float b) {
  f32x2 v = {a, b};
  bf2v r = __builtin_convertvector(v, bf2v);
  return __builtin_bit_cast(u32, r);
}
__device__ __forceinline__ float lo_f(u32 w) { return __uint_as_float(w << 16); }
__device__ __forceinline__ float hi_f(u32 w) { return __uint_as_float(w & 0xffff0000u); }
__device__ __forceinline__ float rbf(float f) { return lo_f(pack2(f, f)); }
__device__ __forceinline__ bf16x8 as_frag(uint4 v) {
  union { uint4 u; bf16x8 f; } c; c.u = v; return c.f;
}
__device__ __forceinline__ float2 cmul(float2 a, float2 b) {
  return make_float2(a.x * b.x - a.y * b.y, a.x * b.y + a.y * b.x);
}
__device__ __forceinline__ float2 cexp_scaled(float zr, float zi, float e) {
  float ar = zr * e, ai = zi * e;
  float m = __expf(ar);
  const float INV2PI_HI = 0.15915493667125702f;
  const float INV2PI_LO = 6.4206382432985265e-09f;
  float t = ai * INV2PI_HI;
  float err = fmaf(ai, INV2PI_HI, -t);
  float r = t - rintf(t);
  r = r + (err + ai * INV2PI_LO);
  float s = __builtin_amdgcn_sinf(r), c = __builtin_amdgcn_cosf(r);
  return make_float2(m * c, m * s);
}
__device__ __forceinline__ void cexp2(f32x2 zr, f32x2 zi, float e, f32x2& re, f32x2& im) {
  f32x2 ar = zr * e, ai = zi * e;
  f32x2 m = {__expf(ar.x), __expf(ar.y)};
  const float INV2PI_HI = 0.15915493667125702f;
  const float INV2PI_LO = 6.4206382432985265e-09f;
  f32x2 t = ai * INV2PI_HI;
  f32x2 err = {fmaf(ai.x, INV2PI_HI, -t.x), fmaf(ai.y, INV2PI_HI, -t.y)};
  f32x2 r = t - (f32x2){rintf(t.x), rintf(t.y)};
  r = r + (err + ai * INV2PI_LO);
  re = m * (f32x2){__builtin_amdgcn_cosf(r.x), __builtin_amdgcn_cosf(r.y)};
  im = m * (f32x2){__builtin_amdgcn_sinf(r.x), __builtin_amdgcn_sinf(r.y)};
}
__device__ __forceinline__ float dpp_ror8(float x) {
  return __builtin_bit_cast(float, __builtin_amdgcn_update_dpp(0, __builtin_bit_cast(int, x), 0x128, 0xf, 0xf, false));
}
__device__ __forceinline__ float BCAST0(float x) {
  return __builtin_bit_cast(float, __builtin_amdgcn_update_dpp(0, __builtin_bit_cast(int, x), 0x150, 0xf, 0xf, false));
}
__device__ __forceinline__ float rowsum16(float x) {
  x += __builtin_bit_cast(float, __builtin_amdgcn_update_dpp(0, __builtin_bit_cast(int, x), 0xB1, 0xf, 0xf, false));
  x += __builtin_bit_cast(float, __builtin_amdgcn_update_dpp(0, __builtin_bit_cast(int, x), 0x4E, 0xf, 0xf, false));
  x += __builtin_bit_cast(float, __builtin_amdgcn_update_dpp(0, __builtin_bit_cast(int, x), 0x141, 0xf, 0xf, false));
  x += __builtin_bit_cast(float, __builtin_amdgcn_update_dpp(0, __builtin_bit_cast(int, x), 0x140, 0xf, 0xf, false));
  return x;
}
__device__ __forceinline__ float dpp_xor1(float x) {
  return __builtin_bit_cast(float, __builtin_amdgcn_update_dpp(0, __builtin_bit_cast(int, x), 0xB1, 0xf, 0xf, false));
}
__device__ __forceinline__ bf16x8 tr_frag(const u16* p0, const u16* p1) {
  s16x4 a = __builtin_amdgcn_ds_read_tr16_b64_v4i16((lds_s16x4_p)p0);
  s16x4 b = __builtin_amdgcn_ds_read_tr16_b64_v4i16((lds_s16x4_p)p1);
  return __builtin_shufflevector(a, b, 0, 1, 2, 3, 4, 5, 6, 7);
}

#define X1_P 248
__device__ __forceinline__ float dot2bf(u32 a, u32 b, float c) {
  return __builtin_amdgcn_fdot2_f32_bf16(__builtin_bit_cast(bf2v, a), __builtin_bit_cast(bf2v, b), c, false);
}
__device__ __forceinline__ float xsum16_32(float x) {
  auto s = __builtin_amdgcn_permlane16_swap(__float_as_uint(x), __float_as_uint(x), false, false);
  x = __uint_as_float(s[0]) + __uint_as_float(s[1]);
  auto t = __builtin_amdgcn_permlane32_swap(__float_as_uint(x), __float_as_uint(x), false, false);
  return __uint_as_float(t[0]) + __uint_as_float(t[1]);
}
__device__ __forceinline__ float xmax16_32(float x) {
  auto s = __builtin_amdgcn_permlane16_swap(__float_as_uint(x), __float_as_uint(x), false, false);
  x = fmaxf(__uint_as_float(s[0]), __uint_as_float(s[1]));
  auto t = __builtin_amdgcn_permlane32_swap(__float_as_uint(x), __float_as_uint(x), false, false);
  return fmaxf(__uint_as_float(t[0]), __uint_as_float(t[1]));
}

#define WT " sc0 sc1"
template <class T> __device__ __forceinline__ void st16(void* p, T v) {
  asm volatile("global_store_dwordx4 %0, %1, off" WT "\n\ts_nop 1" :: "v"(p), "v"(v) : "memory");
}
template <class T> __device__ __forceinline__ void st8(void* p, T v) {
  asm volatile("global_store_dwordx2 %0, %1, off" WT :: "v"(p), "v"(v) : "memory");
}

typedef __attribute__((ext_vector_type(8))) _Float16 f16x8;
typedef __attribute__((ext_vector_type(2))) _Float16 f16x2;
__device__ __forceinline__ u32 packh(float a, float b) {
  f32x2 v = {a, b};
  return __builtin_bit_cast(u32, __builtin_convertvector(v, f16x2));
}
__device__ __forceinline__ float lo_h(u32 w) { return (float)__builtin_bit_cast(f16x2, w).x; }
__device__ __forceinline__ float hi_h(u32 w) { return (float)__builtin_bit_cast(f16x2, w).y; }
__device__ __forceinline__ f16x8 as_hfrag(u32 a, u32 b, u32 c, u32 d) { return __builtin_bit_cast(f16x8, (u32x4){a, b, c, d}); }
__device__ __forceinline__ float wave_max(float x) {
  x = fmaxf(x, __builtin_bit_cast(float, __builtin_amdgcn_update_dpp(0, __builtin_bit_cast(int, x), 0xB1, 0xf, 0xf, false)));
  x = fmaxf(x, __builtin_bit_cast(float, __builtin_amdgcn_update_dpp(0, __builtin_bit_cast(int, x), 0x4E, 0xf, 0xf, false)));
  x = fmaxf(x, __builtin_bit_cast(float, __builtin_amdgcn_update_dpp(0, __builtin_bit_cast(int, x), 0x141, 0xf, 0xf, false)));
  x = fmaxf(x, __builtin_bit_cast(float, __builtin_amdgcn_update_dpp(0, __builtin_bit_cast(int, x), 0x140, 0xf, 0xf, false)));
  return xmax16_32(x);
}
template <int T> __device__ __forceinline__ void pow2_scale(float m, float& s, float& inv) {
  int e = (int)(__float_as_uint(m) >> 23);
  e = min(max(e, T + 2), 250);
  s = __uint_as_float((u32)(254 + T - e) << 23);
  inv = __uint_as_float((u32)(e - T) << 23);
}
__device__ __forceinline__ void cexpr(float zl, float e, float zh, float ep, float& re, float& im) {
  const float m = __builtin_amdgcn_exp2f(zl * e);
  const float r = zh * ep;
  re = m * __builtin_amdgcn_cosf(r);
  im = m * __builtin_amdgcn_sinf(r);
}
struct KgenArgs { const u16 *logA, *Aim, *Cre, *Cim, *logdt, *Dskip; };
#define LDSA(p) ((u32)(size_t)(__attribute__((address_space(3))) void*)(p))
#define PIN(x) asm volatile("" : "+v"(x))
template <int OFF> __device__ __forceinline__ void lds_inc(u32 a, u32 one) {
  asm volatile("s_mov_b64 exec, 1\n\tds_add_u32 %0, %1 offset:%2\n\ts_mov_b64 exec, -1" :: "v"(a), "v"(one), "i"(OFF) : "memory");
}
template <int OFF, int SLEEP> __device__ __forceinline__ void lds_poll(u32 a, int target) {
  u32 v; int s, cnt;
  asm volatile(
      "s_mov_b32 %2, 0x100000\n\t"
      "1:\n\t"
      "ds_read_b32 %0, %3 offset:%5\n\t"
      "s_waitcnt lgkmcnt(0)\n\t"
      "v_readfirstlane_b32 %1, %0\n\t"
      "s_nop 0\n\t"
      "s_cmp_ge_i32 %1, %4\n\t"
      "s_cbranch_scc1 2f\n\t"
      "s_sleep %6\n\t"
      "s_sub_u32 %2, %2, 1\n\t"
      "s_cbranch_scc0 1b\n\t"
      "2:\n\t"
      : "=&v"(v), "=&s"(s), "=&s"(cnt) : "v"(a), "s"(target), "i"(OFF), "i"(SLEEP) : "memory", "scc");
}
template <int OFF, int SLEEP> __device__ __forceinline__ void lds_poll2(u32 a, int target, u32& v) {
  int s, cnt;
  asm volatile(
      "v_readfirstlane_b32 %1, %0\n\t"
      "s_mov_b32 %2, 0x100000\n\t"
      "s_cmp_ge_i32 %1, %4\n\t"
      "s_cbranch_scc1 2f\n\t"
      "1:\n\t"
      "ds_read_b32 %0, %3 offset:%5\n\t"
      "s_waitcnt lgkmcnt(0)\n\t"
      "v_readfirstlane_b32 %1, %0\n\t"
      "s_nop 0\n\t"
      "s_cmp_ge_i32 %1, %4\n\t"
      "s_cbranch_scc1 2f\n\t"
      "s_sleep %6\n\t"
      "s_sub_u32 %2, %2, 1\n\t"
      "s_cbranch_scc0 1b\n\t"
      "2:\n\t"
      : "+v"(v), "=&s"(s), "=&s"(cnt) : "v"(a), "s"(target), "i"(OFF), "i"(SLEEP) : "memory", "scc");
}
#define SY_PART 0
#define SY_FREE 4
#define SY_WO 8
#define SY_FIN 24
#define SY_X1 32
#define SY_FS 36
#define SY_WI 40
#define SY_KP 44
#define SY_MK 48
#define SY_RV 52
#define SY_TI 56
#define SY_PG 64
#define SY_X 128
#define SY_STAT 168
#define SY_W 172
#define SY_RED 192
#define SY_Z 224
template <int SLEEP> __device__ __forceinline__ void pg_poll(u32 a, u32 target) {
  int cnt = 1 << 20;
  _Pragma("unroll 1")
  while (true) {
    u32x4 v;
    asm volatile("ds_read_b128 %0, %1 offset:%2\n\ts_waitcnt lgkmcnt(0)" : "=v"(v) : "v"(a), "i"(SY_PG) : "memory");
    if ((u32)__builtin_amdgcn_readfirstlane(min(min(v[0], v[1]), min(v[2], v[3]))) >= target || --cnt < 0) break;
    if (SLEEP) __builtin_amdgcn_s_sleep(SLEEP);
  }
}
__device__ __forceinline__ void kgen_units(int lane, const float* Kd, int nk, f32x4 (&acc)[3]) {
  const int i = lane & 15, kg = lane >> 4;
  const float eE = 8.f * (float)i;
  const int r_ = i & 3;
  const float e0 = (float)((i < 8 ? 2 * r_ : r_ == 0 ? 512 : r_ == 1 ? 516 : 512 * r_) + 32 * (i & 4));
  const float e0p = i < 8 ? e0 : 0.25f * e0;
  typedef __attribute__((address_space(3))) const f32x4* lf4;
  typedef __attribute__((address_space(3))) const u32x4* lu4;
  u32 pp = LDSA(Kd) + 16 * kg, pc = pp + (i < 8 ? 0 : 256);
#pragma unroll 1
  for (int ks = 0; ks < nk; ++ks) {
    PIN(pp); PIN(pc);
    const f32x4 zl = *(lf4)(size_t)pp, zs = ((lf4)(size_t)pc)[16], zh = ((lf4)(size_t)pp)[16];
    const f32x4 lc = ((lf4)(size_t)pp)[48], ph = ((lf4)(size_t)pp)[64];
    const u32x4 ma[2] = {((lu4)(size_t)pc)[80], ((lu4)(size_t)pp)[112]};
    pp += 64; pc += 64;
    float r[4], q[4], gr[4], gi[4];
#pragma unroll
    for (int e = 0; e < 4; ++e) cexpr(zl[e], e0, zs[e], e0p, gr[e], gi[e]);
#pragma unroll
    for (int e = 0; e < 4; ++e) {
      const float m = __builtin_amdgcn_exp2f(fmaf(zl[e], eE, lc[e])), th = fmaf(zh[e], -eE, ph[e]);
      r[e] = m * __builtin_amdgcn_cosf(th);
      q[e] = m * __builtin_amdgcn_sinf(th);
    }
    const f16x8 eh = as_hfrag(packh(r[0], r[1]), packh(r[2], r[3]), packh(q[0], q[1]), packh(q[2], q[3]));
    const u32 g[4] = {packh(gr[0], gr[1]), packh(gr[2], gr[3]), packh(gi[0], gi[1]), packh(gi[2], gi[3])};
    acc[0] = __builtin_amdgcn_mfma_f32_16x16x32_f16(as_hfrag(g[0], g[1], g[2], g[3]), eh, acc[0], 0, 0, 0);
#pragma unroll
    for (int m = 0; m < 2; ++m) {
      u32 d[4];
#pragma unroll
      for (int e = 0; e < 2; ++e) {
        const f16x2 xr = __builtin_bit_cast(f16x2, g[e]), xi = __builtin_bit_cast(f16x2, g[2 + e]);
        const u32 ur = ma[m][e], ui = ma[m][2 + e];
        const f16x2 mr = __builtin_bit_cast(f16x2, ur), mi = __builtin_bit_cast(f16x2, ui);
        d[e] = __builtin_bit_cast(u32, __builtin_elementwise_fma(xr, mr, -(xi * mi)));
        d[2 + e] = __builtin_bit_cast(u32, __builtin_elementwise_fma(xi, mr, xr * mi));
      }
      acc[m + 1] = __builtin_amdgcn_mfma_f32_16x16x32_f16(as_hfrag(d[0], d[1], d[2], d[3]), eh, acc[m + 1], 0, 0, 0);
    }
  }
}
template <int NQ> struct KgenIn { u16 la[NQ], ai[NQ], cr[NQ], ci[NQ], ld, ds; };
template <int NQ, int MODE>
__device__ __forceinline__ void kgen_load(int c, int ks0, int nk, int lane, const KgenArgs& ka, KgenIn<NQ>& in) {
  in.ld = ka.logdt[c];
#pragma unroll
  for (int q = 0; q < NQ; ++q) {
    const int n = 64 * q + lane, gi = c * NST + 16 * ks0 + (n < 16 * nk ? n : 0);
    in.la[q] = ka.logA[gi]; in.ai[q] = ka.Aim[gi]; in.cr[q] = ka.Cre[gi]; in.ci[q] = ka.Cim[gi];
  }
  if (MODE == 1) in.ds = ka.Dskip[c];
}
#define ATC0 1.591508832e-01f
#define ATC1 -5.292989150e-02f
#define ATC2 3.075435195e-02f
#define ATC3 -1.841891266e-02f
#define ATC4 8.268183977e-03f
#define ATC5 -1.824840286e-03f
#define KU 4
__device__ __forceinline__ float kgen_wave(int lane, const KgenIn<1>& in, float* Kd, f32x4 (&acc)[3]) {
#pragma unroll
  for (int m = 0; m < 3; ++m) acc[m] = (f32x4){0.f, 0.f, 0.f, 0.f};
  float cs, csi;
  const u32 za[1] = {in.la[0] | ((u32)in.ai[0] << 16)}, ca[1] = {in.cr[0] | ((u32)in.ci[0] << 16)};
  const float dt = rbf(__expf(bf2f(in.ld)));
  pow2_scale<10>(wave_max(fmaxf(fabsf(bf2f(in.cr[0])), fabsf(bf2f(in.ci[0])))), cs, csi);
  {
    const float zl = (-rbf(__expf(lo_f(za[0]))) * dt) * 1.4426950408889634f, zi = hi_f(za[0]) * dt;
    const float zh = zi * 0.15915494309189535f;
    const float z1 = zh - rintf(zh);
    float ar, ai, br, bi;
    cexpr(zl, 1.0f, z1, 1.0f, ar, ai);
    cexpr(zl, 256.0f, 256.0f * zh - rintf(256.0f * zh), 1.0f, br, bi);
    Kd[lane] = zl;
    Kd[64 + lane] = z1;
    Kd[128 + lane] = 4.0f * zh - rintf(4.0f * zh);
    const float cr = lo_f(ca[0]) * cs, ci = hi_f(ca[0]) * cs, ax = fabsf(cr), ay = fabsf(ci);
    const float tq = fminf(ax, ay) * __builtin_amdgcn_rcpf(fmaxf(fmaxf(ax, ay), 1e-30f)), t2 = tq * tq;
    float an = tq * fmaf(t2, fmaf(t2, fmaf(t2, fmaf(t2, fmaf(t2, ATC5, ATC4), ATC3), ATC2), ATC1), ATC0);
    an = ay > ax ? 0.25f - an : an;
    an = cr < 0.f ? 0.5f - an : an;
    Kd[192 + lane] = 0.5f * __builtin_amdgcn_logf(fmaf(cr, cr, ci * ci));
    Kd[256 + lane] = ci < 0.f ? an : -an;
    u16* kh = (u16*)(Kd + 320) + 8 * (lane >> 2) + (lane & 3);
    const u32 h0 = packh(ar, ai), h1 = packh(br * br - bi * bi, 2.0f * br * bi), h2 = packh(br, bi);
    kh[0] = (u16)h0; kh[4] = (u16)(h0 >> 16);
    kh[128] = (u16)h1; kh[132] = (u16)(h1 >> 16);
    kh[256] = (u16)h2; kh[260] = (u16)(h2 >> 16);
    asm volatile("s_waitcnt lgkmcnt(0)" ::: "memory");
    kgen_units(lane, Kd, KU, acc);
    asm volatile("" ::: "memory");
  }
#pragma unroll
  for (int m = 0; m < 3; ++m) acc[m] *= csi;
  return dt;
}
__device__ __forceinline__ void kgen_fin(int lane, float dt, float dsk, float* Kd, f32x4 (&acc)[3]) {
  const int i = lane & 15, kg = lane >> 4;
  float km = 0.f, ks_, ksi;
#pragma unroll
  for (int m = 0; m < 3; ++m)
#pragma unroll
    for (int r = 0; r < 4; ++r) km = fmaxf(km, fabsf(acc[m][r]));
  pow2_scale<14>(wave_max(km), ks_, ksi);
#pragma unroll
  for (int m = 0; m < 3; ++m) acc[m] *= ks_;
  u16* kq = (u16*)Kd;
  const u32 t0 = packh(acc[2][0], acc[2][1]), t1 = packh(acc[2][2], acc[2][3]);
  if (kg < 2) {
    *(u32x4*)&kq[128 * kg + 8 * i] = (u32x4){packh(acc[0][0], acc[1][0]), packh(acc[0][1], acc[1][1]), packh(acc[0][2], acc[1][2]), packh(acc[0][3], acc[1][3])};
    *(u32x2*)&kq[256 + 64 * kg + 4 * i] = (u32x2){t0, t1};
  } else {
    const u32 s1 = packh(acc[0][2], acc[0][3]);
    *(u32*)&kq[320 + 32 * kg + 2 * i] = packh(acc[0][0], acc[0][1]);
    *(u32*)&kq[384 + 32 * kg + 2 * i] = t0;
    u16* kr = kq + 480 + lane;
    kr[0] = (u16)s1; kr[64] = (u16)(s1 >> 16); kr[32] = (u16)t1; kr[96] = (u16)(t1 >> 16);
    if (lane == 32) kq[640] = (u16)packh(acc[1][3], 0.f);
  }
  if (lane == 0) { Kd[384] = (dt * (2.0f / 256.0f)) * ksi; Kd[385] = dsk * (1.0f / 256.0f); }
}

#define A_R1 105600
#define A_R2 (A_R1 + 80 * X1_P * 2)
#define SY_OFF (A_R2 + 1280)
#define CW_OFF (SY_OFF + 512)
#define KQ_OFF (CW_OFF + 2304)
#define BND_OFF (KQ_OFF + 3 * 3072)
#define WO_OFF (A_R2 - 102400)
#define A_LDS (BND_OFF + 4096)
#define VMCNT(n) asm volatile("s_waitcnt vmcnt(" #n ")" ::: "memory")
template <int N> __device__ __forceinline__ void vmwait() { asm volatile("s_waitcnt vmcnt(%0)" :: "i"(N) : "memory"); }
__device__ __forceinline__ void vmwaitn(int n) {
  switch (n) {
    case 0: VMCNT(0); break; case 5: VMCNT(5); break; case 10: VMCNT(10); break; case 15: VMCNT(15); break; default: VMCNT(20); break;
  }
}
__device__ __forceinline__ u32 umax3(u32 a, u32 b, u32 c) { return max(max(a, b), c); }
#define XDEP 2
#define RS0 4
#define RS1 5
#define OSLEEP1 2
#define SSLEEP 11
#define OSLEEP 14
#define PG1B 0
#define PG1 2
#define PG2 2
#define XSLEEP 14
#define WOMODE 2
#define PG1P 3
#define XPOLL 4
#define XEARLY 1
#define WPW 3
#define WSLEEP0 0
#define GCAP (1 << 16)
#define EX_OFF 1024
#define X1T_OFF 25600
#define MARK16 ((u32x4){0xffffffffu, 0xffffffffu, 0xffffffffu, 0xffffffffu})
#define WSLEEP 2
#define IMPOS 2
#define XMPOS 1
#define IMSL 0
#define MSLEEP 32
#define STX(i)
#define STY(i) ST(i)
#define LANE(x) int x; asm volatile("v_mbcnt_lo_u32_b32 %0, -1, 0\n\tv_mbcnt_hi_u32_b32 %0, -1, %0" : "=v"(x))
#define NST0 12
#define NST1 10
#define PF 2
#define PTX 1
#define PTW 1
#define PTA 2
#define WOAT 32
#define FPOS 2
#define PFK 0
#define WARM 0
#define WOSL 0
#define EXP 0
#define XSSL0 24
#define XE 2
#define MSL0 1
#define MSL1 2
#define MSL2 2
#define P0E 2
#define P1F 1
#define MKW 4
#define MKAT 12
#define DQ 5
#define XSAT 20
#define CWAT 4
#define OSLEEP2 0
#define CWW 6
#define PFS 3
#define PTA1 2
#define APS 1
#define OSLEEP3 8
#define PFD 3
#define XSLEEP1 10
#define XJ0 2
#define XJ1 3
#define XSAT0 16
#define PTO 1
#define MKPOS 1
#define PCW 3
#define XSRT 8
#define XJ2 XJ1
#define WOAT0 28
#define OSLEEPB 9
#define XE3 0
#define GREM 0
#define MPW 9
#define EWF 0
#define G1F 1
#define RVF 0
#define XSSL 5
template <int layer>
__device__ __forceinline__ void a_phase(unsigned char* smem, const int b, const int t, const u16* xt, const u16* wqkv, const u16* wo, unsigned char* ws,
                                        u16* obuf, u16* xtout, float2* stats, const KgenArgs& ka, u32x4& sink, u32& tg0, u32& tg1, u32x4 (&wst)[8]) {
  u16* wq = (u16*)(ws + WS_WQ);
  u16* wot = (u16*)(ws + WS_WOT);
  const __amdgpu_buffer_rsrc_t frs = __builtin_amdgcn_make_buffer_rsrc(ws + WS_FLG, 0, FL_END, 0x00020000);
  const int kid = layer ? 2 : 1;
  u16* At = (u16*)smem;
  u16* St = (u16*)smem;
  u16* Sc = (u16*)(smem + A_R1);
  const int xcd = b & 7, idx = b >> 3;
  const int rb = 8 * (xcd >> 1) + (idx & 7), hg = 4 * (xcd & 1) + (idx >> 3), s0 = rb * 64;
  const int rot = hg >> 1, xs0 = 3 * rot + 2 * (hg & 1);
#define PAIR(p, r) ((p) < 6 ? (p) : 6 + (((p) - 6 + (r)) & 3))
  const int lane = t & 63, w = __builtin_amdgcn_readfirstlane(t >> 6);
  const int l15 = lane & 15, g = lane >> 4;
  u32 sya = LDSA(smem) + SY_OFF + 256 * layer, one = 1u;
  const bf16x8 onesf = as_frag(make_uint4(0x3f803f80u, 0x3f803f80u, 0x3f803f80u, 0x3f803f80u));
  u32x2 tag;
  ST_DECL

  const __amdgpu_buffer_rsrc_t xsrs = __builtin_amdgcn_make_buffer_rsrc(ws + WS_IM0 + layer * 262144, 0, 262144, 0x00020000);
  if (!layer) {
#define FLAG(o) asm volatile("buffer_store_dwordx2 %0, %3, %1, %2 offen sc1" :: "v"(tag), "s"(frs), "s"(o), "v"(g ? -1 : lane * 8) : "memory")
#define XDMA0(P) { const int pc = PAIR(P, rot); \
      const u16* sp = xt + (size_t)(s0 - 16 + (lane >> 3)) * D_ + 8 * ((lane & 7) ^ ((lane >> 3) & 6)) + pc * 64; \
      _Pragma("unroll") for (int i = 0; i < 10; ++i) \
        __builtin_amdgcn_global_load_lds((const u32*)(sp + 8 * i * D_ + (i < 2 && !rb ? 16 * D_ : 0)), (u32*)(At + ((P) * 10 + i) * 512), 16, 0, 0); }
#define WPOLL { \
        __builtin_amdgcn_s_sleep(WSLEEP0); \
        int spins = 0; \
        _Pragma("unroll 1") \
        while (true) { \
          asm volatile("" ::: "memory"); \
          const u32x2 f = __builtin_amdgcn_raw_buffer_load_b64(frs, min(lane, 59) * 128, FL_W + hg * 7680, 16); \
          if (__builtin_amdgcn_ballot_w64(f[0] != tg0 || f[1] != tg1) == 0 || ++spins > GCAP) break; \
          __builtin_amdgcn_s_sleep(WSLEEP); \
        } \
        RETRY(0, spins); \
        lds_inc<SY_W>(sya, one); \
      }
    if (__builtin_expect(w < 2, 1)) {
      const int r6 = (lane * 205) >> 11, c10 = lane - 10 * r6;
      const int q = (lane >> 2) & 3, p4 = lane & 3;
      __builtin_amdgcn_s_setprio(3);
      const bool tw = b >= 16;
      const int j = 2 * b - 32 + w, jq = j / 3, part = j - 3 * jq, jk = jq >> 3, jh = jq & 7;
      if (__builtin_expect(tw, 1)) {
        const u16* src = wqkv + (size_t)(32 * jk + r6) * 1920 + part * 640 + jh * 80 + 8 * c10;
        u16* tl = Sc + 13952 + 2560 * w;
        if (lane < 60) {
#pragma unroll
          for (int i = 0; i < 5; ++i) __builtin_amdgcn_global_load_lds((const u32*)(src + i * 11520), (u32*)(tl + i * 480), 16, 0, 0);
          if (lane < 20) __builtin_amdgcn_global_load_lds((const u32*)(src + 5 * 11520), (u32*)(tl + 5 * 480), 16, 0, 0);
        }
      }
      asm volatile("buffer_load_dwordx2 %0, off, %1, 0 sc1" : "=v"(tag) : "s"(frs) : "memory");
      __builtin_amdgcn_s_barrier();
      const int pb = l15 + (part ? 40 * part - 40 : 80), pd = part ? 120 : 0;
      int vo[5];
#pragma unroll
      for (int ct = 0; ct < 5; ++ct) {
        const int col = pb + 16 * ct + (16 * ct + l15 >= 40 ? pd : 0);
        vo[ct] = (int)((0xeda6273b51c9840ull >> (4 * (col >> 4))) & 15) * 1024 + (g * 16 + (col & 15)) * 16;
        PIN(vo[ct]);
      }
      int co = lane * 4;
      u32 ta = LDSA(Sc + 13952 + 2560 * w) + (8 * g + q) * 160 + 8 * p4;
      PIN(ta); PIN(co);
      asm volatile("s_waitcnt vmcnt(0)" : "+v"(tag), "+v"(sink) :: "memory");
      tag[0] += 1u;
      if (__builtin_expect(tw, 1)) {
        const int kp = 2 * PAIR(jk >> 1, -(jh >> 1)) + (jk & 1);
        const __amdgpu_buffer_rsrc_t trs = __builtin_amdgcn_make_buffer_rsrc((void*)(wq + (size_t)jh * 153600 + kp * 7680), 0, 15360, 0x00020000);
        const __amdgpu_buffer_rsrc_t crs = __builtin_amdgcn_make_buffer_rsrc(ws + WS_CW + ((jh * 20 + jk) * 3 + part) * 384, 0, 384, 0x00020000);
        s16x4 fl[5], fh[5];
        asm volatile(
            "ds_read_b64_tr_b16 %0, %10\n\t"
            "ds_read_b64_tr_b16 %5, %10 offset:640\n\t"
            "ds_read_b64_tr_b16 %1, %10 offset:32\n\t"
            "ds_read_b64_tr_b16 %6, %10 offset:672\n\t"
            "ds_read_b64_tr_b16 %2, %10 offset:64\n\t"
            "ds_read_b64_tr_b16 %7, %10 offset:704\n\t"
            "ds_read_b64_tr_b16 %3, %10 offset:96\n\t"
            "ds_read_b64_tr_b16 %8, %10 offset:736\n\t"
            "ds_read_b64_tr_b16 %4, %10 offset:128\n\t"
            "ds_read_b64_tr_b16 %9, %10 offset:768\n\t"
            "s_waitcnt lgkmcnt(0)"
            : "=&v"(fl[0]), "=&v"(fl[1]), "=&v"(fl[2]), "=&v"(fl[3]), "=&v"(fl[4]),
              "=&v"(fh[0]), "=&v"(fh[1]), "=&v"(fh[2]), "=&v"(fh[3]), "=&v"(fh[4])
            : "v"(ta) : "memory");
        ST(1);
        float cs[5];
#pragma unroll
        for (int ct = 0; ct < 5; ++ct) {
          const bf16x8 fr = __builtin_shufflevector(fl[ct], fh[ct], 0, 1, 2, 3, 4, 5, 6, 7);
          asm volatile("buffer_store_dwordx4 %0, %1, %2, 0 offen sc1\n\ts_nop 1" :: "v"(fr), "v"(vo[ct]), "s"(trs) : "memory");
          cs[ct] = __builtin_amdgcn_mfma_f32_16x16x32_bf16(onesf, fr, (f32x4){0.f, 0.f, 0.f, 0.f}, 0, 0, 0)[0];
        }
        const float c03 = g < 2 ? (g ? cs[1] : cs[0]) : (g == 2 ? cs[2] : cs[3]), c4 = g ? cs[0] : cs[4];
        asm volatile("buffer_store_dword %0, %1, %2, 0 offen sc1\n\tbuffer_store_dword %3, %1, %2, 0 offen offset:256 sc1" :: "v"(c03), "v"(co), "s"(crs), "v"(c4) : "memory");
      }
      lds_inc<SY_TI>(sya, one);
      tg0 = __builtin_amdgcn_readfirstlane(tag[0]); tg1 = __builtin_amdgcn_readfirstlane(tag[1]);
      ST(3);
      if (FPOS) XDMA0(2 + w)
      if (tw) {
        if (FPOS) VMCNT(10); else VMCNT(0);
        FLAG(FL_W + (jh * 60 + jk * 3 + part) * 128);
      }
      ST(7);
      if (!FPOS) XDMA0(2 + w)
      XDMA0(5 + w)
      XDMA0(8 + w)
      if (w == 0) {
        if (WPW == 0) WPOLL
        VMCNT(0);
        lds_inc<SY_X>(sya, one);
      } else {
        const __amdgpu_buffer_rsrc_t mrs = __builtin_amdgcn_make_buffer_rsrc(obuf, 0, HO_BYTES, 0x00020000);
        _Pragma("unroll 1")
        for (int i = 0; i < 11; ++i) {
          __builtin_amdgcn_raw_buffer_store_b128(MARK16, mrs, lane * 16, i < 10 ? (rb * 8 + hg) * 10240 + i * 1024 : HO_ST + rb * 8192 + hg * 1024, 17);
          __builtin_amdgcn_s_sleep(MSL0);
        }
        VMCNT(11);
        lds_inc<SY_X>(sya, one);
        ST(6);
        VMCNT(0);
        FLAG(FL_M + b * 128);
      }
      ST(9);
    } else if (w == 2) {
      const int r6 = (lane * 205) >> 11, c10 = lane - 10 * r6, q = (lane >> 2) & 3, p4 = lane & 3;
      const bool to = b < 128;
      __builtin_amdgcn_s_setprio(3);
      asm volatile("buffer_load_dwordx2 %0, off, %1, 0 sc1" : "=v"(tag) : "s"(frs) : "memory");
      XDMA0(0)
      __builtin_amdgcn_s_barrier();
      XDMA0(1)
      if (to) {
        const u16* src = wo + (size_t)(80 * (b >> 4) + 40 * (b & 1) + r6) * D_ + ((b >> 1) & 7) * 80 + 8 * c10;
        u16* tl = (u16*)(smem + KQ_OFF);
        if (lane < 60) {
#pragma unroll
          for (int i = 0; i < 6; ++i) __builtin_amdgcn_global_load_lds((const u32*)(src + i * 6 * D_), (u32*)(tl + i * 480), 16, 0, 0);
          if (lane < 40) __builtin_amdgcn_global_load_lds((const u32*)(src + 36 * D_), (u32*)(tl + 6 * 480), 16, 0, 0);
        }
      }
      if (XE3) XDMA0(4)
      lds_poll<SY_TI, 1>(sya, 2);
      ST(1);
      if (!XE3) XDMA0(4)
      XDMA0(7)
      asm volatile("s_waitcnt vmcnt(20)" : "+v"(tag) :: "memory");
      tag[0] += 1u;
      tg0 = __builtin_amdgcn_readfirstlane(tag[0]); tg1 = __builtin_amdgcn_readfirstlane(tag[1]);
      if (to) {
        const int s = b >> 4;
        const __amdgpu_buffer_rsrc_t trs = __builtin_amdgcn_make_buffer_rsrc((void*)(wot + (size_t)((b >> 1) & 7) * 51200), 0, 102400, 0x00020000);
        const u32 t0 = LDSA(smem) + KQ_OFF + (4 * g + q) * 160 + 8 * p4, t1 = LDSA(smem) + KQ_OFF + (16 + q) * 160 + 8 * p4;
        const int o0 = lane * 16, o1 = g ? -1 : lane * 16;
#pragma unroll
        for (int nt = 0; nt < 5; ++nt) {
          s16x4 f[4];
          const int np = s < 4 ? (nt + 2) % 5 : nt, i0 = (s & 3) * 5 + np, fb = b & 1 ? 12 + 25 * (s >> 2) : 56 + 19 * (s >> 2);
          asm volatile("ds_read_b64_tr_b16 %0, %4 offset:%6\n\tds_read_b64_tr_b16 %1, %4 offset:%6+3200\n\tds_read_b64_tr_b16 %2, %5 offset:%6\n\tds_read_b64_tr_b16 %3, %5 offset:%6+3200\n\ts_waitcnt lgkmcnt(0)"
                       : "=&v"(f[0]), "=&v"(f[1]), "=&v"(f[2]), "=&v"(f[3]) : "v"(t0), "v"(t1), "i"(32 * nt) : "memory");
          asm volatile("buffer_store_dwordx4 %0, %2, %4, %5 offen sc1\n\tbuffer_store_dwordx4 %1, %3, %4, %6 offen sc1\n\ts_nop 1"
                       :: "v"(__builtin_shufflevector(f[0], f[1], 0, 1, 2, 3, 4, 5, 6, 7)), "v"(__builtin_shufflevector(f[2], f[3], 0, 1, 2, 3, 4, 5, 6, 7)),
                          "v"(o0), "v"(o1), "s"(trs), "s"((i0 + (!(b & 1) && i0 < 6 ? 6 * (s >> 2) : fb)) * 1024), "s"((fb + 20 + np) * 1024 + (s & 3) * 256) : "memory");
        }
      }
      ST(3);
      if (to) VMCNT(10); else VMCNT(0);
      lds_inc<SY_X>(sya, one);
      ST(6);
      __builtin_amdgcn_s_waitcnt(0x0070);
      if (to) FLAG(FL_M + (256 + b) * 128);
      ST(9);
    } else {
      const int ar = GREM ? (w == 11 ? 5 : w >= 8 ? w - 2 : w - 3) : w - 3, ci = ar % 3;
      KgenIn<1> kin;
      tag = __builtin_amdgcn_raw_buffer_load_b64(frs, 0, 0, 16);
      kgen_load<1, 1>(CHAN(b, ci), 4 * (ar / 3), 4, lane, ka, kin);
      asm volatile("" ::: "memory");
      if (w == 4) asm volatile("ds_write_b64 %0, %1\n\ts_waitcnt lgkmcnt(0)" :: "v"(sya + 8 * lane), "v"((u32x2){0u, 0u}) : "memory");
      __builtin_amdgcn_s_barrier();
      if (w == 8) {
        bf16x8 be, bo, b5, b3;
#pragma unroll
        for (int e = 0; e < 8; ++e) {
          const int d = 8 * g + e - l15, d4 = (g >> 1) == (l15 >> 3) ? d - (l15 & 8) : -1;
          be[e] = (d >= 4 && d <= 12) ? (short)0x3c00 : (short)0;
          bo[e] = (d >= 5 && d <= 11) ? (short)0x3c00 : (short)0;
          b5[e] = (d4 >= 2 && d4 <= 6) ? (short)0x3c00 : (short)0;
          b3[e] = (d4 >= 3 && d4 <= 5) ? (short)0x3c00 : (short)0;
        }
        asm volatile("ds_write_b128 %0, %1\n\tds_write_b128 %0, %2 offset:1024\n\tds_write_b128 %0, %3 offset:2048\n\tds_write_b128 %0, %4 offset:3072"
                     :: "v"(LDSA(smem) + BND_OFF + 16 * lane), "v"(be), "v"(bo), "v"(b5), "v"(b3) : "memory");
      }
      if (w == 3) __builtin_amdgcn_s_setprio(2); else __builtin_amdgcn_s_setprio(1);
      tg0 = __builtin_amdgcn_readfirstlane(tag[0]) + 1u; tg1 = __builtin_amdgcn_readfirstlane(tag[1]);
      f32x4 kacc[3];
      f32x4* ka_ = (f32x4*)(Sc + ar * 1536);
      const float dt = kgen_wave(lane, kin, (float*)ka_, kacc);
      if (w == 3) ST(6); else ST(4);
      if (ar < 6) {
#pragma unroll
        for (int m = 0; m < 3; ++m) ka_[64 * m + lane] = kacc[m];
        lds_inc<SY_KP>(sya, one);
      } else {
        const f32x4* pa = (const f32x4*)Sc + ci * 192 + lane;
        lds_poll<SY_KP, 2>(sya, 6);
#pragma unroll
        for (int m = 0; m < 3; ++m) { kacc[m] += pa[64 * m] + pa[576 + 64 * m]; PIN(kacc[m]); }
        lds_poll<SY_X, 2>(sya, 3);
        kgen_fin(lane, dt, bf2f(kin.ds), (float*)(smem + KQ_OFF + ci * 3072), kacc);
      }
      __builtin_amdgcn_s_waitcnt(0x0070);
    }
  }
  if (layer) lds_inc<SY_TI>(sya, one);
  if (w < 4) {
    {
    LANE(lane);
    const int l15 = lane & 15, g = lane >> 4;
    const __amdgpu_buffer_rsrc_t wrsrc = __builtin_amdgcn_make_buffer_rsrc((void*)(wq + (size_t)hg * 153600), 0, 307200, 0x00020000);
    const int tb = w == 1 ? 1 : 0;
    u32 wa = (w == 3 ? 11 : 4 * w) * 1024 + lane * 16;
    u32 xa = LDSA(At) + (layer ? (4 * g + ((lane >> 2) & 3)) * 32 + 8 * (lane & 3) : (l15 >> 3) * 1024 + (l15 & 7) * 128 + ((g ^ (l15 & 6)) << 4)), xb = xa ^ 64;
    u32 sy = sya + SY_X, pga = sya + SY_PG + 4 * w, cwa = LDSA(smem) + CW_OFF + tb * 128 + l15 * 8, mra = LDSA(smem) + A_R2 + l15 * 16;
    u32 sta = LDSA(St) + (l15 * X1_P + tb * 16 + 4 * g) * 2, stb = LDSA(St) + ((l15 + 16 * (g & 1)) * X1_P + tb * 16 + 8 * (g >> 1)) * 2, pv;
    int so, s0_, s1_;
    if (!layer && WPW && w == WPW) {
      __builtin_amdgcn_s_setprio(3);
      PIN(wa); PIN(xa); PIN(xb); PIN(sy); PIN(pga); PIN(cwa); PIN(mra); PIN(sta); PIN(stb);
      WPOLL
      ST(10);
    } else __builtin_amdgcn_s_setprio(PF);
    if (!layer) {
      asm volatile(
        "s_cmp_lt_u32 %[w], 2\ns_cbranch_scc0 .LB%=\ns_mov_b32 %[s1], 0x100000\n.LA0w%=:\nds_read_b32 %[pv], %[sy] offset:%[ow]\ns_waitcnt lgkmcnt(0)\nv_readfirstlane_b32 %[s0], %[pv]\ns_nop 0\n"
        "s_cmp_ge_i32 %[s0], 1\ns_cbranch_scc1 .LA0h%=\ns_sleep 1\ns_sub_u32 %[s1], %[s1], 1\ns_cbranch_scc0 .LA0w%=\n.LA0h%=:\ns_mov_b32 %[so], 0\n"
        "buffer_load_dwordx4 v[32:35], %[wa], %[rs], %[so] offen offset:0 sc1\nbuffer_load_dwordx4 v[36:39], %[wa], %[rs], %[so] offen offset:1024 sc1\n"
        "buffer_load_dwordx4 v[40:43], %[wa], %[rs], %[so] offen offset:2048 sc1\nbuffer_load_dwordx4 v[44:47], %[wa], %[rs], %[so] offen offset:3072 sc1\ns_mov_b32 %[so], 15360\n"
        "buffer_load_dwordx4 v[48:51], %[wa], %[rs], %[so] offen offset:0 sc1\nbuffer_load_dwordx4 v[52:55], %[wa], %[rs], %[so] offen offset:1024 sc1\n"
        "buffer_load_dwordx4 v[56:59], %[wa], %[rs], %[so] offen offset:2048 sc1\nbuffer_load_dwordx4 v[60:63], %[wa], %[rs], %[so] offen offset:3072 sc1\ns_mov_b32 %[so], 30720\n"
        "buffer_load_dwordx4 v[64:67], %[wa], %[rs], %[so] offen offset:0 sc1\nbuffer_load_dwordx4 v[68:71], %[wa], %[rs], %[so] offen offset:1024 sc1\n"
        "buffer_load_dwordx4 v[72:75], %[wa], %[rs], %[so] offen offset:2048 sc1\nbuffer_load_dwordx4 v[76:79], %[wa], %[rs], %[so] offen offset:3072 sc1\ns_mov_b32 %[so], 46080\n"
        "buffer_load_dwordx4 v[80:83], %[wa], %[rs], %[so] offen offset:0 sc1\nbuffer_load_dwordx4 v[84:87], %[wa], %[rs], %[so] offen offset:1024 sc1\n"
        "buffer_load_dwordx4 v[88:91], %[wa], %[rs], %[so] offen offset:2048 sc1\nbuffer_load_dwordx4 v[92:95], %[wa], %[rs], %[so] offen offset:3072 sc1\ns_setprio %[pf]\ns_mov_b32 %[s1], 0x100000\n"
        ".LA0v%=:\nds_read_b32 %[pv], %[sy] offset:0\ns_waitcnt lgkmcnt(0)\nv_readfirstlane_b32 %[s0], %[pv]\ns_nop 0\ns_cmp_ge_i32 %[s0], 3\ns_cbranch_scc1 .LA0g%=\ns_sleep 1\n"
        "s_sub_u32 %[s1], %[s1], 1\ns_cbranch_scc0 .LA0v%=\n.LA0g%=:\nds_read_b128 v[16:19], %[xa] offset:2048\nds_read_b128 v[20:23], %[xa] offset:4096\nds_read_b128 v[12:15], %[xa] offset:0\n"
        "ds_read_b128 v[24:27], %[xa] offset:6144\ns_waitcnt vmcnt(12) lgkmcnt(3)\nv_mfma_f32_16x16x32_bf16 v[100:103], v[32:35], v[16:19], 0\nds_read_b128 v[28:31], %[xa] offset:8192\n"
        "v_mfma_f32_16x16x32_bf16 v[116:119], v[36:39], v[16:19], 0\nv_mfma_f32_16x16x32_bf16 v[136:139], v[40:43], v[16:19], 0\nv_mfma_f32_16x16x32_bf16 v[152:155], v[44:47], v[16:19], 0\n"
        "ds_read_b128 v[16:19], %[xb] offset:2048\ns_waitcnt lgkmcnt(4)\nv_mfma_f32_16x16x32_bf16 v[104:107], v[32:35], v[20:23], 0\nv_mfma_f32_16x16x32_bf16 v[120:123], v[36:39], v[20:23], 0\n"
        "v_mfma_f32_16x16x32_bf16 v[140:143], v[40:43], v[20:23], 0\nv_mfma_f32_16x16x32_bf16 v[156:159], v[44:47], v[20:23], 0\nds_read_b128 v[20:23], %[xb] offset:4096\ns_waitcnt lgkmcnt(4)\n"
        "v_mfma_f32_16x16x32_bf16 v[96:99], v[32:35], v[12:15], 0\nv_mfma_f32_16x16x32_bf16 v[132:135], v[40:43], v[12:15], 0\nds_read_b128 v[12:15], %[xb] offset:0\ns_waitcnt lgkmcnt(4)\n"
        "v_mfma_f32_16x16x32_bf16 v[108:111], v[32:35], v[24:27], 0\nv_mfma_f32_16x16x32_bf16 v[124:127], v[36:39], v[24:27], 0\nv_mfma_f32_16x16x32_bf16 v[144:147], v[40:43], v[24:27], 0\n"
        "v_mfma_f32_16x16x32_bf16 v[160:163], v[44:47], v[24:27], 0\nds_read_b128 v[24:27], %[xb] offset:6144\ns_waitcnt lgkmcnt(4)\nv_mfma_f32_16x16x32_bf16 v[112:115], v[32:35], v[28:31], 0\n"
        "v_mfma_f32_16x16x32_bf16 v[128:131], v[36:39], v[28:31], 0\nv_mfma_f32_16x16x32_bf16 v[148:151], v[40:43], v[28:31], 0\nv_mfma_f32_16x16x32_bf16 v[164:167], v[44:47], v[28:31], 0\n.LA0l%=:\n"
        "s_waitcnt vmcnt(8) lgkmcnt(3)\nv_mfma_f32_16x16x32_bf16 v[100:103], v[48:51], v[16:19], v[100:103]\ns_addk_i32 %[so], 15360\nds_read_b128 v[28:31], %[xb] offset:8192\n"
        "v_mfma_f32_16x16x32_bf16 v[116:119], v[52:55], v[16:19], v[116:119]\nv_mfma_f32_16x16x32_bf16 v[136:139], v[56:59], v[16:19], v[136:139]\n"
        "buffer_load_dwordx4 v[32:35], %[wa], %[rs], %[so] offen offset:0 sc1\nv_mfma_f32_16x16x32_bf16 v[152:155], v[60:63], v[16:19], v[152:155]\nds_read_b128 v[16:19], %[xa] offset:12288\n"
        "s_waitcnt lgkmcnt(4)\nv_mfma_f32_16x16x32_bf16 v[104:107], v[48:51], v[20:23], v[104:107]\nv_mfma_f32_16x16x32_bf16 v[120:123], v[52:55], v[20:23], v[120:123]\n"
        "buffer_load_dwordx4 v[36:39], %[wa], %[rs], %[so] offen offset:1024 sc1\nv_mfma_f32_16x16x32_bf16 v[140:143], v[56:59], v[20:23], v[140:143]\n"
        "v_mfma_f32_16x16x32_bf16 v[156:159], v[60:63], v[20:23], v[156:159]\nds_read_b128 v[20:23], %[xa] offset:14336\ns_waitcnt lgkmcnt(4)\n"
        "v_mfma_f32_16x16x32_bf16 v[96:99], v[48:51], v[12:15], v[96:99]\nv_mfma_f32_16x16x32_bf16 v[132:135], v[56:59], v[12:15], v[132:135]\nds_read_b128 v[12:15], %[xa] offset:10240\n"
        "s_waitcnt lgkmcnt(4)\nv_mfma_f32_16x16x32_bf16 v[108:111], v[48:51], v[24:27], v[108:111]\nbuffer_load_dwordx4 v[40:43], %[wa], %[rs], %[so] offen offset:2048 sc1\n"
        "v_mfma_f32_16x16x32_bf16 v[124:127], v[52:55], v[24:27], v[124:127]\nv_mfma_f32_16x16x32_bf16 v[144:147], v[56:59], v[24:27], v[144:147]\n"
        "buffer_load_dwordx4 v[44:47], %[wa], %[rs], %[so] offen offset:3072 sc1\nv_mfma_f32_16x16x32_bf16 v[160:163], v[60:63], v[24:27], v[160:163]\nds_read_b128 v[24:27], %[xa] offset:16384\n"
        "s_waitcnt lgkmcnt(4)\nv_mfma_f32_16x16x32_bf16 v[112:115], v[48:51], v[28:31], v[112:115]\nv_mfma_f32_16x16x32_bf16 v[128:131], v[52:55], v[28:31], v[128:131]\nv_add_u32 %[sy], 4, %[sy]\n"
        "v_mfma_f32_16x16x32_bf16 v[148:151], v[56:59], v[28:31], v[148:151]\nds_write_b32 %[pg], %[sy]\nv_mfma_f32_16x16x32_bf16 v[164:167], v[60:63], v[28:31], v[164:167]\n"
        "s_waitcnt vmcnt(8) lgkmcnt(4)\nv_mfma_f32_16x16x32_bf16 v[100:103], v[64:67], v[16:19], v[100:103]\ns_addk_i32 %[so], 15360\nds_read_b128 v[28:31], %[xa] offset:18432\n"
        "v_mfma_f32_16x16x32_bf16 v[116:119], v[68:71], v[16:19], v[116:119]\nv_mfma_f32_16x16x32_bf16 v[136:139], v[72:75], v[16:19], v[136:139]\n"
        "buffer_load_dwordx4 v[48:51], %[wa], %[rs], %[so] offen offset:0 sc1\nv_mfma_f32_16x16x32_bf16 v[152:155], v[76:79], v[16:19], v[152:155]\nds_read_b128 v[16:19], %[xb] offset:12288\n"
        "s_waitcnt lgkmcnt(5)\nv_mfma_f32_16x16x32_bf16 v[104:107], v[64:67], v[20:23], v[104:107]\nv_mfma_f32_16x16x32_bf16 v[120:123], v[68:71], v[20:23], v[120:123]\n"
        "buffer_load_dwordx4 v[52:55], %[wa], %[rs], %[so] offen offset:1024 sc1\nv_mfma_f32_16x16x32_bf16 v[140:143], v[72:75], v[20:23], v[140:143]\n"
        "v_mfma_f32_16x16x32_bf16 v[156:159], v[76:79], v[20:23], v[156:159]\nds_read_b128 v[20:23], %[xb] offset:14336\ns_waitcnt lgkmcnt(5)\n"
        "v_mfma_f32_16x16x32_bf16 v[96:99], v[64:67], v[12:15], v[96:99]\nv_mfma_f32_16x16x32_bf16 v[132:135], v[72:75], v[12:15], v[132:135]\nds_read_b128 v[12:15], %[xb] offset:10240\n"
        "s_waitcnt lgkmcnt(5)\nv_mfma_f32_16x16x32_bf16 v[108:111], v[64:67], v[24:27], v[108:111]\nbuffer_load_dwordx4 v[56:59], %[wa], %[rs], %[so] offen offset:2048 sc1\n"
        "v_mfma_f32_16x16x32_bf16 v[124:127], v[68:71], v[24:27], v[124:127]\nv_mfma_f32_16x16x32_bf16 v[144:147], v[72:75], v[24:27], v[144:147]\n"
        "buffer_load_dwordx4 v[60:63], %[wa], %[rs], %[so] offen offset:3072 sc1\nv_mfma_f32_16x16x32_bf16 v[160:163], v[76:79], v[24:27], v[160:163]\nds_read_b128 v[24:27], %[xb] offset:16384\n"
        "s_waitcnt lgkmcnt(4)\nv_mfma_f32_16x16x32_bf16 v[112:115], v[64:67], v[28:31], v[112:115]\nv_mfma_f32_16x16x32_bf16 v[128:131], v[68:71], v[28:31], v[128:131]\n"
        "v_mfma_f32_16x16x32_bf16 v[148:151], v[72:75], v[28:31], v[148:151]\nv_mfma_f32_16x16x32_bf16 v[164:167], v[76:79], v[28:31], v[164:167]\ns_waitcnt vmcnt(8) lgkmcnt(3)\n"
        "v_mfma_f32_16x16x32_bf16 v[100:103], v[80:83], v[16:19], v[100:103]\ns_addk_i32 %[so], 15360\nds_read_b128 v[28:31], %[xb] offset:18432\n"
        "v_mfma_f32_16x16x32_bf16 v[116:119], v[84:87], v[16:19], v[116:119]\nv_mfma_f32_16x16x32_bf16 v[136:139], v[88:91], v[16:19], v[136:139]\n"
        "buffer_load_dwordx4 v[64:67], %[wa], %[rs], %[so] offen offset:0 sc1\nv_mfma_f32_16x16x32_bf16 v[152:155], v[92:95], v[16:19], v[152:155]\nds_read_b128 v[16:19], %[xa] offset:22528\n"
        "s_waitcnt lgkmcnt(4)\nv_mfma_f32_16x16x32_bf16 v[104:107], v[80:83], v[20:23], v[104:107]\nv_mfma_f32_16x16x32_bf16 v[120:123], v[84:87], v[20:23], v[120:123]\n"
        "buffer_load_dwordx4 v[68:71], %[wa], %[rs], %[so] offen offset:1024 sc1\nv_mfma_f32_16x16x32_bf16 v[140:143], v[88:91], v[20:23], v[140:143]\n"
        "v_mfma_f32_16x16x32_bf16 v[156:159], v[92:95], v[20:23], v[156:159]\nds_read_b128 v[20:23], %[xa] offset:24576\ns_waitcnt lgkmcnt(4)\n"
        "v_mfma_f32_16x16x32_bf16 v[96:99], v[80:83], v[12:15], v[96:99]\nv_mfma_f32_16x16x32_bf16 v[132:135], v[88:91], v[12:15], v[132:135]\nds_read_b128 v[12:15], %[xa] offset:20480\n"
        "s_waitcnt lgkmcnt(4)\nv_mfma_f32_16x16x32_bf16 v[108:111], v[80:83], v[24:27], v[108:111]\nbuffer_load_dwordx4 v[72:75], %[wa], %[rs], %[so] offen offset:2048 sc1\n"
        "v_mfma_f32_16x16x32_bf16 v[124:127], v[84:87], v[24:27], v[124:127]\nv_mfma_f32_16x16x32_bf16 v[144:147], v[88:91], v[24:27], v[144:147]\n"
        "buffer_load_dwordx4 v[76:79], %[wa], %[rs], %[so] offen offset:3072 sc1\nv_mfma_f32_16x16x32_bf16 v[160:163], v[92:95], v[24:27], v[160:163]\nds_read_b128 v[24:27], %[xa] offset:26624\n"
        "s_waitcnt lgkmcnt(4)\nv_mfma_f32_16x16x32_bf16 v[112:115], v[80:83], v[28:31], v[112:115]\nv_mfma_f32_16x16x32_bf16 v[128:131], v[84:87], v[28:31], v[128:131]\nv_add_u32 %[sy], 4, %[sy]\n"
        "v_mfma_f32_16x16x32_bf16 v[148:151], v[88:91], v[28:31], v[148:151]\nds_write_b32 %[pg], %[sy]\nv_mfma_f32_16x16x32_bf16 v[164:167], v[92:95], v[28:31], v[164:167]\n"
        "s_waitcnt vmcnt(8) lgkmcnt(4)\nv_mfma_f32_16x16x32_bf16 v[100:103], v[32:35], v[16:19], v[100:103]\ns_addk_i32 %[so], 15360\nds_read_b128 v[28:31], %[xa] offset:28672\n"
        "v_mfma_f32_16x16x32_bf16 v[116:119], v[36:39], v[16:19], v[116:119]\nv_mfma_f32_16x16x32_bf16 v[136:139], v[40:43], v[16:19], v[136:139]\n"
        "buffer_load_dwordx4 v[80:83], %[wa], %[rs], %[so] offen offset:0 sc1\nv_mfma_f32_16x16x32_bf16 v[152:155], v[44:47], v[16:19], v[152:155]\nds_read_b128 v[16:19], %[xb] offset:22528\n"
        "s_waitcnt lgkmcnt(5)\nv_mfma_f32_16x16x32_bf16 v[104:107], v[32:35], v[20:23], v[104:107]\nv_mfma_f32_16x16x32_bf16 v[120:123], v[36:39], v[20:23], v[120:123]\n"
        "buffer_load_dwordx4 v[84:87], %[wa], %[rs], %[so] offen offset:1024 sc1\nv_mfma_f32_16x16x32_bf16 v[140:143], v[40:43], v[20:23], v[140:143]\n"
        "v_mfma_f32_16x16x32_bf16 v[156:159], v[44:47], v[20:23], v[156:159]\nds_read_b128 v[20:23], %[xb] offset:24576\ns_waitcnt lgkmcnt(5)\n"
        "v_mfma_f32_16x16x32_bf16 v[96:99], v[32:35], v[12:15], v[96:99]\nv_mfma_f32_16x16x32_bf16 v[132:135], v[40:43], v[12:15], v[132:135]\nds_read_b128 v[12:15], %[xb] offset:20480\n"
        "s_waitcnt lgkmcnt(5)\nv_mfma_f32_16x16x32_bf16 v[108:111], v[32:35], v[24:27], v[108:111]\nbuffer_load_dwordx4 v[88:91], %[wa], %[rs], %[so] offen offset:2048 sc1\n"
        "v_mfma_f32_16x16x32_bf16 v[124:127], v[36:39], v[24:27], v[124:127]\nv_mfma_f32_16x16x32_bf16 v[144:147], v[40:43], v[24:27], v[144:147]\n"
        "buffer_load_dwordx4 v[92:95], %[wa], %[rs], %[so] offen offset:3072 sc1\nv_mfma_f32_16x16x32_bf16 v[160:163], v[44:47], v[24:27], v[160:163]\nds_read_b128 v[24:27], %[xb] offset:26624\n"
        "s_waitcnt lgkmcnt(4)\nv_mfma_f32_16x16x32_bf16 v[112:115], v[32:35], v[28:31], v[112:115]\nv_mfma_f32_16x16x32_bf16 v[128:131], v[36:39], v[28:31], v[128:131]\n"
        "v_mfma_f32_16x16x32_bf16 v[148:151], v[40:43], v[28:31], v[148:151]\ns_cmp_lg_u32 %[so], 291840\nv_mfma_f32_16x16x32_bf16 v[164:167], v[44:47], v[28:31], v[164:167]\n"
        "v_add_u32 %[xa], 20480, %[xa]\nv_add_u32 %[xb], 20480, %[xb]\ns_cbranch_scc1 .LA0l%=\ns_waitcnt vmcnt(8) lgkmcnt(3)\nv_mfma_f32_16x16x32_bf16 v[100:103], v[48:51], v[16:19], v[100:103]\n"
        "ds_read_b128 v[28:31], %[xb] offset:8192\nv_mfma_f32_16x16x32_bf16 v[116:119], v[52:55], v[16:19], v[116:119]\nv_mfma_f32_16x16x32_bf16 v[136:139], v[56:59], v[16:19], v[136:139]\n"
        "v_mfma_f32_16x16x32_bf16 v[152:155], v[60:63], v[16:19], v[152:155]\nds_read_b128 v[16:19], %[xa] offset:12288\ns_waitcnt lgkmcnt(4)\n"
        "v_mfma_f32_16x16x32_bf16 v[104:107], v[48:51], v[20:23], v[104:107]\nv_mfma_f32_16x16x32_bf16 v[120:123], v[52:55], v[20:23], v[120:123]\n"
        "v_mfma_f32_16x16x32_bf16 v[140:143], v[56:59], v[20:23], v[140:143]\nv_mfma_f32_16x16x32_bf16 v[156:159], v[60:63], v[20:23], v[156:159]\nds_read_b128 v[20:23], %[xa] offset:14336\n"
        "s_waitcnt lgkmcnt(4)\nv_mfma_f32_16x16x32_bf16 v[96:99], v[48:51], v[12:15], v[96:99]\nv_mfma_f32_16x16x32_bf16 v[132:135], v[56:59], v[12:15], v[132:135]\n"
        "ds_read_b128 v[12:15], %[xa] offset:10240\ns_waitcnt lgkmcnt(4)\nv_mfma_f32_16x16x32_bf16 v[108:111], v[48:51], v[24:27], v[108:111]\n"
        "v_mfma_f32_16x16x32_bf16 v[124:127], v[52:55], v[24:27], v[124:127]\nv_mfma_f32_16x16x32_bf16 v[144:147], v[56:59], v[24:27], v[144:147]\n"
        "v_mfma_f32_16x16x32_bf16 v[160:163], v[60:63], v[24:27], v[160:163]\nds_read_b128 v[24:27], %[xa] offset:16384\ns_waitcnt lgkmcnt(4)\n"
        "v_mfma_f32_16x16x32_bf16 v[112:115], v[48:51], v[28:31], v[112:115]\nv_mfma_f32_16x16x32_bf16 v[128:131], v[52:55], v[28:31], v[128:131]\nv_add_u32 %[sy], 4, %[sy]\n"
        "v_mfma_f32_16x16x32_bf16 v[148:151], v[56:59], v[28:31], v[148:151]\nv_mfma_f32_16x16x32_bf16 v[164:167], v[60:63], v[28:31], v[164:167]\ns_waitcnt vmcnt(4) lgkmcnt(3)\n"
        "v_mfma_f32_16x16x32_bf16 v[100:103], v[64:67], v[16:19], v[100:103]\nds_read_b128 v[28:31], %[xa] offset:18432\nv_mfma_f32_16x16x32_bf16 v[116:119], v[68:71], v[16:19], v[116:119]\n"
        "v_mfma_f32_16x16x32_bf16 v[136:139], v[72:75], v[16:19], v[136:139]\nds_read_b32 %[pv], %[sy] offset:4\nv_mfma_f32_16x16x32_bf16 v[152:155], v[76:79], v[16:19], v[152:155]\n"
        "ds_read_b128 v[16:19], %[xb] offset:12288\ns_waitcnt lgkmcnt(5)\nv_mfma_f32_16x16x32_bf16 v[104:107], v[64:67], v[20:23], v[104:107]\n"
        "v_mfma_f32_16x16x32_bf16 v[120:123], v[68:71], v[20:23], v[120:123]\nv_mfma_f32_16x16x32_bf16 v[140:143], v[72:75], v[20:23], v[140:143]\n"
        "v_mfma_f32_16x16x32_bf16 v[156:159], v[76:79], v[20:23], v[156:159]\nds_read_b128 v[20:23], %[xb] offset:14336\ns_waitcnt lgkmcnt(5)\n"
        "v_mfma_f32_16x16x32_bf16 v[96:99], v[64:67], v[12:15], v[96:99]\nv_mfma_f32_16x16x32_bf16 v[132:135], v[72:75], v[12:15], v[132:135]\nds_read_b128 v[12:15], %[xb] offset:10240\n"
        "s_waitcnt lgkmcnt(5)\nv_mfma_f32_16x16x32_bf16 v[108:111], v[64:67], v[24:27], v[108:111]\nv_mfma_f32_16x16x32_bf16 v[124:127], v[68:71], v[24:27], v[124:127]\n"
        "v_mfma_f32_16x16x32_bf16 v[144:147], v[72:75], v[24:27], v[144:147]\nv_mfma_f32_16x16x32_bf16 v[160:163], v[76:79], v[24:27], v[160:163]\nds_read_b128 v[24:27], %[xb] offset:16384\n"
        "s_waitcnt lgkmcnt(5)\nv_mfma_f32_16x16x32_bf16 v[112:115], v[64:67], v[28:31], v[112:115]\nv_mfma_f32_16x16x32_bf16 v[128:131], v[68:71], v[28:31], v[128:131]\n"
        "v_mfma_f32_16x16x32_bf16 v[148:151], v[72:75], v[28:31], v[148:151]\nv_mfma_f32_16x16x32_bf16 v[164:167], v[76:79], v[28:31], v[164:167]\ns_waitcnt vmcnt(0) lgkmcnt(3)\n"
        "v_mfma_f32_16x16x32_bf16 v[100:103], v[80:83], v[16:19], v[100:103]\nv_readfirstlane_b32 %[s0], %[pv]\nds_read_b128 v[28:31], %[xb] offset:18432\n"
        "v_mfma_f32_16x16x32_bf16 v[116:119], v[84:87], v[16:19], v[116:119]\nv_mfma_f32_16x16x32_bf16 v[136:139], v[88:91], v[16:19], v[136:139]\ns_cmp_ge_i32 %[s0], %[nst]\n"
        "v_mfma_f32_16x16x32_bf16 v[152:155], v[92:95], v[16:19], v[152:155]\ns_cbranch_scc0 .LA0t%=\n.LA0u%=:\ns_waitcnt lgkmcnt(3)\n"
        "v_mfma_f32_16x16x32_bf16 v[104:107], v[80:83], v[20:23], v[104:107]\nv_add_u32 %[sy], 4, %[sy]\nv_mfma_f32_16x16x32_bf16 v[120:123], v[84:87], v[20:23], v[120:123]\n"
        "ds_read_b64 v[32:33], %[cwa] offset:0\nv_mfma_f32_16x16x32_bf16 v[140:143], v[88:91], v[20:23], v[140:143]\nds_read_b64 v[36:37], %[cwa] offset:640\n"
        "v_mfma_f32_16x16x32_bf16 v[156:159], v[92:95], v[20:23], v[156:159]\nds_read_b64 v[40:41], %[cwa] offset:1280\ns_waitcnt lgkmcnt(5)\n"
        "v_mfma_f32_16x16x32_bf16 v[96:99], v[80:83], v[12:15], v[96:99]\nds_read_b64 v[44:45], %[cwa] offset:1024\nv_mfma_f32_16x16x32_bf16 v[132:135], v[88:91], v[12:15], v[132:135]\n"
        "ds_read_b128 v[48:51], %[mra] offset:0\ns_waitcnt lgkmcnt(6)\nv_mfma_f32_16x16x32_bf16 v[108:111], v[80:83], v[24:27], v[108:111]\nds_read_b128 v[52:55], %[mra] offset:256\n"
        "v_mfma_f32_16x16x32_bf16 v[124:127], v[84:87], v[24:27], v[124:127]\nds_read_b128 v[56:59], %[mra] offset:512\nv_mfma_f32_16x16x32_bf16 v[144:147], v[88:91], v[24:27], v[144:147]\n"
        "ds_read_b128 v[60:63], %[mra] offset:768\nv_mfma_f32_16x16x32_bf16 v[160:163], v[92:95], v[24:27], v[160:163]\nds_read_b128 v[64:67], %[mra] offset:1024\ns_waitcnt lgkmcnt(9)\n"
        "v_mfma_f32_16x16x32_bf16 v[112:115], v[80:83], v[28:31], v[112:115]\nv_add_u32 %[sy], 4, %[sy]\nv_mfma_f32_16x16x32_bf16 v[128:131], v[84:87], v[28:31], v[128:131]\ns_setprio %[pfs]\n"
        "v_mfma_f32_16x16x32_bf16 v[148:151], v[88:91], v[28:31], v[148:151]\nv_mfma_f32_16x16x32_bf16 v[164:167], v[92:95], v[28:31], v[164:167]\ns_waitcnt lgkmcnt(0)\n"
        "v_mfma_f32_16x16x32_bf16 v[100:103], v[32:35], v[52:55], v[100:103]\nv_mfma_f32_16x16x32_bf16 v[104:107], v[32:35], v[56:59], v[104:107]\n"
        "v_mfma_f32_16x16x32_bf16 v[116:119], v[36:39], v[52:55], v[116:119]\nv_mfma_f32_16x16x32_bf16 v[120:123], v[36:39], v[56:59], v[120:123]\n"
        "v_mfma_f32_16x16x32_bf16 v[96:99], v[32:35], v[48:51], v[96:99]\nv_cvt_pk_bf16_f32 v100, v100, v101\nv_cvt_pk_bf16_f32 v101, v102, v103\nv_cvt_pk_bf16_f32 v102, v104, v105\n"
        "v_cvt_pk_bf16_f32 v103, v106, v107\nv_mfma_f32_16x16x32_bf16 v[108:111], v[32:35], v[60:63], v[108:111]\nv_permlane16_swap_b32 v100, v102\nv_permlane16_swap_b32 v101, v103\n"
        "ds_write_b128 %[stb], v[100:103] offset:7936\nv_mfma_f32_16x16x32_bf16 v[112:115], v[32:35], v[64:67], v[112:115]\nv_cvt_pk_bf16_f32 v116, v116, v117\nv_cvt_pk_bf16_f32 v117, v118, v119\n"
        "v_cvt_pk_bf16_f32 v118, v120, v121\nv_cvt_pk_bf16_f32 v119, v122, v123\nv_mfma_f32_16x16x32_bf16 v[124:127], v[36:39], v[60:63], v[124:127]\nv_permlane16_swap_b32 v116, v118\n"
        "v_permlane16_swap_b32 v117, v119\nds_write_b128 %[stb], v[116:119] offset:8096\nv_cvt_pk_bf16_f32 v96, v96, v97\nv_mfma_f32_16x16x32_bf16 v[128:131], v[36:39], v[64:67], v[128:131]\n"
        "v_cvt_pk_bf16_f32 v97, v98, v99\nds_write_b64 %[sta], v[96:97] offset:0\nv_mfma_f32_16x16x32_bf16 v[136:139], v[40:43], v[52:55], v[136:139]\nv_cvt_pk_bf16_f32 v108, v108, v109\n"
        "v_cvt_pk_bf16_f32 v109, v110, v111\nv_cvt_pk_bf16_f32 v110, v112, v113\nv_cvt_pk_bf16_f32 v111, v114, v115\nv_mfma_f32_16x16x32_bf16 v[140:143], v[40:43], v[56:59], v[140:143]\n"
        "v_permlane16_swap_b32 v108, v110\nv_permlane16_swap_b32 v109, v111\nds_write_b128 %[stb], v[108:111] offset:23808\nv_mfma_f32_16x16x32_bf16 v[152:155], v[44:47], v[52:55], v[152:155]\n"
        "v_cvt_pk_bf16_f32 v124, v124, v125\nv_cvt_pk_bf16_f32 v125, v126, v127\nv_cvt_pk_bf16_f32 v126, v128, v129\nv_cvt_pk_bf16_f32 v127, v130, v131\n"
        "v_mfma_f32_16x16x32_bf16 v[156:159], v[44:47], v[56:59], v[156:159]\nv_permlane16_swap_b32 v124, v126\nv_permlane16_swap_b32 v125, v127\nds_write_b128 %[stb], v[124:127] offset:23968\n"
        "ds_write_b32 %[pg], %[sy]\nv_mfma_f32_16x16x32_bf16 v[132:135], v[40:43], v[48:51], v[132:135]\nv_add_u32 %[sy], 4, %[sy]\nv_cvt_pk_bf16_f32 v136, v136, v137\n"
        "v_cvt_pk_bf16_f32 v137, v138, v139\nv_cvt_pk_bf16_f32 v138, v140, v141\nv_mfma_f32_16x16x32_bf16 v[144:147], v[40:43], v[60:63], v[144:147]\nv_cvt_pk_bf16_f32 v139, v142, v143\ns_nop 0\n"
        "v_permlane16_swap_b32 v136, v138\nv_permlane16_swap_b32 v137, v139\nv_mfma_f32_16x16x32_bf16 v[148:151], v[40:43], v[64:67], v[148:151]\nds_write_b128 %[stb], v[136:139] offset:8256\n"
        "v_cvt_pk_bf16_f32 v152, v152, v153\nv_cvt_pk_bf16_f32 v153, v154, v155\nv_cvt_pk_bf16_f32 v154, v156, v157\nv_mfma_f32_16x16x32_bf16 v[160:163], v[44:47], v[60:63], v[160:163]\n"
        "v_cvt_pk_bf16_f32 v155, v158, v159\ns_nop 0\nv_permlane16_swap_b32 v152, v154\nv_permlane16_swap_b32 v153, v155\nv_mfma_f32_16x16x32_bf16 v[164:167], v[44:47], v[64:67], v[164:167]\n"
        "ds_write_b128 %[stb], v[152:155] offset:8192\nv_cvt_pk_bf16_f32 v132, v132, v133\nv_cvt_pk_bf16_f32 v133, v134, v135\nds_write_b64 %[sta], v[132:133] offset:320\ns_nop 3\n"
        "v_cvt_pk_bf16_f32 v144, v144, v145\nv_cvt_pk_bf16_f32 v145, v146, v147\nv_cvt_pk_bf16_f32 v146, v148, v149\nv_cvt_pk_bf16_f32 v147, v150, v151\ns_nop 0\nv_permlane16_swap_b32 v144, v146\n"
        "v_permlane16_swap_b32 v145, v147\nds_write_b128 %[stb], v[144:147] offset:24128\nv_cvt_pk_bf16_f32 v160, v160, v161\nv_cvt_pk_bf16_f32 v161, v162, v163\nv_cvt_pk_bf16_f32 v162, v164, v165\n"
        "v_cvt_pk_bf16_f32 v163, v166, v167\ns_nop 0\nv_permlane16_swap_b32 v160, v162\nv_permlane16_swap_b32 v161, v163\nds_write_b128 %[stb], v[160:163] offset:24064\nds_write_b32 %[pg], %[sy]\n"
        "s_branch .LE%=\n.LA0t%=:\ns_mov_b32 %[s1], 0x100000\n.LA0q%=:\nds_read_b32 %[pv], %[sy] offset:4\ns_waitcnt lgkmcnt(0)\nv_readfirstlane_b32 %[s0], %[pv]\ns_nop 0\ns_cmp_ge_i32 %[s0], %[nst]\n"
        "s_cbranch_scc1 .LA0u%=\ns_sleep 1\ns_sub_u32 %[s1], %[s1], 1\ns_cbranch_scc0 .LA0q%=\ns_branch .LA0u%=\n.LB%=:\ns_cmp_eq_u32 %[w], 2\ns_cbranch_scc0 .LC%=\ns_mov_b32 %[s1], 0x100000\n"
        ".LB0w%=:\nds_read_b32 %[pv], %[sy] offset:%[ow]\ns_waitcnt lgkmcnt(0)\nv_readfirstlane_b32 %[s0], %[pv]\ns_nop 0\ns_cmp_ge_i32 %[s0], 1\ns_cbranch_scc1 .LB0h%=\ns_sleep 1\n"
        "s_sub_u32 %[s1], %[s1], 1\ns_cbranch_scc0 .LB0w%=\n.LB0h%=:\ns_mov_b32 %[so], 0\nbuffer_load_dwordx4 v[32:35], %[wa], %[rs], %[so] offen offset:3072 sc1\n"
        "buffer_load_dwordx4 v[36:39], %[wa], %[rs], %[so] offen offset:0 sc1\nbuffer_load_dwordx4 v[40:43], %[wa], %[rs], %[so] offen offset:1024 sc1\n"
        "buffer_load_dwordx4 v[44:47], %[wa], %[rs], %[so] offen offset:2048 sc1\ns_mov_b32 %[so], 15360\nbuffer_load_dwordx4 v[48:51], %[wa], %[rs], %[so] offen offset:3072 sc1\n"
        "buffer_load_dwordx4 v[52:55], %[wa], %[rs], %[so] offen offset:0 sc1\nbuffer_load_dwordx4 v[56:59], %[wa], %[rs], %[so] offen offset:1024 sc1\n"
        "buffer_load_dwordx4 v[60:63], %[wa], %[rs], %[so] offen offset:2048 sc1\ns_mov_b32 %[so], 30720\nbuffer_load_dwordx4 v[64:67], %[wa], %[rs], %[so] offen offset:3072 sc1\n"
        "buffer_load_dwordx4 v[68:71], %[wa], %[rs], %[so] offen offset:0 sc1\nbuffer_load_dwordx4 v[72:75], %[wa], %[rs], %[so] offen offset:1024 sc1\n"
        "buffer_load_dwordx4 v[76:79], %[wa], %[rs], %[so] offen offset:2048 sc1\ns_mov_b32 %[so], 46080\nbuffer_load_dwordx4 v[80:83], %[wa], %[rs], %[so] offen offset:3072 sc1\n"
        "buffer_load_dwordx4 v[84:87], %[wa], %[rs], %[so] offen offset:0 sc1\nbuffer_load_dwordx4 v[88:91], %[wa], %[rs], %[so] offen offset:1024 sc1\n"
        "buffer_load_dwordx4 v[92:95], %[wa], %[rs], %[so] offen offset:2048 sc1\ns_setprio %[pf]\ns_mov_b32 %[s1], 0x100000\n.LB0v%=:\nds_read_b32 %[pv], %[sy] offset:0\ns_waitcnt lgkmcnt(0)\n"
        "v_readfirstlane_b32 %[s0], %[pv]\ns_nop 0\ns_cmp_ge_i32 %[s0], 3\ns_cbranch_scc1 .LB0g%=\ns_sleep 1\ns_sub_u32 %[s1], %[s1], 1\ns_cbranch_scc0 .LB0v%=\n.LB0g%=:\n"
        "ds_read_b128 v[16:19], %[xa] offset:2048\nds_read_b128 v[20:23], %[xa] offset:4096\nds_read_b128 v[12:15], %[xa] offset:0\nds_read_b128 v[24:27], %[xa] offset:6144\n"
        "s_waitcnt vmcnt(12) lgkmcnt(3)\nv_mfma_f32_16x16x32_bf16 v[96:99], v[32:35], v[16:19], 0\nds_read_b128 v[28:31], %[xa] offset:8192\nv_mfma_f32_16x16x32_bf16 v[108:111], v[36:39], v[16:19], 0\n"
        "v_mfma_f32_16x16x32_bf16 v[128:131], v[40:43], v[16:19], 0\nv_mfma_f32_16x16x32_bf16 v[148:151], v[44:47], v[16:19], 0\nds_read_b128 v[16:19], %[xb] offset:2048\ns_waitcnt lgkmcnt(4)\n"
        "v_mfma_f32_16x16x32_bf16 v[100:103], v[32:35], v[20:23], 0\nv_mfma_f32_16x16x32_bf16 v[112:115], v[36:39], v[20:23], 0\nv_mfma_f32_16x16x32_bf16 v[132:135], v[40:43], v[20:23], 0\n"
        "v_mfma_f32_16x16x32_bf16 v[152:155], v[44:47], v[20:23], 0\nds_read_b128 v[20:23], %[xb] offset:4096\ns_waitcnt lgkmcnt(4)\nv_mfma_f32_16x16x32_bf16 v[104:107], v[36:39], v[12:15], 0\n"
        "v_mfma_f32_16x16x32_bf16 v[124:127], v[40:43], v[12:15], 0\nv_mfma_f32_16x16x32_bf16 v[144:147], v[44:47], v[12:15], 0\nds_read_b128 v[12:15], %[xb] offset:0\ns_waitcnt lgkmcnt(4)\n"
        "v_mfma_f32_16x16x32_bf16 v[116:119], v[36:39], v[24:27], 0\nv_mfma_f32_16x16x32_bf16 v[136:139], v[40:43], v[24:27], 0\nv_mfma_f32_16x16x32_bf16 v[156:159], v[44:47], v[24:27], 0\n"
        "ds_read_b128 v[24:27], %[xb] offset:6144\ns_waitcnt lgkmcnt(4)\nv_mfma_f32_16x16x32_bf16 v[120:123], v[36:39], v[28:31], 0\nv_mfma_f32_16x16x32_bf16 v[140:143], v[40:43], v[28:31], 0\n"
        "v_mfma_f32_16x16x32_bf16 v[160:163], v[44:47], v[28:31], 0\n.LB0l%=:\ns_waitcnt vmcnt(8) lgkmcnt(3)\nv_mfma_f32_16x16x32_bf16 v[96:99], v[48:51], v[16:19], v[96:99]\ns_addk_i32 %[so], 15360\n"
        "ds_read_b128 v[28:31], %[xb] offset:8192\nv_mfma_f32_16x16x32_bf16 v[108:111], v[52:55], v[16:19], v[108:111]\nv_mfma_f32_16x16x32_bf16 v[128:131], v[56:59], v[16:19], v[128:131]\n"
        "buffer_load_dwordx4 v[32:35], %[wa], %[rs], %[so] offen offset:3072 sc1\nv_mfma_f32_16x16x32_bf16 v[148:151], v[60:63], v[16:19], v[148:151]\nds_read_b128 v[16:19], %[xa] offset:12288\n"
        "s_waitcnt lgkmcnt(4)\nv_mfma_f32_16x16x32_bf16 v[100:103], v[48:51], v[20:23], v[100:103]\nv_mfma_f32_16x16x32_bf16 v[112:115], v[52:55], v[20:23], v[112:115]\n"
        "buffer_load_dwordx4 v[36:39], %[wa], %[rs], %[so] offen offset:0 sc1\nv_mfma_f32_16x16x32_bf16 v[132:135], v[56:59], v[20:23], v[132:135]\n"
        "v_mfma_f32_16x16x32_bf16 v[152:155], v[60:63], v[20:23], v[152:155]\nds_read_b128 v[20:23], %[xa] offset:14336\ns_waitcnt lgkmcnt(4)\n"
        "v_mfma_f32_16x16x32_bf16 v[104:107], v[52:55], v[12:15], v[104:107]\nv_mfma_f32_16x16x32_bf16 v[124:127], v[56:59], v[12:15], v[124:127]\n"
        "buffer_load_dwordx4 v[40:43], %[wa], %[rs], %[so] offen offset:1024 sc1\nv_mfma_f32_16x16x32_bf16 v[144:147], v[60:63], v[12:15], v[144:147]\nds_read_b128 v[12:15], %[xa] offset:10240\n"
        "s_waitcnt lgkmcnt(4)\nv_mfma_f32_16x16x32_bf16 v[116:119], v[52:55], v[24:27], v[116:119]\nv_mfma_f32_16x16x32_bf16 v[136:139], v[56:59], v[24:27], v[136:139]\n"
        "buffer_load_dwordx4 v[44:47], %[wa], %[rs], %[so] offen offset:2048 sc1\nv_mfma_f32_16x16x32_bf16 v[156:159], v[60:63], v[24:27], v[156:159]\nds_read_b128 v[24:27], %[xa] offset:16384\n"
        "s_waitcnt lgkmcnt(4)\nv_mfma_f32_16x16x32_bf16 v[120:123], v[52:55], v[28:31], v[120:123]\nv_add_u32 %[sy], 4, %[sy]\nv_mfma_f32_16x16x32_bf16 v[140:143], v[56:59], v[28:31], v[140:143]\n"
        "ds_write_b32 %[pg], %[sy]\nv_mfma_f32_16x16x32_bf16 v[160:163], v[60:63], v[28:31], v[160:163]\ns_waitcnt vmcnt(8) lgkmcnt(4)\nv_mfma_f32_16x16x32_bf16 v[96:99], v[64:67], v[16:19], v[96:99]\n"
        "s_addk_i32 %[so], 15360\nds_read_b128 v[28:31], %[xa] offset:18432\nv_mfma_f32_16x16x32_bf16 v[108:111], v[68:71], v[16:19], v[108:111]\n"
        "v_mfma_f32_16x16x32_bf16 v[128:131], v[72:75], v[16:19], v[128:131]\nbuffer_load_dwordx4 v[48:51], %[wa], %[rs], %[so] offen offset:3072 sc1\n"
        "v_mfma_f32_16x16x32_bf16 v[148:151], v[76:79], v[16:19], v[148:151]\nds_read_b128 v[16:19], %[xb] offset:12288\ns_waitcnt lgkmcnt(5)\n"
        "v_mfma_f32_16x16x32_bf16 v[100:103], v[64:67], v[20:23], v[100:103]\nv_mfma_f32_16x16x32_bf16 v[112:115], v[68:71], v[20:23], v[112:115]\n"
        "buffer_load_dwordx4 v[52:55], %[wa], %[rs], %[so] offen offset:0 sc1\nv_mfma_f32_16x16x32_bf16 v[132:135], v[72:75], v[20:23], v[132:135]\n"
        "v_mfma_f32_16x16x32_bf16 v[152:155], v[76:79], v[20:23], v[152:155]\nds_read_b128 v[20:23], %[xb] offset:14336\ns_waitcnt lgkmcnt(5)\n"
        "v_mfma_f32_16x16x32_bf16 v[104:107], v[68:71], v[12:15], v[104:107]\nv_mfma_f32_16x16x32_bf16 v[124:127], v[72:75], v[12:15], v[124:127]\n"
        "buffer_load_dwordx4 v[56:59], %[wa], %[rs], %[so] offen offset:1024 sc1\nv_mfma_f32_16x16x32_bf16 v[144:147], v[76:79], v[12:15], v[144:147]\nds_read_b128 v[12:15], %[xb] offset:10240\n"
        "s_waitcnt lgkmcnt(5)\nv_mfma_f32_16x16x32_bf16 v[116:119], v[68:71], v[24:27], v[116:119]\nv_mfma_f32_16x16x32_bf16 v[136:139], v[72:75], v[24:27], v[136:139]\n"
        "buffer_load_dwordx4 v[60:63], %[wa], %[rs], %[so] offen offset:2048 sc1\nv_mfma_f32_16x16x32_bf16 v[156:159], v[76:79], v[24:27], v[156:159]\nds_read_b128 v[24:27], %[xb] offset:16384\n"
        "s_waitcnt lgkmcnt(4)\nv_mfma_f32_16x16x32_bf16 v[120:123], v[68:71], v[28:31], v[120:123]\nv_mfma_f32_16x16x32_bf16 v[140:143], v[72:75], v[28:31], v[140:143]\n"
        "v_mfma_f32_16x16x32_bf16 v[160:163], v[76:79], v[28:31], v[160:163]\ns_waitcnt vmcnt(8) lgkmcnt(3)\nv_mfma_f32_16x16x32_bf16 v[96:99], v[80:83], v[16:19], v[96:99]\ns_addk_i32 %[so], 15360\n"
        "ds_read_b128 v[28:31], %[xb] offset:18432\nv_mfma_f32_16x16x32_bf16 v[108:111], v[84:87], v[16:19], v[108:111]\nv_mfma_f32_16x16x32_bf16 v[128:131], v[88:91], v[16:19], v[128:131]\n"
        "buffer_load_dwordx4 v[64:67], %[wa], %[rs], %[so] offen offset:3072 sc1\nv_mfma_f32_16x16x32_bf16 v[148:151], v[92:95], v[16:19], v[148:151]\nds_read_b128 v[16:19], %[xa] offset:22528\n"
        "s_waitcnt lgkmcnt(4)\nv_mfma_f32_16x16x32_bf16 v[100:103], v[80:83], v[20:23], v[100:103]\nv_mfma_f32_16x16x32_bf16 v[112:115], v[84:87], v[20:23], v[112:115]\n"
        "buffer_load_dwordx4 v[68:71], %[wa], %[rs], %[so] offen offset:0 sc1\nv_mfma_f32_16x16x32_bf16 v[132:135], v[88:91], v[20:23], v[132:135]\n"
        "v_mfma_f32_16x16x32_bf16 v[152:155], v[92:95], v[20:23], v[152:155]\nds_read_b128 v[20:23], %[xa] offset:24576\ns_waitcnt lgkmcnt(4)\n"
        "v_mfma_f32_16x16x32_bf16 v[104:107], v[84:87], v[12:15], v[104:107]\nv_mfma_f32_16x16x32_bf16 v[124:127], v[88:91], v[12:15], v[124:127]\n"
        "buffer_load_dwordx4 v[72:75], %[wa], %[rs], %[so] offen offset:1024 sc1\nv_mfma_f32_16x16x32_bf16 v[144:147], v[92:95], v[12:15], v[144:147]\nds_read_b128 v[12:15], %[xa] offset:20480\n"
        "s_waitcnt lgkmcnt(4)\nv_mfma_f32_16x16x32_bf16 v[116:119], v[84:87], v[24:27], v[116:119]\nv_mfma_f32_16x16x32_bf16 v[136:139], v[88:91], v[24:27], v[136:139]\n"
        "buffer_load_dwordx4 v[76:79], %[wa], %[rs], %[so] offen offset:2048 sc1\nv_mfma_f32_16x16x32_bf16 v[156:159], v[92:95], v[24:27], v[156:159]\nds_read_b128 v[24:27], %[xa] offset:26624\n"
        "s_waitcnt lgkmcnt(4)\nv_mfma_f32_16x16x32_bf16 v[120:123], v[84:87], v[28:31], v[120:123]\nv_add_u32 %[sy], 4, %[sy]\nv_mfma_f32_16x16x32_bf16 v[140:143], v[88:91], v[28:31], v[140:143]\n"
        "ds_write_b32 %[pg], %[sy]\nv_mfma_f32_16x16x32_bf16 v[160:163], v[92:95], v[28:31], v[160:163]\ns_waitcnt vmcnt(8) lgkmcnt(4)\nv_mfma_f32_16x16x32_bf16 v[96:99], v[32:35], v[16:19], v[96:99]\n"
        "s_addk_i32 %[so], 15360\nds_read_b128 v[28:31], %[xa] offset:28672\nv_mfma_f32_16x16x32_bf16 v[108:111], v[36:39], v[16:19], v[108:111]\n"
        "v_mfma_f32_16x16x32_bf16 v[128:131], v[40:43], v[16:19], v[128:131]\nbuffer_load_dwordx4 v[80:83], %[wa], %[rs], %[so] offen offset:3072 sc1\n"
        "v_mfma_f32_16x16x32_bf16 v[148:151], v[44:47], v[16:19], v[148:151]\nds_read_b128 v[16:19], %[xb] offset:22528\ns_waitcnt lgkmcnt(5)\n"
        "v_mfma_f32_16x16x32_bf16 v[100:103], v[32:35], v[20:23], v[100:103]\nv_mfma_f32_16x16x32_bf16 v[112:115], v[36:39], v[20:23], v[112:115]\n"
        "buffer_load_dwordx4 v[84:87], %[wa], %[rs], %[so] offen offset:0 sc1\nv_mfma_f32_16x16x32_bf16 v[132:135], v[40:43], v[20:23], v[132:135]\n"
        "v_mfma_f32_16x16x32_bf16 v[152:155], v[44:47], v[20:23], v[152:155]\nds_read_b128 v[20:23], %[xb] offset:24576\ns_waitcnt lgkmcnt(5)\n"
        "v_mfma_f32_16x16x32_bf16 v[104:107], v[36:39], v[12:15], v[104:107]\nv_mfma_f32_16x16x32_bf16 v[124:127], v[40:43], v[12:15], v[124:127]\n"
        "buffer_load_dwordx4 v[88:91], %[wa], %[rs], %[so] offen offset:1024 sc1\nv_mfma_f32_16x16x32_bf16 v[144:147], v[44:47], v[12:15], v[144:147]\nds_read_b128 v[12:15], %[xb] offset:20480\n"
        "s_waitcnt lgkmcnt(5)\nv_mfma_f32_16x16x32_bf16 v[116:119], v[36:39], v[24:27], v[116:119]\nv_mfma_f32_16x16x32_bf16 v[136:139], v[40:43], v[24:27], v[136:139]\n"
        "buffer_load_dwordx4 v[92:95], %[wa], %[rs], %[so] offen offset:2048 sc1\nv_mfma_f32_16x16x32_bf16 v[156:159], v[44:47], v[24:27], v[156:159]\nds_read_b128 v[24:27], %[xb] offset:26624\n"
        "s_waitcnt lgkmcnt(4)\nv_mfma_f32_16x16x32_bf16 v[120:123], v[36:39], v[28:31], v[120:123]\nv_mfma_f32_16x16x32_bf16 v[140:143], v[40:43], v[28:31], v[140:143]\ns_cmp_lg_u32 %[so], 291840\n"
        "v_mfma_f32_16x16x32_bf16 v[160:163], v[44:47], v[28:31], v[160:163]\nv_add_u32 %[xa], 20480, %[xa]\nv_add_u32 %[xb], 20480, %[xb]\ns_cbranch_scc1 .LB0l%=\ns_waitcnt vmcnt(8) lgkmcnt(3)\n"
        "v_mfma_f32_16x16x32_bf16 v[96:99], v[48:51], v[16:19], v[96:99]\nds_read_b128 v[28:31], %[xb] offset:8192\nv_mfma_f32_16x16x32_bf16 v[108:111], v[52:55], v[16:19], v[108:111]\n"
        "v_mfma_f32_16x16x32_bf16 v[128:131], v[56:59], v[16:19], v[128:131]\nv_mfma_f32_16x16x32_bf16 v[148:151], v[60:63], v[16:19], v[148:151]\nds_read_b128 v[16:19], %[xa] offset:12288\n"
        "s_waitcnt lgkmcnt(4)\nv_mfma_f32_16x16x32_bf16 v[100:103], v[48:51], v[20:23], v[100:103]\nv_mfma_f32_16x16x32_bf16 v[112:115], v[52:55], v[20:23], v[112:115]\n"
        "v_mfma_f32_16x16x32_bf16 v[132:135], v[56:59], v[20:23], v[132:135]\nv_mfma_f32_16x16x32_bf16 v[152:155], v[60:63], v[20:23], v[152:155]\nds_read_b128 v[20:23], %[xa] offset:14336\n"
        "s_waitcnt lgkmcnt(4)\nv_mfma_f32_16x16x32_bf16 v[104:107], v[52:55], v[12:15], v[104:107]\nv_mfma_f32_16x16x32_bf16 v[124:127], v[56:59], v[12:15], v[124:127]\n"
        "v_mfma_f32_16x16x32_bf16 v[144:147], v[60:63], v[12:15], v[144:147]\nds_read_b128 v[12:15], %[xa] offset:10240\ns_waitcnt lgkmcnt(4)\n"
        "v_mfma_f32_16x16x32_bf16 v[116:119], v[52:55], v[24:27], v[116:119]\nv_mfma_f32_16x16x32_bf16 v[136:139], v[56:59], v[24:27], v[136:139]\n"
        "v_mfma_f32_16x16x32_bf16 v[156:159], v[60:63], v[24:27], v[156:159]\nds_read_b128 v[24:27], %[xa] offset:16384\ns_waitcnt lgkmcnt(4)\n"
        "v_mfma_f32_16x16x32_bf16 v[120:123], v[52:55], v[28:31], v[120:123]\nv_add_u32 %[sy], 4, %[sy]\nv_mfma_f32_16x16x32_bf16 v[140:143], v[56:59], v[28:31], v[140:143]\n"
        "v_mfma_f32_16x16x32_bf16 v[160:163], v[60:63], v[28:31], v[160:163]\ns_waitcnt vmcnt(4) lgkmcnt(3)\nv_mfma_f32_16x16x32_bf16 v[96:99], v[64:67], v[16:19], v[96:99]\n"
        "ds_read_b128 v[28:31], %[xa] offset:18432\nv_mfma_f32_16x16x32_bf16 v[108:111], v[68:71], v[16:19], v[108:111]\nv_mfma_f32_16x16x32_bf16 v[128:131], v[72:75], v[16:19], v[128:131]\n"
        "ds_read_b32 %[pv], %[sy] offset:4\nv_mfma_f32_16x16x32_bf16 v[148:151], v[76:79], v[16:19], v[148:151]\nds_read_b128 v[16:19], %[xb] offset:12288\ns_waitcnt lgkmcnt(5)\n"
        "v_mfma_f32_16x16x32_bf16 v[100:103], v[64:67], v[20:23], v[100:103]\nv_mfma_f32_16x16x32_bf16 v[112:115], v[68:71], v[20:23], v[112:115]\n"
        "v_mfma_f32_16x16x32_bf16 v[132:135], v[72:75], v[20:23], v[132:135]\nv_mfma_f32_16x16x32_bf16 v[152:155], v[76:79], v[20:23], v[152:155]\nds_read_b128 v[20:23], %[xb] offset:14336\n"
        "s_waitcnt lgkmcnt(5)\nv_mfma_f32_16x16x32_bf16 v[104:107], v[68:71], v[12:15], v[104:107]\nv_mfma_f32_16x16x32_bf16 v[124:127], v[72:75], v[12:15], v[124:127]\n"
        "v_mfma_f32_16x16x32_bf16 v[144:147], v[76:79], v[12:15], v[144:147]\nds_read_b128 v[12:15], %[xb] offset:10240\ns_waitcnt lgkmcnt(5)\n"
        "v_mfma_f32_16x16x32_bf16 v[116:119], v[68:71], v[24:27], v[116:119]\nv_mfma_f32_16x16x32_bf16 v[136:139], v[72:75], v[24:27], v[136:139]\n"
        "v_mfma_f32_16x16x32_bf16 v[156:159], v[76:79], v[24:27], v[156:159]\nds_read_b128 v[24:27], %[xb] offset:16384\ns_waitcnt lgkmcnt(5)\n"
        "v_mfma_f32_16x16x32_bf16 v[120:123], v[68:71], v[28:31], v[120:123]\nv_mfma_f32_16x16x32_bf16 v[140:143], v[72:75], v[28:31], v[140:143]\n"
        "v_mfma_f32_16x16x32_bf16 v[160:163], v[76:79], v[28:31], v[160:163]\ns_waitcnt vmcnt(0) lgkmcnt(3)\nv_mfma_f32_16x16x32_bf16 v[96:99], v[80:83], v[16:19], v[96:99]\n"
        "v_readfirstlane_b32 %[s0], %[pv]\nds_read_b128 v[28:31], %[xb] offset:18432\nv_mfma_f32_16x16x32_bf16 v[108:111], v[84:87], v[16:19], v[108:111]\n"
        "v_mfma_f32_16x16x32_bf16 v[128:131], v[88:91], v[16:19], v[128:131]\ns_cmp_ge_i32 %[s0], %[nst]\nv_mfma_f32_16x16x32_bf16 v[148:151], v[92:95], v[16:19], v[148:151]\ns_cbranch_scc0 .LB0t%=\n"
        ".LB0u%=:\ns_waitcnt lgkmcnt(3)\nv_mfma_f32_16x16x32_bf16 v[100:103], v[80:83], v[20:23], v[100:103]\nv_add_u32 %[sy], 4, %[sy]\n"
        "v_mfma_f32_16x16x32_bf16 v[112:115], v[84:87], v[20:23], v[112:115]\nds_read_b64 v[32:33], %[cwa] offset:896\nv_mfma_f32_16x16x32_bf16 v[132:135], v[88:91], v[20:23], v[132:135]\n"
        "ds_read_b64 v[36:37], %[cwa] offset:256\nv_mfma_f32_16x16x32_bf16 v[152:155], v[92:95], v[20:23], v[152:155]\nds_read_b64 v[40:41], %[cwa] offset:384\ns_waitcnt lgkmcnt(5)\n"
        "v_mfma_f32_16x16x32_bf16 v[104:107], v[84:87], v[12:15], v[104:107]\nds_read_b64 v[44:45], %[cwa] offset:1536\nv_mfma_f32_16x16x32_bf16 v[124:127], v[88:91], v[12:15], v[124:127]\n"
        "ds_read_b128 v[48:51], %[mra] offset:0\nv_mfma_f32_16x16x32_bf16 v[144:147], v[92:95], v[12:15], v[144:147]\nds_read_b128 v[52:55], %[mra] offset:256\ns_waitcnt lgkmcnt(7)\n"
        "v_mfma_f32_16x16x32_bf16 v[116:119], v[84:87], v[24:27], v[116:119]\nds_read_b128 v[56:59], %[mra] offset:512\nv_mfma_f32_16x16x32_bf16 v[136:139], v[88:91], v[24:27], v[136:139]\n"
        "ds_read_b128 v[60:63], %[mra] offset:768\nv_mfma_f32_16x16x32_bf16 v[156:159], v[92:95], v[24:27], v[156:159]\nds_read_b128 v[64:67], %[mra] offset:1024\ns_waitcnt lgkmcnt(9)\n"
        "v_mfma_f32_16x16x32_bf16 v[120:123], v[84:87], v[28:31], v[120:123]\nv_add_u32 %[sy], 4, %[sy]\nv_mfma_f32_16x16x32_bf16 v[140:143], v[88:91], v[28:31], v[140:143]\ns_setprio %[pfs]\n"
        "v_mfma_f32_16x16x32_bf16 v[160:163], v[92:95], v[28:31], v[160:163]\ns_waitcnt lgkmcnt(0)\nv_mfma_f32_16x16x32_bf16 v[96:99], v[32:35], v[52:55], v[96:99]\n"
        "v_mfma_f32_16x16x32_bf16 v[100:103], v[32:35], v[56:59], v[100:103]\nv_mfma_f32_16x16x32_bf16 v[108:111], v[36:39], v[52:55], v[108:111]\n"
        "v_mfma_f32_16x16x32_bf16 v[112:115], v[36:39], v[56:59], v[112:115]\nv_mfma_f32_16x16x32_bf16 v[128:131], v[40:43], v[52:55], v[128:131]\nv_cvt_pk_bf16_f32 v96, v96, v97\n"
        "v_cvt_pk_bf16_f32 v97, v98, v99\nv_cvt_pk_bf16_f32 v98, v100, v101\nv_cvt_pk_bf16_f32 v99, v102, v103\nv_mfma_f32_16x16x32_bf16 v[132:135], v[40:43], v[56:59], v[132:135]\n"
        "v_permlane16_swap_b32 v96, v98\nv_permlane16_swap_b32 v97, v99\nds_write_b128 %[stb], v[96:99] offset:8160\nv_mfma_f32_16x16x32_bf16 v[104:107], v[36:39], v[48:51], v[104:107]\n"
        "v_cvt_pk_bf16_f32 v108, v108, v109\nv_cvt_pk_bf16_f32 v109, v110, v111\nv_cvt_pk_bf16_f32 v110, v112, v113\nv_cvt_pk_bf16_f32 v111, v114, v115\n"
        "v_mfma_f32_16x16x32_bf16 v[124:127], v[40:43], v[48:51], v[124:127]\nv_permlane16_swap_b32 v108, v110\nv_permlane16_swap_b32 v109, v111\nds_write_b128 %[stb], v[108:111] offset:8000\n"
        "v_mfma_f32_16x16x32_bf16 v[116:119], v[36:39], v[60:63], v[116:119]\nv_cvt_pk_bf16_f32 v128, v128, v129\nv_cvt_pk_bf16_f32 v129, v130, v131\nv_cvt_pk_bf16_f32 v130, v132, v133\n"
        "v_cvt_pk_bf16_f32 v131, v134, v135\nv_mfma_f32_16x16x32_bf16 v[120:123], v[36:39], v[64:67], v[120:123]\nv_permlane16_swap_b32 v128, v130\nv_permlane16_swap_b32 v129, v131\n"
        "ds_write_b128 %[stb], v[128:131] offset:8032\nv_cvt_pk_bf16_f32 v104, v104, v105\nv_mfma_f32_16x16x32_bf16 v[136:139], v[40:43], v[60:63], v[136:139]\nv_cvt_pk_bf16_f32 v105, v106, v107\n"
        "ds_write_b64 %[sta], v[104:105] offset:64\nv_cvt_pk_bf16_f32 v124, v124, v125\nv_cvt_pk_bf16_f32 v125, v126, v127\nv_mfma_f32_16x16x32_bf16 v[140:143], v[40:43], v[64:67], v[140:143]\n"
        "ds_write_b64 %[sta], v[124:125] offset:96\nv_mfma_f32_16x16x32_bf16 v[148:151], v[44:47], v[52:55], v[148:151]\nv_cvt_pk_bf16_f32 v116, v116, v117\nv_cvt_pk_bf16_f32 v117, v118, v119\n"
        "v_cvt_pk_bf16_f32 v118, v120, v121\nv_cvt_pk_bf16_f32 v119, v122, v123\nv_mfma_f32_16x16x32_bf16 v[152:155], v[44:47], v[56:59], v[152:155]\nv_permlane16_swap_b32 v116, v118\n"
        "v_permlane16_swap_b32 v117, v119\nds_write_b128 %[stb], v[116:119] offset:23872\nv_mfma_f32_16x16x32_bf16 v[144:147], v[44:47], v[48:51], v[144:147]\nv_cvt_pk_bf16_f32 v136, v136, v137\n"
        "v_cvt_pk_bf16_f32 v137, v138, v139\nv_cvt_pk_bf16_f32 v138, v140, v141\nv_cvt_pk_bf16_f32 v139, v142, v143\nv_mfma_f32_16x16x32_bf16 v[156:159], v[44:47], v[60:63], v[156:159]\n"
        "v_permlane16_swap_b32 v136, v138\nv_permlane16_swap_b32 v137, v139\nds_write_b128 %[stb], v[136:139] offset:23904\nds_write_b32 %[pg], %[sy]\n"
        "v_mfma_f32_16x16x32_bf16 v[160:163], v[44:47], v[64:67], v[160:163]\nv_add_u32 %[sy], 4, %[sy]\nv_cvt_pk_bf16_f32 v148, v148, v149\nv_cvt_pk_bf16_f32 v149, v150, v151\n"
        "v_cvt_pk_bf16_f32 v150, v152, v153\ns_nop 3\nv_cvt_pk_bf16_f32 v151, v154, v155\ns_nop 0\nv_permlane16_swap_b32 v148, v150\nv_permlane16_swap_b32 v149, v151\n"
        "ds_write_b128 %[stb], v[148:151] offset:8320\nv_cvt_pk_bf16_f32 v144, v144, v145\nv_cvt_pk_bf16_f32 v145, v146, v147\nds_write_b64 %[sta], v[144:145] offset:384\n"
        "v_cvt_pk_bf16_f32 v156, v156, v157\nv_cvt_pk_bf16_f32 v157, v158, v159\nv_cvt_pk_bf16_f32 v158, v160, v161\nv_cvt_pk_bf16_f32 v159, v162, v163\ns_nop 0\nv_permlane16_swap_b32 v156, v158\n"
        "v_permlane16_swap_b32 v157, v159\nds_write_b128 %[stb], v[156:159] offset:24192\nds_write_b32 %[pg], %[sy]\ns_branch .LE%=\n.LB0t%=:\ns_mov_b32 %[s1], 0x100000\n.LB0q%=:\n"
        "ds_read_b32 %[pv], %[sy] offset:4\ns_waitcnt lgkmcnt(0)\nv_readfirstlane_b32 %[s0], %[pv]\ns_nop 0\ns_cmp_ge_i32 %[s0], %[nst]\ns_cbranch_scc1 .LB0u%=\ns_sleep 1\ns_sub_u32 %[s1], %[s1], 1\n"
        "s_cbranch_scc0 .LB0q%=\ns_branch .LB0u%=\n.LC%=:\ns_mov_b32 %[s1], 0x100000\n.LC0w%=:\nds_read_b32 %[pv], %[sy] offset:%[ow]\ns_waitcnt lgkmcnt(0)\nv_readfirstlane_b32 %[s0], %[pv]\ns_nop 0\n"
        "s_cmp_ge_i32 %[s0], 1\ns_cbranch_scc1 .LC0h%=\ns_sleep 1\ns_sub_u32 %[s1], %[s1], 1\ns_cbranch_scc0 .LC0w%=\n.LC0h%=:\ns_mov_b32 %[so], 0\n"
        "buffer_load_dwordx4 v[32:35], %[wa], %[rs], %[so] offen offset:0 sc1\nbuffer_load_dwordx4 v[36:39], %[wa], %[rs], %[so] offen offset:1024 sc1\n"
        "buffer_load_dwordx4 v[40:43], %[wa], %[rs], %[so] offen offset:2048 sc1\nbuffer_load_dwordx4 v[44:47], %[wa], %[rs], %[so] offen offset:3072 sc1\ns_mov_b32 %[so], 15360\n"
        "buffer_load_dwordx4 v[48:51], %[wa], %[rs], %[so] offen offset:0 sc1\nbuffer_load_dwordx4 v[52:55], %[wa], %[rs], %[so] offen offset:1024 sc1\n"
        "buffer_load_dwordx4 v[56:59], %[wa], %[rs], %[so] offen offset:2048 sc1\nbuffer_load_dwordx4 v[60:63], %[wa], %[rs], %[so] offen offset:3072 sc1\ns_mov_b32 %[so], 30720\n"
        "buffer_load_dwordx4 v[64:67], %[wa], %[rs], %[so] offen offset:0 sc1\nbuffer_load_dwordx4 v[68:71], %[wa], %[rs], %[so] offen offset:1024 sc1\n"
        "buffer_load_dwordx4 v[72:75], %[wa], %[rs], %[so] offen offset:2048 sc1\nbuffer_load_dwordx4 v[76:79], %[wa], %[rs], %[so] offen offset:3072 sc1\ns_mov_b32 %[so], 46080\n"
        "buffer_load_dwordx4 v[80:83], %[wa], %[rs], %[so] offen offset:0 sc1\nbuffer_load_dwordx4 v[84:87], %[wa], %[rs], %[so] offen offset:1024 sc1\n"
        "buffer_load_dwordx4 v[88:91], %[wa], %[rs], %[so] offen offset:2048 sc1\nbuffer_load_dwordx4 v[92:95], %[wa], %[rs], %[so] offen offset:3072 sc1\ns_setprio %[pf]\ns_mov_b32 %[s1], 0x100000\n"
        ".LC0v%=:\nds_read_b32 %[pv], %[sy] offset:0\ns_waitcnt lgkmcnt(0)\nv_readfirstlane_b32 %[s0], %[pv]\ns_nop 0\ns_cmp_ge_i32 %[s0], 3\ns_cbranch_scc1 .LC0g%=\ns_sleep 1\n"
        "s_sub_u32 %[s1], %[s1], 1\ns_cbranch_scc0 .LC0v%=\n.LC0g%=:\nds_read_b128 v[24:27], %[xa] offset:6144\nds_read_b128 v[28:31], %[xa] offset:8192\nds_read_b128 v[12:15], %[xa] offset:0\n"
        "ds_read_b128 v[16:19], %[xa] offset:2048\ns_waitcnt vmcnt(12) lgkmcnt(3)\nv_mfma_f32_16x16x32_bf16 v[96:99], v[32:35], v[24:27], 0\nds_read_b128 v[20:23], %[xa] offset:4096\n"
        "v_mfma_f32_16x16x32_bf16 v[116:119], v[36:39], v[24:27], 0\nv_mfma_f32_16x16x32_bf16 v[136:139], v[40:43], v[24:27], 0\nv_mfma_f32_16x16x32_bf16 v[156:159], v[44:47], v[24:27], 0\n"
        "ds_read_b128 v[24:27], %[xb] offset:6144\ns_waitcnt lgkmcnt(4)\nv_mfma_f32_16x16x32_bf16 v[100:103], v[32:35], v[28:31], 0\nv_mfma_f32_16x16x32_bf16 v[120:123], v[36:39], v[28:31], 0\n"
        "v_mfma_f32_16x16x32_bf16 v[140:143], v[40:43], v[28:31], 0\nv_mfma_f32_16x16x32_bf16 v[160:163], v[44:47], v[28:31], 0\nds_read_b128 v[28:31], %[xb] offset:8192\ns_waitcnt lgkmcnt(4)\n"
        "v_mfma_f32_16x16x32_bf16 v[104:107], v[36:39], v[12:15], 0\nv_mfma_f32_16x16x32_bf16 v[124:127], v[40:43], v[12:15], 0\nv_mfma_f32_16x16x32_bf16 v[144:147], v[44:47], v[12:15], 0\n"
        "ds_read_b128 v[12:15], %[xb] offset:0\ns_waitcnt lgkmcnt(4)\nv_mfma_f32_16x16x32_bf16 v[108:111], v[36:39], v[16:19], 0\nv_mfma_f32_16x16x32_bf16 v[128:131], v[40:43], v[16:19], 0\n"
        "v_mfma_f32_16x16x32_bf16 v[148:151], v[44:47], v[16:19], 0\nds_read_b128 v[16:19], %[xb] offset:2048\ns_waitcnt lgkmcnt(4)\nv_mfma_f32_16x16x32_bf16 v[112:115], v[36:39], v[20:23], 0\n"
        "v_mfma_f32_16x16x32_bf16 v[132:135], v[40:43], v[20:23], 0\nv_mfma_f32_16x16x32_bf16 v[152:155], v[44:47], v[20:23], 0\n.LC0l%=:\ns_waitcnt vmcnt(8) lgkmcnt(3)\n"
        "v_mfma_f32_16x16x32_bf16 v[96:99], v[48:51], v[24:27], v[96:99]\ns_addk_i32 %[so], 15360\nds_read_b128 v[20:23], %[xb] offset:4096\n"
        "v_mfma_f32_16x16x32_bf16 v[116:119], v[52:55], v[24:27], v[116:119]\nv_mfma_f32_16x16x32_bf16 v[136:139], v[56:59], v[24:27], v[136:139]\n"
        "buffer_load_dwordx4 v[32:35], %[wa], %[rs], %[so] offen offset:0 sc1\nv_mfma_f32_16x16x32_bf16 v[156:159], v[60:63], v[24:27], v[156:159]\nds_read_b128 v[24:27], %[xa] offset:16384\n"
        "s_waitcnt lgkmcnt(4)\nv_mfma_f32_16x16x32_bf16 v[100:103], v[48:51], v[28:31], v[100:103]\nv_mfma_f32_16x16x32_bf16 v[120:123], v[52:55], v[28:31], v[120:123]\n"
        "buffer_load_dwordx4 v[36:39], %[wa], %[rs], %[so] offen offset:1024 sc1\nv_mfma_f32_16x16x32_bf16 v[140:143], v[56:59], v[28:31], v[140:143]\n"
        "v_mfma_f32_16x16x32_bf16 v[160:163], v[60:63], v[28:31], v[160:163]\nds_read_b128 v[28:31], %[xa] offset:18432\ns_waitcnt lgkmcnt(4)\n"
        "v_mfma_f32_16x16x32_bf16 v[104:107], v[52:55], v[12:15], v[104:107]\nv_mfma_f32_16x16x32_bf16 v[124:127], v[56:59], v[12:15], v[124:127]\n"
        "buffer_load_dwordx4 v[40:43], %[wa], %[rs], %[so] offen offset:2048 sc1\nv_mfma_f32_16x16x32_bf16 v[144:147], v[60:63], v[12:15], v[144:147]\nds_read_b128 v[12:15], %[xa] offset:10240\n"
        "s_waitcnt lgkmcnt(4)\nv_mfma_f32_16x16x32_bf16 v[108:111], v[52:55], v[16:19], v[108:111]\nv_mfma_f32_16x16x32_bf16 v[128:131], v[56:59], v[16:19], v[128:131]\n"
        "buffer_load_dwordx4 v[44:47], %[wa], %[rs], %[so] offen offset:3072 sc1\nv_mfma_f32_16x16x32_bf16 v[148:151], v[60:63], v[16:19], v[148:151]\nds_read_b128 v[16:19], %[xa] offset:12288\n"
        "s_waitcnt lgkmcnt(4)\nv_mfma_f32_16x16x32_bf16 v[112:115], v[52:55], v[20:23], v[112:115]\nv_add_u32 %[sy], 4, %[sy]\nv_mfma_f32_16x16x32_bf16 v[132:135], v[56:59], v[20:23], v[132:135]\n"
        "ds_write_b32 %[pg], %[sy]\nv_mfma_f32_16x16x32_bf16 v[152:155], v[60:63], v[20:23], v[152:155]\ns_waitcnt vmcnt(8) lgkmcnt(4)\nv_mfma_f32_16x16x32_bf16 v[96:99], v[64:67], v[24:27], v[96:99]\n"
        "s_addk_i32 %[so], 15360\nds_read_b128 v[20:23], %[xa] offset:14336\nv_mfma_f32_16x16x32_bf16 v[116:119], v[68:71], v[24:27], v[116:119]\n"
        "v_mfma_f32_16x16x32_bf16 v[136:139], v[72:75], v[24:27], v[136:139]\nbuffer_load_dwordx4 v[48:51], %[wa], %[rs], %[so] offen offset:0 sc1\n"
        "v_mfma_f32_16x16x32_bf16 v[156:159], v[76:79], v[24:27], v[156:159]\nds_read_b128 v[24:27], %[xb] offset:16384\ns_waitcnt lgkmcnt(5)\n"
        "v_mfma_f32_16x16x32_bf16 v[100:103], v[64:67], v[28:31], v[100:103]\nv_mfma_f32_16x16x32_bf16 v[120:123], v[68:71], v[28:31], v[120:123]\n"
        "buffer_load_dwordx4 v[52:55], %[wa], %[rs], %[so] offen offset:1024 sc1\nv_mfma_f32_16x16x32_bf16 v[140:143], v[72:75], v[28:31], v[140:143]\n"
        "v_mfma_f32_16x16x32_bf16 v[160:163], v[76:79], v[28:31], v[160:163]\nds_read_b128 v[28:31], %[xb] offset:18432\ns_waitcnt lgkmcnt(5)\n"
        "v_mfma_f32_16x16x32_bf16 v[104:107], v[68:71], v[12:15], v[104:107]\nv_mfma_f32_16x16x32_bf16 v[124:127], v[72:75], v[12:15], v[124:127]\n"
        "buffer_load_dwordx4 v[56:59], %[wa], %[rs], %[so] offen offset:2048 sc1\nv_mfma_f32_16x16x32_bf16 v[144:147], v[76:79], v[12:15], v[144:147]\nds_read_b128 v[12:15], %[xb] offset:10240\n"
        "s_waitcnt lgkmcnt(5)\nv_mfma_f32_16x16x32_bf16 v[108:111], v[68:71], v[16:19], v[108:111]\nv_mfma_f32_16x16x32_bf16 v[128:131], v[72:75], v[16:19], v[128:131]\n"
        "buffer_load_dwordx4 v[60:63], %[wa], %[rs], %[so] offen offset:3072 sc1\nv_mfma_f32_16x16x32_bf16 v[148:151], v[76:79], v[16:19], v[148:151]\nds_read_b128 v[16:19], %[xb] offset:12288\n"
        "s_waitcnt lgkmcnt(4)\nv_mfma_f32_16x16x32_bf16 v[112:115], v[68:71], v[20:23], v[112:115]\nv_mfma_f32_16x16x32_bf16 v[132:135], v[72:75], v[20:23], v[132:135]\n"
        "v_mfma_f32_16x16x32_bf16 v[152:155], v[76:79], v[20:23], v[152:155]\ns_waitcnt vmcnt(8) lgkmcnt(3)\nv_mfma_f32_16x16x32_bf16 v[96:99], v[80:83], v[24:27], v[96:99]\ns_addk_i32 %[so], 15360\n"
        "ds_read_b128 v[20:23], %[xb] offset:14336\nv_mfma_f32_16x16x32_bf16 v[116:119], v[84:87], v[24:27], v[116:119]\nv_mfma_f32_16x16x32_bf16 v[136:139], v[88:91], v[24:27], v[136:139]\n"
        "buffer_load_dwordx4 v[64:67], %[wa], %[rs], %[so] offen offset:0 sc1\nv_mfma_f32_16x16x32_bf16 v[156:159], v[92:95], v[24:27], v[156:159]\nds_read_b128 v[24:27], %[xa] offset:26624\n"
        "s_waitcnt lgkmcnt(4)\nv_mfma_f32_16x16x32_bf16 v[100:103], v[80:83], v[28:31], v[100:103]\nv_mfma_f32_16x16x32_bf16 v[120:123], v[84:87], v[28:31], v[120:123]\n"
        "buffer_load_dwordx4 v[68:71], %[wa], %[rs], %[so] offen offset:1024 sc1\nv_mfma_f32_16x16x32_bf16 v[140:143], v[88:91], v[28:31], v[140:143]\n"
        "v_mfma_f32_16x16x32_bf16 v[160:163], v[92:95], v[28:31], v[160:163]\nds_read_b128 v[28:31], %[xa] offset:28672\ns_waitcnt lgkmcnt(4)\n"
        "v_mfma_f32_16x16x32_bf16 v[104:107], v[84:87], v[12:15], v[104:107]\nv_mfma_f32_16x16x32_bf16 v[124:127], v[88:91], v[12:15], v[124:127]\n"
        "buffer_load_dwordx4 v[72:75], %[wa], %[rs], %[so] offen offset:2048 sc1\nv_mfma_f32_16x16x32_bf16 v[144:147], v[92:95], v[12:15], v[144:147]\nds_read_b128 v[12:15], %[xa] offset:20480\n"
        "s_waitcnt lgkmcnt(4)\nv_mfma_f32_16x16x32_bf16 v[108:111], v[84:87], v[16:19], v[108:111]\nv_mfma_f32_16x16x32_bf16 v[128:131], v[88:91], v[16:19], v[128:131]\n"
        "buffer_load_dwordx4 v[76:79], %[wa], %[rs], %[so] offen offset:3072 sc1\nv_mfma_f32_16x16x32_bf16 v[148:151], v[92:95], v[16:19], v[148:151]\nds_read_b128 v[16:19], %[xa] offset:22528\n"
        "s_waitcnt lgkmcnt(4)\nv_mfma_f32_16x16x32_bf16 v[112:115], v[84:87], v[20:23], v[112:115]\nv_add_u32 %[sy], 4, %[sy]\nv_mfma_f32_16x16x32_bf16 v[132:135], v[88:91], v[20:23], v[132:135]\n"
        "ds_write_b32 %[pg], %[sy]\nv_mfma_f32_16x16x32_bf16 v[152:155], v[92:95], v[20:23], v[152:155]\ns_waitcnt vmcnt(8) lgkmcnt(4)\nv_mfma_f32_16x16x32_bf16 v[96:99], v[32:35], v[24:27], v[96:99]\n"
        "s_addk_i32 %[so], 15360\nds_read_b128 v[20:23], %[xa] offset:24576\nv_mfma_f32_16x16x32_bf16 v[116:119], v[36:39], v[24:27], v[116:119]\n"
        "v_mfma_f32_16x16x32_bf16 v[136:139], v[40:43], v[24:27], v[136:139]\nbuffer_load_dwordx4 v[80:83], %[wa], %[rs], %[so] offen offset:0 sc1\n"
        "v_mfma_f32_16x16x32_bf16 v[156:159], v[44:47], v[24:27], v[156:159]\nds_read_b128 v[24:27], %[xb] offset:26624\ns_waitcnt lgkmcnt(5)\n"
        "v_mfma_f32_16x16x32_bf16 v[100:103], v[32:35], v[28:31], v[100:103]\nv_mfma_f32_16x16x32_bf16 v[120:123], v[36:39], v[28:31], v[120:123]\n"
        "buffer_load_dwordx4 v[84:87], %[wa], %[rs], %[so] offen offset:1024 sc1\nv_mfma_f32_16x16x32_bf16 v[140:143], v[40:43], v[28:31], v[140:143]\n"
        "v_mfma_f32_16x16x32_bf16 v[160:163], v[44:47], v[28:31], v[160:163]\nds_read_b128 v[28:31], %[xb] offset:28672\ns_waitcnt lgkmcnt(5)\n"
        "v_mfma_f32_16x16x32_bf16 v[104:107], v[36:39], v[12:15], v[104:107]\nv_mfma_f32_16x16x32_bf16 v[124:127], v[40:43], v[12:15], v[124:127]\n"
        "buffer_load_dwordx4 v[88:91], %[wa], %[rs], %[so] offen offset:2048 sc1\nv_mfma_f32_16x16x32_bf16 v[144:147], v[44:47], v[12:15], v[144:147]\nds_read_b128 v[12:15], %[xb] offset:20480\n"
        "s_waitcnt lgkmcnt(5)\nv_mfma_f32_16x16x32_bf16 v[108:111], v[36:39], v[16:19], v[108:111]\nv_mfma_f32_16x16x32_bf16 v[128:131], v[40:43], v[16:19], v[128:131]\n"
        "buffer_load_dwordx4 v[92:95], %[wa], %[rs], %[so] offen offset:3072 sc1\nv_mfma_f32_16x16x32_bf16 v[148:151], v[44:47], v[16:19], v[148:151]\nds_read_b128 v[16:19], %[xb] offset:22528\n"
        "s_waitcnt lgkmcnt(4)\nv_mfma_f32_16x16x32_bf16 v[112:115], v[36:39], v[20:23], v[112:115]\nv_mfma_f32_16x16x32_bf16 v[132:135], v[40:43], v[20:23], v[132:135]\ns_cmp_lg_u32 %[so], 291840\n"
        "v_mfma_f32_16x16x32_bf16 v[152:155], v[44:47], v[20:23], v[152:155]\nv_add_u32 %[xa], 20480, %[xa]\nv_add_u32 %[xb], 20480, %[xb]\ns_cbranch_scc1 .LC0l%=\ns_waitcnt vmcnt(8) lgkmcnt(3)\n"
        "v_mfma_f32_16x16x32_bf16 v[96:99], v[48:51], v[24:27], v[96:99]\nds_read_b128 v[20:23], %[xb] offset:4096\nv_mfma_f32_16x16x32_bf16 v[116:119], v[52:55], v[24:27], v[116:119]\n"
        "v_mfma_f32_16x16x32_bf16 v[136:139], v[56:59], v[24:27], v[136:139]\nv_mfma_f32_16x16x32_bf16 v[156:159], v[60:63], v[24:27], v[156:159]\nds_read_b128 v[24:27], %[xa] offset:16384\n"
        "s_waitcnt lgkmcnt(4)\nv_mfma_f32_16x16x32_bf16 v[100:103], v[48:51], v[28:31], v[100:103]\nv_mfma_f32_16x16x32_bf16 v[120:123], v[52:55], v[28:31], v[120:123]\n"
        "v_mfma_f32_16x16x32_bf16 v[140:143], v[56:59], v[28:31], v[140:143]\nv_mfma_f32_16x16x32_bf16 v[160:163], v[60:63], v[28:31], v[160:163]\nds_read_b128 v[28:31], %[xa] offset:18432\n"
        "s_waitcnt lgkmcnt(4)\nv_mfma_f32_16x16x32_bf16 v[104:107], v[52:55], v[12:15], v[104:107]\nv_mfma_f32_16x16x32_bf16 v[124:127], v[56:59], v[12:15], v[124:127]\n"
        "v_mfma_f32_16x16x32_bf16 v[144:147], v[60:63], v[12:15], v[144:147]\nds_read_b128 v[12:15], %[xa] offset:10240\ns_waitcnt lgkmcnt(4)\n"
        "v_mfma_f32_16x16x32_bf16 v[108:111], v[52:55], v[16:19], v[108:111]\nv_mfma_f32_16x16x32_bf16 v[128:131], v[56:59], v[16:19], v[128:131]\n"
        "v_mfma_f32_16x16x32_bf16 v[148:151], v[60:63], v[16:19], v[148:151]\nds_read_b128 v[16:19], %[xa] offset:12288\ns_waitcnt lgkmcnt(4)\n"
        "v_mfma_f32_16x16x32_bf16 v[112:115], v[52:55], v[20:23], v[112:115]\nv_add_u32 %[sy], 4, %[sy]\nv_mfma_f32_16x16x32_bf16 v[132:135], v[56:59], v[20:23], v[132:135]\n"
        "v_mfma_f32_16x16x32_bf16 v[152:155], v[60:63], v[20:23], v[152:155]\ns_waitcnt vmcnt(4) lgkmcnt(3)\nv_mfma_f32_16x16x32_bf16 v[96:99], v[64:67], v[24:27], v[96:99]\n"
        "ds_read_b128 v[20:23], %[xa] offset:14336\nv_mfma_f32_16x16x32_bf16 v[116:119], v[68:71], v[24:27], v[116:119]\nv_mfma_f32_16x16x32_bf16 v[136:139], v[72:75], v[24:27], v[136:139]\n"
        "ds_read_b32 %[pv], %[sy] offset:4\nv_mfma_f32_16x16x32_bf16 v[156:159], v[76:79], v[24:27], v[156:159]\nds_read_b128 v[24:27], %[xb] offset:16384\ns_waitcnt lgkmcnt(5)\n"
        "v_mfma_f32_16x16x32_bf16 v[100:103], v[64:67], v[28:31], v[100:103]\nv_mfma_f32_16x16x32_bf16 v[120:123], v[68:71], v[28:31], v[120:123]\n"
        "v_mfma_f32_16x16x32_bf16 v[140:143], v[72:75], v[28:31], v[140:143]\nv_mfma_f32_16x16x32_bf16 v[160:163], v[76:79], v[28:31], v[160:163]\nds_read_b128 v[28:31], %[xb] offset:18432\n"
        "s_waitcnt lgkmcnt(5)\nv_mfma_f32_16x16x32_bf16 v[104:107], v[68:71], v[12:15], v[104:107]\nv_mfma_f32_16x16x32_bf16 v[124:127], v[72:75], v[12:15], v[124:127]\n"
        "v_mfma_f32_16x16x32_bf16 v[144:147], v[76:79], v[12:15], v[144:147]\nds_read_b128 v[12:15], %[xb] offset:10240\ns_waitcnt lgkmcnt(5)\n"
        "v_mfma_f32_16x16x32_bf16 v[108:111], v[68:71], v[16:19], v[108:111]\nv_mfma_f32_16x16x32_bf16 v[128:131], v[72:75], v[16:19], v[128:131]\n"
        "v_mfma_f32_16x16x32_bf16 v[148:151], v[76:79], v[16:19], v[148:151]\nds_read_b128 v[16:19], %[xb] offset:12288\ns_waitcnt lgkmcnt(5)\n"
        "v_mfma_f32_16x16x32_bf16 v[112:115], v[68:71], v[20:23], v[112:115]\nv_mfma_f32_16x16x32_bf16 v[132:135], v[72:75], v[20:23], v[132:135]\n"
        "v_mfma_f32_16x16x32_bf16 v[152:155], v[76:79], v[20:23], v[152:155]\ns_waitcnt vmcnt(0) lgkmcnt(3)\nv_mfma_f32_16x16x32_bf16 v[96:99], v[80:83], v[24:27], v[96:99]\n"
        "v_readfirstlane_b32 %[s0], %[pv]\nds_read_b128 v[20:23], %[xb] offset:14336\nv_mfma_f32_16x16x32_bf16 v[116:119], v[84:87], v[24:27], v[116:119]\n"
        "v_mfma_f32_16x16x32_bf16 v[136:139], v[88:91], v[24:27], v[136:139]\ns_cmp_ge_i32 %[s0], %[nst]\nv_mfma_f32_16x16x32_bf16 v[156:159], v[92:95], v[24:27], v[156:159]\ns_cbranch_scc0 .LC0t%=\n"
        ".LC0u%=:\ns_waitcnt lgkmcnt(3)\nv_mfma_f32_16x16x32_bf16 v[100:103], v[80:83], v[28:31], v[100:103]\nv_add_u32 %[sy], 4, %[sy]\n"
        "v_mfma_f32_16x16x32_bf16 v[120:123], v[84:87], v[28:31], v[120:123]\nds_read_b64 v[32:33], %[cwa] offset:896\nv_mfma_f32_16x16x32_bf16 v[140:143], v[88:91], v[28:31], v[140:143]\n"
        "ds_read_b64 v[36:37], %[cwa] offset:512\nv_mfma_f32_16x16x32_bf16 v[160:163], v[92:95], v[28:31], v[160:163]\nds_read_b64 v[40:41], %[cwa] offset:1664\ns_waitcnt lgkmcnt(5)\n"
        "v_mfma_f32_16x16x32_bf16 v[104:107], v[84:87], v[12:15], v[104:107]\nds_read_b64 v[44:45], %[cwa] offset:1792\nv_mfma_f32_16x16x32_bf16 v[124:127], v[88:91], v[12:15], v[124:127]\n"
        "ds_read_b128 v[48:51], %[mra] offset:0\nv_mfma_f32_16x16x32_bf16 v[144:147], v[92:95], v[12:15], v[144:147]\nds_read_b128 v[52:55], %[mra] offset:256\ns_waitcnt lgkmcnt(7)\n"
        "v_mfma_f32_16x16x32_bf16 v[108:111], v[84:87], v[16:19], v[108:111]\nds_read_b128 v[56:59], %[mra] offset:512\nv_mfma_f32_16x16x32_bf16 v[128:131], v[88:91], v[16:19], v[128:131]\n"
        "ds_read_b128 v[60:63], %[mra] offset:768\nv_mfma_f32_16x16x32_bf16 v[148:151], v[92:95], v[16:19], v[148:151]\nds_read_b128 v[64:67], %[mra] offset:1024\ns_waitcnt lgkmcnt(9)\n"
        "v_mfma_f32_16x16x32_bf16 v[112:115], v[84:87], v[20:23], v[112:115]\nv_add_u32 %[sy], 4, %[sy]\nv_mfma_f32_16x16x32_bf16 v[132:135], v[88:91], v[20:23], v[132:135]\ns_setprio %[pfs]\n"
        "v_mfma_f32_16x16x32_bf16 v[152:155], v[92:95], v[20:23], v[152:155]\ns_waitcnt lgkmcnt(0)\nv_mfma_f32_16x16x32_bf16 v[96:99], v[32:35], v[60:63], v[96:99]\n"
        "v_mfma_f32_16x16x32_bf16 v[100:103], v[32:35], v[64:67], v[100:103]\nv_mfma_f32_16x16x32_bf16 v[116:119], v[36:39], v[60:63], v[116:119]\n"
        "v_mfma_f32_16x16x32_bf16 v[120:123], v[36:39], v[64:67], v[120:123]\nv_mfma_f32_16x16x32_bf16 v[104:107], v[36:39], v[48:51], v[104:107]\nv_cvt_pk_bf16_f32 v96, v96, v97\n"
        "v_cvt_pk_bf16_f32 v97, v98, v99\nv_cvt_pk_bf16_f32 v98, v100, v101\nv_cvt_pk_bf16_f32 v99, v102, v103\nv_mfma_f32_16x16x32_bf16 v[108:111], v[36:39], v[52:55], v[108:111]\n"
        "v_permlane16_swap_b32 v96, v98\nv_permlane16_swap_b32 v97, v99\nds_write_b128 %[stb], v[96:99] offset:24032\nv_mfma_f32_16x16x32_bf16 v[112:115], v[36:39], v[56:59], v[112:115]\n"
        "v_cvt_pk_bf16_f32 v116, v116, v117\nv_cvt_pk_bf16_f32 v117, v118, v119\nv_cvt_pk_bf16_f32 v118, v120, v121\nv_cvt_pk_bf16_f32 v119, v122, v123\n"
        "v_mfma_f32_16x16x32_bf16 v[136:139], v[40:43], v[60:63], v[136:139]\nv_permlane16_swap_b32 v116, v118\nv_permlane16_swap_b32 v117, v119\nds_write_b128 %[stb], v[116:119] offset:23936\n"
        "v_cvt_pk_bf16_f32 v104, v104, v105\nv_mfma_f32_16x16x32_bf16 v[140:143], v[40:43], v[64:67], v[140:143]\nv_cvt_pk_bf16_f32 v105, v106, v107\nds_write_b64 %[sta], v[104:105] offset:128\n"
        "v_mfma_f32_16x16x32_bf16 v[156:159], v[44:47], v[60:63], v[156:159]\nv_cvt_pk_bf16_f32 v108, v108, v109\nv_cvt_pk_bf16_f32 v109, v110, v111\nv_cvt_pk_bf16_f32 v110, v112, v113\n"
        "v_cvt_pk_bf16_f32 v111, v114, v115\nv_mfma_f32_16x16x32_bf16 v[160:163], v[44:47], v[64:67], v[160:163]\nv_permlane16_swap_b32 v108, v110\nv_permlane16_swap_b32 v109, v111\n"
        "ds_write_b128 %[stb], v[108:111] offset:8064\nds_write_b32 %[pg], %[sy]\nv_mfma_f32_16x16x32_bf16 v[124:127], v[40:43], v[48:51], v[124:127]\nv_add_u32 %[sy], 4, %[sy]\n"
        "v_cvt_pk_bf16_f32 v136, v136, v137\nv_cvt_pk_bf16_f32 v137, v138, v139\nv_cvt_pk_bf16_f32 v138, v140, v141\nv_mfma_f32_16x16x32_bf16 v[144:147], v[44:47], v[48:51], v[144:147]\n"
        "v_cvt_pk_bf16_f32 v139, v142, v143\ns_nop 0\nv_permlane16_swap_b32 v136, v138\nv_permlane16_swap_b32 v137, v139\nv_mfma_f32_16x16x32_bf16 v[128:131], v[40:43], v[52:55], v[128:131]\n"
        "ds_write_b128 %[stb], v[136:139] offset:24224\nv_cvt_pk_bf16_f32 v156, v156, v157\nv_cvt_pk_bf16_f32 v157, v158, v159\nv_cvt_pk_bf16_f32 v158, v160, v161\n"
        "v_mfma_f32_16x16x32_bf16 v[132:135], v[40:43], v[56:59], v[132:135]\nv_cvt_pk_bf16_f32 v159, v162, v163\ns_nop 0\nv_permlane16_swap_b32 v156, v158\nv_permlane16_swap_b32 v157, v159\n"
        "v_mfma_f32_16x16x32_bf16 v[148:151], v[44:47], v[52:55], v[148:151]\nds_write_b128 %[stb], v[156:159] offset:24256\nv_cvt_pk_bf16_f32 v124, v124, v125\nv_cvt_pk_bf16_f32 v125, v126, v127\n"
        "ds_write_b64 %[sta], v[124:125] offset:416\nv_mfma_f32_16x16x32_bf16 v[152:155], v[44:47], v[56:59], v[152:155]\nv_cvt_pk_bf16_f32 v144, v144, v145\nv_cvt_pk_bf16_f32 v145, v146, v147\n"
        "ds_write_b64 %[sta], v[144:145] offset:448\ns_nop 3\nv_cvt_pk_bf16_f32 v128, v128, v129\nv_cvt_pk_bf16_f32 v129, v130, v131\nv_cvt_pk_bf16_f32 v130, v132, v133\n"
        "v_cvt_pk_bf16_f32 v131, v134, v135\ns_nop 0\nv_permlane16_swap_b32 v128, v130\nv_permlane16_swap_b32 v129, v131\nds_write_b128 %[stb], v[128:131] offset:8352\n"
        "v_cvt_pk_bf16_f32 v148, v148, v149\nv_cvt_pk_bf16_f32 v149, v150, v151\nv_cvt_pk_bf16_f32 v150, v152, v153\nv_cvt_pk_bf16_f32 v151, v154, v155\ns_nop 0\nv_permlane16_swap_b32 v148, v150\n"
        "v_permlane16_swap_b32 v149, v151\nds_write_b128 %[stb], v[148:151] offset:8384\nds_write_b32 %[pg], %[sy]\ns_branch .LE%=\n.LC0t%=:\ns_mov_b32 %[s1], 0x100000\n.LC0q%=:\n"
        "ds_read_b32 %[pv], %[sy] offset:4\ns_waitcnt lgkmcnt(0)\nv_readfirstlane_b32 %[s0], %[pv]\ns_nop 0\ns_cmp_ge_i32 %[s0], %[nst]\ns_cbranch_scc1 .LC0u%=\ns_sleep 1\ns_sub_u32 %[s1], %[s1], 1\n"
        "s_cbranch_scc0 .LC0q%=\ns_branch .LC0u%=\n.LE%=:\n"
        : [pv] "=&v"(pv), [xa] "+v"(xa), [xb] "+v"(xb), [sy] "+v"(sy), [so] "=&s"(so), [s0] "=&s"(s0_), [s1] "=&s"(s1_)
        : [wa] "v"(wa), [rs] "s"(wrsrc), [pg] "v"(pga), [cwa] "v"(cwa), [mra] "v"(mra), [sta] "v"(sta), [stb] "v"(stb), [w] "s"(w), [ow] "i"(SY_W - SY_X), [pfs] "i"(PFS), [pf] "i"(PF), [nst] "i"(NST0)
        : "memory", "scc", "v12", "v13", "v14", "v15", "v16", "v17", "v18", "v19", "v20", "v21", "v22", "v23", "v24", "v25", "v26", "v27", "v28", "v29", "v30", "v31", "v32", "v33", "v34", "v35", "v36", "v37", "v38", "v39", "v40", "v41", "v42", "v43", "v44", "v45", "v46", "v47", "v48", "v49", "v50", "v51", "v52", "v53", "v54", "v55", "v56", "v57", "v58", "v59", "v60", "v61", "v62", "v63", "v64", "v65", "v66", "v67", "v68", "v69", "v70", "v71", "v72", "v73", "v74", "v75", "v76", "v77", "v78", "v79", "v80", "v81", "v82", "v83", "v84", "v85", "v86", "v87", "v88", "v89", "v90", "v91", "v92", "v93", "v94", "v95", "v96", "v97", "v98", "v99", "v100", "v101", "v102", "v103", "v104", "v105", "v106", "v107", "v108", "v109", "v110", "v111", "v112", "v113", "v114", "v115", "v116", "v117", "v118", "v119", "v120", "v121", "v122", "v123", "v124", "v125", "v126", "v127", "v128", "v129", "v130", "v131", "v132", "v133", "v134", "v135", "v136", "v137", "v138", "v139", "v140", "v141", "v142", "v143", "v144", "v145", "v146", "v147", "v148", "v149", "v150", "v151", "v152", "v153", "v154", "v155", "v156", "v157", "v158", "v159", "v160", "v161", "v162", "v163", "v164", "v165", "v166", "v167");
    } else {
      asm volatile(
        "s_cmp_lt_u32 %[w], 2\ns_cbranch_scc0 .LB%=\ns_mov_b32 %[so], 0\nbuffer_load_dwordx4 v[32:35], %[wa], %[rs], %[so] offen offset:0 sc1\n"
        "buffer_load_dwordx4 v[36:39], %[wa], %[rs], %[so] offen offset:1024 sc1\nbuffer_load_dwordx4 v[40:43], %[wa], %[rs], %[so] offen offset:2048 sc1\n"
        "buffer_load_dwordx4 v[44:47], %[wa], %[rs], %[so] offen offset:3072 sc1\ns_mov_b32 %[so], 15360\nbuffer_load_dwordx4 v[48:51], %[wa], %[rs], %[so] offen offset:0 sc1\n"
        "buffer_load_dwordx4 v[52:55], %[wa], %[rs], %[so] offen offset:1024 sc1\nbuffer_load_dwordx4 v[56:59], %[wa], %[rs], %[so] offen offset:2048 sc1\n"
        "buffer_load_dwordx4 v[60:63], %[wa], %[rs], %[so] offen offset:3072 sc1\ns_mov_b32 %[so], 30720\nbuffer_load_dwordx4 v[64:67], %[wa], %[rs], %[so] offen offset:0 sc1\n"
        "buffer_load_dwordx4 v[68:71], %[wa], %[rs], %[so] offen offset:1024 sc1\nbuffer_load_dwordx4 v[72:75], %[wa], %[rs], %[so] offen offset:2048 sc1\n"
        "buffer_load_dwordx4 v[76:79], %[wa], %[rs], %[so] offen offset:3072 sc1\ns_mov_b32 %[so], 46080\nbuffer_load_dwordx4 v[80:83], %[wa], %[rs], %[so] offen offset:0 sc1\n"
        "buffer_load_dwordx4 v[84:87], %[wa], %[rs], %[so] offen offset:1024 sc1\nbuffer_load_dwordx4 v[88:91], %[wa], %[rs], %[so] offen offset:2048 sc1\n"
        "buffer_load_dwordx4 v[92:95], %[wa], %[rs], %[so] offen offset:3072 sc1\ns_mov_b32 %[s1], 0x100000\n.LA1w%=:\nds_read_b32 %[pv], %[sy] offset:0\ns_waitcnt lgkmcnt(0)\n"
        "v_readfirstlane_b32 %[s0], %[pv]\ns_nop 0\ns_cmp_ge_i32 %[s0], 6\ns_cbranch_scc1 .LA1g%=\ns_sleep 1\ns_sub_u32 %[s1], %[s1], 1\ns_cbranch_scc0 .LA1w%=\n.LA1g%=:\n"
        "ds_read_b64_tr_b16 v[16:17], %[xa] offset:1056\nds_read_b64_tr_b16 v[18:19], %[xa] offset:1568\nds_read_b64_tr_b16 v[20:21], %[xa] offset:2112\nds_read_b64_tr_b16 v[22:23], %[xa] offset:2624\n"
        "ds_read_b64_tr_b16 v[12:13], %[xa] offset:0\nds_read_b64_tr_b16 v[14:15], %[xa] offset:512\nds_read_b64_tr_b16 v[24:25], %[xa] offset:3168\nds_read_b64_tr_b16 v[26:27], %[xa] offset:3680\n"
        "s_waitcnt vmcnt(12) lgkmcnt(6)\nv_mfma_f32_16x16x32_bf16 v[100:103], v[32:35], v[16:19], 0\nds_read_b64_tr_b16 v[28:29], %[xa] offset:4224\n"
        "v_mfma_f32_16x16x32_bf16 v[116:119], v[36:39], v[16:19], 0\nds_read_b64_tr_b16 v[30:31], %[xa] offset:4736\nv_mfma_f32_16x16x32_bf16 v[136:139], v[40:43], v[16:19], 0\n"
        "ds_read_b32 %[pv], %[sy] offset:4\nv_mfma_f32_16x16x32_bf16 v[152:155], v[44:47], v[16:19], 0\nds_read_b64_tr_b16 v[16:17], %[xa] offset:6336\ns_waitcnt lgkmcnt(8)\n"
        "v_mfma_f32_16x16x32_bf16 v[104:107], v[32:35], v[20:23], 0\nds_read_b64_tr_b16 v[18:19], %[xa] offset:6848\nv_mfma_f32_16x16x32_bf16 v[120:123], v[36:39], v[20:23], 0\n"
        "v_mfma_f32_16x16x32_bf16 v[140:143], v[40:43], v[20:23], 0\nv_mfma_f32_16x16x32_bf16 v[156:159], v[44:47], v[20:23], 0\nds_read_b64_tr_b16 v[20:21], %[xa] offset:7392\ns_waitcnt lgkmcnt(8)\n"
        "v_mfma_f32_16x16x32_bf16 v[96:99], v[32:35], v[12:15], 0\nds_read_b64_tr_b16 v[22:23], %[xa] offset:7904\nv_mfma_f32_16x16x32_bf16 v[132:135], v[40:43], v[12:15], 0\n"
        "ds_read_b64_tr_b16 v[12:13], %[xa] offset:5280\ns_waitcnt lgkmcnt(8)\nv_mfma_f32_16x16x32_bf16 v[108:111], v[32:35], v[24:27], 0\nds_read_b64_tr_b16 v[14:15], %[xa] offset:5792\n"
        "v_mfma_f32_16x16x32_bf16 v[124:127], v[36:39], v[24:27], 0\nv_mfma_f32_16x16x32_bf16 v[144:147], v[40:43], v[24:27], 0\nv_mfma_f32_16x16x32_bf16 v[160:163], v[44:47], v[24:27], 0\n"
        "ds_read_b64_tr_b16 v[24:25], %[xa] offset:8448\ns_waitcnt lgkmcnt(8)\nv_mfma_f32_16x16x32_bf16 v[112:115], v[32:35], v[28:31], 0\nds_read_b64_tr_b16 v[26:27], %[xa] offset:8960\n"
        "v_mfma_f32_16x16x32_bf16 v[128:131], v[36:39], v[28:31], 0\nv_mfma_f32_16x16x32_bf16 v[148:151], v[40:43], v[28:31], 0\nv_mfma_f32_16x16x32_bf16 v[164:167], v[44:47], v[28:31], 0\n.LA1l%=:\n"
        "s_waitcnt vmcnt(8) lgkmcnt(6)\nv_mfma_f32_16x16x32_bf16 v[100:103], v[48:51], v[16:19], v[100:103]\ns_addk_i32 %[so], 15360\nv_readfirstlane_b32 %[s0], %[pv]\n"
        "v_mfma_f32_16x16x32_bf16 v[116:119], v[52:55], v[16:19], v[116:119]\ns_cmp_ge_i32 %[s0], 6\nds_read_b64_tr_b16 v[28:29], %[xa] offset:9504\n"
        "v_mfma_f32_16x16x32_bf16 v[136:139], v[56:59], v[16:19], v[136:139]\ns_cbranch_scc0 .LA1s0_%=\n.LA1r0_%=:\nds_read_b64_tr_b16 v[30:31], %[xa] offset:10016\n"
        "v_mfma_f32_16x16x32_bf16 v[152:155], v[60:63], v[16:19], v[152:155]\nds_read_b64_tr_b16 v[16:17], %[xa] offset:11616\ns_waitcnt lgkmcnt(7)\n"
        "v_mfma_f32_16x16x32_bf16 v[104:107], v[48:51], v[20:23], v[104:107]\nds_read_b64_tr_b16 v[18:19], %[xa] offset:12128\nbuffer_load_dwordx4 v[32:35], %[wa], %[rs], %[so] offen offset:0 sc1\n"
        "v_mfma_f32_16x16x32_bf16 v[120:123], v[52:55], v[20:23], v[120:123]\nbuffer_load_dwordx4 v[36:39], %[wa], %[rs], %[so] offen offset:1024 sc1\n"
        "v_mfma_f32_16x16x32_bf16 v[140:143], v[56:59], v[20:23], v[140:143]\nv_mfma_f32_16x16x32_bf16 v[156:159], v[60:63], v[20:23], v[156:159]\nds_read_b64_tr_b16 v[20:21], %[xa] offset:12672\n"
        "s_waitcnt lgkmcnt(7)\nv_mfma_f32_16x16x32_bf16 v[96:99], v[48:51], v[12:15], v[96:99]\nds_read_b64_tr_b16 v[22:23], %[xa] offset:13184\n"
        "v_mfma_f32_16x16x32_bf16 v[132:135], v[56:59], v[12:15], v[132:135]\nds_read_b64_tr_b16 v[12:13], %[xa] offset:10560\ns_waitcnt lgkmcnt(7)\n"
        "v_mfma_f32_16x16x32_bf16 v[108:111], v[48:51], v[24:27], v[108:111]\nds_read_b64_tr_b16 v[14:15], %[xa] offset:11072\nv_mfma_f32_16x16x32_bf16 v[124:127], v[52:55], v[24:27], v[124:127]\n"
        "buffer_load_dwordx4 v[40:43], %[wa], %[rs], %[so] offen offset:2048 sc1\nv_mfma_f32_16x16x32_bf16 v[144:147], v[56:59], v[24:27], v[144:147]\n"
        "buffer_load_dwordx4 v[44:47], %[wa], %[rs], %[so] offen offset:3072 sc1\nv_mfma_f32_16x16x32_bf16 v[160:163], v[60:63], v[24:27], v[160:163]\nds_read_b64_tr_b16 v[24:25], %[xa] offset:13728\n"
        "s_waitcnt lgkmcnt(7)\nv_mfma_f32_16x16x32_bf16 v[112:115], v[48:51], v[28:31], v[112:115]\nds_read_b64_tr_b16 v[26:27], %[xa] offset:14240\n"
        "v_mfma_f32_16x16x32_bf16 v[128:131], v[52:55], v[28:31], v[128:131]\nv_add_u32 %[sy], 4, %[sy]\nv_mfma_f32_16x16x32_bf16 v[148:151], v[56:59], v[28:31], v[148:151]\nds_write_b32 %[pg], %[sy]\n"
        "v_mfma_f32_16x16x32_bf16 v[164:167], v[60:63], v[28:31], v[164:167]\ns_waitcnt vmcnt(8) lgkmcnt(7)\nv_mfma_f32_16x16x32_bf16 v[100:103], v[64:67], v[16:19], v[100:103]\n"
        "s_addk_i32 %[so], 15360\nds_read_b64_tr_b16 v[28:29], %[xa] offset:14784\nv_mfma_f32_16x16x32_bf16 v[116:119], v[68:71], v[16:19], v[116:119]\nds_read_b64_tr_b16 v[30:31], %[xa] offset:15296\n"
        "v_mfma_f32_16x16x32_bf16 v[136:139], v[72:75], v[16:19], v[136:139]\nds_read_b32 %[pv], %[sy] offset:4\nbuffer_load_dwordx4 v[48:51], %[wa], %[rs], %[so] offen offset:0 sc1\n"
        "v_mfma_f32_16x16x32_bf16 v[152:155], v[76:79], v[16:19], v[152:155]\nds_read_b64_tr_b16 v[16:17], %[xa] offset:16896\ns_waitcnt lgkmcnt(9)\n"
        "v_mfma_f32_16x16x32_bf16 v[104:107], v[64:67], v[20:23], v[104:107]\nds_read_b64_tr_b16 v[18:19], %[xa] offset:17408\nv_mfma_f32_16x16x32_bf16 v[120:123], v[68:71], v[20:23], v[120:123]\n"
        "buffer_load_dwordx4 v[52:55], %[wa], %[rs], %[so] offen offset:1024 sc1\nv_mfma_f32_16x16x32_bf16 v[140:143], v[72:75], v[20:23], v[140:143]\n"
        "v_mfma_f32_16x16x32_bf16 v[156:159], v[76:79], v[20:23], v[156:159]\nds_read_b64_tr_b16 v[20:21], %[xa] offset:17952\ns_waitcnt lgkmcnt(9)\n"
        "v_mfma_f32_16x16x32_bf16 v[96:99], v[64:67], v[12:15], v[96:99]\nds_read_b64_tr_b16 v[22:23], %[xa] offset:18464\nv_mfma_f32_16x16x32_bf16 v[132:135], v[72:75], v[12:15], v[132:135]\n"
        "ds_read_b64_tr_b16 v[12:13], %[xa] offset:15840\ns_waitcnt lgkmcnt(9)\nv_mfma_f32_16x16x32_bf16 v[108:111], v[64:67], v[24:27], v[108:111]\nds_read_b64_tr_b16 v[14:15], %[xa] offset:16352\n"
        "v_mfma_f32_16x16x32_bf16 v[124:127], v[68:71], v[24:27], v[124:127]\nbuffer_load_dwordx4 v[56:59], %[wa], %[rs], %[so] offen offset:2048 sc1\n"
        "v_mfma_f32_16x16x32_bf16 v[144:147], v[72:75], v[24:27], v[144:147]\nbuffer_load_dwordx4 v[60:63], %[wa], %[rs], %[so] offen offset:3072 sc1\n"
        "v_mfma_f32_16x16x32_bf16 v[160:163], v[76:79], v[24:27], v[160:163]\nds_read_b64_tr_b16 v[24:25], %[xa] offset:19008\ns_waitcnt lgkmcnt(8)\n"
        "v_mfma_f32_16x16x32_bf16 v[112:115], v[64:67], v[28:31], v[112:115]\nds_read_b64_tr_b16 v[26:27], %[xa] offset:19520\nv_mfma_f32_16x16x32_bf16 v[128:131], v[68:71], v[28:31], v[128:131]\n"
        "v_mfma_f32_16x16x32_bf16 v[148:151], v[72:75], v[28:31], v[148:151]\nv_mfma_f32_16x16x32_bf16 v[164:167], v[76:79], v[28:31], v[164:167]\ns_waitcnt vmcnt(8) lgkmcnt(6)\n"
        "v_mfma_f32_16x16x32_bf16 v[100:103], v[80:83], v[16:19], v[100:103]\ns_addk_i32 %[so], 15360\nv_readfirstlane_b32 %[s0], %[pv]\n"
        "v_mfma_f32_16x16x32_bf16 v[116:119], v[84:87], v[16:19], v[116:119]\ns_cmp_ge_i32 %[s0], 6\nds_read_b64_tr_b16 v[28:29], %[xa] offset:20064\n"
        "v_mfma_f32_16x16x32_bf16 v[136:139], v[88:91], v[16:19], v[136:139]\ns_cbranch_scc0 .LA1s1_%=\n.LA1r1_%=:\nds_read_b64_tr_b16 v[30:31], %[xa] offset:20576\n"
        "v_mfma_f32_16x16x32_bf16 v[152:155], v[92:95], v[16:19], v[152:155]\nds_read_b64_tr_b16 v[16:17], %[xa] offset:22176\ns_waitcnt lgkmcnt(7)\n"
        "v_mfma_f32_16x16x32_bf16 v[104:107], v[80:83], v[20:23], v[104:107]\nds_read_b64_tr_b16 v[18:19], %[xa] offset:22688\nbuffer_load_dwordx4 v[64:67], %[wa], %[rs], %[so] offen offset:0 sc1\n"
        "v_mfma_f32_16x16x32_bf16 v[120:123], v[84:87], v[20:23], v[120:123]\nbuffer_load_dwordx4 v[68:71], %[wa], %[rs], %[so] offen offset:1024 sc1\n"
        "v_mfma_f32_16x16x32_bf16 v[140:143], v[88:91], v[20:23], v[140:143]\nv_mfma_f32_16x16x32_bf16 v[156:159], v[92:95], v[20:23], v[156:159]\nds_read_b64_tr_b16 v[20:21], %[xa] offset:23232\n"
        "s_waitcnt lgkmcnt(7)\nv_mfma_f32_16x16x32_bf16 v[96:99], v[80:83], v[12:15], v[96:99]\nds_read_b64_tr_b16 v[22:23], %[xa] offset:23744\n"
        "v_mfma_f32_16x16x32_bf16 v[132:135], v[88:91], v[12:15], v[132:135]\nds_read_b64_tr_b16 v[12:13], %[xa] offset:21120\ns_waitcnt lgkmcnt(7)\n"
        "v_mfma_f32_16x16x32_bf16 v[108:111], v[80:83], v[24:27], v[108:111]\nds_read_b64_tr_b16 v[14:15], %[xa] offset:21632\nv_mfma_f32_16x16x32_bf16 v[124:127], v[84:87], v[24:27], v[124:127]\n"
        "buffer_load_dwordx4 v[72:75], %[wa], %[rs], %[so] offen offset:2048 sc1\nv_mfma_f32_16x16x32_bf16 v[144:147], v[88:91], v[24:27], v[144:147]\n"
        "buffer_load_dwordx4 v[76:79], %[wa], %[rs], %[so] offen offset:3072 sc1\nv_mfma_f32_16x16x32_bf16 v[160:163], v[92:95], v[24:27], v[160:163]\nds_read_b64_tr_b16 v[24:25], %[xa] offset:24288\n"
        "s_waitcnt lgkmcnt(7)\nv_mfma_f32_16x16x32_bf16 v[112:115], v[80:83], v[28:31], v[112:115]\nds_read_b64_tr_b16 v[26:27], %[xa] offset:24800\n"
        "v_mfma_f32_16x16x32_bf16 v[128:131], v[84:87], v[28:31], v[128:131]\nv_add_u32 %[sy], 4, %[sy]\nv_mfma_f32_16x16x32_bf16 v[148:151], v[88:91], v[28:31], v[148:151]\nds_write_b32 %[pg], %[sy]\n"
        "v_mfma_f32_16x16x32_bf16 v[164:167], v[92:95], v[28:31], v[164:167]\ns_waitcnt vmcnt(8) lgkmcnt(7)\nv_mfma_f32_16x16x32_bf16 v[100:103], v[32:35], v[16:19], v[100:103]\n"
        "s_addk_i32 %[so], 15360\nds_read_b64_tr_b16 v[28:29], %[xa] offset:25344\nv_mfma_f32_16x16x32_bf16 v[116:119], v[36:39], v[16:19], v[116:119]\nds_read_b64_tr_b16 v[30:31], %[xa] offset:25856\n"
        "v_mfma_f32_16x16x32_bf16 v[136:139], v[40:43], v[16:19], v[136:139]\nds_read_b32 %[pv], %[sy] offset:4\nbuffer_load_dwordx4 v[80:83], %[wa], %[rs], %[so] offen offset:0 sc1\n"
        "v_mfma_f32_16x16x32_bf16 v[152:155], v[44:47], v[16:19], v[152:155]\nds_read_b64_tr_b16 v[16:17], %[xa] offset:27456\ns_waitcnt lgkmcnt(9)\n"
        "v_mfma_f32_16x16x32_bf16 v[104:107], v[32:35], v[20:23], v[104:107]\nds_read_b64_tr_b16 v[18:19], %[xa] offset:27968\nv_mfma_f32_16x16x32_bf16 v[120:123], v[36:39], v[20:23], v[120:123]\n"
        "buffer_load_dwordx4 v[84:87], %[wa], %[rs], %[so] offen offset:1024 sc1\nv_mfma_f32_16x16x32_bf16 v[140:143], v[40:43], v[20:23], v[140:143]\n"
        "v_mfma_f32_16x16x32_bf16 v[156:159], v[44:47], v[20:23], v[156:159]\nds_read_b64_tr_b16 v[20:21], %[xa] offset:28512\ns_waitcnt lgkmcnt(9)\n"
        "v_mfma_f32_16x16x32_bf16 v[96:99], v[32:35], v[12:15], v[96:99]\nds_read_b64_tr_b16 v[22:23], %[xa] offset:29024\nv_mfma_f32_16x16x32_bf16 v[132:135], v[40:43], v[12:15], v[132:135]\n"
        "ds_read_b64_tr_b16 v[12:13], %[xa] offset:26400\ns_waitcnt lgkmcnt(9)\nv_mfma_f32_16x16x32_bf16 v[108:111], v[32:35], v[24:27], v[108:111]\nds_read_b64_tr_b16 v[14:15], %[xa] offset:26912\n"
        "v_mfma_f32_16x16x32_bf16 v[124:127], v[36:39], v[24:27], v[124:127]\nbuffer_load_dwordx4 v[88:91], %[wa], %[rs], %[so] offen offset:2048 sc1\n"
        "v_mfma_f32_16x16x32_bf16 v[144:147], v[40:43], v[24:27], v[144:147]\nbuffer_load_dwordx4 v[92:95], %[wa], %[rs], %[so] offen offset:3072 sc1\n"
        "v_mfma_f32_16x16x32_bf16 v[160:163], v[44:47], v[24:27], v[160:163]\nds_read_b64_tr_b16 v[24:25], %[xa] offset:29568\ns_waitcnt lgkmcnt(8)\n"
        "v_mfma_f32_16x16x32_bf16 v[112:115], v[32:35], v[28:31], v[112:115]\nds_read_b64_tr_b16 v[26:27], %[xa] offset:30080\nv_mfma_f32_16x16x32_bf16 v[128:131], v[36:39], v[28:31], v[128:131]\n"
        "v_mfma_f32_16x16x32_bf16 v[148:151], v[40:43], v[28:31], v[148:151]\ns_cmp_lg_u32 %[so], 291840\nv_mfma_f32_16x16x32_bf16 v[164:167], v[44:47], v[28:31], v[164:167]\n"
        "v_add_u32 %[xa], 21120, %[xa]\ns_cbranch_scc1 .LA1l%=\ns_waitcnt vmcnt(8) lgkmcnt(6)\nv_mfma_f32_16x16x32_bf16 v[100:103], v[48:51], v[16:19], v[100:103]\nv_readfirstlane_b32 %[s0], %[pv]\n"
        "ds_read_b64_tr_b16 v[28:29], %[xa] offset:9504\nv_mfma_f32_16x16x32_bf16 v[116:119], v[52:55], v[16:19], v[116:119]\ns_cmp_ge_i32 %[s0], 6\nds_read_b64_tr_b16 v[30:31], %[xa] offset:10016\n"
        "v_mfma_f32_16x16x32_bf16 v[136:139], v[56:59], v[16:19], v[136:139]\ns_cbranch_scc0 .LA1s2_%=\n.LA1r2_%=:\nv_mfma_f32_16x16x32_bf16 v[152:155], v[60:63], v[16:19], v[152:155]\n"
        "ds_read_b64_tr_b16 v[16:17], %[xa] offset:11616\ns_waitcnt lgkmcnt(7)\nv_mfma_f32_16x16x32_bf16 v[104:107], v[48:51], v[20:23], v[104:107]\nds_read_b64_tr_b16 v[18:19], %[xa] offset:12128\n"
        "v_mfma_f32_16x16x32_bf16 v[120:123], v[52:55], v[20:23], v[120:123]\nv_mfma_f32_16x16x32_bf16 v[140:143], v[56:59], v[20:23], v[140:143]\n"
        "v_mfma_f32_16x16x32_bf16 v[156:159], v[60:63], v[20:23], v[156:159]\nds_read_b64_tr_b16 v[20:21], %[xa] offset:12672\ns_waitcnt lgkmcnt(7)\n"
        "v_mfma_f32_16x16x32_bf16 v[96:99], v[48:51], v[12:15], v[96:99]\nds_read_b64_tr_b16 v[22:23], %[xa] offset:13184\nv_mfma_f32_16x16x32_bf16 v[132:135], v[56:59], v[12:15], v[132:135]\n"
        "ds_read_b64_tr_b16 v[12:13], %[xa] offset:10560\ns_waitcnt lgkmcnt(7)\nv_mfma_f32_16x16x32_bf16 v[108:111], v[48:51], v[24:27], v[108:111]\nds_read_b64_tr_b16 v[14:15], %[xa] offset:11072\n"
        "v_mfma_f32_16x16x32_bf16 v[124:127], v[52:55], v[24:27], v[124:127]\nv_mfma_f32_16x16x32_bf16 v[144:147], v[56:59], v[24:27], v[144:147]\n"
        "v_mfma_f32_16x16x32_bf16 v[160:163], v[60:63], v[24:27], v[160:163]\nds_read_b64_tr_b16 v[24:25], %[xa] offset:13728\ns_waitcnt lgkmcnt(7)\n"
        "v_mfma_f32_16x16x32_bf16 v[112:115], v[48:51], v[28:31], v[112:115]\nds_read_b64_tr_b16 v[26:27], %[xa] offset:14240\nv_mfma_f32_16x16x32_bf16 v[128:131], v[52:55], v[28:31], v[128:131]\n"
        "v_add_u32 %[sy], 4, %[sy]\nv_mfma_f32_16x16x32_bf16 v[148:151], v[56:59], v[28:31], v[148:151]\nv_mfma_f32_16x16x32_bf16 v[164:167], v[60:63], v[28:31], v[164:167]\n"
        "s_waitcnt vmcnt(4) lgkmcnt(6)\nv_mfma_f32_16x16x32_bf16 v[100:103], v[64:67], v[16:19], v[100:103]\nds_read_b64_tr_b16 v[28:29], %[xa] offset:14784\n"
        "v_mfma_f32_16x16x32_bf16 v[116:119], v[68:71], v[16:19], v[116:119]\nds_read_b64_tr_b16 v[30:31], %[xa] offset:15296\nv_mfma_f32_16x16x32_bf16 v[136:139], v[72:75], v[16:19], v[136:139]\n"
        "ds_read_b32 %[pv], %[sy] offset:4\nv_mfma_f32_16x16x32_bf16 v[152:155], v[76:79], v[16:19], v[152:155]\nds_read_b64_tr_b16 v[16:17], %[xa] offset:16896\ns_waitcnt lgkmcnt(8)\n"
        "v_mfma_f32_16x16x32_bf16 v[104:107], v[64:67], v[20:23], v[104:107]\nds_read_b64_tr_b16 v[18:19], %[xa] offset:17408\nv_mfma_f32_16x16x32_bf16 v[120:123], v[68:71], v[20:23], v[120:123]\n"
        "v_mfma_f32_16x16x32_bf16 v[140:143], v[72:75], v[20:23], v[140:143]\nv_mfma_f32_16x16x32_bf16 v[156:159], v[76:79], v[20:23], v[156:159]\nds_read_b64_tr_b16 v[20:21], %[xa] offset:17952\n"
        "s_waitcnt lgkmcnt(8)\nv_mfma_f32_16x16x32_bf16 v[96:99], v[64:67], v[12:15], v[96:99]\nds_read_b64_tr_b16 v[22:23], %[xa] offset:18464\n"
        "v_mfma_f32_16x16x32_bf16 v[132:135], v[72:75], v[12:15], v[132:135]\nds_read_b64_tr_b16 v[12:13], %[xa] offset:15840\ns_waitcnt lgkmcnt(8)\n"
        "v_mfma_f32_16x16x32_bf16 v[108:111], v[64:67], v[24:27], v[108:111]\nds_read_b64_tr_b16 v[14:15], %[xa] offset:16352\nv_mfma_f32_16x16x32_bf16 v[124:127], v[68:71], v[24:27], v[124:127]\n"
        "v_mfma_f32_16x16x32_bf16 v[144:147], v[72:75], v[24:27], v[144:147]\nv_mfma_f32_16x16x32_bf16 v[160:163], v[76:79], v[24:27], v[160:163]\nds_read_b64_tr_b16 v[24:25], %[xa] offset:19008\n"
        "s_waitcnt lgkmcnt(8)\nv_mfma_f32_16x16x32_bf16 v[112:115], v[64:67], v[28:31], v[112:115]\nds_read_b64_tr_b16 v[26:27], %[xa] offset:19520\n"
        "v_mfma_f32_16x16x32_bf16 v[128:131], v[68:71], v[28:31], v[128:131]\nv_mfma_f32_16x16x32_bf16 v[148:151], v[72:75], v[28:31], v[148:151]\n"
        "v_mfma_f32_16x16x32_bf16 v[164:167], v[76:79], v[28:31], v[164:167]\ns_waitcnt vmcnt(0) lgkmcnt(6)\nv_mfma_f32_16x16x32_bf16 v[100:103], v[80:83], v[16:19], v[100:103]\n"
        "v_readfirstlane_b32 %[s0], %[pv]\nds_read_b64_tr_b16 v[28:29], %[xa] offset:20064\nv_mfma_f32_16x16x32_bf16 v[116:119], v[84:87], v[16:19], v[116:119]\n"
        "ds_read_b64_tr_b16 v[30:31], %[xa] offset:20576\nv_mfma_f32_16x16x32_bf16 v[136:139], v[88:91], v[16:19], v[136:139]\ns_cmp_ge_i32 %[s0], %[nst]\n"
        "v_mfma_f32_16x16x32_bf16 v[152:155], v[92:95], v[16:19], v[152:155]\ns_cbranch_scc0 .LA1t%=\n.LA1u%=:\ns_waitcnt lgkmcnt(6)\n"
        "v_mfma_f32_16x16x32_bf16 v[104:107], v[80:83], v[20:23], v[104:107]\nv_add_u32 %[sy], 4, %[sy]\nv_mfma_f32_16x16x32_bf16 v[120:123], v[84:87], v[20:23], v[120:123]\n"
        "ds_read_b64 v[32:33], %[cwa] offset:0\nv_mfma_f32_16x16x32_bf16 v[140:143], v[88:91], v[20:23], v[140:143]\nds_read_b64 v[36:37], %[cwa] offset:640\n"
        "v_mfma_f32_16x16x32_bf16 v[156:159], v[92:95], v[20:23], v[156:159]\nds_read_b64 v[40:41], %[cwa] offset:1280\ns_waitcnt lgkmcnt(7)\n"
        "v_mfma_f32_16x16x32_bf16 v[96:99], v[80:83], v[12:15], v[96:99]\nds_read_b64 v[44:45], %[cwa] offset:1024\nv_mfma_f32_16x16x32_bf16 v[132:135], v[88:91], v[12:15], v[132:135]\n"
        "ds_read_b128 v[48:51], %[mra] offset:0\ns_waitcnt lgkmcnt(7)\nv_mfma_f32_16x16x32_bf16 v[108:111], v[80:83], v[24:27], v[108:111]\nds_read_b128 v[52:55], %[mra] offset:256\n"
        "v_mfma_f32_16x16x32_bf16 v[124:127], v[84:87], v[24:27], v[124:127]\nds_read_b128 v[56:59], %[mra] offset:512\nv_mfma_f32_16x16x32_bf16 v[144:147], v[88:91], v[24:27], v[144:147]\n"
        "ds_read_b128 v[60:63], %[mra] offset:768\nv_mfma_f32_16x16x32_bf16 v[160:163], v[92:95], v[24:27], v[160:163]\nds_read_b128 v[64:67], %[mra] offset:1024\ns_waitcnt lgkmcnt(9)\n"
        "v_mfma_f32_16x16x32_bf16 v[112:115], v[80:83], v[28:31], v[112:115]\nv_add_u32 %[sy], 4, %[sy]\nv_mfma_f32_16x16x32_bf16 v[128:131], v[84:87], v[28:31], v[128:131]\ns_setprio %[pfs]\n"
        "v_mfma_f32_16x16x32_bf16 v[148:151], v[88:91], v[28:31], v[148:151]\nv_mfma_f32_16x16x32_bf16 v[164:167], v[92:95], v[28:31], v[164:167]\ns_waitcnt lgkmcnt(0)\n"
        "v_mfma_f32_16x16x32_bf16 v[100:103], v[32:35], v[52:55], v[100:103]\nv_mfma_f32_16x16x32_bf16 v[104:107], v[32:35], v[56:59], v[104:107]\n"
        "v_mfma_f32_16x16x32_bf16 v[116:119], v[36:39], v[52:55], v[116:119]\nv_mfma_f32_16x16x32_bf16 v[120:123], v[36:39], v[56:59], v[120:123]\n"
        "v_mfma_f32_16x16x32_bf16 v[96:99], v[32:35], v[48:51], v[96:99]\nv_cvt_pk_bf16_f32 v100, v100, v101\nv_cvt_pk_bf16_f32 v101, v102, v103\nv_cvt_pk_bf16_f32 v102, v104, v105\n"
        "v_cvt_pk_bf16_f32 v103, v106, v107\nv_mfma_f32_16x16x32_bf16 v[108:111], v[32:35], v[60:63], v[108:111]\nv_permlane16_swap_b32 v100, v102\nv_permlane16_swap_b32 v101, v103\n"
        "ds_write_b128 %[stb], v[100:103] offset:7936\nv_mfma_f32_16x16x32_bf16 v[112:115], v[32:35], v[64:67], v[112:115]\nv_cvt_pk_bf16_f32 v116, v116, v117\nv_cvt_pk_bf16_f32 v117, v118, v119\n"
        "v_cvt_pk_bf16_f32 v118, v120, v121\nv_cvt_pk_bf16_f32 v119, v122, v123\nv_mfma_f32_16x16x32_bf16 v[124:127], v[36:39], v[60:63], v[124:127]\nv_permlane16_swap_b32 v116, v118\n"
        "v_permlane16_swap_b32 v117, v119\nds_write_b128 %[stb], v[116:119] offset:8096\nv_cvt_pk_bf16_f32 v96, v96, v97\nv_mfma_f32_16x16x32_bf16 v[128:131], v[36:39], v[64:67], v[128:131]\n"
        "v_cvt_pk_bf16_f32 v97, v98, v99\nds_write_b64 %[sta], v[96:97] offset:0\nv_mfma_f32_16x16x32_bf16 v[136:139], v[40:43], v[52:55], v[136:139]\nv_cvt_pk_bf16_f32 v108, v108, v109\n"
        "v_cvt_pk_bf16_f32 v109, v110, v111\nv_cvt_pk_bf16_f32 v110, v112, v113\nv_cvt_pk_bf16_f32 v111, v114, v115\nv_mfma_f32_16x16x32_bf16 v[140:143], v[40:43], v[56:59], v[140:143]\n"
        "v_permlane16_swap_b32 v108, v110\nv_permlane16_swap_b32 v109, v111\nds_write_b128 %[stb], v[108:111] offset:23808\nv_mfma_f32_16x16x32_bf16 v[152:155], v[44:47], v[52:55], v[152:155]\n"
        "v_cvt_pk_bf16_f32 v124, v124, v125\nv_cvt_pk_bf16_f32 v125, v126, v127\nv_cvt_pk_bf16_f32 v126, v128, v129\nv_cvt_pk_bf16_f32 v127, v130, v131\n"
        "v_mfma_f32_16x16x32_bf16 v[156:159], v[44:47], v[56:59], v[156:159]\nv_permlane16_swap_b32 v124, v126\nv_permlane16_swap_b32 v125, v127\nds_write_b128 %[stb], v[124:127] offset:23968\n"
        "ds_write_b32 %[pg], %[sy]\nv_mfma_f32_16x16x32_bf16 v[132:135], v[40:43], v[48:51], v[132:135]\nv_add_u32 %[sy], 4, %[sy]\nv_cvt_pk_bf16_f32 v136, v136, v137\n"
        "v_cvt_pk_bf16_f32 v137, v138, v139\nv_cvt_pk_bf16_f32 v138, v140, v141\nv_mfma_f32_16x16x32_bf16 v[144:147], v[40:43], v[60:63], v[144:147]\nv_cvt_pk_bf16_f32 v139, v142, v143\ns_nop 0\n"
        "v_permlane16_swap_b32 v136, v138\nv_permlane16_swap_b32 v137, v139\nv_mfma_f32_16x16x32_bf16 v[148:151], v[40:43], v[64:67], v[148:151]\nds_write_b128 %[stb], v[136:139] offset:8256\n"
        "v_cvt_pk_bf16_f32 v152, v152, v153\nv_cvt_pk_bf16_f32 v153, v154, v155\nv_cvt_pk_bf16_f32 v154, v156, v157\nv_mfma_f32_16x16x32_bf16 v[160:163], v[44:47], v[60:63], v[160:163]\n"
        "v_cvt_pk_bf16_f32 v155, v158, v159\ns_nop 0\nv_permlane16_swap_b32 v152, v154\nv_permlane16_swap_b32 v153, v155\nv_mfma_f32_16x16x32_bf16 v[164:167], v[44:47], v[64:67], v[164:167]\n"
        "ds_write_b128 %[stb], v[152:155] offset:8192\nv_cvt_pk_bf16_f32 v132, v132, v133\nv_cvt_pk_bf16_f32 v133, v134, v135\nds_write_b64 %[sta], v[132:133] offset:320\ns_nop 3\n"
        "v_cvt_pk_bf16_f32 v144, v144, v145\nv_cvt_pk_bf16_f32 v145, v146, v147\nv_cvt_pk_bf16_f32 v146, v148, v149\nv_cvt_pk_bf16_f32 v147, v150, v151\ns_nop 0\nv_permlane16_swap_b32 v144, v146\n"
        "v_permlane16_swap_b32 v145, v147\nds_write_b128 %[stb], v[144:147] offset:24128\nv_cvt_pk_bf16_f32 v160, v160, v161\nv_cvt_pk_bf16_f32 v161, v162, v163\nv_cvt_pk_bf16_f32 v162, v164, v165\n"
        "v_cvt_pk_bf16_f32 v163, v166, v167\ns_nop 0\nv_permlane16_swap_b32 v160, v162\nv_permlane16_swap_b32 v161, v163\nds_write_b128 %[stb], v[160:163] offset:24064\nds_write_b32 %[pg], %[sy]\n"
        "s_branch .LE%=\n.LA1s0_%=:\ns_mov_b32 %[s1], 0x100000\n.LA1p0_%=:\nds_read_b32 %[pv], %[sy] offset:4\ns_waitcnt lgkmcnt(0)\nv_readfirstlane_b32 %[s0], %[pv]\ns_nop 0\ns_cmp_ge_i32 %[s0], 6\n"
        "s_cbranch_scc1 .LA1r0_%=\ns_sleep 1\ns_sub_u32 %[s1], %[s1], 1\ns_cbranch_scc0 .LA1p0_%=\ns_branch .LA1r0_%=\n.LA1s1_%=:\ns_mov_b32 %[s1], 0x100000\n.LA1p1_%=:\n"
        "ds_read_b32 %[pv], %[sy] offset:4\ns_waitcnt lgkmcnt(0)\nv_readfirstlane_b32 %[s0], %[pv]\ns_nop 0\ns_cmp_ge_i32 %[s0], 6\ns_cbranch_scc1 .LA1r1_%=\ns_sleep 1\ns_sub_u32 %[s1], %[s1], 1\n"
        "s_cbranch_scc0 .LA1p1_%=\ns_branch .LA1r1_%=\n.LA1s2_%=:\ns_mov_b32 %[s1], 0x100000\n.LA1p2_%=:\nds_read_b32 %[pv], %[sy] offset:4\ns_waitcnt lgkmcnt(0)\nv_readfirstlane_b32 %[s0], %[pv]\n"
        "s_nop 0\ns_cmp_ge_i32 %[s0], 6\ns_cbranch_scc1 .LA1r2_%=\ns_sleep 1\ns_sub_u32 %[s1], %[s1], 1\ns_cbranch_scc0 .LA1p2_%=\ns_branch .LA1r2_%=\n.LA1t%=:\ns_mov_b32 %[s1], 0x100000\n.LA1q%=:\n"
        "ds_read_b32 %[pv], %[sy] offset:4\ns_waitcnt lgkmcnt(0)\nv_readfirstlane_b32 %[s0], %[pv]\ns_nop 0\ns_cmp_ge_i32 %[s0], %[nst]\ns_cbranch_scc1 .LA1u%=\ns_sleep 1\ns_sub_u32 %[s1], %[s1], 1\n"
        "s_cbranch_scc0 .LA1q%=\ns_branch .LA1u%=\n.LB%=:\ns_cmp_eq_u32 %[w], 2\ns_cbranch_scc0 .LC%=\ns_mov_b32 %[so], 0\nbuffer_load_dwordx4 v[32:35], %[wa], %[rs], %[so] offen offset:3072 sc1\n"
        "buffer_load_dwordx4 v[36:39], %[wa], %[rs], %[so] offen offset:0 sc1\nbuffer_load_dwordx4 v[40:43], %[wa], %[rs], %[so] offen offset:1024 sc1\n"
        "buffer_load_dwordx4 v[44:47], %[wa], %[rs], %[so] offen offset:2048 sc1\ns_mov_b32 %[so], 15360\nbuffer_load_dwordx4 v[48:51], %[wa], %[rs], %[so] offen offset:3072 sc1\n"
        "buffer_load_dwordx4 v[52:55], %[wa], %[rs], %[so] offen offset:0 sc1\nbuffer_load_dwordx4 v[56:59], %[wa], %[rs], %[so] offen offset:1024 sc1\n"
        "buffer_load_dwordx4 v[60:63], %[wa], %[rs], %[so] offen offset:2048 sc1\ns_mov_b32 %[so], 30720\nbuffer_load_dwordx4 v[64:67], %[wa], %[rs], %[so] offen offset:3072 sc1\n"
        "buffer_load_dwordx4 v[68:71], %[wa], %[rs], %[so] offen offset:0 sc1\nbuffer_load_dwordx4 v[72:75], %[wa], %[rs], %[so] offen offset:1024 sc1\n"
        "buffer_load_dwordx4 v[76:79], %[wa], %[rs], %[so] offen offset:2048 sc1\ns_mov_b32 %[so], 46080\nbuffer_load_dwordx4 v[80:83], %[wa], %[rs], %[so] offen offset:3072 sc1\n"
        "buffer_load_dwordx4 v[84:87], %[wa], %[rs], %[so] offen offset:0 sc1\nbuffer_load_dwordx4 v[88:91], %[wa], %[rs], %[so] offen offset:1024 sc1\n"
        "buffer_load_dwordx4 v[92:95], %[wa], %[rs], %[so] offen offset:2048 sc1\ns_mov_b32 %[s1], 0x100000\n.LB1w%=:\nds_read_b32 %[pv], %[sy] offset:0\ns_waitcnt lgkmcnt(0)\n"
        "v_readfirstlane_b32 %[s0], %[pv]\ns_nop 0\ns_cmp_ge_i32 %[s0], 6\ns_cbranch_scc1 .LB1g%=\ns_sleep 1\ns_sub_u32 %[s1], %[s1], 1\ns_cbranch_scc0 .LB1w%=\n.LB1g%=:\n"
        "ds_read_b64_tr_b16 v[16:17], %[xa] offset:1056\nds_read_b64_tr_b16 v[18:19], %[xa] offset:1568\nds_read_b64_tr_b16 v[20:21], %[xa] offset:2112\nds_read_b64_tr_b16 v[22:23], %[xa] offset:2624\n"
        "ds_read_b64_tr_b16 v[12:13], %[xa] offset:0\nds_read_b64_tr_b16 v[14:15], %[xa] offset:512\nds_read_b64_tr_b16 v[24:25], %[xa] offset:3168\nds_read_b64_tr_b16 v[26:27], %[xa] offset:3680\n"
        "s_waitcnt vmcnt(12) lgkmcnt(6)\nv_mfma_f32_16x16x32_bf16 v[96:99], v[32:35], v[16:19], 0\nds_read_b64_tr_b16 v[28:29], %[xa] offset:4224\n"
        "v_mfma_f32_16x16x32_bf16 v[108:111], v[36:39], v[16:19], 0\nds_read_b64_tr_b16 v[30:31], %[xa] offset:4736\nv_mfma_f32_16x16x32_bf16 v[128:131], v[40:43], v[16:19], 0\n"
        "ds_read_b32 %[pv], %[sy] offset:4\nv_mfma_f32_16x16x32_bf16 v[148:151], v[44:47], v[16:19], 0\nds_read_b64_tr_b16 v[16:17], %[xa] offset:6336\ns_waitcnt lgkmcnt(8)\n"
        "v_mfma_f32_16x16x32_bf16 v[100:103], v[32:35], v[20:23], 0\nds_read_b64_tr_b16 v[18:19], %[xa] offset:6848\nv_mfma_f32_16x16x32_bf16 v[112:115], v[36:39], v[20:23], 0\n"
        "v_mfma_f32_16x16x32_bf16 v[132:135], v[40:43], v[20:23], 0\nv_mfma_f32_16x16x32_bf16 v[152:155], v[44:47], v[20:23], 0\nds_read_b64_tr_b16 v[20:21], %[xa] offset:7392\ns_waitcnt lgkmcnt(8)\n"
        "v_mfma_f32_16x16x32_bf16 v[104:107], v[36:39], v[12:15], 0\nds_read_b64_tr_b16 v[22:23], %[xa] offset:7904\nv_mfma_f32_16x16x32_bf16 v[124:127], v[40:43], v[12:15], 0\n"
        "v_mfma_f32_16x16x32_bf16 v[144:147], v[44:47], v[12:15], 0\nds_read_b64_tr_b16 v[12:13], %[xa] offset:5280\ns_waitcnt lgkmcnt(8)\nv_mfma_f32_16x16x32_bf16 v[116:119], v[36:39], v[24:27], 0\n"
        "ds_read_b64_tr_b16 v[14:15], %[xa] offset:5792\nv_mfma_f32_16x16x32_bf16 v[136:139], v[40:43], v[24:27], 0\nv_mfma_f32_16x16x32_bf16 v[156:159], v[44:47], v[24:27], 0\n"
        "ds_read_b64_tr_b16 v[24:25], %[xa] offset:8448\ns_waitcnt lgkmcnt(8)\nv_mfma_f32_16x16x32_bf16 v[120:123], v[36:39], v[28:31], 0\nds_read_b64_tr_b16 v[26:27], %[xa] offset:8960\n"
        "v_mfma_f32_16x16x32_bf16 v[140:143], v[40:43], v[28:31], 0\nv_mfma_f32_16x16x32_bf16 v[160:163], v[44:47], v[28:31], 0\n.LB1l%=:\ns_waitcnt vmcnt(8) lgkmcnt(6)\n"
        "v_mfma_f32_16x16x32_bf16 v[96:99], v[48:51], v[16:19], v[96:99]\ns_addk_i32 %[so], 15360\nv_readfirstlane_b32 %[s0], %[pv]\n"
        "v_mfma_f32_16x16x32_bf16 v[108:111], v[52:55], v[16:19], v[108:111]\ns_cmp_ge_i32 %[s0], 6\nds_read_b64_tr_b16 v[28:29], %[xa] offset:9504\n"
        "v_mfma_f32_16x16x32_bf16 v[128:131], v[56:59], v[16:19], v[128:131]\ns_cbranch_scc0 .LB1s0_%=\n.LB1r0_%=:\nds_read_b64_tr_b16 v[30:31], %[xa] offset:10016\n"
        "v_mfma_f32_16x16x32_bf16 v[148:151], v[60:63], v[16:19], v[148:151]\nds_read_b64_tr_b16 v[16:17], %[xa] offset:11616\ns_waitcnt lgkmcnt(7)\n"
        "v_mfma_f32_16x16x32_bf16 v[100:103], v[48:51], v[20:23], v[100:103]\nds_read_b64_tr_b16 v[18:19], %[xa] offset:12128\nbuffer_load_dwordx4 v[32:35], %[wa], %[rs], %[so] offen offset:3072 sc1\n"
        "v_mfma_f32_16x16x32_bf16 v[112:115], v[52:55], v[20:23], v[112:115]\nbuffer_load_dwordx4 v[36:39], %[wa], %[rs], %[so] offen offset:0 sc1\n"
        "v_mfma_f32_16x16x32_bf16 v[132:135], v[56:59], v[20:23], v[132:135]\nv_mfma_f32_16x16x32_bf16 v[152:155], v[60:63], v[20:23], v[152:155]\nds_read_b64_tr_b16 v[20:21], %[xa] offset:12672\n"
        "s_waitcnt lgkmcnt(7)\nv_mfma_f32_16x16x32_bf16 v[104:107], v[52:55], v[12:15], v[104:107]\nds_read_b64_tr_b16 v[22:23], %[xa] offset:13184\n"
        "v_mfma_f32_16x16x32_bf16 v[124:127], v[56:59], v[12:15], v[124:127]\nbuffer_load_dwordx4 v[40:43], %[wa], %[rs], %[so] offen offset:1024 sc1\n"
        "v_mfma_f32_16x16x32_bf16 v[144:147], v[60:63], v[12:15], v[144:147]\nds_read_b64_tr_b16 v[12:13], %[xa] offset:10560\ns_waitcnt lgkmcnt(7)\n"
        "v_mfma_f32_16x16x32_bf16 v[116:119], v[52:55], v[24:27], v[116:119]\nds_read_b64_tr_b16 v[14:15], %[xa] offset:11072\nv_mfma_f32_16x16x32_bf16 v[136:139], v[56:59], v[24:27], v[136:139]\n"
        "buffer_load_dwordx4 v[44:47], %[wa], %[rs], %[so] offen offset:2048 sc1\nv_mfma_f32_16x16x32_bf16 v[156:159], v[60:63], v[24:27], v[156:159]\nds_read_b64_tr_b16 v[24:25], %[xa] offset:13728\n"
        "s_waitcnt lgkmcnt(7)\nv_mfma_f32_16x16x32_bf16 v[120:123], v[52:55], v[28:31], v[120:123]\nv_add_u32 %[sy], 4, %[sy]\nds_read_b64_tr_b16 v[26:27], %[xa] offset:14240\n"
        "v_mfma_f32_16x16x32_bf16 v[140:143], v[56:59], v[28:31], v[140:143]\nds_write_b32 %[pg], %[sy]\nv_mfma_f32_16x16x32_bf16 v[160:163], v[60:63], v[28:31], v[160:163]\n"
        "s_waitcnt vmcnt(8) lgkmcnt(7)\nv_mfma_f32_16x16x32_bf16 v[96:99], v[64:67], v[16:19], v[96:99]\ns_addk_i32 %[so], 15360\nds_read_b64_tr_b16 v[28:29], %[xa] offset:14784\n"
        "v_mfma_f32_16x16x32_bf16 v[108:111], v[68:71], v[16:19], v[108:111]\nds_read_b64_tr_b16 v[30:31], %[xa] offset:15296\nv_mfma_f32_16x16x32_bf16 v[128:131], v[72:75], v[16:19], v[128:131]\n"
        "ds_read_b32 %[pv], %[sy] offset:4\nbuffer_load_dwordx4 v[48:51], %[wa], %[rs], %[so] offen offset:3072 sc1\nv_mfma_f32_16x16x32_bf16 v[148:151], v[76:79], v[16:19], v[148:151]\n"
        "ds_read_b64_tr_b16 v[16:17], %[xa] offset:16896\ns_waitcnt lgkmcnt(9)\nv_mfma_f32_16x16x32_bf16 v[100:103], v[64:67], v[20:23], v[100:103]\nds_read_b64_tr_b16 v[18:19], %[xa] offset:17408\n"
        "v_mfma_f32_16x16x32_bf16 v[112:115], v[68:71], v[20:23], v[112:115]\nbuffer_load_dwordx4 v[52:55], %[wa], %[rs], %[so] offen offset:0 sc1\n"
        "v_mfma_f32_16x16x32_bf16 v[132:135], v[72:75], v[20:23], v[132:135]\nv_mfma_f32_16x16x32_bf16 v[152:155], v[76:79], v[20:23], v[152:155]\nds_read_b64_tr_b16 v[20:21], %[xa] offset:17952\n"
        "s_waitcnt lgkmcnt(9)\nv_mfma_f32_16x16x32_bf16 v[104:107], v[68:71], v[12:15], v[104:107]\nds_read_b64_tr_b16 v[22:23], %[xa] offset:18464\n"
        "v_mfma_f32_16x16x32_bf16 v[124:127], v[72:75], v[12:15], v[124:127]\nbuffer_load_dwordx4 v[56:59], %[wa], %[rs], %[so] offen offset:1024 sc1\n"
        "v_mfma_f32_16x16x32_bf16 v[144:147], v[76:79], v[12:15], v[144:147]\nds_read_b64_tr_b16 v[12:13], %[xa] offset:15840\ns_waitcnt lgkmcnt(9)\n"
        "v_mfma_f32_16x16x32_bf16 v[116:119], v[68:71], v[24:27], v[116:119]\nds_read_b64_tr_b16 v[14:15], %[xa] offset:16352\nv_mfma_f32_16x16x32_bf16 v[136:139], v[72:75], v[24:27], v[136:139]\n"
        "buffer_load_dwordx4 v[60:63], %[wa], %[rs], %[so] offen offset:2048 sc1\nv_mfma_f32_16x16x32_bf16 v[156:159], v[76:79], v[24:27], v[156:159]\nds_read_b64_tr_b16 v[24:25], %[xa] offset:19008\n"
        "s_waitcnt lgkmcnt(8)\nv_mfma_f32_16x16x32_bf16 v[120:123], v[68:71], v[28:31], v[120:123]\nds_read_b64_tr_b16 v[26:27], %[xa] offset:19520\n"
        "v_mfma_f32_16x16x32_bf16 v[140:143], v[72:75], v[28:31], v[140:143]\nv_mfma_f32_16x16x32_bf16 v[160:163], v[76:79], v[28:31], v[160:163]\ns_waitcnt vmcnt(8) lgkmcnt(6)\n"
        "v_mfma_f32_16x16x32_bf16 v[96:99], v[80:83], v[16:19], v[96:99]\ns_addk_i32 %[so], 15360\nv_readfirstlane_b32 %[s0], %[pv]\n"
        "v_mfma_f32_16x16x32_bf16 v[108:111], v[84:87], v[16:19], v[108:111]\ns_cmp_ge_i32 %[s0], 6\nds_read_b64_tr_b16 v[28:29], %[xa] offset:20064\n"
        "v_mfma_f32_16x16x32_bf16 v[128:131], v[88:91], v[16:19], v[128:131]\ns_cbranch_scc0 .LB1s1_%=\n.LB1r1_%=:\nds_read_b64_tr_b16 v[30:31], %[xa] offset:20576\n"
        "v_mfma_f32_16x16x32_bf16 v[148:151], v[92:95], v[16:19], v[148:151]\nds_read_b64_tr_b16 v[16:17], %[xa] offset:22176\ns_waitcnt lgkmcnt(7)\n"
        "v_mfma_f32_16x16x32_bf16 v[100:103], v[80:83], v[20:23], v[100:103]\nds_read_b64_tr_b16 v[18:19], %[xa] offset:22688\nbuffer_load_dwordx4 v[64:67], %[wa], %[rs], %[so] offen offset:3072 sc1\n"
        "v_mfma_f32_16x16x32_bf16 v[112:115], v[84:87], v[20:23], v[112:115]\nbuffer_load_dwordx4 v[68:71], %[wa], %[rs], %[so] offen offset:0 sc1\n"
        "v_mfma_f32_16x16x32_bf16 v[132:135], v[88:91], v[20:23], v[132:135]\nv_mfma_f32_16x16x32_bf16 v[152:155], v[92:95], v[20:23], v[152:155]\nds_read_b64_tr_b16 v[20:21], %[xa] offset:23232\n"
        "s_waitcnt lgkmcnt(7)\nv_mfma_f32_16x16x32_bf16 v[104:107], v[84:87], v[12:15], v[104:107]\nds_read_b64_tr_b16 v[22:23], %[xa] offset:23744\n"
        "v_mfma_f32_16x16x32_bf16 v[124:127], v[88:91], v[12:15], v[124:127]\nbuffer_load_dwordx4 v[72:75], %[wa], %[rs], %[so] offen offset:1024 sc1\n"
        "v_mfma_f32_16x16x32_bf16 v[144:147], v[92:95], v[12:15], v[144:147]\nds_read_b64_tr_b16 v[12:13], %[xa] offset:21120\ns_waitcnt lgkmcnt(7)\n"
        "v_mfma_f32_16x16x32_bf16 v[116:119], v[84:87], v[24:27], v[116:119]\nds_read_b64_tr_b16 v[14:15], %[xa] offset:21632\nv_mfma_f32_16x16x32_bf16 v[136:139], v[88:91], v[24:27], v[136:139]\n"
        "buffer_load_dwordx4 v[76:79], %[wa], %[rs], %[so] offen offset:2048 sc1\nv_mfma_f32_16x16x32_bf16 v[156:159], v[92:95], v[24:27], v[156:159]\nds_read_b64_tr_b16 v[24:25], %[xa] offset:24288\n"
        "s_waitcnt lgkmcnt(7)\nv_mfma_f32_16x16x32_bf16 v[120:123], v[84:87], v[28:31], v[120:123]\nv_add_u32 %[sy], 4, %[sy]\nds_read_b64_tr_b16 v[26:27], %[xa] offset:24800\n"
        "v_mfma_f32_16x16x32_bf16 v[140:143], v[88:91], v[28:31], v[140:143]\nds_write_b32 %[pg], %[sy]\nv_mfma_f32_16x16x32_bf16 v[160:163], v[92:95], v[28:31], v[160:163]\n"
        "s_waitcnt vmcnt(8) lgkmcnt(7)\nv_mfma_f32_16x16x32_bf16 v[96:99], v[32:35], v[16:19], v[96:99]\ns_addk_i32 %[so], 15360\nds_read_b64_tr_b16 v[28:29], %[xa] offset:25344\n"
        "v_mfma_f32_16x16x32_bf16 v[108:111], v[36:39], v[16:19], v[108:111]\nds_read_b64_tr_b16 v[30:31], %[xa] offset:25856\nv_mfma_f32_16x16x32_bf16 v[128:131], v[40:43], v[16:19], v[128:131]\n"
        "ds_read_b32 %[pv], %[sy] offset:4\nbuffer_load_dwordx4 v[80:83], %[wa], %[rs], %[so] offen offset:3072 sc1\nv_mfma_f32_16x16x32_bf16 v[148:151], v[44:47], v[16:19], v[148:151]\n"
        "ds_read_b64_tr_b16 v[16:17], %[xa] offset:27456\ns_waitcnt lgkmcnt(9)\nv_mfma_f32_16x16x32_bf16 v[100:103], v[32:35], v[20:23], v[100:103]\nds_read_b64_tr_b16 v[18:19], %[xa] offset:27968\n"
        "v_mfma_f32_16x16x32_bf16 v[112:115], v[36:39], v[20:23], v[112:115]\nbuffer_load_dwordx4 v[84:87], %[wa], %[rs], %[so] offen offset:0 sc1\n"
        "v_mfma_f32_16x16x32_bf16 v[132:135], v[40:43], v[20:23], v[132:135]\nv_mfma_f32_16x16x32_bf16 v[152:155], v[44:47], v[20:23], v[152:155]\nds_read_b64_tr_b16 v[20:21], %[xa] offset:28512\n"
        "s_waitcnt lgkmcnt(9)\nv_mfma_f32_16x16x32_bf16 v[104:107], v[36:39], v[12:15], v[104:107]\nds_read_b64_tr_b16 v[22:23], %[xa] offset:29024\n"
        "v_mfma_f32_16x16x32_bf16 v[124:127], v[40:43], v[12:15], v[124:127]\nbuffer_load_dwordx4 v[88:91], %[wa], %[rs], %[so] offen offset:1024 sc1\n"
        "v_mfma_f32_16x16x32_bf16 v[144:147], v[44:47], v[12:15], v[144:147]\nds_read_b64_tr_b16 v[12:13], %[xa] offset:26400\ns_waitcnt lgkmcnt(9)\n"
        "v_mfma_f32_16x16x32_bf16 v[116:119], v[36:39], v[24:27], v[116:119]\nds_read_b64_tr_b16 v[14:15], %[xa] offset:26912\nv_mfma_f32_16x16x32_bf16 v[136:139], v[40:43], v[24:27], v[136:139]\n"
        "buffer_load_dwordx4 v[92:95], %[wa], %[rs], %[so] offen offset:2048 sc1\nv_mfma_f32_16x16x32_bf16 v[156:159], v[44:47], v[24:27], v[156:159]\nds_read_b64_tr_b16 v[24:25], %[xa] offset:29568\n"
        "s_waitcnt lgkmcnt(8)\nv_mfma_f32_16x16x32_bf16 v[120:123], v[36:39], v[28:31], v[120:123]\nds_read_b64_tr_b16 v[26:27], %[xa] offset:30080\n"
        "v_mfma_f32_16x16x32_bf16 v[140:143], v[40:43], v[28:31], v[140:143]\ns_cmp_lg_u32 %[so], 291840\nv_mfma_f32_16x16x32_bf16 v[160:163], v[44:47], v[28:31], v[160:163]\n"
        "v_add_u32 %[xa], 21120, %[xa]\ns_cbranch_scc1 .LB1l%=\ns_waitcnt vmcnt(8) lgkmcnt(6)\nv_mfma_f32_16x16x32_bf16 v[96:99], v[48:51], v[16:19], v[96:99]\nv_readfirstlane_b32 %[s0], %[pv]\n"
        "ds_read_b64_tr_b16 v[28:29], %[xa] offset:9504\nv_mfma_f32_16x16x32_bf16 v[108:111], v[52:55], v[16:19], v[108:111]\ns_cmp_ge_i32 %[s0], 6\nds_read_b64_tr_b16 v[30:31], %[xa] offset:10016\n"
        "v_mfma_f32_16x16x32_bf16 v[128:131], v[56:59], v[16:19], v[128:131]\ns_cbranch_scc0 .LB1s2_%=\n.LB1r2_%=:\nv_mfma_f32_16x16x32_bf16 v[148:151], v[60:63], v[16:19], v[148:151]\n"
        "ds_read_b64_tr_b16 v[16:17], %[xa] offset:11616\ns_waitcnt lgkmcnt(7)\nv_mfma_f32_16x16x32_bf16 v[100:103], v[48:51], v[20:23], v[100:103]\nds_read_b64_tr_b16 v[18:19], %[xa] offset:12128\n"
        "v_mfma_f32_16x16x32_bf16 v[112:115], v[52:55], v[20:23], v[112:115]\nv_mfma_f32_16x16x32_bf16 v[132:135], v[56:59], v[20:23], v[132:135]\n"
        "v_mfma_f32_16x16x32_bf16 v[152:155], v[60:63], v[20:23], v[152:155]\nds_read_b64_tr_b16 v[20:21], %[xa] offset:12672\ns_waitcnt lgkmcnt(7)\n"
        "v_mfma_f32_16x16x32_bf16 v[104:107], v[52:55], v[12:15], v[104:107]\nds_read_b64_tr_b16 v[22:23], %[xa] offset:13184\nv_mfma_f32_16x16x32_bf16 v[124:127], v[56:59], v[12:15], v[124:127]\n"
        "v_mfma_f32_16x16x32_bf16 v[144:147], v[60:63], v[12:15], v[144:147]\nds_read_b64_tr_b16 v[12:13], %[xa] offset:10560\ns_waitcnt lgkmcnt(7)\n"
        "v_mfma_f32_16x16x32_bf16 v[116:119], v[52:55], v[24:27], v[116:119]\nds_read_b64_tr_b16 v[14:15], %[xa] offset:11072\nv_mfma_f32_16x16x32_bf16 v[136:139], v[56:59], v[24:27], v[136:139]\n"
        "v_mfma_f32_16x16x32_bf16 v[156:159], v[60:63], v[24:27], v[156:159]\nds_read_b64_tr_b16 v[24:25], %[xa] offset:13728\ns_waitcnt lgkmcnt(7)\n"
        "v_mfma_f32_16x16x32_bf16 v[120:123], v[52:55], v[28:31], v[120:123]\nv_add_u32 %[sy], 4, %[sy]\nds_read_b64_tr_b16 v[26:27], %[xa] offset:14240\n"
        "v_mfma_f32_16x16x32_bf16 v[140:143], v[56:59], v[28:31], v[140:143]\nv_mfma_f32_16x16x32_bf16 v[160:163], v[60:63], v[28:31], v[160:163]\ns_waitcnt vmcnt(4) lgkmcnt(6)\n"
        "v_mfma_f32_16x16x32_bf16 v[96:99], v[64:67], v[16:19], v[96:99]\nds_read_b64_tr_b16 v[28:29], %[xa] offset:14784\nv_mfma_f32_16x16x32_bf16 v[108:111], v[68:71], v[16:19], v[108:111]\n"
        "ds_read_b64_tr_b16 v[30:31], %[xa] offset:15296\nv_mfma_f32_16x16x32_bf16 v[128:131], v[72:75], v[16:19], v[128:131]\nds_read_b32 %[pv], %[sy] offset:4\n"
        "v_mfma_f32_16x16x32_bf16 v[148:151], v[76:79], v[16:19], v[148:151]\nds_read_b64_tr_b16 v[16:17], %[xa] offset:16896\ns_waitcnt lgkmcnt(8)\n"
        "v_mfma_f32_16x16x32_bf16 v[100:103], v[64:67], v[20:23], v[100:103]\nds_read_b64_tr_b16 v[18:19], %[xa] offset:17408\nv_mfma_f32_16x16x32_bf16 v[112:115], v[68:71], v[20:23], v[112:115]\n"
        "v_mfma_f32_16x16x32_bf16 v[132:135], v[72:75], v[20:23], v[132:135]\nv_mfma_f32_16x16x32_bf16 v[152:155], v[76:79], v[20:23], v[152:155]\nds_read_b64_tr_b16 v[20:21], %[xa] offset:17952\n"
        "s_waitcnt lgkmcnt(8)\nv_mfma_f32_16x16x32_bf16 v[104:107], v[68:71], v[12:15], v[104:107]\nds_read_b64_tr_b16 v[22:23], %[xa] offset:18464\n"
        "v_mfma_f32_16x16x32_bf16 v[124:127], v[72:75], v[12:15], v[124:127]\nv_mfma_f32_16x16x32_bf16 v[144:147], v[76:79], v[12:15], v[144:147]\nds_read_b64_tr_b16 v[12:13], %[xa] offset:15840\n"
        "s_waitcnt lgkmcnt(8)\nv_mfma_f32_16x16x32_bf16 v[116:119], v[68:71], v[24:27], v[116:119]\nds_read_b64_tr_b16 v[14:15], %[xa] offset:16352\n"
        "v_mfma_f32_16x16x32_bf16 v[136:139], v[72:75], v[24:27], v[136:139]\nv_mfma_f32_16x16x32_bf16 v[156:159], v[76:79], v[24:27], v[156:159]\nds_read_b64_tr_b16 v[24:25], %[xa] offset:19008\n"
        "s_waitcnt lgkmcnt(8)\nv_mfma_f32_16x16x32_bf16 v[120:123], v[68:71], v[28:31], v[120:123]\nds_read_b64_tr_b16 v[26:27], %[xa] offset:19520\n"
        "v_mfma_f32_16x16x32_bf16 v[140:143], v[72:75], v[28:31], v[140:143]\nv_mfma_f32_16x16x32_bf16 v[160:163], v[76:79], v[28:31], v[160:163]\ns_waitcnt vmcnt(0) lgkmcnt(6)\n"
        "v_mfma_f32_16x16x32_bf16 v[96:99], v[80:83], v[16:19], v[96:99]\nv_readfirstlane_b32 %[s0], %[pv]\nds_read_b64_tr_b16 v[28:29], %[xa] offset:20064\n"
        "v_mfma_f32_16x16x32_bf16 v[108:111], v[84:87], v[16:19], v[108:111]\nds_read_b64_tr_b16 v[30:31], %[xa] offset:20576\nv_mfma_f32_16x16x32_bf16 v[128:131], v[88:91], v[16:19], v[128:131]\n"
        "s_cmp_ge_i32 %[s0], %[nst]\nv_mfma_f32_16x16x32_bf16 v[148:151], v[92:95], v[16:19], v[148:151]\ns_cbranch_scc0 .LB1t%=\n.LB1u%=:\ns_waitcnt lgkmcnt(6)\n"
        "v_mfma_f32_16x16x32_bf16 v[100:103], v[80:83], v[20:23], v[100:103]\nv_add_u32 %[sy], 4, %[sy]\nv_mfma_f32_16x16x32_bf16 v[112:115], v[84:87], v[20:23], v[112:115]\n"
        "ds_read_b64 v[32:33], %[cwa] offset:896\nv_mfma_f32_16x16x32_bf16 v[132:135], v[88:91], v[20:23], v[132:135]\nds_read_b64 v[36:37], %[cwa] offset:256\n"
        "v_mfma_f32_16x16x32_bf16 v[152:155], v[92:95], v[20:23], v[152:155]\nds_read_b64 v[40:41], %[cwa] offset:384\ns_waitcnt lgkmcnt(7)\n"
        "v_mfma_f32_16x16x32_bf16 v[104:107], v[84:87], v[12:15], v[104:107]\nds_read_b64 v[44:45], %[cwa] offset:1536\nv_mfma_f32_16x16x32_bf16 v[124:127], v[88:91], v[12:15], v[124:127]\n"
        "ds_read_b128 v[48:51], %[mra] offset:0\nv_mfma_f32_16x16x32_bf16 v[144:147], v[92:95], v[12:15], v[144:147]\nds_read_b128 v[52:55], %[mra] offset:256\ns_waitcnt lgkmcnt(8)\n"
        "v_mfma_f32_16x16x32_bf16 v[116:119], v[84:87], v[24:27], v[116:119]\nds_read_b128 v[56:59], %[mra] offset:512\nv_mfma_f32_16x16x32_bf16 v[136:139], v[88:91], v[24:27], v[136:139]\n"
        "ds_read_b128 v[60:63], %[mra] offset:768\nv_mfma_f32_16x16x32_bf16 v[156:159], v[92:95], v[24:27], v[156:159]\nds_read_b128 v[64:67], %[mra] offset:1024\ns_waitcnt lgkmcnt(9)\n"
        "v_mfma_f32_16x16x32_bf16 v[120:123], v[84:87], v[28:31], v[120:123]\nv_add_u32 %[sy], 4, %[sy]\nv_mfma_f32_16x16x32_bf16 v[140:143], v[88:91], v[28:31], v[140:143]\ns_setprio %[pfs]\n"
        "v_mfma_f32_16x16x32_bf16 v[160:163], v[92:95], v[28:31], v[160:163]\ns_waitcnt lgkmcnt(0)\nv_mfma_f32_16x16x32_bf16 v[96:99], v[32:35], v[52:55], v[96:99]\n"
        "v_mfma_f32_16x16x32_bf16 v[100:103], v[32:35], v[56:59], v[100:103]\nv_mfma_f32_16x16x32_bf16 v[108:111], v[36:39], v[52:55], v[108:111]\n"
        "v_mfma_f32_16x16x32_bf16 v[112:115], v[36:39], v[56:59], v[112:115]\nv_mfma_f32_16x16x32_bf16 v[128:131], v[40:43], v[52:55], v[128:131]\nv_cvt_pk_bf16_f32 v96, v96, v97\n"
        "v_cvt_pk_bf16_f32 v97, v98, v99\nv_cvt_pk_bf16_f32 v98, v100, v101\nv_cvt_pk_bf16_f32 v99, v102, v103\nv_mfma_f32_16x16x32_bf16 v[132:135], v[40:43], v[56:59], v[132:135]\n"
        "v_permlane16_swap_b32 v96, v98\nv_permlane16_swap_b32 v97, v99\nds_write_b128 %[stb], v[96:99] offset:8160\nv_mfma_f32_16x16x32_bf16 v[104:107], v[36:39], v[48:51], v[104:107]\n"
        "v_cvt_pk_bf16_f32 v108, v108, v109\nv_cvt_pk_bf16_f32 v109, v110, v111\nv_cvt_pk_bf16_f32 v110, v112, v113\nv_cvt_pk_bf16_f32 v111, v114, v115\n"
        "v_mfma_f32_16x16x32_bf16 v[124:127], v[40:43], v[48:51], v[124:127]\nv_permlane16_swap_b32 v108, v110\nv_permlane16_swap_b32 v109, v111\nds_write_b128 %[stb], v[108:111] offset:8000\n"
        "v_mfma_f32_16x16x32_bf16 v[116:119], v[36:39], v[60:63], v[116:119]\nv_cvt_pk_bf16_f32 v128, v128, v129\nv_cvt_pk_bf16_f32 v129, v130, v131\nv_cvt_pk_bf16_f32 v130, v132, v133\n"
        "v_cvt_pk_bf16_f32 v131, v134, v135\nv_mfma_f32_16x16x32_bf16 v[120:123], v[36:39], v[64:67], v[120:123]\nv_permlane16_swap_b32 v128, v130\nv_permlane16_swap_b32 v129, v131\n"
        "ds_write_b128 %[stb], v[128:131] offset:8032\nv_cvt_pk_bf16_f32 v104, v104, v105\nv_mfma_f32_16x16x32_bf16 v[136:139], v[40:43], v[60:63], v[136:139]\nv_cvt_pk_bf16_f32 v105, v106, v107\n"
        "ds_write_b64 %[sta], v[104:105] offset:64\nv_cvt_pk_bf16_f32 v124, v124, v125\nv_cvt_pk_bf16_f32 v125, v126, v127\nv_mfma_f32_16x16x32_bf16 v[140:143], v[40:43], v[64:67], v[140:143]\n"
        "ds_write_b64 %[sta], v[124:125] offset:96\nv_mfma_f32_16x16x32_bf16 v[148:151], v[44:47], v[52:55], v[148:151]\nv_cvt_pk_bf16_f32 v116, v116, v117\nv_cvt_pk_bf16_f32 v117, v118, v119\n"
        "v_cvt_pk_bf16_f32 v118, v120, v121\nv_cvt_pk_bf16_f32 v119, v122, v123\nv_mfma_f32_16x16x32_bf16 v[152:155], v[44:47], v[56:59], v[152:155]\nv_permlane16_swap_b32 v116, v118\n"
        "v_permlane16_swap_b32 v117, v119\nds_write_b128 %[stb], v[116:119] offset:23872\nv_mfma_f32_16x16x32_bf16 v[144:147], v[44:47], v[48:51], v[144:147]\nv_cvt_pk_bf16_f32 v136, v136, v137\n"
        "v_cvt_pk_bf16_f32 v137, v138, v139\nv_cvt_pk_bf16_f32 v138, v140, v141\nv_cvt_pk_bf16_f32 v139, v142, v143\nv_mfma_f32_16x16x32_bf16 v[156:159], v[44:47], v[60:63], v[156:159]\n"
        "v_permlane16_swap_b32 v136, v138\nv_permlane16_swap_b32 v137, v139\nds_write_b128 %[stb], v[136:139] offset:23904\nds_write_b32 %[pg], %[sy]\n"
        "v_mfma_f32_16x16x32_bf16 v[160:163], v[44:47], v[64:67], v[160:163]\nv_add_u32 %[sy], 4, %[sy]\nv_cvt_pk_bf16_f32 v148, v148, v149\nv_cvt_pk_bf16_f32 v149, v150, v151\n"
        "v_cvt_pk_bf16_f32 v150, v152, v153\ns_nop 3\nv_cvt_pk_bf16_f32 v151, v154, v155\ns_nop 0\nv_permlane16_swap_b32 v148, v150\nv_permlane16_swap_b32 v149, v151\n"
        "ds_write_b128 %[stb], v[148:151] offset:8320\nv_cvt_pk_bf16_f32 v144, v144, v145\nv_cvt_pk_bf16_f32 v145, v146, v147\nds_write_b64 %[sta], v[144:145] offset:384\n"
        "v_cvt_pk_bf16_f32 v156, v156, v157\nv_cvt_pk_bf16_f32 v157, v158, v159\nv_cvt_pk_bf16_f32 v158, v160, v161\nv_cvt_pk_bf16_f32 v159, v162, v163\ns_nop 0\nv_permlane16_swap_b32 v156, v158\n"
        "v_permlane16_swap_b32 v157, v159\nds_write_b128 %[stb], v[156:159] offset:24192\nds_write_b32 %[pg], %[sy]\ns_branch .LE%=\n.LB1s0_%=:\ns_mov_b32 %[s1], 0x100000\n.LB1p0_%=:\n"
        "ds_read_b32 %[pv], %[sy] offset:4\ns_waitcnt lgkmcnt(0)\nv_readfirstlane_b32 %[s0], %[pv]\ns_nop 0\ns_cmp_ge_i32 %[s0], 6\ns_cbranch_scc1 .LB1r0_%=\ns_sleep 1\ns_sub_u32 %[s1], %[s1], 1\n"
        "s_cbranch_scc0 .LB1p0_%=\ns_branch .LB1r0_%=\n.LB1s1_%=:\ns_mov_b32 %[s1], 0x100000\n.LB1p1_%=:\nds_read_b32 %[pv], %[sy] offset:4\ns_waitcnt lgkmcnt(0)\nv_readfirstlane_b32 %[s0], %[pv]\n"
        "s_nop 0\ns_cmp_ge_i32 %[s0], 6\ns_cbranch_scc1 .LB1r1_%=\ns_sleep 1\ns_sub_u32 %[s1], %[s1], 1\ns_cbranch_scc0 .LB1p1_%=\ns_branch .LB1r1_%=\n.LB1s2_%=:\ns_mov_b32 %[s1], 0x100000\n"
        ".LB1p2_%=:\nds_read_b32 %[pv], %[sy] offset:4\ns_waitcnt lgkmcnt(0)\nv_readfirstlane_b32 %[s0], %[pv]\ns_nop 0\ns_cmp_ge_i32 %[s0], 6\ns_cbranch_scc1 .LB1r2_%=\ns_sleep 1\n"
        "s_sub_u32 %[s1], %[s1], 1\ns_cbranch_scc0 .LB1p2_%=\ns_branch .LB1r2_%=\n.LB1t%=:\ns_mov_b32 %[s1], 0x100000\n.LB1q%=:\nds_read_b32 %[pv], %[sy] offset:4\ns_waitcnt lgkmcnt(0)\n"
        "v_readfirstlane_b32 %[s0], %[pv]\ns_nop 0\ns_cmp_ge_i32 %[s0], %[nst]\ns_cbranch_scc1 .LB1u%=\ns_sleep 1\ns_sub_u32 %[s1], %[s1], 1\ns_cbranch_scc0 .LB1q%=\ns_branch .LB1u%=\n.LC%=:\n"
        "s_mov_b32 %[so], 0\nbuffer_load_dwordx4 v[32:35], %[wa], %[rs], %[so] offen offset:0 sc1\nbuffer_load_dwordx4 v[36:39], %[wa], %[rs], %[so] offen offset:1024 sc1\n"
        "buffer_load_dwordx4 v[40:43], %[wa], %[rs], %[so] offen offset:2048 sc1\nbuffer_load_dwordx4 v[44:47], %[wa], %[rs], %[so] offen offset:3072 sc1\ns_mov_b32 %[so], 15360\n"
        "buffer_load_dwordx4 v[48:51], %[wa], %[rs], %[so] offen offset:0 sc1\nbuffer_load_dwordx4 v[52:55], %[wa], %[rs], %[so] offen offset:1024 sc1\n"
        "buffer_load_dwordx4 v[56:59], %[wa], %[rs], %[so] offen offset:2048 sc1\nbuffer_load_dwordx4 v[60:63], %[wa], %[rs], %[so] offen offset:3072 sc1\ns_mov_b32 %[so], 30720\n"
        "buffer_load_dwordx4 v[64:67], %[wa], %[rs], %[so] offen offset:0 sc1\nbuffer_load_dwordx4 v[68:71], %[wa], %[rs], %[so] offen offset:1024 sc1\n"
        "buffer_load_dwordx4 v[72:75], %[wa], %[rs], %[so] offen offset:2048 sc1\nbuffer_load_dwordx4 v[76:79], %[wa], %[rs], %[so] offen offset:3072 sc1\ns_mov_b32 %[so], 46080\n"
        "buffer_load_dwordx4 v[80:83], %[wa], %[rs], %[so] offen offset:0 sc1\nbuffer_load_dwordx4 v[84:87], %[wa], %[rs], %[so] offen offset:1024 sc1\n"
        "buffer_load_dwordx4 v[88:91], %[wa], %[rs], %[so] offen offset:2048 sc1\nbuffer_load_dwordx4 v[92:95], %[wa], %[rs], %[so] offen offset:3072 sc1\ns_mov_b32 %[s1], 0x100000\n.LC1w%=:\n"
        "ds_read_b32 %[pv], %[sy] offset:0\ns_waitcnt lgkmcnt(0)\nv_readfirstlane_b32 %[s0], %[pv]\ns_nop 0\ns_cmp_ge_i32 %[s0], 6\ns_cbranch_scc1 .LC1g%=\ns_sleep 1\ns_sub_u32 %[s1], %[s1], 1\n"
        "s_cbranch_scc0 .LC1w%=\n.LC1g%=:\nds_read_b64_tr_b16 v[24:25], %[xa] offset:3168\nds_read_b64_tr_b16 v[26:27], %[xa] offset:3680\nds_read_b64_tr_b16 v[28:29], %[xa] offset:4224\n"
        "ds_read_b64_tr_b16 v[30:31], %[xa] offset:4736\nds_read_b64_tr_b16 v[12:13], %[xa] offset:0\nds_read_b64_tr_b16 v[14:15], %[xa] offset:512\nds_read_b64_tr_b16 v[16:17], %[xa] offset:1056\n"
        "ds_read_b64_tr_b16 v[18:19], %[xa] offset:1568\ns_waitcnt vmcnt(12) lgkmcnt(6)\nv_mfma_f32_16x16x32_bf16 v[96:99], v[32:35], v[24:27], 0\nds_read_b64_tr_b16 v[20:21], %[xa] offset:2112\n"
        "v_mfma_f32_16x16x32_bf16 v[116:119], v[36:39], v[24:27], 0\nds_read_b64_tr_b16 v[22:23], %[xa] offset:2624\nv_mfma_f32_16x16x32_bf16 v[136:139], v[40:43], v[24:27], 0\n"
        "ds_read_b32 %[pv], %[sy] offset:4\nv_mfma_f32_16x16x32_bf16 v[156:159], v[44:47], v[24:27], 0\nds_read_b64_tr_b16 v[24:25], %[xa] offset:8448\ns_waitcnt lgkmcnt(8)\n"
        "v_mfma_f32_16x16x32_bf16 v[100:103], v[32:35], v[28:31], 0\nds_read_b64_tr_b16 v[26:27], %[xa] offset:8960\nv_mfma_f32_16x16x32_bf16 v[120:123], v[36:39], v[28:31], 0\n"
        "v_mfma_f32_16x16x32_bf16 v[140:143], v[40:43], v[28:31], 0\nv_mfma_f32_16x16x32_bf16 v[160:163], v[44:47], v[28:31], 0\nds_read_b64_tr_b16 v[28:29], %[xa] offset:9504\ns_waitcnt lgkmcnt(8)\n"
        "v_mfma_f32_16x16x32_bf16 v[104:107], v[36:39], v[12:15], 0\nds_read_b64_tr_b16 v[30:31], %[xa] offset:10016\nv_mfma_f32_16x16x32_bf16 v[124:127], v[40:43], v[12:15], 0\n"
        "v_mfma_f32_16x16x32_bf16 v[144:147], v[44:47], v[12:15], 0\nds_read_b64_tr_b16 v[12:13], %[xa] offset:5280\ns_waitcnt lgkmcnt(8)\nv_mfma_f32_16x16x32_bf16 v[108:111], v[36:39], v[16:19], 0\n"
        "ds_read_b64_tr_b16 v[14:15], %[xa] offset:5792\nv_mfma_f32_16x16x32_bf16 v[128:131], v[40:43], v[16:19], 0\nv_mfma_f32_16x16x32_bf16 v[148:151], v[44:47], v[16:19], 0\n"
        "ds_read_b64_tr_b16 v[16:17], %[xa] offset:6336\ns_waitcnt lgkmcnt(8)\nv_mfma_f32_16x16x32_bf16 v[112:115], v[36:39], v[20:23], 0\nds_read_b64_tr_b16 v[18:19], %[xa] offset:6848\n"
        "v_mfma_f32_16x16x32_bf16 v[132:135], v[40:43], v[20:23], 0\nv_mfma_f32_16x16x32_bf16 v[152:155], v[44:47], v[20:23], 0\n.LC1l%=:\ns_waitcnt vmcnt(8) lgkmcnt(6)\n"
        "v_mfma_f32_16x16x32_bf16 v[96:99], v[48:51], v[24:27], v[96:99]\ns_addk_i32 %[so], 15360\nv_readfirstlane_b32 %[s0], %[pv]\n"
        "v_mfma_f32_16x16x32_bf16 v[116:119], v[52:55], v[24:27], v[116:119]\ns_cmp_ge_i32 %[s0], 6\nds_read_b64_tr_b16 v[20:21], %[xa] offset:7392\n"
        "v_mfma_f32_16x16x32_bf16 v[136:139], v[56:59], v[24:27], v[136:139]\ns_cbranch_scc0 .LC1s0_%=\n.LC1r0_%=:\nds_read_b64_tr_b16 v[22:23], %[xa] offset:7904\n"
        "v_mfma_f32_16x16x32_bf16 v[156:159], v[60:63], v[24:27], v[156:159]\nds_read_b64_tr_b16 v[24:25], %[xa] offset:13728\ns_waitcnt lgkmcnt(7)\n"
        "v_mfma_f32_16x16x32_bf16 v[100:103], v[48:51], v[28:31], v[100:103]\nds_read_b64_tr_b16 v[26:27], %[xa] offset:14240\nbuffer_load_dwordx4 v[32:35], %[wa], %[rs], %[so] offen offset:0 sc1\n"
        "v_mfma_f32_16x16x32_bf16 v[120:123], v[52:55], v[28:31], v[120:123]\nbuffer_load_dwordx4 v[36:39], %[wa], %[rs], %[so] offen offset:1024 sc1\n"
        "v_mfma_f32_16x16x32_bf16 v[140:143], v[56:59], v[28:31], v[140:143]\nv_mfma_f32_16x16x32_bf16 v[160:163], v[60:63], v[28:31], v[160:163]\nds_read_b64_tr_b16 v[28:29], %[xa] offset:14784\n"
        "s_waitcnt lgkmcnt(7)\nv_mfma_f32_16x16x32_bf16 v[104:107], v[52:55], v[12:15], v[104:107]\nds_read_b64_tr_b16 v[30:31], %[xa] offset:15296\n"
        "v_mfma_f32_16x16x32_bf16 v[124:127], v[56:59], v[12:15], v[124:127]\nbuffer_load_dwordx4 v[40:43], %[wa], %[rs], %[so] offen offset:2048 sc1\n"
        "v_mfma_f32_16x16x32_bf16 v[144:147], v[60:63], v[12:15], v[144:147]\nds_read_b64_tr_b16 v[12:13], %[xa] offset:10560\ns_waitcnt lgkmcnt(7)\n"
        "v_mfma_f32_16x16x32_bf16 v[108:111], v[52:55], v[16:19], v[108:111]\nds_read_b64_tr_b16 v[14:15], %[xa] offset:11072\nv_mfma_f32_16x16x32_bf16 v[128:131], v[56:59], v[16:19], v[128:131]\n"
        "buffer_load_dwordx4 v[44:47], %[wa], %[rs], %[so] offen offset:3072 sc1\nv_mfma_f32_16x16x32_bf16 v[148:151], v[60:63], v[16:19], v[148:151]\nds_read_b64_tr_b16 v[16:17], %[xa] offset:11616\n"
        "s_waitcnt lgkmcnt(7)\nv_mfma_f32_16x16x32_bf16 v[112:115], v[52:55], v[20:23], v[112:115]\nv_add_u32 %[sy], 4, %[sy]\nds_read_b64_tr_b16 v[18:19], %[xa] offset:12128\n"
        "v_mfma_f32_16x16x32_bf16 v[132:135], v[56:59], v[20:23], v[132:135]\nds_write_b32 %[pg], %[sy]\nv_mfma_f32_16x16x32_bf16 v[152:155], v[60:63], v[20:23], v[152:155]\n"
        "s_waitcnt vmcnt(8) lgkmcnt(7)\nv_mfma_f32_16x16x32_bf16 v[96:99], v[64:67], v[24:27], v[96:99]\ns_addk_i32 %[so], 15360\nds_read_b64_tr_b16 v[20:21], %[xa] offset:12672\n"
        "v_mfma_f32_16x16x32_bf16 v[116:119], v[68:71], v[24:27], v[116:119]\nds_read_b64_tr_b16 v[22:23], %[xa] offset:13184\nv_mfma_f32_16x16x32_bf16 v[136:139], v[72:75], v[24:27], v[136:139]\n"
        "ds_read_b32 %[pv], %[sy] offset:4\nbuffer_load_dwordx4 v[48:51], %[wa], %[rs], %[so] offen offset:0 sc1\nv_mfma_f32_16x16x32_bf16 v[156:159], v[76:79], v[24:27], v[156:159]\n"
        "ds_read_b64_tr_b16 v[24:25], %[xa] offset:19008\ns_waitcnt lgkmcnt(9)\nv_mfma_f32_16x16x32_bf16 v[100:103], v[64:67], v[28:31], v[100:103]\nds_read_b64_tr_b16 v[26:27], %[xa] offset:19520\n"
        "v_mfma_f32_16x16x32_bf16 v[120:123], v[68:71], v[28:31], v[120:123]\nbuffer_load_dwordx4 v[52:55], %[wa], %[rs], %[so] offen offset:1024 sc1\n"
        "v_mfma_f32_16x16x32_bf16 v[140:143], v[72:75], v[28:31], v[140:143]\nv_mfma_f32_16x16x32_bf16 v[160:163], v[76:79], v[28:31], v[160:163]\nds_read_b64_tr_b16 v[28:29], %[xa] offset:20064\n"
        "s_waitcnt lgkmcnt(9)\nv_mfma_f32_16x16x32_bf16 v[104:107], v[68:71], v[12:15], v[104:107]\nds_read_b64_tr_b16 v[30:31], %[xa] offset:20576\n"
        "v_mfma_f32_16x16x32_bf16 v[124:127], v[72:75], v[12:15], v[124:127]\nbuffer_load_dwordx4 v[56:59], %[wa], %[rs], %[so] offen offset:2048 sc1\n"
        "v_mfma_f32_16x16x32_bf16 v[144:147], v[76:79], v[12:15], v[144:147]\nds_read_b64_tr_b16 v[12:13], %[xa] offset:15840\ns_waitcnt lgkmcnt(9)\n"
        "v_mfma_f32_16x16x32_bf16 v[108:111], v[68:71], v[16:19], v[108:111]\nds_read_b64_tr_b16 v[14:15], %[xa] offset:16352\nv_mfma_f32_16x16x32_bf16 v[128:131], v[72:75], v[16:19], v[128:131]\n"
        "buffer_load_dwordx4 v[60:63], %[wa], %[rs], %[so] offen offset:3072 sc1\nv_mfma_f32_16x16x32_bf16 v[148:151], v[76:79], v[16:19], v[148:151]\nds_read_b64_tr_b16 v[16:17], %[xa] offset:16896\n"
        "s_waitcnt lgkmcnt(8)\nv_mfma_f32_16x16x32_bf16 v[112:115], v[68:71], v[20:23], v[112:115]\nds_read_b64_tr_b16 v[18:19], %[xa] offset:17408\n"
        "v_mfma_f32_16x16x32_bf16 v[132:135], v[72:75], v[20:23], v[132:135]\nv_mfma_f32_16x16x32_bf16 v[152:155], v[76:79], v[20:23], v[152:155]\ns_waitcnt vmcnt(8) lgkmcnt(6)\n"
        "v_mfma_f32_16x16x32_bf16 v[96:99], v[80:83], v[24:27], v[96:99]\ns_addk_i32 %[so], 15360\nv_readfirstlane_b32 %[s0], %[pv]\n"
        "v_mfma_f32_16x16x32_bf16 v[116:119], v[84:87], v[24:27], v[116:119]\ns_cmp_ge_i32 %[s0], 6\nds_read_b64_tr_b16 v[20:21], %[xa] offset:17952\n"
        "v_mfma_f32_16x16x32_bf16 v[136:139], v[88:91], v[24:27], v[136:139]\ns_cbranch_scc0 .LC1s1_%=\n.LC1r1_%=:\nds_read_b64_tr_b16 v[22:23], %[xa] offset:18464\n"
        "v_mfma_f32_16x16x32_bf16 v[156:159], v[92:95], v[24:27], v[156:159]\nds_read_b64_tr_b16 v[24:25], %[xa] offset:24288\ns_waitcnt lgkmcnt(7)\n"
        "v_mfma_f32_16x16x32_bf16 v[100:103], v[80:83], v[28:31], v[100:103]\nds_read_b64_tr_b16 v[26:27], %[xa] offset:24800\nbuffer_load_dwordx4 v[64:67], %[wa], %[rs], %[so] offen offset:0 sc1\n"
        "v_mfma_f32_16x16x32_bf16 v[120:123], v[84:87], v[28:31], v[120:123]\nbuffer_load_dwordx4 v[68:71], %[wa], %[rs], %[so] offen offset:1024 sc1\n"
        "v_mfma_f32_16x16x32_bf16 v[140:143], v[88:91], v[28:31], v[140:143]\nv_mfma_f32_16x16x32_bf16 v[160:163], v[92:95], v[28:31], v[160:163]\nds_read_b64_tr_b16 v[28:29], %[xa] offset:25344\n"
        "s_waitcnt lgkmcnt(7)\nv_mfma_f32_16x16x32_bf16 v[104:107], v[84:87], v[12:15], v[104:107]\nds_read_b64_tr_b16 v[30:31], %[xa] offset:25856\n"
        "v_mfma_f32_16x16x32_bf16 v[124:127], v[88:91], v[12:15], v[124:127]\nbuffer_load_dwordx4 v[72:75], %[wa], %[rs], %[so] offen offset:2048 sc1\n"
        "v_mfma_f32_16x16x32_bf16 v[144:147], v[92:95], v[12:15], v[144:147]\nds_read_b64_tr_b16 v[12:13], %[xa] offset:21120\ns_waitcnt lgkmcnt(7)\n"
        "v_mfma_f32_16x16x32_bf16 v[108:111], v[84:87], v[16:19], v[108:111]\nds_read_b64_tr_b16 v[14:15], %[xa] offset:21632\nv_mfma_f32_16x16x32_bf16 v[128:131], v[88:91], v[16:19], v[128:131]\n"
        "buffer_load_dwordx4 v[76:79], %[wa], %[rs], %[so] offen offset:3072 sc1\nv_mfma_f32_16x16x32_bf16 v[148:151], v[92:95], v[16:19], v[148:151]\nds_read_b64_tr_b16 v[16:17], %[xa] offset:22176\n"
        "s_waitcnt lgkmcnt(7)\nv_mfma_f32_16x16x32_bf16 v[112:115], v[84:87], v[20:23], v[112:115]\nv_add_u32 %[sy], 4, %[sy]\nds_read_b64_tr_b16 v[18:19], %[xa] offset:22688\n"
        "v_mfma_f32_16x16x32_bf16 v[132:135], v[88:91], v[20:23], v[132:135]\nds_write_b32 %[pg], %[sy]\nv_mfma_f32_16x16x32_bf16 v[152:155], v[92:95], v[20:23], v[152:155]\n"
        "s_waitcnt vmcnt(8) lgkmcnt(7)\nv_mfma_f32_16x16x32_bf16 v[96:99], v[32:35], v[24:27], v[96:99]\ns_addk_i32 %[so], 15360\nds_read_b64_tr_b16 v[20:21], %[xa] offset:23232\n"
        "v_mfma_f32_16x16x32_bf16 v[116:119], v[36:39], v[24:27], v[116:119]\nds_read_b64_tr_b16 v[22:23], %[xa] offset:23744\nv_mfma_f32_16x16x32_bf16 v[136:139], v[40:43], v[24:27], v[136:139]\n"
        "ds_read_b32 %[pv], %[sy] offset:4\nbuffer_load_dwordx4 v[80:83], %[wa], %[rs], %[so] offen offset:0 sc1\nv_mfma_f32_16x16x32_bf16 v[156:159], v[44:47], v[24:27], v[156:159]\n"
        "ds_read_b64_tr_b16 v[24:25], %[xa] offset:29568\ns_waitcnt lgkmcnt(9)\nv_mfma_f32_16x16x32_bf16 v[100:103], v[32:35], v[28:31], v[100:103]\nds_read_b64_tr_b16 v[26:27], %[xa] offset:30080\n"
        "v_mfma_f32_16x16x32_bf16 v[120:123], v[36:39], v[28:31], v[120:123]\nbuffer_load_dwordx4 v[84:87], %[wa], %[rs], %[so] offen offset:1024 sc1\n"
        "v_mfma_f32_16x16x32_bf16 v[140:143], v[40:43], v[28:31], v[140:143]\nv_mfma_f32_16x16x32_bf16 v[160:163], v[44:47], v[28:31], v[160:163]\nds_read_b64_tr_b16 v[28:29], %[xa] offset:30624\n"
        "s_waitcnt lgkmcnt(9)\nv_mfma_f32_16x16x32_bf16 v[104:107], v[36:39], v[12:15], v[104:107]\nds_read_b64_tr_b16 v[30:31], %[xa] offset:31136\n"
        "v_mfma_f32_16x16x32_bf16 v[124:127], v[40:43], v[12:15], v[124:127]\nbuffer_load_dwordx4 v[88:91], %[wa], %[rs], %[so] offen offset:2048 sc1\n"
        "v_mfma_f32_16x16x32_bf16 v[144:147], v[44:47], v[12:15], v[144:147]\nds_read_b64_tr_b16 v[12:13], %[xa] offset:26400\ns_waitcnt lgkmcnt(9)\n"
        "v_mfma_f32_16x16x32_bf16 v[108:111], v[36:39], v[16:19], v[108:111]\nds_read_b64_tr_b16 v[14:15], %[xa] offset:26912\nv_mfma_f32_16x16x32_bf16 v[128:131], v[40:43], v[16:19], v[128:131]\n"
        "buffer_load_dwordx4 v[92:95], %[wa], %[rs], %[so] offen offset:3072 sc1\nv_mfma_f32_16x16x32_bf16 v[148:151], v[44:47], v[16:19], v[148:151]\nds_read_b64_tr_b16 v[16:17], %[xa] offset:27456\n"
        "s_waitcnt lgkmcnt(8)\nv_mfma_f32_16x16x32_bf16 v[112:115], v[36:39], v[20:23], v[112:115]\nds_read_b64_tr_b16 v[18:19], %[xa] offset:27968\n"
        "v_mfma_f32_16x16x32_bf16 v[132:135], v[40:43], v[20:23], v[132:135]\ns_cmp_lg_u32 %[so], 291840\nv_mfma_f32_16x16x32_bf16 v[152:155], v[44:47], v[20:23], v[152:155]\n"
        "v_add_u32 %[xa], 21120, %[xa]\ns_cbranch_scc1 .LC1l%=\ns_waitcnt vmcnt(8) lgkmcnt(6)\nv_mfma_f32_16x16x32_bf16 v[96:99], v[48:51], v[24:27], v[96:99]\nv_readfirstlane_b32 %[s0], %[pv]\n"
        "ds_read_b64_tr_b16 v[20:21], %[xa] offset:7392\nv_mfma_f32_16x16x32_bf16 v[116:119], v[52:55], v[24:27], v[116:119]\ns_cmp_ge_i32 %[s0], 6\nds_read_b64_tr_b16 v[22:23], %[xa] offset:7904\n"
        "v_mfma_f32_16x16x32_bf16 v[136:139], v[56:59], v[24:27], v[136:139]\ns_cbranch_scc0 .LC1s2_%=\n.LC1r2_%=:\nv_mfma_f32_16x16x32_bf16 v[156:159], v[60:63], v[24:27], v[156:159]\n"
        "ds_read_b64_tr_b16 v[24:25], %[xa] offset:13728\ns_waitcnt lgkmcnt(7)\nv_mfma_f32_16x16x32_bf16 v[100:103], v[48:51], v[28:31], v[100:103]\nds_read_b64_tr_b16 v[26:27], %[xa] offset:14240\n"
        "v_mfma_f32_16x16x32_bf16 v[120:123], v[52:55], v[28:31], v[120:123]\nv_mfma_f32_16x16x32_bf16 v[140:143], v[56:59], v[28:31], v[140:143]\n"
        "v_mfma_f32_16x16x32_bf16 v[160:163], v[60:63], v[28:31], v[160:163]\nds_read_b64_tr_b16 v[28:29], %[xa] offset:14784\ns_waitcnt lgkmcnt(7)\n"
        "v_mfma_f32_16x16x32_bf16 v[104:107], v[52:55], v[12:15], v[104:107]\nds_read_b64_tr_b16 v[30:31], %[xa] offset:15296\nv_mfma_f32_16x16x32_bf16 v[124:127], v[56:59], v[12:15], v[124:127]\n"
        "v_mfma_f32_16x16x32_bf16 v[144:147], v[60:63], v[12:15], v[144:147]\nds_read_b64_tr_b16 v[12:13], %[xa] offset:10560\ns_waitcnt lgkmcnt(7)\n"
        "v_mfma_f32_16x16x32_bf16 v[108:111], v[52:55], v[16:19], v[108:111]\nds_read_b64_tr_b16 v[14:15], %[xa] offset:11072\nv_mfma_f32_16x16x32_bf16 v[128:131], v[56:59], v[16:19], v[128:131]\n"
        "v_mfma_f32_16x16x32_bf16 v[148:151], v[60:63], v[16:19], v[148:151]\nds_read_b64_tr_b16 v[16:17], %[xa] offset:11616\ns_waitcnt lgkmcnt(7)\n"
        "v_mfma_f32_16x16x32_bf16 v[112:115], v[52:55], v[20:23], v[112:115]\nv_add_u32 %[sy], 4, %[sy]\nds_read_b64_tr_b16 v[18:19], %[xa] offset:12128\n"
        "v_mfma_f32_16x16x32_bf16 v[132:135], v[56:59], v[20:23], v[132:135]\nv_mfma_f32_16x16x32_bf16 v[152:155], v[60:63], v[20:23], v[152:155]\ns_waitcnt vmcnt(4) lgkmcnt(6)\n"
        "v_mfma_f32_16x16x32_bf16 v[96:99], v[64:67], v[24:27], v[96:99]\nds_read_b64_tr_b16 v[20:21], %[xa] offset:12672\nv_mfma_f32_16x16x32_bf16 v[116:119], v[68:71], v[24:27], v[116:119]\n"
        "ds_read_b64_tr_b16 v[22:23], %[xa] offset:13184\nv_mfma_f32_16x16x32_bf16 v[136:139], v[72:75], v[24:27], v[136:139]\nds_read_b32 %[pv], %[sy] offset:4\n"
        "v_mfma_f32_16x16x32_bf16 v[156:159], v[76:79], v[24:27], v[156:159]\nds_read_b64_tr_b16 v[24:25], %[xa] offset:19008\ns_waitcnt lgkmcnt(8)\n"
        "v_mfma_f32_16x16x32_bf16 v[100:103], v[64:67], v[28:31], v[100:103]\nds_read_b64_tr_b16 v[26:27], %[xa] offset:19520\nv_mfma_f32_16x16x32_bf16 v[120:123], v[68:71], v[28:31], v[120:123]\n"
        "v_mfma_f32_16x16x32_bf16 v[140:143], v[72:75], v[28:31], v[140:143]\nv_mfma_f32_16x16x32_bf16 v[160:163], v[76:79], v[28:31], v[160:163]\nds_read_b64_tr_b16 v[28:29], %[xa] offset:20064\n"
        "s_waitcnt lgkmcnt(8)\nv_mfma_f32_16x16x32_bf16 v[104:107], v[68:71], v[12:15], v[104:107]\nds_read_b64_tr_b16 v[30:31], %[xa] offset:20576\n"
        "v_mfma_f32_16x16x32_bf16 v[124:127], v[72:75], v[12:15], v[124:127]\nv_mfma_f32_16x16x32_bf16 v[144:147], v[76:79], v[12:15], v[144:147]\nds_read_b64_tr_b16 v[12:13], %[xa] offset:15840\n"
        "s_waitcnt lgkmcnt(8)\nv_mfma_f32_16x16x32_bf16 v[108:111], v[68:71], v[16:19], v[108:111]\nds_read_b64_tr_b16 v[14:15], %[xa] offset:16352\n"
        "v_mfma_f32_16x16x32_bf16 v[128:131], v[72:75], v[16:19], v[128:131]\nv_mfma_f32_16x16x32_bf16 v[148:151], v[76:79], v[16:19], v[148:151]\nds_read_b64_tr_b16 v[16:17], %[xa] offset:16896\n"
        "s_waitcnt lgkmcnt(8)\nv_mfma_f32_16x16x32_bf16 v[112:115], v[68:71], v[20:23], v[112:115]\nds_read_b64_tr_b16 v[18:19], %[xa] offset:17408\n"
        "v_mfma_f32_16x16x32_bf16 v[132:135], v[72:75], v[20:23], v[132:135]\nv_mfma_f32_16x16x32_bf16 v[152:155], v[76:79], v[20:23], v[152:155]\ns_waitcnt vmcnt(0) lgkmcnt(6)\n"
        "v_mfma_f32_16x16x32_bf16 v[96:99], v[80:83], v[24:27], v[96:99]\nv_readfirstlane_b32 %[s0], %[pv]\nds_read_b64_tr_b16 v[20:21], %[xa] offset:17952\n"
        "v_mfma_f32_16x16x32_bf16 v[116:119], v[84:87], v[24:27], v[116:119]\nds_read_b64_tr_b16 v[22:23], %[xa] offset:18464\nv_mfma_f32_16x16x32_bf16 v[136:139], v[88:91], v[24:27], v[136:139]\n"
        "s_cmp_ge_i32 %[s0], %[nst]\nv_mfma_f32_16x16x32_bf16 v[156:159], v[92:95], v[24:27], v[156:159]\ns_cbranch_scc0 .LC1t%=\n.LC1u%=:\ns_waitcnt lgkmcnt(6)\n"
        "v_mfma_f32_16x16x32_bf16 v[100:103], v[80:83], v[28:31], v[100:103]\nv_add_u32 %[sy], 4, %[sy]\nv_mfma_f32_16x16x32_bf16 v[120:123], v[84:87], v[28:31], v[120:123]\n"
        "ds_read_b64 v[32:33], %[cwa] offset:896\nv_mfma_f32_16x16x32_bf16 v[140:143], v[88:91], v[28:31], v[140:143]\nds_read_b64 v[36:37], %[cwa] offset:512\n"
        "v_mfma_f32_16x16x32_bf16 v[160:163], v[92:95], v[28:31], v[160:163]\nds_read_b64 v[40:41], %[cwa] offset:1664\ns_waitcnt lgkmcnt(7)\n"
        "v_mfma_f32_16x16x32_bf16 v[104:107], v[84:87], v[12:15], v[104:107]\nds_read_b64 v[44:45], %[cwa] offset:1792\nv_mfma_f32_16x16x32_bf16 v[124:127], v[88:91], v[12:15], v[124:127]\n"
        "ds_read_b128 v[48:51], %[mra] offset:0\nv_mfma_f32_16x16x32_bf16 v[144:147], v[92:95], v[12:15], v[144:147]\nds_read_b128 v[52:55], %[mra] offset:256\ns_waitcnt lgkmcnt(8)\n"
        "v_mfma_f32_16x16x32_bf16 v[108:111], v[84:87], v[16:19], v[108:111]\nds_read_b128 v[56:59], %[mra] offset:512\nv_mfma_f32_16x16x32_bf16 v[128:131], v[88:91], v[16:19], v[128:131]\n"
        "ds_read_b128 v[60:63], %[mra] offset:768\nv_mfma_f32_16x16x32_bf16 v[148:151], v[92:95], v[16:19], v[148:151]\nds_read_b128 v[64:67], %[mra] offset:1024\ns_waitcnt lgkmcnt(9)\n"
        "v_mfma_f32_16x16x32_bf16 v[112:115], v[84:87], v[20:23], v[112:115]\nv_add_u32 %[sy], 4, %[sy]\nv_mfma_f32_16x16x32_bf16 v[132:135], v[88:91], v[20:23], v[132:135]\ns_setprio %[pfs]\n"
        "v_mfma_f32_16x16x32_bf16 v[152:155], v[92:95], v[20:23], v[152:155]\ns_waitcnt lgkmcnt(0)\nv_mfma_f32_16x16x32_bf16 v[96:99], v[32:35], v[60:63], v[96:99]\n"
        "v_mfma_f32_16x16x32_bf16 v[100:103], v[32:35], v[64:67], v[100:103]\nv_mfma_f32_16x16x32_bf16 v[116:119], v[36:39], v[60:63], v[116:119]\n"
        "v_mfma_f32_16x16x32_bf16 v[120:123], v[36:39], v[64:67], v[120:123]\nv_mfma_f32_16x16x32_bf16 v[104:107], v[36:39], v[48:51], v[104:107]\nv_cvt_pk_bf16_f32 v96, v96, v97\n"
        "v_cvt_pk_bf16_f32 v97, v98, v99\nv_cvt_pk_bf16_f32 v98, v100, v101\nv_cvt_pk_bf16_f32 v99, v102, v103\nv_mfma_f32_16x16x32_bf16 v[108:111], v[36:39], v[52:55], v[108:111]\n"
        "v_permlane16_swap_b32 v96, v98\nv_permlane16_swap_b32 v97, v99\nds_write_b128 %[stb], v[96:99] offset:24032\nv_mfma_f32_16x16x32_bf16 v[112:115], v[36:39], v[56:59], v[112:115]\n"
        "v_cvt_pk_bf16_f32 v116, v116, v117\nv_cvt_pk_bf16_f32 v117, v118, v119\nv_cvt_pk_bf16_f32 v118, v120, v121\nv_cvt_pk_bf16_f32 v119, v122, v123\n"
        "v_mfma_f32_16x16x32_bf16 v[136:139], v[40:43], v[60:63], v[136:139]\nv_permlane16_swap_b32 v116, v118\nv_permlane16_swap_b32 v117, v119\nds_write_b128 %[stb], v[116:119] offset:23936\n"
        "v_cvt_pk_bf16_f32 v104, v104, v105\nv_mfma_f32_16x16x32_bf16 v[140:143], v[40:43], v[64:67], v[140:143]\nv_cvt_pk_bf16_f32 v105, v106, v107\nds_write_b64 %[sta], v[104:105] offset:128\n"
        "v_mfma_f32_16x16x32_bf16 v[156:159], v[44:47], v[60:63], v[156:159]\nv_cvt_pk_bf16_f32 v108, v108, v109\nv_cvt_pk_bf16_f32 v109, v110, v111\nv_cvt_pk_bf16_f32 v110, v112, v113\n"
        "v_cvt_pk_bf16_f32 v111, v114, v115\nv_mfma_f32_16x16x32_bf16 v[160:163], v[44:47], v[64:67], v[160:163]\nv_permlane16_swap_b32 v108, v110\nv_permlane16_swap_b32 v109, v111\n"
        "ds_write_b128 %[stb], v[108:111] offset:8064\nds_write_b32 %[pg], %[sy]\nv_mfma_f32_16x16x32_bf16 v[124:127], v[40:43], v[48:51], v[124:127]\nv_add_u32 %[sy], 4, %[sy]\n"
        "v_cvt_pk_bf16_f32 v136, v136, v137\nv_cvt_pk_bf16_f32 v137, v138, v139\nv_cvt_pk_bf16_f32 v138, v140, v141\nv_mfma_f32_16x16x32_bf16 v[144:147], v[44:47], v[48:51], v[144:147]\n"
        "v_cvt_pk_bf16_f32 v139, v142, v143\ns_nop 0\nv_permlane16_swap_b32 v136, v138\nv_permlane16_swap_b32 v137, v139\nv_mfma_f32_16x16x32_bf16 v[128:131], v[40:43], v[52:55], v[128:131]\n"
        "ds_write_b128 %[stb], v[136:139] offset:24224\nv_cvt_pk_bf16_f32 v156, v156, v157\nv_cvt_pk_bf16_f32 v157, v158, v159\nv_cvt_pk_bf16_f32 v158, v160, v161\n"
        "v_mfma_f32_16x16x32_bf16 v[132:135], v[40:43], v[56:59], v[132:135]\nv_cvt_pk_bf16_f32 v159, v162, v163\ns_nop 0\nv_permlane16_swap_b32 v156, v158\nv_permlane16_swap_b32 v157, v159\n"
        "v_mfma_f32_16x16x32_bf16 v[148:151], v[44:47], v[52:55], v[148:151]\nds_write_b128 %[stb], v[156:159] offset:24256\nv_cvt_pk_bf16_f32 v124, v124, v125\nv_cvt_pk_bf16_f32 v125, v126, v127\n"
        "ds_write_b64 %[sta], v[124:125] offset:416\nv_mfma_f32_16x16x32_bf16 v[152:155], v[44:47], v[56:59], v[152:155]\nv_cvt_pk_bf16_f32 v144, v144, v145\nv_cvt_pk_bf16_f32 v145, v146, v147\n"
        "ds_write_b64 %[sta], v[144:145] offset:448\ns_nop 3\nv_cvt_pk_bf16_f32 v128, v128, v129\nv_cvt_pk_bf16_f32 v129, v130, v131\nv_cvt_pk_bf16_f32 v130, v132, v133\n"
        "v_cvt_pk_bf16_f32 v131, v134, v135\ns_nop 0\nv_permlane16_swap_b32 v128, v130\nv_permlane16_swap_b32 v129, v131\nds_write_b128 %[stb], v[128:131] offset:8352\n"
        "v_cvt_pk_bf16_f32 v148, v148, v149\nv_cvt_pk_bf16_f32 v149, v150, v151\nv_cvt_pk_bf16_f32 v150, v152, v153\nv_cvt_pk_bf16_f32 v151, v154, v155\ns_nop 0\nv_permlane16_swap_b32 v148, v150\n"
        "v_permlane16_swap_b32 v149, v151\nds_write_b128 %[stb], v[148:151] offset:8384\nds_write_b32 %[pg], %[sy]\ns_branch .LE%=\n.LC1s0_%=:\ns_mov_b32 %[s1], 0x100000\n.LC1p0_%=:\n"
        "ds_read_b32 %[pv], %[sy] offset:4\ns_waitcnt lgkmcnt(0)\nv_readfirstlane_b32 %[s0], %[pv]\ns_nop 0\ns_cmp_ge_i32 %[s0], 6\ns_cbranch_scc1 .LC1r0_%=\ns_sleep 1\ns_sub_u32 %[s1], %[s1], 1\n"
        "s_cbranch_scc0 .LC1p0_%=\ns_branch .LC1r0_%=\n.LC1s1_%=:\ns_mov_b32 %[s1], 0x100000\n.LC1p1_%=:\nds_read_b32 %[pv], %[sy] offset:4\ns_waitcnt lgkmcnt(0)\nv_readfirstlane_b32 %[s0], %[pv]\n"
        "s_nop 0\ns_cmp_ge_i32 %[s0], 6\ns_cbranch_scc1 .LC1r1_%=\ns_sleep 1\ns_sub_u32 %[s1], %[s1], 1\ns_cbranch_scc0 .LC1p1_%=\ns_branch .LC1r1_%=\n.LC1s2_%=:\ns_mov_b32 %[s1], 0x100000\n"
        ".LC1p2_%=:\nds_read_b32 %[pv], %[sy] offset:4\ns_waitcnt lgkmcnt(0)\nv_readfirstlane_b32 %[s0], %[pv]\ns_nop 0\ns_cmp_ge_i32 %[s0], 6\ns_cbranch_scc1 .LC1r2_%=\ns_sleep 1\n"
        "s_sub_u32 %[s1], %[s1], 1\ns_cbranch_scc0 .LC1p2_%=\ns_branch .LC1r2_%=\n.LC1t%=:\ns_mov_b32 %[s1], 0x100000\n.LC1q%=:\nds_read_b32 %[pv], %[sy] offset:4\ns_waitcnt lgkmcnt(0)\n"
        "v_readfirstlane_b32 %[s0], %[pv]\ns_nop 0\ns_cmp_ge_i32 %[s0], %[nst]\ns_cbranch_scc1 .LC1u%=\ns_sleep 1\ns_sub_u32 %[s1], %[s1], 1\ns_cbranch_scc0 .LC1q%=\ns_branch .LC1u%=\n.LE%=:\n"
        : [pv] "=&v"(pv), [xa] "+v"(xa), [sy] "+v"(sy), [so] "=&s"(so), [s0] "=&s"(s0_), [s1] "=&s"(s1_)
        : [wa] "v"(wa), [rs] "s"(wrsrc), [pg] "v"(pga), [cwa] "v"(cwa), [mra] "v"(mra), [sta] "v"(sta), [stb] "v"(stb), [w] "s"(w), [ow] "i"(SY_W - SY_X), [pfs] "i"(PFS), [pf] "i"(PF), [nst] "i"(NST1)
        : "memory", "scc", "v12", "v13", "v14", "v15", "v16", "v17", "v18", "v19", "v20", "v21", "v22", "v23", "v24", "v25", "v26", "v27", "v28", "v29", "v30", "v31", "v32", "v33", "v34", "v35", "v36", "v37", "v38", "v39", "v40", "v41", "v42", "v43", "v44", "v45", "v46", "v47", "v48", "v49", "v50", "v51", "v52", "v53", "v54", "v55", "v56", "v57", "v58", "v59", "v60", "v61", "v62", "v63", "v64", "v65", "v66", "v67", "v68", "v69", "v70", "v71", "v72", "v73", "v74", "v75", "v76", "v77", "v78", "v79", "v80", "v81", "v82", "v83", "v84", "v85", "v86", "v87", "v88", "v89", "v90", "v91", "v92", "v93", "v94", "v95", "v96", "v97", "v98", "v99", "v100", "v101", "v102", "v103", "v104", "v105", "v106", "v107", "v108", "v109", "v110", "v111", "v112", "v113", "v114", "v115", "v116", "v117", "v118", "v119", "v120", "v121", "v122", "v123", "v124", "v125", "v126", "v127", "v128", "v129", "v130", "v131", "v132", "v133", "v134", "v135", "v136", "v137", "v138", "v139", "v140", "v141", "v142", "v143", "v144", "v145", "v146", "v147", "v148", "v149", "v150", "v151", "v152", "v153", "v154", "v155", "v156", "v157", "v158", "v159", "v160", "v161", "v162", "v163", "v164", "v165", "v166", "v167");
    }
    }
    ST(5);
    LANE(lane);
    const int t = 64 * w + lane;
    if (!layer && w == 3) {
      int spins = 0;
      _Pragma("unroll 1")
      while (true) {
        asm volatile("" ::: "memory");
        u32 d = 0;
#pragma unroll
        for (int i = 0; i < 2; ++i) {
          const u32x4 f = __builtin_amdgcn_raw_buffer_load_b128(frs, lane * 16, FL_M2 + i * 1024, 16);
          d |= (f[0] ^ tg0) | (f[1] ^ tg1) | (f[2] ^ tg0) | (f[3] ^ tg1);
        }
        if (__builtin_amdgcn_ballot_w64(d != 0) == 0 || ++spins > GCAP) break;
        __builtin_amdgcn_s_sleep(8);
      }
      VMCNT(0);
      ST(8);
    }
    if (layer && b == 0 && w == 1) {
      if (lane == 0) __builtin_amdgcn_raw_buffer_store_b64((u32x2){tg0, tg1}, frs, 0, 0, 16);
    }
    ST_FLUSH(kid);
    return;
  }
  const int j = w - 4, grp = j >> 2, mt2 = j & 3, kh = grp, qt = mt2, tt = t - 256;
  u32 l16 = lane * 16;
  int xso = g == (l15 >> 2) ? (rb * 8 + hg) * 1024 + mt2 * 256 + l15 * 16 : -1;
  const u32 tq1 = tg0 + tg1;
  if (layer) {
    const __amdgpu_buffer_rsrc_t xrs = __builtin_amdgcn_make_buffer_rsrc((void*)(xt + (size_t)rb * 51200), 0, 102400, 0x00020000);
    int gp[5], gh[3];
#pragma unroll
    for (int i = 0; i < 5; ++i) { const int p = i < 4 ? 4 * (i >> 1) + (i & 1) + 2 * grp : 8 + grp; gp[i] = PAIR(p, rot) * 10240; }
    const int ho = (mt2 & 1) * 5120 + (rb ? 4096 : 0);
    gh[0] = (mt2 & 2 ? gp[1] : gp[0]) + ho; gh[1] = (mt2 & 2 ? gp[3] : gp[2]) + ho; gh[2] = mt2 & 2 ? 102400 : gp[4] + ho;
    const int ck = (lane & 7) ^ ((lane >> 3) & 6);
    u32 vm0 = l16 + mt2 * 1024, vm1 = vm0 + 5120;
    u32 lm0 = LDSA(At) + grp * 21120 + (1 + (ck >> 1)) * 1056 + (8 * mt2 + (lane >> 3)) * 32 + (ck & 1) * 16, lm1 = lm0 + 84480 - grp * 10560;
    u32 lh0 = LDSA(At) + j * 5280 + l16, lh1 = lh0 + 84480 - grp * 10560, sy0 = sya + 8 * grp, sy1 = sya + 32 + 4 * grp, syh = sy0 + 2 * (mt2 & 2), tm;
    u32 xsb = LDSA(At) + (mt2 + 1) * 1056 + (4 * g + ((lane >> 2) & 3)) * 32 + 8 * (lane & 3), xsa = xsb + (12 + (hg & 1)) * 5280;
    xsb += xs0 * 5280;
    const int ns = hg;
    const u64 m1 = 0xaaaaaaaaaaaaaaaaull, m2 = 0xccccccccccccccccull;
    int cap, s0_, s1_;
    __builtin_amdgcn_s_setprio(PTX);
    if (grp) __builtin_amdgcn_s_sleep(XSLEEP1); else __builtin_amdgcn_s_sleep(XSLEEP);
    u32x4 xq[5];
      asm volatile(
        "buffer_load_dwordx4 v[44:47], %[vm0], %[xrs], %[gp0] offen sc1\nbuffer_load_dwordx4 v[48:51], %[vm1], %[xrs], %[gp0] offen sc1\nbuffer_load_dwordx4 v[52:55], %[l16], %[xrs], %[gh0] offen sc1\n"
        "buffer_load_dwordx4 v[56:59], %[vm0], %[xrs], %[gp1] offen sc1\n"
        : "=&{v[44:47]}"(xq[0]), "=&{v[48:51]}"(xq[1]), "=&{v[52:55]}"(xq[2]), "=&{v[56:59]}"(xq[3])
        : [l16] "v"(l16), [vm0] "v"(vm0), [vm1] "v"(vm1), [xrs] "s"(xrs), [gp0] "s"(gp[0]), [gp1] "s"(gp[1]), [gh0] "s"(gh[0])
        : "memory", "scc", "vcc");
      asm volatile(
        "s_mov_b32 %[cap], 0x10000\ns_mov_b32 %[s1], 0x100000\n.LB%=:\nds_read_b32 %[tm], %[sy] offset:%[obd]\ns_waitcnt lgkmcnt(0)\nv_readfirstlane_b32 %[s0], %[tm]\ns_nop 0\ns_cmp_ge_i32 %[s0], 12\n"
        "s_cbranch_scc1 .LP0_%=\ns_sleep 1\ns_sub_u32 %[s1], %[s1], 1\ns_cbranch_scc0 .LB%=\n.LP0_%=:\ns_waitcnt vmcnt(3)\nv_max3_u32 %[tm], v44, v45, v46\nv_max_u32 %[tm], %[tm], v47\n"
        "v_cmp_eq_u32 vcc, -1, %[tm]\ns_cbranch_vccnz .LR0_%=\n.LK0_%=:\nds_write_b128 %[lm0], v[44:47] offset:0\nbuffer_load_dwordx4 v[60:63], %[vm1], %[xrs], %[gp1] offen sc1\n.LP1_%=:\n"
        "s_waitcnt vmcnt(3)\nv_max3_u32 %[tm], v48, v49, v50\nv_max_u32 %[tm], %[tm], v51\nv_cmp_eq_u32 vcc, -1, %[tm]\ns_cbranch_vccnz .LR1_%=\n.LK1_%=:\nds_write_b128 %[lm0], v[48:51] offset:5280\n"
        "s_mov_b64 exec, 1\nds_add_u32 %[sy0], %[one] offset:%[ox]+0\ns_mov_b64 exec, -1\nbuffer_load_dwordx4 v[44:47], %[vm0], %[xrs], %[gp2] offen sc1\n.LP2_%=:\ns_waitcnt vmcnt(3)\n"
        "v_max3_u32 %[tm], v52, v53, v54\nv_max_u32 %[tm], %[tm], v55\nv_cmp_eq_u32 vcc, -1, %[tm]\ns_cbranch_vccnz .LR2_%=\n.LK2_%=:\nds_write_b128 %[lh0], v[52:55] offset:0\ns_mov_b64 exec, 1\n"
        "ds_add_u32 %[syh], %[one] offset:%[ox]+0\ns_mov_b64 exec, -1\nbuffer_load_dwordx4 v[48:51], %[vm1], %[xrs], %[gp2] offen sc1\n.LP3_%=:\ns_waitcnt vmcnt(3)\nv_max3_u32 %[tm], v56, v57, v58\n"
        "v_max_u32 %[tm], %[tm], v59\nv_cmp_eq_u32 vcc, -1, %[tm]\ns_cbranch_vccnz .LR3_%=\n.LK3_%=:\nds_write_b128 %[lm0], v[56:59] offset:10560\n"
        "buffer_load_dwordx4 v[52:55], %[l16], %[xrs], %[gh1] offen sc1\n.LP4_%=:\ns_waitcnt vmcnt(3)\nv_max3_u32 %[tm], v60, v61, v62\nv_max_u32 %[tm], %[tm], v63\nv_cmp_eq_u32 vcc, -1, %[tm]\n"
        "s_cbranch_vccnz .LR4_%=\n.LK4_%=:\nds_write_b128 %[lm0], v[60:63] offset:15840\ns_mov_b64 exec, 1\nds_add_u32 %[sy0], %[one] offset:%[ox]+4\ns_mov_b64 exec, -1\n"
        "buffer_load_dwordx4 v[56:59], %[vm0], %[xrs], %[gp3] offen sc1\n.LP5_%=:\ns_waitcnt vmcnt(3)\nv_max3_u32 %[tm], v44, v45, v46\nv_max_u32 %[tm], %[tm], v47\nv_cmp_eq_u32 vcc, -1, %[tm]\n"
        "s_cbranch_vccnz .LR5_%=\n.LK5_%=:\nds_write_b128 %[lm0], v[44:47] offset:42240\nbuffer_load_dwordx4 v[60:63], %[vm1], %[xrs], %[gp3] offen sc1\n.LP6_%=:\ns_waitcnt vmcnt(3)\n"
        "v_max3_u32 %[tm], v48, v49, v50\nv_max_u32 %[tm], %[tm], v51\nv_cmp_eq_u32 vcc, -1, %[tm]\ns_cbranch_vccnz .LR6_%=\n.LK6_%=:\nds_write_b128 %[lm0], v[48:51] offset:47520\ns_mov_b64 exec, 1\n"
        "ds_add_u32 %[sy0], %[one] offset:%[ox]+16\ns_mov_b64 exec, -1\nbuffer_load_dwordx4 v[44:47], %[vm0], %[xrs], %[gp4] offen sc1\n.LP7_%=:\ns_waitcnt vmcnt(3)\nv_max3_u32 %[tm], v52, v53, v54\n"
        "v_max_u32 %[tm], %[tm], v55\nv_cmp_eq_u32 vcc, -1, %[tm]\ns_cbranch_vccnz .LR7_%=\n.LK7_%=:\nds_write_b128 %[lh0], v[52:55] offset:42240\ns_mov_b64 exec, 1\n"
        "ds_add_u32 %[syh], %[one] offset:%[ox]+16\ns_mov_b64 exec, -1\nbuffer_load_dwordx4 v[48:51], %[vm1], %[xrs], %[gp4] offen sc1\n.LP8_%=:\ns_waitcnt vmcnt(3)\nv_max3_u32 %[tm], v56, v57, v58\n"
        "v_max_u32 %[tm], %[tm], v59\nv_cmp_eq_u32 vcc, -1, %[tm]\ns_cbranch_vccnz .LR8_%=\n.LK8_%=:\nds_write_b128 %[lm0], v[56:59] offset:52800\n"
        "buffer_load_dwordx4 v[52:55], %[l16], %[xrs], %[gh2] offen sc1\ns_cmp_lt_u32 %[j], 4\ns_cbranch_scc1 .LT%=\ns_mov_b32 %[s1], 0x100000\n.LU%=:\nds_read_b128 v[36:39], %[sy0] offset:%[ox]-8\n"
        "ds_read_b128 v[40:43], %[sy0] offset:%[ox]+8\nds_read_b64_tr_b16 v[28:29], %[xsa]\nds_read_b64_tr_b16 v[30:31], %[xsa] offset:512\nds_read_b64_tr_b16 v[32:33], %[xsb]\n"
        "ds_read_b64_tr_b16 v[34:35], %[xsb] offset:512\ns_waitcnt lgkmcnt(4)\nv_min3_u32 v36, v36, v37, v38\nv_min3_u32 v40, v40, v41, v42\nv_min3_u32 v36, v36, v39, v40\ns_nop 0\n"
        "v_readfirstlane_b32 %[s0], v36\ns_waitcnt lgkmcnt(0)\ns_cmp_ge_i32 %[s0], 6\ns_cbranch_scc1 .LV%=\ns_sleep 1\ns_sub_u32 %[s1], %[s1], 1\ns_cbranch_scc0 .LU%=\n.LV%=:\n"
        "v_mfma_f32_16x16x32_bf16 v[36:39], %[ones], v[28:31], 0\nv_mfma_f32_16x16x32_bf16 v[40:43], v[28:31], v[28:31], 0\nds_read_b64_tr_b16 v[28:29], %[xsb] offset:5280\n"
        "ds_read_b64_tr_b16 v[30:31], %[xsb] offset:5792\ns_bitcmp1_b32 %[ns], 0\nv_mfma_f32_16x16x32_bf16 v[36:39], %[ones], v[32:35], v[36:39]\n"
        "v_mfma_f32_16x16x32_bf16 v[40:43], v[32:35], v[32:35], v[40:43]\ns_waitcnt lgkmcnt(0)\ns_cbranch_scc1 .LW%=\nv_mfma_f32_16x16x32_bf16 v[36:39], %[ones], v[28:31], v[36:39]\n"
        "v_mfma_f32_16x16x32_bf16 v[40:43], v[28:31], v[28:31], v[40:43]\n.LW%=:\ns_nop 7\nv_cndmask_b32 v40, v40, v41, %[m1]\nv_cndmask_b32 v42, v42, v43, %[m1]\nv_mov_b32 v37, %[tg0]\n"
        "v_mov_b32 v39, %[tq1]\nv_cndmask_b32 v38, v40, v42, %[m2]\nbuffer_store_dwordx4 v[36:39], %[xso], %[srs], 0 offen sc1\n.LT%=:\n.LP9_%=:\ns_waitcnt vmcnt(3)\nv_max3_u32 %[tm], v60, v61, v62\n"
        "v_max_u32 %[tm], %[tm], v63\nv_cmp_eq_u32 vcc, -1, %[tm]\ns_cbranch_vccnz .LR9_%=\n.LK9_%=:\nds_write_b128 %[lm0], v[60:63] offset:58080\ns_mov_b64 exec, 1\n"
        "ds_add_u32 %[sy0], %[one] offset:%[ox]+20\ns_mov_b64 exec, -1\n.LP10_%=:\ns_waitcnt vmcnt(2)\nv_max3_u32 %[tm], v44, v45, v46\nv_max_u32 %[tm], %[tm], v47\nv_cmp_eq_u32 vcc, -1, %[tm]\n"
        "s_cbranch_vccnz .LR10_%=\n.LK10_%=:\nds_write_b128 %[lm1], v[44:47] offset:0\n.LP11_%=:\ns_waitcnt vmcnt(1)\nv_max3_u32 %[tm], v48, v49, v50\nv_max_u32 %[tm], %[tm], v51\n"
        "v_cmp_eq_u32 vcc, -1, %[tm]\ns_cbranch_vccnz .LR11_%=\n.LK11_%=:\nds_write_b128 %[lm1], v[48:51] offset:5280\ns_mov_b64 exec, 1\nds_add_u32 %[sy1], %[one] offset:%[ox]+0\ns_mov_b64 exec, -1\n"
        ".LP12_%=:\ns_waitcnt vmcnt(0)\nv_max3_u32 %[tm], v52, v53, v54\nv_max_u32 %[tm], %[tm], v55\nv_cmp_eq_u32 vcc, -1, %[tm]\ns_cbranch_vccnz .LR12_%=\n.LK12_%=:\ns_bitcmp1_b32 %[j], 1\n"
        "s_cbranch_scc1 .LE%=\nds_write_b128 %[lh1], v[52:55] offset:0\ns_mov_b64 exec, 1\nds_add_u32 %[sy1], %[one] offset:%[ox]\ns_mov_b64 exec, -1\ns_branch .LE%=\n.LR0_%=:\n"
        "s_sub_u32 %[cap], %[cap], 1\ns_cbranch_scc0 .LQ0_%=\ns_mov_b32 %[cap], 0\ns_branch .LK0_%=\n.LQ0_%=:\ns_sleep %[xp]\nbuffer_load_dwordx4 v[44:47], %[vm0], %[xrs], %[gp0] offen sc1\n"
        "buffer_load_dwordx4 v[48:51], %[vm1], %[xrs], %[gp0] offen sc1\nbuffer_load_dwordx4 v[52:55], %[l16], %[xrs], %[gh0] offen sc1\nbuffer_load_dwordx4 v[56:59], %[vm0], %[xrs], %[gp1] offen sc1\n"
        "s_branch .LP0_%=\n.LR1_%=:\ns_sub_u32 %[cap], %[cap], 1\ns_cbranch_scc0 .LQ1_%=\ns_mov_b32 %[cap], 0\ns_branch .LK1_%=\n.LQ1_%=:\ns_sleep %[xp]\n"
        "buffer_load_dwordx4 v[48:51], %[vm1], %[xrs], %[gp0] offen sc1\nbuffer_load_dwordx4 v[52:55], %[l16], %[xrs], %[gh0] offen sc1\nbuffer_load_dwordx4 v[56:59], %[vm0], %[xrs], %[gp1] offen sc1\n"
        "buffer_load_dwordx4 v[60:63], %[vm1], %[xrs], %[gp1] offen sc1\ns_branch .LP1_%=\n.LR2_%=:\ns_sub_u32 %[cap], %[cap], 1\ns_cbranch_scc0 .LQ2_%=\ns_mov_b32 %[cap], 0\ns_branch .LK2_%=\n"
        ".LQ2_%=:\ns_sleep %[xp]\nbuffer_load_dwordx4 v[52:55], %[l16], %[xrs], %[gh0] offen sc1\nbuffer_load_dwordx4 v[56:59], %[vm0], %[xrs], %[gp1] offen sc1\n"
        "buffer_load_dwordx4 v[60:63], %[vm1], %[xrs], %[gp1] offen sc1\nbuffer_load_dwordx4 v[44:47], %[vm0], %[xrs], %[gp2] offen sc1\ns_branch .LP2_%=\n.LR3_%=:\ns_sub_u32 %[cap], %[cap], 1\n"
        "s_cbranch_scc0 .LQ3_%=\ns_mov_b32 %[cap], 0\ns_branch .LK3_%=\n.LQ3_%=:\ns_sleep %[xp]\nbuffer_load_dwordx4 v[56:59], %[vm0], %[xrs], %[gp1] offen sc1\n"
        "buffer_load_dwordx4 v[60:63], %[vm1], %[xrs], %[gp1] offen sc1\nbuffer_load_dwordx4 v[44:47], %[vm0], %[xrs], %[gp2] offen sc1\nbuffer_load_dwordx4 v[48:51], %[vm1], %[xrs], %[gp2] offen sc1\n"
        "s_branch .LP3_%=\n.LR4_%=:\ns_sub_u32 %[cap], %[cap], 1\ns_cbranch_scc0 .LQ4_%=\ns_mov_b32 %[cap], 0\ns_branch .LK4_%=\n.LQ4_%=:\ns_sleep %[xp]\n"
        "buffer_load_dwordx4 v[60:63], %[vm1], %[xrs], %[gp1] offen sc1\nbuffer_load_dwordx4 v[44:47], %[vm0], %[xrs], %[gp2] offen sc1\nbuffer_load_dwordx4 v[48:51], %[vm1], %[xrs], %[gp2] offen sc1\n"
        "buffer_load_dwordx4 v[52:55], %[l16], %[xrs], %[gh1] offen sc1\ns_branch .LP4_%=\n.LR5_%=:\ns_sub_u32 %[cap], %[cap], 1\ns_cbranch_scc0 .LQ5_%=\ns_mov_b32 %[cap], 0\ns_branch .LK5_%=\n"
        ".LQ5_%=:\ns_sleep %[xp]\nbuffer_load_dwordx4 v[44:47], %[vm0], %[xrs], %[gp2] offen sc1\nbuffer_load_dwordx4 v[48:51], %[vm1], %[xrs], %[gp2] offen sc1\n"
        "buffer_load_dwordx4 v[52:55], %[l16], %[xrs], %[gh1] offen sc1\nbuffer_load_dwordx4 v[56:59], %[vm0], %[xrs], %[gp3] offen sc1\ns_branch .LP5_%=\n.LR6_%=:\ns_sub_u32 %[cap], %[cap], 1\n"
        "s_cbranch_scc0 .LQ6_%=\ns_mov_b32 %[cap], 0\ns_branch .LK6_%=\n.LQ6_%=:\ns_sleep %[xp]\nbuffer_load_dwordx4 v[48:51], %[vm1], %[xrs], %[gp2] offen sc1\n"
        "buffer_load_dwordx4 v[52:55], %[l16], %[xrs], %[gh1] offen sc1\nbuffer_load_dwordx4 v[56:59], %[vm0], %[xrs], %[gp3] offen sc1\nbuffer_load_dwordx4 v[60:63], %[vm1], %[xrs], %[gp3] offen sc1\n"
        "s_branch .LP6_%=\n.LR7_%=:\ns_sub_u32 %[cap], %[cap], 1\ns_cbranch_scc0 .LQ7_%=\ns_mov_b32 %[cap], 0\ns_branch .LK7_%=\n.LQ7_%=:\ns_sleep %[xp]\n"
        "buffer_load_dwordx4 v[52:55], %[l16], %[xrs], %[gh1] offen sc1\nbuffer_load_dwordx4 v[56:59], %[vm0], %[xrs], %[gp3] offen sc1\nbuffer_load_dwordx4 v[60:63], %[vm1], %[xrs], %[gp3] offen sc1\n"
        "buffer_load_dwordx4 v[44:47], %[vm0], %[xrs], %[gp4] offen sc1\ns_branch .LP7_%=\n.LR8_%=:\ns_sub_u32 %[cap], %[cap], 1\ns_cbranch_scc0 .LQ8_%=\ns_mov_b32 %[cap], 0\ns_branch .LK8_%=\n"
        ".LQ8_%=:\ns_sleep %[xp]\nbuffer_load_dwordx4 v[56:59], %[vm0], %[xrs], %[gp3] offen sc1\nbuffer_load_dwordx4 v[60:63], %[vm1], %[xrs], %[gp3] offen sc1\n"
        "buffer_load_dwordx4 v[44:47], %[vm0], %[xrs], %[gp4] offen sc1\nbuffer_load_dwordx4 v[48:51], %[vm1], %[xrs], %[gp4] offen sc1\ns_branch .LP8_%=\n.LR9_%=:\ns_sub_u32 %[cap], %[cap], 1\n"
        "s_cbranch_scc0 .LQ9_%=\ns_mov_b32 %[cap], 0\ns_branch .LK9_%=\n.LQ9_%=:\ns_sleep %[xp]\nbuffer_load_dwordx4 v[60:63], %[vm1], %[xrs], %[gp3] offen sc1\n"
        "buffer_load_dwordx4 v[44:47], %[vm0], %[xrs], %[gp4] offen sc1\nbuffer_load_dwordx4 v[48:51], %[vm1], %[xrs], %[gp4] offen sc1\nbuffer_load_dwordx4 v[52:55], %[l16], %[xrs], %[gh2] offen sc1\n"
        "s_branch .LP9_%=\n.LR10_%=:\ns_sub_u32 %[cap], %[cap], 1\ns_cbranch_scc0 .LQ10_%=\ns_mov_b32 %[cap], 0\ns_branch .LK10_%=\n.LQ10_%=:\ns_sleep %[xp]\n"
        "buffer_load_dwordx4 v[44:47], %[vm0], %[xrs], %[gp4] offen sc1\nbuffer_load_dwordx4 v[48:51], %[vm1], %[xrs], %[gp4] offen sc1\nbuffer_load_dwordx4 v[52:55], %[l16], %[xrs], %[gh2] offen sc1\n"
        "s_branch .LP10_%=\n.LR11_%=:\ns_sub_u32 %[cap], %[cap], 1\ns_cbranch_scc0 .LQ11_%=\ns_mov_b32 %[cap], 0\ns_branch .LK11_%=\n.LQ11_%=:\ns_sleep %[xp]\n"
        "buffer_load_dwordx4 v[48:51], %[vm1], %[xrs], %[gp4] offen sc1\nbuffer_load_dwordx4 v[52:55], %[l16], %[xrs], %[gh2] offen sc1\ns_branch .LP11_%=\n.LR12_%=:\ns_sub_u32 %[cap], %[cap], 1\n"
        "s_cbranch_scc0 .LQ12_%=\ns_mov_b32 %[cap], 0\ns_branch .LK12_%=\n.LQ12_%=:\ns_sleep %[xp]\nbuffer_load_dwordx4 v[52:55], %[l16], %[xrs], %[gh2] offen sc1\ns_branch .LP12_%=\n.LE%=:\n"
        : [tm] "=&v"(tm), [cap] "=&s"(cap), [s0] "=&s"(s0_), [s1] "=&s"(s1_), "+{v[44:47]}"(xq[0]), "+{v[48:51]}"(xq[1]), "+{v[52:55]}"(xq[2]), "+{v[56:59]}"(xq[3])
        : [l16] "v"(l16), [vm0] "v"(vm0), [vm1] "v"(vm1), [xrs] "s"(xrs), [gp0] "s"(gp[0]), [gp1] "s"(gp[1]), [gp2] "s"(gp[2]), [gp3] "s"(gp[3]), [gp4] "s"(gp[4]), [gh0] "s"(gh[0]), [gh1] "s"(gh[1]), [gh2] "s"(gh[2]), [lm0] "v"(lm0), [lm1] "v"(lm1), [lh0] "v"(lh0), [lh1] "v"(lh1), [sy0] "v"(sy0), [sy1] "v"(sy1), [syh] "v"(syh), [xsb] "v"(xsb), [one] "v"(one), [j] "s"(j), [xsa] "v"(xsa), [ns] "s"(ns), [ones] "v"(onesf), [m1] "s"(m1), [m2] "s"(m2), [xso] "v"(xso), [srs] "s"(xsrs), [tg0] "s"(tg0), [tq1] "s"(tq1), [ox] "i"(SY_X), [xp] "i"(XPOLL), [sy] "v"(sya), [obd] "i"(SY_TI)
        : "memory", "scc", "vcc", "v28", "v29", "v30", "v31", "v32", "v33", "v34", "v35", "v36", "v37", "v38", "v39", "v40", "v41", "v42", "v43", "v60", "v61", "v62", "v63");
    RETRY(8, 0x10000 - cap);
    ST(3);
  } else {

#define MARKS \
    if (w == MKW) { \
      const __amdgpu_buffer_rsrc_t mrs = __builtin_amdgcn_make_buffer_rsrc(obuf, 0, HO_BYTES, 0x00020000); \
      const __amdgpu_buffer_rsrc_t irs = __builtin_amdgcn_make_buffer_rsrc(ws + WS_IM1 + b * 12800, 0, 12800, 0x00020000); \
      __builtin_amdgcn_raw_buffer_store_b128(MARK16, mrs, lane < 16 ? (lane >> 3) * 4096 + (lane & 7) * 16 : -1, HO_FS + hg * 8192 + s0 * 2, 17); \
      _Pragma("unroll 1") \
      for (int i = 0; i < 10; ++i) { \
        __builtin_amdgcn_raw_buffer_store_b128(MARK16, mrs, (lane >> 3) * (XP * 2) + (lane & 7) * 16, HO_X1 + (hg * 80 + 8 * i) * (XP * 2) + s0 * 2, 17); \
        __builtin_amdgcn_s_sleep(MSL1); \
      } \
      _Pragma("unroll 1") \
      for (int i = 0; i < 13; ++i) { \
        __builtin_amdgcn_raw_buffer_store_b128(MARK16, irs, lane * 16, i * 1024, 17); \
        __builtin_amdgcn_s_sleep(MSL2); \
      } \
      VMCNT(0); \
      if (lane == 0) __builtin_amdgcn_raw_buffer_store_b64((u32x2){tg0, tg1}, frs, 0, FL_M2 + b * 8, 16); \
       \
    } \

    if (MKPOS == 0) MARKS
    lds_poll<SY_X, 2>(sya, 3);
    if (j == 0 ? MKPOS : j == 4 ? !MKPOS : j < 4) {
      const u32 xa = LDSA(At) + (2 * mt2 + 2 + (l15 >> 3)) * 1024 + (l15 & 7) * 128 + ((g ^ (l15 & 6)) << 4);
      bf16x8 fv[3];
      asm volatile("ds_read_b128 %0, %3 offset:61440\n\tds_read_b128 %1, %4\n\tds_read_b128 %2, %5\n\ts_waitcnt lgkmcnt(0)" : "=&v"(fv[0]), "=&v"(fv[1]), "=&v"(fv[2])
                   : "v"(xa ^ (64 * (hg & 1))), "v"((xa ^ (64 * (xs0 & 1))) + (xs0 >> 1) * 10240), "v"((xa ^ (64 * (~xs0 & 1))) + ((xs0 + 1) >> 1) * 10240) : "memory");
      f32x4 s1 = {0.f, 0.f, 0.f, 0.f}, s2 = s1;
#pragma unroll
      for (int q = 0; q < 3; ++q) {
        if (q < 2 || !(hg & 1)) {
          s1 = __builtin_amdgcn_mfma_f32_16x16x32_bf16(onesf, fv[q], s1, 0, 0, 0);
          s2 = __builtin_amdgcn_mfma_f32_16x16x32_bf16(fv[q], fv[q], s2, 0, 0, 0);
        }
      }
      const float d2 = (l15 & 2) ? ((l15 & 1) ? s2[3] : s2[2]) : ((l15 & 1) ? s2[1] : s2[0]);
      __builtin_amdgcn_raw_buffer_store_b128((u32x4){__float_as_uint(s1[0]), tg0, __float_as_uint(d2), tq1}, xsrs, xso, 0, 16);
    }
  }
  __builtin_amdgcn_s_setprio(PTW);
  if (layer) lds_inc<SY_PART>(sya, one);
  u32 ra[3];
  {
    const int rr_ = 4 * (g & 1) + ((lane >> 2) & 3), pr_ = 4 * (l15 >> 3) + (l15 & 3) + 4 * (l15 & 4);
    const u32 ro = LDSA(At) + (layer ? (mt2 + 1) * 1056 + pr_ * 32 + 8 * g : (2 * mt2 + 2 + (g >> 1)) * 1024 + rr_ * 128 + ((rr_ & 6) << 4) + (lane & 3) * 8);
#pragma unroll
    for (int i = 0; i < 3; ++i) {
      const int kc = hg * 80 + 16 * (2 * kh + i), kr = 64 * PAIR(kc >> 6, -rot) + (kc & 63);
      ra[i] = layer ? ro + (kr >> 5) * 5280 + (kr & 16) * 16 : (ro ^ ((kr & 48) * 2)) + (kr >> 6) * 10240;
      if (i == 0 && kh) ra[i] = sya + SY_Z;
      PIN(ra[i]);
    }
  }
  u32x2 rv[3];
#define RVR { lds_poll<SY_PART, 1>(sya, 8); \
    asm volatile("ds_read_b64 %0, %3\n\tds_read_b64 %1, %4\n\tds_read_b64 %2, %5\n\ts_waitcnt lgkmcnt(0)" : "=&v"(rv[0]), "=&v"(rv[1]), "=&v"(rv[2]) : "v"(ra[0]), "v"(ra[1]), "v"(ra[2]) : "memory"); \
    lds_inc<SY_RV>(sya, one); lds_inc<SY_STAT>(sya, one); }
  STX(8);
  if (!layer) {
    asm volatile("ds_read_b64_tr_b16 %0, %3\n\tds_read_b64_tr_b16 %1, %4\n\tds_read_b64_tr_b16 %2, %5\n\ts_waitcnt lgkmcnt(0)" : "=&v"(rv[0]), "=&v"(rv[1]), "=&v"(rv[2]) : "v"(ra[0]), "v"(ra[1]), "v"(ra[2]) : "memory");
    lds_inc<SY_RV>(sya, one); lds_inc<SY_STAT>(sya, one);
    if (MKPOS == 1) MARKS
  } else if (grp) RVR
  STX(10);
  __amdgpu_buffer_rsrc_t orsrc = __builtin_amdgcn_make_buffer_rsrc(obuf + (size_t)rb * 40960, 0, 81920, 0x00020000);
  u32 qka = LDSA(St) + ((16 * qt + l15) * X1_P + 160 * grp + 8 * g) * 2;
  u32 vta = LDSA(St) + ((16 * qt + 4 * g + ((lane >> 2) & 3)) * X1_P + 160 * grp + 40 + 4 * (lane & 3)) * 2;
  const bool par = l15 & 1;
  const int kk0 = 4 * g + (l15 & 1), kk1 = kk0 + 2;
  const bool c0 = kk0 >= l15 + 2, c1 = kk1 >= l15 + 2;
  const u32 e0 = par ? 0x01000c0cu : 0x0c0c0100u, e1 = par ? 0x03020c0cu : 0x0c0c0302u;
  u32 sA0 = c0 ? e0 : 0x0c0c0c0cu, sB0 = c0 ? 0x0c0c0c0cu : e0;
  u32 sA1 = c1 ? e1 : 0x0c0c0c0cu, sB1 = c1 ? 0x0c0c0c0cu : e1;
  u32 oha = g * 10240 + l15 * 16, qa[4];
#pragma unroll
  for (int i = 0; i < 4; ++i) { qa[i] = g >= 3 - (i & 1) ? sya + SY_Z : qka + (32 * X1_P + 160 - 240 * grp) + 40 * (i >> 1) + 8 * (i & 1); PIN(qa[i]); }
  const float nb = s0 + qt ? 0.f : -1e38f;
  f32x4 bias = {nb, nb, nb, nb};
  int ohs = g ? -1 : l16;
  u64 c0m = __builtin_amdgcn_ballot_w64(c0), c1m = __builtin_amdgcn_ballot_w64(c1);
  int so0 = (hg * 4 + qt) * 2560 + grp * 1024, so1 = (hg * 4 + qt) * 2560 + 2048 + grp * 256;
  float nc = 0.22360679774997896f * 1.4426950408889634f;
  u32 rq = LDSA(smem) + CW_OFF + 1920 + 64 * qt, rk0 = rq + 4 * (kk0 + (c0 ? 0 : 16)), rk1 = rq + 4 * (kk1 + (c1 ? 0 : 16));
  rq += 64 + 4 * l15;
  PIN(rq); PIN(rk0); PIN(rk1);
  asm volatile("" : "+s"(c0m), "+s"(c1m), "+s"(so0), "+s"(so1), "+s"(nc));
  PIN(qka); PIN(vta); PIN(sA0); PIN(sA1); PIN(sB0); PIN(sB1); PIN(l16); PIN(oha); PIN(sya); PIN(one); PIN(bias); PIN(ohs);
  u32 rw = LDSA(smem) + EX_OFF + j * 3072 + l16, fw = sya + SY_RED + 4 * j, f0 = 1 - kh;
  u32 xo_ = LDSA(smem + X1T_OFF) + (32 * kh + l15) * 144 + 32 * mt2 + 8 * g;
  u32 xi_ = LDSA(smem + X1T_OFF) + (tt >> 3) * 144 + (tt & 7) * 16;
  __amdgpu_buffer_rsrc_t x1rs = __builtin_amdgcn_make_buffer_rsrc(xtout + (size_t)hg * 80 * XP + s0, 0, 80 * XP * 2, 0x00020000);
  int xg_ = (tt >> 3) * (XP * 2) + (tt & 7) * 16;
  __amdgpu_buffer_rsrc_t srsrc = __builtin_amdgcn_make_buffer_rsrc(stats + (size_t)s0 * 16, 0, 8192, 0x00020000);
  u32 rr = LDSA(smem) + EX_OFF + (j ^ 4) * 3072 + l16 - 1024 * kh;
  int so_ = (l15 & 3) ? -1 : (lane >> 2) * 8;
  const int wf = layer ? 8 * ((j + 6) & 7) : j;
  u32 bb = LDSA(smem) + WO_OFF + kh * 6144 + l16, fr = fw ^ 16, wd = LDSA(smem) + WO_OFF + wf * 1024 + l16;
  PIN(rw); PIN(fw); PIN(f0); PIN(xo_); PIN(xi_); PIN(xg_); PIN(rr); PIN(so_); PIN(bb); PIN(fr); PIN(wd);
  __amdgpu_buffer_rsrc_t wos = __builtin_amdgcn_make_buffer_rsrc((void*)(wot + (size_t)hg * 51200 + wf * 512), 0, 102400 - wf * 1024, 0x00020000);
  int wg = (j < 4 ? 8 : 58) * 1024;
  u32 wd0 = wd + wg;
  if (!layer) { PIN(wd0); asm volatile("" : "+s"(wg)); }
  int pgt = LDSA(smem) + SY_OFF + 256 * layer + SY_X + 44 + 4 * grp, ob[6];
#pragma unroll
  for (int i = 0; i < 6; ++i) { ob[i] = i == 5 ? mt2 * 128 + (2 * hg + kh) * 512 : (16 * kh + mt2) * 2560 + (i < 4 ? 10240 * i : 2048); asm volatile("" : "+s"(ob[i])); }
  asm volatile("" : "+s"(wos), "+s"(pgt), "+s"(srsrc), "+s"(x1rs), "+s"(orsrc));
  STX(11);
  if (layer && !grp && RVF) RVR
    if (!layer) {
    if (MKPOS == 2) MARKS
      if (w == CWW) {
        const __amdgpu_buffer_rsrc_t crs = __builtin_amdgcn_make_buffer_rsrc(ws + WS_CW + hg * 23040, 0, 23040, 0x00020000);
        const int cp = min(lane, 59) / 20, cc = min(lane, 59) - 20 * cp;
        pg_poll<4>(sya, LDSA(smem) + SY_OFF + SY_X + CWAT);
        __builtin_amdgcn_s_setprio(PCW);
        f32x4 cv[20];
#pragma unroll
        for (int i = 0; i < 20; ++i) cv[i] = __builtin_bit_cast(f32x4, __builtin_amdgcn_raw_buffer_load_b128(crs, cp * 384 + cc * 16, i * 1152, 16));
#pragma unroll
        for (int i = 0; i < 10; ++i) cv[i] += cv[i + 10];
        const f32x4 cs = ((cv[0] + cv[1]) + (cv[2] + cv[3])) + ((cv[4] + cv[5]) + (cv[6] + cv[7])) + (cv[8] + cv[9]);
        u32 cq[8];
#pragma unroll
        for (int r = 0; r < 4; ++r) {
          const float h0 = rbf(cs[r]), h1 = rbf(cs[r] - h0);
          cq[2 * r] = pack2(h0, h1); cq[2 * r + 1] = pack2(cs[r] - h0 - h1, 0.f);
        }
        asm volatile("ds_write_b128 %0, %1\n\tds_write_b128 %0, %2 offset:16" :: "v"(LDSA(smem) + CW_OFF + (cc + (cp ? 10 * cp - 10 + (cc >= 10 ? 30 : 0) : 20)) * 32),
                     "v"((u32x4){cq[0], cq[1], cq[2], cq[3]}), "v"((u32x4){cq[4], cq[5], cq[6], cq[7]}) : "memory");
        lds_inc<SY_STAT>(sya, one);
        __builtin_amdgcn_s_setprio(PTW);

      }
      if (w == MPW) {
        const int fb = lane < 8 ? (b & 62) + (lane & 1) + 64 * (lane >> 1) : 256 + 16 * ((lane >> 1) & 7) + 2 * hg + (lane & 1);
        pg_poll<8>(sya, LDSA(smem) + SY_OFF + SY_X + MKAT);
        int spins = 0;
        _Pragma("unroll 1")
        while (true) {
          asm volatile("" ::: "memory");
          const u32x2 f = __builtin_amdgcn_raw_buffer_load_b64(frs, lane < 24 ? fb * 128 : b * 128, FL_M, 16);
          if (__builtin_amdgcn_ballot_w64(f[0] != tg0 || f[1] != tg1) == 0 || ++spins > GCAP) break;
          __builtin_amdgcn_s_sleep(8);
        }
        RETRY(15, spins);
        lds_inc<SY_STAT>(sya, one);
        lds_inc<SY_MK>(sya, one);

      }
    }
    if (j == XJ0 || j == (layer ? XJ1 : XJ2)) {
      const int xo = j == XJ0 ? rb * 8192 + l16 : (rb ? rb - 1 : 0) * 8192 + 768 + l15 * 16;
      pg_poll<4>(sya, LDSA(smem) + SY_OFF + 256 * layer + SY_X + (layer ? XSAT : XSAT0));
      __builtin_amdgcn_s_sleep(layer ? XSSL : 0);
      __builtin_amdgcn_s_setprio(3);
      u32x4 pv[8];
      int spins = 0;
      _Pragma("unroll 1")
      while (true) {
        asm volatile("" ::: "memory");
        u32 m = 0;
#pragma unroll
        for (int i = 0; i < 8; ++i) pv[i] = __builtin_amdgcn_raw_buffer_load_b128(xsrs, xo, i * 1024, 16);
#pragma unroll
        for (int i = 0; i < 8; ++i) m |= (pv[i][1] ^ tg0) | (pv[i][3] ^ tq1);
        if (__builtin_amdgcn_ballot_w64(m != 0) == 0 || ++spins > GCAP) break;
        __builtin_amdgcn_s_sleep(XSRT);
      }
      RETRY(15, spins);
      float sv[2];
#pragma unroll
      for (int i = 0; i < 2; ++i)
        sv[i] = ((__uint_as_float(pv[0][2 * i]) + __uint_as_float(pv[1][2 * i])) + (__uint_as_float(pv[2][2 * i]) + __uint_as_float(pv[3][2 * i]))) +
                ((__uint_as_float(pv[4][2 * i]) + __uint_as_float(pv[5][2 * i])) + (__uint_as_float(pv[6][2 * i]) + __uint_as_float(pv[7][2 * i])));
      const float mean = sv[0] * (1.0f / 640.0f), var = fmaxf(sv[1] * (1.0f / 640.0f) - mean * mean, 0.f);
      const u32 mq = pack2(-0.25f * mean, -0.25f * mean);
      const int row = j == XJ0 ? 16 + lane : l15;
      asm volatile("ds_write_b128 %0, %1\n\tds_write_b32 %2, %3" :: "v"(LDSA(smem) + A_R2 + row * 16), "v"((u32x4){mq, mq & 0xffffu, 0u, 0u}),
                   "v"(LDSA(smem) + CW_OFF + 1920 + row * 4), "v"(__builtin_amdgcn_rcpf(rbf(__builtin_amdgcn_sqrtf(rbf(rbf(var) + 1e-5f))))) : "memory");
      lds_inc<SY_STAT>(sya, one);
      __builtin_amdgcn_s_setprio(PTW);

    }
  if (layer && !grp && !RVF) RVR
  STX(7);
  if (!layer) lds_poll<SY_MK, 2>(sya, 1);
  STX(2);
  pg_poll<2>(sya, LDSA(smem) + SY_OFF + 256 * layer + SY_X + (layer ? WOAT : WOAT0));
  STX(12);
  __builtin_amdgcn_s_setprio(PTO);
  {
    int s0_, s1_;
    if (layer) {
      asm volatile(
        "s_bitcmp1_b32 0x3c, %[j]\ns_cbranch_scc0 .LA%=\ns_mov_b32 %[s1], 0x100000\n.LC%=:\nds_read_b32 v30, %[sy] offset:%[orv]\ns_waitcnt lgkmcnt(0)\nv_readfirstlane_b32 %[s0], v30\ns_nop 0\n"
        "s_cmp_ge_u32 %[s0], 8\ns_cbranch_scc1 .LCx%=\ns_sleep 1\ns_sub_u32 %[s1], %[s1], 1\ns_cbranch_scc0 .LC%=\n.LCx%=:\nds_write_b128 %[wd], v[128:131] offset:0\n"
        "ds_write_b128 %[wd], v[132:135] offset:1024\nds_write_b128 %[wd], v[144:147] offset:2048\nds_write_b128 %[wd], v[148:151] offset:3072\nds_write_b128 %[wd], v[152:155] offset:4096\n"
        "ds_write_b128 %[wd], v[156:159] offset:5120\nds_write_b128 %[wd], v[160:163] offset:6144\nds_write_b128 %[wd], v[164:167] offset:7168\ns_branch .LD%=\n.LA%=:\ns_movk_i32 %[s0], 4096\n"
        "buffer_load_dwordx4 v[128:131], %[l16], %[wos], 0 offen offset:0 sc1\nbuffer_load_dwordx4 v[132:135], %[l16], %[wos], 0 offen offset:1024 sc1\n"
        "buffer_load_dwordx4 v[144:147], %[l16], %[wos], 0 offen offset:2048 sc1\nbuffer_load_dwordx4 v[148:151], %[l16], %[wos], 0 offen offset:3072 sc1\n"
        "buffer_load_dwordx4 v[152:155], %[l16], %[wos], %[s0] offen offset:0 sc1\nbuffer_load_dwordx4 v[156:159], %[l16], %[wos], %[s0] offen offset:1024 sc1\ns_cmp_eq_u32 %[j], 1\n"
        "s_cbranch_scc1 .LD%=\nbuffer_load_dwordx4 v[160:163], %[l16], %[wos], %[s0] offen offset:2048 sc1\nbuffer_load_dwordx4 v[164:167], %[l16], %[wos], %[s0] offen offset:3072 sc1\n.LD%=:\n"
        "s_mov_b64 exec, 1\nds_add_u32 %[sy], %[one] offset:%[owo]\ns_mov_b64 exec, -1\ns_setprio %[pta]\ns_mov_b32 %[s1], 0x100000\n.LE%=:\nds_read_b128 v[30:33], %[sy] offset:%[opg]\n"
        "s_waitcnt lgkmcnt(0)\nv_min3_u32 v30, v30, v31, v32\nv_min_u32 v30, v30, v33\ns_nop 0\nv_readfirstlane_b32 %[s0], v30\ns_nop 0\ns_cmp_ge_u32 %[s0], %[pgt]\ns_cbranch_scc1 .LEx%=\n"
        "s_sleep %[aps]\ns_sub_u32 %[s1], %[s1], 1\ns_cbranch_scc0 .LE%=\n.LEx%=:\nds_read_b32 v86, %[rq]\nds_read_b32 v28, %[rk0]\nds_read_b32 v29, %[rk1]\nds_read_b64 v[30:31], %[q0]\n"
        "ds_read_b64 v[32:33], %[q1]\nds_read_b64 v[38:39], %[ka] offset:0\nds_read_b64 v[40:41], %[ka] offset:8\nds_read_b64 v[42:43], %[ka] offset:7936\nds_read_b64 v[44:45], %[ka] offset:7944\n"
        "ds_read_b64 v[34:35], %[q2]\nds_read_b64 v[36:37], %[q3]\nds_read_b64 v[46:47], %[ka] offset:40\nds_read_b64 v[48:49], %[ka] offset:48\nds_read_b64 v[50:51], %[ka] offset:7976\n"
        "ds_read_b64 v[52:53], %[ka] offset:7984\nds_read_b64_tr_b16 v[54:55], %[va] offset:0\nds_read_b64_tr_b16 v[56:57], %[va] offset:7936\nds_read_b64_tr_b16 v[58:59], %[va] offset:32\n"
        "ds_read_b64_tr_b16 v[60:61], %[va] offset:7968\nds_read_b64_tr_b16 v[62:63], %[va] offset:40\nds_read_b64_tr_b16 v[64:65], %[va] offset:7976\nds_read_b64_tr_b16 v[66:67], %[va] offset:72\n"
        "ds_read_b64_tr_b16 v[68:69], %[va] offset:8008\ns_waitcnt lgkmcnt(14)\nv_mfma_f32_16x16x32_bf16 v[70:73], v[38:41], v[30:33], %[bias]\n"
        "v_mfma_f32_16x16x32_bf16 v[74:77], v[42:45], v[30:33], 0\ns_waitcnt lgkmcnt(8)\nv_mfma_f32_16x16x32_bf16 v[78:81], v[46:49], v[34:37], %[bias]\n"
        "v_mfma_f32_16x16x32_bf16 v[82:85], v[50:53], v[34:37], 0\nv_mul_f32 v86, %[nc], v86\ns_nop 0\nv_mul_f32 v87, v86, v29\nv_mul_f32 v86, v86, v28\ns_nop 0\nv_cndmask_b32 v70, v70, v71, %[par]\n"
        "v_cndmask_b32 v72, v72, v73, %[par]\nv_cndmask_b32 v74, v74, v75, %[par]\nv_cndmask_b32 v76, v76, v77, %[par]\nv_cndmask_b32 v30, v74, v70, %[c0]\nv_cndmask_b32 v31, v76, v72, %[c1]\n"
        "v_mul_f32 v30, v30, v86\nv_mul_f32 v31, v31, v87\nv_max_f32 v34, v30, v31\nv_cndmask_b32 v78, v78, v79, %[par]\nv_cndmask_b32 v80, v80, v81, %[par]\nv_cndmask_b32 v82, v82, v83, %[par]\n"
        "v_cndmask_b32 v84, v84, v85, %[par]\nv_cndmask_b32 v32, v82, v78, %[c0]\nv_cndmask_b32 v33, v84, v80, %[c1]\nv_mul_f32 v32, v32, v86\nv_mul_f32 v33, v33, v87\nv_max_f32 v35, v32, v33\n"
        "s_nop 1\nv_permlane16_swap_b32 v34, v35\nv_max_f32 v34, v34, v35\nv_mov_b32 v35, v34\ns_nop 1\nv_permlane32_swap_b32 v34, v35\nv_max_f32 v34, v34, v35\nv_mov_b32 v35, v34\ns_nop 1\n"
        "v_permlane16_swap_b32 v34, v35\nv_sub_f32 v30, v30, v34\nv_sub_f32 v31, v31, v34\nv_sub_f32 v32, v32, v35\nv_sub_f32 v33, v33, v35\nv_exp_f32 v30, v30\nv_exp_f32 v31, v31\nv_exp_f32 v32, v32\n"
        "v_exp_f32 v33, v33\nv_add_f32 v34, v30, v31\nv_add_f32 v35, v32, v33\nv_mul_f32 v30, v30, v28\nv_mul_f32 v31, v31, v29\nv_permlane16_swap_b32 v34, v35\nv_add_f32 v34, v34, v35\n"
        "v_mov_b32 v35, v34\nv_mul_f32 v32, v32, v28\nv_mul_f32 v33, v33, v29\nv_permlane32_swap_b32 v34, v35\nv_add_f32 v34, v34, v35\nv_rcp_f32 v34, v34\ns_nop 0\nv_mov_b32 v35, v34\ns_nop 1\n"
        "v_permlane16_swap_b32 v34, v35\nv_mul_f32 v30, v30, v34\nv_mul_f32 v31, v31, v34\nv_mul_f32 v32, v32, v35\nv_mul_f32 v33, v33, v35\nv_cvt_pk_bf16_f32 v30, v30, v31\n"
        "v_cvt_pk_bf16_f32 v32, v32, v33\nv_perm_b32 v38, v30, v30, %[sa0]\nv_perm_b32 v39, v30, v30, %[sa1]\nv_perm_b32 v40, v30, v30, %[sb0]\nv_perm_b32 v41, v30, v30, %[sb1]\n"
        "v_perm_b32 v42, v32, v32, %[sa0]\nv_perm_b32 v43, v32, v32, %[sa1]\nv_perm_b32 v44, v32, v32, %[sb0]\nv_perm_b32 v45, v32, v32, %[sb1]\ns_waitcnt lgkmcnt(0)\n"
        "v_mfma_f32_16x16x32_bf16 v[70:73], v[54:57], v[38:41], 0\nv_mfma_f32_16x16x32_bf16 v[78:81], v[62:65], v[42:45], 0\nv_mfma_f32_16x16x32_bf16 v[74:77], v[58:61], v[38:41], 0\n"
        "v_mfma_f32_16x16x32_bf16 v[82:85], v[66:69], v[42:45], 0\ns_nop 4\nv_cvt_pk_bf16_f32 v46, v70, v71\nv_cvt_pk_bf16_f32 v47, v72, v73\nv_cvt_pk_bf16_f32 v48, v78, v79\n"
        "v_cvt_pk_bf16_f32 v49, v80, v81\nv_min_u32 v46, -2, v46\nv_min_u32 v47, -2, v47\nv_min_u32 v48, -2, v48\nv_min_u32 v49, -2, v49\n"
        "buffer_store_dwordx4 v[46:49], %[l16], %[ors], %[so0] offen sc1\nv_cvt_pk_bf16_f32 v50, v74, v75\nv_cvt_pk_bf16_f32 v51, v76, v77\nv_cvt_pk_bf16_f32 v52, v82, v83\n"
        "v_cvt_pk_bf16_f32 v53, v84, v85\nv_min_u32 v50, -2, v50\nv_min_u32 v51, -2, v51\nv_min_u32 v52, -2, v52\nv_min_u32 v53, -2, v53\n"
        "buffer_store_dwordx4 v[50:53], %[oh], %[ors], %[so1] offen sc1\ns_bitcmp1_b32 0x3c, %[j]\ns_cbranch_scc1 .LF%=\ns_waitcnt vmcnt(2)\nds_write_b128 %[wd], v[128:131] offset:0\n"
        "ds_write_b128 %[wd], v[132:135] offset:1024\nds_write_b128 %[wd], v[144:147] offset:2048\nds_write_b128 %[wd], v[148:151] offset:3072\nds_write_b128 %[wd], v[152:155] offset:4096\n"
        "ds_write_b128 %[wd], v[156:159] offset:5120\ns_cmp_eq_u32 %[j], 1\ns_cbranch_scc1 .LF%=\nds_write_b128 %[wd], v[160:163] offset:6144\nds_write_b128 %[wd], v[164:167] offset:7168\n.LF%=:\n"
        "s_mov_b64 exec, 1\nds_add_u32 %[sy], %[one] offset:%[owo]+4\ns_mov_b64 exec, -1\n"
        : [wd] "+{v107}"(wd), [s0] "=&s"(s0_), [s1] "=&s"(s1_), "+{v[128:131]}"(wst[0]), "+{v[132:135]}"(wst[1]), "+{v[144:147]}"(wst[2]), "+{v[148:151]}"(wst[3]), "+{v[152:155]}"(wst[4]), "+{v[156:159]}"(wst[5]), "+{v[160:163]}"(wst[6]), "+{v[164:167]}"(wst[7])
        : [q0] "{v92}"(qa[0]), [q1] "{v93}"(qa[1]), [q2] "{v94}"(qa[2]), [q3] "{v95}"(qa[3]), [ka] "{v96}"(qka), [va] "{v97}"(vta), [bias] "{v[88:91]}"(bias), [par] "s"(0xaaaaaaaaaaaaaaaaull), [c0] "s"(c0m), [c1] "s"(c1m), [nc] "s"(nc), [rq] "{v103}"(rq), [rk0] "{v104}"(rk0), [rk1] "{v105}"(rk1), [sa0] "{v98}"(sA0), [sa1] "{v99}"(sA1), [sb0] "{v100}"(sB0), [sb1] "{v101}"(sB1), [l16] "{v8}"(l16), [oh] "{v102}"(ohs), [ors] "s"(orsrc), [so0] "s"(so0), [so1] "s"(so1), [wos] "s"(wos), [j] "s"(j), [sy] "{v10}"(sya), [one] "{v11}"(one), [pgt] "s"(pgt), [orv] "i"(SY_RV), [owo] "i"(SY_WO), [opg] "i"(SY_PG), [pta] "i"(PTA), [aps] "i"(APS)
        : "memory", "scc", "v28", "v29", "v30", "v31", "v32", "v33", "v34", "v35", "v36", "v37", "v38", "v39", "v40", "v41", "v42", "v43", "v44", "v45", "v46", "v47", "v48", "v49", "v50", "v51", "v52", "v53", "v54", "v55", "v56", "v57", "v58", "v59", "v60", "v61", "v62", "v63", "v64", "v65", "v66", "v67", "v68", "v69", "v70", "v71", "v72", "v73", "v74", "v75", "v76", "v77", "v78", "v79", "v80", "v81", "v82", "v83", "v84", "v85", "v86", "v87");
    } else {
      asm volatile(
        "s_cmp_lt_u32 %[j], 2\ns_cbranch_scc0 .LB%=\ns_mov_b32 %[s0], 100352\nbuffer_load_dwordx4 v[164:167], %[l16], %[wos], %[s0] offen sc1\ns_mov_b32 %[s0], 61440\n"
        "buffer_load_dwordx4 v[136:139], %[l16], %[wos], %[s0] offen sc1\n.LB%=:\ns_mov_b32 %[s0], 0\nbuffer_load_dwordx4 v[140:143], %[l16], %[wos], %[s0] offen sc1\n"
        "buffer_load_dwordx4 v[144:147], %[l16], %[wos], %[g0] offen sc1\ns_mov_b32 %[s0], 67584\nbuffer_load_dwordx4 v[148:151], %[l16], %[wos], %[s0] offen sc1\ns_mov_b32 %[s0], 75776\n"
        "buffer_load_dwordx4 v[152:155], %[l16], %[wos], %[s0] offen sc1\ns_mov_b32 %[s0], 83968\nbuffer_load_dwordx4 v[156:159], %[l16], %[wos], %[s0] offen sc1\ns_mov_b32 %[s0], 92160\n"
        "buffer_load_dwordx4 v[160:163], %[l16], %[wos], %[s0] offen sc1\ns_mov_b32 %[s0], 12288\nbuffer_load_dwordx4 v[112:115], %[l16], %[wos], %[s0] offen sc1\ns_mov_b32 %[s0], 20480\n"
        "buffer_load_dwordx4 v[116:119], %[l16], %[wos], %[s0] offen sc1\ns_mov_b32 %[s0], 28672\nbuffer_load_dwordx4 v[120:123], %[l16], %[wos], %[s0] offen sc1\ns_mov_b32 %[s0], 36864\n"
        "buffer_load_dwordx4 v[124:127], %[l16], %[wos], %[s0] offen sc1\ns_mov_b32 %[s0], 45056\nbuffer_load_dwordx4 v[128:131], %[l16], %[wos], %[s0] offen sc1\ns_mov_b32 %[s0], 53248\n"
        "buffer_load_dwordx4 v[132:135], %[l16], %[wos], %[s0] offen sc1\ns_mov_b32 %[s1], 0x100000\n.LC%=:\nds_read_b32 v30, %[sy] offset:%[orv]\ns_waitcnt lgkmcnt(0)\nv_readfirstlane_b32 %[s0], v30\n"
        "s_nop 0\ns_cmp_ge_u32 %[s0], 8\ns_cbranch_scc1 .LCx%=\ns_sleep 1\ns_sub_u32 %[s1], %[s1], 1\ns_cbranch_scc0 .LC%=\n.LCx%=:\ns_waitcnt vmcnt(11)\nds_write_b128 %[wd], v[140:143] offset:0\n"
        "v_add_u32 %[wd], 67584, %[wd]\ns_waitcnt vmcnt(10)\nds_write_b128 %[wd0], v[144:147] offset:0\ns_waitcnt vmcnt(9)\nds_write_b128 %[wd], v[148:151] offset:0\ns_waitcnt vmcnt(8)\n"
        "ds_write_b128 %[wd], v[152:155] offset:8192\ns_waitcnt vmcnt(7)\nds_write_b128 %[wd], v[156:159] offset:16384\ns_waitcnt vmcnt(6)\nds_write_b128 %[wd], v[160:163] offset:24576\n"
        "s_cmp_lt_u32 %[j], 2\ns_cbranch_scc0 .LD%=\nds_write_b128 %[wd], v[164:167] offset:32768\n.LD%=:\nv_subrev_u32 %[wd], 55296, %[wd]\ns_mov_b64 exec, 1\nds_add_u32 %[sy], %[one] offset:%[owo]\n"
        "s_mov_b64 exec, -1\ns_setprio %[pta]\ns_mov_b32 %[s1], 0x100000\n.LE%=:\nds_read_b128 v[30:33], %[sy] offset:%[opg]\ns_waitcnt lgkmcnt(0)\nv_min3_u32 v30, v30, v31, v32\n"
        "v_min_u32 v30, v30, v33\ns_nop 0\nv_readfirstlane_b32 %[s0], v30\ns_nop 0\ns_cmp_ge_u32 %[s0], %[pgt]\ns_cbranch_scc1 .LEx%=\ns_sleep %[aps]\ns_sub_u32 %[s1], %[s1], 1\ns_cbranch_scc0 .LE%=\n"
        ".LEx%=:\nds_read_b32 v86, %[rq]\nds_read_b32 v28, %[rk0]\nds_read_b32 v29, %[rk1]\nds_read_b64 v[30:31], %[q0]\nds_read_b64 v[32:33], %[q1]\nds_read_b64 v[38:39], %[ka] offset:0\n"
        "ds_read_b64 v[40:41], %[ka] offset:8\nds_read_b64 v[42:43], %[ka] offset:7936\nds_read_b64 v[44:45], %[ka] offset:7944\nds_read_b64 v[34:35], %[q2]\nds_read_b64 v[36:37], %[q3]\n"
        "ds_read_b64 v[46:47], %[ka] offset:40\nds_read_b64 v[48:49], %[ka] offset:48\nds_read_b64 v[50:51], %[ka] offset:7976\nds_read_b64 v[52:53], %[ka] offset:7984\n"
        "ds_read_b64_tr_b16 v[54:55], %[va] offset:0\nds_read_b64_tr_b16 v[56:57], %[va] offset:7936\nds_read_b64_tr_b16 v[58:59], %[va] offset:32\nds_read_b64_tr_b16 v[60:61], %[va] offset:7968\n"
        "ds_read_b64_tr_b16 v[62:63], %[va] offset:40\nds_read_b64_tr_b16 v[64:65], %[va] offset:7976\nds_read_b64_tr_b16 v[66:67], %[va] offset:72\nds_read_b64_tr_b16 v[68:69], %[va] offset:8008\n"
        "s_waitcnt lgkmcnt(14)\nv_mfma_f32_16x16x32_bf16 v[70:73], v[38:41], v[30:33], %[bias]\nv_mfma_f32_16x16x32_bf16 v[74:77], v[42:45], v[30:33], 0\ns_waitcnt lgkmcnt(8)\n"
        "v_mfma_f32_16x16x32_bf16 v[78:81], v[46:49], v[34:37], %[bias]\nv_mfma_f32_16x16x32_bf16 v[82:85], v[50:53], v[34:37], 0\nv_mul_f32 v86, %[nc], v86\ns_nop 0\nv_mul_f32 v87, v86, v29\n"
        "v_mul_f32 v86, v86, v28\ns_nop 0\nv_cndmask_b32 v70, v70, v71, %[par]\nv_cndmask_b32 v72, v72, v73, %[par]\nv_cndmask_b32 v74, v74, v75, %[par]\nv_cndmask_b32 v76, v76, v77, %[par]\n"
        "v_cndmask_b32 v30, v74, v70, %[c0]\nv_cndmask_b32 v31, v76, v72, %[c1]\nv_mul_f32 v30, v30, v86\nv_mul_f32 v31, v31, v87\nv_max_f32 v34, v30, v31\nv_cndmask_b32 v78, v78, v79, %[par]\n"
        "v_cndmask_b32 v80, v80, v81, %[par]\nv_cndmask_b32 v82, v82, v83, %[par]\nv_cndmask_b32 v84, v84, v85, %[par]\nv_cndmask_b32 v32, v82, v78, %[c0]\nv_cndmask_b32 v33, v84, v80, %[c1]\n"
        "v_mul_f32 v32, v32, v86\nv_mul_f32 v33, v33, v87\nv_max_f32 v35, v32, v33\ns_nop 1\nv_permlane16_swap_b32 v34, v35\nv_max_f32 v34, v34, v35\nv_mov_b32 v35, v34\ns_nop 1\n"
        "v_permlane32_swap_b32 v34, v35\nv_max_f32 v34, v34, v35\nv_mov_b32 v35, v34\ns_nop 1\nv_permlane16_swap_b32 v34, v35\nv_sub_f32 v30, v30, v34\nv_sub_f32 v31, v31, v34\n"
        "v_sub_f32 v32, v32, v35\nv_sub_f32 v33, v33, v35\nv_exp_f32 v30, v30\nv_exp_f32 v31, v31\nv_exp_f32 v32, v32\nv_exp_f32 v33, v33\nv_add_f32 v34, v30, v31\nv_add_f32 v35, v32, v33\n"
        "v_mul_f32 v30, v30, v28\nv_mul_f32 v31, v31, v29\nv_permlane16_swap_b32 v34, v35\nv_add_f32 v34, v34, v35\nv_mov_b32 v35, v34\nv_mul_f32 v32, v32, v28\nv_mul_f32 v33, v33, v29\n"
        "v_permlane32_swap_b32 v34, v35\nv_add_f32 v34, v34, v35\nv_rcp_f32 v34, v34\ns_nop 0\nv_mov_b32 v35, v34\ns_nop 1\nv_permlane16_swap_b32 v34, v35\nv_mul_f32 v30, v30, v34\n"
        "v_mul_f32 v31, v31, v34\nv_mul_f32 v32, v32, v35\nv_mul_f32 v33, v33, v35\nv_cvt_pk_bf16_f32 v30, v30, v31\nv_cvt_pk_bf16_f32 v32, v32, v33\nv_perm_b32 v38, v30, v30, %[sa0]\n"
        "v_perm_b32 v39, v30, v30, %[sa1]\nv_perm_b32 v40, v30, v30, %[sb0]\nv_perm_b32 v41, v30, v30, %[sb1]\nv_perm_b32 v42, v32, v32, %[sa0]\nv_perm_b32 v43, v32, v32, %[sa1]\n"
        "v_perm_b32 v44, v32, v32, %[sb0]\nv_perm_b32 v45, v32, v32, %[sb1]\ns_waitcnt lgkmcnt(0)\nv_mfma_f32_16x16x32_bf16 v[70:73], v[54:57], v[38:41], 0\n"
        "v_mfma_f32_16x16x32_bf16 v[78:81], v[62:65], v[42:45], 0\nv_mfma_f32_16x16x32_bf16 v[74:77], v[58:61], v[38:41], 0\nv_mfma_f32_16x16x32_bf16 v[82:85], v[66:69], v[42:45], 0\ns_nop 4\n"
        "v_cvt_pk_bf16_f32 v46, v70, v71\nv_cvt_pk_bf16_f32 v47, v72, v73\nv_cvt_pk_bf16_f32 v48, v78, v79\nv_cvt_pk_bf16_f32 v49, v80, v81\nv_min_u32 v46, -2, v46\nv_min_u32 v47, -2, v47\n"
        "v_min_u32 v48, -2, v48\nv_min_u32 v49, -2, v49\nbuffer_store_dwordx4 v[46:49], %[l16], %[ors], %[so0] offen sc1\nv_cvt_pk_bf16_f32 v50, v74, v75\nv_cvt_pk_bf16_f32 v51, v76, v77\n"
        "v_cvt_pk_bf16_f32 v52, v82, v83\nv_cvt_pk_bf16_f32 v53, v84, v85\nv_min_u32 v50, -2, v50\nv_min_u32 v51, -2, v51\nv_min_u32 v52, -2, v52\nv_min_u32 v53, -2, v53\n"
        "buffer_store_dwordx4 v[50:53], %[oh], %[ors], %[so1] offen sc1\ns_waitcnt vmcnt(2)\nds_write_b128 %[wd], v[112:115] offset:0\nds_write_b128 %[wd], v[116:119] offset:8192\n"
        "ds_write_b128 %[wd], v[120:123] offset:16384\nds_write_b128 %[wd], v[124:127] offset:24576\nds_write_b128 %[wd], v[128:131] offset:32768\nds_write_b128 %[wd], v[132:135] offset:40960\n"
        "s_cmp_lt_u32 %[j], 2\ns_cbranch_scc0 .LF%=\nds_write_b128 %[wd], v[136:139] offset:49152\n.LF%=:\ns_mov_b64 exec, 1\nds_add_u32 %[sy], %[one] offset:%[owo]+4\ns_mov_b64 exec, -1\n"
        : [wd] "+{v107}"(wd), [s0] "=&s"(s0_), [s1] "=&s"(s1_)
        : [q0] "{v92}"(qa[0]), [q1] "{v93}"(qa[1]), [q2] "{v94}"(qa[2]), [q3] "{v95}"(qa[3]), [ka] "{v96}"(qka), [va] "{v97}"(vta), [bias] "{v[88:91]}"(bias), [par] "s"(0xaaaaaaaaaaaaaaaaull), [c0] "s"(c0m), [c1] "s"(c1m), [nc] "s"(nc), [rq] "{v103}"(rq), [rk0] "{v104}"(rk0), [rk1] "{v105}"(rk1), [sa0] "{v98}"(sA0), [sa1] "{v99}"(sA1), [sb0] "{v100}"(sB0), [sb1] "{v101}"(sB1), [l16] "{v8}"(l16), [oh] "{v102}"(ohs), [ors] "s"(orsrc), [so0] "s"(so0), [so1] "s"(so1), [wos] "s"(wos), [j] "s"(j), [sy] "{v10}"(sya), [one] "{v11}"(one), [pgt] "s"(pgt), [orv] "i"(SY_RV), [owo] "i"(SY_WO), [opg] "i"(SY_PG), [pta] "i"(PTA), [aps] "i"(APS), [wd0] "{v106}"(wd0), [g0] "s"(wg)
        : "memory", "scc", "v28", "v29", "v30", "v31", "v32", "v33", "v34", "v35", "v36", "v37", "v38", "v39", "v40", "v41", "v42", "v43", "v44", "v45", "v46", "v47", "v48", "v49", "v50", "v51", "v52", "v53", "v54", "v55", "v56", "v57", "v58", "v59", "v60", "v61", "v62", "v63", "v64", "v65", "v66", "v67", "v68", "v69", "v70", "v71", "v72", "v73", "v74", "v75", "v76", "v77", "v78", "v79", "v80", "v81", "v82", "v83", "v84", "v85", "v86", "v87", "v112", "v113", "v114", "v115", "v116", "v117", "v118", "v119", "v120", "v121", "v122", "v123", "v124", "v125", "v126", "v127", "v128", "v129", "v130", "v131", "v132", "v133", "v134", "v135", "v136", "v137", "v138", "v139", "v140", "v141", "v142", "v143", "v144", "v145", "v146", "v147", "v148", "v149", "v150", "v151", "v152", "v153", "v154", "v155", "v156", "v157", "v158", "v159", "v160", "v161", "v162", "v163", "v164", "v165", "v166", "v167");
    }
  }
  {
    u32 tm;
    int cap, s0_, s1_, rt = 0;
      asm volatile(
        "s_mov_b32 %[cap], 0x10000\ns_cmp_eq_u32 %[kh], 0\ns_cbranch_scc1 .LS0_%=\ns_sleep %[sl1]\nbuffer_load_dwordx4 v[28:31], %[l16], %[ors], %[o0] offen offset:0 sc1\n"
        "buffer_load_dwordx4 v[32:35], %[l16], %[ors], %[o1] offen offset:0 sc1\nbuffer_load_dwordx4 v[36:39], %[l16], %[ors], %[o2] offen offset:0 sc1\n"
        "buffer_load_dwordx4 v[40:43], %[l16], %[ors], %[o3] offen offset:0 sc1\nbuffer_load_dwordx4 v[44:47], %[oha], %[ors], %[o4] offen offset:0 sc1\n.LS0_%=:\ns_mov_b32 %[s1], 0x100000\n.LWa%=:\n"
        "ds_read_b32 %[tm], %[sy] offset:%[ow1]-4\ns_waitcnt lgkmcnt(0)\nv_readfirstlane_b32 %[s0], %[tm]\ns_nop 0\ns_cmp_ge_i32 %[s0], %[nwo]\ns_cbranch_scc1 .LXa%=\ns_sleep 1\n"
        "s_sub_u32 %[s1], %[s1], 1\ns_cbranch_scc0 .LWa%=\n.LXa%=:\nds_read_b128 v[68:71], %[bb] offset:0\nds_read_b128 v[72:75], %[bb] offset:1024\nds_read_b128 v[76:79], %[bb] offset:2048\n"
        "ds_read_b128 v[80:83], %[bb] offset:3072\nds_read_b128 v[84:87], %[bb] offset:4096\nds_read_b128 v[88:91], %[bb] offset:5120\nv_add_u32 %[bb], %[d1], %[bb]\n"
        "ds_read_b128 v[92:95], %[bb] offset:6144\nds_read_b128 v[96:99], %[bb] offset:7168\nds_read_b128 v[100:103], %[bb] offset:8192\nds_read_b128 v[104:107], %[bb] offset:9216\n"
        "ds_read_b128 v[108:111], %[bb] offset:10240\nds_read_b128 v[112:115], %[bb] offset:11264\nds_read_b128 v[116:119], %[bb] offset:12288\nds_read_b128 v[120:123], %[bb] offset:13312\n"
        "ds_read_b128 v[124:127], %[bb] offset:14336\nds_read_b128 v[128:131], %[bb] offset:15360\nds_read_b128 v[132:135], %[bb] offset:16384\nds_read_b128 v[136:139], %[bb] offset:17408\n"
        "ds_read_b128 v[140:143], %[bb] offset:18432\nds_read_b128 v[144:147], %[bb] offset:19456\nds_read_b128 v[148:151], %[bb] offset:20480\nds_read_b128 v[152:155], %[bb] offset:21504\n"
        "ds_read_b128 v[156:159], %[bb] offset:22528\nds_read_b128 v[160:163], %[bb] offset:23552\nds_read_b128 v[164:167], %[bb] offset:24576\ns_mov_b64 exec, 1\n"
        "ds_add_u32 %[sy], %[one] offset:%[ofr]\ns_mov_b64 exec, -1\nv_add_u32 %[bb], %[d2], %[bb]\nv_lshlrev_b32 v48, 16, v22\nv_and_b32 v49, 0xffff0000, v22\nv_lshlrev_b32 v50, 16, v23\n"
        "v_and_b32 v51, 0xffff0000, v23\nv_lshlrev_b32 v52, 16, v24\nv_and_b32 v53, 0xffff0000, v24\nv_lshlrev_b32 v54, 16, v25\nv_and_b32 v55, 0xffff0000, v25\nv_lshlrev_b32 v56, 16, v26\n"
        "v_and_b32 v57, 0xffff0000, v26\nv_lshlrev_b32 v58, 16, v27\nv_and_b32 v59, 0xffff0000, v27\ns_cmp_eq_u32 %[kh], 0\ns_cbranch_scc0 .LS1_%=\ns_sleep %[sl0]\n"
        "buffer_load_dwordx4 v[28:31], %[l16], %[ors], %[o0] offen offset:0 sc1\nbuffer_load_dwordx4 v[32:35], %[l16], %[ors], %[o1] offen offset:0 sc1\n"
        "buffer_load_dwordx4 v[36:39], %[l16], %[ors], %[o2] offen offset:0 sc1\nbuffer_load_dwordx4 v[40:43], %[l16], %[ors], %[o3] offen offset:0 sc1\n"
        "buffer_load_dwordx4 v[44:47], %[oha], %[ors], %[o4] offen offset:0 sc1\n.LS1_%=:\ns_mov_b32 %[s1], 0x100000\n.LWb%=:\nds_read_b32 %[tm], %[sy] offset:%[ow1]\ns_waitcnt lgkmcnt(0)\n"
        "v_readfirstlane_b32 %[s0], %[tm]\ns_nop 0\ns_cmp_ge_i32 %[s0], %[nwo]\ns_cbranch_scc1 .LXb%=\ns_sleep 1\ns_sub_u32 %[s1], %[s1], 1\ns_cbranch_scc0 .LWb%=\n.LXb%=:\n.LC0_%=:\n"
        "s_waitcnt vmcnt(4)\nv_max_u32 %[tm], v28, v29\nv_max3_u32 %[tm], %[tm], v30, v31\ns_waitcnt vmcnt(3)\nv_max3_u32 %[tm], %[tm], v32, v33\nv_max3_u32 %[tm], %[tm], v34, v35\ns_waitcnt vmcnt(2)\n"
        "v_max3_u32 %[tm], %[tm], v36, v37\nv_max3_u32 %[tm], %[tm], v38, v39\ns_waitcnt vmcnt(1)\nv_max3_u32 %[tm], %[tm], v40, v41\nv_max3_u32 %[tm], %[tm], v42, v43\ns_waitcnt vmcnt(0)\n"
        "v_max3_u32 %[tm], %[tm], v44, v45\nv_max3_u32 %[tm], %[tm], v46, v47\nv_cmp_eq_u32 vcc, -1, %[tm]\ns_cbranch_vccnz .LR0_%=\n.LK0_%=:\nv_mfma_f32_16x16x32_bf16 v[68:71], v[28:31], v[68:71], 0\n"
        "v_mfma_f32_16x16x32_bf16 v[72:75], v[28:31], v[72:75], 0\nv_mfma_f32_16x16x32_bf16 v[76:79], v[28:31], v[76:79], v[48:51]\nv_mfma_f32_16x16x32_bf16 v[80:83], v[28:31], v[80:83], v[52:55]\n"
        "v_mfma_f32_16x16x32_bf16 v[84:87], v[28:31], v[84:87], v[56:59]\nbuffer_load_dwordx4 v[48:51], %[l16], %[ors], %[o0] offen offset:1024 sc1\n"
        "buffer_load_dwordx4 v[52:55], %[l16], %[ors], %[o1] offen offset:1024 sc1\nbuffer_load_dwordx4 v[56:59], %[l16], %[ors], %[o2] offen offset:1024 sc1\n"
        "buffer_load_dwordx4 v[60:63], %[l16], %[ors], %[o3] offen offset:1024 sc1\nbuffer_load_dwordx4 v[64:67], %[oha], %[ors], %[o4] offen offset:256 sc1\n"
        "v_mfma_f32_16x16x32_bf16 v[68:71], v[32:35], v[88:91], v[68:71]\nds_read_b128 v[88:91], %[bb] offset:0\nv_mfma_f32_16x16x32_bf16 v[72:75], v[32:35], v[92:95], v[72:75]\n"
        "ds_read_b128 v[92:95], %[bb] offset:5120\nv_mfma_f32_16x16x32_bf16 v[76:79], v[32:35], v[96:99], v[76:79]\nds_read_b128 v[96:99], %[bb] offset:10240\n"
        "v_mfma_f32_16x16x32_bf16 v[80:83], v[32:35], v[100:103], v[80:83]\nds_read_b128 v[100:103], %[bb] offset:15360\nv_mfma_f32_16x16x32_bf16 v[84:87], v[32:35], v[104:107], v[84:87]\n"
        "ds_read_b128 v[104:107], %[bb] offset:20480\nv_mfma_f32_16x16x32_bf16 v[68:71], v[36:39], v[108:111], v[68:71]\nds_read_b128 v[108:111], %[bb] offset:1024\n"
        "v_mfma_f32_16x16x32_bf16 v[72:75], v[36:39], v[112:115], v[72:75]\nds_read_b128 v[112:115], %[bb] offset:6144\nv_mfma_f32_16x16x32_bf16 v[76:79], v[36:39], v[116:119], v[76:79]\n"
        "ds_read_b128 v[116:119], %[bb] offset:11264\nv_mfma_f32_16x16x32_bf16 v[80:83], v[36:39], v[120:123], v[80:83]\nds_read_b128 v[120:123], %[bb] offset:16384\n"
        "v_mfma_f32_16x16x32_bf16 v[84:87], v[36:39], v[124:127], v[84:87]\nds_read_b128 v[124:127], %[bb] offset:21504\nv_mfma_f32_16x16x32_bf16 v[68:71], v[40:43], v[128:131], v[68:71]\n"
        "ds_read_b128 v[128:131], %[bb] offset:2048\nv_mfma_f32_16x16x32_bf16 v[72:75], v[40:43], v[132:135], v[72:75]\nds_read_b128 v[132:135], %[bb] offset:7168\n"
        "v_mfma_f32_16x16x32_bf16 v[76:79], v[40:43], v[136:139], v[76:79]\nds_read_b128 v[136:139], %[bb] offset:12288\nv_mfma_f32_16x16x32_bf16 v[80:83], v[40:43], v[140:143], v[80:83]\n"
        "ds_read_b128 v[140:143], %[bb] offset:17408\nv_mfma_f32_16x16x32_bf16 v[84:87], v[40:43], v[144:147], v[84:87]\nds_read_b128 v[144:147], %[bb] offset:22528\n"
        "v_mfma_f32_16x16x32_bf16 v[68:71], v[44:47], v[148:151], v[68:71]\nds_read_b128 v[148:151], %[bb] offset:3072\nv_mfma_f32_16x16x32_bf16 v[72:75], v[44:47], v[152:155], v[72:75]\n"
        "ds_read_b128 v[152:155], %[bb] offset:8192\nv_mfma_f32_16x16x32_bf16 v[76:79], v[44:47], v[156:159], v[76:79]\nds_read_b128 v[156:159], %[bb] offset:13312\n"
        "v_mfma_f32_16x16x32_bf16 v[80:83], v[44:47], v[160:163], v[80:83]\nds_read_b128 v[160:163], %[bb] offset:18432\nv_mfma_f32_16x16x32_bf16 v[84:87], v[44:47], v[164:167], v[84:87]\n"
        "ds_read_b128 v[164:167], %[bb] offset:23552\nds_read_b128 v[28:31], %[bb] offset:4096\nds_read_b128 v[32:35], %[bb] offset:9216\nds_read_b128 v[36:39], %[bb] offset:14336\n"
        "ds_read_b128 v[40:43], %[bb] offset:19456\nds_read_b128 v[44:47], %[bb] offset:24576\ns_mov_b32 %[s1], 0x100000\n.LWc%=:\nds_read_b32 %[tm], %[sy] offset:%[ofr]\ns_waitcnt lgkmcnt(0)\n"
        "v_readfirstlane_b32 %[s0], %[tm]\ns_nop 0\ns_cmp_ge_i32 %[s0], 8\ns_cbranch_scc1 .LXc%=\ns_sleep 1\ns_sub_u32 %[s1], %[s1], 1\ns_cbranch_scc0 .LWc%=\n.LXc%=:\n.LC1_%=:\ns_waitcnt vmcnt(4)\n"
        "v_max_u32 %[tm], v48, v49\nv_max3_u32 %[tm], %[tm], v50, v51\ns_waitcnt vmcnt(3)\nv_max3_u32 %[tm], %[tm], v52, v53\nv_max3_u32 %[tm], %[tm], v54, v55\ns_waitcnt vmcnt(2)\n"
        "v_max3_u32 %[tm], %[tm], v56, v57\nv_max3_u32 %[tm], %[tm], v58, v59\ns_waitcnt vmcnt(1)\nv_max3_u32 %[tm], %[tm], v60, v61\nv_max3_u32 %[tm], %[tm], v62, v63\ns_waitcnt vmcnt(0)\n"
        "v_max3_u32 %[tm], %[tm], v64, v65\nv_max3_u32 %[tm], %[tm], v66, v67\nv_cmp_eq_u32 vcc, -1, %[tm]\ns_cbranch_vccnz .LR1_%=\n.LK1_%=:\n"
        "v_mfma_f32_16x16x32_bf16 v[68:71], v[48:51], v[88:91], v[68:71]\nv_mfma_f32_16x16x32_bf16 v[68:71], v[52:55], v[92:95], v[68:71]\n"
        "v_mfma_f32_16x16x32_bf16 v[68:71], v[56:59], v[96:99], v[68:71]\nv_mfma_f32_16x16x32_bf16 v[68:71], v[60:63], v[100:103], v[68:71]\n"
        "v_mfma_f32_16x16x32_bf16 v[68:71], v[64:67], v[104:107], v[68:71]\nv_mfma_f32_16x16x32_bf16 v[72:75], v[48:51], v[108:111], v[72:75]\n"
        "v_mfma_f32_16x16x32_bf16 v[72:75], v[52:55], v[112:115], v[72:75]\nv_mfma_f32_16x16x32_bf16 v[72:75], v[56:59], v[116:119], v[72:75]\nds_write_b128 %[rw], v[68:71]\n"
        "v_mfma_f32_16x16x32_bf16 v[72:75], v[60:63], v[120:123], v[72:75]\nv_mfma_f32_16x16x32_bf16 v[72:75], v[64:67], v[124:127], v[72:75]\n"
        "v_mfma_f32_16x16x32_bf16 v[76:79], v[48:51], v[128:131], v[76:79]\nv_mfma_f32_16x16x32_bf16 v[76:79], v[52:55], v[132:135], v[76:79]\n"
        "v_mfma_f32_16x16x32_bf16 v[76:79], v[56:59], v[136:139], v[76:79]\nds_write_b128 %[rw], v[72:75] offset:1024\nv_mfma_f32_16x16x32_bf16 v[76:79], v[60:63], v[140:143], v[76:79]\n"
        "ds_write_b32 %[fw], %[f0]\nv_mfma_f32_16x16x32_bf16 v[76:79], v[64:67], v[144:147], v[76:79]\nv_mfma_f32_16x16x32_bf16 v[80:83], v[48:51], v[148:151], v[80:83]\n"
        "v_mfma_f32_16x16x32_bf16 v[80:83], v[52:55], v[152:155], v[80:83]\nv_mfma_f32_16x16x32_bf16 v[80:83], v[56:59], v[156:159], v[80:83]\nds_write_b128 %[rw], v[76:79] offset:2048\n"
        "v_mfma_f32_16x16x32_bf16 v[80:83], v[60:63], v[160:163], v[80:83]\nds_write_b32 %[fw], %[one]\nv_mfma_f32_16x16x32_bf16 v[80:83], v[64:67], v[164:167], v[80:83]\n"
        "v_mfma_f32_16x16x32_bf16 v[84:87], v[48:51], v[28:31], v[84:87]\nv_mfma_f32_16x16x32_bf16 v[84:87], v[52:55], v[32:35], v[84:87]\n"
        "v_mfma_f32_16x16x32_bf16 v[84:87], v[56:59], v[36:39], v[84:87]\nv_mfma_f32_16x16x32_bf16 v[84:87], v[60:63], v[40:43], v[84:87]\n"
        "v_mfma_f32_16x16x32_bf16 v[84:87], v[64:67], v[44:47], v[84:87]\ns_mov_b32 %[s1], 0x100000\n.LY%=:\nds_read_b32 %[tm], %[fr]\nds_read_b128 v[96:99], %[rr] offset:2048\n"
        "ds_read_b128 v[92:95], %[rr] offset:1024\nds_read_b128 v[88:91], %[rr] offset:0\ns_waitcnt lgkmcnt(3)\nv_readfirstlane_b32 %[s0], %[tm]\ns_waitcnt lgkmcnt(0)\ns_cmp_lg_u32 %[s0], 0\n"
        "s_cbranch_scc1 .LZ%=\ns_sub_u32 %[s1], %[s1], 1\ns_cbranch_scc0 .LY%=\n.LZ%=:\nv_add_f32 v80, v80, v92\nv_add_f32 v84, v84, v96\nv_add_f32 v81, v81, v93\nv_add_f32 v85, v85, v97\n"
        "v_add_f32 v82, v82, v94\nv_add_f32 v86, v86, v98\nv_add_f32 v83, v83, v95\nv_add_f32 v87, v87, v99\nv_cvt_pk_bf16_f32 v92, v80, v81\nv_cvt_pk_bf16_f32 v96, v84, v85\n"
        "v_cvt_pk_bf16_f32 v93, v82, v83\nv_cvt_pk_bf16_f32 v97, v86, v87\nv_min_u32 v92, -2, v92\nv_min_u32 v96, -2, v96\nv_min_u32 v93, -2, v93\nv_min_u32 v97, -2, v97\n"
        "ds_write_b64 %[xo], v[92:93] offset:2304\nds_write_b64 %[xo], v[96:97] offset:4608\ns_cmp_eq_u32 %[kh], 0\nv_add_f32 v100, v80, v84\nv_add_f32 v101, v81, v85\nv_add_f32 v102, v82, v86\n"
        "v_add_f32 v103, v83, v87\nv_mul_f32 v104, v84, v84\nv_mul_f32 v105, v85, v85\nv_mul_f32 v106, v86, v86\nv_mul_f32 v107, v87, v87\nv_fmac_f32 v104, v80, v80\nv_fmac_f32 v105, v81, v81\n"
        "v_fmac_f32 v106, v82, v82\nv_fmac_f32 v107, v83, v83\ns_cbranch_scc0 .LV%=\nv_add_f32 v76, v76, v88\nv_add_f32 v77, v77, v89\nv_add_f32 v78, v78, v90\nv_add_f32 v79, v79, v91\n"
        "v_cvt_pk_bf16_f32 v88, v76, v77\nv_cvt_pk_bf16_f32 v89, v78, v79\nv_min_u32 v88, -2, v88\nv_min_u32 v89, -2, v89\nds_write_b64 %[xo], v[88:89] offset:0\nv_add_f32 v100, v100, v76\n"
        "v_add_f32 v101, v101, v77\nv_add_f32 v102, v102, v78\nv_add_f32 v103, v103, v79\nv_fmac_f32 v104, v76, v76\nv_fmac_f32 v105, v77, v77\nv_fmac_f32 v106, v78, v78\nv_fmac_f32 v107, v79, v79\n"
        ".LV%=:\ns_mov_b64 exec, 1\nds_add_u32 %[sy], %[one] offset:%[ox1]\ns_mov_b64 exec, -1\nv_add_f32_dpp v100, v100, v100 row_ror:8 row_mask:0xf bank_mask:0x3\n"
        "v_add_f32_dpp v101, v101, v101 row_ror:8 row_mask:0xf bank_mask:0x3\nv_add_f32_dpp v104, v104, v104 row_ror:8 row_mask:0xf bank_mask:0x3\n"
        "v_add_f32_dpp v105, v105, v105 row_ror:8 row_mask:0xf bank_mask:0x3\nv_add_f32_dpp v100, v102, v102 row_ror:8 row_mask:0xf bank_mask:0xc\n"
        "v_add_f32_dpp v101, v103, v103 row_ror:8 row_mask:0xf bank_mask:0xc\nv_add_f32_dpp v104, v106, v106 row_ror:8 row_mask:0xf bank_mask:0xc\n"
        "v_add_f32_dpp v105, v107, v107 row_ror:8 row_mask:0xf bank_mask:0xc\nv_add_f32_dpp v102, v100, v100 row_half_mirror row_mask:0xf bank_mask:0x5\n"
        "v_add_f32_dpp v103, v104, v104 row_half_mirror row_mask:0xf bank_mask:0x5\ns_nop 0\nv_add_f32_dpp v102, v101, v101 row_half_mirror row_mask:0xf bank_mask:0xa\n"
        "v_add_f32_dpp v103, v105, v105 row_half_mirror row_mask:0xf bank_mask:0xa\ns_nop 0\nv_add_f32_dpp v102, v102, v102 quad_perm:[1,0,3,2] row_mask:0xf bank_mask:0xf\n"
        "v_add_f32_dpp v103, v103, v103 quad_perm:[1,0,3,2] row_mask:0xf bank_mask:0xf\ns_nop 0\nv_add_f32_dpp v102, v102, v102 quad_perm:[2,3,0,1] row_mask:0xf bank_mask:0xf\n"
        "v_add_f32_dpp v103, v103, v103 quad_perm:[2,3,0,1] row_mask:0xf bank_mask:0xf\nv_min_u32 v102, -2, v102\nv_min_u32 v103, -2, v103\n"
        "buffer_store_dwordx2 v[102:103], %[sv], %[srs], %[ss] offen sc1\ns_branch .LE%=\n.LR0_%=:\ns_sub_u32 %[cap], %[cap], 1\ns_cbranch_scc0 .LQ0_%=\ns_mov_b32 %[cap], 0\ns_branch .LK0_%=\n"
        ".LQ0_%=:\ns_sleep 2\nbuffer_load_dwordx4 v[28:31], %[l16], %[ors], %[o0] offen offset:0 sc1\nbuffer_load_dwordx4 v[32:35], %[l16], %[ors], %[o1] offen offset:0 sc1\n"
        "buffer_load_dwordx4 v[36:39], %[l16], %[ors], %[o2] offen offset:0 sc1\nbuffer_load_dwordx4 v[40:43], %[l16], %[ors], %[o3] offen offset:0 sc1\n"
        "buffer_load_dwordx4 v[44:47], %[oha], %[ors], %[o4] offen offset:0 sc1\ns_branch .LC0_%=\n.LR1_%=:\ns_sub_u32 %[cap], %[cap], 1\ns_cbranch_scc0 .LQ1_%=\ns_mov_b32 %[cap], 0\ns_branch .LK1_%=\n"
        ".LQ1_%=:\ns_sleep 2\nbuffer_load_dwordx4 v[48:51], %[l16], %[ors], %[o0] offen offset:1024 sc1\nbuffer_load_dwordx4 v[52:55], %[l16], %[ors], %[o1] offen offset:1024 sc1\n"
        "buffer_load_dwordx4 v[56:59], %[l16], %[ors], %[o2] offen offset:1024 sc1\nbuffer_load_dwordx4 v[60:63], %[l16], %[ors], %[o3] offen offset:1024 sc1\n"
        "buffer_load_dwordx4 v[64:67], %[oha], %[ors], %[o4] offen offset:256 sc1\ns_branch .LC1_%=\n.LE%=:\n"
        : [tm] "=&v"(tm), [bb] "+{v19}"(bb), [cap] "=&s"(cap), [s0] "=&s"(s0_), [s1] "=&s"(s1_), "+{v20}"(xi_), "+{v21}"(xg_)
        : "{v[22:23]}"(rv[0]), "{v[24:25]}"(rv[1]), "{v[26:27]}"(rv[2]), [l16] "{v8}"(l16), [oha] "{v9}"(oha), [ors] "s"(orsrc), [o0] "s"(ob[0]), [o1] "s"(ob[1]), [o2] "s"(ob[2]), [o3] "s"(ob[3]), [o4] "s"(ob[4]), [sy] "{v10}"(sya), [one] "{v11}"(one), [rw] "{v12}"(rw), [fw] "{v13}"(fw), [f0] "{v14}"(f0), [rr] "{v15}"(rr), [fr] "{v18}"(fr), [xo] "{v16}"(xo_), [sv] "{v17}"(so_), [srs] "s"(srsrc), [ss] "s"(ob[5]), [kh] "s"(kh), [d1] "s"((56 + 13 * kh) * 1024), [d2] "s"((6 * kh - 44) * 1024), [ofr] "i"(SY_FREE), [ox1] "i"(SY_X1), [ow1] "i"(SY_WO + 4), [nwo] "i"(8), [sl0] "i"(layer ? OSLEEPB : OSLEEP), [sl1] "i"(OSLEEP1)
        : "memory", "scc", "vcc", "v28", "v29", "v30", "v31", "v32", "v33", "v34", "v35", "v36", "v37", "v38", "v39", "v40", "v41", "v42", "v43", "v44", "v45", "v46", "v47", "v48", "v49", "v50", "v51", "v52", "v53", "v54", "v55", "v56", "v57", "v58", "v59", "v60", "v61", "v62", "v63", "v64", "v65", "v66", "v67", "v68", "v69", "v70", "v71", "v72", "v73", "v74", "v75", "v76", "v77", "v78", "v79", "v80", "v81", "v82", "v83", "v84", "v85", "v86", "v87", "v88", "v89", "v90", "v91", "v92", "v93", "v94", "v95", "v96", "v97", "v98", "v99", "v100", "v101", "v102", "v103", "v104", "v105", "v106", "v107", "v108", "v109", "v110", "v111", "v112", "v113", "v114", "v115", "v116", "v117", "v118", "v119", "v120", "v121", "v122", "v123", "v124", "v125", "v126", "v127", "v128", "v129", "v130", "v131", "v132", "v133", "v134", "v135", "v136", "v137", "v138", "v139", "v140", "v141", "v142", "v143", "v144", "v145", "v146", "v147", "v148", "v149", "v150", "v151", "v152", "v153", "v154", "v155", "v156", "v157", "v158", "v159", "v160", "v161", "v162", "v163", "v164", "v165", "v166", "v167");
    RETRY(8 * layer + 1 + grp, 0x10000 - cap);
    RETRY(8 * layer + 3 + grp, rt >> 16);
  }
  lds_inc<SY_FIN>(sya, one);
  lds_poll<SY_X1, 0>(sya, 8);
  {
    typedef __attribute__((address_space(3))) const u32x4* lp4;
    __builtin_amdgcn_raw_buffer_store_b128(*(lp4)(size_t)xi_, x1rs, xg_, 0, 17);
    if (j < 2) __builtin_amdgcn_raw_buffer_store_b128(*(lp4)(size_t)(xi_ + 64 * 144), x1rs, xg_, 64 * XP * 2, 17);
  }
  lds_inc<SY_WI>(sya, one);
  STY(13);
  {
    LANE(lane);
    const int t = 64 * w + lane;
    ST_FLUSH(kid);
  }
}

#define BAUX 17
#define PC8 (gq == 0)
#define PC4 (gq == 0)
#define UPAD (5 * 144)
#define UF_N (UPAD + 16 * 144)
#define KY0 1058
#define KC_N 1120
#define KZ0 130
#define K8_N 192
#define CH_EL (UF_N + 4 * KC_N + 2 * K8_N)
#define AUX_B 18944

__device__ __forceinline__ void kc_store(u16* kd, const u32* d) {
  u32 sw[5], mx[5];
#pragma unroll
  for (int i = 0; i < 5; ++i) { sw[i] = __builtin_amdgcn_alignbit(d[i], d[i], 16); mx[i] = __builtin_amdgcn_perm(d[i], d[i + 1], 0x07060100); }
  *(uint4*)kd = make_uint4(mx[4], mx[3], mx[2], mx[1]);
  *(uint4*)(kd + KC_N) = make_uint4(sw[4], sw[3], sw[2], sw[1]);
  *(uint4*)(kd + 2 * KC_N) = make_uint4(mx[3], mx[2], mx[1], mx[0]);
  *(uint4*)(kd + 3 * KC_N) = make_uint4(sw[3], sw[2], sw[1], sw[0]);
}
#define DUPLO_(x) __builtin_amdgcn_perm(x, x, 0x01000100)
#define DUPHI_(x) __builtin_amdgcn_perm(x, x, 0x03020302)

#define FSLEEP 18
#define TSLEEP 24
#define OAUX 0
#define FPOLL 1
__device__ __forceinline__ void b_tables(unsigned char* smem, const int ch, const int lane) {
  const u16* kc = (const u16*)(smem + KQ_OFF) + ch * 1536;
  u16* Uf = (u16*)smem + ch * CH_EL;
  u16* KC = Uf + UF_N;
  const u16* p = kc + (lane < 32 ? 8 * lane : 256 + 4 * (lane - 32));
  const uint2 a0 = *(const uint2*)p, a1 = *(const uint2*)(p + 4), a2 = *(const uint2*)(p + 8);
  const u32 b0 = *(const u32*)&kc[384 + 2 * lane], b1 = *(const u32*)&kc[386 + 2 * lane];
  const uint2 eh = *(const uint2*)(kc + (lane < 3 ? 0 : lane == 3 ? 384 : 512));
  const u32 c0 = *(const u32*)&kc[638 - 2 * lane], c1 = *(const u32*)&kc[640 - 2 * lane];
  u32 dh[6];
  {
    const bool r1 = lane < 32;
    dh[0] = r1 ? a0.x : DUPLO_(a0.x);
    dh[1] = r1 ? a0.y : __builtin_amdgcn_perm(a0.y, a0.x, 0x05040302);
    dh[2] = r1 ? a1.x : DUPLO_(a0.y);
    dh[3] = r1 ? a1.y : __builtin_amdgcn_perm(a1.x, a0.y, 0x05040302);
    const u32 nx = r1 ? a2.x : a1.x;
    dh[4] = lane < 31 ? nx : DUPLO_(nx);
    dh[5] = lane < 31 ? a2.y : (lane == 63 ? nx : nx >> 16);
    kc_store(KC + 8 * (131 - lane), dh);
  }
  {
    u16* R = KC + 128;
#pragma unroll
    for (int c = 0; c < 4; ++c) if (lane < 36) *(uint4*)&R[c * KC_N + 8 * lane] = make_uint4(0, 0, 0, 0);
    asm volatile("" ::: "memory");
#pragma unroll
    for (int c = 0; c < 4; ++c) { R[c * (KC_N - 1) + 95 - lane] = (u16)b1; R[c * (KC_N - 1) + 239 - lane] = (u16)(b0 >> 16); }
  }
  {
    u32* K8 = (u32*)(KC + 4 * KC_N);
    const u32 wh = __builtin_amdgcn_perm(c0, c1, 0x07060100);
    K8[lane + 1] = wh;
    K8[K8_N / 2 + lane] = wh;
    K8[lane < 32 ? (lane ? 64 + lane : 0) : 128 + lane] = 0u;
  }
  *(uint4*)&Uf[8 * lane] = make_uint4(0, 0, 0, 0);
  *(uint4*)&Uf[8 * lane + UPAD - 512] = make_uint4(0, 0, 0, 0);
  if (lane < 23) {
    const bool e0 = lane == 2, e1 = lane == 3, e2 = lane == 19;
    dh[0] = e1 ? DUPLO_(eh.x) : 0u; dh[1] = e1 ? eh.x & 0xffffu : e2 ? eh.x << 16 : 0u; dh[2] = e2 ? eh.x & 0xffffu : 0u; dh[3] = 0u; dh[4] = e0 ? eh.x : 0u; dh[5] = e0 ? eh.y : 0u;
    kc_store(KC + 8 * (131 - (lane < 3 ? lane - 3 : lane < 11 ? 61 + lane : 109 + lane)), dh);
  }
}
#define FSH 10
#define FSX 1
#define FSH0 13
#define TBX 1
#define PTB 2
#define BPS 0
#define PBU 1
#define PBU2 1
#define PBUF 1
#define PBL1 2
#define FSLEEP0 FSLEEP
#define PBL 2
#define PBH 3
#define PBE 3
template <bool FULL, int layer>
__device__ __forceinline__ void b_conv(unsigned char* smem, const int b, const int w, const int gq, const int wq, const int cG, const u16* xtin, float2* stats, u32* fstat,
                                       u16* xtout, u32x4* mark, const u16* wot, u32x4 (&wst)[8]
                                       ) {
  LANE(lane);
  const int t = 64 * w + lane, n = lane & 15, g = lane >> 4, ck = 64 * wq + lane;
  const int v = FULL ? (wq + 2 * gq) & 3 : (0x1023 >> (4 * wq)) & 3;
  const int p = FULL ? (v & 1) : (b & 1);
  const u32 Uf = LDSA(smem) + gq * (CH_EL * 2), Kb = Uf + 2 * UF_N;
  const u32 aux = LDSA(smem) + 47360 + gq * AUX_B;
  const u32 sya = LDSA(smem) + SY_OFF + 256 * layer;
  u32 cna = sya + 80 + 16 * gq, one = 1u;
  u32 ra, rb, wa, ka, kb = Kb;
  if (v < 2) {
    const int r1 = FULL ? 4 : 2 + 2 * v, y0 = KY0 - 16 - n + 8 * g - 128 * r1;
    ra = Uf + 2 * (UPAD + 144 * (n - r1) + 8 * g + 16 * p - 32);
    rb = ra + ((g >= 2 || p) ? 32 : 0);
    wa = aux + (FULL ? (g & 1) * 4096 + n * 256 + (((2 * p + (g >> 1)) ^ (n & 7)) << 4) : v * 4096 + (g & 1) * 2048 + n * 128 + (((g >> 1) ^ (n & 7)) << 4));
    ka = Kb + 2 * ((y0 & 3) * KC_N + (y0 & ~3));
  } else if (v == 2) {
    const int h = n + (g & 1) - 1, zs = 31 - (FULL ? n : 4 * (n >> 1) + 2 * p + (n & 1)) + 4 * g;
    ra = Uf + 2 * (UPAD + 864 * (g >> 1) - 576 + 8 * h - (h < 0 ? 16 : 0));
    rb = ra - (h == 0 ? 40 : 8);
    wa = aux + 12800 + 3072 * (g & 1) + 16 * ((FULL ? 96 : 48) * (g >> 1) + n);
    ka = Kb + 2 * ((zs & 3) * KC_N + 128 + (zs & ~3));
  } else {
    const int h = n + g - 3, tx = (FULL ? 16 * n : 32 * n + 16 * p) - 8 + 8 * g;
    const int z0 = KZ0 - 2 - (FULL ? 2 * n + 96 : 4 * n + 2 * p + 64) + 4 * g, yx = KY0 - 1032 - n + 8 * g;
    ra = Uf + 2 * (UPAD + 8 * h - (h < 0 ? 16 : 0));
    rb = Uf + 2 * (UPAD + tx + 16 * (tx >> 7));
    wa = aux + 8192 + (FULL ? (g & 1) * 2304 + (g >> 1) * 1152 : (g & 1) * 1152 + (g >> 1) * 640) + n * 16;
    ka = Kb + 2 * (4 * KC_N + ((z0 >> 1) & 1) * K8_N + (z0 & ~3));
    kb = Kb + 2 * ((yx & 3) * KC_N + (yx & ~3));
  }
  u32 uw_ = Uf + 2 * (UPAD + 8 * ck + 16 * (ck >> 4));
  const int eck = FULL ? ck : (4 * (ck >> 1) + 2 * p + (ck & 1)) & 255;
  u32 ue = Uf + 2 * (UPAD + 8 * eck + 16 * (eck >> 4));
  u32 exr = aux + (FULL ? (ck ^ ((ck >> 4) & 7)) : ((ck & 127) ^ ((ck >> 3) & 7))) * 16;
  u32 frr = aux + 8192 + (FULL ? (ck & 1) * 1152 + (ck & 126) * 8 : (ck & 1) * 640 + (ck & 62) * 8);
  u32 mr_ = aux + 12800 + 16 * (FULL ? ck - 64 : ck >= 32 ? ck - 32 : ck);
  int od, oh;
  if (layer) { od = (((eck >> 3) * 640 + cG) * 64 + 8 * (eck & 7)) * 2; oh = -1; }
  else {
    const int dst = (eck >> 3) * 51200 + (cG >> 5) * 2560;
    const int pr = 4 * ((cG >> 3) & 3) + (cG & 3) + 4 * (cG & 4);
    od = (dst + pr * 64 + 8 * ((eck & 7) ^ (pr & 6))) * 2;
    oh = ((eck & 7) >= 6 && eck < 248) ? (dst + 51200 + 2048 + pr * 16 + 8 * (eck & 1)) * 2 : -1;
  }
  u32 bna = LDSA(smem) + BND_OFF + (v == 2 ? 2048 : 0) + 16 * lane, kfa = LDSA(smem) + KQ_OFF + gq * 3072 + 1536;
  int c16 = ck * 16;
  const __amdgpu_buffer_rsrc_t xrs = __builtin_amdgcn_make_buffer_rsrc((void*)(xtin + (size_t)cG * XP), 0, XP * 2, 0x00020000);
  const __amdgpu_buffer_rsrc_t frs = __builtin_amdgcn_make_buffer_rsrc((void*)(fstat + (b & 7) * 2048), 0, 8192, 0x00020000);
  const __amdgpu_buffer_rsrc_t ors = __builtin_amdgcn_make_buffer_rsrc(xtout, 0, 32 * 102400, 0x00020000);
  PIN(ra); PIN(rb); PIN(wa); PIN(uw_); PIN(exr); PIN(frr); PIN(mr_); PIN(od);
  int xo = eck * 16;
  if (!FULL) { PIN(ue); PIN(xo); }
  const __amdgpu_buffer_rsrc_t mrs = __builtin_amdgcn_make_buffer_rsrc(mark + b * 2032, 0, 2032 * 16, 0x00020000);
  int mo = t * 16;
  if (!layer) { PIN(oh); PIN(mo); }
  PIN(cna); PIN(one); PIN(ka); PIN(kb); PIN(bna); PIN(kfa); PIN(c16);
  ST(6);
  if (!FULL && wq < 3) {
    lds_poll<SY_WI, 1>(sya, 8);
    __builtin_amdgcn_s_setprio(PTB);
    b_tables(smem, wq, lane);
    lds_inc<80 + 12>(sya + 16 * wq, 1u);
    ST(9);
  }
  if (!FULL && wq == 3) {
    const int xcd = b & 7, idx = b >> 3;
    const int s0 = 64 * (8 * (xcd >> 1) + (idx & 7)), hg = 4 * (xcd & 1) + (idx >> 3);
    const __amdgpu_buffer_rsrc_t srsrc = __builtin_amdgcn_make_buffer_rsrc(stats + (size_t)s0 * 16, 0, 8192, 0x00020000);
    const __amdgpu_buffer_rsrc_t wrs = __builtin_amdgcn_make_buffer_rsrc(fstat + hg * 2048 + s0 / 2, 0, 4096 + 128, 0x00020000);
    int so = lane * 16;
    int wo_ = (lane >> 5) * 4096 + (lane & 31) * 4;
    PIN(so); PIN(wo_);
    lds_poll<SY_FIN, 2>(sya, 8);
    __builtin_amdgcn_s_sleep(SSLEEP);
    f32x4 sm;
    int spins = 0;
    _Pragma("unroll 1")
    while (true) {
      asm volatile("" ::: "memory");
      u32x4 pv[8];
#pragma unroll
      for (int i = 0; i < 8; ++i) pv[i] = __builtin_amdgcn_raw_buffer_load_b128(srsrc, so, i * 1024, 16);
      u32 m = 0;
      sm = __builtin_bit_cast(f32x4, pv[0]);
#pragma unroll
      for (int i = 0; i < 8; ++i) {
        m = umax3(umax3(m, pv[i][0], pv[i][1]), pv[i][2], pv[i][3]);
        if (i) sm += __builtin_bit_cast(f32x4, pv[i]);
      }
      if (__builtin_amdgcn_ballot_w64(m == 0xffffffffu) == 0 || ++spins > GCAP) break;
      __builtin_amdgcn_s_sleep(4);
    }
    RETRY(8 * layer + 5, spins);
    ST(7);
    const auto q0_ = __builtin_amdgcn_permlane32_swap(__float_as_uint(sm[0]), __float_as_uint(sm[2]), false, false);
    const auto q1_ = __builtin_amdgcn_permlane32_swap(__float_as_uint(sm[1]), __float_as_uint(sm[3]), false, false);
    const float mean = (__uint_as_float(q0_[0]) + __uint_as_float(q0_[1])) * (1.0f / 640.0f);
    const float var = fmaxf(fmaf(-mean, mean, (__uint_as_float(q1_[0]) + __uint_as_float(q1_[1])) * (1.0f / 640.0f)), 0.f);
    const float r = 256.0f * __builtin_amdgcn_rcpf(rbf(__builtin_amdgcn_sqrtf(rbf(rbf(var) + 1e-5f))));
    const u32 d = pack2(-mean * r, 0.f);
    const u32 o = ((max(__float_as_uint(r) + 0x8000u, d) - d) & 0xffff0000u) | d;
    __builtin_amdgcn_raw_buffer_store_b32(min(o, 0xfffffffeu), wrs, wo_, 0, 17);
    lds_inc<SY_FS>(sya, one);
    ST(8);
    __builtin_amdgcn_s_setprio(PBU);
  }
  f32x2 kd;
  u32x4 kt[17];
  {
    u32 tm; int s0_, s1_;
    if (FULL && !layer) {
      const __amdgpu_buffer_rsrc_t wos = __builtin_amdgcn_make_buffer_rsrc((void*)(wot + (size_t)(4 * (b & 1) + (b >> 6)) * 51200 + ((w - 6) & 3) * 4096), 0, 8192, 0x00020000);
      asm volatile(
        "s_cmp_lt_u32 %[v], 2\ns_cbranch_scc1 .LW%=\ns_movk_i32 %[s0], 4096\nbuffer_load_dwordx4 v[128:131], %[l16], %[wos], 0 offen offset:0 sc1\n"
        "buffer_load_dwordx4 v[132:135], %[l16], %[wos], 0 offen offset:1024 sc1\nbuffer_load_dwordx4 v[144:147], %[l16], %[wos], 0 offen offset:2048 sc1\n"
        "buffer_load_dwordx4 v[148:151], %[l16], %[wos], 0 offen offset:3072 sc1\nbuffer_load_dwordx4 v[152:155], %[l16], %[wos], %[s0] offen offset:0 sc1\n"
        "buffer_load_dwordx4 v[156:159], %[l16], %[wos], %[s0] offen offset:1024 sc1\nbuffer_load_dwordx4 v[160:163], %[l16], %[wos], %[s0] offen offset:2048 sc1\n"
        "buffer_load_dwordx4 v[164:167], %[l16], %[wos], %[s0] offen offset:3072 sc1\n.LW%=:\ns_mov_b32 %[s1], 0x100000\n.LP%=:\nds_read_b32 %[tm], %[cna] offset:12\ns_waitcnt lgkmcnt(0)\n"
        "v_readfirstlane_b32 %[s0], %[tm]\ns_nop 0\ns_cmp_ge_i32 %[s0], 1\ns_cbranch_scc1 .LQ%=\ns_sleep 1\ns_sub_u32 %[s1], %[s1], 1\ns_cbranch_scc0 .LP%=\n.LQ%=:\nds_read_b64 %[kd], %[kfa]\n"
        "s_cmp_lt_u32 %[v], 2\ns_cbranch_scc0 .LA%=\nds_read_b64 v[100:101], %[ka] offset:1024\nds_read_b64 v[102:103], %[ka] offset:1032\nds_read_b64 v[104:105], %[ka] offset:960\n"
        "ds_read_b64 v[106:107], %[ka] offset:968\nds_read_b64 v[108:109], %[ka] offset:896\nds_read_b64 v[110:111], %[ka] offset:904\nds_read_b64 v[112:113], %[ka] offset:832\n"
        "ds_read_b64 v[114:115], %[ka] offset:840\nds_read_b64 v[116:117], %[ka] offset:768\nds_read_b64 v[118:119], %[ka] offset:776\nds_read_b64 v[120:121], %[ka] offset:704\n"
        "ds_read_b64 v[122:123], %[ka] offset:712\nds_read_b64 v[124:125], %[ka] offset:640\nds_read_b64 v[126:127], %[ka] offset:648\nds_read_b64 v[128:129], %[ka] offset:576\n"
        "ds_read_b64 v[130:131], %[ka] offset:584\nds_read_b64 v[132:133], %[ka] offset:512\nds_read_b64 v[134:135], %[ka] offset:520\nds_read_b64 v[136:137], %[ka] offset:448\n"
        "ds_read_b64 v[138:139], %[ka] offset:456\nds_read_b64 v[140:141], %[ka] offset:384\nds_read_b64 v[142:143], %[ka] offset:392\nds_read_b64 v[144:145], %[ka] offset:320\n"
        "ds_read_b64 v[146:147], %[ka] offset:328\nds_read_b64 v[148:149], %[ka] offset:256\nds_read_b64 v[150:151], %[ka] offset:264\nds_read_b64 v[152:153], %[ka] offset:192\n"
        "ds_read_b64 v[154:155], %[ka] offset:200\nds_read_b64 v[156:157], %[ka] offset:128\nds_read_b64 v[158:159], %[ka] offset:136\nds_read_b64 v[160:161], %[ka] offset:64\n"
        "ds_read_b64 v[162:163], %[ka] offset:72\nds_read_b64 v[164:165], %[ka] offset:0\nds_read_b64 v[166:167], %[ka] offset:8\ns_branch .LE%=\n.LA%=:\nds_read_b128 v[100:103], %[bna]\n"
        "ds_read_b128 v[104:107], %[bna] offset:1024\ns_cmp_eq_u32 %[v], 2\ns_cbranch_scc0 .LB%=\nds_read_b64 v[108:109], %[ka] offset:0\nds_read_b64 v[110:111], %[ka] offset:288\n"
        "ds_read_b64 v[112:113], %[ka] offset:32\nds_read_b64 v[114:115], %[ka] offset:320\nds_read_b64 v[116:117], %[ka] offset:64\nds_read_b64 v[118:119], %[ka] offset:352\n"
        "ds_read_b64 v[120:121], %[ka] offset:96\nds_read_b64 v[122:123], %[ka] offset:384\nds_read_b64 v[124:125], %[ka] offset:128\nds_read_b64 v[126:127], %[ka] offset:416\ns_branch .LE%=\n.LB%=:\n"
        "ds_read_b64 v[108:109], %[ka] offset:192\nds_read_b64 v[110:111], %[ka] offset:224\nds_read_b64 v[112:113], %[ka] offset:128\nds_read_b64 v[114:115], %[ka] offset:160\n"
        "ds_read_b64 v[116:117], %[ka] offset:64\nds_read_b64 v[118:119], %[ka] offset:96\nds_read_b64 v[120:121], %[ka] offset:0\nds_read_b64 v[122:123], %[ka] offset:32\n"
        "ds_read_b64 v[124:125], %[kb] offset:0\nds_read_b64 v[126:127], %[kb] offset:8\n.LE%=:\ns_waitcnt lgkmcnt(0)\n"
        : "=&{v[100:103]}"(kt[0]), "=&{v[104:107]}"(kt[1]), "=&{v[108:111]}"(kt[2]), "=&{v[112:115]}"(kt[3]), "=&{v[116:119]}"(kt[4]), "=&{v[120:123]}"(kt[5]), "=&{v[124:127]}"(kt[6]), "=&{v[128:131]}"(kt[7]), "=&{v[132:135]}"(kt[8]), "=&{v[136:139]}"(kt[9]), "=&{v[140:143]}"(kt[10]), "=&{v[144:147]}"(kt[11]), "=&{v[148:151]}"(kt[12]), "=&{v[152:155]}"(kt[13]), "=&{v[156:159]}"(kt[14]), "=&{v[160:163]}"(kt[15]), "=&{v[164:167]}"(kt[16]), [kd] "=&v"(kd), [tm] "=&v"(tm), [s0] "=&s"(s0_), [s1] "=&s"(s1_)
        : [ka] "v"(ka), [kb] "v"(kb), [bna] "v"(bna), [kfa] "v"(kfa), [v] "s"(v), [cna] "v"(cna), [l16] "v"(lane * 16), [wos] "s"(wos)
        : "memory", "scc", "vcc");
    } else if (FULL) {
      asm volatile(
        "s_mov_b32 %[s1], 0x100000\n.LP%=:\nds_read_b32 %[tm], %[cna] offset:12\ns_waitcnt lgkmcnt(0)\nv_readfirstlane_b32 %[s0], %[tm]\ns_nop 0\ns_cmp_ge_i32 %[s0], 1\ns_cbranch_scc1 .LQ%=\n"
        "s_sleep 1\ns_sub_u32 %[s1], %[s1], 1\ns_cbranch_scc0 .LP%=\n.LQ%=:\nds_read_b64 %[kd], %[kfa]\ns_cmp_lt_u32 %[v], 2\ns_cbranch_scc0 .LA%=\nds_read_b64 v[100:101], %[ka] offset:1024\n"
        "ds_read_b64 v[102:103], %[ka] offset:1032\nds_read_b64 v[104:105], %[ka] offset:960\nds_read_b64 v[106:107], %[ka] offset:968\nds_read_b64 v[108:109], %[ka] offset:896\n"
        "ds_read_b64 v[110:111], %[ka] offset:904\nds_read_b64 v[112:113], %[ka] offset:832\nds_read_b64 v[114:115], %[ka] offset:840\nds_read_b64 v[116:117], %[ka] offset:768\n"
        "ds_read_b64 v[118:119], %[ka] offset:776\nds_read_b64 v[120:121], %[ka] offset:704\nds_read_b64 v[122:123], %[ka] offset:712\nds_read_b64 v[124:125], %[ka] offset:640\n"
        "ds_read_b64 v[126:127], %[ka] offset:648\nds_read_b64 v[128:129], %[ka] offset:576\nds_read_b64 v[130:131], %[ka] offset:584\nds_read_b64 v[132:133], %[ka] offset:512\n"
        "ds_read_b64 v[134:135], %[ka] offset:520\nds_read_b64 v[136:137], %[ka] offset:448\nds_read_b64 v[138:139], %[ka] offset:456\nds_read_b64 v[140:141], %[ka] offset:384\n"
        "ds_read_b64 v[142:143], %[ka] offset:392\nds_read_b64 v[144:145], %[ka] offset:320\nds_read_b64 v[146:147], %[ka] offset:328\nds_read_b64 v[148:149], %[ka] offset:256\n"
        "ds_read_b64 v[150:151], %[ka] offset:264\nds_read_b64 v[152:153], %[ka] offset:192\nds_read_b64 v[154:155], %[ka] offset:200\nds_read_b64 v[156:157], %[ka] offset:128\n"
        "ds_read_b64 v[158:159], %[ka] offset:136\nds_read_b64 v[160:161], %[ka] offset:64\nds_read_b64 v[162:163], %[ka] offset:72\nds_read_b64 v[164:165], %[ka] offset:0\n"
        "ds_read_b64 v[166:167], %[ka] offset:8\ns_branch .LE%=\n.LA%=:\nds_read_b128 v[100:103], %[bna]\nds_read_b128 v[104:107], %[bna] offset:1024\ns_cmp_eq_u32 %[v], 2\ns_cbranch_scc0 .LB%=\n"
        "ds_read_b64 v[108:109], %[ka] offset:0\nds_read_b64 v[110:111], %[ka] offset:288\nds_read_b64 v[112:113], %[ka] offset:32\nds_read_b64 v[114:115], %[ka] offset:320\n"
        "ds_read_b64 v[116:117], %[ka] offset:64\nds_read_b64 v[118:119], %[ka] offset:352\nds_read_b64 v[120:121], %[ka] offset:96\nds_read_b64 v[122:123], %[ka] offset:384\n"
        "ds_read_b64 v[124:125], %[ka] offset:128\nds_read_b64 v[126:127], %[ka] offset:416\ns_branch .LE%=\n.LB%=:\nds_read_b64 v[108:109], %[ka] offset:192\nds_read_b64 v[110:111], %[ka] offset:224\n"
        "ds_read_b64 v[112:113], %[ka] offset:128\nds_read_b64 v[114:115], %[ka] offset:160\nds_read_b64 v[116:117], %[ka] offset:64\nds_read_b64 v[118:119], %[ka] offset:96\n"
        "ds_read_b64 v[120:121], %[ka] offset:0\nds_read_b64 v[122:123], %[ka] offset:32\nds_read_b64 v[124:125], %[kb] offset:0\nds_read_b64 v[126:127], %[kb] offset:8\n.LE%=:\ns_waitcnt lgkmcnt(0)\n"
        : "=&{v[100:103]}"(kt[0]), "=&{v[104:107]}"(kt[1]), "=&{v[108:111]}"(kt[2]), "=&{v[112:115]}"(kt[3]), "=&{v[116:119]}"(kt[4]), "=&{v[120:123]}"(kt[5]), "=&{v[124:127]}"(kt[6]), "=&{v[128:131]}"(kt[7]), "=&{v[132:135]}"(kt[8]), "=&{v[136:139]}"(kt[9]), "=&{v[140:143]}"(kt[10]), "=&{v[144:147]}"(kt[11]), "=&{v[148:151]}"(kt[12]), "=&{v[152:155]}"(kt[13]), "=&{v[156:159]}"(kt[14]), "=&{v[160:163]}"(kt[15]), "=&{v[164:167]}"(kt[16]), [kd] "=&v"(kd), [tm] "=&v"(tm), [s0] "=&s"(s0_), [s1] "=&s"(s1_)
        : [ka] "v"(ka), [kb] "v"(kb), [bna] "v"(bna), [kfa] "v"(kfa), [v] "s"(v), [cna] "v"(cna)
        : "memory", "scc", "vcc");
    } else {
      asm volatile(
        "s_mov_b32 %[s1], 0x100000\n.LP%=:\nds_read_b32 %[tm], %[cna] offset:12\ns_waitcnt lgkmcnt(0)\nv_readfirstlane_b32 %[s0], %[tm]\ns_nop 0\ns_cmp_ge_i32 %[s0], 1\ns_cbranch_scc1 .LQ%=\n"
        "s_sleep 1\ns_sub_u32 %[s1], %[s1], 1\ns_cbranch_scc0 .LP%=\n.LQ%=:\nds_read_b64 %[kd], %[kfa]\ns_cmp_lt_u32 %[v], 2\ns_cbranch_scc0 .LA%=\nds_read_b64 v[100:101], %[ka] offset:512\n"
        "ds_read_b64 v[102:103], %[ka] offset:520\nds_read_b64 v[104:105], %[ka] offset:448\nds_read_b64 v[106:107], %[ka] offset:456\nds_read_b64 v[108:109], %[ka] offset:384\n"
        "ds_read_b64 v[110:111], %[ka] offset:392\nds_read_b64 v[112:113], %[ka] offset:320\nds_read_b64 v[114:115], %[ka] offset:328\nds_read_b64 v[116:117], %[ka] offset:256\n"
        "ds_read_b64 v[118:119], %[ka] offset:264\nds_read_b64 v[120:121], %[ka] offset:192\nds_read_b64 v[122:123], %[ka] offset:200\nds_read_b64 v[124:125], %[ka] offset:128\n"
        "ds_read_b64 v[126:127], %[ka] offset:136\nds_read_b64 v[128:129], %[ka] offset:64\nds_read_b64 v[130:131], %[ka] offset:72\nds_read_b64 v[132:133], %[ka] offset:0\n"
        "ds_read_b64 v[134:135], %[ka] offset:8\ns_branch .LE%=\n.LA%=:\nds_read_b128 v[100:103], %[bna]\nds_read_b128 v[104:107], %[bna] offset:1024\ns_cmp_eq_u32 %[v], 2\ns_cbranch_scc0 .LB%=\n"
        "ds_read_b64 v[108:109], %[ka] offset:0\nds_read_b64 v[110:111], %[ka] offset:288\nds_read_b64 v[112:113], %[ka] offset:32\nds_read_b64 v[114:115], %[ka] offset:320\n"
        "ds_read_b64 v[116:117], %[ka] offset:64\nds_read_b64 v[118:119], %[ka] offset:352\nds_read_b64 v[120:121], %[ka] offset:96\nds_read_b64 v[122:123], %[ka] offset:384\n"
        "ds_read_b64 v[124:125], %[ka] offset:128\nds_read_b64 v[126:127], %[ka] offset:416\nds_read_b64 v[128:129], %[ka] offset:160\nds_read_b64 v[130:131], %[ka] offset:448\ns_branch .LE%=\n.LB%=:\n"
        "ds_read_b64 v[108:109], %[ka] offset:192\nds_read_b64 v[110:111], %[ka] offset:224\nds_read_b64 v[112:113], %[ka] offset:128\nds_read_b64 v[114:115], %[ka] offset:160\n"
        "ds_read_b64 v[116:117], %[ka] offset:64\nds_read_b64 v[118:119], %[ka] offset:96\nds_read_b64 v[120:121], %[ka] offset:0\nds_read_b64 v[122:123], %[ka] offset:32\n"
        "ds_read_b64 v[124:125], %[kb] offset:0\nds_read_b64 v[126:127], %[kb] offset:8\n.LE%=:\ns_waitcnt lgkmcnt(0)\n"
        : "=&{v[100:103]}"(kt[0]), "=&{v[104:107]}"(kt[1]), "=&{v[108:111]}"(kt[2]), "=&{v[112:115]}"(kt[3]), "=&{v[116:119]}"(kt[4]), "=&{v[120:123]}"(kt[5]), "=&{v[124:127]}"(kt[6]), "=&{v[128:131]}"(kt[7]), "=&{v[132:135]}"(kt[8]), [kd] "=&v"(kd), [tm] "=&v"(tm), [s0] "=&s"(s0_), [s1] "=&s"(s1_)
        : [ka] "v"(ka), [kb] "v"(kb), [bna] "v"(bna), [kfa] "v"(kfa), [v] "s"(v), [cna] "v"(cna)
        : "memory", "scc", "vcc");
    }
  }
  float kf4 = ck >= 32 ? kd[0] : 0.f;
  if (!FULL) PIN(kf4);
  ST(1);
  lds_poll<SY_FS, 2>(sya, 1);
  __builtin_amdgcn_s_setprio(FULL ? PBUF : PBU2);
  {
    u32 pv; int s0_, s1_, cap;
    if (FULL && layer) {
      asm volatile(
        "s_mov_b32 %[cap], 0x100000\ns_movk_i32 %[s1], 4096\ns_sleep %[fsx]\nbuffer_load_dwordx4 v[36:39], %[c16], %[xrs], 0 offen sc1\n.LA%=:\nds_read_b32 %[pv], %[sy] offset:%[ofs]\n"
        "s_waitcnt lgkmcnt(0)\nv_readfirstlane_b32 %[s0], %[pv]\ns_nop 0\ns_cmp_ge_i32 %[s0], 2\ns_cbranch_scc1 .LX%=\ns_sub_u32 %[cap], %[cap], 1\ns_cbranch_scc0 .LA%=\n.LX%=:\n"
        "s_mov_b32 %[cap], 0x10000\nbuffer_load_dwordx4 v[40:43], %[c16], %[frs], 0 offen sc1\nbuffer_load_dwordx4 v[44:47], %[c16], %[frs], %[s1] offen sc1\ns_waitcnt vmcnt(2)\n.LB%=:\n"
        "v_max3_u32 %[pv], v36, v37, v38\nv_max_u32 %[pv], %[pv], v39\nv_cmp_eq_u32 vcc, -1, %[pv]\ns_cbranch_vccnz .LC%=\n.LD%=:\nv_lshlrev_b32 v26, 16, v36\nv_and_b32 v27, 0xffff0000, v36\n"
        "v_lshlrev_b32 v28, 16, v37\nv_and_b32 v29, 0xffff0000, v37\nv_lshlrev_b32 v30, 16, v38\nv_and_b32 v31, 0xffff0000, v38\nv_lshlrev_b32 v32, 16, v39\nv_and_b32 v33, 0xffff0000, v39\n.LF%=:\n"
        "s_waitcnt vmcnt(1)\nv_max3_u32 %[pv], v40, v41, v42\nv_lshlrev_b32 v48, 16, v40\nv_lshlrev_b32 v50, 16, v41\nv_lshlrev_b32 v52, 16, v42\nv_lshlrev_b32 v54, 16, v43\nv_fmac_f32 v48, v26, v40\n"
        "v_fmac_f32 v50, v28, v41\nv_fmac_f32 v52, v30, v42\nv_fmac_f32 v54, v32, v43\ns_waitcnt vmcnt(0)\nv_max3_u32 %[pv], %[pv], v43, v44\nv_max3_u32 %[pv], %[pv], v45, v46\n"
        "v_max_u32 %[pv], %[pv], v47\nv_cmp_eq_u32 vcc, -1, %[pv]\nv_lshlrev_b32 v49, 16, v44\nv_lshlrev_b32 v51, 16, v45\nv_lshlrev_b32 v53, 16, v46\nv_lshlrev_b32 v55, 16, v47\n"
        "s_cbranch_vccnz .LI%=\n.LM%=:\nv_fmac_f32 v49, v27, v44\nv_fmac_f32 v51, v29, v45\nv_fmac_f32 v53, v31, v46\nv_fmac_f32 v55, v33, v47\nv_cvt_pk_f16_f32 v36, v48, v49\n"
        "v_cvt_pk_f16_f32 v37, v50, v51\nv_cvt_pk_f16_f32 v38, v52, v53\nv_cvt_pk_f16_f32 v39, v54, v55\nds_write_b128 %[uw], v[36:39]\ns_mov_b64 exec, 1\nds_add_u32 %[cna], %[one] offset:0\n"
        "s_mov_b64 exec, -1\nv_fmac_f32 v26, v48, %[kd1]\nv_fmac_f32 v27, v49, %[kd1]\nv_fmac_f32 v28, v50, %[kd1]\nv_fmac_f32 v29, v51, %[kd1]\nv_fmac_f32 v30, v52, %[kd1]\n"
        "v_fmac_f32 v31, v53, %[kd1]\nv_fmac_f32 v32, v54, %[kd1]\nv_fmac_f32 v33, v55, %[kd1]\ns_setprio %[pbl]\ns_mov_b32 %[s1], 0x100000\n.LN%=:\nds_read_b32 %[pv], %[cna]\ns_waitcnt lgkmcnt(0)\n"
        "v_readfirstlane_b32 %[s0], %[pv]\ns_nop 0\ns_cmp_ge_i32 %[s0], 4\ns_cbranch_scc1 .LO%=\ns_sub_u32 %[s1], %[s1], 1\ns_cbranch_scc0 .LN%=\n.LO%=:\ns_cmp_lt_u32 %[v], 2\ns_cbranch_scc0 .LT%=\n"
        "ds_read_b128 v[52:55], %[ra] offset:1376\nds_read_b128 v[56:59], %[ra] offset:1312\nds_read_b128 v[60:63], %[ra] offset:1248\nds_read_b128 v[64:67], %[rb] offset:1152\n"
        "ds_read_b128 v[68:71], %[ra] offset:1088\nds_read_b128 v[72:75], %[ra] offset:1024\nds_read_b128 v[76:79], %[ra] offset:960\nds_read_b128 v[80:83], %[rb] offset:864\nv_xor_b32 v14, 64, %[wa]\n"
        "s_waitcnt lgkmcnt(7)\nv_mfma_f32_16x16x32_f16 v[48:51], v[100:103], v[52:55], 0\nds_read_b128 v[52:55], %[ra] offset:800\ns_waitcnt lgkmcnt(7)\n"
        "v_mfma_f32_16x16x32_f16 v[44:47], v[100:103], v[56:59], 0\ns_waitcnt lgkmcnt(6)\nv_mfma_f32_16x16x32_f16 v[40:43], v[100:103], v[60:63], 0\ns_waitcnt lgkmcnt(5)\n"
        "v_mfma_f32_16x16x32_f16 v[36:39], v[100:103], v[64:67], 0\nv_mfma_f32_16x16x32_f16 v[48:51], v[104:107], v[56:59], v[48:51]\nds_read_b128 v[56:59], %[ra] offset:736\n"
        "v_mfma_f32_16x16x32_f16 v[44:47], v[104:107], v[60:63], v[44:47]\nv_mfma_f32_16x16x32_f16 v[40:43], v[104:107], v[64:67], v[40:43]\ns_waitcnt lgkmcnt(5)\n"
        "v_mfma_f32_16x16x32_f16 v[36:39], v[104:107], v[68:71], v[36:39]\nv_mfma_f32_16x16x32_f16 v[48:51], v[108:111], v[60:63], v[48:51]\nds_read_b128 v[60:63], %[ra] offset:672\n"
        "v_mfma_f32_16x16x32_f16 v[44:47], v[108:111], v[64:67], v[44:47]\nv_mfma_f32_16x16x32_f16 v[40:43], v[108:111], v[68:71], v[40:43]\ns_waitcnt lgkmcnt(5)\n"
        "v_mfma_f32_16x16x32_f16 v[36:39], v[108:111], v[72:75], v[36:39]\nv_mfma_f32_16x16x32_f16 v[48:51], v[112:115], v[64:67], v[48:51]\nds_read_b128 v[64:67], %[rb] offset:576\n"
        "v_mfma_f32_16x16x32_f16 v[44:47], v[112:115], v[68:71], v[44:47]\nv_mfma_f32_16x16x32_f16 v[40:43], v[112:115], v[72:75], v[40:43]\ns_waitcnt lgkmcnt(5)\n"
        "v_mfma_f32_16x16x32_f16 v[36:39], v[112:115], v[76:79], v[36:39]\nv_mfma_f32_16x16x32_f16 v[48:51], v[116:119], v[68:71], v[48:51]\nds_read_b128 v[68:71], %[ra] offset:512\n"
        "v_mfma_f32_16x16x32_f16 v[44:47], v[116:119], v[72:75], v[44:47]\nv_mfma_f32_16x16x32_f16 v[40:43], v[116:119], v[76:79], v[40:43]\ns_waitcnt lgkmcnt(5)\n"
        "v_mfma_f32_16x16x32_f16 v[36:39], v[116:119], v[80:83], v[36:39]\nv_mfma_f32_16x16x32_f16 v[48:51], v[120:123], v[72:75], v[48:51]\nds_read_b128 v[72:75], %[ra] offset:448\n"
        "v_mfma_f32_16x16x32_f16 v[44:47], v[120:123], v[76:79], v[44:47]\nv_mfma_f32_16x16x32_f16 v[40:43], v[120:123], v[80:83], v[40:43]\ns_waitcnt lgkmcnt(5)\n"
        "v_mfma_f32_16x16x32_f16 v[36:39], v[120:123], v[52:55], v[36:39]\nv_mfma_f32_16x16x32_f16 v[48:51], v[124:127], v[76:79], v[48:51]\nds_read_b128 v[76:79], %[ra] offset:384\n"
        "v_mfma_f32_16x16x32_f16 v[44:47], v[124:127], v[80:83], v[44:47]\nv_mfma_f32_16x16x32_f16 v[40:43], v[124:127], v[52:55], v[40:43]\ns_waitcnt lgkmcnt(5)\n"
        "v_mfma_f32_16x16x32_f16 v[36:39], v[124:127], v[56:59], v[36:39]\nv_mfma_f32_16x16x32_f16 v[48:51], v[128:131], v[80:83], v[48:51]\nds_read_b128 v[80:83], %[rb] offset:288\n"
        "v_mfma_f32_16x16x32_f16 v[44:47], v[128:131], v[52:55], v[44:47]\nv_mfma_f32_16x16x32_f16 v[40:43], v[128:131], v[56:59], v[40:43]\ns_waitcnt lgkmcnt(5)\n"
        "v_mfma_f32_16x16x32_f16 v[36:39], v[128:131], v[60:63], v[36:39]\nv_mfma_f32_16x16x32_f16 v[48:51], v[132:135], v[52:55], v[48:51]\nds_read_b128 v[52:55], %[ra] offset:224\n"
        "v_mfma_f32_16x16x32_f16 v[44:47], v[132:135], v[56:59], v[44:47]\nv_mfma_f32_16x16x32_f16 v[40:43], v[132:135], v[60:63], v[40:43]\ns_waitcnt lgkmcnt(5)\n"
        "v_mfma_f32_16x16x32_f16 v[36:39], v[132:135], v[64:67], v[36:39]\nv_mfma_f32_16x16x32_f16 v[48:51], v[136:139], v[56:59], v[48:51]\nds_read_b128 v[56:59], %[ra] offset:160\n"
        "v_mfma_f32_16x16x32_f16 v[44:47], v[136:139], v[60:63], v[44:47]\nv_mfma_f32_16x16x32_f16 v[40:43], v[136:139], v[64:67], v[40:43]\ns_waitcnt lgkmcnt(5)\n"
        "v_mfma_f32_16x16x32_f16 v[36:39], v[136:139], v[68:71], v[36:39]\nv_mfma_f32_16x16x32_f16 v[48:51], v[140:143], v[60:63], v[48:51]\nds_read_b128 v[60:63], %[ra] offset:96\n"
        "v_mfma_f32_16x16x32_f16 v[44:47], v[140:143], v[64:67], v[44:47]\nv_mfma_f32_16x16x32_f16 v[40:43], v[140:143], v[68:71], v[40:43]\ns_waitcnt lgkmcnt(5)\n"
        "v_mfma_f32_16x16x32_f16 v[36:39], v[140:143], v[72:75], v[36:39]\nv_mfma_f32_16x16x32_f16 v[48:51], v[144:147], v[64:67], v[48:51]\nds_read_b128 v[64:67], %[rb] offset:0\n"
        "v_mfma_f32_16x16x32_f16 v[44:47], v[144:147], v[68:71], v[44:47]\nv_mfma_f32_16x16x32_f16 v[40:43], v[144:147], v[72:75], v[40:43]\ns_waitcnt lgkmcnt(5)\n"
        "v_mfma_f32_16x16x32_f16 v[36:39], v[144:147], v[76:79], v[36:39]\nv_mfma_f32_16x16x32_f16 v[48:51], v[148:151], v[68:71], v[48:51]\n"
        "v_mfma_f32_16x16x32_f16 v[44:47], v[148:151], v[72:75], v[44:47]\nv_mfma_f32_16x16x32_f16 v[40:43], v[148:151], v[76:79], v[40:43]\ns_waitcnt lgkmcnt(4)\n"
        "v_mfma_f32_16x16x32_f16 v[36:39], v[148:151], v[80:83], v[36:39]\nv_mfma_f32_16x16x32_f16 v[48:51], v[152:155], v[72:75], v[48:51]\n"
        "v_mfma_f32_16x16x32_f16 v[44:47], v[152:155], v[76:79], v[44:47]\nv_mfma_f32_16x16x32_f16 v[40:43], v[152:155], v[80:83], v[40:43]\ns_waitcnt lgkmcnt(3)\n"
        "v_mfma_f32_16x16x32_f16 v[36:39], v[152:155], v[52:55], v[36:39]\nv_mfma_f32_16x16x32_f16 v[48:51], v[156:159], v[76:79], v[48:51]\n"
        "v_mfma_f32_16x16x32_f16 v[44:47], v[156:159], v[80:83], v[44:47]\nv_mfma_f32_16x16x32_f16 v[40:43], v[156:159], v[52:55], v[40:43]\ns_waitcnt lgkmcnt(2)\n"
        "v_mfma_f32_16x16x32_f16 v[36:39], v[156:159], v[56:59], v[36:39]\nv_mfma_f32_16x16x32_f16 v[48:51], v[160:163], v[80:83], v[48:51]\n"
        "v_mfma_f32_16x16x32_f16 v[44:47], v[160:163], v[52:55], v[44:47]\nv_mfma_f32_16x16x32_f16 v[40:43], v[160:163], v[56:59], v[40:43]\ns_waitcnt lgkmcnt(1)\n"
        "v_mfma_f32_16x16x32_f16 v[36:39], v[160:163], v[60:63], v[36:39]\nv_mfma_f32_16x16x32_f16 v[48:51], v[164:167], v[52:55], v[48:51]\n"
        "v_mfma_f32_16x16x32_f16 v[44:47], v[164:167], v[56:59], v[44:47]\nv_mfma_f32_16x16x32_f16 v[40:43], v[164:167], v[60:63], v[40:43]\ns_waitcnt lgkmcnt(0)\n"
        "v_mfma_f32_16x16x32_f16 v[36:39], v[164:167], v[64:67], v[36:39]\ns_nop 4\nds_write_b128 v14, v[48:51] offset:128\nds_write_b128 %[wa], v[44:47] offset:128\n"
        "ds_write_b128 v14, v[40:43] offset:0\nds_write_b128 %[wa], v[36:39] offset:0\ns_branch .LE%=\n.LT%=:\ns_cmp_eq_u32 %[v], 2\ns_cbranch_scc0 .LV%=\nds_read_b64 v[60:61], %[rb] offset:0\n"
        "ds_read_b128 v[62:65], %[ra] offset:0\nds_read_b64 v[66:67], %[rb] offset:288\nds_read_b128 v[68:71], %[ra] offset:288\nds_read_b64 v[72:73], %[rb] offset:576\n"
        "ds_read_b128 v[74:77], %[ra] offset:576\nds_read_b64 v[78:79], %[rb] offset:864\nds_read_b128 v[80:83], %[ra] offset:864\ns_waitcnt lgkmcnt(6)\n"
        "v_mfma_f32_16x16x32_f16 v[84:87], v[60:63], v[100:103], 0\nv_mfma_f32_16x16x32_f16 v[88:91], v[62:65], v[104:107], 0\nds_read_b64 v[60:61], %[rb] offset:1152\n"
        "ds_read_b128 v[62:65], %[ra] offset:1152\ns_waitcnt lgkmcnt(6)\nv_mfma_f32_16x16x32_f16 v[92:95], v[66:69], v[100:103], 0\nv_mfma_f32_16x16x32_f16 v[96:99], v[68:71], v[104:107], 0\n"
        "ds_read_b64 v[66:67], %[rb] offset:1440\nds_read_b128 v[68:71], %[ra] offset:1440\ns_nop 7\nv_cvt_pk_f16_f32 v136, v84, v85\nv_cvt_pk_f16_f32 v137, v86, v87\nv_cvt_pk_f16_f32 v138, v88, v89\n"
        "v_cvt_pk_f16_f32 v139, v90, v91\ns_nop 1\nv_mfma_f32_16x16x32_f16 v[36:39], v[136:139], v[108:111], 0\ns_waitcnt lgkmcnt(6)\nv_mfma_f32_16x16x32_f16 v[84:87], v[72:75], v[100:103], 0\n"
        "v_mfma_f32_16x16x32_f16 v[88:91], v[74:77], v[104:107], 0\nds_read_b64 v[72:73], %[rb] offset:1728\nds_read_b128 v[74:77], %[ra] offset:1728\nv_cvt_pk_f16_f32 v140, v92, v93\n"
        "v_cvt_pk_f16_f32 v141, v94, v95\nv_cvt_pk_f16_f32 v142, v96, v97\nv_cvt_pk_f16_f32 v143, v98, v99\ns_nop 1\nv_mfma_f32_16x16x32_f16 v[36:39], v[140:143], v[112:115], v[36:39]\n"
        "v_mfma_f32_16x16x32_f16 v[40:43], v[140:143], v[108:111], 0\ns_waitcnt lgkmcnt(6)\nv_mfma_f32_16x16x32_f16 v[92:95], v[78:81], v[100:103], 0\nv_cvt_pk_f16_f32 v136, v84, v85\n"
        "v_cvt_pk_f16_f32 v137, v86, v87\nv_mfma_f32_16x16x32_f16 v[96:99], v[80:83], v[104:107], 0\nv_cvt_pk_f16_f32 v138, v88, v89\nv_cvt_pk_f16_f32 v139, v90, v91\ns_nop 1\n"
        "v_mfma_f32_16x16x32_f16 v[36:39], v[136:139], v[116:119], v[36:39]\nds_read_b64 v[78:79], %[rb] offset:2016\nds_read_b128 v[80:83], %[ra] offset:2016\n"
        "v_mfma_f32_16x16x32_f16 v[40:43], v[136:139], v[112:115], v[40:43]\nv_mfma_f32_16x16x32_f16 v[44:47], v[136:139], v[108:111], 0\nv_cvt_pk_f16_f32 v140, v92, v93\n"
        "v_cvt_pk_f16_f32 v141, v94, v95\ns_waitcnt lgkmcnt(6)\nv_mfma_f32_16x16x32_f16 v[84:87], v[60:63], v[100:103], 0\nv_cvt_pk_f16_f32 v142, v96, v97\nv_cvt_pk_f16_f32 v143, v98, v99\n"
        "v_mfma_f32_16x16x32_f16 v[88:91], v[62:65], v[104:107], 0\nds_read_b64 v[60:61], %[rb] offset:2304\nds_read_b128 v[62:65], %[ra] offset:2304\n"
        "v_mfma_f32_16x16x32_f16 v[36:39], v[140:143], v[120:123], v[36:39]\nv_mfma_f32_16x16x32_f16 v[40:43], v[140:143], v[116:119], v[40:43]\n"
        "v_mfma_f32_16x16x32_f16 v[44:47], v[140:143], v[112:115], v[44:47]\nv_mfma_f32_16x16x32_f16 v[48:51], v[140:143], v[108:111], 0\nv_cvt_pk_f16_f32 v136, v84, v85\n"
        "v_cvt_pk_f16_f32 v137, v86, v87\ns_waitcnt lgkmcnt(6)\nv_mfma_f32_16x16x32_f16 v[92:95], v[66:69], v[100:103], 0\nv_cvt_pk_f16_f32 v138, v88, v89\nv_cvt_pk_f16_f32 v139, v90, v91\n"
        "v_mfma_f32_16x16x32_f16 v[96:99], v[68:71], v[104:107], 0\nds_read_b64 v[66:67], %[rb] offset:2592\nds_read_b128 v[68:71], %[ra] offset:2592\n"
        "v_mfma_f32_16x16x32_f16 v[36:39], v[136:139], v[124:127], v[36:39]\nv_mfma_f32_16x16x32_f16 v[40:43], v[136:139], v[120:123], v[40:43]\n"
        "v_mfma_f32_16x16x32_f16 v[44:47], v[136:139], v[116:119], v[44:47]\nv_mfma_f32_16x16x32_f16 v[48:51], v[136:139], v[112:115], v[48:51]\nds_write_b128 %[wa], v[36:39] offset:0\n"
        "v_mfma_f32_16x16x32_f16 v[52:55], v[136:139], v[108:111], 0\nv_cvt_pk_f16_f32 v140, v92, v93\nv_cvt_pk_f16_f32 v141, v94, v95\ns_waitcnt lgkmcnt(7)\n"
        "v_mfma_f32_16x16x32_f16 v[84:87], v[72:75], v[100:103], 0\nv_cvt_pk_f16_f32 v142, v96, v97\nv_cvt_pk_f16_f32 v143, v98, v99\nv_mfma_f32_16x16x32_f16 v[88:91], v[74:77], v[104:107], 0\n"
        "s_nop 0\nv_mfma_f32_16x16x32_f16 v[40:43], v[140:143], v[124:127], v[40:43]\nv_mfma_f32_16x16x32_f16 v[44:47], v[140:143], v[120:123], v[44:47]\n"
        "v_mfma_f32_16x16x32_f16 v[48:51], v[140:143], v[116:119], v[48:51]\nv_mfma_f32_16x16x32_f16 v[52:55], v[140:143], v[112:115], v[52:55]\nds_write_b128 %[wa], v[40:43] offset:256\n"
        "v_mfma_f32_16x16x32_f16 v[56:59], v[140:143], v[108:111], 0\nv_cvt_pk_f16_f32 v136, v84, v85\nv_cvt_pk_f16_f32 v137, v86, v87\ns_waitcnt lgkmcnt(6)\n"
        "v_mfma_f32_16x16x32_f16 v[92:95], v[78:81], v[100:103], 0\nv_cvt_pk_f16_f32 v138, v88, v89\nv_cvt_pk_f16_f32 v139, v90, v91\nv_mfma_f32_16x16x32_f16 v[96:99], v[80:83], v[104:107], 0\n"
        "s_nop 0\nv_mfma_f32_16x16x32_f16 v[44:47], v[136:139], v[124:127], v[44:47]\nv_mfma_f32_16x16x32_f16 v[48:51], v[136:139], v[120:123], v[48:51]\n"
        "v_mfma_f32_16x16x32_f16 v[52:55], v[136:139], v[116:119], v[52:55]\nv_mfma_f32_16x16x32_f16 v[56:59], v[136:139], v[112:115], v[56:59]\nds_write_b128 %[wa], v[44:47] offset:512\n"
        "v_cvt_pk_f16_f32 v140, v92, v93\ns_waitcnt lgkmcnt(5)\nv_mfma_f32_16x16x32_f16 v[84:87], v[60:63], v[100:103], 0\nv_cvt_pk_f16_f32 v141, v94, v95\nv_cvt_pk_f16_f32 v142, v96, v97\n"
        "v_mfma_f32_16x16x32_f16 v[88:91], v[62:65], v[104:107], 0\nv_cvt_pk_f16_f32 v143, v98, v99\ns_nop 1\nv_mfma_f32_16x16x32_f16 v[48:51], v[140:143], v[124:127], v[48:51]\n"
        "v_mfma_f32_16x16x32_f16 v[52:55], v[140:143], v[120:123], v[52:55]\nv_mfma_f32_16x16x32_f16 v[56:59], v[140:143], v[116:119], v[56:59]\nv_cvt_pk_f16_f32 v136, v84, v85\n"
        "v_cvt_pk_f16_f32 v137, v86, v87\ns_waitcnt lgkmcnt(3)\nv_mfma_f32_16x16x32_f16 v[92:95], v[66:69], v[100:103], 0\nv_cvt_pk_f16_f32 v138, v88, v89\nv_cvt_pk_f16_f32 v139, v90, v91\n"
        "v_mfma_f32_16x16x32_f16 v[96:99], v[68:71], v[104:107], 0\nds_write_b128 %[wa], v[48:51] offset:768\nv_mfma_f32_16x16x32_f16 v[52:55], v[136:139], v[124:127], v[52:55]\n"
        "v_mfma_f32_16x16x32_f16 v[56:59], v[136:139], v[120:123], v[56:59]\ns_nop 7\nv_cvt_pk_f16_f32 v140, v92, v93\nv_cvt_pk_f16_f32 v141, v94, v95\nv_cvt_pk_f16_f32 v142, v96, v97\n"
        "v_cvt_pk_f16_f32 v143, v98, v99\ns_nop 1\nv_mfma_f32_16x16x32_f16 v[56:59], v[140:143], v[124:127], v[56:59]\ns_nop 7\nds_write_b128 %[wa], v[52:55] offset:1024\n"
        "ds_write_b128 %[wa], v[56:59] offset:1280\ns_branch .LE%=\n.LV%=:\nds_read_b128 v[52:55], %[ra] offset:0\nds_read_b128 v[56:59], %[ra] offset:288\nds_read_b128 v[60:63], %[ra] offset:576\n"
        "ds_read_b128 v[64:67], %[ra] offset:864\nds_read_b128 v[84:87], %[rb] offset:0\nds_read_b128 v[88:91], %[rb] offset:576\nds_read_b128 v[92:95], %[rb] offset:1152\n"
        "ds_read_b128 v[96:99], %[rb] offset:1728\ns_waitcnt lgkmcnt(7)\nv_mfma_f32_16x16x32_f16 v[68:71], v[52:55], v[100:103], 0\nv_mfma_f32_16x16x32_f16 v[72:75], v[52:55], v[104:107], 0\n"
        "ds_read_b128 v[52:55], %[ra] offset:1152\ns_waitcnt lgkmcnt(7)\nv_mfma_f32_16x16x32_f16 v[76:79], v[56:59], v[100:103], 0\nv_mfma_f32_16x16x32_f16 v[80:83], v[56:59], v[104:107], 0\n"
        "ds_read_b128 v[56:59], %[ra] offset:1440\ns_waitcnt lgkmcnt(5)\nv_mfma_f32_16x16x32_f16 v[36:39], v[124:127], v[84:87], 0\nv_cvt_pk_f16_f32 v136, v68, v73\nv_cvt_pk_f16_f32 v137, v70, v75\n"
        "s_waitcnt lgkmcnt(4)\nv_mfma_f32_16x16x32_f16 v[40:43], v[124:127], v[88:91], 0\ns_waitcnt lgkmcnt(3)\nv_mfma_f32_16x16x32_f16 v[44:47], v[124:127], v[92:95], 0\n"
        "v_cvt_pk_f16_f32 v138, v76, v81\nv_cvt_pk_f16_f32 v139, v78, v83\ns_waitcnt lgkmcnt(2)\nv_mfma_f32_16x16x32_f16 v[48:51], v[124:127], v[96:99], 0\n"
        "v_mfma_f32_16x16x32_f16 v[68:71], v[60:63], v[100:103], 0\nv_mfma_f32_16x16x32_f16 v[72:75], v[60:63], v[104:107], 0\nds_read_b128 v[60:63], %[ra] offset:1728\n"
        "v_mfma_f32_16x16x32_f16 v[36:39], v[136:139], v[108:111], v[36:39]\nv_mfma_f32_16x16x32_f16 v[40:43], v[136:139], v[112:115], v[40:43]\n"
        "v_mfma_f32_16x16x32_f16 v[44:47], v[136:139], v[116:119], v[44:47]\nv_cvt_pk_f16_f32 v140, v68, v73\nv_cvt_pk_f16_f32 v141, v70, v75\n"
        "v_mfma_f32_16x16x32_f16 v[48:51], v[136:139], v[120:123], v[48:51]\nds_write_b128 %[wa], v[36:39] offset:0\nv_mfma_f32_16x16x32_f16 v[76:79], v[64:67], v[100:103], 0\n"
        "v_mfma_f32_16x16x32_f16 v[80:83], v[64:67], v[104:107], 0\nds_read_b128 v[64:67], %[ra] offset:2016\ns_waitcnt lgkmcnt(4)\nv_mfma_f32_16x16x32_f16 v[68:71], v[52:55], v[100:103], 0\n"
        "v_mfma_f32_16x16x32_f16 v[72:75], v[52:55], v[104:107], 0\ns_nop 7\nv_cvt_pk_f16_f32 v142, v76, v81\nv_cvt_pk_f16_f32 v143, v78, v83\ns_nop 1\n"
        "v_mfma_f32_16x16x32_f16 v[40:43], v[140:143], v[108:111], v[40:43]\nv_mfma_f32_16x16x32_f16 v[44:47], v[140:143], v[112:115], v[44:47]\n"
        "v_mfma_f32_16x16x32_f16 v[48:51], v[140:143], v[116:119], v[48:51]\nv_cvt_pk_f16_f32 v136, v68, v73\nv_cvt_pk_f16_f32 v137, v70, v75\ns_waitcnt lgkmcnt(3)\n"
        "v_mfma_f32_16x16x32_f16 v[76:79], v[56:59], v[100:103], 0\nds_write_b128 %[wa], v[40:43] offset:256\nv_mfma_f32_16x16x32_f16 v[80:83], v[56:59], v[104:107], 0\ns_waitcnt lgkmcnt(3)\n"
        "v_mfma_f32_16x16x32_f16 v[68:71], v[60:63], v[100:103], 0\nv_mfma_f32_16x16x32_f16 v[72:75], v[60:63], v[104:107], 0\ns_nop 7\nv_cvt_pk_f16_f32 v138, v76, v81\n"
        "v_cvt_pk_f16_f32 v139, v78, v83\ns_nop 1\nv_mfma_f32_16x16x32_f16 v[44:47], v[136:139], v[108:111], v[44:47]\nv_mfma_f32_16x16x32_f16 v[48:51], v[136:139], v[112:115], v[48:51]\n"
        "s_waitcnt lgkmcnt(1)\nv_mfma_f32_16x16x32_f16 v[76:79], v[64:67], v[100:103], 0\nv_cvt_pk_f16_f32 v140, v68, v73\nv_cvt_pk_f16_f32 v141, v70, v75\n"
        "v_mfma_f32_16x16x32_f16 v[80:83], v[64:67], v[104:107], 0\nds_write_b128 %[wa], v[44:47] offset:512\ns_nop 7\nv_cvt_pk_f16_f32 v142, v76, v81\nv_cvt_pk_f16_f32 v143, v78, v83\ns_nop 1\n"
        "v_mfma_f32_16x16x32_f16 v[48:51], v[140:143], v[108:111], v[48:51]\ns_nop 7\nds_write_b128 %[wa], v[48:51] offset:768\ns_branch .LE%=\n.LC%=:\ns_sub_u32 %[cap], %[cap], 1\n"
        "s_cbranch_scc0 .LCy%=\ns_mov_b32 %[cap], 0\ns_branch .LD%=\n.LCy%=:\nbuffer_load_dwordx4 v[36:39], %[c16], %[xrs], 0 offen sc1\ns_waitcnt vmcnt(0)\ns_branch .LB%=\n.LI%=:\n"
        "s_sub_u32 %[cap], %[cap], 1\ns_cbranch_scc0 .LIy%=\ns_mov_b32 %[cap], 0\ns_branch .LM%=\n.LIy%=:\ns_sleep %[fp]\nbuffer_load_dwordx4 v[40:43], %[c16], %[frs], 0 offen sc1\n"
        "buffer_load_dwordx4 v[44:47], %[c16], %[frs], %[s1] offen sc1\ns_branch .LF%=\n.LE%=:\ns_mov_b64 exec, 1\nds_add_u32 %[cna], %[one] offset:8\ns_mov_b64 exec, -1\ns_setprio %[pbe]\n"
        "s_mov_b32 %[s1], 0x100000\n.LP%=:\nds_read_b32 %[pv], %[cna] offset:8\ns_waitcnt lgkmcnt(0)\nv_readfirstlane_b32 %[s0], %[pv]\ns_nop 0\ns_cmp_ge_i32 %[s0], 4\ns_cbranch_scc1 .LR%=\n"
        "s_sub_u32 %[s1], %[s1], 1\ns_cbranch_scc0 .LP%=\n.LR%=:\ns_cmp_lt_u32 %[wq], 2\ns_cbranch_scc1 .LS3_%=\nds_read_b128 v[36:39], %[exr] offset:0\nds_read_b128 v[40:43], %[exr] offset:4096\n"
        "ds_read_b128 v[44:47], %[mr] offset:0\nds_read_b128 v[48:51], %[mr] offset:3072\nds_read_b128 v[52:55], %[frr] offset:0\nds_read_b128 v[56:59], %[frr] offset:2304\ns_waitcnt lgkmcnt(4)\n"
        "v_fmac_f32 v26, v36, %[kf]\nv_fmac_f32 v27, v37, %[kf]\nv_fmac_f32 v28, v38, %[kf]\nv_fmac_f32 v29, v39, %[kf]\nv_fmac_f32 v30, v40, %[kf]\nv_fmac_f32 v31, v41, %[kf]\n"
        "v_fmac_f32 v32, v42, %[kf]\nv_fmac_f32 v33, v43, %[kf]\ns_waitcnt lgkmcnt(2)\nv_fmac_f32 v26, v44, %[kf]\nv_fmac_f32 v27, v45, %[kf]\nv_fmac_f32 v28, v46, %[kf]\nv_fmac_f32 v29, v47, %[kf]\n"
        "v_fmac_f32 v30, v48, %[kf]\nv_fmac_f32 v31, v49, %[kf]\nv_fmac_f32 v32, v50, %[kf]\nv_fmac_f32 v33, v51, %[kf]\ns_waitcnt lgkmcnt(0)\nv_fmac_f32 v26, v52, %[kf]\nv_fmac_f32 v27, v53, %[kf]\n"
        "v_fmac_f32 v28, v54, %[kf]\nv_fmac_f32 v29, v55, %[kf]\nv_fmac_f32 v30, v56, %[kf]\nv_fmac_f32 v31, v57, %[kf]\nv_fmac_f32 v32, v58, %[kf]\nv_fmac_f32 v33, v59, %[kf]\ns_branch .LS%=\n"
        ".LS3_%=:\ns_cmp_lt_u32 %[wq], 1\ns_cbranch_scc1 .LS2_%=\nds_read_b128 v[36:39], %[exr] offset:0\nds_read_b128 v[40:43], %[exr] offset:4096\nds_read_b128 v[44:47], %[mr] offset:0\n"
        "ds_read_b128 v[48:51], %[mr] offset:3072\ns_waitcnt lgkmcnt(2)\nv_fmac_f32 v26, v36, %[kf]\nv_fmac_f32 v27, v37, %[kf]\nv_fmac_f32 v28, v38, %[kf]\nv_fmac_f32 v29, v39, %[kf]\n"
        "v_fmac_f32 v30, v40, %[kf]\nv_fmac_f32 v31, v41, %[kf]\nv_fmac_f32 v32, v42, %[kf]\nv_fmac_f32 v33, v43, %[kf]\ns_waitcnt lgkmcnt(0)\nv_fmac_f32 v26, v44, %[kf]\nv_fmac_f32 v27, v45, %[kf]\n"
        "v_fmac_f32 v28, v46, %[kf]\nv_fmac_f32 v29, v47, %[kf]\nv_fmac_f32 v30, v48, %[kf]\nv_fmac_f32 v31, v49, %[kf]\nv_fmac_f32 v32, v50, %[kf]\nv_fmac_f32 v33, v51, %[kf]\ns_branch .LS%=\n"
        ".LS2_%=:\nds_read_b128 v[36:39], %[exr] offset:0\nds_read_b128 v[40:43], %[exr] offset:4096\ns_waitcnt lgkmcnt(0)\nv_fmac_f32 v26, v36, %[kf]\nv_fmac_f32 v27, v37, %[kf]\n"
        "v_fmac_f32 v28, v38, %[kf]\nv_fmac_f32 v29, v39, %[kf]\nv_fmac_f32 v30, v40, %[kf]\nv_fmac_f32 v31, v41, %[kf]\nv_fmac_f32 v32, v42, %[kf]\nv_fmac_f32 v33, v43, %[kf]\n.LS%=:\n"
        "v_cvt_pk_bf16_f32 v36, v26, v27\nv_cvt_pk_bf16_f32 v37, v28, v29\nv_cvt_pk_bf16_f32 v38, v30, v31\nv_cvt_pk_bf16_f32 v39, v32, v33\nv_min_u32 v36, -2, v36\nv_min_u32 v37, -2, v37\n"
        "v_min_u32 v38, -2, v38\nv_min_u32 v39, -2, v39\nbuffer_store_dwordx4 v[36:39], %[od], %[ors], 0 offen sc0 sc1\ns_nop 1\n.LZ%=:\n"
        : "+{v[100:103]}"(kt[0]), "+{v[104:107]}"(kt[1]), "+{v[108:111]}"(kt[2]), "+{v[112:115]}"(kt[3]), "+{v[116:119]}"(kt[4]), "+{v[120:123]}"(kt[5]), "+{v[124:127]}"(kt[6]), "+{v[128:131]}"(kt[7]), "+{v[132:135]}"(kt[8]), "+{v[136:139]}"(kt[9]), "+{v[140:143]}"(kt[10]), "+{v[144:147]}"(kt[11]), "+{v[148:151]}"(kt[12]), "+{v[152:155]}"(kt[13]), "+{v[156:159]}"(kt[14]), "+{v[160:163]}"(kt[15]), "+{v[164:167]}"(kt[16]), [pv] "=&{v16}"(pv), [s0] "=&s"(s0_), [s1] "=&s"(s1_), [cap] "=&s"(cap)
        : [ra] "{v1}"(ra), [rb] "{v2}"(rb), [wa] "{v3}"(wa), [cna] "{v4}"(cna), [one] "{v5}"(one), [exr] "{v6}"(exr), [frr] "{v7}"(frr), [mr] "{v8}"(mr_), [od] "{v9}"(od), [kf] "{v10}"(kd[0]), [kd1] "{v11}"(kd[1]), [c16] "{v12}"(c16), [uw] "{v17}"(uw_), [sy] "{v23}"(sya), [ofs] "i"(SY_FS), [pbl] "i"(PBL), [v] "s"(v), [wq] "s"(wq), [ors] "s"(ors), [xrs] "s"(xrs), [frs] "s"(frs), [pbe] "i"(PBE), [fsx] "i"(FSX), [fp] "i"(FPOLL)
        : "memory", "scc", "v13", "v14", "v15", "v26", "v27", "v28", "v29", "v30", "v31", "v32", "v33", "v36", "v37", "v38", "v39", "v40", "v41", "v42", "v43", "v44", "v45", "v46", "v47", "v48", "v49", "v50", "v51", "v52", "v53", "v54", "v55", "v56", "v57", "v58", "v59", "v60", "v61", "v62", "v63", "v64", "v65", "v66", "v67", "v68", "v69", "v70", "v71", "v72", "v73", "v74", "v75", "v76", "v77", "v78", "v79", "v80", "v81", "v82", "v83", "v84", "v85", "v86", "v87", "v88", "v89", "v90", "v91", "v92", "v93", "v94", "v95", "v96", "v97", "v98", "v99", "vcc");
    } else if (FULL) {
      asm volatile(
        "s_mov_b32 %[cap], 0x100000\ns_movk_i32 %[s1], 4096\nv_mov_b32 v56, -1\nv_mov_b32 v57, -1\nv_mov_b32 v58, -1\nv_mov_b32 v59, -1\ns_sleep %[fsx]\n"
        "buffer_load_dwordx4 v[36:39], %[c16], %[xrs], 0 offen sc1\n.LA%=:\nds_read_b32 %[pv], %[sy] offset:%[ofs]\ns_waitcnt lgkmcnt(0)\nv_readfirstlane_b32 %[s0], %[pv]\ns_nop 0\n"
        "s_cmp_ge_i32 %[s0], 2\ns_cbranch_scc1 .LX%=\ns_sub_u32 %[cap], %[cap], 1\ns_cbranch_scc0 .LA%=\n.LX%=:\ns_mov_b32 %[cap], 0x10000\nbuffer_load_dwordx4 v[40:43], %[c16], %[frs], 0 offen sc1\n"
        "buffer_load_dwordx4 v[44:47], %[c16], %[frs], %[s1] offen sc1\ns_waitcnt vmcnt(2)\n.LB%=:\nv_max3_u32 %[pv], v36, v37, v38\nv_max_u32 %[pv], %[pv], v39\nv_cmp_eq_u32 vcc, -1, %[pv]\n"
        "s_cbranch_vccnz .LC%=\n.LD%=:\nv_lshlrev_b32 v26, 16, v36\nv_and_b32 v27, 0xffff0000, v36\nv_lshlrev_b32 v28, 16, v37\nv_and_b32 v29, 0xffff0000, v37\nv_lshlrev_b32 v30, 16, v38\n"
        "v_and_b32 v31, 0xffff0000, v38\nv_lshlrev_b32 v32, 16, v39\nv_and_b32 v33, 0xffff0000, v39\n.LF%=:\ns_waitcnt vmcnt(1)\nv_max3_u32 %[pv], v40, v41, v42\nv_lshlrev_b32 v48, 16, v40\n"
        "v_lshlrev_b32 v50, 16, v41\nv_lshlrev_b32 v52, 16, v42\nv_lshlrev_b32 v54, 16, v43\nv_fmac_f32 v48, v26, v40\nv_fmac_f32 v50, v28, v41\nv_fmac_f32 v52, v30, v42\nv_fmac_f32 v54, v32, v43\n"
        "s_waitcnt vmcnt(0)\nv_max3_u32 %[pv], %[pv], v43, v44\nv_max3_u32 %[pv], %[pv], v45, v46\nv_max_u32 %[pv], %[pv], v47\nv_cmp_eq_u32 vcc, -1, %[pv]\nv_lshlrev_b32 v49, 16, v44\n"
        "v_lshlrev_b32 v51, 16, v45\nv_lshlrev_b32 v53, 16, v46\nv_lshlrev_b32 v55, 16, v47\ns_cbranch_vccnz .LI%=\n.LM%=:\nv_fmac_f32 v49, v27, v44\nv_fmac_f32 v51, v29, v45\n"
        "v_fmac_f32 v53, v31, v46\nv_fmac_f32 v55, v33, v47\nv_cvt_pk_f16_f32 v36, v48, v49\nv_cvt_pk_f16_f32 v37, v50, v51\nv_cvt_pk_f16_f32 v38, v52, v53\nv_cvt_pk_f16_f32 v39, v54, v55\n"
        "ds_write_b128 %[uw], v[36:39]\ns_mov_b64 exec, 1\nds_add_u32 %[cna], %[one] offset:0\ns_mov_b64 exec, -1\ns_movk_i32 %[s0], 0\n"
        "buffer_store_dwordx4 v[56:59], %[mo], %[mrs], %[s0] offen sc0 sc1\ns_movk_i32 %[s0], 12288\nbuffer_store_dwordx4 v[56:59], %[mo], %[mrs], %[s0] offen sc0 sc1\ns_movk_i32 %[s0], 24576\n"
        "buffer_store_dwordx4 v[56:59], %[mo], %[mrs], %[s0] offen sc0 sc1\nv_fmac_f32 v26, v48, %[kd1]\nv_fmac_f32 v27, v49, %[kd1]\nv_fmac_f32 v28, v50, %[kd1]\nv_fmac_f32 v29, v51, %[kd1]\n"
        "v_fmac_f32 v30, v52, %[kd1]\nv_fmac_f32 v31, v53, %[kd1]\nv_fmac_f32 v32, v54, %[kd1]\nv_fmac_f32 v33, v55, %[kd1]\ns_setprio %[pbl]\ns_mov_b32 %[s1], 0x100000\n.LN%=:\n"
        "ds_read_b32 %[pv], %[cna]\ns_waitcnt lgkmcnt(0)\nv_readfirstlane_b32 %[s0], %[pv]\ns_nop 0\ns_cmp_ge_i32 %[s0], 4\ns_cbranch_scc1 .LO%=\ns_sub_u32 %[s1], %[s1], 1\ns_cbranch_scc0 .LN%=\n"
        ".LO%=:\ns_cmp_lt_u32 %[v], 2\ns_cbranch_scc0 .LT%=\nds_read_b128 v[52:55], %[ra] offset:1376\nds_read_b128 v[56:59], %[ra] offset:1312\nds_read_b128 v[60:63], %[ra] offset:1248\n"
        "ds_read_b128 v[64:67], %[rb] offset:1152\nds_read_b128 v[68:71], %[ra] offset:1088\nds_read_b128 v[72:75], %[ra] offset:1024\nds_read_b128 v[76:79], %[ra] offset:960\n"
        "ds_read_b128 v[80:83], %[rb] offset:864\nv_xor_b32 v14, 64, %[wa]\ns_waitcnt lgkmcnt(7)\nv_mfma_f32_16x16x32_f16 v[48:51], v[100:103], v[52:55], 0\nds_read_b128 v[52:55], %[ra] offset:800\n"
        "s_waitcnt lgkmcnt(7)\nv_mfma_f32_16x16x32_f16 v[44:47], v[100:103], v[56:59], 0\ns_waitcnt lgkmcnt(6)\nv_mfma_f32_16x16x32_f16 v[40:43], v[100:103], v[60:63], 0\ns_waitcnt lgkmcnt(5)\n"
        "v_mfma_f32_16x16x32_f16 v[36:39], v[100:103], v[64:67], 0\nv_mfma_f32_16x16x32_f16 v[48:51], v[104:107], v[56:59], v[48:51]\nds_read_b128 v[56:59], %[ra] offset:736\n"
        "v_mfma_f32_16x16x32_f16 v[44:47], v[104:107], v[60:63], v[44:47]\nv_mfma_f32_16x16x32_f16 v[40:43], v[104:107], v[64:67], v[40:43]\ns_waitcnt lgkmcnt(5)\n"
        "v_mfma_f32_16x16x32_f16 v[36:39], v[104:107], v[68:71], v[36:39]\nv_mfma_f32_16x16x32_f16 v[48:51], v[108:111], v[60:63], v[48:51]\nds_read_b128 v[60:63], %[ra] offset:672\n"
        "v_mfma_f32_16x16x32_f16 v[44:47], v[108:111], v[64:67], v[44:47]\nv_mfma_f32_16x16x32_f16 v[40:43], v[108:111], v[68:71], v[40:43]\ns_waitcnt lgkmcnt(5)\n"
        "v_mfma_f32_16x16x32_f16 v[36:39], v[108:111], v[72:75], v[36:39]\nv_mfma_f32_16x16x32_f16 v[48:51], v[112:115], v[64:67], v[48:51]\nds_read_b128 v[64:67], %[rb] offset:576\n"
        "v_mfma_f32_16x16x32_f16 v[44:47], v[112:115], v[68:71], v[44:47]\nv_mfma_f32_16x16x32_f16 v[40:43], v[112:115], v[72:75], v[40:43]\ns_waitcnt lgkmcnt(5)\n"
        "v_mfma_f32_16x16x32_f16 v[36:39], v[112:115], v[76:79], v[36:39]\nv_mfma_f32_16x16x32_f16 v[48:51], v[116:119], v[68:71], v[48:51]\nds_read_b128 v[68:71], %[ra] offset:512\n"
        "v_mfma_f32_16x16x32_f16 v[44:47], v[116:119], v[72:75], v[44:47]\nv_mfma_f32_16x16x32_f16 v[40:43], v[116:119], v[76:79], v[40:43]\ns_waitcnt lgkmcnt(5)\n"
        "v_mfma_f32_16x16x32_f16 v[36:39], v[116:119], v[80:83], v[36:39]\nv_mfma_f32_16x16x32_f16 v[48:51], v[120:123], v[72:75], v[48:51]\nds_read_b128 v[72:75], %[ra] offset:448\n"
        "v_mfma_f32_16x16x32_f16 v[44:47], v[120:123], v[76:79], v[44:47]\nv_mfma_f32_16x16x32_f16 v[40:43], v[120:123], v[80:83], v[40:43]\ns_waitcnt lgkmcnt(5)\n"
        "v_mfma_f32_16x16x32_f16 v[36:39], v[120:123], v[52:55], v[36:39]\nv_mfma_f32_16x16x32_f16 v[48:51], v[124:127], v[76:79], v[48:51]\nds_read_b128 v[76:79], %[ra] offset:384\n"
        "v_mfma_f32_16x16x32_f16 v[44:47], v[124:127], v[80:83], v[44:47]\nv_mfma_f32_16x16x32_f16 v[40:43], v[124:127], v[52:55], v[40:43]\ns_waitcnt lgkmcnt(5)\n"
        "v_mfma_f32_16x16x32_f16 v[36:39], v[124:127], v[56:59], v[36:39]\nv_mfma_f32_16x16x32_f16 v[48:51], v[128:131], v[80:83], v[48:51]\nds_read_b128 v[80:83], %[rb] offset:288\n"
        "v_mfma_f32_16x16x32_f16 v[44:47], v[128:131], v[52:55], v[44:47]\nv_mfma_f32_16x16x32_f16 v[40:43], v[128:131], v[56:59], v[40:43]\ns_waitcnt lgkmcnt(5)\n"
        "v_mfma_f32_16x16x32_f16 v[36:39], v[128:131], v[60:63], v[36:39]\nv_mfma_f32_16x16x32_f16 v[48:51], v[132:135], v[52:55], v[48:51]\nds_read_b128 v[52:55], %[ra] offset:224\n"
        "v_mfma_f32_16x16x32_f16 v[44:47], v[132:135], v[56:59], v[44:47]\nv_mfma_f32_16x16x32_f16 v[40:43], v[132:135], v[60:63], v[40:43]\ns_waitcnt lgkmcnt(5)\n"
        "v_mfma_f32_16x16x32_f16 v[36:39], v[132:135], v[64:67], v[36:39]\nv_mfma_f32_16x16x32_f16 v[48:51], v[136:139], v[56:59], v[48:51]\nds_read_b128 v[56:59], %[ra] offset:160\n"
        "v_mfma_f32_16x16x32_f16 v[44:47], v[136:139], v[60:63], v[44:47]\nv_mfma_f32_16x16x32_f16 v[40:43], v[136:139], v[64:67], v[40:43]\ns_waitcnt lgkmcnt(5)\n"
        "v_mfma_f32_16x16x32_f16 v[36:39], v[136:139], v[68:71], v[36:39]\nv_mfma_f32_16x16x32_f16 v[48:51], v[140:143], v[60:63], v[48:51]\nds_read_b128 v[60:63], %[ra] offset:96\n"
        "v_mfma_f32_16x16x32_f16 v[44:47], v[140:143], v[64:67], v[44:47]\nv_mfma_f32_16x16x32_f16 v[40:43], v[140:143], v[68:71], v[40:43]\ns_waitcnt lgkmcnt(5)\n"
        "v_mfma_f32_16x16x32_f16 v[36:39], v[140:143], v[72:75], v[36:39]\nv_mfma_f32_16x16x32_f16 v[48:51], v[144:147], v[64:67], v[48:51]\nds_read_b128 v[64:67], %[rb] offset:0\n"
        "v_mfma_f32_16x16x32_f16 v[44:47], v[144:147], v[68:71], v[44:47]\nv_mfma_f32_16x16x32_f16 v[40:43], v[144:147], v[72:75], v[40:43]\ns_waitcnt lgkmcnt(5)\n"
        "v_mfma_f32_16x16x32_f16 v[36:39], v[144:147], v[76:79], v[36:39]\nv_mfma_f32_16x16x32_f16 v[48:51], v[148:151], v[68:71], v[48:51]\n"
        "v_mfma_f32_16x16x32_f16 v[44:47], v[148:151], v[72:75], v[44:47]\nv_mfma_f32_16x16x32_f16 v[40:43], v[148:151], v[76:79], v[40:43]\ns_waitcnt lgkmcnt(4)\n"
        "v_mfma_f32_16x16x32_f16 v[36:39], v[148:151], v[80:83], v[36:39]\nv_mfma_f32_16x16x32_f16 v[48:51], v[152:155], v[72:75], v[48:51]\n"
        "v_mfma_f32_16x16x32_f16 v[44:47], v[152:155], v[76:79], v[44:47]\nv_mfma_f32_16x16x32_f16 v[40:43], v[152:155], v[80:83], v[40:43]\ns_waitcnt lgkmcnt(3)\n"
        "v_mfma_f32_16x16x32_f16 v[36:39], v[152:155], v[52:55], v[36:39]\nv_mfma_f32_16x16x32_f16 v[48:51], v[156:159], v[76:79], v[48:51]\n"
        "v_mfma_f32_16x16x32_f16 v[44:47], v[156:159], v[80:83], v[44:47]\nv_mfma_f32_16x16x32_f16 v[40:43], v[156:159], v[52:55], v[40:43]\ns_waitcnt lgkmcnt(2)\n"
        "v_mfma_f32_16x16x32_f16 v[36:39], v[156:159], v[56:59], v[36:39]\nv_mfma_f32_16x16x32_f16 v[48:51], v[160:163], v[80:83], v[48:51]\n"
        "v_mfma_f32_16x16x32_f16 v[44:47], v[160:163], v[52:55], v[44:47]\nv_mfma_f32_16x16x32_f16 v[40:43], v[160:163], v[56:59], v[40:43]\ns_waitcnt lgkmcnt(1)\n"
        "v_mfma_f32_16x16x32_f16 v[36:39], v[160:163], v[60:63], v[36:39]\nv_mfma_f32_16x16x32_f16 v[48:51], v[164:167], v[52:55], v[48:51]\n"
        "v_mfma_f32_16x16x32_f16 v[44:47], v[164:167], v[56:59], v[44:47]\nv_mfma_f32_16x16x32_f16 v[40:43], v[164:167], v[60:63], v[40:43]\ns_waitcnt lgkmcnt(0)\n"
        "v_mfma_f32_16x16x32_f16 v[36:39], v[164:167], v[64:67], v[36:39]\ns_nop 4\nds_write_b128 v14, v[48:51] offset:128\nds_write_b128 %[wa], v[44:47] offset:128\n"
        "ds_write_b128 v14, v[40:43] offset:0\nds_write_b128 %[wa], v[36:39] offset:0\ns_branch .LE%=\n.LT%=:\ns_cmp_eq_u32 %[v], 2\ns_cbranch_scc0 .LV%=\nds_read_b64 v[60:61], %[rb] offset:0\n"
        "ds_read_b128 v[62:65], %[ra] offset:0\nds_read_b64 v[66:67], %[rb] offset:288\nds_read_b128 v[68:71], %[ra] offset:288\nds_read_b64 v[72:73], %[rb] offset:576\n"
        "ds_read_b128 v[74:77], %[ra] offset:576\nds_read_b64 v[78:79], %[rb] offset:864\nds_read_b128 v[80:83], %[ra] offset:864\ns_waitcnt lgkmcnt(6)\n"
        "v_mfma_f32_16x16x32_f16 v[84:87], v[60:63], v[100:103], 0\nv_mfma_f32_16x16x32_f16 v[88:91], v[62:65], v[104:107], 0\nds_read_b64 v[60:61], %[rb] offset:1152\n"
        "ds_read_b128 v[62:65], %[ra] offset:1152\ns_waitcnt lgkmcnt(6)\nv_mfma_f32_16x16x32_f16 v[92:95], v[66:69], v[100:103], 0\nv_mfma_f32_16x16x32_f16 v[96:99], v[68:71], v[104:107], 0\n"
        "ds_read_b64 v[66:67], %[rb] offset:1440\nds_read_b128 v[68:71], %[ra] offset:1440\ns_nop 7\nv_cvt_pk_f16_f32 v136, v84, v85\nv_cvt_pk_f16_f32 v137, v86, v87\nv_cvt_pk_f16_f32 v138, v88, v89\n"
        "v_cvt_pk_f16_f32 v139, v90, v91\ns_nop 1\nv_mfma_f32_16x16x32_f16 v[36:39], v[136:139], v[108:111], 0\ns_waitcnt lgkmcnt(6)\nv_mfma_f32_16x16x32_f16 v[84:87], v[72:75], v[100:103], 0\n"
        "v_mfma_f32_16x16x32_f16 v[88:91], v[74:77], v[104:107], 0\nds_read_b64 v[72:73], %[rb] offset:1728\nds_read_b128 v[74:77], %[ra] offset:1728\nv_cvt_pk_f16_f32 v140, v92, v93\n"
        "v_cvt_pk_f16_f32 v141, v94, v95\nv_cvt_pk_f16_f32 v142, v96, v97\nv_cvt_pk_f16_f32 v143, v98, v99\ns_nop 1\nv_mfma_f32_16x16x32_f16 v[36:39], v[140:143], v[112:115], v[36:39]\n"
        "v_mfma_f32_16x16x32_f16 v[40:43], v[140:143], v[108:111], 0\ns_waitcnt lgkmcnt(6)\nv_mfma_f32_16x16x32_f16 v[92:95], v[78:81], v[100:103], 0\nv_cvt_pk_f16_f32 v136, v84, v85\n"
        "v_cvt_pk_f16_f32 v137, v86, v87\nv_mfma_f32_16x16x32_f16 v[96:99], v[80:83], v[104:107], 0\nv_cvt_pk_f16_f32 v138, v88, v89\nv_cvt_pk_f16_f32 v139, v90, v91\ns_nop 1\n"
        "v_mfma_f32_16x16x32_f16 v[36:39], v[136:139], v[116:119], v[36:39]\nds_read_b64 v[78:79], %[rb] offset:2016\nds_read_b128 v[80:83], %[ra] offset:2016\n"
        "v_mfma_f32_16x16x32_f16 v[40:43], v[136:139], v[112:115], v[40:43]\nv_mfma_f32_16x16x32_f16 v[44:47], v[136:139], v[108:111], 0\nv_cvt_pk_f16_f32 v140, v92, v93\n"
        "v_cvt_pk_f16_f32 v141, v94, v95\ns_waitcnt lgkmcnt(6)\nv_mfma_f32_16x16x32_f16 v[84:87], v[60:63], v[100:103], 0\nv_cvt_pk_f16_f32 v142, v96, v97\nv_cvt_pk_f16_f32 v143, v98, v99\n"
        "v_mfma_f32_16x16x32_f16 v[88:91], v[62:65], v[104:107], 0\nds_read_b64 v[60:61], %[rb] offset:2304\nds_read_b128 v[62:65], %[ra] offset:2304\n"
        "v_mfma_f32_16x16x32_f16 v[36:39], v[140:143], v[120:123], v[36:39]\nv_mfma_f32_16x16x32_f16 v[40:43], v[140:143], v[116:119], v[40:43]\n"
        "v_mfma_f32_16x16x32_f16 v[44:47], v[140:143], v[112:115], v[44:47]\nv_mfma_f32_16x16x32_f16 v[48:51], v[140:143], v[108:111], 0\nv_cvt_pk_f16_f32 v136, v84, v85\n"
        "v_cvt_pk_f16_f32 v137, v86, v87\ns_waitcnt lgkmcnt(6)\nv_mfma_f32_16x16x32_f16 v[92:95], v[66:69], v[100:103], 0\nv_cvt_pk_f16_f32 v138, v88, v89\nv_cvt_pk_f16_f32 v139, v90, v91\n"
        "v_mfma_f32_16x16x32_f16 v[96:99], v[68:71], v[104:107], 0\nds_read_b64 v[66:67], %[rb] offset:2592\nds_read_b128 v[68:71], %[ra] offset:2592\n"
        "v_mfma_f32_16x16x32_f16 v[36:39], v[136:139], v[124:127], v[36:39]\nv_mfma_f32_16x16x32_f16 v[40:43], v[136:139], v[120:123], v[40:43]\n"
        "v_mfma_f32_16x16x32_f16 v[44:47], v[136:139], v[116:119], v[44:47]\nv_mfma_f32_16x16x32_f16 v[48:51], v[136:139], v[112:115], v[48:51]\nds_write_b128 %[wa], v[36:39] offset:0\n"
        "v_mfma_f32_16x16x32_f16 v[52:55], v[136:139], v[108:111], 0\nv_cvt_pk_f16_f32 v140, v92, v93\nv_cvt_pk_f16_f32 v141, v94, v95\ns_waitcnt lgkmcnt(7)\n"
        "v_mfma_f32_16x16x32_f16 v[84:87], v[72:75], v[100:103], 0\nv_cvt_pk_f16_f32 v142, v96, v97\nv_cvt_pk_f16_f32 v143, v98, v99\nv_mfma_f32_16x16x32_f16 v[88:91], v[74:77], v[104:107], 0\n"
        "s_nop 0\nv_mfma_f32_16x16x32_f16 v[40:43], v[140:143], v[124:127], v[40:43]\nv_mfma_f32_16x16x32_f16 v[44:47], v[140:143], v[120:123], v[44:47]\n"
        "v_mfma_f32_16x16x32_f16 v[48:51], v[140:143], v[116:119], v[48:51]\nv_mfma_f32_16x16x32_f16 v[52:55], v[140:143], v[112:115], v[52:55]\nds_write_b128 %[wa], v[40:43] offset:256\n"
        "v_mfma_f32_16x16x32_f16 v[56:59], v[140:143], v[108:111], 0\nv_cvt_pk_f16_f32 v136, v84, v85\nv_cvt_pk_f16_f32 v137, v86, v87\ns_waitcnt lgkmcnt(6)\n"
        "v_mfma_f32_16x16x32_f16 v[92:95], v[78:81], v[100:103], 0\nv_cvt_pk_f16_f32 v138, v88, v89\nv_cvt_pk_f16_f32 v139, v90, v91\nv_mfma_f32_16x16x32_f16 v[96:99], v[80:83], v[104:107], 0\n"
        "s_nop 0\nv_mfma_f32_16x16x32_f16 v[44:47], v[136:139], v[124:127], v[44:47]\nv_mfma_f32_16x16x32_f16 v[48:51], v[136:139], v[120:123], v[48:51]\n"
        "v_mfma_f32_16x16x32_f16 v[52:55], v[136:139], v[116:119], v[52:55]\nv_mfma_f32_16x16x32_f16 v[56:59], v[136:139], v[112:115], v[56:59]\nds_write_b128 %[wa], v[44:47] offset:512\n"
        "v_cvt_pk_f16_f32 v140, v92, v93\ns_waitcnt lgkmcnt(5)\nv_mfma_f32_16x16x32_f16 v[84:87], v[60:63], v[100:103], 0\nv_cvt_pk_f16_f32 v141, v94, v95\nv_cvt_pk_f16_f32 v142, v96, v97\n"
        "v_mfma_f32_16x16x32_f16 v[88:91], v[62:65], v[104:107], 0\nv_cvt_pk_f16_f32 v143, v98, v99\ns_nop 1\nv_mfma_f32_16x16x32_f16 v[48:51], v[140:143], v[124:127], v[48:51]\n"
        "v_mfma_f32_16x16x32_f16 v[52:55], v[140:143], v[120:123], v[52:55]\nv_mfma_f32_16x16x32_f16 v[56:59], v[140:143], v[116:119], v[56:59]\nv_cvt_pk_f16_f32 v136, v84, v85\n"
        "v_cvt_pk_f16_f32 v137, v86, v87\ns_waitcnt lgkmcnt(3)\nv_mfma_f32_16x16x32_f16 v[92:95], v[66:69], v[100:103], 0\nv_cvt_pk_f16_f32 v138, v88, v89\nv_cvt_pk_f16_f32 v139, v90, v91\n"
        "v_mfma_f32_16x16x32_f16 v[96:99], v[68:71], v[104:107], 0\nds_write_b128 %[wa], v[48:51] offset:768\nv_mfma_f32_16x16x32_f16 v[52:55], v[136:139], v[124:127], v[52:55]\n"
        "v_mfma_f32_16x16x32_f16 v[56:59], v[136:139], v[120:123], v[56:59]\ns_nop 7\nv_cvt_pk_f16_f32 v140, v92, v93\nv_cvt_pk_f16_f32 v141, v94, v95\nv_cvt_pk_f16_f32 v142, v96, v97\n"
        "v_cvt_pk_f16_f32 v143, v98, v99\ns_nop 1\nv_mfma_f32_16x16x32_f16 v[56:59], v[140:143], v[124:127], v[56:59]\ns_nop 7\nds_write_b128 %[wa], v[52:55] offset:1024\n"
        "ds_write_b128 %[wa], v[56:59] offset:1280\ns_branch .LE%=\n.LV%=:\nds_read_b128 v[52:55], %[ra] offset:0\nds_read_b128 v[56:59], %[ra] offset:288\nds_read_b128 v[60:63], %[ra] offset:576\n"
        "ds_read_b128 v[64:67], %[ra] offset:864\nds_read_b128 v[84:87], %[rb] offset:0\nds_read_b128 v[88:91], %[rb] offset:576\nds_read_b128 v[92:95], %[rb] offset:1152\n"
        "ds_read_b128 v[96:99], %[rb] offset:1728\ns_waitcnt lgkmcnt(7)\nv_mfma_f32_16x16x32_f16 v[68:71], v[52:55], v[100:103], 0\nv_mfma_f32_16x16x32_f16 v[72:75], v[52:55], v[104:107], 0\n"
        "ds_read_b128 v[52:55], %[ra] offset:1152\ns_waitcnt lgkmcnt(7)\nv_mfma_f32_16x16x32_f16 v[76:79], v[56:59], v[100:103], 0\nv_mfma_f32_16x16x32_f16 v[80:83], v[56:59], v[104:107], 0\n"
        "ds_read_b128 v[56:59], %[ra] offset:1440\ns_waitcnt lgkmcnt(5)\nv_mfma_f32_16x16x32_f16 v[36:39], v[124:127], v[84:87], 0\nv_cvt_pk_f16_f32 v136, v68, v73\nv_cvt_pk_f16_f32 v137, v70, v75\n"
        "s_waitcnt lgkmcnt(4)\nv_mfma_f32_16x16x32_f16 v[40:43], v[124:127], v[88:91], 0\ns_waitcnt lgkmcnt(3)\nv_mfma_f32_16x16x32_f16 v[44:47], v[124:127], v[92:95], 0\n"
        "v_cvt_pk_f16_f32 v138, v76, v81\nv_cvt_pk_f16_f32 v139, v78, v83\ns_waitcnt lgkmcnt(2)\nv_mfma_f32_16x16x32_f16 v[48:51], v[124:127], v[96:99], 0\n"
        "v_mfma_f32_16x16x32_f16 v[68:71], v[60:63], v[100:103], 0\nv_mfma_f32_16x16x32_f16 v[72:75], v[60:63], v[104:107], 0\nds_read_b128 v[60:63], %[ra] offset:1728\n"
        "v_mfma_f32_16x16x32_f16 v[36:39], v[136:139], v[108:111], v[36:39]\nv_mfma_f32_16x16x32_f16 v[40:43], v[136:139], v[112:115], v[40:43]\n"
        "v_mfma_f32_16x16x32_f16 v[44:47], v[136:139], v[116:119], v[44:47]\nv_cvt_pk_f16_f32 v140, v68, v73\nv_cvt_pk_f16_f32 v141, v70, v75\n"
        "v_mfma_f32_16x16x32_f16 v[48:51], v[136:139], v[120:123], v[48:51]\nds_write_b128 %[wa], v[36:39] offset:0\nv_mfma_f32_16x16x32_f16 v[76:79], v[64:67], v[100:103], 0\n"
        "v_mfma_f32_16x16x32_f16 v[80:83], v[64:67], v[104:107], 0\nds_read_b128 v[64:67], %[ra] offset:2016\ns_waitcnt lgkmcnt(4)\nv_mfma_f32_16x16x32_f16 v[68:71], v[52:55], v[100:103], 0\n"
        "v_mfma_f32_16x16x32_f16 v[72:75], v[52:55], v[104:107], 0\ns_nop 7\nv_cvt_pk_f16_f32 v142, v76, v81\nv_cvt_pk_f16_f32 v143, v78, v83\ns_nop 1\n"
        "v_mfma_f32_16x16x32_f16 v[40:43], v[140:143], v[108:111], v[40:43]\nv_mfma_f32_16x16x32_f16 v[44:47], v[140:143], v[112:115], v[44:47]\n"
        "v_mfma_f32_16x16x32_f16 v[48:51], v[140:143], v[116:119], v[48:51]\nv_cvt_pk_f16_f32 v136, v68, v73\nv_cvt_pk_f16_f32 v137, v70, v75\ns_waitcnt lgkmcnt(3)\n"
        "v_mfma_f32_16x16x32_f16 v[76:79], v[56:59], v[100:103], 0\nds_write_b128 %[wa], v[40:43] offset:256\nv_mfma_f32_16x16x32_f16 v[80:83], v[56:59], v[104:107], 0\ns_waitcnt lgkmcnt(3)\n"
        "v_mfma_f32_16x16x32_f16 v[68:71], v[60:63], v[100:103], 0\nv_mfma_f32_16x16x32_f16 v[72:75], v[60:63], v[104:107], 0\ns_nop 7\nv_cvt_pk_f16_f32 v138, v76, v81\n"
        "v_cvt_pk_f16_f32 v139, v78, v83\ns_nop 1\nv_mfma_f32_16x16x32_f16 v[44:47], v[136:139], v[108:111], v[44:47]\nv_mfma_f32_16x16x32_f16 v[48:51], v[136:139], v[112:115], v[48:51]\n"
        "s_waitcnt lgkmcnt(1)\nv_mfma_f32_16x16x32_f16 v[76:79], v[64:67], v[100:103], 0\nv_cvt_pk_f16_f32 v140, v68, v73\nv_cvt_pk_f16_f32 v141, v70, v75\n"
        "v_mfma_f32_16x16x32_f16 v[80:83], v[64:67], v[104:107], 0\nds_write_b128 %[wa], v[44:47] offset:512\ns_nop 7\nv_cvt_pk_f16_f32 v142, v76, v81\nv_cvt_pk_f16_f32 v143, v78, v83\ns_nop 1\n"
        "v_mfma_f32_16x16x32_f16 v[48:51], v[140:143], v[108:111], v[48:51]\ns_nop 7\nds_write_b128 %[wa], v[48:51] offset:768\ns_branch .LE%=\n.LC%=:\ns_sub_u32 %[cap], %[cap], 1\n"
        "s_cbranch_scc0 .LCy%=\ns_mov_b32 %[cap], 0\ns_branch .LD%=\n.LCy%=:\nbuffer_load_dwordx4 v[36:39], %[c16], %[xrs], 0 offen sc1\ns_waitcnt vmcnt(0)\ns_branch .LB%=\n.LI%=:\n"
        "s_sub_u32 %[cap], %[cap], 1\ns_cbranch_scc0 .LIy%=\ns_mov_b32 %[cap], 0\ns_branch .LM%=\n.LIy%=:\ns_sleep %[fp]\nbuffer_load_dwordx4 v[40:43], %[c16], %[frs], 0 offen sc1\n"
        "buffer_load_dwordx4 v[44:47], %[c16], %[frs], %[s1] offen sc1\ns_branch .LF%=\n.LE%=:\ns_waitcnt vmcnt(0)\ns_mov_b64 exec, 1\nds_add_u32 %[cna], %[one] offset:8\ns_mov_b64 exec, -1\n"
        "s_setprio %[pbe]\ns_mov_b32 %[s1], 0x100000\n.LP%=:\nds_read_b32 %[pv], %[cna] offset:8\ns_waitcnt lgkmcnt(0)\nv_readfirstlane_b32 %[s0], %[pv]\ns_nop 0\ns_cmp_ge_i32 %[s0], 4\n"
        "s_cbranch_scc1 .LR%=\ns_sub_u32 %[s1], %[s1], 1\ns_cbranch_scc0 .LP%=\n.LR%=:\ns_cmp_lt_u32 %[wq], 2\ns_cbranch_scc1 .LS3_%=\nds_read_b128 v[36:39], %[exr] offset:0\n"
        "ds_read_b128 v[40:43], %[exr] offset:4096\nds_read_b128 v[44:47], %[mr] offset:0\nds_read_b128 v[48:51], %[mr] offset:3072\nds_read_b128 v[52:55], %[frr] offset:0\n"
        "ds_read_b128 v[56:59], %[frr] offset:2304\ns_waitcnt lgkmcnt(4)\nv_fmac_f32 v26, v36, %[kf]\nv_fmac_f32 v27, v37, %[kf]\nv_fmac_f32 v28, v38, %[kf]\nv_fmac_f32 v29, v39, %[kf]\n"
        "v_fmac_f32 v30, v40, %[kf]\nv_fmac_f32 v31, v41, %[kf]\nv_fmac_f32 v32, v42, %[kf]\nv_fmac_f32 v33, v43, %[kf]\ns_waitcnt lgkmcnt(2)\nv_fmac_f32 v26, v44, %[kf]\nv_fmac_f32 v27, v45, %[kf]\n"
        "v_fmac_f32 v28, v46, %[kf]\nv_fmac_f32 v29, v47, %[kf]\nv_fmac_f32 v30, v48, %[kf]\nv_fmac_f32 v31, v49, %[kf]\nv_fmac_f32 v32, v50, %[kf]\nv_fmac_f32 v33, v51, %[kf]\ns_waitcnt lgkmcnt(0)\n"
        "v_fmac_f32 v26, v52, %[kf]\nv_fmac_f32 v27, v53, %[kf]\nv_fmac_f32 v28, v54, %[kf]\nv_fmac_f32 v29, v55, %[kf]\nv_fmac_f32 v30, v56, %[kf]\nv_fmac_f32 v31, v57, %[kf]\n"
        "v_fmac_f32 v32, v58, %[kf]\nv_fmac_f32 v33, v59, %[kf]\ns_branch .LS%=\n.LS3_%=:\ns_cmp_lt_u32 %[wq], 1\ns_cbranch_scc1 .LS2_%=\nds_read_b128 v[36:39], %[exr] offset:0\n"
        "ds_read_b128 v[40:43], %[exr] offset:4096\nds_read_b128 v[44:47], %[mr] offset:0\nds_read_b128 v[48:51], %[mr] offset:3072\ns_waitcnt lgkmcnt(2)\nv_fmac_f32 v26, v36, %[kf]\n"
        "v_fmac_f32 v27, v37, %[kf]\nv_fmac_f32 v28, v38, %[kf]\nv_fmac_f32 v29, v39, %[kf]\nv_fmac_f32 v30, v40, %[kf]\nv_fmac_f32 v31, v41, %[kf]\nv_fmac_f32 v32, v42, %[kf]\n"
        "v_fmac_f32 v33, v43, %[kf]\ns_waitcnt lgkmcnt(0)\nv_fmac_f32 v26, v44, %[kf]\nv_fmac_f32 v27, v45, %[kf]\nv_fmac_f32 v28, v46, %[kf]\nv_fmac_f32 v29, v47, %[kf]\nv_fmac_f32 v30, v48, %[kf]\n"
        "v_fmac_f32 v31, v49, %[kf]\nv_fmac_f32 v32, v50, %[kf]\nv_fmac_f32 v33, v51, %[kf]\ns_branch .LS%=\n.LS2_%=:\nds_read_b128 v[36:39], %[exr] offset:0\n"
        "ds_read_b128 v[40:43], %[exr] offset:4096\ns_waitcnt lgkmcnt(0)\nv_fmac_f32 v26, v36, %[kf]\nv_fmac_f32 v27, v37, %[kf]\nv_fmac_f32 v28, v38, %[kf]\nv_fmac_f32 v29, v39, %[kf]\n"
        "v_fmac_f32 v30, v40, %[kf]\nv_fmac_f32 v31, v41, %[kf]\nv_fmac_f32 v32, v42, %[kf]\nv_fmac_f32 v33, v43, %[kf]\n.LS%=:\nv_cvt_pk_bf16_f32 v36, v26, v27\nv_cvt_pk_bf16_f32 v37, v28, v29\n"
        "v_cvt_pk_bf16_f32 v38, v30, v31\nv_cvt_pk_bf16_f32 v39, v32, v33\nv_min_u32 v36, -2, v36\nv_min_u32 v37, -2, v37\nv_min_u32 v38, -2, v38\nv_min_u32 v39, -2, v39\n"
        "buffer_store_dwordx4 v[36:39], %[od], %[ors], 0 offen sc0 sc1\nbuffer_store_dwordx4 v[36:39], %[oh], %[ors], 0 offen sc0 sc1\ns_nop 1\n.LZ%=:\n"
        : "+{v[100:103]}"(kt[0]), "+{v[104:107]}"(kt[1]), "+{v[108:111]}"(kt[2]), "+{v[112:115]}"(kt[3]), "+{v[116:119]}"(kt[4]), "+{v[120:123]}"(kt[5]), "+{v[124:127]}"(kt[6]), "+{v[128:131]}"(kt[7]), "+{v[132:135]}"(kt[8]), "+{v[136:139]}"(kt[9]), "+{v[140:143]}"(kt[10]), "+{v[144:147]}"(kt[11]), "+{v[148:151]}"(kt[12]), "+{v[152:155]}"(kt[13]), "+{v[156:159]}"(kt[14]), "+{v[160:163]}"(kt[15]), "+{v[164:167]}"(kt[16]), [pv] "=&{v16}"(pv), [s0] "=&s"(s0_), [s1] "=&s"(s1_), [cap] "=&s"(cap)
        : [ra] "{v1}"(ra), [rb] "{v2}"(rb), [wa] "{v3}"(wa), [cna] "{v4}"(cna), [one] "{v5}"(one), [exr] "{v6}"(exr), [frr] "{v7}"(frr), [mr] "{v8}"(mr_), [od] "{v9}"(od), [kf] "{v10}"(kd[0]), [kd1] "{v11}"(kd[1]), [c16] "{v12}"(c16), [uw] "{v17}"(uw_), [sy] "{v23}"(sya), [ofs] "i"(SY_FS), [oh] "{v18}"(oh), [mo] "{v19}"(mo), [mrs] "s"(mrs), [pbl] "i"(PBL), [v] "s"(v), [wq] "s"(wq), [ors] "s"(ors), [xrs] "s"(xrs), [frs] "s"(frs), [pbe] "i"(PBE), [fsx] "i"(FSX), [fp] "i"(FPOLL)
        : "memory", "scc", "v13", "v14", "v15", "v26", "v27", "v28", "v29", "v30", "v31", "v32", "v33", "v36", "v37", "v38", "v39", "v40", "v41", "v42", "v43", "v44", "v45", "v46", "v47", "v48", "v49", "v50", "v51", "v52", "v53", "v54", "v55", "v56", "v57", "v58", "v59", "v60", "v61", "v62", "v63", "v64", "v65", "v66", "v67", "v68", "v69", "v70", "v71", "v72", "v73", "v74", "v75", "v76", "v77", "v78", "v79", "v80", "v81", "v82", "v83", "v84", "v85", "v86", "v87", "v88", "v89", "v90", "v91", "v92", "v93", "v94", "v95", "v96", "v97", "v98", "v99", "vcc");
    } else if (layer) {
      asm volatile(
        "s_mov_b32 %[cap], 0x100000\ns_movk_i32 %[s1], 4096\ns_sleep %[fsx]\nbuffer_load_dwordx4 v[36:39], %[c16], %[xrs], 0 offen sc1\ns_cmp_eq_u32 %[wq], 3\ns_cbranch_scc0 .LA%=\ns_sleep %[fsa]\n"
        "s_mov_b64 exec, 1\nds_add_u32 %[sy], %[one] offset:%[ofs]\ns_mov_b64 exec, -1\ns_sleep 1\ns_branch .LX%=\n.LA%=:\nds_read_b32 %[pv], %[sy] offset:%[ofs]\ns_waitcnt lgkmcnt(0)\n"
        "v_readfirstlane_b32 %[s0], %[pv]\ns_nop 0\ns_cmp_ge_i32 %[s0], 2\ns_cbranch_scc1 .LX%=\ns_sub_u32 %[cap], %[cap], 1\ns_cbranch_scc0 .LA%=\n.LX%=:\ns_mov_b32 %[cap], 0x10000\n"
        "buffer_load_dwordx4 v[40:43], %[c16], %[frs], 0 offen sc1\nbuffer_load_dwordx4 v[44:47], %[c16], %[frs], %[s1] offen sc1\ns_waitcnt vmcnt(2)\n.LB%=:\nv_max3_u32 %[pv], v36, v37, v38\n"
        "v_max_u32 %[pv], %[pv], v39\nv_cmp_eq_u32 vcc, -1, %[pv]\ns_cbranch_vccnz .LC%=\n.LD%=:\nv_lshlrev_b32 v26, 16, v36\nv_and_b32 v27, 0xffff0000, v36\nv_lshlrev_b32 v28, 16, v37\n"
        "v_and_b32 v29, 0xffff0000, v37\nv_lshlrev_b32 v30, 16, v38\nv_and_b32 v31, 0xffff0000, v38\nv_lshlrev_b32 v32, 16, v39\nv_and_b32 v33, 0xffff0000, v39\n.LF%=:\ns_waitcnt vmcnt(1)\n"
        "v_max3_u32 %[pv], v40, v41, v42\nv_lshlrev_b32 v48, 16, v40\nv_lshlrev_b32 v50, 16, v41\nv_lshlrev_b32 v52, 16, v42\nv_lshlrev_b32 v54, 16, v43\nv_fmac_f32 v48, v26, v40\n"
        "v_fmac_f32 v50, v28, v41\nv_fmac_f32 v52, v30, v42\nv_fmac_f32 v54, v32, v43\ns_waitcnt vmcnt(0)\nv_max3_u32 %[pv], %[pv], v43, v44\nv_max3_u32 %[pv], %[pv], v45, v46\n"
        "v_max_u32 %[pv], %[pv], v47\nv_cmp_eq_u32 vcc, -1, %[pv]\nv_lshlrev_b32 v49, 16, v44\nv_lshlrev_b32 v51, 16, v45\nv_lshlrev_b32 v53, 16, v46\nv_lshlrev_b32 v55, 16, v47\n"
        "s_cbranch_vccnz .LI%=\n.LM%=:\nv_fmac_f32 v49, v27, v44\nv_fmac_f32 v51, v29, v45\nv_fmac_f32 v53, v31, v46\nv_fmac_f32 v55, v33, v47\nv_cvt_pk_f16_f32 v36, v48, v49\n"
        "v_cvt_pk_f16_f32 v37, v50, v51\nv_cvt_pk_f16_f32 v38, v52, v53\nv_cvt_pk_f16_f32 v39, v54, v55\nds_write_b128 %[uw], v[36:39]\ns_mov_b64 exec, 1\nds_add_u32 %[cna], %[one] offset:0\n"
        "s_mov_b64 exec, -1\ns_setprio %[pbl]\ns_mov_b32 %[s1], 0x100000\n.LN%=:\nds_read_b32 %[pv], %[cna]\ns_waitcnt lgkmcnt(0)\nv_readfirstlane_b32 %[s0], %[pv]\ns_nop 0\ns_cmp_ge_i32 %[s0], 4\n"
        "s_cbranch_scc1 .LO%=\ns_sub_u32 %[s1], %[s1], 1\ns_cbranch_scc0 .LN%=\n.LO%=:\nbuffer_load_dwordx4 v[152:155], %[xo], %[xrs], 0 offen sc1\nds_read_b128 v[156:159], %[ue]\n"
        "s_cmp_lt_u32 %[v], 2\ns_cbranch_scc0 .LT%=\nds_read_b128 v[52:55], %[ra] offset:800\nds_read_b128 v[56:59], %[ra] offset:736\nds_read_b128 v[60:63], %[ra] offset:672\n"
        "ds_read_b128 v[64:67], %[rb] offset:576\nds_read_b128 v[68:71], %[ra] offset:512\nds_read_b128 v[72:75], %[ra] offset:448\nds_read_b128 v[76:79], %[ra] offset:384\n"
        "ds_read_b128 v[80:83], %[rb] offset:288\nv_xor_b32 v14, 32, %[wa]\nv_xor_b32 v15, 64, %[wa]\nv_xor_b32 v13, 96, %[wa]\ns_waitcnt lgkmcnt(7)\n"
        "v_mfma_f32_16x16x32_f16 v[48:51], v[100:103], v[52:55], 0\nds_read_b128 v[52:55], %[ra] offset:224\ns_waitcnt lgkmcnt(7)\nv_mfma_f32_16x16x32_f16 v[44:47], v[100:103], v[56:59], 0\n"
        "s_waitcnt lgkmcnt(6)\nv_mfma_f32_16x16x32_f16 v[40:43], v[100:103], v[60:63], 0\ns_waitcnt lgkmcnt(5)\nv_mfma_f32_16x16x32_f16 v[36:39], v[100:103], v[64:67], 0\n"
        "v_mfma_f32_16x16x32_f16 v[48:51], v[104:107], v[56:59], v[48:51]\nds_read_b128 v[56:59], %[ra] offset:160\nv_mfma_f32_16x16x32_f16 v[44:47], v[104:107], v[60:63], v[44:47]\n"
        "v_mfma_f32_16x16x32_f16 v[40:43], v[104:107], v[64:67], v[40:43]\ns_waitcnt lgkmcnt(5)\nv_mfma_f32_16x16x32_f16 v[36:39], v[104:107], v[68:71], v[36:39]\n"
        "v_mfma_f32_16x16x32_f16 v[48:51], v[108:111], v[60:63], v[48:51]\nds_read_b128 v[60:63], %[ra] offset:96\nv_mfma_f32_16x16x32_f16 v[44:47], v[108:111], v[64:67], v[44:47]\n"
        "v_mfma_f32_16x16x32_f16 v[40:43], v[108:111], v[68:71], v[40:43]\ns_waitcnt lgkmcnt(5)\nv_mfma_f32_16x16x32_f16 v[36:39], v[108:111], v[72:75], v[36:39]\n"
        "v_mfma_f32_16x16x32_f16 v[48:51], v[112:115], v[64:67], v[48:51]\nds_read_b128 v[64:67], %[rb] offset:0\nv_mfma_f32_16x16x32_f16 v[44:47], v[112:115], v[68:71], v[44:47]\n"
        "v_mfma_f32_16x16x32_f16 v[40:43], v[112:115], v[72:75], v[40:43]\ns_waitcnt lgkmcnt(5)\nv_mfma_f32_16x16x32_f16 v[36:39], v[112:115], v[76:79], v[36:39]\n"
        "v_mfma_f32_16x16x32_f16 v[48:51], v[116:119], v[68:71], v[48:51]\nv_mfma_f32_16x16x32_f16 v[44:47], v[116:119], v[72:75], v[44:47]\n"
        "v_mfma_f32_16x16x32_f16 v[40:43], v[116:119], v[76:79], v[40:43]\ns_waitcnt lgkmcnt(4)\nv_mfma_f32_16x16x32_f16 v[36:39], v[116:119], v[80:83], v[36:39]\n"
        "v_mfma_f32_16x16x32_f16 v[48:51], v[120:123], v[72:75], v[48:51]\nv_mfma_f32_16x16x32_f16 v[44:47], v[120:123], v[76:79], v[44:47]\n"
        "v_mfma_f32_16x16x32_f16 v[40:43], v[120:123], v[80:83], v[40:43]\ns_waitcnt lgkmcnt(3)\nv_mfma_f32_16x16x32_f16 v[36:39], v[120:123], v[52:55], v[36:39]\n"
        "v_mfma_f32_16x16x32_f16 v[48:51], v[124:127], v[76:79], v[48:51]\nv_mfma_f32_16x16x32_f16 v[44:47], v[124:127], v[80:83], v[44:47]\n"
        "v_mfma_f32_16x16x32_f16 v[40:43], v[124:127], v[52:55], v[40:43]\ns_waitcnt lgkmcnt(2)\nv_mfma_f32_16x16x32_f16 v[36:39], v[124:127], v[56:59], v[36:39]\n"
        "v_mfma_f32_16x16x32_f16 v[48:51], v[128:131], v[80:83], v[48:51]\nv_mfma_f32_16x16x32_f16 v[44:47], v[128:131], v[52:55], v[44:47]\n"
        "v_mfma_f32_16x16x32_f16 v[40:43], v[128:131], v[56:59], v[40:43]\ns_waitcnt lgkmcnt(1)\nv_mfma_f32_16x16x32_f16 v[36:39], v[128:131], v[60:63], v[36:39]\ns_cmp_eq_u32 %[v], 0\n"
        "s_cbranch_scc1 .LU%=\nv_mfma_f32_16x16x32_f16 v[48:51], v[132:135], v[52:55], v[48:51]\nv_mfma_f32_16x16x32_f16 v[44:47], v[132:135], v[56:59], v[44:47]\n"
        "v_mfma_f32_16x16x32_f16 v[40:43], v[132:135], v[60:63], v[40:43]\ns_waitcnt lgkmcnt(0)\nv_mfma_f32_16x16x32_f16 v[36:39], v[132:135], v[64:67], v[36:39]\n.LU%=:\ns_nop 4\n"
        "ds_write_b128 v13, v[48:51]\nds_write_b128 v15, v[44:47]\nds_write_b128 v14, v[40:43]\nds_write_b128 %[wa], v[36:39]\ns_branch .LE%=\n.LT%=:\ns_cmp_eq_u32 %[v], 2\ns_cbranch_scc0 .LV%=\n"
        "ds_read_b64 v[60:61], %[rb] offset:0\nds_read_b128 v[62:65], %[ra] offset:0\nds_read_b64 v[66:67], %[rb] offset:288\nds_read_b128 v[68:71], %[ra] offset:288\n"
        "ds_read_b64 v[72:73], %[rb] offset:576\nds_read_b128 v[74:77], %[ra] offset:576\nds_read_b64 v[78:79], %[rb] offset:864\nds_read_b128 v[80:83], %[ra] offset:864\ns_waitcnt lgkmcnt(6)\n"
        "v_mfma_f32_16x16x32_f16 v[84:87], v[60:63], v[100:103], 0\nv_mfma_f32_16x16x32_f16 v[88:91], v[62:65], v[104:107], 0\nds_read_b64 v[60:61], %[rb] offset:1152\n"
        "ds_read_b128 v[62:65], %[ra] offset:1152\ns_waitcnt lgkmcnt(6)\nv_mfma_f32_16x16x32_f16 v[92:95], v[66:69], v[100:103], 0\nv_mfma_f32_16x16x32_f16 v[96:99], v[68:71], v[104:107], 0\n"
        "ds_read_b64 v[66:67], %[rb] offset:1440\nds_read_b128 v[68:71], %[ra] offset:1440\ns_nop 7\nv_cvt_pk_f16_f32 v136, v84, v85\nv_cvt_pk_f16_f32 v137, v86, v87\nv_cvt_pk_f16_f32 v138, v88, v89\n"
        "v_cvt_pk_f16_f32 v139, v90, v91\ns_nop 1\nv_mfma_f32_16x16x32_f16 v[36:39], v[136:139], v[108:111], 0\ns_waitcnt lgkmcnt(6)\nv_mfma_f32_16x16x32_f16 v[84:87], v[72:75], v[100:103], 0\n"
        "v_mfma_f32_16x16x32_f16 v[88:91], v[74:77], v[104:107], 0\nds_read_b64 v[72:73], %[rb] offset:1728\nds_read_b128 v[74:77], %[ra] offset:1728\nv_cvt_pk_f16_f32 v140, v92, v93\n"
        "v_cvt_pk_f16_f32 v141, v94, v95\nv_cvt_pk_f16_f32 v142, v96, v97\nv_cvt_pk_f16_f32 v143, v98, v99\ns_nop 1\nv_mfma_f32_16x16x32_f16 v[36:39], v[140:143], v[112:115], v[36:39]\n"
        "s_waitcnt lgkmcnt(6)\nv_mfma_f32_16x16x32_f16 v[92:95], v[78:81], v[100:103], 0\nv_mfma_f32_16x16x32_f16 v[96:99], v[80:83], v[104:107], 0\nds_read_b64 v[78:79], %[rb] offset:2016\n"
        "ds_read_b128 v[80:83], %[ra] offset:2016\nv_cvt_pk_f16_f32 v136, v84, v85\nv_cvt_pk_f16_f32 v137, v86, v87\nv_cvt_pk_f16_f32 v138, v88, v89\nv_cvt_pk_f16_f32 v139, v90, v91\ns_nop 1\n"
        "v_mfma_f32_16x16x32_f16 v[36:39], v[136:139], v[116:119], v[36:39]\nv_mfma_f32_16x16x32_f16 v[40:43], v[136:139], v[108:111], 0\ns_waitcnt lgkmcnt(6)\n"
        "v_mfma_f32_16x16x32_f16 v[84:87], v[60:63], v[100:103], 0\nv_cvt_pk_f16_f32 v140, v92, v93\nv_cvt_pk_f16_f32 v141, v94, v95\nv_mfma_f32_16x16x32_f16 v[88:91], v[62:65], v[104:107], 0\n"
        "v_cvt_pk_f16_f32 v142, v96, v97\nv_cvt_pk_f16_f32 v143, v98, v99\ns_nop 1\nv_mfma_f32_16x16x32_f16 v[36:39], v[140:143], v[120:123], v[36:39]\nds_read_b64 v[60:61], %[rb] offset:2304\n"
        "ds_read_b128 v[62:65], %[ra] offset:2304\nv_mfma_f32_16x16x32_f16 v[40:43], v[140:143], v[112:115], v[40:43]\ns_waitcnt lgkmcnt(6)\nv_mfma_f32_16x16x32_f16 v[92:95], v[66:69], v[100:103], 0\n"
        "v_cvt_pk_f16_f32 v136, v84, v85\nv_cvt_pk_f16_f32 v137, v86, v87\nv_mfma_f32_16x16x32_f16 v[96:99], v[68:71], v[104:107], 0\nv_cvt_pk_f16_f32 v138, v88, v89\nv_cvt_pk_f16_f32 v139, v90, v91\n"
        "s_nop 1\nv_mfma_f32_16x16x32_f16 v[36:39], v[136:139], v[124:127], v[36:39]\nds_read_b64 v[66:67], %[rb] offset:2592\nds_read_b128 v[68:71], %[ra] offset:2592\n"
        "v_mfma_f32_16x16x32_f16 v[40:43], v[136:139], v[116:119], v[40:43]\nv_mfma_f32_16x16x32_f16 v[44:47], v[136:139], v[108:111], 0\nv_cvt_pk_f16_f32 v140, v92, v93\n"
        "v_cvt_pk_f16_f32 v141, v94, v95\ns_waitcnt lgkmcnt(6)\nv_mfma_f32_16x16x32_f16 v[84:87], v[72:75], v[100:103], 0\nv_cvt_pk_f16_f32 v142, v96, v97\nv_cvt_pk_f16_f32 v143, v98, v99\n"
        "v_mfma_f32_16x16x32_f16 v[88:91], v[74:77], v[104:107], 0\ns_nop 0\nv_mfma_f32_16x16x32_f16 v[36:39], v[140:143], v[128:131], v[36:39]\n"
        "v_mfma_f32_16x16x32_f16 v[40:43], v[140:143], v[120:123], v[40:43]\nv_mfma_f32_16x16x32_f16 v[44:47], v[140:143], v[112:115], v[44:47]\nv_cvt_pk_f16_f32 v136, v84, v85\n"
        "v_cvt_pk_f16_f32 v137, v86, v87\ns_waitcnt lgkmcnt(4)\nv_mfma_f32_16x16x32_f16 v[92:95], v[78:81], v[100:103], 0\nv_cvt_pk_f16_f32 v138, v88, v89\nv_cvt_pk_f16_f32 v139, v90, v91\n"
        "v_mfma_f32_16x16x32_f16 v[96:99], v[80:83], v[104:107], 0\nds_write_b128 %[wa], v[36:39] offset:0\nv_mfma_f32_16x16x32_f16 v[40:43], v[136:139], v[124:127], v[40:43]\n"
        "v_mfma_f32_16x16x32_f16 v[44:47], v[136:139], v[116:119], v[44:47]\ns_waitcnt lgkmcnt(3)\nv_mfma_f32_16x16x32_f16 v[84:87], v[60:63], v[100:103], 0\nv_cvt_pk_f16_f32 v140, v92, v93\n"
        "v_cvt_pk_f16_f32 v141, v94, v95\nv_mfma_f32_16x16x32_f16 v[88:91], v[62:65], v[104:107], 0\nv_cvt_pk_f16_f32 v142, v96, v97\nv_cvt_pk_f16_f32 v143, v98, v99\ns_nop 1\n"
        "v_mfma_f32_16x16x32_f16 v[40:43], v[140:143], v[128:131], v[40:43]\nv_mfma_f32_16x16x32_f16 v[44:47], v[140:143], v[120:123], v[44:47]\ns_waitcnt lgkmcnt(1)\n"
        "v_mfma_f32_16x16x32_f16 v[92:95], v[66:69], v[100:103], 0\nv_cvt_pk_f16_f32 v136, v84, v85\nv_cvt_pk_f16_f32 v137, v86, v87\nv_mfma_f32_16x16x32_f16 v[96:99], v[68:71], v[104:107], 0\n"
        "v_cvt_pk_f16_f32 v138, v88, v89\nv_cvt_pk_f16_f32 v139, v90, v91\ns_nop 1\nv_mfma_f32_16x16x32_f16 v[44:47], v[136:139], v[124:127], v[44:47]\nds_write_b128 %[wa], v[40:43] offset:256\n"
        "s_nop 7\nv_cvt_pk_f16_f32 v140, v92, v93\nv_cvt_pk_f16_f32 v141, v94, v95\nv_cvt_pk_f16_f32 v142, v96, v97\nv_cvt_pk_f16_f32 v143, v98, v99\ns_nop 1\n"
        "v_mfma_f32_16x16x32_f16 v[44:47], v[140:143], v[128:131], v[44:47]\ns_nop 7\nds_write_b128 %[wa], v[44:47] offset:512\ns_branch .LE%=\n.LV%=:\nds_read_b128 v[52:55], %[ra] offset:0\n"
        "ds_read_b128 v[56:59], %[ra] offset:288\nds_read_b128 v[60:63], %[ra] offset:576\nds_read_b128 v[64:67], %[ra] offset:864\nds_read_b128 v[84:87], %[rb] offset:0\n"
        "ds_read_b128 v[88:91], %[rb] offset:1152\ns_waitcnt lgkmcnt(5)\nv_mfma_f32_16x16x32_f16 v[68:71], v[52:55], v[100:103], 0\nv_mfma_f32_16x16x32_f16 v[72:75], v[52:55], v[104:107], 0\n"
        "ds_read_b128 v[52:55], %[ra] offset:1152\ns_waitcnt lgkmcnt(5)\nv_mfma_f32_16x16x32_f16 v[76:79], v[56:59], v[100:103], 0\nv_mfma_f32_16x16x32_f16 v[80:83], v[56:59], v[104:107], 0\n"
        "ds_read_b128 v[56:59], %[ra] offset:1440\ns_waitcnt lgkmcnt(3)\nv_mfma_f32_16x16x32_f16 v[36:39], v[124:127], v[84:87], 0\nv_cvt_pk_f16_f32 v136, v68, v73\nv_cvt_pk_f16_f32 v137, v70, v75\n"
        "s_waitcnt lgkmcnt(2)\nv_mfma_f32_16x16x32_f16 v[40:43], v[124:127], v[88:91], 0\nv_mfma_f32_16x16x32_f16 v[68:71], v[60:63], v[100:103], 0\nv_cvt_pk_f16_f32 v138, v76, v81\n"
        "v_cvt_pk_f16_f32 v139, v78, v83\nv_mfma_f32_16x16x32_f16 v[72:75], v[60:63], v[104:107], 0\nds_read_b128 v[60:63], %[ra] offset:1728\n"
        "v_mfma_f32_16x16x32_f16 v[36:39], v[136:139], v[112:115], v[36:39]\nv_mfma_f32_16x16x32_f16 v[40:43], v[136:139], v[120:123], v[40:43]\n"
        "v_mfma_f32_16x16x32_f16 v[76:79], v[64:67], v[100:103], 0\nv_cvt_pk_f16_f32 v140, v68, v73\nv_cvt_pk_f16_f32 v141, v70, v75\nv_mfma_f32_16x16x32_f16 v[80:83], v[64:67], v[104:107], 0\n"
        "ds_read_b128 v[64:67], %[ra] offset:2016\ns_waitcnt lgkmcnt(3)\nv_mfma_f32_16x16x32_f16 v[68:71], v[52:55], v[100:103], 0\nv_mfma_f32_16x16x32_f16 v[72:75], v[52:55], v[104:107], 0\ns_nop 7\n"
        "v_cvt_pk_f16_f32 v142, v76, v81\nv_cvt_pk_f16_f32 v143, v78, v83\ns_nop 1\nv_mfma_f32_16x16x32_f16 v[36:39], v[140:143], v[108:111], v[36:39]\n"
        "v_mfma_f32_16x16x32_f16 v[40:43], v[140:143], v[116:119], v[40:43]\ns_waitcnt lgkmcnt(2)\nv_mfma_f32_16x16x32_f16 v[76:79], v[56:59], v[100:103], 0\nv_cvt_pk_f16_f32 v136, v68, v73\n"
        "v_cvt_pk_f16_f32 v137, v70, v75\nv_mfma_f32_16x16x32_f16 v[80:83], v[56:59], v[104:107], 0\nds_write_b128 %[wa], v[36:39] offset:0\ns_waitcnt lgkmcnt(2)\n"
        "v_mfma_f32_16x16x32_f16 v[68:71], v[60:63], v[100:103], 0\nv_mfma_f32_16x16x32_f16 v[72:75], v[60:63], v[104:107], 0\ns_nop 7\nv_cvt_pk_f16_f32 v138, v76, v81\n"
        "v_cvt_pk_f16_f32 v139, v78, v83\ns_nop 1\nv_mfma_f32_16x16x32_f16 v[40:43], v[136:139], v[112:115], v[40:43]\ns_waitcnt lgkmcnt(1)\nv_mfma_f32_16x16x32_f16 v[76:79], v[64:67], v[100:103], 0\n"
        "v_mfma_f32_16x16x32_f16 v[80:83], v[64:67], v[104:107], 0\nv_cvt_pk_f16_f32 v140, v68, v73\nv_cvt_pk_f16_f32 v141, v70, v75\ns_nop 7\nv_cvt_pk_f16_f32 v142, v76, v81\n"
        "v_cvt_pk_f16_f32 v143, v78, v83\ns_nop 1\nv_mfma_f32_16x16x32_f16 v[40:43], v[140:143], v[108:111], v[40:43]\ns_nop 7\nds_write_b128 %[wa], v[40:43] offset:256\ns_branch .LE%=\n.LC%=:\n"
        "s_sub_u32 %[cap], %[cap], 1\ns_cbranch_scc0 .LCy%=\ns_mov_b32 %[cap], 0\ns_branch .LD%=\n.LCy%=:\nbuffer_load_dwordx4 v[36:39], %[c16], %[xrs], 0 offen sc1\ns_waitcnt vmcnt(0)\n"
        "s_branch .LB%=\n.LI%=:\ns_sub_u32 %[cap], %[cap], 1\ns_cbranch_scc0 .LIy%=\ns_mov_b32 %[cap], 0\ns_branch .LM%=\n.LIy%=:\ns_sleep %[fp]\n"
        "buffer_load_dwordx4 v[40:43], %[c16], %[frs], 0 offen sc1\nbuffer_load_dwordx4 v[44:47], %[c16], %[frs], %[s1] offen sc1\ns_branch .LF%=\n.LE%=:\ns_waitcnt vmcnt(0)\ns_mov_b64 exec, 1\n"
        "ds_add_u32 %[cna], %[one] offset:8\ns_mov_b64 exec, -1\ns_setprio %[pbe]\ns_cmp_lt_u32 %[wq], 2\ns_cbranch_scc0 .LZ%=\nv_lshlrev_b32 v26, 16, v152\nv_and_b32 v27, 0xffff0000, v152\n"
        "v_cvt_f32_f16 v152, v156\nv_lshrrev_b32 v156, 16, v156\nv_lshlrev_b32 v28, 16, v153\nv_and_b32 v29, 0xffff0000, v153\nv_cvt_f32_f16 v153, v157\nv_lshrrev_b32 v157, 16, v157\n"
        "v_lshlrev_b32 v30, 16, v154\nv_and_b32 v31, 0xffff0000, v154\nv_cvt_f32_f16 v154, v158\nv_lshrrev_b32 v158, 16, v158\nv_lshlrev_b32 v32, 16, v155\nv_and_b32 v33, 0xffff0000, v155\n"
        "v_cvt_f32_f16 v155, v159\nv_lshrrev_b32 v159, 16, v159\nv_cvt_f32_f16 v156, v156\nv_fmac_f32 v26, v152, %[kd1]\nv_cvt_f32_f16 v157, v157\nv_fmac_f32 v28, v153, %[kd1]\n"
        "v_cvt_f32_f16 v158, v158\nv_fmac_f32 v30, v154, %[kd1]\nv_cvt_f32_f16 v159, v159\nv_fmac_f32 v32, v155, %[kd1]\nv_fmac_f32 v27, v156, %[kd1]\nv_fmac_f32 v29, v157, %[kd1]\n"
        "v_fmac_f32 v31, v158, %[kd1]\nv_fmac_f32 v33, v159, %[kd1]\ns_mov_b32 %[s1], 0x100000\n.LP%=:\nds_read_b32 %[pv], %[cna] offset:8\ns_waitcnt lgkmcnt(0)\nv_readfirstlane_b32 %[s0], %[pv]\n"
        "s_nop 0\ns_cmp_ge_i32 %[s0], 4\ns_cbranch_scc1 .LR%=\ns_sub_u32 %[s1], %[s1], 1\ns_cbranch_scc0 .LP%=\n.LR%=:\ns_cmp_lt_u32 %[wq], 1\ns_cbranch_scc1 .LS4_%=\n"
        "ds_read_b128 v[36:39], %[exr] offset:0\nds_read_b128 v[40:43], %[exr] offset:2048\nds_read_b128 v[44:47], %[exr] offset:4096\nds_read_b128 v[48:51], %[exr] offset:6144\n"
        "ds_read_b128 v[52:55], %[mr] offset:0\nds_read_b128 v[56:59], %[mr] offset:3072\nds_read_b128 v[60:63], %[frr] offset:0\nds_read_b128 v[64:67], %[frr] offset:1152\ns_waitcnt lgkmcnt(6)\n"
        "v_fmac_f32 v26, v36, %[kf]\nv_fmac_f32 v27, v37, %[kf]\nv_fmac_f32 v28, v38, %[kf]\nv_fmac_f32 v29, v39, %[kf]\nv_fmac_f32 v30, v40, %[kf]\nv_fmac_f32 v31, v41, %[kf]\n"
        "v_fmac_f32 v32, v42, %[kf]\nv_fmac_f32 v33, v43, %[kf]\ns_waitcnt lgkmcnt(4)\nv_fmac_f32 v26, v44, %[kf]\nv_fmac_f32 v27, v45, %[kf]\nv_fmac_f32 v28, v46, %[kf]\nv_fmac_f32 v29, v47, %[kf]\n"
        "v_fmac_f32 v30, v48, %[kf]\nv_fmac_f32 v31, v49, %[kf]\nv_fmac_f32 v32, v50, %[kf]\nv_fmac_f32 v33, v51, %[kf]\ns_waitcnt lgkmcnt(2)\nv_fmac_f32 v26, v52, %[kf4]\nv_fmac_f32 v27, v53, %[kf4]\n"
        "v_fmac_f32 v28, v54, %[kf4]\nv_fmac_f32 v29, v55, %[kf4]\nv_fmac_f32 v30, v56, %[kf4]\nv_fmac_f32 v31, v57, %[kf4]\nv_fmac_f32 v32, v58, %[kf4]\nv_fmac_f32 v33, v59, %[kf4]\n"
        "s_waitcnt lgkmcnt(0)\nv_fmac_f32 v26, v60, %[kf]\nv_fmac_f32 v27, v61, %[kf]\nv_fmac_f32 v28, v62, %[kf]\nv_fmac_f32 v29, v63, %[kf]\nv_fmac_f32 v30, v64, %[kf]\nv_fmac_f32 v31, v65, %[kf]\n"
        "v_fmac_f32 v32, v66, %[kf]\nv_fmac_f32 v33, v67, %[kf]\ns_branch .LS%=\n.LS4_%=:\nds_read_b128 v[36:39], %[exr] offset:0\nds_read_b128 v[40:43], %[exr] offset:2048\n"
        "ds_read_b128 v[44:47], %[exr] offset:4096\nds_read_b128 v[48:51], %[exr] offset:6144\nds_read_b128 v[52:55], %[mr] offset:0\nds_read_b128 v[56:59], %[mr] offset:3072\ns_waitcnt lgkmcnt(4)\n"
        "v_fmac_f32 v26, v36, %[kf]\nv_fmac_f32 v27, v37, %[kf]\nv_fmac_f32 v28, v38, %[kf]\nv_fmac_f32 v29, v39, %[kf]\nv_fmac_f32 v30, v40, %[kf]\nv_fmac_f32 v31, v41, %[kf]\n"
        "v_fmac_f32 v32, v42, %[kf]\nv_fmac_f32 v33, v43, %[kf]\ns_waitcnt lgkmcnt(2)\nv_fmac_f32 v26, v44, %[kf]\nv_fmac_f32 v27, v45, %[kf]\nv_fmac_f32 v28, v46, %[kf]\nv_fmac_f32 v29, v47, %[kf]\n"
        "v_fmac_f32 v30, v48, %[kf]\nv_fmac_f32 v31, v49, %[kf]\nv_fmac_f32 v32, v50, %[kf]\nv_fmac_f32 v33, v51, %[kf]\ns_waitcnt lgkmcnt(0)\nv_fmac_f32 v26, v52, %[kf4]\nv_fmac_f32 v27, v53, %[kf4]\n"
        "v_fmac_f32 v28, v54, %[kf4]\nv_fmac_f32 v29, v55, %[kf4]\nv_fmac_f32 v30, v56, %[kf4]\nv_fmac_f32 v31, v57, %[kf4]\nv_fmac_f32 v32, v58, %[kf4]\nv_fmac_f32 v33, v59, %[kf4]\n.LS%=:\n"
        "v_cvt_pk_bf16_f32 v36, v26, v27\nv_cvt_pk_bf16_f32 v37, v28, v29\nv_cvt_pk_bf16_f32 v38, v30, v31\nv_cvt_pk_bf16_f32 v39, v32, v33\nv_min_u32 v36, -2, v36\nv_min_u32 v37, -2, v37\n"
        "v_min_u32 v38, -2, v38\nv_min_u32 v39, -2, v39\nbuffer_store_dwordx4 v[36:39], %[od], %[ors], 0 offen sc0 sc1\ns_nop 1\n.LZ%=:\n"
        : "+{v[100:103]}"(kt[0]), "+{v[104:107]}"(kt[1]), "+{v[108:111]}"(kt[2]), "+{v[112:115]}"(kt[3]), "+{v[116:119]}"(kt[4]), "+{v[120:123]}"(kt[5]), "+{v[124:127]}"(kt[6]), "+{v[128:131]}"(kt[7]), "+{v[132:135]}"(kt[8]), [pv] "=&{v16}"(pv), [s0] "=&s"(s0_), [s1] "=&s"(s1_), [cap] "=&s"(cap)
        : [ra] "{v1}"(ra), [rb] "{v2}"(rb), [wa] "{v3}"(wa), [cna] "{v4}"(cna), [one] "{v5}"(one), [exr] "{v6}"(exr), [frr] "{v7}"(frr), [mr] "{v8}"(mr_), [od] "{v9}"(od), [kf] "{v10}"(kd[0]), [kd1] "{v11}"(kd[1]), [c16] "{v12}"(c16), [uw] "{v17}"(uw_), [sy] "{v23}"(sya), [ofs] "i"(SY_FS), [ue] "{v20}"(ue), [kf4] "{v21}"(kf4), [xo] "{v22}"(xo), [fsa] "i"((layer ? FSH : FSH0) - FSX), [pbl] "i"(PBH), [v] "s"(v), [wq] "s"(wq), [ors] "s"(ors), [xrs] "s"(xrs), [frs] "s"(frs), [pbe] "i"(PBE), [fsx] "i"(FSX), [fp] "i"(FPOLL)
        : "memory", "scc", "v13", "v14", "v15", "v26", "v27", "v28", "v29", "v30", "v31", "v32", "v33", "v36", "v37", "v38", "v39", "v40", "v41", "v42", "v43", "v44", "v45", "v46", "v47", "v48", "v49", "v50", "v51", "v52", "v53", "v54", "v55", "v56", "v57", "v58", "v59", "v60", "v61", "v62", "v63", "v64", "v65", "v66", "v67", "v68", "v69", "v70", "v71", "v72", "v73", "v74", "v75", "v76", "v77", "v78", "v79", "v80", "v81", "v82", "v83", "v84", "v85", "v86", "v87", "v88", "v89", "v90", "v91", "v92", "v93", "v94", "v95", "v96", "v97", "v98", "v99", "v136", "v137", "v138", "v139", "v140", "v141", "v142", "v143", "v144", "v145", "v146", "v147", "v148", "v149", "v150", "v151", "v152", "v153", "v154", "v155", "v156", "v157", "v158", "v159", "v160", "v161", "v162", "v163", "v164", "v165", "v166", "v167", "vcc");
    } else {
      asm volatile(
        "s_mov_b32 %[cap], 0x100000\ns_movk_i32 %[s1], 4096\nv_mov_b32 v56, -1\nv_mov_b32 v57, -1\nv_mov_b32 v58, -1\nv_mov_b32 v59, -1\ns_sleep %[fsx]\n"
        "buffer_load_dwordx4 v[36:39], %[c16], %[xrs], 0 offen sc1\ns_cmp_eq_u32 %[wq], 3\ns_cbranch_scc0 .LA%=\ns_sleep %[fsa]\ns_mov_b64 exec, 1\nds_add_u32 %[sy], %[one] offset:%[ofs]\n"
        "s_mov_b64 exec, -1\ns_sleep 1\ns_branch .LX%=\n.LA%=:\nds_read_b32 %[pv], %[sy] offset:%[ofs]\ns_waitcnt lgkmcnt(0)\nv_readfirstlane_b32 %[s0], %[pv]\ns_nop 0\ns_cmp_ge_i32 %[s0], 2\n"
        "s_cbranch_scc1 .LX%=\ns_sub_u32 %[cap], %[cap], 1\ns_cbranch_scc0 .LA%=\n.LX%=:\ns_mov_b32 %[cap], 0x10000\nbuffer_load_dwordx4 v[40:43], %[c16], %[frs], 0 offen sc1\n"
        "buffer_load_dwordx4 v[44:47], %[c16], %[frs], %[s1] offen sc1\ns_waitcnt vmcnt(2)\n.LB%=:\nv_max3_u32 %[pv], v36, v37, v38\nv_max_u32 %[pv], %[pv], v39\nv_cmp_eq_u32 vcc, -1, %[pv]\n"
        "s_cbranch_vccnz .LC%=\n.LD%=:\nv_lshlrev_b32 v26, 16, v36\nv_and_b32 v27, 0xffff0000, v36\nv_lshlrev_b32 v28, 16, v37\nv_and_b32 v29, 0xffff0000, v37\nv_lshlrev_b32 v30, 16, v38\n"
        "v_and_b32 v31, 0xffff0000, v38\nv_lshlrev_b32 v32, 16, v39\nv_and_b32 v33, 0xffff0000, v39\n.LF%=:\ns_waitcnt vmcnt(1)\nv_max3_u32 %[pv], v40, v41, v42\nv_lshlrev_b32 v48, 16, v40\n"
        "v_lshlrev_b32 v50, 16, v41\nv_lshlrev_b32 v52, 16, v42\nv_lshlrev_b32 v54, 16, v43\nv_fmac_f32 v48, v26, v40\nv_fmac_f32 v50, v28, v41\nv_fmac_f32 v52, v30, v42\nv_fmac_f32 v54, v32, v43\n"
        "s_waitcnt vmcnt(0)\nv_max3_u32 %[pv], %[pv], v43, v44\nv_max3_u32 %[pv], %[pv], v45, v46\nv_max_u32 %[pv], %[pv], v47\nv_cmp_eq_u32 vcc, -1, %[pv]\nv_lshlrev_b32 v49, 16, v44\n"
        "v_lshlrev_b32 v51, 16, v45\nv_lshlrev_b32 v53, 16, v46\nv_lshlrev_b32 v55, 16, v47\ns_cbranch_vccnz .LI%=\n.LM%=:\nv_fmac_f32 v49, v27, v44\nv_fmac_f32 v51, v29, v45\n"
        "v_fmac_f32 v53, v31, v46\nv_fmac_f32 v55, v33, v47\nv_cvt_pk_f16_f32 v36, v48, v49\nv_cvt_pk_f16_f32 v37, v50, v51\nv_cvt_pk_f16_f32 v38, v52, v53\nv_cvt_pk_f16_f32 v39, v54, v55\n"
        "ds_write_b128 %[uw], v[36:39]\ns_mov_b64 exec, 1\nds_add_u32 %[cna], %[one] offset:0\ns_mov_b64 exec, -1\ns_movk_i32 %[s0], 0\n"
        "buffer_store_dwordx4 v[56:59], %[mo], %[mrs], %[s0] offen sc0 sc1\ns_movk_i32 %[s0], 12288\nbuffer_store_dwordx4 v[56:59], %[mo], %[mrs], %[s0] offen sc0 sc1\ns_movk_i32 %[s0], 24576\n"
        "buffer_store_dwordx4 v[56:59], %[mo], %[mrs], %[s0] offen sc0 sc1\ns_setprio %[pbl]\ns_mov_b32 %[s1], 0x100000\n.LN%=:\nds_read_b32 %[pv], %[cna]\ns_waitcnt lgkmcnt(0)\n"
        "v_readfirstlane_b32 %[s0], %[pv]\ns_nop 0\ns_cmp_ge_i32 %[s0], 4\ns_cbranch_scc1 .LO%=\ns_sub_u32 %[s1], %[s1], 1\ns_cbranch_scc0 .LN%=\n.LO%=:\n"
        "buffer_load_dwordx4 v[152:155], %[xo], %[xrs], 0 offen sc1\nds_read_b128 v[156:159], %[ue]\ns_cmp_lt_u32 %[v], 2\ns_cbranch_scc0 .LT%=\nds_read_b128 v[52:55], %[ra] offset:800\n"
        "ds_read_b128 v[56:59], %[ra] offset:736\nds_read_b128 v[60:63], %[ra] offset:672\nds_read_b128 v[64:67], %[rb] offset:576\nds_read_b128 v[68:71], %[ra] offset:512\n"
        "ds_read_b128 v[72:75], %[ra] offset:448\nds_read_b128 v[76:79], %[ra] offset:384\nds_read_b128 v[80:83], %[rb] offset:288\nv_xor_b32 v14, 32, %[wa]\nv_xor_b32 v15, 64, %[wa]\n"
        "v_xor_b32 v13, 96, %[wa]\ns_waitcnt lgkmcnt(7)\nv_mfma_f32_16x16x32_f16 v[48:51], v[100:103], v[52:55], 0\nds_read_b128 v[52:55], %[ra] offset:224\ns_waitcnt lgkmcnt(7)\n"
        "v_mfma_f32_16x16x32_f16 v[44:47], v[100:103], v[56:59], 0\ns_waitcnt lgkmcnt(6)\nv_mfma_f32_16x16x32_f16 v[40:43], v[100:103], v[60:63], 0\ns_waitcnt lgkmcnt(5)\n"
        "v_mfma_f32_16x16x32_f16 v[36:39], v[100:103], v[64:67], 0\nv_mfma_f32_16x16x32_f16 v[48:51], v[104:107], v[56:59], v[48:51]\nds_read_b128 v[56:59], %[ra] offset:160\n"
        "v_mfma_f32_16x16x32_f16 v[44:47], v[104:107], v[60:63], v[44:47]\nv_mfma_f32_16x16x32_f16 v[40:43], v[104:107], v[64:67], v[40:43]\ns_waitcnt lgkmcnt(5)\n"
        "v_mfma_f32_16x16x32_f16 v[36:39], v[104:107], v[68:71], v[36:39]\nv_mfma_f32_16x16x32_f16 v[48:51], v[108:111], v[60:63], v[48:51]\nds_read_b128 v[60:63], %[ra] offset:96\n"
        "v_mfma_f32_16x16x32_f16 v[44:47], v[108:111], v[64:67], v[44:47]\nv_mfma_f32_16x16x32_f16 v[40:43], v[108:111], v[68:71], v[40:43]\ns_waitcnt lgkmcnt(5)\n"
        "v_mfma_f32_16x16x32_f16 v[36:39], v[108:111], v[72:75], v[36:39]\nv_mfma_f32_16x16x32_f16 v[48:51], v[112:115], v[64:67], v[48:51]\nds_read_b128 v[64:67], %[rb] offset:0\n"
        "v_mfma_f32_16x16x32_f16 v[44:47], v[112:115], v[68:71], v[44:47]\nv_mfma_f32_16x16x32_f16 v[40:43], v[112:115], v[72:75], v[40:43]\ns_waitcnt lgkmcnt(5)\n"
        "v_mfma_f32_16x16x32_f16 v[36:39], v[112:115], v[76:79], v[36:39]\nv_mfma_f32_16x16x32_f16 v[48:51], v[116:119], v[68:71], v[48:51]\n"
        "v_mfma_f32_16x16x32_f16 v[44:47], v[116:119], v[72:75], v[44:47]\nv_mfma_f32_16x16x32_f16 v[40:43], v[116:119], v[76:79], v[40:43]\ns_waitcnt lgkmcnt(4)\n"
        "v_mfma_f32_16x16x32_f16 v[36:39], v[116:119], v[80:83], v[36:39]\nv_mfma_f32_16x16x32_f16 v[48:51], v[120:123], v[72:75], v[48:51]\n"
        "v_mfma_f32_16x16x32_f16 v[44:47], v[120:123], v[76:79], v[44:47]\nv_mfma_f32_16x16x32_f16 v[40:43], v[120:123], v[80:83], v[40:43]\ns_waitcnt lgkmcnt(3)\n"
        "v_mfma_f32_16x16x32_f16 v[36:39], v[120:123], v[52:55], v[36:39]\nv_mfma_f32_16x16x32_f16 v[48:51], v[124:127], v[76:79], v[48:51]\n"
        "v_mfma_f32_16x16x32_f16 v[44:47], v[124:127], v[80:83], v[44:47]\nv_mfma_f32_16x16x32_f16 v[40:43], v[124:127], v[52:55], v[40:43]\ns_waitcnt lgkmcnt(2)\n"
        "v_mfma_f32_16x16x32_f16 v[36:39], v[124:127], v[56:59], v[36:39]\nv_mfma_f32_16x16x32_f16 v[48:51], v[128:131], v[80:83], v[48:51]\n"
        "v_mfma_f32_16x16x32_f16 v[44:47], v[128:131], v[52:55], v[44:47]\nv_mfma_f32_16x16x32_f16 v[40:43], v[128:131], v[56:59], v[40:43]\ns_waitcnt lgkmcnt(1)\n"
        "v_mfma_f32_16x16x32_f16 v[36:39], v[128:131], v[60:63], v[36:39]\ns_cmp_eq_u32 %[v], 0\ns_cbranch_scc1 .LU%=\nv_mfma_f32_16x16x32_f16 v[48:51], v[132:135], v[52:55], v[48:51]\n"
        "v_mfma_f32_16x16x32_f16 v[44:47], v[132:135], v[56:59], v[44:47]\nv_mfma_f32_16x16x32_f16 v[40:43], v[132:135], v[60:63], v[40:43]\ns_waitcnt lgkmcnt(0)\n"
        "v_mfma_f32_16x16x32_f16 v[36:39], v[132:135], v[64:67], v[36:39]\n.LU%=:\ns_nop 4\nds_write_b128 v13, v[48:51]\nds_write_b128 v15, v[44:47]\nds_write_b128 v14, v[40:43]\n"
        "ds_write_b128 %[wa], v[36:39]\ns_branch .LE%=\n.LT%=:\ns_cmp_eq_u32 %[v], 2\ns_cbranch_scc0 .LV%=\nds_read_b64 v[60:61], %[rb] offset:0\nds_read_b128 v[62:65], %[ra] offset:0\n"
        "ds_read_b64 v[66:67], %[rb] offset:288\nds_read_b128 v[68:71], %[ra] offset:288\nds_read_b64 v[72:73], %[rb] offset:576\nds_read_b128 v[74:77], %[ra] offset:576\n"
        "ds_read_b64 v[78:79], %[rb] offset:864\nds_read_b128 v[80:83], %[ra] offset:864\ns_waitcnt lgkmcnt(6)\nv_mfma_f32_16x16x32_f16 v[84:87], v[60:63], v[100:103], 0\n"
        "v_mfma_f32_16x16x32_f16 v[88:91], v[62:65], v[104:107], 0\nds_read_b64 v[60:61], %[rb] offset:1152\nds_read_b128 v[62:65], %[ra] offset:1152\ns_waitcnt lgkmcnt(6)\n"
        "v_mfma_f32_16x16x32_f16 v[92:95], v[66:69], v[100:103], 0\nv_mfma_f32_16x16x32_f16 v[96:99], v[68:71], v[104:107], 0\nds_read_b64 v[66:67], %[rb] offset:1440\n"
        "ds_read_b128 v[68:71], %[ra] offset:1440\ns_nop 7\nv_cvt_pk_f16_f32 v136, v84, v85\nv_cvt_pk_f16_f32 v137, v86, v87\nv_cvt_pk_f16_f32 v138, v88, v89\nv_cvt_pk_f16_f32 v139, v90, v91\ns_nop 1\n"
        "v_mfma_f32_16x16x32_f16 v[36:39], v[136:139], v[108:111], 0\ns_waitcnt lgkmcnt(6)\nv_mfma_f32_16x16x32_f16 v[84:87], v[72:75], v[100:103], 0\n"
        "v_mfma_f32_16x16x32_f16 v[88:91], v[74:77], v[104:107], 0\nds_read_b64 v[72:73], %[rb] offset:1728\nds_read_b128 v[74:77], %[ra] offset:1728\nv_cvt_pk_f16_f32 v140, v92, v93\n"
        "v_cvt_pk_f16_f32 v141, v94, v95\nv_cvt_pk_f16_f32 v142, v96, v97\nv_cvt_pk_f16_f32 v143, v98, v99\ns_nop 1\nv_mfma_f32_16x16x32_f16 v[36:39], v[140:143], v[112:115], v[36:39]\n"
        "s_waitcnt lgkmcnt(6)\nv_mfma_f32_16x16x32_f16 v[92:95], v[78:81], v[100:103], 0\nv_mfma_f32_16x16x32_f16 v[96:99], v[80:83], v[104:107], 0\nds_read_b64 v[78:79], %[rb] offset:2016\n"
        "ds_read_b128 v[80:83], %[ra] offset:2016\nv_cvt_pk_f16_f32 v136, v84, v85\nv_cvt_pk_f16_f32 v137, v86, v87\nv_cvt_pk_f16_f32 v138, v88, v89\nv_cvt_pk_f16_f32 v139, v90, v91\ns_nop 1\n"
        "v_mfma_f32_16x16x32_f16 v[36:39], v[136:139], v[116:119], v[36:39]\nv_mfma_f32_16x16x32_f16 v[40:43], v[136:139], v[108:111], 0\ns_waitcnt lgkmcnt(6)\n"
        "v_mfma_f32_16x16x32_f16 v[84:87], v[60:63], v[100:103], 0\nv_cvt_pk_f16_f32 v140, v92, v93\nv_cvt_pk_f16_f32 v141, v94, v95\nv_mfma_f32_16x16x32_f16 v[88:91], v[62:65], v[104:107], 0\n"
        "v_cvt_pk_f16_f32 v142, v96, v97\nv_cvt_pk_f16_f32 v143, v98, v99\ns_nop 1\nv_mfma_f32_16x16x32_f16 v[36:39], v[140:143], v[120:123], v[36:39]\nds_read_b64 v[60:61], %[rb] offset:2304\n"
        "ds_read_b128 v[62:65], %[ra] offset:2304\nv_mfma_f32_16x16x32_f16 v[40:43], v[140:143], v[112:115], v[40:43]\ns_waitcnt lgkmcnt(6)\nv_mfma_f32_16x16x32_f16 v[92:95], v[66:69], v[100:103], 0\n"
        "v_cvt_pk_f16_f32 v136, v84, v85\nv_cvt_pk_f16_f32 v137, v86, v87\nv_mfma_f32_16x16x32_f16 v[96:99], v[68:71], v[104:107], 0\nv_cvt_pk_f16_f32 v138, v88, v89\nv_cvt_pk_f16_f32 v139, v90, v91\n"
        "s_nop 1\nv_mfma_f32_16x16x32_f16 v[36:39], v[136:139], v[124:127], v[36:39]\nds_read_b64 v[66:67], %[rb] offset:2592\nds_read_b128 v[68:71], %[ra] offset:2592\n"
        "v_mfma_f32_16x16x32_f16 v[40:43], v[136:139], v[116:119], v[40:43]\nv_mfma_f32_16x16x32_f16 v[44:47], v[136:139], v[108:111], 0\nv_cvt_pk_f16_f32 v140, v92, v93\n"
        "v_cvt_pk_f16_f32 v141, v94, v95\ns_waitcnt lgkmcnt(6)\nv_mfma_f32_16x16x32_f16 v[84:87], v[72:75], v[100:103], 0\nv_cvt_pk_f16_f32 v142, v96, v97\nv_cvt_pk_f16_f32 v143, v98, v99\n"
        "v_mfma_f32_16x16x32_f16 v[88:91], v[74:77], v[104:107], 0\ns_nop 0\nv_mfma_f32_16x16x32_f16 v[36:39], v[140:143], v[128:131], v[36:39]\n"
        "v_mfma_f32_16x16x32_f16 v[40:43], v[140:143], v[120:123], v[40:43]\nv_mfma_f32_16x16x32_f16 v[44:47], v[140:143], v[112:115], v[44:47]\nv_cvt_pk_f16_f32 v136, v84, v85\n"
        "v_cvt_pk_f16_f32 v137, v86, v87\ns_waitcnt lgkmcnt(4)\nv_mfma_f32_16x16x32_f16 v[92:95], v[78:81], v[100:103], 0\nv_cvt_pk_f16_f32 v138, v88, v89\nv_cvt_pk_f16_f32 v139, v90, v91\n"
        "v_mfma_f32_16x16x32_f16 v[96:99], v[80:83], v[104:107], 0\nds_write_b128 %[wa], v[36:39] offset:0\nv_mfma_f32_16x16x32_f16 v[40:43], v[136:139], v[124:127], v[40:43]\n"
        "v_mfma_f32_16x16x32_f16 v[44:47], v[136:139], v[116:119], v[44:47]\ns_waitcnt lgkmcnt(3)\nv_mfma_f32_16x16x32_f16 v[84:87], v[60:63], v[100:103], 0\nv_cvt_pk_f16_f32 v140, v92, v93\n"
        "v_cvt_pk_f16_f32 v141, v94, v95\nv_mfma_f32_16x16x32_f16 v[88:91], v[62:65], v[104:107], 0\nv_cvt_pk_f16_f32 v142, v96, v97\nv_cvt_pk_f16_f32 v143, v98, v99\ns_nop 1\n"
        "v_mfma_f32_16x16x32_f16 v[40:43], v[140:143], v[128:131], v[40:43]\nv_mfma_f32_16x16x32_f16 v[44:47], v[140:143], v[120:123], v[44:47]\ns_waitcnt lgkmcnt(1)\n"
        "v_mfma_f32_16x16x32_f16 v[92:95], v[66:69], v[100:103], 0\nv_cvt_pk_f16_f32 v136, v84, v85\nv_cvt_pk_f16_f32 v137, v86, v87\nv_mfma_f32_16x16x32_f16 v[96:99], v[68:71], v[104:107], 0\n"
        "v_cvt_pk_f16_f32 v138, v88, v89\nv_cvt_pk_f16_f32 v139, v90, v91\ns_nop 1\nv_mfma_f32_16x16x32_f16 v[44:47], v[136:139], v[124:127], v[44:47]\nds_write_b128 %[wa], v[40:43] offset:256\n"
        "s_nop 7\nv_cvt_pk_f16_f32 v140, v92, v93\nv_cvt_pk_f16_f32 v141, v94, v95\nv_cvt_pk_f16_f32 v142, v96, v97\nv_cvt_pk_f16_f32 v143, v98, v99\ns_nop 1\n"
        "v_mfma_f32_16x16x32_f16 v[44:47], v[140:143], v[128:131], v[44:47]\ns_nop 7\nds_write_b128 %[wa], v[44:47] offset:512\ns_branch .LE%=\n.LV%=:\nds_read_b128 v[52:55], %[ra] offset:0\n"
        "ds_read_b128 v[56:59], %[ra] offset:288\nds_read_b128 v[60:63], %[ra] offset:576\nds_read_b128 v[64:67], %[ra] offset:864\nds_read_b128 v[84:87], %[rb] offset:0\n"
        "ds_read_b128 v[88:91], %[rb] offset:1152\ns_waitcnt lgkmcnt(5)\nv_mfma_f32_16x16x32_f16 v[68:71], v[52:55], v[100:103], 0\nv_mfma_f32_16x16x32_f16 v[72:75], v[52:55], v[104:107], 0\n"
        "ds_read_b128 v[52:55], %[ra] offset:1152\ns_waitcnt lgkmcnt(5)\nv_mfma_f32_16x16x32_f16 v[76:79], v[56:59], v[100:103], 0\nv_mfma_f32_16x16x32_f16 v[80:83], v[56:59], v[104:107], 0\n"
        "ds_read_b128 v[56:59], %[ra] offset:1440\ns_waitcnt lgkmcnt(3)\nv_mfma_f32_16x16x32_f16 v[36:39], v[124:127], v[84:87], 0\nv_cvt_pk_f16_f32 v136, v68, v73\nv_cvt_pk_f16_f32 v137, v70, v75\n"
        "s_waitcnt lgkmcnt(2)\nv_mfma_f32_16x16x32_f16 v[40:43], v[124:127], v[88:91], 0\nv_mfma_f32_16x16x32_f16 v[68:71], v[60:63], v[100:103], 0\nv_cvt_pk_f16_f32 v138, v76, v81\n"
        "v_cvt_pk_f16_f32 v139, v78, v83\nv_mfma_f32_16x16x32_f16 v[72:75], v[60:63], v[104:107], 0\nds_read_b128 v[60:63], %[ra] offset:1728\n"
        "v_mfma_f32_16x16x32_f16 v[36:39], v[136:139], v[112:115], v[36:39]\nv_mfma_f32_16x16x32_f16 v[40:43], v[136:139], v[120:123], v[40:43]\n"
        "v_mfma_f32_16x16x32_f16 v[76:79], v[64:67], v[100:103], 0\nv_cvt_pk_f16_f32 v140, v68, v73\nv_cvt_pk_f16_f32 v141, v70, v75\nv_mfma_f32_16x16x32_f16 v[80:83], v[64:67], v[104:107], 0\n"
        "ds_read_b128 v[64:67], %[ra] offset:2016\ns_waitcnt lgkmcnt(3)\nv_mfma_f32_16x16x32_f16 v[68:71], v[52:55], v[100:103], 0\nv_mfma_f32_16x16x32_f16 v[72:75], v[52:55], v[104:107], 0\ns_nop 7\n"
        "v_cvt_pk_f16_f32 v142, v76, v81\nv_cvt_pk_f16_f32 v143, v78, v83\ns_nop 1\nv_mfma_f32_16x16x32_f16 v[36:39], v[140:143], v[108:111], v[36:39]\n"
        "v_mfma_f32_16x16x32_f16 v[40:43], v[140:143], v[116:119], v[40:43]\ns_waitcnt lgkmcnt(2)\nv_mfma_f32_16x16x32_f16 v[76:79], v[56:59], v[100:103], 0\nv_cvt_pk_f16_f32 v136, v68, v73\n"
        "v_cvt_pk_f16_f32 v137, v70, v75\nv_mfma_f32_16x16x32_f16 v[80:83], v[56:59], v[104:107], 0\nds_write_b128 %[wa], v[36:39] offset:0\ns_waitcnt lgkmcnt(2)\n"
        "v_mfma_f32_16x16x32_f16 v[68:71], v[60:63], v[100:103], 0\nv_mfma_f32_16x16x32_f16 v[72:75], v[60:63], v[104:107], 0\ns_nop 7\nv_cvt_pk_f16_f32 v138, v76, v81\n"
        "v_cvt_pk_f16_f32 v139, v78, v83\ns_nop 1\nv_mfma_f32_16x16x32_f16 v[40:43], v[136:139], v[112:115], v[40:43]\ns_waitcnt lgkmcnt(1)\nv_mfma_f32_16x16x32_f16 v[76:79], v[64:67], v[100:103], 0\n"
        "v_mfma_f32_16x16x32_f16 v[80:83], v[64:67], v[104:107], 0\nv_cvt_pk_f16_f32 v140, v68, v73\nv_cvt_pk_f16_f32 v141, v70, v75\ns_nop 7\nv_cvt_pk_f16_f32 v142, v76, v81\n"
        "v_cvt_pk_f16_f32 v143, v78, v83\ns_nop 1\nv_mfma_f32_16x16x32_f16 v[40:43], v[140:143], v[108:111], v[40:43]\ns_nop 7\nds_write_b128 %[wa], v[40:43] offset:256\ns_branch .LE%=\n.LC%=:\n"
        "s_sub_u32 %[cap], %[cap], 1\ns_cbranch_scc0 .LCy%=\ns_mov_b32 %[cap], 0\ns_branch .LD%=\n.LCy%=:\nbuffer_load_dwordx4 v[36:39], %[c16], %[xrs], 0 offen sc1\ns_waitcnt vmcnt(0)\n"
        "s_branch .LB%=\n.LI%=:\ns_sub_u32 %[cap], %[cap], 1\ns_cbranch_scc0 .LIy%=\ns_mov_b32 %[cap], 0\ns_branch .LM%=\n.LIy%=:\ns_sleep %[fp]\n"
        "buffer_load_dwordx4 v[40:43], %[c16], %[frs], 0 offen sc1\nbuffer_load_dwordx4 v[44:47], %[c16], %[frs], %[s1] offen sc1\ns_branch .LF%=\n.LE%=:\ns_waitcnt vmcnt(0)\ns_mov_b64 exec, 1\n"
        "ds_add_u32 %[cna], %[one] offset:8\ns_mov_b64 exec, -1\ns_setprio %[pbe]\ns_cmp_lt_u32 %[wq], 2\ns_cbranch_scc0 .LZ%=\nv_lshlrev_b32 v26, 16, v152\nv_and_b32 v27, 0xffff0000, v152\n"
        "v_cvt_f32_f16 v152, v156\nv_lshrrev_b32 v156, 16, v156\nv_lshlrev_b32 v28, 16, v153\nv_and_b32 v29, 0xffff0000, v153\nv_cvt_f32_f16 v153, v157\nv_lshrrev_b32 v157, 16, v157\n"
        "v_lshlrev_b32 v30, 16, v154\nv_and_b32 v31, 0xffff0000, v154\nv_cvt_f32_f16 v154, v158\nv_lshrrev_b32 v158, 16, v158\nv_lshlrev_b32 v32, 16, v155\nv_and_b32 v33, 0xffff0000, v155\n"
        "v_cvt_f32_f16 v155, v159\nv_lshrrev_b32 v159, 16, v159\nv_cvt_f32_f16 v156, v156\nv_fmac_f32 v26, v152, %[kd1]\nv_cvt_f32_f16 v157, v157\nv_fmac_f32 v28, v153, %[kd1]\n"
        "v_cvt_f32_f16 v158, v158\nv_fmac_f32 v30, v154, %[kd1]\nv_cvt_f32_f16 v159, v159\nv_fmac_f32 v32, v155, %[kd1]\nv_fmac_f32 v27, v156, %[kd1]\nv_fmac_f32 v29, v157, %[kd1]\n"
        "v_fmac_f32 v31, v158, %[kd1]\nv_fmac_f32 v33, v159, %[kd1]\ns_mov_b32 %[s1], 0x100000\n.LP%=:\nds_read_b32 %[pv], %[cna] offset:8\ns_waitcnt lgkmcnt(0)\nv_readfirstlane_b32 %[s0], %[pv]\n"
        "s_nop 0\ns_cmp_ge_i32 %[s0], 4\ns_cbranch_scc1 .LR%=\ns_sub_u32 %[s1], %[s1], 1\ns_cbranch_scc0 .LP%=\n.LR%=:\ns_cmp_lt_u32 %[wq], 1\ns_cbranch_scc1 .LS4_%=\n"
        "ds_read_b128 v[36:39], %[exr] offset:0\nds_read_b128 v[40:43], %[exr] offset:2048\nds_read_b128 v[44:47], %[exr] offset:4096\nds_read_b128 v[48:51], %[exr] offset:6144\n"
        "ds_read_b128 v[52:55], %[mr] offset:0\nds_read_b128 v[56:59], %[mr] offset:3072\nds_read_b128 v[60:63], %[frr] offset:0\nds_read_b128 v[64:67], %[frr] offset:1152\ns_waitcnt lgkmcnt(6)\n"
        "v_fmac_f32 v26, v36, %[kf]\nv_fmac_f32 v27, v37, %[kf]\nv_fmac_f32 v28, v38, %[kf]\nv_fmac_f32 v29, v39, %[kf]\nv_fmac_f32 v30, v40, %[kf]\nv_fmac_f32 v31, v41, %[kf]\n"
        "v_fmac_f32 v32, v42, %[kf]\nv_fmac_f32 v33, v43, %[kf]\ns_waitcnt lgkmcnt(4)\nv_fmac_f32 v26, v44, %[kf]\nv_fmac_f32 v27, v45, %[kf]\nv_fmac_f32 v28, v46, %[kf]\nv_fmac_f32 v29, v47, %[kf]\n"
        "v_fmac_f32 v30, v48, %[kf]\nv_fmac_f32 v31, v49, %[kf]\nv_fmac_f32 v32, v50, %[kf]\nv_fmac_f32 v33, v51, %[kf]\ns_waitcnt lgkmcnt(2)\nv_fmac_f32 v26, v52, %[kf4]\nv_fmac_f32 v27, v53, %[kf4]\n"
        "v_fmac_f32 v28, v54, %[kf4]\nv_fmac_f32 v29, v55, %[kf4]\nv_fmac_f32 v30, v56, %[kf4]\nv_fmac_f32 v31, v57, %[kf4]\nv_fmac_f32 v32, v58, %[kf4]\nv_fmac_f32 v33, v59, %[kf4]\n"
        "s_waitcnt lgkmcnt(0)\nv_fmac_f32 v26, v60, %[kf]\nv_fmac_f32 v27, v61, %[kf]\nv_fmac_f32 v28, v62, %[kf]\nv_fmac_f32 v29, v63, %[kf]\nv_fmac_f32 v30, v64, %[kf]\nv_fmac_f32 v31, v65, %[kf]\n"
        "v_fmac_f32 v32, v66, %[kf]\nv_fmac_f32 v33, v67, %[kf]\ns_branch .LS%=\n.LS4_%=:\nds_read_b128 v[36:39], %[exr] offset:0\nds_read_b128 v[40:43], %[exr] offset:2048\n"
        "ds_read_b128 v[44:47], %[exr] offset:4096\nds_read_b128 v[48:51], %[exr] offset:6144\nds_read_b128 v[52:55], %[mr] offset:0\nds_read_b128 v[56:59], %[mr] offset:3072\ns_waitcnt lgkmcnt(4)\n"
        "v_fmac_f32 v26, v36, %[kf]\nv_fmac_f32 v27, v37, %[kf]\nv_fmac_f32 v28, v38, %[kf]\nv_fmac_f32 v29, v39, %[kf]\nv_fmac_f32 v30, v40, %[kf]\nv_fmac_f32 v31, v41, %[kf]\n"
        "v_fmac_f32 v32, v42, %[kf]\nv_fmac_f32 v33, v43, %[kf]\ns_waitcnt lgkmcnt(2)\nv_fmac_f32 v26, v44, %[kf]\nv_fmac_f32 v27, v45, %[kf]\nv_fmac_f32 v28, v46, %[kf]\nv_fmac_f32 v29, v47, %[kf]\n"
        "v_fmac_f32 v30, v48, %[kf]\nv_fmac_f32 v31, v49, %[kf]\nv_fmac_f32 v32, v50, %[kf]\nv_fmac_f32 v33, v51, %[kf]\ns_waitcnt lgkmcnt(0)\nv_fmac_f32 v26, v52, %[kf4]\nv_fmac_f32 v27, v53, %[kf4]\n"
        "v_fmac_f32 v28, v54, %[kf4]\nv_fmac_f32 v29, v55, %[kf4]\nv_fmac_f32 v30, v56, %[kf4]\nv_fmac_f32 v31, v57, %[kf4]\nv_fmac_f32 v32, v58, %[kf4]\nv_fmac_f32 v33, v59, %[kf4]\n.LS%=:\n"
        "v_cvt_pk_bf16_f32 v36, v26, v27\nv_cvt_pk_bf16_f32 v37, v28, v29\nv_cvt_pk_bf16_f32 v38, v30, v31\nv_cvt_pk_bf16_f32 v39, v32, v33\nv_min_u32 v36, -2, v36\nv_min_u32 v37, -2, v37\n"
        "v_min_u32 v38, -2, v38\nv_min_u32 v39, -2, v39\nbuffer_store_dwordx4 v[36:39], %[od], %[ors], 0 offen sc0 sc1\nbuffer_store_dwordx4 v[36:39], %[oh], %[ors], 0 offen sc0 sc1\ns_nop 1\n.LZ%=:\n"
        : "+{v[100:103]}"(kt[0]), "+{v[104:107]}"(kt[1]), "+{v[108:111]}"(kt[2]), "+{v[112:115]}"(kt[3]), "+{v[116:119]}"(kt[4]), "+{v[120:123]}"(kt[5]), "+{v[124:127]}"(kt[6]), "+{v[128:131]}"(kt[7]), "+{v[132:135]}"(kt[8]), [pv] "=&{v16}"(pv), [s0] "=&s"(s0_), [s1] "=&s"(s1_), [cap] "=&s"(cap)
        : [ra] "{v1}"(ra), [rb] "{v2}"(rb), [wa] "{v3}"(wa), [cna] "{v4}"(cna), [one] "{v5}"(one), [exr] "{v6}"(exr), [frr] "{v7}"(frr), [mr] "{v8}"(mr_), [od] "{v9}"(od), [kf] "{v10}"(kd[0]), [kd1] "{v11}"(kd[1]), [c16] "{v12}"(c16), [uw] "{v17}"(uw_), [sy] "{v23}"(sya), [ofs] "i"(SY_FS), [oh] "{v18}"(oh), [mo] "{v19}"(mo), [mrs] "s"(mrs), [ue] "{v20}"(ue), [kf4] "{v21}"(kf4), [xo] "{v22}"(xo), [fsa] "i"((layer ? FSH : FSH0) - FSX), [pbl] "i"(PBH), [v] "s"(v), [wq] "s"(wq), [ors] "s"(ors), [xrs] "s"(xrs), [frs] "s"(frs), [pbe] "i"(PBE), [fsx] "i"(FSX), [fp] "i"(FPOLL)
        : "memory", "scc", "v13", "v14", "v15", "v26", "v27", "v28", "v29", "v30", "v31", "v32", "v33", "v36", "v37", "v38", "v39", "v40", "v41", "v42", "v43", "v44", "v45", "v46", "v47", "v48", "v49", "v50", "v51", "v52", "v53", "v54", "v55", "v56", "v57", "v58", "v59", "v60", "v61", "v62", "v63", "v64", "v65", "v66", "v67", "v68", "v69", "v70", "v71", "v72", "v73", "v74", "v75", "v76", "v77", "v78", "v79", "v80", "v81", "v82", "v83", "v84", "v85", "v86", "v87", "v88", "v89", "v90", "v91", "v92", "v93", "v94", "v95", "v96", "v97", "v98", "v99", "v136", "v137", "v138", "v139", "v140", "v141", "v142", "v143", "v144", "v145", "v146", "v147", "v148", "v149", "v150", "v151", "v152", "v153", "v154", "v155", "v156", "v157", "v158", "v159", "v160", "v161", "v162", "v163", "v164", "v165", "v166", "v167", "vcc");
    }
    RETRY(8 * layer + 6, 0x10000 - cap);
    if (FULL && !layer) { wst[0] = kt[7]; wst[1] = kt[8]; for (int i = 2; i < 8; ++i) wst[i] = kt[9 + i]; }
  }
}

template <int layer>
__device__ __forceinline__ void b_phase(unsigned char* smem, const int b, const int t, const u16* xtin, float2* stats, u32* fstat,
                                        u16* xtout, u32x4* mark, const u16* wot, u32x4 (&wst)[8]) {
  const int kid = layer ? 3 : 5;
  const int lane = t & 63, w = __builtin_amdgcn_readfirstlane(t >> 6);
  const int gq = w < 4 ? 2 : (w >> 2) - 1, wq = w & 3;
  const int cG = CHAN(b, gq);
  ST_DECL
  if (w != 3) __builtin_amdgcn_s_setprio(PBU); else __builtin_amdgcn_s_setprio(3);
#define TSARG
  if (gq < 2) b_conv<true, layer>(smem, b, w, gq, wq, cG, xtin, stats, fstat, xtout, mark, wot, wst TSARG);
  else b_conv<false, layer>(smem, b, w, gq, wq, cG, xtin, stats, fstat, xtout, mark, wot, wst TSARG);
  ST(5);
  {
    LANE(lane);
    const int t = 64 * w + lane;
    ST_FLUSH(kid);
  }
}

struct MainArgs { const u16 *x, *wqkv, *wo; unsigned char* ws; u16* out; };
__global__ __launch_bounds__(768, 3) void k_main(MainArgs ma, KgenArgs ka) {
  __shared__ __attribute__((aligned(16))) unsigned char smem[A_LDS + 64];
  const int b_ = BIDX;
  u32x4 sink;
  int sb = b_, t0 = TIDX;
  asm volatile(
      "s_cmp_lt_u32 %1, 16\n\t"
      "s_cbranch_scc0 1f\n\t"
      "v_cmp_gt_u32 vcc, 0x80, %2\n\t"
      "s_and_b64 exec, exec, vcc\n\t"
      "s_getpc_b64 s[40:41]\n\t"
      "s_add_u32 s40, s40, _Z6k_main8MainArgs8KgenArgs@rel32@lo+4\n\t"
      "s_addc_u32 s41, s41, _Z6k_main8MainArgs8KgenArgs@rel32@hi+12\n\t"
      "s_lshl_b32 s42, %1, 11\n\t"
      "s_and_b32 s42, s42, 0x4000\n\t"
      "v_lshl_add_u32 %3, %2, 7, s42\n\t"
      "s_mov_b32 s42, kmain_tail-_Z6k_main8MainArgs8KgenArgs\n\t"
      "global_load_dword %0, %3, s[40:41]\n\t"
      "v_add_u32 %3, 0x8000, %3\n\t"
      "v_min_u32 %4, s42, %3\n\t"
      "global_load_dword %4, %4, s[40:41]\n\t"
      "v_add_u32 %3, 0x8000, %3\n\t"
      "v_min_u32 %3, s42, %3\n\t"
      "global_load_dword %3, %3, s[40:41]\n\t"
      "s_mov_b64 exec, -1\n\t"
      "1:"
      : "=&v"(sink[0]), "+s"(sb), "+v"(t0), "=&v"(sink[1]), "=&v"(sink[2]) :: "scc", "vcc", "s40", "s41", "s42", "memory");
  unsigned char* const ws_ = ma.ws;
  const int w64 = __builtin_amdgcn_readfirstlane(t0) & ~63;
#define GETT(t) asm volatile("v_mbcnt_lo_u32_b32 %0, -1, %1\n\tv_mbcnt_hi_u32_b32 %0, -1, %0" : "=v"(t) : "s"(w64))
#define FRESH int t, b = b_; unsigned char* ws = ws_; GETT(t); asm volatile("" : "+s"(b), "+s"(ws));
#define LAYER(L) { \
    { FRESH unsigned char* ho = ws + WS_O + L * HO_BYTES; \
      a_phase<L>(smem, b, t, L ? (const u16*)(ws + WS_IM1) : ma.x, ma.wqkv, ma.wo, ws, (u16*)ho, (u16*)(ho + HO_X1), (float2*)(ho + HO_ST), ka, sink, tg0, tg1, wst); } \
    { FRESH unsigned char* ho = ws + WS_O + L * HO_BYTES; \
      b_phase<L>(smem, b, t, (u16*)(ho + HO_X1), (float2*)(ho + HO_ST), (u32*)(ho + HO_FS), (u16*)(ws + (L ? WS_FIN : WS_IM1)), (u32x4*)(ws + WS_O + HO_BYTES), (const u16*)(ws + WS_WOT), wst); } }
  u32x4 wst[8];
  u32 tg0, tg1;
  LAYER(0)
  LAYER(1)
  {
    FRESH
    const int lane = t & 63, w = __builtin_amdgcn_readfirstlane(t >> 6);
    ST_DECL
    const int i7 = (b >> 3) & 7;
    if (w == 2 || w == 3 || (w == 1 && i7 < 4)) {
      const int rb = 4 * (b & 7) + (b >> 6), cg = i7 + (w == 2 ? 12 : w == 3 ? 4 : 0);
      const __amdgpu_buffer_rsrc_t rs = __builtin_amdgcn_make_buffer_rsrc(ws + WS_FIN + (size_t)(rb * 640 + cg * 32) * 128, 0, 4096, 0x00020000);
      const __amdgpu_buffer_rsrc_t os = __builtin_amdgcn_make_buffer_rsrc(ma.out + (size_t)rb * 64 * D_ + cg * 32, 0, 63 * D_ * 2 + 64, 0x00020000);
      u32 sw = LDSA(smem) + A_R1 + (w - 1) * 4608, sr = sw + (8 * (lane >> 4) + ((lane >> 2) & 3)) * 144 + 8 * (lane & 3);
      sw += (lane >> 3) * 144 + (lane & 7) * 16;
      int l16 = lane * 16, oo = (lane & 15) * (D_ * 2) + (lane >> 4) * 16;
      PIN(sw); PIN(sr); PIN(l16); PIN(oo);
      __builtin_amdgcn_s_setprio(3);
      if (w != 1) lds_poll<88, 2>(LDSA(smem) + SY_OFF + 256 + (w == 2 ? 16 : 0), 4);
      if (w == 1) __builtin_amdgcn_s_sleep(TSLEEP - 8); else __builtin_amdgcn_s_sleep(TSLEEP);
      u32x4 v[4];
      int spins = 0;
      _Pragma("unroll 1")
      while (true) {
        asm volatile("" ::: "memory");
        u32 m = 0;
#pragma unroll
        for (int i = 0; i < 4; ++i) v[i] = __builtin_amdgcn_raw_buffer_load_b128(rs, l16, i * 1024, 16);
#pragma unroll
        for (int i = 0; i < 4; ++i) {
          asm volatile("ds_write_b128 %0, %1 offset:%2" :: "v"(sw), "v"(v[i]), "i"(i * 1152) : "memory");
          m = umax3(umax3(m, v[i][0], v[i][1]), v[i][2], v[i][3]);
        }
        if (__builtin_amdgcn_ballot_w64(m == 0xffffffffu) == 0 || ++spins > GCAP) break;
        __builtin_amdgcn_s_sleep(FPOLL);
      }
      RETRY(7, spins);
      ST(1);
      s16x4 f[8];
      asm volatile("ds_read_b64_tr_b16 %0, %8\n\tds_read_b64_tr_b16 %1, %8 offset:576\n\tds_read_b64_tr_b16 %2, %8 offset:32\n\tds_read_b64_tr_b16 %3, %8 offset:608\n\t"
                   "ds_read_b64_tr_b16 %4, %8 offset:64\n\tds_read_b64_tr_b16 %5, %8 offset:640\n\tds_read_b64_tr_b16 %6, %8 offset:96\n\tds_read_b64_tr_b16 %7, %8 offset:672\n\ts_waitcnt lgkmcnt(0)"
                   : "=&v"(f[0]), "=&v"(f[1]), "=&v"(f[2]), "=&v"(f[3]), "=&v"(f[4]), "=&v"(f[5]), "=&v"(f[6]), "=&v"(f[7]) : "v"(sr) : "memory");
#pragma unroll
      for (int i = 0; i < 4; ++i)
        __builtin_amdgcn_raw_buffer_store_b128(__builtin_bit_cast(u32x4, __builtin_shufflevector(f[2 * i], f[2 * i + 1], 0, 1, 2, 3, 4, 5, 6, 7)), os, oo, i * 16 * D_ * 2, OAUX);
    }
    ST(2);
    ST_FLUSH(4);
  }
}
extern "C" __device__ __attribute__((used, noinline)) void kmain_tail() {}
extern "C" void kernel_launch(void* const* d_in, const int* in_sizes, int n_in, void* d_out, int out_size,
                              void* d_ws, size_t ws_size, hipStream_t stream) {
  unsigned char* ws = (unsigned char*)d_ws;
  KgenArgs ka;
  ka.logA = (const u16*)d_in[4]; ka.Aim = (const u16*)d_in[5]; ka.Cre = (const u16*)d_in[6]; ka.Cim = (const u16*)d_in[7];
  ka.logdt = (const u16*)d_in[8]; ka.Dskip = (const u16*)d_in[9];
  MainArgs ma;
  ma.x = (const u16*)d_in[0];
  ma.wqkv = (const u16*)d_in[1]; ma.wo = (const u16*)d_in[2];
  ma.ws = ws; ma.out = (u16*)d_out;
  k_main<<<256, 768, 0, stream>>>(ma, ka);
}
#if defined(__HIP_DEVICE_COMPILE__) && !defined(ALLOW_PK)
#pragma clang attribute pop
#endif
```

```cpp
#if defined(__HIP_DEVICE_COMPILE__) && !defined(ALLOW_PK)
#pragma clang attribute push(__attribute__((target("no-packed-fp32-ops"))), apply_to = function)
#endif
#include <hip/hip_runtime.h>
#include <hip/hip_bf16.h>
#include <stdint.h>

#define BIDX ((int)__builtin_amdgcn_workgroup_id_x())
#define TIDX ((int)__builtin_amdgcn_workitem_id_x())
typedef unsigned short u16;
typedef unsigned int u32;
typedef unsigned long long u64;
typedef __attribute__((ext_vector_type(8))) short bf16x8;
typedef __attribute__((ext_vector_type(4))) short s16x4;
typedef __attribute__((ext_vector_type(4))) float f32x4;
typedef __attribute__((ext_vector_type(2))) float f32x2;
typedef __attribute__((ext_vector_type(2))) __bf16 bf2v;
typedef __attribute__((ext_vector_type(4))) u32 u32x4;
typedef __attribute__((ext_vector_type(2))) u32 u32x2;

#define XP 2112
#define D_ 640
#define NST 192
#define CHAN(b, q) ((q) == 2 ? (b) >> 1 : 128 + 256 * (q) + (b))

#define WS_WQ  0
#define WS_WOT (WS_WQ + 1920 * 640 * 2)
#define WS_CW  (WS_WOT + 640 * 640 * 2)
#define WS_KP  (WS_CW + 4 * 2048 * 4)
#define WS_IM0 (WS_KP + 640 * 768 * 4)
#define WS_IM1 (WS_IM0 + 32 * 102400)
#define HO_ST (2048 * 640 * 2)
#define HO_X1 (HO_ST + 2048 * 128)
#define HO_FS (HO_X1 + 640 * XP * 2)
#define HO_BYTES (256 * 1392 * 16)
#define WS_O   (WS_IM1 + 32 * 102400)
#define WS_FIN (WS_O + 2 * HO_BYTES)
#define WS_FLG (WS_FIN + 2048 * 640 * 2)
#define FL_W 128
#define FL_M (FL_W + 480 * 128)
#define FL_M2 (FL_M + 384 * 128)
#define FL_END (FL_M2 + 256 * 8)

#define RETRY(i, n)
#define ST_DECL
#define ST(i)
#define ST_FLUSH(kid)

__device__ __forceinline__ float bf2f(u16 x) { return __uint_as_float(((u32)x) << 16); }
__device__ __forceinline__ u32 pack2(float a, float b) {
  f32x2 v = {a, b};
  bf2v r = __builtin_convertvector(v, bf2v);
  return __builtin_bit_cast(u32, r);
}
__device__ __forceinline__ float lo_f(u32 w) { return __uint_as_float(w << 16); }
__device__ __forceinline__ float hi_f(u32 w) { return __uint_as_float(w & 0xffff0000u); }
__device__ __forceinline__ float rbf(float f) { return lo_f(pack2(f, f)); }
__device__ __forceinline__ bf16x8 as_frag(uint4 v) {
  union { uint4 u; bf16x8 f; } c; c.u = v; return c.f;
}

#define X1_P 248
__device__ __forceinline__ float xmax16_32(float x) {
  auto s = __builtin_amdgcn_permlane16_swap(__float_as_uint(x), __float_as_uint(x), false, false);
  x = fmaxf(__uint_as_float(s[0]), __uint_as_float(s[1]));
  auto t = __builtin_amdgcn_permlane32_swap(__float_as_uint(x), __float_as_uint(x), false, false);
  return fmaxf(__uint_as_float(t[0]), __uint_as_float(t[1]));
}

typedef __attribute__((ext_vector_type(8))) _Float16 f16x8;
typedef __attribute__((ext_vector_type(2))) _Float16 f16x2;
__device__ __forceinline__ u32 packh(float a, float b) {
  f32x2 v = {a, b};
  return __builtin_bit_cast(u32, __builtin_convertvector(v, f16x2));
}
__device__ __forceinline__ f16x8 as_hfrag(u32 a, u32 b, u32 c, u32 d) { return __builtin_bit_cast(f16x8, (u32x4){a, b, c, d}); }
__device__ __forceinline__ float wave_max(float x) {
  x = fmaxf(x, __builtin_bit_cast(float, __builtin_amdgcn_update_dpp(0, __builtin_bit_cast(int, x), 0xB1, 0xf, 0xf, false)));
  x = fmaxf(x, __builtin_bit_cast(float, __builtin_amdgcn_update_dpp(0, __builtin_bit_cast(int, x), 0x4E, 0xf, 0xf, false)));
  x = fmaxf(x, __builtin_bit_cast(float, __builtin_amdgcn_update_dpp(0, __builtin_bit_cast(int, x), 0x141, 0xf, 0xf, false)));
  x = fmaxf(x, __builtin_bit_cast(float, __builtin_amdgcn_update_dpp(0, __builtin_bit_cast(int, x), 0x140, 0xf, 0xf, false)));
  return xmax16_32(x);
}
template <int T> __device__ __forceinline__ void pow2_scale(float m, float& s, float& inv) {
  int e = (int)(__float_as_uint(m) >> 23);
  e = min(max(e, T + 2), 250);
  s = __uint_as_float((u32)(254 + T - e) << 23);
  inv = __uint_as_float((u32)(e - T) << 23);
}
__device__ __forceinline__ void cexpr(float zl, float e, float zh, float ep, float& re, float& im) {
  const float m = __builtin_amdgcn_exp2f(zl * e);
  const float r = zh * ep;
  re = m * __builtin_amdgcn_cosf(r);
  im = m * __builtin_amdgcn_sinf(r);
}
struct KgenArgs { const u16 *logA, *Aim, *Cre, *Cim, *logdt, *Dskip; };
#define LDSA(p) ((u32)(size_t)(__attribute__((address_space(3))) void*)(p))
#define PIN(x) asm volatile("" : "+v"(x))
template <int OFF> __device__ __forceinline__ void lds_inc(u32 a, u32 one) {
  asm volatile("s_mov_b64 exec, 1\n\tds_add_u32 %0, %1 offset:%2\n\ts_mov_b64 exec, -1" :: "v"(a), "v"(one), "i"(OFF) : "memory");
}
template <int OFF, int SLEEP> __device__ __forceinline__ void lds_poll(u32 a, int target) {
  u32 v; int s, cnt;
  asm volatile(
      "s_mov_b32 %2, 0x100000\n\t"
      "1:\n\t"
      "ds_read_b32 %0, %3 offset:%5\n\t"
      "s_waitcnt lgkmcnt(0)\n\t"
      "v_readfirstlane_b32 %1, %0\n\t"
      "s_nop 0\n\t"
      "s_cmp_ge_i32 %1, %4\n\t"
      "s_cbranch_scc1 2f\n\t"
      "s_sleep %6\n\t"
      "s_sub_u32 %2, %2, 1\n\t"
      "s_cbranch_scc0 1b\n\t"
      "2:\n\t"
      : "=&v"(v), "=&s"(s), "=&s"(cnt) : "v"(a), "s"(target), "i"(OFF), "i"(SLEEP) : "memory", "scc");
}
#define SY_PART 0
#define SY_FREE 4
#define SY_WO 8
#define SY_FIN 24
#define SY_X1 32
#define SY_FS 36
#define SY_WI 40
#define SY_KP 44
#define SY_MK 48
#define SY_RV 52
#define SY_TI 56
#define SY_PG 64
#define SY_X 128
#define SY_STAT 168
#define SY_W 172
#define SY_RED 192
#define SY_Z 224
template <int SLEEP> __device__ __forceinline__ void pg_poll(u32 a, u32 target) {
  int cnt = 1 << 20;
  _Pragma("unroll 1")
  while (true) {
    u32x4 v;
    asm volatile("ds_read_b128 %0, %1 offset:%2\n\ts_waitcnt lgkmcnt(0)" : "=v"(v) : "v"(a), "i"(SY_PG) : "memory");
    if ((u32)__builtin_amdgcn_readfirstlane(min(min(v[0], v[1]), min(v[2], v[3]))) >= target || --cnt < 0) break;
    if (SLEEP) __builtin_amdgcn_s_sleep(SLEEP);
  }
}
__device__ __forceinline__ void kgen_units(int lane, const float* Kd, int nk, f32x4 (&acc)[3]) {
  const int i = lane & 15, kg = lane >> 4;
  const float eE = 8.f * (float)i;
  const int r_ = i & 3;
  const float e0 = (float)((i < 8 ? 2 * r_ : r_ == 0 ? 512 : r_ == 1 ? 516 : 512 * r_) + 32 * (i & 4));
  const float e0p = i < 8 ? e0 : 0.25f * e0;
  typedef __attribute__((address_space(3))) const f32x4* lf4;
  typedef __attribute__((address_space(3))) const u32x4* lu4;
  u32 pp = LDSA(Kd) + 16 * kg, pc = pp + (i < 8 ? 0 : 256);
#pragma unroll 1
  for (int ks = 0; ks < nk; ++ks) {
    PIN(pp); PIN(pc);
    const f32x4 zl = *(lf4)(size_t)pp, zs = ((lf4)(size_t)pc)[16], zh = ((lf4)(size_t)pp)[16];
    const f32x4 lc = ((lf4)(size_t)pp)[48], ph = ((lf4)(size_t)pp)[64];
    const u32x4 ma[2] = {((lu4)(size_t)pc)[80], ((lu4)(size_t)pp)[112]};
    pp += 64; pc += 64;
    float r[4], q[4], gr[4], gi[4];
#pragma unroll
    for (int e = 0; e < 4; ++e) cexpr(zl[e], e0, zs[e], e0p, gr[e], gi[e]);
#pragma unroll
    for (int e = 0; e < 4; ++e) {
      const float m = __builtin_amdgcn_exp2f(fmaf(zl[e], eE, lc[e])), th = fmaf(zh[e], -eE, ph[e]);
      r[e] = m * __builtin_amdgcn_cosf(th);
      q[e] = m * __builtin_amdgcn_sinf(th);
    }
    const f16x8 eh = as_hfrag(packh(r[0], r[1]), packh(r[2], r[3]), packh(q[0], q[1]), packh(q[2], q[3]));
    const u32 g[4] = {packh(gr[0], gr[1]), packh(gr[2], gr[3]), packh(gi[0], gi[1]), packh(gi[2], gi[3])};
    acc[0] = __builtin_amdgcn_mfma_f32_16x16x32_f16(as_hfrag(g[0], g[1], g[2], g[3]), eh, acc[0], 0, 0, 0);
#pragma unroll
    for (int m = 0; m < 2; ++m) {
      u32 d[4];
#pragma unroll
      for (int e = 0; e < 2; ++e) {
        const f16x2 xr = __builtin_bit_cast(f16x2, g[e]), xi = __builtin_bit_cast(f16x2, g[2 + e]);
        const u32 ur = ma[m][e], ui = ma[m][2 + e];
        const f16x2 mr = __builtin_bit_cast(f16x2, ur), mi = __builtin_bit_cast(f16x2, ui);
        d[e] = __builtin_bit_cast(u32, __builtin_elementwise_fma(xr, mr, -(xi * mi)));
        d[2 + e] = __builtin_bit_cast(u32, __builtin_elementwise_fma(xi, mr, xr * mi));
      }
      acc[m + 1] = __builtin_amdgcn_mfma_f32_16x16x32_f16(as_hfrag(d[0], d[1], d[2], d[3]), eh, acc[m + 1], 0, 0, 0);
    }
  }
}
template <int NQ> struct KgenIn { u16 la[NQ], ai[NQ], cr[NQ], ci[NQ], ld, ds; };
template <int NQ, int MODE>
__device__ __forceinline__ void kgen_load(int c, int ks0, int nk, int lane, const KgenArgs& ka, KgenIn<NQ>& in) {
  in.ld = ka.logdt[c];
#pragma unroll
  for (int q = 0; q < NQ; ++q) {
    const int n = 64 * q + lane, gi = c * NST + 16 * ks0 + (n < 16 * nk ? n : 0);
    in.la[q] = ka.logA[gi]; in.ai[q] = ka.Aim[gi]; in.cr[q] = ka.Cre[gi]; in.ci[q] = ka.Cim[gi];
  }
  if (MODE == 1) in.ds = ka.Dskip[c];
}
#define ATC0 1.591508832e-01f
#define ATC1 -5.292989150e-02f
#define ATC2 3.075435195e-02f
#define ATC3 -1.841891266e-02f
#define ATC4 8.268183977e-03f
#define ATC5 -1.824840286e-03f
__device__ __forceinline__ float kgen_wave(int lane, const KgenIn<1>& in, float* Kd, f32x4 (&acc)[3]) {
#pragma unroll
  for (int m = 0; m < 3; ++m) acc[m] = (f32x4){0.f, 0.f, 0.f, 0.f};
  float cs, csi;
  const u32 za[1] = {in.la[0] | ((u32)in.ai[0] << 16)}, ca[1] = {in.cr[0] | ((u32)in.ci[0] << 16)};
  const float dt = rbf(__expf(bf2f(in.ld)));
  pow2_scale<10>(wave_max(fmaxf(fabsf(bf2f(in.cr[0])), fabsf(bf2f(in.ci[0])))), cs, csi);
  {
    const float zl = (-rbf(__expf(lo_f(za[0]))) * dt) * 1.4426950408889634f, zi = hi_f(za[0]) * dt;
    const float zh = zi * 0.15915494309189535f;
    const float z1 = zh - rintf(zh);
    float ar, ai, br, bi;
    cexpr(zl, 1.0f, z1, 1.0f, ar, ai);
    cexpr(zl, 256.0f, 256.0f * zh - rintf(256.0f * zh), 1.0f, br, bi);
    Kd[lane] = zl;
    Kd[64 + lane] = z1;
    Kd[128 + lane] = 4.0f * zh - rintf(4.0f * zh);
    const float cr = lo_f(ca[0]) * cs, ci = hi_f(ca[0]) * cs, ax = fabsf(cr), ay = fabsf(ci);
    const float tq = fminf(ax, ay) * __builtin_amdgcn_rcpf(fmaxf(fmaxf(ax, ay), 1e-30f)), t2 = tq * tq;
    float an = tq * fmaf(t2, fmaf(t2, fmaf(t2, fmaf(t2, fmaf(t2, ATC5, ATC4), ATC3), ATC2), ATC1), ATC0);
    an = ay > ax ? 0.25f - an : an;
    an = cr < 0.f ? 0.5f - an : an;
    Kd[192 + lane] = 0.5f * __builtin_amdgcn_logf(fmaf(cr, cr, ci * ci));
    Kd[256 + lane] = ci < 0.f ? an : -an;
    u16* kh = (u16*)(Kd + 320) + 8 * (lane >> 2) + (lane & 3);
    const u32 h0 = packh(ar, ai), h1 = packh(br * br - bi * bi, 2.0f * br * bi), h2 = packh(br, bi);
    kh[0] = (u16)h0; kh[4] = (u16)(h0 >> 16);
    kh[128] = (u16)h1; kh[132] = (u16)(h1 >> 16);
    kh[256] = (u16)h2; kh[260] = (u16)(h2 >> 16);
    asm volatile("s_waitcnt lgkmcnt(0)" ::: "memory");
    kgen_units(lane, Kd, 4, acc);
    asm volatile("" ::: "memory");
  }
#pragma unroll
  for (int m = 0; m < 3; ++m) acc[m] *= csi;
  return dt;
}
__device__ __forceinline__ void kgen_fin(int lane, float dt, float dsk, float* Kd, f32x4 (&acc)[3]) {
  const int i = lane & 15, kg = lane >> 4;
  float km = 0.f, ks_, ksi;
#pragma unroll
  for (int m = 0; m < 3; ++m)
#pragma unroll
    for (int r = 0; r < 4; ++r) km = fmaxf(km, fabsf(acc[m][r]));
  pow2_scale<14>(wave_max(km), ks_, ksi);
#pragma unroll
  for (int m = 0; m < 3; ++m) acc[m] *= ks_;
  u16* kq = (u16*)Kd;
  const u32 t0 = packh(acc[2][0], acc[2][1]), t1 = packh(acc[2][2], acc[2][3]);
  if (kg < 2) {
    *(u32x4*)&kq[128 * kg + 8 * i] = (u32x4){packh(acc[0][0], acc[1][0]), packh(acc[0][1], acc[1][1]), packh(acc[0][2], acc[1][2]), packh(acc[0][3], acc[1][3])};
    *(u32x2*)&kq[256 + 64 * kg + 4 * i] = (u32x2){t0, t1};
  } else {
    const u32 s1 = packh(acc[0][2], acc[0][3]);
    *(u32*)&kq[320 + 32 * kg + 2 * i] = packh(acc[0][0], acc[0][1]);
    *(u32*)&kq[384 + 32 * kg + 2 * i] = t0;
    u16* kr = kq + 480 + lane;
    kr[0] = (u16)s1; kr[64] = (u16)(s1 >> 16); kr[32] = (u16)t1; kr[96] = (u16)(t1 >> 16);
    if (lane == 32) kq[640] = (u16)packh(acc[1][3], 0.f);
  }
  if (lane == 0) { Kd[384] = (dt * (2.0f / 256.0f)) * ksi; Kd[385] = dsk * (1.0f / 256.0f); }
}

#define A_R1 105600
#define A_R2 (A_R1 + 80 * X1_P * 2)
#define SY_OFF (A_R2 + 1280)
#define CW_OFF (SY_OFF + 512)
#define KQ_OFF (CW_OFF + 2304)
#define BND_OFF (KQ_OFF + 3 * 3072)
#define WO_OFF (A_R2 - 102400)
#define A_LDS (BND_OFF + 4096)
#define VMCNT(n) asm volatile("s_waitcnt vmcnt(" #n ")" ::: "memory")
__device__ __forceinline__ u32 umax3(u32 a, u32 b, u32 c) { return max(max(a, b), c); }
#define OSLEEP1 2
#define SSLEEP 11
#define OSLEEP 14
#define XSLEEP 14
#define XPOLL 4
#define WSLEEP0 0
#define GCAP (1 << 16)
#define EX_OFF 1024
#define X1T_OFF 25600
#define MARK16 ((u32x4){0xffffffffu, 0xffffffffu, 0xffffffffu, 0xffffffffu})
#define WSLEEP 2
#define STX(i)
#define STY(i) ST(i)
#define LANE(x) int x; asm volatile("v_mbcnt_lo_u32_b32 %0, -1, 0\n\tv_mbcnt_hi_u32_b32 %0, -1, %0" : "=v"(x))
#define NST0 12
#define NST1 10
#define PF 2
#define PTX 1
#define PTW 1
#define PTA 2
#define WOAT 32
#define MSL0 1
#define MSL1 2
#define MSL2 2
#define MKW 4
#define MKAT 12
#define XSAT 20
#define CWAT 4
#define CWW 6
#define PFS 3
#define APS 1
#define XSLEEP1 10
#define XJ0 2
#define XJ1 3
#define XSAT0 16
#define PTO 1
#define PCW 3
#define XSRT 8
#define WOAT0 28
#define OSLEEPB 9
#define MPW 9
#define XSSL 5
template <int layer>
__device__ __forceinline__ void a_phase(unsigned char* smem, const int b, const int t, const u16* xt, const u16* wqkv, const u16* wo, unsigned char* ws,
                                        u16* obuf, u16* xtout, float2* stats, const KgenArgs& ka, u32x4& sink, u32& tg0, u32& tg1, u32x4 (&wst)[8]) {
  u16* wq = (u16*)(ws + WS_WQ);
  u16* wot = (u16*)(ws + WS_WOT);
  const __amdgpu_buffer_rsrc_t frs = __builtin_amdgcn_make_buffer_rsrc(ws + WS_FLG, 0, FL_END, 0x00020000);
  const int kid = layer ? 2 : 1;
  u16* At = (u16*)smem;
  u16* St = (u16*)smem;
  u16* Sc = (u16*)(smem + A_R1);
  const int xcd = b & 7, idx = b >> 3;
  const int rb = 8 * (xcd >> 1) + (idx & 7), hg = 4 * (xcd & 1) + (idx >> 3), s0 = rb * 64;
  const int rot = hg >> 1, xs0 = 3 * rot + 2 * (hg & 1);
#define PAIR(p, r) ((p) < 6 ? (p) : 6 + (((p) - 6 + (r)) & 3))
  const int lane = t & 63, w = __builtin_amdgcn_readfirstlane(t >> 6);
  const int l15 = lane & 15, g = lane >> 4;
  u32 sya = LDSA(smem) + SY_OFF + 256 * layer, one = 1u;
  const bf16x8 onesf = as_frag(make_uint4(0x3f803f80u, 0x3f803f80u, 0x3f803f80u, 0x3f803f80u));
  u32x2 tag;
  ST_DECL

  const __amdgpu_buffer_rsrc_t xsrs = __builtin_amdgcn_make_buffer_rsrc(ws + WS_IM0 + layer * 262144, 0, 262144, 0x00020000);
  if (!layer) {
#define FLAG(o) asm volatile("buffer_store_dwordx2 %0, %3, %1, %2 offen sc1" :: "v"(tag), "s"(frs), "s"(o), "v"(g ? -1 : lane * 8) : "memory")
#define XDMA0(P) { const int pc = PAIR(P, rot); \
      const u16* sp = xt + (size_t)(s0 - 16 + (lane >> 3)) * D_ + 8 * ((lane & 7) ^ ((lane >> 3) & 6)) + pc * 64; \
      _Pragma("unroll") for (int i = 0; i < 10; ++i) \
        __builtin_amdgcn_global_load_lds((const u32*)(sp + 8 * i * D_ + (i < 2 && !rb ? 16 * D_ : 0)), (u32*)(At + ((P) * 10 + i) * 512), 16, 0, 0); }
#define WPOLL { \
        __builtin_amdgcn_s_sleep(WSLEEP0); \
        int spins = 0; \
        _Pragma("unroll 1") \
        while (true) { \
          asm volatile("" ::: "memory"); \
          const u32x2 f = __builtin_amdgcn_raw_buffer_load_b64(frs, min(lane, 59) * 128, FL_W + hg * 7680, 16); \
          if (__builtin_amdgcn_ballot_w64(f[0] != tg0 || f[1] != tg1) == 0 || ++spins > GCAP) break; \
          __builtin_amdgcn_s_sleep(WSLEEP); \
        } \
        RETRY(0, spins); \
        lds_inc<SY_W>(sya, one); \
      }
    if (__builtin_expect(w < 2, 1)) {
      const int r6 = (lane * 205) >> 11, c10 = lane - 10 * r6;
      const int q = (lane >> 2) & 3, p4 = lane & 3;
      __builtin_amdgcn_s_setprio(3);
      const bool tw = b >= 16;
      const int j = 2 * b - 32 + w, jq = j / 3, part = j - 3 * jq, jk = jq >> 3, jh = jq & 7;
      if (__builtin_expect(tw, 1)) {
        const u16* src = wqkv + (size_t)(32 * jk + r6) * 1920 + part * 640 + jh * 80 + 8 * c10;
        u16* tl = Sc + 13952 + 2560 * w;
        if (lane < 60) {
#pragma unroll
          for (int i = 0; i < 5; ++i) __builtin_amdgcn_global_load_lds((const u32*)(src + i * 11520), (u32*)(tl + i * 480), 16, 0, 0);
          if (lane < 20) __builtin_amdgcn_global_load_lds((const u32*)(src + 5 * 11520), (u32*)(tl + 5 * 480), 16, 0, 0);
        }
      }
      asm volatile("buffer_load_dwordx2 %0, off, %1, 0 sc1" : "=v"(tag) : "s"(frs) : "memory");
      __builtin_amdgcn_s_barrier();
      const int pb = l15 + (part ? 40 * part - 40 : 80), pd = part ? 120 : 0;
      int vo[5];
#pragma unroll
      for (int ct = 0; ct < 5; ++ct) {
        const int col = pb + 16 * ct + (16 * ct + l15 >= 40 ? pd : 0);
        vo[ct] = (int)((0xeda6273b51c9840ull >> (4 * (col >> 4))) & 15) * 1024 + (g * 16 + (col & 15)) * 16;
        PIN(vo[ct]);
      }
      int co = lane * 4;
      u32 ta = LDSA(Sc + 13952 + 2560 * w) + (8 * g + q) * 160 + 8 * p4;
      PIN(ta); PIN(co);
      asm volatile("s_waitcnt vmcnt(0)" : "+v"(tag), "+v"(sink) :: "memory");
      tag[0] += 1u;
      if (__builtin_expect(tw, 1)) {
        const int kp = 2 * PAIR(jk >> 1, -(jh >> 1)) + (jk & 1);
        const __amdgpu_buffer_rsrc_t trs = __builtin_amdgcn_make_buffer_rsrc((void*)(wq + (size_t)jh * 153600 + kp * 7680), 0, 15360, 0x00020000);
        const __amdgpu_buffer_rsrc_t crs = __builtin_amdgcn_make_buffer_rsrc(ws + WS_CW + ((jh * 20 + jk) * 3 + part) * 384, 0, 384, 0x00020000);
        s16x4 fl[5], fh[5];
        asm volatile(
            "ds_read_b64_tr_b16 %0, %10\n\t"
            "ds_read_b64_tr_b16 %5, %10 offset:640\n\t"
            "ds_read_b64_tr_b16 %1, %10 offset:32\n\t"
            "ds_read_b64_tr_b16 %6, %10 offset:672\n\t"
            "ds_read_b64_tr_b16 %2, %10 offset:64\n\t"
            "ds_read_b64_tr_b16 %7, %10 offset:704\n\t"
            "ds_read_b64_tr_b16 %3, %10 offset:96\n\t"
            "ds_read_b64_tr_b16 %8, %10 offset:736\n\t"
            "ds_read_b64_tr_b16 %4, %10 offset:128\n\t"
            "ds_read_b64_tr_b16 %9, %10 offset:768\n\t"
            "s_waitcnt lgkmcnt(0)"
            : "=&v"(fl[0]), "=&v"(fl[1]), "=&v"(fl[2]), "=&v"(fl[3]), "=&v"(fl[4]),
              "=&v"(fh[0]), "=&v"(fh[1]), "=&v"(fh[2]), "=&v"(fh[3]), "=&v"(fh[4])
            : "v"(ta) : "memory");
        ST(1);
        float cs[5];
#pragma unroll
        for (int ct = 0; ct < 5; ++ct) {
          const bf16x8 fr = __builtin_shufflevector(fl[ct], fh[ct], 0, 1, 2, 3, 4, 5, 6, 7);
          asm volatile("buffer_store_dwordx4 %0, %1, %2, 0 offen sc1\n\ts_nop 1" :: "v"(fr), "v"(vo[ct]), "s"(trs) : "memory");
          cs[ct] = __builtin_amdgcn_mfma_f32_16x16x32_bf16(onesf, fr, (f32x4){0.f, 0.f, 0.f, 0.f}, 0, 0, 0)[0];
        }
        const float c03 = g < 2 ? (g ? cs[1] : cs[0]) : (g == 2 ? cs[2] : cs[3]), c4 = g ? cs[0] : cs[4];
        asm volatile("buffer_store_dword %0, %1, %2, 0 offen sc1\n\tbuffer_store_dword %3, %1, %2, 0 offen offset:256 sc1" :: "v"(c03), "v"(co), "s"(crs), "v"(c4) : "memory");
      }
      lds_inc<SY_TI>(sya, one);
      tg0 = __builtin_amdgcn_readfirstlane(tag[0]); tg1 = __builtin_amdgcn_readfirstlane(tag[1]);
      ST(3);
      XDMA0(2 + w)
      if (tw) {
        VMCNT(10);
        FLAG(FL_W + (jh * 60 + jk * 3 + part) * 128);
      }
      ST(7);
      XDMA0(5 + w)
      XDMA0(8 + w)
      if (w == 0) {
        VMCNT(0);
        lds_inc<SY_X>(sya, one);
      } else {
        const __amdgpu_buffer_rsrc_t mrs = __builtin_amdgcn_make_buffer_rsrc(obuf, 0, HO_BYTES, 0x00020000);
        _Pragma("unroll 1")
        for (int i = 0; i < 11; ++i) {
          __builtin_amdgcn_raw_buffer_store_b128(MARK16, mrs, lane * 16, i < 10 ? (rb * 8 + hg) * 10240 + i * 1024 : HO_ST + rb * 8192 + hg * 1024, 17);
          __builtin_amdgcn_s_sleep(MSL0);
        }
        VMCNT(11);
        lds_inc<SY_X>(sya, one);
        ST(6);
        VMCNT(0);
        FLAG(FL_M + b * 128);
      }
      ST(9);
    } else if (w == 2) {
      const int r6 = (lane * 205) >> 11, c10 = lane - 10 * r6, q = (lane >> 2) & 3, p4 = lane & 3;
      const bool to = b < 128;
      __builtin_amdgcn_s_setprio(3);
      asm volatile("buffer_load_dwordx2 %0, off, %1, 0 sc1" : "=v"(tag) : "s"(frs) : "memory");
      XDMA0(0)
      __builtin_amdgcn_s_barrier();
      XDMA0(1)
      if (to) {
        const u16* src = wo + (size_t)(80 * (b >> 4) + 40 * (b & 1) + r6) * D_ + ((b >> 1) & 7) * 80 + 8 * c10;
        u16* tl = (u16*)(smem + KQ_OFF);
        if (lane < 60) {
#pragma unroll
          for (int i = 0; i < 6; ++i) __builtin_amdgcn_global_load_lds((const u32*)(src + i * 6 * D_), (u32*)(tl + i * 480), 16, 0, 0);
          if (lane < 40) __builtin_amdgcn_global_load_lds((const u32*)(src + 36 * D_), (u32*)(tl + 6 * 480), 16, 0, 0);
        }
      }
      lds_poll<SY_TI, 1>(sya, 2);
      ST(1);
      XDMA0(4)
      XDMA0(7)
      asm volatile("s_waitcnt vmcnt(20)" : "+v"(tag) :: "memory");
      tag[0] += 1u;
      tg0 = __builtin_amdgcn_readfirstlane(tag[0]); tg1 = __builtin_amdgcn_readfirstlane(tag[1]);
      if (to) {
        const int s = b >> 4;
        const __amdgpu_buffer_rsrc_t trs = __builtin_amdgcn_make_buffer_rsrc((void*)(wot + (size_t)((b >> 1) & 7) * 51200), 0, 102400, 0x00020000);
        const u32 t0 = LDSA(smem) + KQ_OFF + (4 * g + q) * 160 + 8 * p4, t1 = LDSA(smem) + KQ_OFF + (16 + q) * 160 + 8 * p4;
        const int o0 = lane * 16, o1 = g ? -1 : lane * 16;
#pragma unroll
        for (int nt = 0; nt < 5; ++nt) {
          s16x4 f[4];
          const int np = s < 4 ? (nt + 2) % 5 : nt, i0 = (s & 3) * 5 + np, fb = b & 1 ? 12 + 25 * (s >> 2) : 56 + 19 * (s >> 2);
          asm volatile("ds_read_b64_tr_b16 %0, %4 offset:%6\n\tds_read_b64_tr_b16 %1, %4 offset:%6+3200\n\tds_read_b64_tr_b16 %2, %5 offset:%6\n\tds_read_b64_tr_b16 %3, %5 offset:%6+3200\n\ts_waitcnt lgkmcnt(0)"
                       : "=&v"(f[0]), "=&v"(f[1]), "=&v"(f[2]), "=&v"(f[3]) : "v"(t0), "v"(t1), "i"(32 * nt) : "memory");
          asm volatile("buffer_store_dwordx4 %0, %2, %4, %5 offen sc1\n\tbuffer_store_dwordx4 %1, %3, %4, %6 offen sc1\n\ts_nop 1"
                       :: "v"(__builtin_shufflevector(f[0], f[1], 0, 1, 2, 3, 4, 5, 6, 7)), "v"(__builtin_shufflevector(f[2], f[3], 0, 1, 2, 3, 4, 5, 6, 7)),
                          "v"(o0), "v"(o1), "s"(trs), "s"((i0 + (!(b & 1) && i0 < 6 ? 6 * (s >> 2) : fb)) * 1024), "s"((fb + 20 + np) * 1024 + (s & 3) * 256) : "memory");
        }
      }
      ST(3);
      if (to) VMCNT(10); else VMCNT(0);
      lds_inc<SY_X>(sya, one);
      ST(6);
      __builtin_amdgcn_s_waitcnt(0x0070);
      if (to) FLAG(FL_M + (256 + b) * 128);
      ST(9);
    } else {
      const int ar = w - 3, ci = ar % 3;
      KgenIn<1> kin;
      tag = __builtin_amdgcn_raw_buffer_load_b64(frs, 0, 0, 16);
      kgen_load<1, 1>(CHAN(b, ci), 4 * (ar / 3), 4, lane, ka, kin);
      asm volatile("" ::: "memory");
      if (w == 4) asm volatile("ds_write_b64 %0, %1\n\ts_waitcnt lgkmcnt(0)" :: "v"(sya + 8 * lane), "v"((u32x2){0u, 0u}) : "memory");
      __builtin_amdgcn_s_barrier();
      if (w == 8) {
        bf16x8 be, bo, b5, b3;
#pragma unroll
        for (int e = 0; e < 8; ++e) {
          const int d = 8 * g + e - l15, d4 = (g >> 1) == (l15 >> 3) ? d - (l15 & 8) : -1;
          be[e] = (d >= 4 && d <= 12) ? (short)0x3c00 : (short)0;
          bo[e] = (d >= 5 && d <= 11) ? (short)0x3c00 : (short)0;
          b5[e] = (d4 >= 2 && d4 <= 6) ? (short)0x3c00 : (short)0;
          b3[e] = (d4 >= 3 && d4 <= 5) ? (short)0x3c00 : (short)0;
        }
        asm volatile("ds_write_b128 %0, %1\n\tds_write_b128 %0, %2 offset:1024\n\tds_write_b128 %0, %3 offset:2048\n\tds_write_b128 %0, %4 offset:3072"
                     :: "v"(LDSA(smem) + BND_OFF + 16 * lane), "v"(be), "v"(bo), "v"(b5), "v"(b3) : "memory");
      }
      if (w == 3) __builtin_amdgcn_s_setprio(2); else __builtin_amdgcn_s_setprio(1);
      tg0 = __builtin_amdgcn_readfirstlane(tag[0]) + 1u; tg1 = __builtin_amdgcn_readfirstlane(tag[1]);
      f32x4 kacc[3];
      f32x4* ka_ = (f32x4*)(Sc + ar * 1536);
      const float dt = kgen_wave(lane, kin, (float*)ka_, kacc);
      if (w == 3) ST(6); else ST(4);
      if (ar < 6) {
#pragma unroll
        for (int m = 0; m < 3; ++m) ka_[64 * m + lane] = kacc[m];
        lds_inc<SY_KP>(sya, one);
      } else {
        const f32x4* pa = (const f32x4*)Sc + ci * 192 + lane;
        lds_poll<SY_KP, 2>(sya, 6);
#pragma unroll
        for (int m = 0; m < 3; ++m) { kacc[m] += pa[64 * m] + pa[576 + 64 * m]; PIN(kacc[m]); }
        lds_poll<SY_X, 2>(sya, 3);
        kgen_fin(lane, dt, bf2f(kin.ds), (float*)(smem + KQ_OFF + ci * 3072), kacc);
      }
      __builtin_amdgcn_s_waitcnt(0x0070);
    }
  }
  if (layer) lds_inc<SY_TI>(sya, one);
  if (w < 4) {
    {
    LANE(lane);
    const int l15 = lane & 15, g = lane >> 4;
    const __amdgpu_buffer_rsrc_t wrsrc = __builtin_amdgcn_make_buffer_rsrc((void*)(wq + (size_t)hg * 153600), 0, 307200, 0x00020000);
    const int tb = w == 1 ? 1 : 0;
    u32 wa = (w == 2 ? 11 : w == 3 ? 8 : 4 * w) * 1024 + lane * 16;
    u32 xa = LDSA(At) + (layer ? (4 * g + ((lane >> 2) & 3)) * 32 + 8 * (lane & 3) : (l15 >> 3) * 1024 + (l15 & 7) * 128 + ((g ^ (l15 & 6)) << 4)), xb = xa ^ 64;
    u32 sy = sya + SY_X, pga = sya + SY_PG + 4 * w, pv;
    int so, s0_, s1_;
    if (!layer && w == 3) {
      __builtin_amdgcn_s_setprio(3);
      PIN(wa); PIN(xa); PIN(xb); PIN(sy); PIN(pga);
      WPOLL
      ST(10);
    } else __builtin_amdgcn_s_setprio(PF);
    if (!layer) {
      asm volatile(
        "s_cmp_lt_u32 %[w], 2\ns_cbranch_scc0 .LB%=\ns_mov_b32 %[s1], 0x100000\n.LA0w%=:\nds_read_b32 %[pv], %[sy] offset:%[ow]\ns_waitcnt lgkmcnt(0)\nv_readfirstlane_b32 %[s0], %[pv]\ns_nop 0\n"
        "s_cmp_ge_i32 %[s0], 1\ns_cbranch_scc1 .LA0h%=\ns_sleep 1\ns_sub_u32 %[s1], %[s1], 1\ns_cbranch_scc0 .LA0w%=\n.LA0h%=:\ns_mov_b32 %[so], 0\n"
        "buffer_load_dwordx4 v[32:35], %[wa], %[rs], %[so] offen offset:0 sc1\nbuffer_load_dwordx4 v[36:39], %[wa], %[rs], %[so] offen offset:1024 sc1\n"
        "buffer_load_dwordx4 v[40:43], %[wa], %[rs], %[so] offen offset:2048 sc1\nbuffer_load_dwordx4 v[44:47], %[wa], %[rs], %[so] offen offset:3072 sc1\ns_mov_b32 %[so], 15360\n"
        "buffer_load_dwordx4 v[48:51], %[wa], %[rs], %[so] offen offset:0 sc1\nbuffer_load_dwordx4 v[52:55], %[wa], %[rs], %[so] offen offset:1024 sc1\n"
        "buffer_load_dwordx4 v[56:59], %[wa], %[rs], %[so] offen offset:2048 sc1\nbuffer_load_dwordx4 v[60:63], %[wa], %[rs], %[so] offen offset:3072 sc1\ns_mov_b32 %[so], 30720\n"
        "buffer_load_dwordx4 v[64:67], %[wa], %[rs], %[so] offen offset:0 sc1\nbuffer_load_dwordx4 v[68:71], %[wa], %[rs], %[so] offen offset:1024 sc1\n"
        "buffer_load_dwordx4 v[72:75], %[wa], %[rs], %[so] offen offset:2048 sc1\nbuffer_load_dwordx4 v[76:79], %[wa], %[rs], %[so] offen offset:3072 sc1\ns_mov_b32 %[so], 46080\n"
        "buffer_load_dwordx4 v[80:83], %[wa], %[rs], %[so] offen offset:0 sc1\nbuffer_load_dwordx4 v[84:87], %[wa], %[rs], %[so] offen offset:1024 sc1\n"
        "buffer_load_dwordx4 v[88:91], %[wa], %[rs], %[so] offen offset:2048 sc1\nbuffer_load_dwordx4 v[92:95], %[wa], %[rs], %[so] offen offset:3072 sc1\ns_setprio %[pf]\ns_mov_b32 %[s1], 0x100000\n"
        ".LA0v%=:\nds_read_b32 %[pv], %[sy] offset:0\ns_waitcnt lgkmcnt(0)\nv_readfirstlane_b32 %[s0], %[pv]\ns_nop 0\ns_cmp_ge_i32 %[s0], 3\ns_cbranch_scc1 .LA0g%=\ns_sleep 1\n"
        "s_sub_u32 %[s1], %[s1], 1\ns_cbranch_scc0 .LA0v%=\n.LA0g%=:\nds_read_b128 v[16:19], %[xa] offset:2048\nds_read_b128 v[20:23], %[xa] offset:4096\nds_read_b128 v[12:15], %[xa] offset:0\n"
        "ds_read_b128 v[24:27], %[xa] offset:6144\ns_waitcnt vmcnt(12) lgkmcnt(3)\nv_mfma_f32_16x16x32_bf16 v[100:103], v[32:35], v[16:19], 0\nds_read_b128 v[28:31], %[xa] offset:8192\n"
        "v_mfma_f32_16x16x32_bf16 v[116:119], v[36:39], v[16:19], 0\nv_mfma_f32_16x16x32_bf16 v[136:139], v[40:43], v[16:19], 0\nv_mfma_f32_16x16x32_bf16 v[152:155], v[44:47], v[16:19], 0\n"
        "ds_read_b128 v[16:19], %[xb] offset:2048\ns_waitcnt lgkmcnt(4)\nv_mfma_f32_16x16x32_bf16 v[104:107], v[32:35], v[20:23], 0\nv_mfma_f32_16x16x32_bf16 v[120:123], v[36:39], v[20:23], 0\n"
        "v_mfma_f32_16x16x32_bf16 v[140:143], v[40:43], v[20:23], 0\nv_mfma_f32_16x16x32_bf16 v[156:159], v[44:47], v[20:23], 0\nds_read_b128 v[20:23], %[xb] offset:4096\ns_waitcnt lgkmcnt(4)\n"
        "v_mfma_f32_16x16x32_bf16 v[96:99], v[32:35], v[12:15], 0\nv_mfma_f32_16x16x32_bf16 v[132:135], v[40:43], v[12:15], 0\nds_read_b128 v[12:15], %[xb] offset:0\ns_waitcnt lgkmcnt(4)\n"
        "v_mfma_f32_16x16x32_bf16 v[108:111], v[32:35], v[24:27], 0\nv_mfma_f32_16x16x32_bf16 v[124:127], v[36:39], v[24:27], 0\nv_mfma_f32_16x16x32_bf16 v[144:147], v[40:43], v[24:27], 0\n"
        "v_mfma_f32_16x16x32_bf16 v[160:163], v[44:47], v[24:27], 0\nds_read_b128 v[24:27], %[xb] offset:6144\ns_waitcnt lgkmcnt(4)\nv_mfma_f32_16x16x32_bf16 v[112:115], v[32:35], v[28:31], 0\n"
        "v_mfma_f32_16x16x32_bf16 v[128:131], v[36:39], v[28:31], 0\nv_mfma_f32_16x16x32_bf16 v[148:151], v[40:43], v[28:31], 0\nv_mfma_f32_16x16x32_bf16 v[164:167], v[44:47], v[28:31], 0\n.LA0l%=:\n"
        "s_waitcnt vmcnt(8) lgkmcnt(3)\nv_mfma_f32_16x16x32_bf16 v[100:103], v[48:51], v[16:19], v[100:103]\ns_addk_i32 %[so], 15360\nds_read_b128 v[28:31], %[xb] offset:8192\n"
        "v_mfma_f32_16x16x32_bf16 v[116:119], v[52:55], v[16:19], v[116:119]\nv_mfma_f32_16x16x32_bf16 v[136:139], v[56:59], v[16:19], v[136:139]\n"
        "buffer_load_dwordx4 v[32:35], %[wa], %[rs], %[so] offen offset:0 sc1\nv_mfma_f32_16x16x32_bf16 v[152:155], v[60:63], v[16:19], v[152:155]\nds_read_b128 v[16:19], %[xa] offset:12288\n"
        "s_waitcnt lgkmcnt(4)\nv_mfma_f32_16x16x32_bf16 v[104:107], v[48:51], v[20:23], v[104:107]\nv_mfma_f32_16x16x32_bf16 v[120:123], v[52:55], v[20:23], v[120:123]\n"
        "buffer_load_dwordx4 v[36:39], %[wa], %[rs], %[so] offen offset:1024 sc1\nv_mfma_f32_16x16x32_bf16 v[140:143], v[56:59], v[20:23], v[140:143]\n"
        "v_mfma_f32_16x16x32_bf16 v[156:159], v[60:63], v[20:23], v[156:159]\nds_read_b128 v[20:23], %[xa] offset:14336\ns_waitcnt lgkmcnt(4)\n"
        "v_mfma_f32_16x16x32_bf16 v[96:99], v[48:51], v[12:15], v[96:99]\nv_mfma_f32_16x16x32_bf16 v[132:135], v[56:59], v[12:15], v[132:135]\nds_read_b128 v[12:15], %[xa] offset:10240\n"
        "s_waitcnt lgkmcnt(4)\nv_mfma_f32_16x16x32_bf16 v[108:111], v[48:51], v[24:27], v[108:111]\nbuffer_load_dwordx4 v[40:43], %[wa], %[rs], %[so] offen offset:2048 sc1\n"
        "v_mfma_f32_16x16x32_bf16 v[124:127], v[52:55], v[24:27], v[124:127]\nv_mfma_f32_16x16x32_bf16 v[144:147], v[56:59], v[24:27], v[144:147]\n"
        "buffer_load_dwordx4 v[44:47], %[wa], %[rs], %[so] offen offset:3072 sc1\nv_mfma_f32_16x16x32_bf16 v[160:163], v[60:63], v[24:27], v[160:163]\nds_read_b128 v[24:27], %[xa] offset:16384\n"
        "s_waitcnt lgkmcnt(4)\nv_mfma_f32_16x16x32_bf16 v[112:115], v[48:51], v[28:31], v[112:115]\nv_mfma_f32_16x16x32_bf16 v[128:131], v[52:55], v[28:31], v[128:131]\nv_add_u32 %[sy], 4, %[sy]\n"
        "v_mfma_f32_16x16x32_bf16 v[148:151], v[56:59], v[28:31], v[148:151]\nds_write_b32 %[pg], %[sy]\nv_mfma_f32_16x16x32_bf16 v[164:167], v[60:63], v[28:31], v[164:167]\n"
        "s_waitcnt vmcnt(8) lgkmcnt(4)\nv_mfma_f32_16x16x32_bf16 v[100:103], v[64:67], v[16:19], v[100:103]\ns_addk_i32 %[so], 15360\nds_read_b128 v[28:31], %[xa] offset:18432\n"
        "v_mfma_f32_16x16x32_bf16 v[116:119], v[68:71], v[16:19], v[116:119]\nv_mfma_f32_16x16x32_bf16 v[136:139], v[72:75], v[16:19], v[136:139]\n"
        "buffer_load_dwordx4 v[48:51], %[wa], %[rs], %[so] offen offset:0 sc1\nv_mfma_f32_16x16x32_bf16 v[152:155], v[76:79], v[16:19], v[152:155]\nds_read_b128 v[16:19], %[xb] offset:12288\n"
        "s_waitcnt lgkmcnt(5)\nv_mfma_f32_16x16x32_bf16 v[104:107], v[64:67], v[20:23], v[104:107]\nv_mfma_f32_16x16x32_bf16 v[120:123], v[68:71], v[20:23], v[120:123]\n"
        "buffer_load_dwordx4 v[52:55], %[wa], %[rs], %[so] offen offset:1024 sc1\nv_mfma_f32_16x16x32_bf16 v[140:143], v[72:75], v[20:23], v[140:143]\n"
        "v_mfma_f32_16x16x32_bf16 v[156:159], v[76:79], v[20:23], v[156:159]\nds_read_b128 v[20:23], %[xb] offset:14336\ns_waitcnt lgkmcnt(5)\n"
        "v_mfma_f32_16x16x32_bf16 v[96:99], v[64:67], v[12:15], v[96:99]\nv_mfma_f32_16x16x32_bf16 v[132:135], v[72:75], v[12:15], v[132:135]\nds_read_b128 v[12:15], %[xb] offset:10240\n"
        "s_waitcnt lgkmcnt(5)\nv_mfma_f32_16x16x32_bf16 v[108:111], v[64:67], v[24:27], v[108:111]\nbuffer_load_dwordx4 v[56:59], %[wa], %[rs], %[so] offen offset:2048 sc1\n"
        "v_mfma_f32_16x16x32_bf16 v[124:127], v[68:71], v[24:27], v[124:127]\nv_mfma_f32_16x16x32_bf16 v[144:147], v[72:75], v[24:27], v[144:147]\n"
        "buffer_load_dwordx4 v[60:63], %[wa], %[rs], %[so] offen offset:3072 sc1\nv_mfma_f32_16x16x32_bf16 v[160:163], v[76:79], v[24:27], v[160:163]\nds_read_b128 v[24:27], %[xb] offset:16384\n"
        "s_waitcnt lgkmcnt(4)\nv_mfma_f32_16x16x32_bf16 v[112:115], v[64:67], v[28:31], v[112:115]\nv_mfma_f32_16x16x32_bf16 v[128:131], v[68:71], v[28:31], v[128:131]\n"
        "v_mfma_f32_16x16x32_bf16 v[148:151], v[72:75], v[28:31], v[148:151]\nv_mfma_f32_16x16x32_bf16 v[164:167], v[76:79], v[28:31], v[164:167]\ns_waitcnt vmcnt(8) lgkmcnt(3)\n"
        "v_mfma_f32_16x16x32_bf16 v[100:103], v[80:83], v[16:19], v[100:103]\ns_addk_i32 %[so], 15360\nds_read_b128 v[28:31], %[xb] offset:18432\n"
        "v_mfma_f32_16x16x32_bf16 v[116:119], v[84:87], v[16:19], v[116:119]\nv_mfma_f32_16x16x32_bf16 v[136:139], v[88:91], v[16:19], v[136:139]\n"
        "buffer_load_dwordx4 v[64:67], %[wa], %[rs], %[so] offen offset:0 sc1\nv_mfma_f32_16x16x32_bf16 v[152:155], v[92:95], v[16:19], v[152:155]\nds_read_b128 v[16:19], %[xa] offset:22528\n"
        "s_waitcnt lgkmcnt(4)\nv_mfma_f32_16x16x32_bf16 v[104:107], v[80:83], v[20:23], v[104:107]\nv_mfma_f32_16x16x32_bf16 v[120:123], v[84:87], v[20:23], v[120:123]\n"
        "buffer_load_dwordx4 v[68:71], %[wa], %[rs], %[so] offen offset:1024 sc1\nv_mfma_f32_16x16x32_bf16 v[140:143], v[88:91], v[20:23], v[140:143]\n"
        "v_mfma_f32_16x16x32_bf16 v[156:159], v[92:95], v[20:23], v[156:159]\nds_read_b128 v[20:23], %[xa] offset:24576\ns_waitcnt lgkmcnt(4)\n"
        "v_mfma_f32_16x16x32_bf16 v[96:99], v[80:83], v[12:15], v[96:99]\nv_mfma_f32_16x16x32_bf16 v[132:135], v[88:91], v[12:15], v[132:135]\nds_read_b128 v[12:15], %[xa] offset:20480\n"
        "s_waitcnt lgkmcnt(4)\nv_mfma_f32_16x16x32_bf16 v[108:111], v[80:83], v[24:27], v[108:111]\nbuffer_load_dwordx4 v[72:75], %[wa], %[rs], %[so] offen offset:2048 sc1\n"
        "v_mfma_f32_16x16x32_bf16 v[124:127], v[84:87], v[24:27], v[124:127]\nv_mfma_f32_16x16x32_bf16 v[144:147], v[88:91], v[24:27], v[144:147]\n"
        "buffer_load_dwordx4 v[76:79], %[wa], %[rs], %[so] offen offset:3072 sc1\nv_mfma_f32_16x16x32_bf16 v[160:163], v[92:95], v[24:27], v[160:163]\nds_read_b128 v[24:27], %[xa] offset:26624\n"
        "s_waitcnt lgkmcnt(4)\nv_mfma_f32_16x16x32_bf16 v[112:115], v[80:83], v[28:31], v[112:115]\nv_mfma_f32_16x16x32_bf16 v[128:131], v[84:87], v[28:31], v[128:131]\nv_add_u32 %[sy], 4, %[sy]\n"
        "v_mfma_f32_16x16x32_bf16 v[148:151], v[88:91], v[28:31], v[148:151]\nds_write_b32 %[pg], %[sy]\nv_mfma_f32_16x16x32_bf16 v[164:167], v[92:95], v[28:31], v[164:167]\n"
        "s_waitcnt vmcnt(8) lgkmcnt(4)\nv_mfma_f32_16x16x32_bf16 v[100:103], v[32:35], v[16:19], v[100:103]\ns_addk_i32 %[so], 15360\nds_read_b128 v[28:31], %[xa] offset:28672\n"
        "v_mfma_f32_16x16x32_bf16 v[116:119], v[36:39], v[16:19], v[116:119]\nv_mfma_f32_16x16x32_bf16 v[136:139], v[40:43], v[16:19], v[136:139]\n"
        "buffer_load_dwordx4 v[80:83], %[wa], %[rs], %[so] offen offset:0 sc1\nv_mfma_f32_16x16x32_bf16 v[152:155], v[44:47], v[16:19], v[152:155]\nds_read_b128 v[16:19], %[xb] offset:22528\n"
        "s_waitcnt lgkmcnt(5)\nv_mfma_f32_16x16x32_bf16 v[104:107], v[32:35], v[20:23], v[104:107]\nv_mfma_f32_16x16x32_bf16 v[120:123], v[36:39], v[20:23], v[120:123]\n"
        "buffer_load_dwordx4 v[84:87], %[wa], %[rs], %[so] offen offset:1024 sc1\nv_mfma_f32_16x16x32_bf16 v[140:143], v[40:43], v[20:23], v[140:143]\n"
        "v_mfma_f32_16x16x32_bf16 v[156:159], v[44:47], v[20:23], v[156:159]\nds_read_b128 v[20:23], %[xb] offset:24576\ns_waitcnt lgkmcnt(5)\n"
        "v_mfma_f32_16x16x32_bf16 v[96:99], v[32:35], v[12:15], v[96:99]\nv_mfma_f32_16x16x32_bf16 v[132:135], v[40:43], v[12:15], v[132:135]\nds_read_b128 v[12:15], %[xb] offset:20480\n"
        "s_waitcnt lgkmcnt(5)\nv_mfma_f32_16x16x32_bf16 v[108:111], v[32:35], v[24:27], v[108:111]\nbuffer_load_dwordx4 v[88:91], %[wa], %[rs], %[so] offen offset:2048 sc1\n"
        "v_mfma_f32_16x16x32_bf16 v[124:127], v[36:39], v[24:27], v[124:127]\nv_mfma_f32_16x16x32_bf16 v[144:147], v[40:43], v[24:27], v[144:147]\n"
        "buffer_load_dwordx4 v[92:95], %[wa], %[rs], %[so] offen offset:3072 sc1\nv_mfma_f32_16x16x32_bf16 v[160:163], v[44:47], v[24:27], v[160:163]\nds_read_b128 v[24:27], %[xb] offset:26624\n"
        "s_waitcnt lgkmcnt(4)\nv_mfma_f32_16x16x32_bf16 v[112:115], v[32:35], v[28:31], v[112:115]\nv_mfma_f32_16x16x32_bf16 v[128:131], v[36:39], v[28:31], v[128:131]\n"
        "v_mfma_f32_16x16x32_bf16 v[148:151], v[40:43], v[28:31], v[148:151]\ns_cmp_lg_u32 %[so], 291840\nv_mfma_f32_16x16x32_bf16 v[164:167], v[44:47], v[28:31], v[164:167]\n"
        "v_add_u32 %[xa], 20480, %[xa]\nv_add_u32 %[xb], 20480, %[xb]\ns_cbranch_scc1 .LA0l%=\ns_waitcnt vmcnt(8) lgkmcnt(3)\nv_mfma_f32_16x16x32_bf16 v[100:103], v[48:51], v[16:19], v[100:103]\n"
        "ds_read_b128 v[28:31], %[xb] offset:8192\nv_mfma_f32_16x16x32_bf16 v[116:119], v[52:55], v[16:19], v[116:119]\nv_mfma_f32_16x16x32_bf16 v[136:139], v[56:59], v[16:19], v[136:139]\n"
        "v_mfma_f32_16x16x32_bf16 v[152:155], v[60:63], v[16:19], v[152:155]\nds_read_b128 v[16:19], %[xa] offset:12288\ns_waitcnt lgkmcnt(4)\n"
        "v_mfma_f32_16x16x32_bf16 v[104:107], v[48:51], v[20:23], v[104:107]\nv_mfma_f32_16x16x32_bf16 v[120:123], v[52:55], v[20:23], v[120:123]\n"
        "v_mfma_f32_16x16x32_bf16 v[140:143], v[56:59], v[20:23], v[140:143]\nv_mfma_f32_16x16x32_bf16 v[156:159], v[60:63], v[20:23], v[156:159]\nds_read_b128 v[20:23], %[xa] offset:14336\n"
        "s_waitcnt lgkmcnt(4)\nv_mfma_f32_16x16x32_bf16 v[96:99], v[48:51], v[12:15], v[96:99]\nv_mfma_f32_16x16x32_bf16 v[132:135], v[56:59], v[12:15], v[132:135]\n"
        "ds_read_b128 v[12:15], %[xa] offset:10240\ns_waitcnt lgkmcnt(4)\nv_mfma_f32_16x16x32_bf16 v[108:111], v[48:51], v[24:27], v[108:111]\n"
        "v_mfma_f32_16x16x32_bf16 v[124:127], v[52:55], v[24:27], v[124:127]\nv_mfma_f32_16x16x32_bf16 v[144:147], v[56:59], v[24:27], v[144:147]\n"
        "v_mfma_f32_16x16x32_bf16 v[160:163], v[60:63], v[24:27], v[160:163]\nds_read_b128 v[24:27], %[xa] offset:16384\ns_waitcnt lgkmcnt(4)\n"
        "v_mfma_f32_16x16x32_bf16 v[112:115], v[48:51], v[28:31], v[112:115]\nv_mfma_f32_16x16x32_bf16 v[128:131], v[52:55], v[28:31], v[128:131]\nv_add_u32 %[sy], 4, %[sy]\n"
        "v_mfma_f32_16x16x32_bf16 v[148:151], v[56:59], v[28:31], v[148:151]\nv_mfma_f32_16x16x32_bf16 v[164:167], v[60:63], v[28:31], v[164:167]\ns_waitcnt vmcnt(4) lgkmcnt(3)\n"
        "v_mfma_f32_16x16x32_bf16 v[100:103], v[64:67], v[16:19], v[100:103]\nds_read_b128 v[28:31], %[xa] offset:18432\nv_mfma_f32_16x16x32_bf16 v[116:119], v[68:71], v[16:19], v[116:119]\n"
        "v_and_b32 v49, 0xf0, %[wa]\nv_mfma_f32_16x16x32_bf16 v[136:139], v[72:75], v[16:19], v[136:139]\nv_lshrrev_b32 v48, 1, v49\n"
        "v_mfma_f32_16x16x32_bf16 v[152:155], v[76:79], v[16:19], v[152:155]\nds_read_b128 v[16:19], %[xb] offset:12288\nv_mad_u32_u24 v50, v49, 31, %[c2]\ns_waitcnt lgkmcnt(4)\n"
        "v_mfma_f32_16x16x32_bf16 v[104:107], v[64:67], v[20:23], v[104:107]\nv_add_u32 v48, %[c0], v48\nv_mfma_f32_16x16x32_bf16 v[120:123], v[68:71], v[20:23], v[120:123]\n"
        "v_mfma_f32_16x16x32_bf16 v[140:143], v[72:75], v[20:23], v[140:143]\nv_add_u32 v49, %[c1], v49\nv_mfma_f32_16x16x32_bf16 v[156:159], v[76:79], v[20:23], v[156:159]\n"
        "ds_read_b128 v[20:23], %[xb] offset:14336\nv_bfe_u32 %[pv], %[wa], 8, 1\ns_waitcnt lgkmcnt(4)\nv_mfma_f32_16x16x32_bf16 v[96:99], v[64:67], v[12:15], v[96:99]\n"
        "v_mul_u32_u24 %[pv], 7936, %[pv]\nv_mfma_f32_16x16x32_bf16 v[132:135], v[72:75], v[12:15], v[132:135]\nds_read_b128 v[12:15], %[xb] offset:10240\nv_bfe_u32 v51, %[wa], 9, 1\n"
        "s_waitcnt lgkmcnt(4)\nv_mfma_f32_16x16x32_bf16 v[108:111], v[64:67], v[24:27], v[108:111]\nv_mfma_f32_16x16x32_bf16 v[124:127], v[68:71], v[24:27], v[124:127]\n"
        "v_lshl_add_u32 v51, v51, 4, v50\nv_mfma_f32_16x16x32_bf16 v[144:147], v[72:75], v[24:27], v[144:147]\nv_add_u32 v51, v51, %[pv]\n"
        "v_mfma_f32_16x16x32_bf16 v[160:163], v[76:79], v[24:27], v[160:163]\nds_read_b128 v[24:27], %[xb] offset:16384\nv_bfe_u32 %[pv], %[wa], 8, 2\ns_waitcnt lgkmcnt(4)\n"
        "v_mfma_f32_16x16x32_bf16 v[112:115], v[64:67], v[28:31], v[112:115]\nv_lshl_add_u32 v50, %[pv], 3, v50\nv_mfma_f32_16x16x32_bf16 v[128:131], v[68:71], v[28:31], v[128:131]\n"
        "v_mfma_f32_16x16x32_bf16 v[148:151], v[72:75], v[28:31], v[148:151]\nv_mfma_f32_16x16x32_bf16 v[164:167], v[76:79], v[28:31], v[164:167]\ns_waitcnt vmcnt(0) lgkmcnt(3)\n"
        "v_mfma_f32_16x16x32_bf16 v[100:103], v[80:83], v[16:19], v[100:103]\nds_read_b128 v[28:31], %[xb] offset:18432\nv_mfma_f32_16x16x32_bf16 v[116:119], v[84:87], v[16:19], v[116:119]\n"
        "v_mfma_f32_16x16x32_bf16 v[136:139], v[88:91], v[16:19], v[136:139]\nv_mfma_f32_16x16x32_bf16 v[152:155], v[92:95], v[16:19], v[152:155]\ns_waitcnt lgkmcnt(3)\n"
        "v_mfma_f32_16x16x32_bf16 v[104:107], v[80:83], v[20:23], v[104:107]\nv_mfma_f32_16x16x32_bf16 v[120:123], v[84:87], v[20:23], v[120:123]\n"
        "v_mfma_f32_16x16x32_bf16 v[140:143], v[88:91], v[20:23], v[140:143]\nv_mfma_f32_16x16x32_bf16 v[156:159], v[92:95], v[20:23], v[156:159]\nds_read_b32 %[pv], %[sy] offset:4\n"
        "ds_read_b64 v[32:33], v48 offset:0\ns_waitcnt lgkmcnt(4)\nv_mfma_f32_16x16x32_bf16 v[96:99], v[80:83], v[12:15], v[96:99]\nds_read_b64 v[36:37], v48 offset:640\n"
        "v_mfma_f32_16x16x32_bf16 v[132:135], v[88:91], v[12:15], v[132:135]\nds_read_b64 v[40:41], v48 offset:1280\nds_read_b64 v[44:45], v48 offset:1024\ns_waitcnt lgkmcnt(6)\n"
        "v_mfma_f32_16x16x32_bf16 v[108:111], v[80:83], v[24:27], v[108:111]\nds_read_b128 v[52:55], v49 offset:0\nv_mfma_f32_16x16x32_bf16 v[124:127], v[84:87], v[24:27], v[124:127]\n"
        "ds_read_b128 v[56:59], v49 offset:256\nds_read_b128 v[60:63], v49 offset:512\nv_mfma_f32_16x16x32_bf16 v[144:147], v[88:91], v[24:27], v[144:147]\nds_read_b128 v[64:67], v49 offset:768\n"
        "v_mfma_f32_16x16x32_bf16 v[160:163], v[92:95], v[24:27], v[160:163]\nds_read_b128 v[68:71], v49 offset:1024\ns_waitcnt lgkmcnt(10)\n"
        "v_mfma_f32_16x16x32_bf16 v[112:115], v[80:83], v[28:31], v[112:115]\nv_mfma_f32_16x16x32_bf16 v[128:131], v[84:87], v[28:31], v[128:131]\ns_setprio %[pfs]\n"
        "v_mfma_f32_16x16x32_bf16 v[148:151], v[88:91], v[28:31], v[148:151]\nv_mfma_f32_16x16x32_bf16 v[164:167], v[92:95], v[28:31], v[164:167]\ns_mov_b32 %[s1], 0x100000\n.LA0q%=:\n"
        "s_waitcnt lgkmcnt(0)\nv_readfirstlane_b32 %[s0], %[pv]\nv_add_u32 %[pv], 8, %[sy]\ns_cmp_ge_i32 %[s0], %[nst]\ns_cbranch_scc0 .LA0t%=\n.LA0u%=:\n"
        "v_mfma_f32_16x16x32_bf16 v[100:103], v[32:35], v[56:59], v[100:103]\nv_mfma_f32_16x16x32_bf16 v[104:107], v[32:35], v[60:63], v[104:107]\n"
        "v_mfma_f32_16x16x32_bf16 v[116:119], v[36:39], v[56:59], v[116:119]\nv_mfma_f32_16x16x32_bf16 v[120:123], v[36:39], v[60:63], v[120:123]\n"
        "v_mfma_f32_16x16x32_bf16 v[96:99], v[32:35], v[52:55], v[96:99]\nv_cvt_pk_bf16_f32 v100, v100, v101\nv_cvt_pk_bf16_f32 v101, v102, v103\nv_cvt_pk_bf16_f32 v102, v104, v105\n"
        "v_cvt_pk_bf16_f32 v103, v106, v107\nv_mfma_f32_16x16x32_bf16 v[108:111], v[32:35], v[64:67], v[108:111]\nv_permlane16_swap_b32 v100, v102\nv_permlane16_swap_b32 v101, v103\n"
        "ds_write_b128 v51, v[100:103] offset:7936\nv_mfma_f32_16x16x32_bf16 v[112:115], v[32:35], v[68:71], v[112:115]\nv_cvt_pk_bf16_f32 v116, v116, v117\nv_cvt_pk_bf16_f32 v117, v118, v119\n"
        "v_cvt_pk_bf16_f32 v118, v120, v121\nv_cvt_pk_bf16_f32 v119, v122, v123\nv_mfma_f32_16x16x32_bf16 v[124:127], v[36:39], v[64:67], v[124:127]\nv_permlane16_swap_b32 v116, v118\n"
        "v_permlane16_swap_b32 v117, v119\nds_write_b128 v51, v[116:119] offset:8096\nv_cvt_pk_bf16_f32 v96, v96, v97\nv_mfma_f32_16x16x32_bf16 v[128:131], v[36:39], v[68:71], v[128:131]\n"
        "v_cvt_pk_bf16_f32 v97, v98, v99\nds_write_b64 v50, v[96:97] offset:0\nv_mfma_f32_16x16x32_bf16 v[136:139], v[40:43], v[56:59], v[136:139]\nv_cvt_pk_bf16_f32 v108, v108, v109\n"
        "v_cvt_pk_bf16_f32 v109, v110, v111\nv_cvt_pk_bf16_f32 v110, v112, v113\nv_cvt_pk_bf16_f32 v111, v114, v115\nv_mfma_f32_16x16x32_bf16 v[140:143], v[40:43], v[60:63], v[140:143]\n"
        "v_permlane16_swap_b32 v108, v110\nv_permlane16_swap_b32 v109, v111\nds_write_b128 v51, v[108:111] offset:23808\nv_mfma_f32_16x16x32_bf16 v[152:155], v[44:47], v[56:59], v[152:155]\n"
        "v_cvt_pk_bf16_f32 v124, v124, v125\nv_cvt_pk_bf16_f32 v125, v126, v127\nv_cvt_pk_bf16_f32 v126, v128, v129\nv_cvt_pk_bf16_f32 v127, v130, v131\n"
        "v_mfma_f32_16x16x32_bf16 v[156:159], v[44:47], v[60:63], v[156:159]\nv_permlane16_swap_b32 v124, v126\nv_permlane16_swap_b32 v125, v127\nds_write_b128 v51, v[124:127] offset:23968\n"
        "ds_write_b32 %[pg], %[pv]\nv_mfma_f32_16x16x32_bf16 v[132:135], v[40:43], v[52:55], v[132:135]\nv_add_u32 %[sy], 12, %[sy]\nv_cvt_pk_bf16_f32 v136, v136, v137\n"
        "v_cvt_pk_bf16_f32 v137, v138, v139\nv_cvt_pk_bf16_f32 v138, v140, v141\nv_mfma_f32_16x16x32_bf16 v[144:147], v[40:43], v[64:67], v[144:147]\nv_cvt_pk_bf16_f32 v139, v142, v143\ns_nop 0\n"
        "v_permlane16_swap_b32 v136, v138\nv_permlane16_swap_b32 v137, v139\nv_mfma_f32_16x16x32_bf16 v[148:151], v[40:43], v[68:71], v[148:151]\nds_write_b128 v51, v[136:139] offset:8256\n"
        "v_cvt_pk_bf16_f32 v152, v152, v153\nv_cvt_pk_bf16_f32 v153, v154, v155\nv_cvt_pk_bf16_f32 v154, v156, v157\nv_mfma_f32_16x16x32_bf16 v[160:163], v[44:47], v[64:67], v[160:163]\n"
        "v_cvt_pk_bf16_f32 v155, v158, v159\ns_nop 0\nv_permlane16_swap_b32 v152, v154\nv_permlane16_swap_b32 v153, v155\nv_mfma_f32_16x16x32_bf16 v[164:167], v[44:47], v[68:71], v[164:167]\n"
        "ds_write_b128 v51, v[152:155] offset:8192\nv_cvt_pk_bf16_f32 v132, v132, v133\nv_cvt_pk_bf16_f32 v133, v134, v135\nds_write_b64 v50, v[132:133] offset:320\ns_nop 3\n"
        "v_cvt_pk_bf16_f32 v144, v144, v145\nv_cvt_pk_bf16_f32 v145, v146, v147\nv_cvt_pk_bf16_f32 v146, v148, v149\nv_cvt_pk_bf16_f32 v147, v150, v151\ns_nop 0\nv_permlane16_swap_b32 v144, v146\n"
        "v_permlane16_swap_b32 v145, v147\nds_write_b128 v51, v[144:147] offset:24128\nv_cvt_pk_bf16_f32 v160, v160, v161\nv_cvt_pk_bf16_f32 v161, v162, v163\nv_cvt_pk_bf16_f32 v162, v164, v165\n"
        "v_cvt_pk_bf16_f32 v163, v166, v167\ns_nop 0\nv_permlane16_swap_b32 v160, v162\nv_permlane16_swap_b32 v161, v163\nds_write_b128 v51, v[160:163] offset:24064\nds_write_b32 %[pg], %[sy]\n"
        "s_branch .LE%=\n.LA0t%=:\ns_sub_u32 %[s1], %[s1], 1\ns_cbranch_scc1 .LA0u%=\ns_sleep 1\nds_read_b32 %[pv], %[sy] offset:4\nds_read_b64 v[32:33], v48 offset:0\n"
        "ds_read_b64 v[36:37], v48 offset:640\nds_read_b64 v[40:41], v48 offset:1280\nds_read_b64 v[44:45], v48 offset:1024\nds_read_b128 v[52:55], v49 offset:0\nds_read_b128 v[56:59], v49 offset:256\n"
        "ds_read_b128 v[60:63], v49 offset:512\nds_read_b128 v[64:67], v49 offset:768\nds_read_b128 v[68:71], v49 offset:1024\ns_branch .LA0q%=\n.LB%=:\ns_cmp_eq_u32 %[w], 2\ns_cbranch_scc0 .LC%=\n"
        "s_mov_b32 %[s1], 0x100000\n.LB0w%=:\nds_read_b32 %[pv], %[sy] offset:%[ow]\ns_waitcnt lgkmcnt(0)\nv_readfirstlane_b32 %[s0], %[pv]\ns_nop 0\ns_cmp_ge_i32 %[s0], 1\ns_cbranch_scc1 .LB0h%=\n"
        "s_sleep 1\ns_sub_u32 %[s1], %[s1], 1\ns_cbranch_scc0 .LB0w%=\n.LB0h%=:\ns_mov_b32 %[so], 0\nbuffer_load_dwordx4 v[32:35], %[wa], %[rs], %[so] offen offset:0 sc1\n"
        "buffer_load_dwordx4 v[36:39], %[wa], %[rs], %[so] offen offset:1024 sc1\nbuffer_load_dwordx4 v[40:43], %[wa], %[rs], %[so] offen offset:2048 sc1\n"
        "buffer_load_dwordx4 v[44:47], %[wa], %[rs], %[so] offen offset:3072 sc1\ns_mov_b32 %[so], 15360\nbuffer_load_dwordx4 v[48:51], %[wa], %[rs], %[so] offen offset:0 sc1\n"
        "buffer_load_dwordx4 v[52:55], %[wa], %[rs], %[so] offen offset:1024 sc1\nbuffer_load_dwordx4 v[56:59], %[wa], %[rs], %[so] offen offset:2048 sc1\n"
        "buffer_load_dwordx4 v[60:63], %[wa], %[rs], %[so] offen offset:3072 sc1\ns_mov_b32 %[so], 30720\nbuffer_load_dwordx4 v[64:67], %[wa], %[rs], %[so] offen offset:0 sc1\n"
        "buffer_load_dwordx4 v[68:71], %[wa], %[rs], %[so] offen offset:1024 sc1\nbuffer_load_dwordx4 v[72:75], %[wa], %[rs], %[so] offen offset:2048 sc1\n"
        "buffer_load_dwordx4 v[76:79], %[wa], %[rs], %[so] offen offset:3072 sc1\ns_mov_b32 %[so], 46080\nbuffer_load_dwordx4 v[80:83], %[wa], %[rs], %[so] offen offset:0 sc1\n"
        "buffer_load_dwordx4 v[84:87], %[wa], %[rs], %[so] offen offset:1024 sc1\nbuffer_load_dwordx4 v[88:91], %[wa], %[rs], %[so] offen offset:2048 sc1\n"
        "buffer_load_dwordx4 v[92:95], %[wa], %[rs], %[so] offen offset:3072 sc1\ns_setprio %[pf]\ns_mov_b32 %[s1], 0x100000\n.LB0v%=:\nds_read_b32 %[pv], %[sy] offset:0\ns_waitcnt lgkmcnt(0)\n"
        "v_readfirstlane_b32 %[s0], %[pv]\ns_nop 0\ns_cmp_ge_i32 %[s0], 3\ns_cbranch_scc1 .LB0g%=\ns_sleep 1\ns_sub_u32 %[s1], %[s1], 1\ns_cbranch_scc0 .LB0v%=\n.LB0g%=:\n"
        "ds_read_b128 v[16:19], %[xa] offset:2048\nds_read_b128 v[20:23], %[xa] offset:4096\nds_read_b128 v[12:15], %[xa] offset:0\nds_read_b128 v[24:27], %[xa] offset:6144\n"
        "s_waitcnt vmcnt(12) lgkmcnt(3)\nv_mfma_f32_16x16x32_bf16 v[96:99], v[32:35], v[16:19], 0\nds_read_b128 v[28:31], %[xa] offset:8192\nv_mfma_f32_16x16x32_bf16 v[116:119], v[36:39], v[16:19], 0\n"
        "v_mfma_f32_16x16x32_bf16 v[136:139], v[40:43], v[16:19], 0\nv_mfma_f32_16x16x32_bf16 v[156:159], v[44:47], v[16:19], 0\nds_read_b128 v[16:19], %[xb] offset:2048\ns_waitcnt lgkmcnt(4)\n"
        "v_mfma_f32_16x16x32_bf16 v[100:103], v[32:35], v[20:23], 0\nv_mfma_f32_16x16x32_bf16 v[120:123], v[36:39], v[20:23], 0\nv_mfma_f32_16x16x32_bf16 v[140:143], v[40:43], v[20:23], 0\n"
        "v_mfma_f32_16x16x32_bf16 v[160:163], v[44:47], v[20:23], 0\nds_read_b128 v[20:23], %[xb] offset:4096\ns_waitcnt lgkmcnt(4)\nv_mfma_f32_16x16x32_bf16 v[112:115], v[36:39], v[12:15], 0\n"
        "v_mfma_f32_16x16x32_bf16 v[132:135], v[40:43], v[12:15], 0\nv_mfma_f32_16x16x32_bf16 v[152:155], v[44:47], v[12:15], 0\nds_read_b128 v[12:15], %[xb] offset:0\ns_waitcnt lgkmcnt(4)\n"
        "v_mfma_f32_16x16x32_bf16 v[104:107], v[32:35], v[24:27], 0\nv_mfma_f32_16x16x32_bf16 v[124:127], v[36:39], v[24:27], 0\nv_mfma_f32_16x16x32_bf16 v[144:147], v[40:43], v[24:27], 0\n"
        "v_mfma_f32_16x16x32_bf16 v[164:167], v[44:47], v[24:27], 0\nds_read_b128 v[24:27], %[xb] offset:6144\ns_waitcnt lgkmcnt(4)\nv_mfma_f32_16x16x32_bf16 v[108:111], v[32:35], v[28:31], 0\n"
        "v_mfma_f32_16x16x32_bf16 v[128:131], v[36:39], v[28:31], 0\nv_mfma_f32_16x16x32_bf16 v[148:151], v[40:43], v[28:31], 0\nv_mfma_f32_16x16x32_bf16 v[8:11], v[44:47], v[28:31], 0\n.LB0l%=:\n"
        "s_waitcnt vmcnt(8) lgkmcnt(3)\nv_mfma_f32_16x16x32_bf16 v[96:99], v[48:51], v[16:19], v[96:99]\ns_addk_i32 %[so], 15360\nds_read_b128 v[28:31], %[xb] offset:8192\n"
        "v_mfma_f32_16x16x32_bf16 v[116:119], v[52:55], v[16:19], v[116:119]\nv_mfma_f32_16x16x32_bf16 v[136:139], v[56:59], v[16:19], v[136:139]\n"
        "buffer_load_dwordx4 v[32:35], %[wa], %[rs], %[so] offen offset:0 sc1\nv_mfma_f32_16x16x32_bf16 v[156:159], v[60:63], v[16:19], v[156:159]\nds_read_b128 v[16:19], %[xa] offset:12288\n"
        "s_waitcnt lgkmcnt(4)\nv_mfma_f32_16x16x32_bf16 v[100:103], v[48:51], v[20:23], v[100:103]\nv_mfma_f32_16x16x32_bf16 v[120:123], v[52:55], v[20:23], v[120:123]\n"
        "buffer_load_dwordx4 v[36:39], %[wa], %[rs], %[so] offen offset:1024 sc1\nv_mfma_f32_16x16x32_bf16 v[140:143], v[56:59], v[20:23], v[140:143]\n"
        "v_mfma_f32_16x16x32_bf16 v[160:163], v[60:63], v[20:23], v[160:163]\nds_read_b128 v[20:23], %[xa] offset:14336\ns_waitcnt lgkmcnt(4)\n"
        "v_mfma_f32_16x16x32_bf16 v[112:115], v[52:55], v[12:15], v[112:115]\nv_mfma_f32_16x16x32_bf16 v[132:135], v[56:59], v[12:15], v[132:135]\n"
        "buffer_load_dwordx4 v[40:43], %[wa], %[rs], %[so] offen offset:2048 sc1\nv_mfma_f32_16x16x32_bf16 v[152:155], v[60:63], v[12:15], v[152:155]\nds_read_b128 v[12:15], %[xa] offset:10240\n"
        "s_waitcnt lgkmcnt(4)\nv_mfma_f32_16x16x32_bf16 v[104:107], v[48:51], v[24:27], v[104:107]\nv_mfma_f32_16x16x32_bf16 v[124:127], v[52:55], v[24:27], v[124:127]\n"
        "buffer_load_dwordx4 v[44:47], %[wa], %[rs], %[so] offen offset:3072 sc1\nv_mfma_f32_16x16x32_bf16 v[144:147], v[56:59], v[24:27], v[144:147]\n"
        "v_mfma_f32_16x16x32_bf16 v[164:167], v[60:63], v[24:27], v[164:167]\nds_read_b128 v[24:27], %[xa] offset:16384\ns_waitcnt lgkmcnt(4)\n"
        "v_mfma_f32_16x16x32_bf16 v[108:111], v[48:51], v[28:31], v[108:111]\nv_mfma_f32_16x16x32_bf16 v[128:131], v[52:55], v[28:31], v[128:131]\nv_add_u32 %[sy], 4, %[sy]\n"
        "v_mfma_f32_16x16x32_bf16 v[148:151], v[56:59], v[28:31], v[148:151]\nds_write_b32 %[pg], %[sy]\nv_mfma_f32_16x16x32_bf16 v[8:11], v[60:63], v[28:31], v[8:11]\ns_waitcnt vmcnt(8) lgkmcnt(4)\n"
        "v_mfma_f32_16x16x32_bf16 v[96:99], v[64:67], v[16:19], v[96:99]\ns_addk_i32 %[so], 15360\nds_read_b128 v[28:31], %[xa] offset:18432\n"
        "v_mfma_f32_16x16x32_bf16 v[116:119], v[68:71], v[16:19], v[116:119]\nv_mfma_f32_16x16x32_bf16 v[136:139], v[72:75], v[16:19], v[136:139]\n"
        "buffer_load_dwordx4 v[48:51], %[wa], %[rs], %[so] offen offset:0 sc1\nv_mfma_f32_16x16x32_bf16 v[156:159], v[76:79], v[16:19], v[156:159]\nds_read_b128 v[16:19], %[xb] offset:12288\n"
        "s_waitcnt lgkmcnt(5)\nv_mfma_f32_16x16x32_bf16 v[100:103], v[64:67], v[20:23], v[100:103]\nv_mfma_f32_16x16x32_bf16 v[120:123], v[68:71], v[20:23], v[120:123]\n"
        "buffer_load_dwordx4 v[52:55], %[wa], %[rs], %[so] offen offset:1024 sc1\nv_mfma_f32_16x16x32_bf16 v[140:143], v[72:75], v[20:23], v[140:143]\n"
        "v_mfma_f32_16x16x32_bf16 v[160:163], v[76:79], v[20:23], v[160:163]\nds_read_b128 v[20:23], %[xb] offset:14336\ns_waitcnt lgkmcnt(5)\n"
        "v_mfma_f32_16x16x32_bf16 v[112:115], v[68:71], v[12:15], v[112:115]\nv_mfma_f32_16x16x32_bf16 v[132:135], v[72:75], v[12:15], v[132:135]\n"
        "buffer_load_dwordx4 v[56:59], %[wa], %[rs], %[so] offen offset:2048 sc1\nv_mfma_f32_16x16x32_bf16 v[152:155], v[76:79], v[12:15], v[152:155]\nds_read_b128 v[12:15], %[xb] offset:10240\n"
        "s_waitcnt lgkmcnt(5)\nv_mfma_f32_16x16x32_bf16 v[104:107], v[64:67], v[24:27], v[104:107]\nv_mfma_f32_16x16x32_bf16 v[124:127], v[68:71], v[24:27], v[124:127]\n"
        "buffer_load_dwordx4 v[60:63], %[wa], %[rs], %[so] offen offset:3072 sc1\nv_mfma_f32_16x16x32_bf16 v[144:147], v[72:75], v[24:27], v[144:147]\n"
        "v_mfma_f32_16x16x32_bf16 v[164:167], v[76:79], v[24:27], v[164:167]\nds_read_b128 v[24:27], %[xb] offset:16384\ns_waitcnt lgkmcnt(4)\n"
        "v_mfma_f32_16x16x32_bf16 v[108:111], v[64:67], v[28:31], v[108:111]\nv_mfma_f32_16x16x32_bf16 v[128:131], v[68:71], v[28:31], v[128:131]\n"
        "v_mfma_f32_16x16x32_bf16 v[148:151], v[72:75], v[28:31], v[148:151]\nv_mfma_f32_16x16x32_bf16 v[8:11], v[76:79], v[28:31], v[8:11]\ns_waitcnt vmcnt(8) lgkmcnt(3)\n"
        "v_mfma_f32_16x16x32_bf16 v[96:99], v[80:83], v[16:19], v[96:99]\ns_addk_i32 %[so], 15360\nds_read_b128 v[28:31], %[xb] offset:18432\n"
        "v_mfma_f32_16x16x32_bf16 v[116:119], v[84:87], v[16:19], v[116:119]\nv_mfma_f32_16x16x32_bf16 v[136:139], v[88:91], v[16:19], v[136:139]\n"
        "buffer_load_dwordx4 v[64:67], %[wa], %[rs], %[so] offen offset:0 sc1\nv_mfma_f32_16x16x32_bf16 v[156:159], v[92:95], v[16:19], v[156:159]\nds_read_b128 v[16:19], %[xa] offset:22528\n"
        "s_waitcnt lgkmcnt(4)\nv_mfma_f32_16x16x32_bf16 v[100:103], v[80:83], v[20:23], v[100:103]\nv_mfma_f32_16x16x32_bf16 v[120:123], v[84:87], v[20:23], v[120:123]\n"
        "buffer_load_dwordx4 v[68:71], %[wa], %[rs], %[so] offen offset:1024 sc1\nv_mfma_f32_16x16x32_bf16 v[140:143], v[88:91], v[20:23], v[140:143]\n"
        "v_mfma_f32_16x16x32_bf16 v[160:163], v[92:95], v[20:23], v[160:163]\nds_read_b128 v[20:23], %[xa] offset:24576\ns_waitcnt lgkmcnt(4)\n"
        "v_mfma_f32_16x16x32_bf16 v[112:115], v[84:87], v[12:15], v[112:115]\nv_mfma_f32_16x16x32_bf16 v[132:135], v[88:91], v[12:15], v[132:135]\n"
        "buffer_load_dwordx4 v[72:75], %[wa], %[rs], %[so] offen offset:2048 sc1\nv_mfma_f32_16x16x32_bf16 v[152:155], v[92:95], v[12:15], v[152:155]\nds_read_b128 v[12:15], %[xa] offset:20480\n"
        "s_waitcnt lgkmcnt(4)\nv_mfma_f32_16x16x32_bf16 v[104:107], v[80:83], v[24:27], v[104:107]\nv_mfma_f32_16x16x32_bf16 v[124:127], v[84:87], v[24:27], v[124:127]\n"
        "buffer_load_dwordx4 v[76:79], %[wa], %[rs], %[so] offen offset:3072 sc1\nv_mfma_f32_16x16x32_bf16 v[144:147], v[88:91], v[24:27], v[144:147]\n"
        "v_mfma_f32_16x16x32_bf16 v[164:167], v[92:95], v[24:27], v[164:167]\nds_read_b128 v[24:27], %[xa] offset:26624\ns_waitcnt lgkmcnt(4)\n"
        "v_mfma_f32_16x16x32_bf16 v[108:111], v[80:83], v[28:31], v[108:111]\nv_mfma_f32_16x16x32_bf16 v[128:131], v[84:87], v[28:31], v[128:131]\nv_add_u32 %[sy], 4, %[sy]\n"
        "v_mfma_f32_16x16x32_bf16 v[148:151], v[88:91], v[28:31], v[148:151]\nds_write_b32 %[pg], %[sy]\nv_mfma_f32_16x16x32_bf16 v[8:11], v[92:95], v[28:31], v[8:11]\ns_waitcnt vmcnt(8) lgkmcnt(4)\n"
        "v_mfma_f32_16x16x32_bf16 v[96:99], v[32:35], v[16:19], v[96:99]\ns_addk_i32 %[so], 15360\nds_read_b128 v[28:31], %[xa] offset:28672\n"
        "v_mfma_f32_16x16x32_bf16 v[116:119], v[36:39], v[16:19], v[116:119]\nv_mfma_f32_16x16x32_bf16 v[136:139], v[40:43], v[16:19], v[136:139]\n"
        "buffer_load_dwordx4 v[80:83], %[wa], %[rs], %[so] offen offset:0 sc1\nv_mfma_f32_16x16x32_bf16 v[156:159], v[44:47], v[16:19], v[156:159]\nds_read_b128 v[16:19], %[xb] offset:22528\n"
        "s_waitcnt lgkmcnt(5)\nv_mfma_f32_16x16x32_bf16 v[100:103], v[32:35], v[20:23], v[100:103]\nv_mfma_f32_16x16x32_bf16 v[120:123], v[36:39], v[20:23], v[120:123]\n"
        "buffer_load_dwordx4 v[84:87], %[wa], %[rs], %[so] offen offset:1024 sc1\nv_mfma_f32_16x16x32_bf16 v[140:143], v[40:43], v[20:23], v[140:143]\n"
        "v_mfma_f32_16x16x32_bf16 v[160:163], v[44:47], v[20:23], v[160:163]\nds_read_b128 v[20:23], %[xb] offset:24576\ns_waitcnt lgkmcnt(5)\n"
        "v_mfma_f32_16x16x32_bf16 v[112:115], v[36:39], v[12:15], v[112:115]\nv_mfma_f32_16x16x32_bf16 v[132:135], v[40:43], v[12:15], v[132:135]\n"
        "buffer_load_dwordx4 v[88:91], %[wa], %[rs], %[so] offen offset:2048 sc1\nv_mfma_f32_16x16x32_bf16 v[152:155], v[44:47], v[12:15], v[152:155]\nds_read_b128 v[12:15], %[xb] offset:20480\n"
        "s_waitcnt lgkmcnt(5)\nv_mfma_f32_16x16x32_bf16 v[104:107], v[32:35], v[24:27], v[104:107]\nv_mfma_f32_16x16x32_bf16 v[124:127], v[36:39], v[24:27], v[124:127]\n"
        "buffer_load_dwordx4 v[92:95], %[wa], %[rs], %[so] offen offset:3072 sc1\nv_mfma_f32_16x16x32_bf16 v[144:147], v[40:43], v[24:27], v[144:147]\n"
        "v_mfma_f32_16x16x32_bf16 v[164:167], v[44:47], v[24:27], v[164:167]\nds_read_b128 v[24:27], %[xb] offset:26624\ns_waitcnt lgkmcnt(4)\n"
        "v_mfma_f32_16x16x32_bf16 v[108:111], v[32:35], v[28:31], v[108:111]\nv_mfma_f32_16x16x32_bf16 v[128:131], v[36:39], v[28:31], v[128:131]\n"
        "v_mfma_f32_16x16x32_bf16 v[148:151], v[40:43], v[28:31], v[148:151]\ns_cmp_lg_u32 %[so], 291840\nv_mfma_f32_16x16x32_bf16 v[8:11], v[44:47], v[28:31], v[8:11]\nv_add_u32 %[xa], 20480, %[xa]\n"
        "v_add_u32 %[xb], 20480, %[xb]\ns_cbranch_scc1 .LB0l%=\ns_waitcnt vmcnt(8) lgkmcnt(3)\nv_mfma_f32_16x16x32_bf16 v[96:99], v[48:51], v[16:19], v[96:99]\n"
        "ds_read_b128 v[28:31], %[xb] offset:8192\nv_mfma_f32_16x16x32_bf16 v[116:119], v[52:55], v[16:19], v[116:119]\nv_mfma_f32_16x16x32_bf16 v[136:139], v[56:59], v[16:19], v[136:139]\n"
        "v_mfma_f32_16x16x32_bf16 v[156:159], v[60:63], v[16:19], v[156:159]\nds_read_b128 v[16:19], %[xa] offset:12288\ns_waitcnt lgkmcnt(4)\n"
        "v_mfma_f32_16x16x32_bf16 v[100:103], v[48:51], v[20:23], v[100:103]\nv_mfma_f32_16x16x32_bf16 v[120:123], v[52:55], v[20:23], v[120:123]\n"
        "v_mfma_f32_16x16x32_bf16 v[140:143], v[56:59], v[20:23], v[140:143]\nv_mfma_f32_16x16x32_bf16 v[160:163], v[60:63], v[20:23], v[160:163]\nds_read_b128 v[20:23], %[xa] offset:14336\n"
        "s_waitcnt lgkmcnt(4)\nv_mfma_f32_16x16x32_bf16 v[112:115], v[52:55], v[12:15], v[112:115]\nv_mfma_f32_16x16x32_bf16 v[132:135], v[56:59], v[12:15], v[132:135]\n"
        "v_mfma_f32_16x16x32_bf16 v[152:155], v[60:63], v[12:15], v[152:155]\nds_read_b128 v[12:15], %[xa] offset:10240\ns_waitcnt lgkmcnt(4)\n"
        "v_mfma_f32_16x16x32_bf16 v[104:107], v[48:51], v[24:27], v[104:107]\nv_mfma_f32_16x16x32_bf16 v[124:127], v[52:55], v[24:27], v[124:127]\n"
        "v_mfma_f32_16x16x32_bf16 v[144:147], v[56:59], v[24:27], v[144:147]\nv_mfma_f32_16x16x32_bf16 v[164:167], v[60:63], v[24:27], v[164:167]\nds_read_b128 v[24:27], %[xa] offset:16384\n"
        "s_waitcnt lgkmcnt(4)\nv_mfma_f32_16x16x32_bf16 v[108:111], v[48:51], v[28:31], v[108:111]\nv_mfma_f32_16x16x32_bf16 v[128:131], v[52:55], v[28:31], v[128:131]\nv_add_u32 %[sy], 4, %[sy]\n"
        "v_mfma_f32_16x16x32_bf16 v[148:151], v[56:59], v[28:31], v[148:151]\nv_mfma_f32_16x16x32_bf16 v[8:11], v[60:63], v[28:31], v[8:11]\ns_waitcnt vmcnt(4) lgkmcnt(3)\n"
        "v_mfma_f32_16x16x32_bf16 v[96:99], v[64:67], v[16:19], v[96:99]\nds_read_b128 v[28:31], %[xa] offset:18432\nv_mfma_f32_16x16x32_bf16 v[116:119], v[68:71], v[16:19], v[116:119]\n"
        "v_and_b32 v49, 0xf0, %[wa]\nv_mfma_f32_16x16x32_bf16 v[136:139], v[72:75], v[16:19], v[136:139]\nv_lshrrev_b32 v48, 1, v49\n"
        "v_mfma_f32_16x16x32_bf16 v[156:159], v[76:79], v[16:19], v[156:159]\nds_read_b128 v[16:19], %[xb] offset:12288\nv_mad_u32_u24 v50, v49, 31, %[c2]\ns_waitcnt lgkmcnt(4)\n"
        "v_mfma_f32_16x16x32_bf16 v[100:103], v[64:67], v[20:23], v[100:103]\nv_mfma_f32_16x16x32_bf16 v[120:123], v[68:71], v[20:23], v[120:123]\nv_add_u32 v48, %[c0], v48\n"
        "v_mfma_f32_16x16x32_bf16 v[140:143], v[72:75], v[20:23], v[140:143]\nv_add_u32 v49, %[c1], v49\nv_mfma_f32_16x16x32_bf16 v[160:163], v[76:79], v[20:23], v[160:163]\n"
        "ds_read_b128 v[20:23], %[xb] offset:14336\nv_bfe_u32 %[pv], %[wa], 8, 1\ns_waitcnt lgkmcnt(4)\nv_mfma_f32_16x16x32_bf16 v[112:115], v[68:71], v[12:15], v[112:115]\n"
        "v_mfma_f32_16x16x32_bf16 v[132:135], v[72:75], v[12:15], v[132:135]\nv_mul_u32_u24 %[pv], 7936, %[pv]\nv_mfma_f32_16x16x32_bf16 v[152:155], v[76:79], v[12:15], v[152:155]\n"
        "ds_read_b128 v[12:15], %[xb] offset:10240\nv_bfe_u32 v51, %[wa], 9, 1\ns_waitcnt lgkmcnt(4)\nv_mfma_f32_16x16x32_bf16 v[104:107], v[64:67], v[24:27], v[104:107]\n"
        "v_lshl_add_u32 v51, v51, 4, v50\nv_mfma_f32_16x16x32_bf16 v[124:127], v[68:71], v[24:27], v[124:127]\nv_mfma_f32_16x16x32_bf16 v[144:147], v[72:75], v[24:27], v[144:147]\n"
        "v_add_u32 v51, v51, %[pv]\nv_mfma_f32_16x16x32_bf16 v[164:167], v[76:79], v[24:27], v[164:167]\nds_read_b128 v[24:27], %[xb] offset:16384\nv_bfe_u32 %[pv], %[wa], 8, 2\ns_waitcnt lgkmcnt(4)\n"
        "v_mfma_f32_16x16x32_bf16 v[108:111], v[64:67], v[28:31], v[108:111]\nv_lshl_add_u32 v50, %[pv], 3, v50\nv_mfma_f32_16x16x32_bf16 v[128:131], v[68:71], v[28:31], v[128:131]\n"
        "v_mfma_f32_16x16x32_bf16 v[148:151], v[72:75], v[28:31], v[148:151]\nv_mfma_f32_16x16x32_bf16 v[8:11], v[76:79], v[28:31], v[8:11]\ns_waitcnt vmcnt(0) lgkmcnt(3)\n"
        "v_mfma_f32_16x16x32_bf16 v[96:99], v[80:83], v[16:19], v[96:99]\nds_read_b128 v[28:31], %[xb] offset:18432\nv_mfma_f32_16x16x32_bf16 v[116:119], v[84:87], v[16:19], v[116:119]\n"
        "v_mfma_f32_16x16x32_bf16 v[136:139], v[88:91], v[16:19], v[136:139]\nv_mfma_f32_16x16x32_bf16 v[156:159], v[92:95], v[16:19], v[156:159]\ns_waitcnt lgkmcnt(3)\n"
        "v_mfma_f32_16x16x32_bf16 v[100:103], v[80:83], v[20:23], v[100:103]\nv_mfma_f32_16x16x32_bf16 v[120:123], v[84:87], v[20:23], v[120:123]\n"
        "v_mfma_f32_16x16x32_bf16 v[140:143], v[88:91], v[20:23], v[140:143]\nv_mfma_f32_16x16x32_bf16 v[160:163], v[92:95], v[20:23], v[160:163]\ns_waitcnt lgkmcnt(2)\n"
        "v_mfma_f32_16x16x32_bf16 v[112:115], v[84:87], v[12:15], v[112:115]\nds_read_b32 %[pv], %[sy] offset:4\nds_read_b64 v[32:33], v48 offset:896\n"
        "v_mfma_f32_16x16x32_bf16 v[132:135], v[88:91], v[12:15], v[132:135]\nds_read_b64 v[36:37], v48 offset:512\nv_mfma_f32_16x16x32_bf16 v[152:155], v[92:95], v[12:15], v[152:155]\n"
        "ds_read_b64 v[40:41], v48 offset:1664\nds_read_b64 v[44:45], v48 offset:1792\ns_waitcnt lgkmcnt(6)\nv_mfma_f32_16x16x32_bf16 v[104:107], v[80:83], v[24:27], v[104:107]\n"
        "ds_read_b128 v[52:55], v49 offset:0\nv_mfma_f32_16x16x32_bf16 v[124:127], v[84:87], v[24:27], v[124:127]\nds_read_b128 v[56:59], v49 offset:256\nds_read_b128 v[60:63], v49 offset:512\n"
        "v_mfma_f32_16x16x32_bf16 v[144:147], v[88:91], v[24:27], v[144:147]\nds_read_b128 v[64:67], v49 offset:768\nv_mfma_f32_16x16x32_bf16 v[164:167], v[92:95], v[24:27], v[164:167]\n"
        "ds_read_b128 v[68:71], v49 offset:1024\ns_waitcnt lgkmcnt(10)\nv_mfma_f32_16x16x32_bf16 v[108:111], v[80:83], v[28:31], v[108:111]\n"
        "v_mfma_f32_16x16x32_bf16 v[128:131], v[84:87], v[28:31], v[128:131]\ns_setprio %[pfs]\nv_mfma_f32_16x16x32_bf16 v[148:151], v[88:91], v[28:31], v[148:151]\n"
        "v_mfma_f32_16x16x32_bf16 v[8:11], v[92:95], v[28:31], v[8:11]\ns_mov_b32 %[s1], 0x100000\n.LB0q%=:\ns_waitcnt lgkmcnt(0)\nv_readfirstlane_b32 %[s0], %[pv]\nv_add_u32 %[pv], 8, %[sy]\n"
        "s_cmp_ge_i32 %[s0], %[nst]\ns_cbranch_scc0 .LB0t%=\n.LB0u%=:\nv_mfma_f32_16x16x32_bf16 v[96:99], v[32:35], v[56:59], v[96:99]\n"
        "v_mfma_f32_16x16x32_bf16 v[100:103], v[32:35], v[60:63], v[100:103]\nv_mfma_f32_16x16x32_bf16 v[116:119], v[36:39], v[56:59], v[116:119]\n"
        "v_mfma_f32_16x16x32_bf16 v[120:123], v[36:39], v[60:63], v[120:123]\nv_mfma_f32_16x16x32_bf16 v[112:115], v[36:39], v[52:55], v[112:115]\nv_cvt_pk_bf16_f32 v96, v96, v97\n"
        "v_cvt_pk_bf16_f32 v97, v98, v99\nv_cvt_pk_bf16_f32 v98, v100, v101\nv_cvt_pk_bf16_f32 v99, v102, v103\nv_mfma_f32_16x16x32_bf16 v[104:107], v[32:35], v[64:67], v[104:107]\n"
        "v_permlane16_swap_b32 v96, v98\nv_permlane16_swap_b32 v97, v99\nds_write_b128 v51, v[96:99] offset:8160\nv_mfma_f32_16x16x32_bf16 v[108:111], v[32:35], v[68:71], v[108:111]\n"
        "v_cvt_pk_bf16_f32 v116, v116, v117\nv_cvt_pk_bf16_f32 v117, v118, v119\nv_cvt_pk_bf16_f32 v118, v120, v121\nv_cvt_pk_bf16_f32 v119, v122, v123\n"
        "v_mfma_f32_16x16x32_bf16 v[124:127], v[36:39], v[64:67], v[124:127]\nv_permlane16_swap_b32 v116, v118\nv_permlane16_swap_b32 v117, v119\nds_write_b128 v51, v[116:119] offset:8064\n"
        "v_cvt_pk_bf16_f32 v112, v112, v113\nv_mfma_f32_16x16x32_bf16 v[128:131], v[36:39], v[68:71], v[128:131]\nv_cvt_pk_bf16_f32 v113, v114, v115\nds_write_b64 v50, v[112:113] offset:128\n"
        "v_mfma_f32_16x16x32_bf16 v[136:139], v[40:43], v[56:59], v[136:139]\nv_cvt_pk_bf16_f32 v104, v104, v105\nv_cvt_pk_bf16_f32 v105, v106, v107\nv_cvt_pk_bf16_f32 v106, v108, v109\n"
        "v_cvt_pk_bf16_f32 v107, v110, v111\nv_mfma_f32_16x16x32_bf16 v[140:143], v[40:43], v[60:63], v[140:143]\nv_permlane16_swap_b32 v104, v106\nv_permlane16_swap_b32 v105, v107\n"
        "ds_write_b128 v51, v[104:107] offset:24032\nv_mfma_f32_16x16x32_bf16 v[156:159], v[44:47], v[56:59], v[156:159]\nv_cvt_pk_bf16_f32 v124, v124, v125\nv_cvt_pk_bf16_f32 v125, v126, v127\n"
        "v_cvt_pk_bf16_f32 v126, v128, v129\nv_cvt_pk_bf16_f32 v127, v130, v131\nv_mfma_f32_16x16x32_bf16 v[160:163], v[44:47], v[60:63], v[160:163]\nv_permlane16_swap_b32 v124, v126\n"
        "v_permlane16_swap_b32 v125, v127\nds_write_b128 v51, v[124:127] offset:23936\nds_write_b32 %[pg], %[pv]\nv_mfma_f32_16x16x32_bf16 v[132:135], v[40:43], v[52:55], v[132:135]\n"
        "v_add_u32 %[sy], 12, %[sy]\nv_cvt_pk_bf16_f32 v136, v136, v137\nv_cvt_pk_bf16_f32 v137, v138, v139\nv_cvt_pk_bf16_f32 v138, v140, v141\n"
        "v_mfma_f32_16x16x32_bf16 v[152:155], v[44:47], v[52:55], v[152:155]\nv_cvt_pk_bf16_f32 v139, v142, v143\ns_nop 0\nv_permlane16_swap_b32 v136, v138\nv_permlane16_swap_b32 v137, v139\n"
        "v_mfma_f32_16x16x32_bf16 v[144:147], v[40:43], v[64:67], v[144:147]\nds_write_b128 v51, v[136:139] offset:8352\nv_cvt_pk_bf16_f32 v156, v156, v157\nv_cvt_pk_bf16_f32 v157, v158, v159\n"
        "v_cvt_pk_bf16_f32 v158, v160, v161\nv_mfma_f32_16x16x32_bf16 v[148:151], v[40:43], v[68:71], v[148:151]\nv_cvt_pk_bf16_f32 v159, v162, v163\ns_nop 0\nv_permlane16_swap_b32 v156, v158\n"
        "v_permlane16_swap_b32 v157, v159\nv_mfma_f32_16x16x32_bf16 v[164:167], v[44:47], v[64:67], v[164:167]\nds_write_b128 v51, v[156:159] offset:8384\nv_cvt_pk_bf16_f32 v132, v132, v133\n"
        "v_cvt_pk_bf16_f32 v133, v134, v135\nds_write_b64 v50, v[132:133] offset:416\nv_mfma_f32_16x16x32_bf16 v[8:11], v[44:47], v[68:71], v[8:11]\nv_cvt_pk_bf16_f32 v152, v152, v153\n"
        "v_cvt_pk_bf16_f32 v153, v154, v155\nds_write_b64 v50, v[152:153] offset:448\ns_nop 3\nv_cvt_pk_bf16_f32 v144, v144, v145\nv_cvt_pk_bf16_f32 v145, v146, v147\n"
        "v_cvt_pk_bf16_f32 v146, v148, v149\nv_cvt_pk_bf16_f32 v147, v150, v151\ns_nop 0\nv_permlane16_swap_b32 v144, v146\nv_permlane16_swap_b32 v145, v147\n"
        "ds_write_b128 v51, v[144:147] offset:24224\nv_cvt_pk_bf16_f32 v164, v164, v165\nv_cvt_pk_bf16_f32 v165, v166, v167\nv_cvt_pk_bf16_f32 v166, v8, v9\nv_cvt_pk_bf16_f32 v167, v10, v11\ns_nop 0\n"
        "v_permlane16_swap_b32 v164, v166\nv_permlane16_swap_b32 v165, v167\nds_write_b128 v51, v[164:167] offset:24256\nds_write_b32 %[pg], %[sy]\ns_branch .LE%=\n.LB0t%=:\ns_sub_u32 %[s1], %[s1], 1\n"
        "s_cbranch_scc1 .LB0u%=\ns_sleep 1\nds_read_b32 %[pv], %[sy] offset:4\nds_read_b64 v[32:33], v48 offset:896\nds_read_b64 v[36:37], v48 offset:512\nds_read_b64 v[40:41], v48 offset:1664\n"
        "ds_read_b64 v[44:45], v48 offset:1792\nds_read_b128 v[52:55], v49 offset:0\nds_read_b128 v[56:59], v49 offset:256\nds_read_b128 v[60:63], v49 offset:512\n"
        "ds_read_b128 v[64:67], v49 offset:768\nds_read_b128 v[68:71], v49 offset:1024\ns_branch .LB0q%=\n.LC%=:\ns_mov_b32 %[s1], 0x100000\n.LC0w%=:\nds_read_b32 %[pv], %[sy] offset:%[ow]\n"
        "s_waitcnt lgkmcnt(0)\nv_readfirstlane_b32 %[s0], %[pv]\ns_nop 0\ns_cmp_ge_i32 %[s0], 1\ns_cbranch_scc1 .LC0h%=\ns_sleep 1\ns_sub_u32 %[s1], %[s1], 1\ns_cbranch_scc0 .LC0w%=\n.LC0h%=:\n"
        "s_mov_b32 %[so], 0\nbuffer_load_dwordx4 v[32:35], %[wa], %[rs], %[so] offen offset:0 sc1\nbuffer_load_dwordx4 v[36:39], %[wa], %[rs], %[so] offen offset:1024 sc1\n"
        "buffer_load_dwordx4 v[40:43], %[wa], %[rs], %[so] offen offset:2048 sc1\ns_mov_b32 %[so], 15360\nbuffer_load_dwordx4 v[48:51], %[wa], %[rs], %[so] offen offset:0 sc1\n"
        "buffer_load_dwordx4 v[52:55], %[wa], %[rs], %[so] offen offset:1024 sc1\nbuffer_load_dwordx4 v[56:59], %[wa], %[rs], %[so] offen offset:2048 sc1\ns_mov_b32 %[so], 30720\n"
        "buffer_load_dwordx4 v[64:67], %[wa], %[rs], %[so] offen offset:0 sc1\nbuffer_load_dwordx4 v[68:71], %[wa], %[rs], %[so] offen offset:1024 sc1\n"
        "buffer_load_dwordx4 v[72:75], %[wa], %[rs], %[so] offen offset:2048 sc1\ns_mov_b32 %[so], 46080\nbuffer_load_dwordx4 v[80:83], %[wa], %[rs], %[so] offen offset:0 sc1\n"
        "buffer_load_dwordx4 v[84:87], %[wa], %[rs], %[so] offen offset:1024 sc1\nbuffer_load_dwordx4 v[88:91], %[wa], %[rs], %[so] offen offset:2048 sc1\ns_setprio %[pf]\ns_mov_b32 %[s1], 0x100000\n"
        ".LC0v%=:\nds_read_b32 %[pv], %[sy] offset:0\ns_waitcnt lgkmcnt(0)\nv_readfirstlane_b32 %[s0], %[pv]\ns_nop 0\ns_cmp_ge_i32 %[s0], 3\ns_cbranch_scc1 .LC0g%=\ns_sleep 1\n"
        "s_sub_u32 %[s1], %[s1], 1\ns_cbranch_scc0 .LC0v%=\n.LC0g%=:\nds_read_b128 v[16:19], %[xa] offset:2048\nds_read_b128 v[20:23], %[xa] offset:4096\nds_read_b128 v[12:15], %[xa] offset:0\n"
        "ds_read_b128 v[24:27], %[xa] offset:6144\ns_waitcnt vmcnt(9) lgkmcnt(3)\nv_mfma_f32_16x16x32_bf16 v[100:103], v[32:35], v[16:19], 0\nds_read_b128 v[28:31], %[xa] offset:8192\n"
        "v_mfma_f32_16x16x32_bf16 v[120:123], v[36:39], v[16:19], 0\nv_mfma_f32_16x16x32_bf16 v[140:143], v[40:43], v[16:19], 0\nds_read_b128 v[16:19], %[xb] offset:2048\ns_waitcnt lgkmcnt(4)\n"
        "v_mfma_f32_16x16x32_bf16 v[104:107], v[32:35], v[20:23], 0\nv_mfma_f32_16x16x32_bf16 v[124:127], v[36:39], v[20:23], 0\nv_mfma_f32_16x16x32_bf16 v[144:147], v[40:43], v[20:23], 0\n"
        "ds_read_b128 v[20:23], %[xb] offset:4096\ns_waitcnt lgkmcnt(4)\nv_mfma_f32_16x16x32_bf16 v[96:99], v[32:35], v[12:15], 0\nv_mfma_f32_16x16x32_bf16 v[116:119], v[36:39], v[12:15], 0\n"
        "v_mfma_f32_16x16x32_bf16 v[136:139], v[40:43], v[12:15], 0\nds_read_b128 v[12:15], %[xb] offset:0\ns_waitcnt lgkmcnt(4)\nv_mfma_f32_16x16x32_bf16 v[108:111], v[32:35], v[24:27], 0\n"
        "v_mfma_f32_16x16x32_bf16 v[128:131], v[36:39], v[24:27], 0\nv_mfma_f32_16x16x32_bf16 v[148:151], v[40:43], v[24:27], 0\nds_read_b128 v[24:27], %[xb] offset:6144\ns_waitcnt lgkmcnt(4)\n"
        "v_mfma_f32_16x16x32_bf16 v[112:115], v[32:35], v[28:31], 0\nv_mfma_f32_16x16x32_bf16 v[132:135], v[36:39], v[28:31], 0\nv_mfma_f32_16x16x32_bf16 v[152:155], v[40:43], v[28:31], 0\n.LC0l%=:\n"
        "s_waitcnt vmcnt(6) lgkmcnt(3)\nv_mfma_f32_16x16x32_bf16 v[100:103], v[48:51], v[16:19], v[100:103]\ns_addk_i32 %[so], 15360\nds_read_b128 v[28:31], %[xb] offset:8192\n"
        "v_mfma_f32_16x16x32_bf16 v[120:123], v[52:55], v[16:19], v[120:123]\nv_mfma_f32_16x16x32_bf16 v[140:143], v[56:59], v[16:19], v[140:143]\nds_read_b128 v[16:19], %[xa] offset:12288\n"
        "s_waitcnt lgkmcnt(4)\nv_mfma_f32_16x16x32_bf16 v[104:107], v[48:51], v[20:23], v[104:107]\nbuffer_load_dwordx4 v[32:35], %[wa], %[rs], %[so] offen offset:0 sc1\n"
        "v_mfma_f32_16x16x32_bf16 v[124:127], v[52:55], v[20:23], v[124:127]\nv_mfma_f32_16x16x32_bf16 v[144:147], v[56:59], v[20:23], v[144:147]\nds_read_b128 v[20:23], %[xa] offset:14336\n"
        "s_waitcnt lgkmcnt(4)\nv_mfma_f32_16x16x32_bf16 v[96:99], v[48:51], v[12:15], v[96:99]\nbuffer_load_dwordx4 v[36:39], %[wa], %[rs], %[so] offen offset:1024 sc1\n"
        "v_mfma_f32_16x16x32_bf16 v[116:119], v[52:55], v[12:15], v[116:119]\nv_mfma_f32_16x16x32_bf16 v[136:139], v[56:59], v[12:15], v[136:139]\nds_read_b128 v[12:15], %[xa] offset:10240\n"
        "s_waitcnt lgkmcnt(4)\nv_mfma_f32_16x16x32_bf16 v[108:111], v[48:51], v[24:27], v[108:111]\nbuffer_load_dwordx4 v[40:43], %[wa], %[rs], %[so] offen offset:2048 sc1\n"
        "v_mfma_f32_16x16x32_bf16 v[128:131], v[52:55], v[24:27], v[128:131]\nv_mfma_f32_16x16x32_bf16 v[148:151], v[56:59], v[24:27], v[148:151]\nds_read_b128 v[24:27], %[xa] offset:16384\n"
        "s_waitcnt lgkmcnt(4)\nv_mfma_f32_16x16x32_bf16 v[112:115], v[48:51], v[28:31], v[112:115]\nv_add_u32 %[sy], 4, %[sy]\nv_mfma_f32_16x16x32_bf16 v[132:135], v[52:55], v[28:31], v[132:135]\n"
        "ds_write_b32 %[pg], %[sy]\nv_mfma_f32_16x16x32_bf16 v[152:155], v[56:59], v[28:31], v[152:155]\ns_waitcnt vmcnt(6) lgkmcnt(4)\n"
        "v_mfma_f32_16x16x32_bf16 v[100:103], v[64:67], v[16:19], v[100:103]\ns_addk_i32 %[so], 15360\nds_read_b128 v[28:31], %[xa] offset:18432\n"
        "v_mfma_f32_16x16x32_bf16 v[120:123], v[68:71], v[16:19], v[120:123]\nv_mfma_f32_16x16x32_bf16 v[140:143], v[72:75], v[16:19], v[140:143]\nds_read_b128 v[16:19], %[xb] offset:12288\n"
        "s_waitcnt lgkmcnt(5)\nv_mfma_f32_16x16x32_bf16 v[104:107], v[64:67], v[20:23], v[104:107]\nbuffer_load_dwordx4 v[48:51], %[wa], %[rs], %[so] offen offset:0 sc1\n"
        "v_mfma_f32_16x16x32_bf16 v[124:127], v[68:71], v[20:23], v[124:127]\nv_mfma_f32_16x16x32_bf16 v[144:147], v[72:75], v[20:23], v[144:147]\nds_read_b128 v[20:23], %[xb] offset:14336\n"
        "s_waitcnt lgkmcnt(5)\nv_mfma_f32_16x16x32_bf16 v[96:99], v[64:67], v[12:15], v[96:99]\nbuffer_load_dwordx4 v[52:55], %[wa], %[rs], %[so] offen offset:1024 sc1\n"
        "v_mfma_f32_16x16x32_bf16 v[116:119], v[68:71], v[12:15], v[116:119]\nv_mfma_f32_16x16x32_bf16 v[136:139], v[72:75], v[12:15], v[136:139]\nds_read_b128 v[12:15], %[xb] offset:10240\n"
        "s_waitcnt lgkmcnt(5)\nv_mfma_f32_16x16x32_bf16 v[108:111], v[64:67], v[24:27], v[108:111]\nbuffer_load_dwordx4 v[56:59], %[wa], %[rs], %[so] offen offset:2048 sc1\n"
        "v_mfma_f32_16x16x32_bf16 v[128:131], v[68:71], v[24:27], v[128:131]\nv_mfma_f32_16x16x32_bf16 v[148:151], v[72:75], v[24:27], v[148:151]\nds_read_b128 v[24:27], %[xb] offset:16384\n"
        "s_waitcnt lgkmcnt(4)\nv_mfma_f32_16x16x32_bf16 v[112:115], v[64:67], v[28:31], v[112:115]\nv_mfma_f32_16x16x32_bf16 v[132:135], v[68:71], v[28:31], v[132:135]\n"
        "v_mfma_f32_16x16x32_bf16 v[152:155], v[72:75], v[28:31], v[152:155]\ns_waitcnt vmcnt(6) lgkmcnt(3)\nv_mfma_f32_16x16x32_bf16 v[100:103], v[80:83], v[16:19], v[100:103]\n"
        "s_addk_i32 %[so], 15360\nds_read_b128 v[28:31], %[xb] offset:18432\nv_mfma_f32_16x16x32_bf16 v[120:123], v[84:87], v[16:19], v[120:123]\n"
        "v_mfma_f32_16x16x32_bf16 v[140:143], v[88:91], v[16:19], v[140:143]\nds_read_b128 v[16:19], %[xa] offset:22528\ns_waitcnt lgkmcnt(4)\n"
        "v_mfma_f32_16x16x32_bf16 v[104:107], v[80:83], v[20:23], v[104:107]\nbuffer_load_dwordx4 v[64:67], %[wa], %[rs], %[so] offen offset:0 sc1\n"
        "v_mfma_f32_16x16x32_bf16 v[124:127], v[84:87], v[20:23], v[124:127]\nv_mfma_f32_16x16x32_bf16 v[144:147], v[88:91], v[20:23], v[144:147]\nds_read_b128 v[20:23], %[xa] offset:24576\n"
        "s_waitcnt lgkmcnt(4)\nv_mfma_f32_16x16x32_bf16 v[96:99], v[80:83], v[12:15], v[96:99]\nbuffer_load_dwordx4 v[68:71], %[wa], %[rs], %[so] offen offset:1024 sc1\n"
        "v_mfma_f32_16x16x32_bf16 v[116:119], v[84:87], v[12:15], v[116:119]\nv_mfma_f32_16x16x32_bf16 v[136:139], v[88:91], v[12:15], v[136:139]\nds_read_b128 v[12:15], %[xa] offset:20480\n"
        "s_waitcnt lgkmcnt(4)\nv_mfma_f32_16x16x32_bf16 v[108:111], v[80:83], v[24:27], v[108:111]\nbuffer_load_dwordx4 v[72:75], %[wa], %[rs], %[so] offen offset:2048 sc1\n"
        "v_mfma_f32_16x16x32_bf16 v[128:131], v[84:87], v[24:27], v[128:131]\nv_mfma_f32_16x16x32_bf16 v[148:151], v[88:91], v[24:27], v[148:151]\nds_read_b128 v[24:27], %[xa] offset:26624\n"
        "s_waitcnt lgkmcnt(4)\nv_mfma_f32_16x16x32_bf16 v[112:115], v[80:83], v[28:31], v[112:115]\nv_add_u32 %[sy], 4, %[sy]\nv_mfma_f32_16x16x32_bf16 v[132:135], v[84:87], v[28:31], v[132:135]\n"
        "ds_write_b32 %[pg], %[sy]\nv_mfma_f32_16x16x32_bf16 v[152:155], v[88:91], v[28:31], v[152:155]\ns_waitcnt vmcnt(6) lgkmcnt(4)\n"
        "v_mfma_f32_16x16x32_bf16 v[100:103], v[32:35], v[16:19], v[100:103]\ns_addk_i32 %[so], 15360\nds_read_b128 v[28:31], %[xa] offset:28672\n"
        "v_mfma_f32_16x16x32_bf16 v[120:123], v[36:39], v[16:19], v[120:123]\nv_mfma_f32_16x16x32_bf16 v[140:143], v[40:43], v[16:19], v[140:143]\nds_read_b128 v[16:19], %[xb] offset:22528\n"
        "s_waitcnt lgkmcnt(5)\nv_mfma_f32_16x16x32_bf16 v[104:107], v[32:35], v[20:23], v[104:107]\nbuffer_load_dwordx4 v[80:83], %[wa], %[rs], %[so] offen offset:0 sc1\n"
        "v_mfma_f32_16x16x32_bf16 v[124:127], v[36:39], v[20:23], v[124:127]\nv_mfma_f32_16x16x32_bf16 v[144:147], v[40:43], v[20:23], v[144:147]\nds_read_b128 v[20:23], %[xb] offset:24576\n"
        "s_waitcnt lgkmcnt(5)\nv_mfma_f32_16x16x32_bf16 v[96:99], v[32:35], v[12:15], v[96:99]\nbuffer_load_dwordx4 v[84:87], %[wa], %[rs], %[so] offen offset:1024 sc1\n"
        "v_mfma_f32_16x16x32_bf16 v[116:119], v[36:39], v[12:15], v[116:119]\nv_mfma_f32_16x16x32_bf16 v[136:139], v[40:43], v[12:15], v[136:139]\nds_read_b128 v[12:15], %[xb] offset:20480\n"
        "s_waitcnt lgkmcnt(5)\nv_mfma_f32_16x16x32_bf16 v[108:111], v[32:35], v[24:27], v[108:111]\nbuffer_load_dwordx4 v[88:91], %[wa], %[rs], %[so] offen offset:2048 sc1\n"
        "v_mfma_f32_16x16x32_bf16 v[128:131], v[36:39], v[24:27], v[128:131]\nv_mfma_f32_16x16x32_bf16 v[148:151], v[40:43], v[24:27], v[148:151]\nds_read_b128 v[24:27], %[xb] offset:26624\n"
        "s_waitcnt lgkmcnt(4)\nv_mfma_f32_16x16x32_bf16 v[112:115], v[32:35], v[28:31], v[112:115]\nv_mfma_f32_16x16x32_bf16 v[132:135], v[36:39], v[28:31], v[132:135]\ns_cmp_lg_u32 %[so], 291840\n"
        "v_mfma_f32_16x16x32_bf16 v[152:155], v[40:43], v[28:31], v[152:155]\nv_add_u32 %[xa], 20480, %[xa]\nv_add_u32 %[xb], 20480, %[xb]\ns_cbranch_scc1 .LC0l%=\ns_waitcnt vmcnt(6) lgkmcnt(3)\n"
        "v_mfma_f32_16x16x32_bf16 v[100:103], v[48:51], v[16:19], v[100:103]\nds_read_b128 v[28:31], %[xb] offset:8192\nv_mfma_f32_16x16x32_bf16 v[120:123], v[52:55], v[16:19], v[120:123]\n"
        "v_mfma_f32_16x16x32_bf16 v[140:143], v[56:59], v[16:19], v[140:143]\nds_read_b128 v[16:19], %[xa] offset:12288\ns_waitcnt lgkmcnt(4)\n"
        "v_mfma_f32_16x16x32_bf16 v[104:107], v[48:51], v[20:23], v[104:107]\nv_mfma_f32_16x16x32_bf16 v[124:127], v[52:55], v[20:23], v[124:127]\n"
        "v_mfma_f32_16x16x32_bf16 v[144:147], v[56:59], v[20:23], v[144:147]\nds_read_b128 v[20:23], %[xa] offset:14336\ns_waitcnt lgkmcnt(4)\n"
        "v_mfma_f32_16x16x32_bf16 v[96:99], v[48:51], v[12:15], v[96:99]\nv_mfma_f32_16x16x32_bf16 v[116:119], v[52:55], v[12:15], v[116:119]\n"
        "v_mfma_f32_16x16x32_bf16 v[136:139], v[56:59], v[12:15], v[136:139]\nds_read_b128 v[12:15], %[xa] offset:10240\ns_waitcnt lgkmcnt(4)\n"
        "v_mfma_f32_16x16x32_bf16 v[108:111], v[48:51], v[24:27], v[108:111]\nv_mfma_f32_16x16x32_bf16 v[128:131], v[52:55], v[24:27], v[128:131]\n"
        "v_mfma_f32_16x16x32_bf16 v[148:151], v[56:59], v[24:27], v[148:151]\nds_read_b128 v[24:27], %[xa] offset:16384\ns_waitcnt lgkmcnt(4)\n"
        "v_mfma_f32_16x16x32_bf16 v[112:115], v[48:51], v[28:31], v[112:115]\nv_add_u32 %[sy], 4, %[sy]\nv_mfma_f32_16x16x32_bf16 v[132:135], v[52:55], v[28:31], v[132:135]\n"
        "v_mfma_f32_16x16x32_bf16 v[152:155], v[56:59], v[28:31], v[152:155]\ns_waitcnt vmcnt(3) lgkmcnt(3)\nv_mfma_f32_16x16x32_bf16 v[100:103], v[64:67], v[16:19], v[100:103]\n"
        "ds_read_b128 v[28:31], %[xa] offset:18432\nv_mfma_f32_16x16x32_bf16 v[120:123], v[68:71], v[16:19], v[120:123]\nv_and_b32 v49, 0xf0, %[wa]\n"
        "v_mfma_f32_16x16x32_bf16 v[140:143], v[72:75], v[16:19], v[140:143]\nds_read_b128 v[16:19], %[xb] offset:12288\nv_lshrrev_b32 v48, 1, v49\ns_waitcnt lgkmcnt(4)\n"
        "v_mfma_f32_16x16x32_bf16 v[104:107], v[64:67], v[20:23], v[104:107]\nv_mad_u32_u24 v50, v49, 31, %[c2]\nv_mfma_f32_16x16x32_bf16 v[124:127], v[68:71], v[20:23], v[124:127]\n"
        "v_add_u32 v48, %[c0], v48\nv_mfma_f32_16x16x32_bf16 v[144:147], v[72:75], v[20:23], v[144:147]\nds_read_b128 v[20:23], %[xb] offset:14336\nv_add_u32 v49, %[c1], v49\ns_waitcnt lgkmcnt(4)\n"
        "v_mfma_f32_16x16x32_bf16 v[96:99], v[64:67], v[12:15], v[96:99]\nv_bfe_u32 %[pv], %[wa], 8, 1\nv_mfma_f32_16x16x32_bf16 v[116:119], v[68:71], v[12:15], v[116:119]\n"
        "v_mul_u32_u24 %[pv], 7936, %[pv]\nv_mfma_f32_16x16x32_bf16 v[136:139], v[72:75], v[12:15], v[136:139]\nds_read_b128 v[12:15], %[xb] offset:10240\nv_bfe_u32 v51, %[wa], 9, 1\n"
        "s_waitcnt lgkmcnt(4)\nv_mfma_f32_16x16x32_bf16 v[108:111], v[64:67], v[24:27], v[108:111]\nv_lshl_add_u32 v51, v51, 4, v50\n"
        "v_mfma_f32_16x16x32_bf16 v[128:131], v[68:71], v[24:27], v[128:131]\nv_add_u32 v51, v51, %[pv]\nv_mfma_f32_16x16x32_bf16 v[148:151], v[72:75], v[24:27], v[148:151]\n"
        "ds_read_b128 v[24:27], %[xb] offset:16384\nv_bfe_u32 %[pv], %[wa], 8, 2\ns_waitcnt lgkmcnt(4)\nv_mfma_f32_16x16x32_bf16 v[112:115], v[64:67], v[28:31], v[112:115]\n"
        "v_lshl_add_u32 v50, %[pv], 3, v50\nv_mfma_f32_16x16x32_bf16 v[132:135], v[68:71], v[28:31], v[132:135]\nv_mfma_f32_16x16x32_bf16 v[152:155], v[72:75], v[28:31], v[152:155]\n"
        "s_waitcnt vmcnt(0) lgkmcnt(3)\nv_mfma_f32_16x16x32_bf16 v[100:103], v[80:83], v[16:19], v[100:103]\nds_read_b128 v[28:31], %[xb] offset:18432\n"
        "v_mfma_f32_16x16x32_bf16 v[120:123], v[84:87], v[16:19], v[120:123]\nv_mfma_f32_16x16x32_bf16 v[140:143], v[88:91], v[16:19], v[140:143]\ns_waitcnt lgkmcnt(3)\n"
        "v_mfma_f32_16x16x32_bf16 v[104:107], v[80:83], v[20:23], v[104:107]\nv_mfma_f32_16x16x32_bf16 v[124:127], v[84:87], v[20:23], v[124:127]\nds_read_b32 %[pv], %[sy] offset:4\n"
        "ds_read_b64 v[32:33], v48 offset:256\nv_mfma_f32_16x16x32_bf16 v[144:147], v[88:91], v[20:23], v[144:147]\nds_read_b64 v[36:37], v48 offset:384\ns_waitcnt lgkmcnt(5)\n"
        "v_mfma_f32_16x16x32_bf16 v[96:99], v[80:83], v[12:15], v[96:99]\nds_read_b64 v[40:41], v48 offset:1536\nds_read_b128 v[52:55], v49 offset:0\n"
        "v_mfma_f32_16x16x32_bf16 v[116:119], v[84:87], v[12:15], v[116:119]\nds_read_b128 v[56:59], v49 offset:256\nv_mfma_f32_16x16x32_bf16 v[136:139], v[88:91], v[12:15], v[136:139]\n"
        "ds_read_b128 v[60:63], v49 offset:512\nds_read_b128 v[64:67], v49 offset:768\ns_waitcnt lgkmcnt(9)\nv_mfma_f32_16x16x32_bf16 v[108:111], v[80:83], v[24:27], v[108:111]\n"
        "ds_read_b128 v[68:71], v49 offset:1024\nv_mfma_f32_16x16x32_bf16 v[128:131], v[84:87], v[24:27], v[128:131]\nv_mfma_f32_16x16x32_bf16 v[148:151], v[88:91], v[24:27], v[148:151]\n"
        "s_waitcnt lgkmcnt(9)\nv_mfma_f32_16x16x32_bf16 v[112:115], v[80:83], v[28:31], v[112:115]\ns_setprio %[pfs]\nv_mfma_f32_16x16x32_bf16 v[132:135], v[84:87], v[28:31], v[132:135]\n"
        "v_mfma_f32_16x16x32_bf16 v[152:155], v[88:91], v[28:31], v[152:155]\ns_mov_b32 %[s1], 0x100000\n.LC0q%=:\ns_waitcnt lgkmcnt(0)\nv_readfirstlane_b32 %[s0], %[pv]\nv_add_u32 %[pv], 8, %[sy]\n"
        "s_cmp_ge_i32 %[s0], %[nst]\ns_cbranch_scc0 .LC0t%=\n.LC0u%=:\nv_mfma_f32_16x16x32_bf16 v[100:103], v[32:35], v[56:59], v[100:103]\n"
        "v_mfma_f32_16x16x32_bf16 v[104:107], v[32:35], v[60:63], v[104:107]\nv_mfma_f32_16x16x32_bf16 v[120:123], v[36:39], v[56:59], v[120:123]\n"
        "v_mfma_f32_16x16x32_bf16 v[124:127], v[36:39], v[60:63], v[124:127]\nv_mfma_f32_16x16x32_bf16 v[96:99], v[32:35], v[52:55], v[96:99]\nv_cvt_pk_bf16_f32 v100, v100, v101\n"
        "v_cvt_pk_bf16_f32 v101, v102, v103\nv_cvt_pk_bf16_f32 v102, v104, v105\nv_cvt_pk_bf16_f32 v103, v106, v107\nv_mfma_f32_16x16x32_bf16 v[116:119], v[36:39], v[52:55], v[116:119]\n"
        "v_permlane16_swap_b32 v100, v102\nv_permlane16_swap_b32 v101, v103\nds_write_b128 v51, v[100:103] offset:8000\nv_mfma_f32_16x16x32_bf16 v[108:111], v[32:35], v[64:67], v[108:111]\n"
        "v_cvt_pk_bf16_f32 v120, v120, v121\nv_cvt_pk_bf16_f32 v121, v122, v123\nv_cvt_pk_bf16_f32 v122, v124, v125\nv_cvt_pk_bf16_f32 v123, v126, v127\n"
        "v_mfma_f32_16x16x32_bf16 v[112:115], v[32:35], v[68:71], v[112:115]\nv_permlane16_swap_b32 v120, v122\nv_permlane16_swap_b32 v121, v123\nds_write_b128 v51, v[120:123] offset:8032\n"
        "v_cvt_pk_bf16_f32 v96, v96, v97\nv_mfma_f32_16x16x32_bf16 v[128:131], v[36:39], v[64:67], v[128:131]\nv_cvt_pk_bf16_f32 v97, v98, v99\nds_write_b64 v50, v[96:97] offset:64\n"
        "v_cvt_pk_bf16_f32 v116, v116, v117\nv_cvt_pk_bf16_f32 v117, v118, v119\nv_mfma_f32_16x16x32_bf16 v[132:135], v[36:39], v[68:71], v[132:135]\nds_write_b64 v50, v[116:117] offset:96\n"
        "v_mfma_f32_16x16x32_bf16 v[140:143], v[40:43], v[56:59], v[140:143]\nv_cvt_pk_bf16_f32 v108, v108, v109\nv_cvt_pk_bf16_f32 v109, v110, v111\nv_cvt_pk_bf16_f32 v110, v112, v113\n"
        "v_cvt_pk_bf16_f32 v111, v114, v115\nv_mfma_f32_16x16x32_bf16 v[144:147], v[40:43], v[60:63], v[144:147]\nv_permlane16_swap_b32 v108, v110\nv_permlane16_swap_b32 v109, v111\n"
        "ds_write_b128 v51, v[108:111] offset:23872\nv_mfma_f32_16x16x32_bf16 v[136:139], v[40:43], v[52:55], v[136:139]\nv_cvt_pk_bf16_f32 v128, v128, v129\nv_cvt_pk_bf16_f32 v129, v130, v131\n"
        "v_cvt_pk_bf16_f32 v130, v132, v133\nv_cvt_pk_bf16_f32 v131, v134, v135\nv_mfma_f32_16x16x32_bf16 v[148:151], v[40:43], v[64:67], v[148:151]\nv_permlane16_swap_b32 v128, v130\n"
        "v_permlane16_swap_b32 v129, v131\nds_write_b128 v51, v[128:131] offset:23904\nds_write_b32 %[pg], %[pv]\nv_mfma_f32_16x16x32_bf16 v[152:155], v[40:43], v[68:71], v[152:155]\n"
        "v_add_u32 %[sy], 12, %[sy]\nv_cvt_pk_bf16_f32 v140, v140, v141\nv_cvt_pk_bf16_f32 v141, v142, v143\nv_cvt_pk_bf16_f32 v142, v144, v145\ns_nop 3\nv_cvt_pk_bf16_f32 v143, v146, v147\ns_nop 0\n"
        "v_permlane16_swap_b32 v140, v142\nv_permlane16_swap_b32 v141, v143\nds_write_b128 v51, v[140:143] offset:8320\nv_cvt_pk_bf16_f32 v136, v136, v137\nv_cvt_pk_bf16_f32 v137, v138, v139\n"
        "ds_write_b64 v50, v[136:137] offset:384\nv_cvt_pk_bf16_f32 v148, v148, v149\nv_cvt_pk_bf16_f32 v149, v150, v151\nv_cvt_pk_bf16_f32 v150, v152, v153\nv_cvt_pk_bf16_f32 v151, v154, v155\n"
        "s_nop 0\nv_permlane16_swap_b32 v148, v150\nv_permlane16_swap_b32 v149, v151\nds_write_b128 v51, v[148:151] offset:24192\nds_write_b32 %[pg], %[sy]\ns_branch .LE%=\n.LC0t%=:\n"
        "s_sub_u32 %[s1], %[s1], 1\ns_cbranch_scc1 .LC0u%=\ns_sleep 1\nds_read_b32 %[pv], %[sy] offset:4\nds_read_b64 v[32:33], v48 offset:256\nds_read_b64 v[36:37], v48 offset:384\n"
        "ds_read_b64 v[40:41], v48 offset:1536\nds_read_b128 v[52:55], v49 offset:0\nds_read_b128 v[56:59], v49 offset:256\nds_read_b128 v[60:63], v49 offset:512\n"
        "ds_read_b128 v[64:67], v49 offset:768\nds_read_b128 v[68:71], v49 offset:1024\ns_branch .LC0q%=\n.LE%=:\n"
        : [pv] "=&v"(pv), [xa] "+v"(xa), [xb] "+v"(xb), [sy] "+v"(sy), [so] "=&s"(so), [s0] "=&s"(s0_), [s1] "=&s"(s1_)
        : [wa] "v"(wa), [rs] "s"(wrsrc), [pg] "v"(pga), [c0] "s"(LDSA(smem) + CW_OFF + tb * 128), [c1] "s"(LDSA(smem) + A_R2), [c2] "s"(LDSA(St) + tb * 32), [w] "s"(w), [ow] "i"(SY_W - SY_X), [pfs] "i"(PFS), [pf] "i"(PF), [nst] "i"(NST0)
        : "memory", "scc", "v8", "v9", "v10", "v11", "v12", "v13", "v14", "v15", "v16", "v17", "v18", "v19", "v20", "v21", "v22", "v23", "v24", "v25", "v26", "v27", "v28", "v29", "v30", "v31", "v32", "v33", "v34", "v35", "v36", "v37", "v38", "v39", "v40", "v41", "v42", "v43", "v44", "v45", "v46", "v47", "v48", "v49", "v50", "v51", "v52", "v53", "v54", "v55", "v56", "v57", "v58", "v59", "v60", "v61", "v62", "v63", "v64", "v65", "v66", "v67", "v68", "v69", "v70", "v71", "v72", "v73", "v74", "v75", "v76", "v77", "v78", "v79", "v80", "v81", "v82", "v83", "v84", "v85", "v86", "v87", "v88", "v89", "v90", "v91", "v92", "v93", "v94", "v95", "v96", "v97", "v98", "v99", "v100", "v101", "v102", "v103", "v104", "v105", "v106", "v107", "v108", "v109", "v110", "v111", "v112", "v113", "v114", "v115", "v116", "v117", "v118", "v119", "v120", "v121", "v122", "v123", "v124", "v125", "v126", "v127", "v128", "v129", "v130", "v131", "v132", "v133", "v134", "v135", "v136", "v137", "v138", "v139", "v140", "v141", "v142", "v143", "v144", "v145", "v146", "v147", "v148", "v149", "v150", "v151", "v152", "v153", "v154", "v155", "v156", "v157", "v158", "v159", "v160", "v161", "v162", "v163", "v164", "v165", "v166", "v167");
    } else {
      asm volatile(
        "s_cmp_lt_u32 %[w], 2\ns_cbranch_scc0 .LB%=\ns_mov_b32 %[so], 0\nbuffer_load_dwordx4 v[32:35], %[wa], %[rs], %[so] offen offset:0 sc1\n"
        "buffer_load_dwordx4 v[36:39], %[wa], %[rs], %[so] offen offset:1024 sc1\nbuffer_load_dwordx4 v[40:43], %[wa], %[rs], %[so] offen offset:2048 sc1\n"
        "buffer_load_dwordx4 v[44:47], %[wa], %[rs], %[so] offen offset:3072 sc1\ns_mov_b32 %[so], 15360\nbuffer_load_dwordx4 v[48:51], %[wa], %[rs], %[so] offen offset:0 sc1\n"
        "buffer_load_dwordx4 v[52:55], %[wa], %[rs], %[so] offen offset:1024 sc1\nbuffer_load_dwordx4 v[56:59], %[wa], %[rs], %[so] offen offset:2048 sc1\n"
        "buffer_load_dwordx4 v[60:63], %[wa], %[rs], %[so] offen offset:3072 sc1\ns_mov_b32 %[so], 30720\nbuffer_load_dwordx4 v[64:67], %[wa], %[rs], %[so] offen offset:0 sc1\n"
        "buffer_load_dwordx4 v[68:71], %[wa], %[rs], %[so] offen offset:1024 sc1\nbuffer_load_dwordx4 v[72:75], %[wa], %[rs], %[so] offen offset:2048 sc1\n"
        "buffer_load_dwordx4 v[76:79], %[wa], %[rs], %[so] offen offset:3072 sc1\ns_mov_b32 %[so], 46080\nbuffer_load_dwordx4 v[80:83], %[wa], %[rs], %[so] offen offset:0 sc1\n"
        "buffer_load_dwordx4 v[84:87], %[wa], %[rs], %[so] offen offset:1024 sc1\nbuffer_load_dwordx4 v[88:91], %[wa], %[rs], %[so] offen offset:2048 sc1\n"
        "buffer_load_dwordx4 v[92:95], %[wa], %[rs], %[so] offen offset:3072 sc1\ns_mov_b32 %[s1], 0x100000\n.LA1w%=:\nds_read_b32 %[pv], %[sy] offset:0\ns_waitcnt lgkmcnt(0)\n"
        "v_readfirstlane_b32 %[s0], %[pv]\ns_nop 0\ns_cmp_ge_i32 %[s0], 6\ns_cbranch_scc1 .LA1g%=\ns_sleep 1\ns_sub_u32 %[s1], %[s1], 1\ns_cbranch_scc0 .LA1w%=\n.LA1g%=:\n"
        "ds_read_b64_tr_b16 v[16:17], %[xa] offset:1056\nds_read_b64_tr_b16 v[18:19], %[xa] offset:1568\nds_read_b64_tr_b16 v[20:21], %[xa] offset:2112\nds_read_b64_tr_b16 v[22:23], %[xa] offset:2624\n"
        "ds_read_b64_tr_b16 v[12:13], %[xa] offset:0\nds_read_b64_tr_b16 v[14:15], %[xa] offset:512\nds_read_b64_tr_b16 v[24:25], %[xa] offset:3168\nds_read_b64_tr_b16 v[26:27], %[xa] offset:3680\n"
        "s_waitcnt vmcnt(12) lgkmcnt(6)\nv_mfma_f32_16x16x32_bf16 v[100:103], v[32:35], v[16:19], 0\nds_read_b64_tr_b16 v[28:29], %[xa] offset:4224\n"
        "v_mfma_f32_16x16x32_bf16 v[116:119], v[36:39], v[16:19], 0\nds_read_b64_tr_b16 v[30:31], %[xa] offset:4736\nv_mfma_f32_16x16x32_bf16 v[136:139], v[40:43], v[16:19], 0\n"
        "ds_read_b32 %[pv], %[sy] offset:4\nv_mfma_f32_16x16x32_bf16 v[152:155], v[44:47], v[16:19], 0\nds_read_b64_tr_b16 v[16:17], %[xa] offset:6336\ns_waitcnt lgkmcnt(8)\n"
        "v_mfma_f32_16x16x32_bf16 v[104:107], v[32:35], v[20:23], 0\nds_read_b64_tr_b16 v[18:19], %[xa] offset:6848\nv_mfma_f32_16x16x32_bf16 v[120:123], v[36:39], v[20:23], 0\n"
        "v_mfma_f32_16x16x32_bf16 v[140:143], v[40:43], v[20:23], 0\nv_mfma_f32_16x16x32_bf16 v[156:159], v[44:47], v[20:23], 0\nds_read_b64_tr_b16 v[20:21], %[xa] offset:7392\ns_waitcnt lgkmcnt(8)\n"
        "v_mfma_f32_16x16x32_bf16 v[96:99], v[32:35], v[12:15], 0\nds_read_b64_tr_b16 v[22:23], %[xa] offset:7904\nv_mfma_f32_16x16x32_bf16 v[132:135], v[40:43], v[12:15], 0\n"
        "ds_read_b64_tr_b16 v[12:13], %[xa] offset:5280\ns_waitcnt lgkmcnt(8)\nv_mfma_f32_16x16x32_bf16 v[108:111], v[32:35], v[24:27], 0\nds_read_b64_tr_b16 v[14:15], %[xa] offset:5792\n"
        "v_mfma_f32_16x16x32_bf16 v[124:127], v[36:39], v[24:27], 0\nv_mfma_f32_16x16x32_bf16 v[144:147], v[40:43], v[24:27], 0\nv_mfma_f32_16x16x32_bf16 v[160:163], v[44:47], v[24:27], 0\n"
        "ds_read_b64_tr_b16 v[24:25], %[xa] offset:8448\ns_waitcnt lgkmcnt(8)\nv_mfma_f32_16x16x32_bf16 v[112:115], v[32:35], v[28:31], 0\nds_read_b64_tr_b16 v[26:27], %[xa] offset:8960\n"
        "v_mfma_f32_16x16x32_bf16 v[128:131], v[36:39], v[28:31], 0\nv_mfma_f32_16x16x32_bf16 v[148:151], v[40:43], v[28:31], 0\nv_mfma_f32_16x16x32_bf16 v[164:167], v[44:47], v[28:31], 0\n.LA1l%=:\n"
        "s_waitcnt vmcnt(8) lgkmcnt(6)\nv_mfma_f32_16x16x32_bf16 v[100:103], v[48:51], v[16:19], v[100:103]\ns_addk_i32 %[so], 15360\nv_readfirstlane_b32 %[s0], %[pv]\n"
        "v_mfma_f32_16x16x32_bf16 v[116:119], v[52:55], v[16:19], v[116:119]\ns_cmp_ge_i32 %[s0], 6\nds_read_b64_tr_b16 v[28:29], %[xa] offset:9504\n"
        "v_mfma_f32_16x16x32_bf16 v[136:139], v[56:59], v[16:19], v[136:139]\ns_cbranch_scc0 .LA1s0_%=\n.LA1r0_%=:\nds_read_b64_tr_b16 v[30:31], %[xa] offset:10016\n"
        "v_mfma_f32_16x16x32_bf16 v[152:155], v[60:63], v[16:19], v[152:155]\nds_read_b64_tr_b16 v[16:17], %[xa] offset:11616\ns_waitcnt lgkmcnt(7)\n"
        "v_mfma_f32_16x16x32_bf16 v[104:107], v[48:51], v[20:23], v[104:107]\nds_read_b64_tr_b16 v[18:19], %[xa] offset:12128\nbuffer_load_dwordx4 v[32:35], %[wa], %[rs], %[so] offen offset:0 sc1\n"
        "v_mfma_f32_16x16x32_bf16 v[120:123], v[52:55], v[20:23], v[120:123]\nbuffer_load_dwordx4 v[36:39], %[wa], %[rs], %[so] offen offset:1024 sc1\n"
        "v_mfma_f32_16x16x32_bf16 v[140:143], v[56:59], v[20:23], v[140:143]\nv_mfma_f32_16x16x32_bf16 v[156:159], v[60:63], v[20:23], v[156:159]\nds_read_b64_tr_b16 v[20:21], %[xa] offset:12672\n"
        "s_waitcnt lgkmcnt(7)\nv_mfma_f32_16x16x32_bf16 v[96:99], v[48:51], v[12:15], v[96:99]\nds_read_b64_tr_b16 v[22:23], %[xa] offset:13184\n"
        "v_mfma_f32_16x16x32_bf16 v[132:135], v[56:59], v[12:15], v[132:135]\nds_read_b64_tr_b16 v[12:13], %[xa] offset:10560\ns_waitcnt lgkmcnt(7)\n"
        "v_mfma_f32_16x16x32_bf16 v[108:111], v[48:51], v[24:27], v[108:111]\nds_read_b64_tr_b16 v[14:15], %[xa] offset:11072\nv_mfma_f32_16x16x32_bf16 v[124:127], v[52:55], v[24:27], v[124:127]\n"
        "buffer_load_dwordx4 v[40:43], %[wa], %[rs], %[so] offen offset:2048 sc1\nv_mfma_f32_16x16x32_bf16 v[144:147], v[56:59], v[24:27], v[144:147]\n"
        "buffer_load_dwordx4 v[44:47], %[wa], %[rs], %[so] offen offset:3072 sc1\nv_mfma_f32_16x16x32_bf16 v[160:163], v[60:63], v[24:27], v[160:163]\nds_read_b64_tr_b16 v[24:25], %[xa] offset:13728\n"
        "s_waitcnt lgkmcnt(7)\nv_mfma_f32_16x16x32_bf16 v[112:115], v[48:51], v[28:31], v[112:115]\nds_read_b64_tr_b16 v[26:27], %[xa] offset:14240\n"
        "v_mfma_f32_16x16x32_bf16 v[128:131], v[52:55], v[28:31], v[128:131]\nv_add_u32 %[sy], 4, %[sy]\nv_mfma_f32_16x16x32_bf16 v[148:151], v[56:59], v[28:31], v[148:151]\nds_write_b32 %[pg], %[sy]\n"
        "v_mfma_f32_16x16x32_bf16 v[164:167], v[60:63], v[28:31], v[164:167]\ns_waitcnt vmcnt(8) lgkmcnt(7)\nv_mfma_f32_16x16x32_bf16 v[100:103], v[64:67], v[16:19], v[100:103]\n"
        "s_addk_i32 %[so], 15360\nds_read_b64_tr_b16 v[28:29], %[xa] offset:14784\nv_mfma_f32_16x16x32_bf16 v[116:119], v[68:71], v[16:19], v[116:119]\nds_read_b64_tr_b16 v[30:31], %[xa] offset:15296\n"
        "v_mfma_f32_16x16x32_bf16 v[136:139], v[72:75], v[16:19], v[136:139]\nds_read_b32 %[pv], %[sy] offset:4\nbuffer_load_dwordx4 v[48:51], %[wa], %[rs], %[so] offen offset:0 sc1\n"
        "v_mfma_f32_16x16x32_bf16 v[152:155], v[76:79], v[16:19], v[152:155]\nds_read_b64_tr_b16 v[16:17], %[xa] offset:16896\ns_waitcnt lgkmcnt(9)\n"
        "v_mfma_f32_16x16x32_bf16 v[104:107], v[64:67], v[20:23], v[104:107]\nds_read_b64_tr_b16 v[18:19], %[xa] offset:17408\nv_mfma_f32_16x16x32_bf16 v[120:123], v[68:71], v[20:23], v[120:123]\n"
        "buffer_load_dwordx4 v[52:55], %[wa], %[rs], %[so] offen offset:1024 sc1\nv_mfma_f32_16x16x32_bf16 v[140:143], v[72:75], v[20:23], v[140:143]\n"
        "v_mfma_f32_16x16x32_bf16 v[156:159], v[76:79], v[20:23], v[156:159]\nds_read_b64_tr_b16 v[20:21], %[xa] offset:17952\ns_waitcnt lgkmcnt(9)\n"
        "v_mfma_f32_16x16x32_bf16 v[96:99], v[64:67], v[12:15], v[96:99]\nds_read_b64_tr_b16 v[22:23], %[xa] offset:18464\nv_mfma_f32_16x16x32_bf16 v[132:135], v[72:75], v[12:15], v[132:135]\n"
        "ds_read_b64_tr_b16 v[12:13], %[xa] offset:15840\ns_waitcnt lgkmcnt(9)\nv_mfma_f32_16x16x32_bf16 v[108:111], v[64:67], v[24:27], v[108:111]\nds_read_b64_tr_b16 v[14:15], %[xa] offset:16352\n"
        "v_mfma_f32_16x16x32_bf16 v[124:127], v[68:71], v[24:27], v[124:127]\nbuffer_load_dwordx4 v[56:59], %[wa], %[rs], %[so] offen offset:2048 sc1\n"
        "v_mfma_f32_16x16x32_bf16 v[144:147], v[72:75], v[24:27], v[144:147]\nbuffer_load_dwordx4 v[60:63], %[wa], %[rs], %[so] offen offset:3072 sc1\n"
        "v_mfma_f32_16x16x32_bf16 v[160:163], v[76:79], v[24:27], v[160:163]\nds_read_b64_tr_b16 v[24:25], %[xa] offset:19008\ns_waitcnt lgkmcnt(8)\n"
        "v_mfma_f32_16x16x32_bf16 v[112:115], v[64:67], v[28:31], v[112:115]\nds_read_b64_tr_b16 v[26:27], %[xa] offset:19520\nv_mfma_f32_16x16x32_bf16 v[128:131], v[68:71], v[28:31], v[128:131]\n"
        "v_mfma_f32_16x16x32_bf16 v[148:151], v[72:75], v[28:31], v[148:151]\nv_mfma_f32_16x16x32_bf16 v[164:167], v[76:79], v[28:31], v[164:167]\ns_waitcnt vmcnt(8) lgkmcnt(6)\n"
        "v_mfma_f32_16x16x32_bf16 v[100:103], v[80:83], v[16:19], v[100:103]\ns_addk_i32 %[so], 15360\nv_readfirstlane_b32 %[s0], %[pv]\n"
        "v_mfma_f32_16x16x32_bf16 v[116:119], v[84:87], v[16:19], v[116:119]\ns_cmp_ge_i32 %[s0], 6\nds_read_b64_tr_b16 v[28:29], %[xa] offset:20064\n"
        "v_mfma_f32_16x16x32_bf16 v[136:139], v[88:91], v[16:19], v[136:139]\ns_cbranch_scc0 .LA1s1_%=\n.LA1r1_%=:\nds_read_b64_tr_b16 v[30:31], %[xa] offset:20576\n"
        "v_mfma_f32_16x16x32_bf16 v[152:155], v[92:95], v[16:19], v[152:155]\nds_read_b64_tr_b16 v[16:17], %[xa] offset:22176\ns_waitcnt lgkmcnt(7)\n"
        "v_mfma_f32_16x16x32_bf16 v[104:107], v[80:83], v[20:23], v[104:107]\nds_read_b64_tr_b16 v[18:19], %[xa] offset:22688\nbuffer_load_dwordx4 v[64:67], %[wa], %[rs], %[so] offen offset:0 sc1\n"
        "v_mfma_f32_16x16x32_bf16 v[120:123], v[84:87], v[20:23], v[120:123]\nbuffer_load_dwordx4 v[68:71], %[wa], %[rs], %[so] offen offset:1024 sc1\n"
        "v_mfma_f32_16x16x32_bf16 v[140:143], v[88:91], v[20:23], v[140:143]\nv_mfma_f32_16x16x32_bf16 v[156:159], v[92:95], v[20:23], v[156:159]\nds_read_b64_tr_b16 v[20:21], %[xa] offset:23232\n"
        "s_waitcnt lgkmcnt(7)\nv_mfma_f32_16x16x32_bf16 v[96:99], v[80:83], v[12:15], v[96:99]\nds_read_b64_tr_b16 v[22:23], %[xa] offset:23744\n"
        "v_mfma_f32_16x16x32_bf16 v[132:135], v[88:91], v[12:15], v[132:135]\nds_read_b64_tr_b16 v[12:13], %[xa] offset:21120\ns_waitcnt lgkmcnt(7)\n"
        "v_mfma_f32_16x16x32_bf16 v[108:111], v[80:83], v[24:27], v[108:111]\nds_read_b64_tr_b16 v[14:15], %[xa] offset:21632\nv_mfma_f32_16x16x32_bf16 v[124:127], v[84:87], v[24:27], v[124:127]\n"
        "buffer_load_dwordx4 v[72:75], %[wa], %[rs], %[so] offen offset:2048 sc1\nv_mfma_f32_16x16x32_bf16 v[144:147], v[88:91], v[24:27], v[144:147]\n"
        "buffer_load_dwordx4 v[76:79], %[wa], %[rs], %[so] offen offset:3072 sc1\nv_mfma_f32_16x16x32_bf16 v[160:163], v[92:95], v[24:27], v[160:163]\nds_read_b64_tr_b16 v[24:25], %[xa] offset:24288\n"
        "s_waitcnt lgkmcnt(7)\nv_mfma_f32_16x16x32_bf16 v[112:115], v[80:83], v[28:31], v[112:115]\nds_read_b64_tr_b16 v[26:27], %[xa] offset:24800\n"
        "v_mfma_f32_16x16x32_bf16 v[128:131], v[84:87], v[28:31], v[128:131]\nv_add_u32 %[sy], 4, %[sy]\nv_mfma_f32_16x16x32_bf16 v[148:151], v[88:91], v[28:31], v[148:151]\nds_write_b32 %[pg], %[sy]\n"
        "v_mfma_f32_16x16x32_bf16 v[164:167], v[92:95], v[28:31], v[164:167]\ns_waitcnt vmcnt(8) lgkmcnt(7)\nv_mfma_f32_16x16x32_bf16 v[100:103], v[32:35], v[16:19], v[100:103]\n"
        "s_addk_i32 %[so], 15360\nds_read_b64_tr_b16 v[28:29], %[xa] offset:25344\nv_mfma_f32_16x16x32_bf16 v[116:119], v[36:39], v[16:19], v[116:119]\nds_read_b64_tr_b16 v[30:31], %[xa] offset:25856\n"
        "v_mfma_f32_16x16x32_bf16 v[136:139], v[40:43], v[16:19], v[136:139]\nds_read_b32 %[pv], %[sy] offset:4\nbuffer_load_dwordx4 v[80:83], %[wa], %[rs], %[so] offen offset:0 sc1\n"
        "v_mfma_f32_16x16x32_bf16 v[152:155], v[44:47], v[16:19], v[152:155]\nds_read_b64_tr_b16 v[16:17], %[xa] offset:27456\ns_waitcnt lgkmcnt(9)\n"
        "v_mfma_f32_16x16x32_bf16 v[104:107], v[32:35], v[20:23], v[104:107]\nds_read_b64_tr_b16 v[18:19], %[xa] offset:27968\nv_mfma_f32_16x16x32_bf16 v[120:123], v[36:39], v[20:23], v[120:123]\n"
        "buffer_load_dwordx4 v[84:87], %[wa], %[rs], %[so] offen offset:1024 sc1\nv_mfma_f32_16x16x32_bf16 v[140:143], v[40:43], v[20:23], v[140:143]\n"
        "v_mfma_f32_16x16x32_bf16 v[156:159], v[44:47], v[20:23], v[156:159]\nds_read_b64_tr_b16 v[20:21], %[xa] offset:28512\ns_waitcnt lgkmcnt(9)\n"
        "v_mfma_f32_16x16x32_bf16 v[96:99], v[32:35], v[12:15], v[96:99]\nds_read_b64_tr_b16 v[22:23], %[xa] offset:29024\nv_mfma_f32_16x16x32_bf16 v[132:135], v[40:43], v[12:15], v[132:135]\n"
        "ds_read_b64_tr_b16 v[12:13], %[xa] offset:26400\ns_waitcnt lgkmcnt(9)\nv_mfma_f32_16x16x32_bf16 v[108:111], v[32:35], v[24:27], v[108:111]\nds_read_b64_tr_b16 v[14:15], %[xa] offset:26912\n"
        "v_mfma_f32_16x16x32_bf16 v[124:127], v[36:39], v[24:27], v[124:127]\nbuffer_load_dwordx4 v[88:91], %[wa], %[rs], %[so] offen offset:2048 sc1\n"
        "v_mfma_f32_16x16x32_bf16 v[144:147], v[40:43], v[24:27], v[144:147]\nbuffer_load_dwordx4 v[92:95], %[wa], %[rs], %[so] offen offset:3072 sc1\n"
        "v_mfma_f32_16x16x32_bf16 v[160:163], v[44:47], v[24:27], v[160:163]\nds_read_b64_tr_b16 v[24:25], %[xa] offset:29568\ns_waitcnt lgkmcnt(8)\n"
        "v_mfma_f32_16x16x32_bf16 v[112:115], v[32:35], v[28:31], v[112:115]\nds_read_b64_tr_b16 v[26:27], %[xa] offset:30080\nv_mfma_f32_16x16x32_bf16 v[128:131], v[36:39], v[28:31], v[128:131]\n"
        "v_mfma_f32_16x16x32_bf16 v[148:151], v[40:43], v[28:31], v[148:151]\ns_cmp_lg_u32 %[so], 291840\nv_mfma_f32_16x16x32_bf16 v[164:167], v[44:47], v[28:31], v[164:167]\n"
        "v_add_u32 %[xa], 21120, %[xa]\ns_cbranch_scc1 .LA1l%=\ns_waitcnt vmcnt(8) lgkmcnt(6)\nv_mfma_f32_16x16x32_bf16 v[100:103], v[48:51], v[16:19], v[100:103]\nv_readfirstlane_b32 %[s0], %[pv]\n"
        "ds_read_b64_tr_b16 v[28:29], %[xa] offset:9504\nv_mfma_f32_16x16x32_bf16 v[116:119], v[52:55], v[16:19], v[116:119]\ns_cmp_ge_i32 %[s0], 6\nds_read_b64_tr_b16 v[30:31], %[xa] offset:10016\n"
        "v_mfma_f32_16x16x32_bf16 v[136:139], v[56:59], v[16:19], v[136:139]\ns_cbranch_scc0 .LA1s2_%=\n.LA1r2_%=:\nv_mfma_f32_16x16x32_bf16 v[152:155], v[60:63], v[16:19], v[152:155]\n"
        "ds_read_b64_tr_b16 v[16:17], %[xa] offset:11616\ns_waitcnt lgkmcnt(7)\nv_mfma_f32_16x16x32_bf16 v[104:107], v[48:51], v[20:23], v[104:107]\nds_read_b64_tr_b16 v[18:19], %[xa] offset:12128\n"
        "v_mfma_f32_16x16x32_bf16 v[120:123], v[52:55], v[20:23], v[120:123]\nv_mfma_f32_16x16x32_bf16 v[140:143], v[56:59], v[20:23], v[140:143]\n"
        "v_mfma_f32_16x16x32_bf16 v[156:159], v[60:63], v[20:23], v[156:159]\nds_read_b64_tr_b16 v[20:21], %[xa] offset:12672\ns_waitcnt lgkmcnt(7)\n"
        "v_mfma_f32_16x16x32_bf16 v[96:99], v[48:51], v[12:15], v[96:99]\nds_read_b64_tr_b16 v[22:23], %[xa] offset:13184\nv_mfma_f32_16x16x32_bf16 v[132:135], v[56:59], v[12:15], v[132:135]\n"
        "ds_read_b64_tr_b16 v[12:13], %[xa] offset:10560\ns_waitcnt lgkmcnt(7)\nv_mfma_f32_16x16x32_bf16 v[108:111], v[48:51], v[24:27], v[108:111]\nds_read_b64_tr_b16 v[14:15], %[xa] offset:11072\n"
        "v_mfma_f32_16x16x32_bf16 v[124:127], v[52:55], v[24:27], v[124:127]\nv_mfma_f32_16x16x32_bf16 v[144:147], v[56:59], v[24:27], v[144:147]\n"
        "v_mfma_f32_16x16x32_bf16 v[160:163], v[60:63], v[24:27], v[160:163]\nds_read_b64_tr_b16 v[24:25], %[xa] offset:13728\ns_waitcnt lgkmcnt(7)\n"
        "v_mfma_f32_16x16x32_bf16 v[112:115], v[48:51], v[28:31], v[112:115]\nds_read_b64_tr_b16 v[26:27], %[xa] offset:14240\nv_mfma_f32_16x16x32_bf16 v[128:131], v[52:55], v[28:31], v[128:131]\n"
        "v_add_u32 %[sy], 4, %[sy]\nv_mfma_f32_16x16x32_bf16 v[148:151], v[56:59], v[28:31], v[148:151]\nv_mfma_f32_16x16x32_bf16 v[164:167], v[60:63], v[28:31], v[164:167]\n"
        "s_waitcnt vmcnt(4) lgkmcnt(6)\nv_mfma_f32_16x16x32_bf16 v[100:103], v[64:67], v[16:19], v[100:103]\nds_read_b64_tr_b16 v[28:29], %[xa] offset:14784\n"
        "v_mfma_f32_16x16x32_bf16 v[116:119], v[68:71], v[16:19], v[116:119]\nds_read_b64_tr_b16 v[30:31], %[xa] offset:15296\nv_and_b32 v49, 0xf0, %[wa]\n"
        "v_mfma_f32_16x16x32_bf16 v[136:139], v[72:75], v[16:19], v[136:139]\nv_lshrrev_b32 v48, 1, v49\nv_mfma_f32_16x16x32_bf16 v[152:155], v[76:79], v[16:19], v[152:155]\n"
        "ds_read_b64_tr_b16 v[16:17], %[xa] offset:16896\nv_mad_u32_u24 v50, v49, 31, %[c2]\ns_waitcnt lgkmcnt(7)\nv_mfma_f32_16x16x32_bf16 v[104:107], v[64:67], v[20:23], v[104:107]\n"
        "ds_read_b64_tr_b16 v[18:19], %[xa] offset:17408\nv_add_u32 v48, %[c0], v48\nv_mfma_f32_16x16x32_bf16 v[120:123], v[68:71], v[20:23], v[120:123]\n"
        "v_mfma_f32_16x16x32_bf16 v[140:143], v[72:75], v[20:23], v[140:143]\nv_add_u32 v49, %[c1], v49\nv_mfma_f32_16x16x32_bf16 v[156:159], v[76:79], v[20:23], v[156:159]\n"
        "ds_read_b64_tr_b16 v[20:21], %[xa] offset:17952\nv_bfe_u32 %[pv], %[wa], 8, 1\ns_waitcnt lgkmcnt(7)\nv_mfma_f32_16x16x32_bf16 v[96:99], v[64:67], v[12:15], v[96:99]\n"
        "ds_read_b64_tr_b16 v[22:23], %[xa] offset:18464\nv_mul_u32_u24 %[pv], 7936, %[pv]\nv_mfma_f32_16x16x32_bf16 v[132:135], v[72:75], v[12:15], v[132:135]\n"
        "ds_read_b64_tr_b16 v[12:13], %[xa] offset:15840\nv_bfe_u32 v51, %[wa], 9, 1\ns_waitcnt lgkmcnt(7)\nv_mfma_f32_16x16x32_bf16 v[108:111], v[64:67], v[24:27], v[108:111]\n"
        "ds_read_b64_tr_b16 v[14:15], %[xa] offset:16352\nv_mfma_f32_16x16x32_bf16 v[124:127], v[68:71], v[24:27], v[124:127]\nv_lshl_add_u32 v51, v51, 4, v50\n"
        "v_mfma_f32_16x16x32_bf16 v[144:147], v[72:75], v[24:27], v[144:147]\nv_add_u32 v51, v51, %[pv]\nv_mfma_f32_16x16x32_bf16 v[160:163], v[76:79], v[24:27], v[160:163]\n"
        "ds_read_b64_tr_b16 v[24:25], %[xa] offset:19008\nv_bfe_u32 %[pv], %[wa], 8, 2\ns_waitcnt lgkmcnt(7)\nv_mfma_f32_16x16x32_bf16 v[112:115], v[64:67], v[28:31], v[112:115]\n"
        "ds_read_b64_tr_b16 v[26:27], %[xa] offset:19520\nv_lshl_add_u32 v50, %[pv], 3, v50\nv_mfma_f32_16x16x32_bf16 v[128:131], v[68:71], v[28:31], v[128:131]\n"
        "v_mfma_f32_16x16x32_bf16 v[148:151], v[72:75], v[28:31], v[148:151]\nv_mfma_f32_16x16x32_bf16 v[164:167], v[76:79], v[28:31], v[164:167]\ns_waitcnt vmcnt(0) lgkmcnt(6)\n"
        "v_mfma_f32_16x16x32_bf16 v[100:103], v[80:83], v[16:19], v[100:103]\nds_read_b64_tr_b16 v[28:29], %[xa] offset:20064\nv_mfma_f32_16x16x32_bf16 v[116:119], v[84:87], v[16:19], v[116:119]\n"
        "ds_read_b64_tr_b16 v[30:31], %[xa] offset:20576\nv_mfma_f32_16x16x32_bf16 v[136:139], v[88:91], v[16:19], v[136:139]\nv_mfma_f32_16x16x32_bf16 v[152:155], v[92:95], v[16:19], v[152:155]\n"
        "s_waitcnt lgkmcnt(6)\nv_mfma_f32_16x16x32_bf16 v[104:107], v[80:83], v[20:23], v[104:107]\nv_mfma_f32_16x16x32_bf16 v[120:123], v[84:87], v[20:23], v[120:123]\n"
        "v_mfma_f32_16x16x32_bf16 v[140:143], v[88:91], v[20:23], v[140:143]\nv_mfma_f32_16x16x32_bf16 v[156:159], v[92:95], v[20:23], v[156:159]\nds_read_b32 %[pv], %[sy] offset:4\n"
        "ds_read_b64 v[32:33], v48 offset:0\ns_waitcnt lgkmcnt(6)\nv_mfma_f32_16x16x32_bf16 v[96:99], v[80:83], v[12:15], v[96:99]\nds_read_b64 v[36:37], v48 offset:640\n"
        "v_mfma_f32_16x16x32_bf16 v[132:135], v[88:91], v[12:15], v[132:135]\nds_read_b64 v[40:41], v48 offset:1280\nds_read_b64 v[44:45], v48 offset:1024\ns_waitcnt lgkmcnt(7)\n"
        "v_mfma_f32_16x16x32_bf16 v[108:111], v[80:83], v[24:27], v[108:111]\nds_read_b128 v[52:55], v49 offset:0\nv_mfma_f32_16x16x32_bf16 v[124:127], v[84:87], v[24:27], v[124:127]\n"
        "ds_read_b128 v[56:59], v49 offset:256\nds_read_b128 v[60:63], v49 offset:512\nv_mfma_f32_16x16x32_bf16 v[144:147], v[88:91], v[24:27], v[144:147]\nds_read_b128 v[64:67], v49 offset:768\n"
        "v_mfma_f32_16x16x32_bf16 v[160:163], v[92:95], v[24:27], v[160:163]\nds_read_b128 v[68:71], v49 offset:1024\ns_waitcnt lgkmcnt(10)\n"
        "v_mfma_f32_16x16x32_bf16 v[112:115], v[80:83], v[28:31], v[112:115]\nv_mfma_f32_16x16x32_bf16 v[128:131], v[84:87], v[28:31], v[128:131]\ns_setprio %[pfs]\n"
        "v_mfma_f32_16x16x32_bf16 v[148:151], v[88:91], v[28:31], v[148:151]\nv_mfma_f32_16x16x32_bf16 v[164:167], v[92:95], v[28:31], v[164:167]\ns_mov_b32 %[s1], 0x100000\n.LA1q%=:\n"
        "s_waitcnt lgkmcnt(0)\nv_readfirstlane_b32 %[s0], %[pv]\nv_add_u32 %[pv], 8, %[sy]\ns_cmp_ge_i32 %[s0], %[nst]\ns_cbranch_scc0 .LA1t%=\n.LA1u%=:\n"
        "v_mfma_f32_16x16x32_bf16 v[100:103], v[32:35], v[56:59], v[100:103]\nv_mfma_f32_16x16x32_bf16 v[104:107], v[32:35], v[60:63], v[104:107]\n"
        "v_mfma_f32_16x16x32_bf16 v[116:119], v[36:39], v[56:59], v[116:119]\nv_mfma_f32_16x16x32_bf16 v[120:123], v[36:39], v[60:63], v[120:123]\n"
        "v_mfma_f32_16x16x32_bf16 v[96:99], v[32:35], v[52:55], v[96:99]\nv_cvt_pk_bf16_f32 v100, v100, v101\nv_cvt_pk_bf16_f32 v101, v102, v103\nv_cvt_pk_bf16_f32 v102, v104, v105\n"
        "v_cvt_pk_bf16_f32 v103, v106, v107\nv_mfma_f32_16x16x32_bf16 v[108:111], v[32:35], v[64:67], v[108:111]\nv_permlane16_swap_b32 v100, v102\nv_permlane16_swap_b32 v101, v103\n"
        "ds_write_b128 v51, v[100:103] offset:7936\nv_mfma_f32_16x16x32_bf16 v[112:115], v[32:35], v[68:71], v[112:115]\nv_cvt_pk_bf16_f32 v116, v116, v117\nv_cvt_pk_bf16_f32 v117, v118, v119\n"
        "v_cvt_pk_bf16_f32 v118, v120, v121\nv_cvt_pk_bf16_f32 v119, v122, v123\nv_mfma_f32_16x16x32_bf16 v[124:127], v[36:39], v[64:67], v[124:127]\nv_permlane16_swap_b32 v116, v118\n"
        "v_permlane16_swap_b32 v117, v119\nds_write_b128 v51, v[116:119] offset:8096\nv_cvt_pk_bf16_f32 v96, v96, v97\nv_mfma_f32_16x16x32_bf16 v[128:131], v[36:39], v[68:71], v[128:131]\n"
        "v_cvt_pk_bf16_f32 v97, v98, v99\nds_write_b64 v50, v[96:97] offset:0\nv_mfma_f32_16x16x32_bf16 v[136:139], v[40:43], v[56:59], v[136:139]\nv_cvt_pk_bf16_f32 v108, v108, v109\n"
        "v_cvt_pk_bf16_f32 v109, v110, v111\nv_cvt_pk_bf16_f32 v110, v112, v113\nv_cvt_pk_bf16_f32 v111, v114, v115\nv_mfma_f32_16x16x32_bf16 v[140:143], v[40:43], v[60:63], v[140:143]\n"
        "v_permlane16_swap_b32 v108, v110\nv_permlane16_swap_b32 v109, v111\nds_write_b128 v51, v[108:111] offset:23808\nv_mfma_f32_16x16x32_bf16 v[152:155], v[44:47], v[56:59], v[152:155]\n"
        "v_cvt_pk_bf16_f32 v124, v124, v125\nv_cvt_pk_bf16_f32 v125, v126, v127\nv_cvt_pk_bf16_f32 v126, v128, v129\nv_cvt_pk_bf16_f32 v127, v130, v131\n"
        "v_mfma_f32_16x16x32_bf16 v[156:159], v[44:47], v[60:63], v[156:159]\nv_permlane16_swap_b32 v124, v126\nv_permlane16_swap_b32 v125, v127\nds_write_b128 v51, v[124:127] offset:23968\n"
        "ds_write_b32 %[pg], %[pv]\nv_mfma_f32_16x16x32_bf16 v[132:135], v[40:43], v[52:55], v[132:135]\nv_add_u32 %[sy], 12, %[sy]\nv_cvt_pk_bf16_f32 v136, v136, v137\n"
        "v_cvt_pk_bf16_f32 v137, v138, v139\nv_cvt_pk_bf16_f32 v138, v140, v141\nv_mfma_f32_16x16x32_bf16 v[144:147], v[40:43], v[64:67], v[144:147]\nv_cvt_pk_bf16_f32 v139, v142, v143\ns_nop 0\n"
        "v_permlane16_swap_b32 v136, v138\nv_permlane16_swap_b32 v137, v139\nv_mfma_f32_16x16x32_bf16 v[148:151], v[40:43], v[68:71], v[148:151]\nds_write_b128 v51, v[136:139] offset:8256\n"
        "v_cvt_pk_bf16_f32 v152, v152, v153\nv_cvt_pk_bf16_f32 v153, v154, v155\nv_cvt_pk_bf16_f32 v154, v156, v157\nv_mfma_f32_16x16x32_bf16 v[160:163], v[44:47], v[64:67], v[160:163]\n"
        "v_cvt_pk_bf16_f32 v155, v158, v159\ns_nop 0\nv_permlane16_swap_b32 v152, v154\nv_permlane16_swap_b32 v153, v155\nv_mfma_f32_16x16x32_bf16 v[164:167], v[44:47], v[68:71], v[164:167]\n"
        "ds_write_b128 v51, v[152:155] offset:8192\nv_cvt_pk_bf16_f32 v132, v132, v133\nv_cvt_pk_bf16_f32 v133, v134, v135\nds_write_b64 v50, v[132:133] offset:320\ns_nop 3\n"
        "v_cvt_pk_bf16_f32 v144, v144, v145\nv_cvt_pk_bf16_f32 v145, v146, v147\nv_cvt_pk_bf16_f32 v146, v148, v149\nv_cvt_pk_bf16_f32 v147, v150, v151\ns_nop 0\nv_permlane16_swap_b32 v144, v146\n"
        "v_permlane16_swap_b32 v145, v147\nds_write_b128 v51, v[144:147] offset:24128\nv_cvt_pk_bf16_f32 v160, v160, v161\nv_cvt_pk_bf16_f32 v161, v162, v163\nv_cvt_pk_bf16_f32 v162, v164, v165\n"
        "v_cvt_pk_bf16_f32 v163, v166, v167\ns_nop 0\nv_permlane16_swap_b32 v160, v162\nv_permlane16_swap_b32 v161, v163\nds_write_b128 v51, v[160:163] offset:24064\nds_write_b32 %[pg], %[sy]\n"
        "s_branch .LE%=\n.LA1s0_%=:\ns_mov_b32 %[s1], 0x100000\n.LA1p0_%=:\nds_read_b32 %[pv], %[sy] offset:4\ns_waitcnt lgkmcnt(0)\nv_readfirstlane_b32 %[s0], %[pv]\ns_nop 0\ns_cmp_ge_i32 %[s0], 6\n"
        "s_cbranch_scc1 .LA1r0_%=\ns_sleep 1\ns_sub_u32 %[s1], %[s1], 1\ns_cbranch_scc0 .LA1p0_%=\ns_branch .LA1r0_%=\n.LA1s1_%=:\ns_mov_b32 %[s1], 0x100000\n.LA1p1_%=:\n"
        "ds_read_b32 %[pv], %[sy] offset:4\ns_waitcnt lgkmcnt(0)\nv_readfirstlane_b32 %[s0], %[pv]\ns_nop 0\ns_cmp_ge_i32 %[s0], 6\ns_cbranch_scc1 .LA1r1_%=\ns_sleep 1\ns_sub_u32 %[s1], %[s1], 1\n"
        "s_cbranch_scc0 .LA1p1_%=\ns_branch .LA1r1_%=\n.LA1s2_%=:\ns_mov_b32 %[s1], 0x100000\n.LA1p2_%=:\nds_read_b32 %[pv], %[sy] offset:4\ns_waitcnt lgkmcnt(0)\nv_readfirstlane_b32 %[s0], %[pv]\n"
        "s_nop 0\ns_cmp_ge_i32 %[s0], 6\ns_cbranch_scc1 .LA1r2_%=\ns_sleep 1\ns_sub_u32 %[s1], %[s1], 1\ns_cbranch_scc0 .LA1p2_%=\ns_branch .LA1r2_%=\n.LA1t%=:\ns_sub_u32 %[s1], %[s1], 1\n"
        "s_cbranch_scc1 .LA1u%=\ns_sleep 1\nds_read_b32 %[pv], %[sy] offset:4\nds_read_b64 v[32:33], v48 offset:0\nds_read_b64 v[36:37], v48 offset:640\nds_read_b64 v[40:41], v48 offset:1280\n"
        "ds_read_b64 v[44:45], v48 offset:1024\nds_read_b128 v[52:55], v49 offset:0\nds_read_b128 v[56:59], v49 offset:256\nds_read_b128 v[60:63], v49 offset:512\n"
        "ds_read_b128 v[64:67], v49 offset:768\nds_read_b128 v[68:71], v49 offset:1024\ns_branch .LA1q%=\n.LB%=:\ns_cmp_eq_u32 %[w], 2\ns_cbranch_scc0 .LC%=\ns_mov_b32 %[so], 0\n"
        "buffer_load_dwordx4 v[32:35], %[wa], %[rs], %[so] offen offset:0 sc1\nbuffer_load_dwordx4 v[36:39], %[wa], %[rs], %[so] offen offset:1024 sc1\n"
        "buffer_load_dwordx4 v[40:43], %[wa], %[rs], %[so] offen offset:2048 sc1\nbuffer_load_dwordx4 v[44:47], %[wa], %[rs], %[so] offen offset:3072 sc1\ns_mov_b32 %[so], 15360\n"
        "buffer_load_dwordx4 v[48:51], %[wa], %[rs], %[so] offen offset:0 sc1\nbuffer_load_dwordx4 v[52:55], %[wa], %[rs], %[so] offen offset:1024 sc1\n"
        "buffer_load_dwordx4 v[56:59], %[wa], %[rs], %[so] offen offset:2048 sc1\nbuffer_load_dwordx4 v[60:63], %[wa], %[rs], %[so] offen offset:3072 sc1\ns_mov_b32 %[so], 30720\n"
        "buffer_load_dwordx4 v[64:67], %[wa], %[rs], %[so] offen offset:0 sc1\nbuffer_load_dwordx4 v[68:71], %[wa], %[rs], %[so] offen offset:1024 sc1\n"
        "buffer_load_dwordx4 v[72:75], %[wa], %[rs], %[so] offen offset:2048 sc1\nbuffer_load_dwordx4 v[76:79], %[wa], %[rs], %[so] offen offset:3072 sc1\ns_mov_b32 %[so], 46080\n"
        "buffer_load_dwordx4 v[80:83], %[wa], %[rs], %[so] offen offset:0 sc1\nbuffer_load_dwordx4 v[84:87], %[wa], %[rs], %[so] offen offset:1024 sc1\n"
        "buffer_load_dwordx4 v[88:91], %[wa], %[rs], %[so] offen offset:2048 sc1\nbuffer_load_dwordx4 v[92:95], %[wa], %[rs], %[so] offen offset:3072 sc1\ns_mov_b32 %[s1], 0x100000\n.LB1w%=:\n"
        "ds_read_b32 %[pv], %[sy] offset:0\ns_waitcnt lgkmcnt(0)\nv_readfirstlane_b32 %[s0], %[pv]\ns_nop 0\ns_cmp_ge_i32 %[s0], 6\ns_cbranch_scc1 .LB1g%=\ns_sleep 1\ns_sub_u32 %[s1], %[s1], 1\n"
        "s_cbranch_scc0 .LB1w%=\n.LB1g%=:\nds_read_b64_tr_b16 v[24:25], %[xa] offset:3168\nds_read_b64_tr_b16 v[26:27], %[xa] offset:3680\nds_read_b64_tr_b16 v[28:29], %[xa] offset:4224\n"
        "ds_read_b64_tr_b16 v[30:31], %[xa] offset:4736\nds_read_b64_tr_b16 v[12:13], %[xa] offset:0\nds_read_b64_tr_b16 v[14:15], %[xa] offset:512\nds_read_b64_tr_b16 v[16:17], %[xa] offset:1056\n"
        "ds_read_b64_tr_b16 v[18:19], %[xa] offset:1568\ns_waitcnt vmcnt(12) lgkmcnt(6)\nv_mfma_f32_16x16x32_bf16 v[96:99], v[32:35], v[24:27], 0\nds_read_b64_tr_b16 v[20:21], %[xa] offset:2112\n"
        "v_mfma_f32_16x16x32_bf16 v[116:119], v[36:39], v[24:27], 0\nds_read_b64_tr_b16 v[22:23], %[xa] offset:2624\nv_mfma_f32_16x16x32_bf16 v[136:139], v[40:43], v[24:27], 0\n"
        "ds_read_b32 %[pv], %[sy] offset:4\nv_mfma_f32_16x16x32_bf16 v[156:159], v[44:47], v[24:27], 0\nds_read_b64_tr_b16 v[24:25], %[xa] offset:8448\ns_waitcnt lgkmcnt(8)\n"
        "v_mfma_f32_16x16x32_bf16 v[100:103], v[32:35], v[28:31], 0\nds_read_b64_tr_b16 v[26:27], %[xa] offset:8960\nv_mfma_f32_16x16x32_bf16 v[120:123], v[36:39], v[28:31], 0\n"
        "v_mfma_f32_16x16x32_bf16 v[140:143], v[40:43], v[28:31], 0\nv_mfma_f32_16x16x32_bf16 v[160:163], v[44:47], v[28:31], 0\nds_read_b64_tr_b16 v[28:29], %[xa] offset:9504\ns_waitcnt lgkmcnt(8)\n"
        "v_mfma_f32_16x16x32_bf16 v[104:107], v[36:39], v[12:15], 0\nds_read_b64_tr_b16 v[30:31], %[xa] offset:10016\nv_mfma_f32_16x16x32_bf16 v[124:127], v[40:43], v[12:15], 0\n"
        "v_mfma_f32_16x16x32_bf16 v[144:147], v[44:47], v[12:15], 0\nds_read_b64_tr_b16 v[12:13], %[xa] offset:5280\ns_waitcnt lgkmcnt(8)\nv_mfma_f32_16x16x32_bf16 v[108:111], v[36:39], v[16:19], 0\n"
        "ds_read_b64_tr_b16 v[14:15], %[xa] offset:5792\nv_mfma_f32_16x16x32_bf16 v[128:131], v[40:43], v[16:19], 0\nv_mfma_f32_16x16x32_bf16 v[148:151], v[44:47], v[16:19], 0\n"
        "ds_read_b64_tr_b16 v[16:17], %[xa] offset:6336\ns_waitcnt lgkmcnt(8)\nv_mfma_f32_16x16x32_bf16 v[112:115], v[36:39], v[20:23], 0\nds_read_b64_tr_b16 v[18:19], %[xa] offset:6848\n"
        "v_mfma_f32_16x16x32_bf16 v[132:135], v[40:43], v[20:23], 0\nv_mfma_f32_16x16x32_bf16 v[152:155], v[44:47], v[20:23], 0\n.LB1l%=:\ns_waitcnt vmcnt(8) lgkmcnt(6)\n"
        "v_mfma_f32_16x16x32_bf16 v[96:99], v[48:51], v[24:27], v[96:99]\ns_addk_i32 %[so], 15360\nv_readfirstlane_b32 %[s0], %[pv]\n"
        "v_mfma_f32_16x16x32_bf16 v[116:119], v[52:55], v[24:27], v[116:119]\ns_cmp_ge_i32 %[s0], 6\nds_read_b64_tr_b16 v[20:21], %[xa] offset:7392\n"
        "v_mfma_f32_16x16x32_bf16 v[136:139], v[56:59], v[24:27], v[136:139]\ns_cbranch_scc0 .LB1s0_%=\n.LB1r0_%=:\nds_read_b64_tr_b16 v[22:23], %[xa] offset:7904\n"
        "v_mfma_f32_16x16x32_bf16 v[156:159], v[60:63], v[24:27], v[156:159]\nds_read_b64_tr_b16 v[24:25], %[xa] offset:13728\ns_waitcnt lgkmcnt(7)\n"
        "v_mfma_f32_16x16x32_bf16 v[100:103], v[48:51], v[28:31], v[100:103]\nds_read_b64_tr_b16 v[26:27], %[xa] offset:14240\nbuffer_load_dwordx4 v[32:35], %[wa], %[rs], %[so] offen offset:0 sc1\n"
        "v_mfma_f32_16x16x32_bf16 v[120:123], v[52:55], v[28:31], v[120:123]\nbuffer_load_dwordx4 v[36:39], %[wa], %[rs], %[so] offen offset:1024 sc1\n"
        "v_mfma_f32_16x16x32_bf16 v[140:143], v[56:59], v[28:31], v[140:143]\nv_mfma_f32_16x16x32_bf16 v[160:163], v[60:63], v[28:31], v[160:163]\nds_read_b64_tr_b16 v[28:29], %[xa] offset:14784\n"
        "s_waitcnt lgkmcnt(7)\nv_mfma_f32_16x16x32_bf16 v[104:107], v[52:55], v[12:15], v[104:107]\nds_read_b64_tr_b16 v[30:31], %[xa] offset:15296\n"
        "v_mfma_f32_16x16x32_bf16 v[124:127], v[56:59], v[12:15], v[124:127]\nbuffer_load_dwordx4 v[40:43], %[wa], %[rs], %[so] offen offset:2048 sc1\n"
        "v_mfma_f32_16x16x32_bf16 v[144:147], v[60:63], v[12:15], v[144:147]\nds_read_b64_tr_b16 v[12:13], %[xa] offset:10560\ns_waitcnt lgkmcnt(7)\n"
        "v_mfma_f32_16x16x32_bf16 v[108:111], v[52:55], v[16:19], v[108:111]\nds_read_b64_tr_b16 v[14:15], %[xa] offset:11072\nv_mfma_f32_16x16x32_bf16 v[128:131], v[56:59], v[16:19], v[128:131]\n"
        "buffer_load_dwordx4 v[44:47], %[wa], %[rs], %[so] offen offset:3072 sc1\nv_mfma_f32_16x16x32_bf16 v[148:151], v[60:63], v[16:19], v[148:151]\nds_read_b64_tr_b16 v[16:17], %[xa] offset:11616\n"
        "s_waitcnt lgkmcnt(7)\nv_mfma_f32_16x16x32_bf16 v[112:115], v[52:55], v[20:23], v[112:115]\nv_add_u32 %[sy], 4, %[sy]\nds_read_b64_tr_b16 v[18:19], %[xa] offset:12128\n"
        "v_mfma_f32_16x16x32_bf16 v[132:135], v[56:59], v[20:23], v[132:135]\nds_write_b32 %[pg], %[sy]\nv_mfma_f32_16x16x32_bf16 v[152:155], v[60:63], v[20:23], v[152:155]\n"
        "s_waitcnt vmcnt(8) lgkmcnt(7)\nv_mfma_f32_16x16x32_bf16 v[96:99], v[64:67], v[24:27], v[96:99]\ns_addk_i32 %[so], 15360\nds_read_b64_tr_b16 v[20:21], %[xa] offset:12672\n"
        "v_mfma_f32_16x16x32_bf16 v[116:119], v[68:71], v[24:27], v[116:119]\nds_read_b64_tr_b16 v[22:23], %[xa] offset:13184\nv_mfma_f32_16x16x32_bf16 v[136:139], v[72:75], v[24:27], v[136:139]\n"
        "ds_read_b32 %[pv], %[sy] offset:4\nbuffer_load_dwordx4 v[48:51], %[wa], %[rs], %[so] offen offset:0 sc1\nv_mfma_f32_16x16x32_bf16 v[156:159], v[76:79], v[24:27], v[156:159]\n"
        "ds_read_b64_tr_b16 v[24:25], %[xa] offset:19008\ns_waitcnt lgkmcnt(9)\nv_mfma_f32_16x16x32_bf16 v[100:103], v[64:67], v[28:31], v[100:103]\nds_read_b64_tr_b16 v[26:27], %[xa] offset:19520\n"
        "v_mfma_f32_16x16x32_bf16 v[120:123], v[68:71], v[28:31], v[120:123]\nbuffer_load_dwordx4 v[52:55], %[wa], %[rs], %[so] offen offset:1024 sc1\n"
        "v_mfma_f32_16x16x32_bf16 v[140:143], v[72:75], v[28:31], v[140:143]\nv_mfma_f32_16x16x32_bf16 v[160:163], v[76:79], v[28:31], v[160:163]\nds_read_b64_tr_b16 v[28:29], %[xa] offset:20064\n"
        "s_waitcnt lgkmcnt(9)\nv_mfma_f32_16x16x32_bf16 v[104:107], v[68:71], v[12:15], v[104:107]\nds_read_b64_tr_b16 v[30:31], %[xa] offset:20576\n"
        "v_mfma_f32_16x16x32_bf16 v[124:127], v[72:75], v[12:15], v[124:127]\nbuffer_load_dwordx4 v[56:59], %[wa], %[rs], %[so] offen offset:2048 sc1\n"
        "v_mfma_f32_16x16x32_bf16 v[144:147], v[76:79], v[12:15], v[144:147]\nds_read_b64_tr_b16 v[12:13], %[xa] offset:15840\ns_waitcnt lgkmcnt(9)\n"
        "v_mfma_f32_16x16x32_bf16 v[108:111], v[68:71], v[16:19], v[108:111]\nds_read_b64_tr_b16 v[14:15], %[xa] offset:16352\nv_mfma_f32_16x16x32_bf16 v[128:131], v[72:75], v[16:19], v[128:131]\n"
        "buffer_load_dwordx4 v[60:63], %[wa], %[rs], %[so] offen offset:3072 sc1\nv_mfma_f32_16x16x32_bf16 v[148:151], v[76:79], v[16:19], v[148:151]\nds_read_b64_tr_b16 v[16:17], %[xa] offset:16896\n"
        "s_waitcnt lgkmcnt(8)\nv_mfma_f32_16x16x32_bf16 v[112:115], v[68:71], v[20:23], v[112:115]\nds_read_b64_tr_b16 v[18:19], %[xa] offset:17408\n"
        "v_mfma_f32_16x16x32_bf16 v[132:135], v[72:75], v[20:23], v[132:135]\nv_mfma_f32_16x16x32_bf16 v[152:155], v[76:79], v[20:23], v[152:155]\ns_waitcnt vmcnt(8) lgkmcnt(6)\n"
        "v_mfma_f32_16x16x32_bf16 v[96:99], v[80:83], v[24:27], v[96:99]\ns_addk_i32 %[so], 15360\nv_readfirstlane_b32 %[s0], %[pv]\n"
        "v_mfma_f32_16x16x32_bf16 v[116:119], v[84:87], v[24:27], v[116:119]\ns_cmp_ge_i32 %[s0], 6\nds_read_b64_tr_b16 v[20:21], %[xa] offset:17952\n"
        "v_mfma_f32_16x16x32_bf16 v[136:139], v[88:91], v[24:27], v[136:139]\ns_cbranch_scc0 .LB1s1_%=\n.LB1r1_%=:\nds_read_b64_tr_b16 v[22:23], %[xa] offset:18464\n"
        "v_mfma_f32_16x16x32_bf16 v[156:159], v[92:95], v[24:27], v[156:159]\nds_read_b64_tr_b16 v[24:25], %[xa] offset:24288\ns_waitcnt lgkmcnt(7)\n"
        "v_mfma_f32_16x16x32_bf16 v[100:103], v[80:83], v[28:31], v[100:103]\nds_read_b64_tr_b16 v[26:27], %[xa] offset:24800\nbuffer_load_dwordx4 v[64:67], %[wa], %[rs], %[so] offen offset:0 sc1\n"
        "v_mfma_f32_16x16x32_bf16 v[120:123], v[84:87], v[28:31], v[120:123]\nbuffer_load_dwordx4 v[68:71], %[wa], %[rs], %[so] offen offset:1024 sc1\n"
        "v_mfma_f32_16x16x32_bf16 v[140:143], v[88:91], v[28:31], v[140:143]\nv_mfma_f32_16x16x32_bf16 v[160:163], v[92:95], v[28:31], v[160:163]\nds_read_b64_tr_b16 v[28:29], %[xa] offset:25344\n"
        "s_waitcnt lgkmcnt(7)\nv_mfma_f32_16x16x32_bf16 v[104:107], v[84:87], v[12:15], v[104:107]\nds_read_b64_tr_b16 v[30:31], %[xa] offset:25856\n"
        "v_mfma_f32_16x16x32_bf16 v[124:127], v[88:91], v[12:15], v[124:127]\nbuffer_load_dwordx4 v[72:75], %[wa], %[rs], %[so] offen offset:2048 sc1\n"
        "v_mfma_f32_16x16x32_bf16 v[144:147], v[92:95], v[12:15], v[144:147]\nds_read_b64_tr_b16 v[12:13], %[xa] offset:21120\ns_waitcnt lgkmcnt(7)\n"
        "v_mfma_f32_16x16x32_bf16 v[108:111], v[84:87], v[16:19], v[108:111]\nds_read_b64_tr_b16 v[14:15], %[xa] offset:21632\nv_mfma_f32_16x16x32_bf16 v[128:131], v[88:91], v[16:19], v[128:131]\n"
        "buffer_load_dwordx4 v[76:79], %[wa], %[rs], %[so] offen offset:3072 sc1\nv_mfma_f32_16x16x32_bf16 v[148:151], v[92:95], v[16:19], v[148:151]\nds_read_b64_tr_b16 v[16:17], %[xa] offset:22176\n"
        "s_waitcnt lgkmcnt(7)\nv_mfma_f32_16x16x32_bf16 v[112:115], v[84:87], v[20:23], v[112:115]\nv_add_u32 %[sy], 4, %[sy]\nds_read_b64_tr_b16 v[18:19], %[xa] offset:22688\n"
        "v_mfma_f32_16x16x32_bf16 v[132:135], v[88:91], v[20:23], v[132:135]\nds_write_b32 %[pg], %[sy]\nv_mfma_f32_16x16x32_bf16 v[152:155], v[92:95], v[20:23], v[152:155]\n"
        "s_waitcnt vmcnt(8) lgkmcnt(7)\nv_mfma_f32_16x16x32_bf16 v[96:99], v[32:35], v[24:27], v[96:99]\ns_addk_i32 %[so], 15360\nds_read_b64_tr_b16 v[20:21], %[xa] offset:23232\n"
        "v_mfma_f32_16x16x32_bf16 v[116:119], v[36:39], v[24:27], v[116:119]\nds_read_b64_tr_b16 v[22:23], %[xa] offset:23744\nv_mfma_f32_16x16x32_bf16 v[136:139], v[40:43], v[24:27], v[136:139]\n"
        "ds_read_b32 %[pv], %[sy] offset:4\nbuffer_load_dwordx4 v[80:83], %[wa], %[rs], %[so] offen offset:0 sc1\nv_mfma_f32_16x16x32_bf16 v[156:159], v[44:47], v[24:27], v[156:159]\n"
        "ds_read_b64_tr_b16 v[24:25], %[xa] offset:29568\ns_waitcnt lgkmcnt(9)\nv_mfma_f32_16x16x32_bf16 v[100:103], v[32:35], v[28:31], v[100:103]\nds_read_b64_tr_b16 v[26:27], %[xa] offset:30080\n"
        "v_mfma_f32_16x16x32_bf16 v[120:123], v[36:39], v[28:31], v[120:123]\nbuffer_load_dwordx4 v[84:87], %[wa], %[rs], %[so] offen offset:1024 sc1\n"
        "v_mfma_f32_16x16x32_bf16 v[140:143], v[40:43], v[28:31], v[140:143]\nv_mfma_f32_16x16x32_bf16 v[160:163], v[44:47], v[28:31], v[160:163]\nds_read_b64_tr_b16 v[28:29], %[xa] offset:30624\n"
        "s_waitcnt lgkmcnt(9)\nv_mfma_f32_16x16x32_bf16 v[104:107], v[36:39], v[12:15], v[104:107]\nds_read_b64_tr_b16 v[30:31], %[xa] offset:31136\n"
        "v_mfma_f32_16x16x32_bf16 v[124:127], v[40:43], v[12:15], v[124:127]\nbuffer_load_dwordx4 v[88:91], %[wa], %[rs], %[so] offen offset:2048 sc1\n"
        "v_mfma_f32_16x16x32_bf16 v[144:147], v[44:47], v[12:15], v[144:147]\nds_read_b64_tr_b16 v[12:13], %[xa] offset:26400\ns_waitcnt lgkmcnt(9)\n"
        "v_mfma_f32_16x16x32_bf16 v[108:111], v[36:39], v[16:19], v[108:111]\nds_read_b64_tr_b16 v[14:15], %[xa] offset:26912\nv_mfma_f32_16x16x32_bf16 v[128:131], v[40:43], v[16:19], v[128:131]\n"
        "buffer_load_dwordx4 v[92:95], %[wa], %[rs], %[so] offen offset:3072 sc1\nv_mfma_f32_16x16x32_bf16 v[148:151], v[44:47], v[16:19], v[148:151]\nds_read_b64_tr_b16 v[16:17], %[xa] offset:27456\n"
        "s_waitcnt lgkmcnt(8)\nv_mfma_f32_16x16x32_bf16 v[112:115], v[36:39], v[20:23], v[112:115]\nds_read_b64_tr_b16 v[18:19], %[xa] offset:27968\n"
        "v_mfma_f32_16x16x32_bf16 v[132:135], v[40:43], v[20:23], v[132:135]\ns_cmp_lg_u32 %[so], 291840\nv_mfma_f32_16x16x32_bf16 v[152:155], v[44:47], v[20:23], v[152:155]\n"
        "v_add_u32 %[xa], 21120, %[xa]\ns_cbranch_scc1 .LB1l%=\ns_waitcnt vmcnt(8) lgkmcnt(6)\nv_mfma_f32_16x16x32_bf16 v[96:99], v[48:51], v[24:27], v[96:99]\nv_readfirstlane_b32 %[s0], %[pv]\n"
        "ds_read_b64_tr_b16 v[20:21], %[xa] offset:7392\nv_mfma_f32_16x16x32_bf16 v[116:119], v[52:55], v[24:27], v[116:119]\ns_cmp_ge_i32 %[s0], 6\nds_read_b64_tr_b16 v[22:23], %[xa] offset:7904\n"
        "v_mfma_f32_16x16x32_bf16 v[136:139], v[56:59], v[24:27], v[136:139]\ns_cbranch_scc0 .LB1s2_%=\n.LB1r2_%=:\nv_mfma_f32_16x16x32_bf16 v[156:159], v[60:63], v[24:27], v[156:159]\n"
        "ds_read_b64_tr_b16 v[24:25], %[xa] offset:13728\ns_waitcnt lgkmcnt(7)\nv_mfma_f32_16x16x32_bf16 v[100:103], v[48:51], v[28:31], v[100:103]\nds_read_b64_tr_b16 v[26:27], %[xa] offset:14240\n"
        "v_mfma_f32_16x16x32_bf16 v[120:123], v[52:55], v[28:31], v[120:123]\nv_mfma_f32_16x16x32_bf16 v[140:143], v[56:59], v[28:31], v[140:143]\n"
        "v_mfma_f32_16x16x32_bf16 v[160:163], v[60:63], v[28:31], v[160:163]\nds_read_b64_tr_b16 v[28:29], %[xa] offset:14784\ns_waitcnt lgkmcnt(7)\n"
        "v_mfma_f32_16x16x32_bf16 v[104:107], v[52:55], v[12:15], v[104:107]\nds_read_b64_tr_b16 v[30:31], %[xa] offset:15296\nv_mfma_f32_16x16x32_bf16 v[124:127], v[56:59], v[12:15], v[124:127]\n"
        "v_mfma_f32_16x16x32_bf16 v[144:147], v[60:63], v[12:15], v[144:147]\nds_read_b64_tr_b16 v[12:13], %[xa] offset:10560\ns_waitcnt lgkmcnt(7)\n"
        "v_mfma_f32_16x16x32_bf16 v[108:111], v[52:55], v[16:19], v[108:111]\nds_read_b64_tr_b16 v[14:15], %[xa] offset:11072\nv_mfma_f32_16x16x32_bf16 v[128:131], v[56:59], v[16:19], v[128:131]\n"
        "v_mfma_f32_16x16x32_bf16 v[148:151], v[60:63], v[16:19], v[148:151]\nds_read_b64_tr_b16 v[16:17], %[xa] offset:11616\ns_waitcnt lgkmcnt(7)\n"
        "v_mfma_f32_16x16x32_bf16 v[112:115], v[52:55], v[20:23], v[112:115]\nv_add_u32 %[sy], 4, %[sy]\nds_read_b64_tr_b16 v[18:19], %[xa] offset:12128\n"
        "v_mfma_f32_16x16x32_bf16 v[132:135], v[56:59], v[20:23], v[132:135]\nv_mfma_f32_16x16x32_bf16 v[152:155], v[60:63], v[20:23], v[152:155]\ns_waitcnt vmcnt(4) lgkmcnt(6)\n"
        "v_mfma_f32_16x16x32_bf16 v[96:99], v[64:67], v[24:27], v[96:99]\nds_read_b64_tr_b16 v[20:21], %[xa] offset:12672\nv_mfma_f32_16x16x32_bf16 v[116:119], v[68:71], v[24:27], v[116:119]\n"
        "ds_read_b64_tr_b16 v[22:23], %[xa] offset:13184\nv_and_b32 v49, 0xf0, %[wa]\nv_mfma_f32_16x16x32_bf16 v[136:139], v[72:75], v[24:27], v[136:139]\nv_lshrrev_b32 v48, 1, v49\n"
        "v_mfma_f32_16x16x32_bf16 v[156:159], v[76:79], v[24:27], v[156:159]\nds_read_b64_tr_b16 v[24:25], %[xa] offset:19008\nv_mad_u32_u24 v50, v49, 31, %[c2]\ns_waitcnt lgkmcnt(7)\n"
        "v_mfma_f32_16x16x32_bf16 v[100:103], v[64:67], v[28:31], v[100:103]\nds_read_b64_tr_b16 v[26:27], %[xa] offset:19520\nv_add_u32 v48, %[c0], v48\n"
        "v_mfma_f32_16x16x32_bf16 v[120:123], v[68:71], v[28:31], v[120:123]\nv_add_u32 v49, %[c1], v49\nv_mfma_f32_16x16x32_bf16 v[140:143], v[72:75], v[28:31], v[140:143]\n"
        "v_bfe_u32 %[pv], %[wa], 8, 1\nv_mfma_f32_16x16x32_bf16 v[160:163], v[76:79], v[28:31], v[160:163]\nds_read_b64_tr_b16 v[28:29], %[xa] offset:20064\ns_waitcnt lgkmcnt(7)\n"
        "v_mfma_f32_16x16x32_bf16 v[104:107], v[68:71], v[12:15], v[104:107]\nds_read_b64_tr_b16 v[30:31], %[xa] offset:20576\nv_mul_u32_u24 %[pv], 7936, %[pv]\n"
        "v_mfma_f32_16x16x32_bf16 v[124:127], v[72:75], v[12:15], v[124:127]\nv_bfe_u32 v51, %[wa], 9, 1\nv_mfma_f32_16x16x32_bf16 v[144:147], v[76:79], v[12:15], v[144:147]\n"
        "ds_read_b64_tr_b16 v[12:13], %[xa] offset:15840\nv_lshl_add_u32 v51, v51, 4, v50\ns_waitcnt lgkmcnt(7)\nv_mfma_f32_16x16x32_bf16 v[108:111], v[68:71], v[16:19], v[108:111]\n"
        "ds_read_b64_tr_b16 v[14:15], %[xa] offset:16352\nv_add_u32 v51, v51, %[pv]\nv_mfma_f32_16x16x32_bf16 v[128:131], v[72:75], v[16:19], v[128:131]\nv_bfe_u32 %[pv], %[wa], 8, 2\n"
        "v_mfma_f32_16x16x32_bf16 v[148:151], v[76:79], v[16:19], v[148:151]\nds_read_b64_tr_b16 v[16:17], %[xa] offset:16896\nv_lshl_add_u32 v50, %[pv], 3, v50\ns_waitcnt lgkmcnt(7)\n"
        "v_mfma_f32_16x16x32_bf16 v[112:115], v[68:71], v[20:23], v[112:115]\nds_read_b64_tr_b16 v[18:19], %[xa] offset:17408\nv_mfma_f32_16x16x32_bf16 v[132:135], v[72:75], v[20:23], v[132:135]\n"
        "v_mfma_f32_16x16x32_bf16 v[152:155], v[76:79], v[20:23], v[152:155]\ns_waitcnt vmcnt(0) lgkmcnt(6)\nv_mfma_f32_16x16x32_bf16 v[96:99], v[80:83], v[24:27], v[96:99]\n"
        "ds_read_b64_tr_b16 v[20:21], %[xa] offset:17952\nv_mfma_f32_16x16x32_bf16 v[116:119], v[84:87], v[24:27], v[116:119]\nds_read_b64_tr_b16 v[22:23], %[xa] offset:18464\n"
        "v_mfma_f32_16x16x32_bf16 v[136:139], v[88:91], v[24:27], v[136:139]\nv_mfma_f32_16x16x32_bf16 v[156:159], v[92:95], v[24:27], v[156:159]\ns_waitcnt lgkmcnt(6)\n"
        "v_mfma_f32_16x16x32_bf16 v[100:103], v[80:83], v[28:31], v[100:103]\nv_mfma_f32_16x16x32_bf16 v[120:123], v[84:87], v[28:31], v[120:123]\n"
        "v_mfma_f32_16x16x32_bf16 v[140:143], v[88:91], v[28:31], v[140:143]\nds_read_b32 %[pv], %[sy] offset:4\nds_read_b64 v[32:33], v48 offset:896\n"
        "v_mfma_f32_16x16x32_bf16 v[160:163], v[92:95], v[28:31], v[160:163]\nds_read_b64 v[36:37], v48 offset:512\ns_waitcnt lgkmcnt(7)\n"
        "v_mfma_f32_16x16x32_bf16 v[104:107], v[84:87], v[12:15], v[104:107]\nds_read_b64 v[40:41], v48 offset:1664\nds_read_b64 v[44:45], v48 offset:1792\n"
        "v_mfma_f32_16x16x32_bf16 v[124:127], v[88:91], v[12:15], v[124:127]\nds_read_b128 v[52:55], v49 offset:0\nv_mfma_f32_16x16x32_bf16 v[144:147], v[92:95], v[12:15], v[144:147]\n"
        "ds_read_b128 v[56:59], v49 offset:256\nds_read_b128 v[60:63], v49 offset:512\ns_waitcnt lgkmcnt(10)\nv_mfma_f32_16x16x32_bf16 v[108:111], v[84:87], v[16:19], v[108:111]\n"
        "ds_read_b128 v[64:67], v49 offset:768\nv_mfma_f32_16x16x32_bf16 v[128:131], v[88:91], v[16:19], v[128:131]\nds_read_b128 v[68:71], v49 offset:1024\n"
        "v_mfma_f32_16x16x32_bf16 v[148:151], v[92:95], v[16:19], v[148:151]\ns_waitcnt lgkmcnt(10)\nv_mfma_f32_16x16x32_bf16 v[112:115], v[84:87], v[20:23], v[112:115]\ns_setprio %[pfs]\n"
        "v_mfma_f32_16x16x32_bf16 v[132:135], v[88:91], v[20:23], v[132:135]\nv_mfma_f32_16x16x32_bf16 v[152:155], v[92:95], v[20:23], v[152:155]\ns_mov_b32 %[s1], 0x100000\n.LB1q%=:\n"
        "s_waitcnt lgkmcnt(0)\nv_readfirstlane_b32 %[s0], %[pv]\nv_add_u32 %[pv], 8, %[sy]\ns_cmp_ge_i32 %[s0], %[nst]\ns_cbranch_scc0 .LB1t%=\n.LB1u%=:\n"
        "v_mfma_f32_16x16x32_bf16 v[96:99], v[32:35], v[64:67], v[96:99]\nv_mfma_f32_16x16x32_bf16 v[100:103], v[32:35], v[68:71], v[100:103]\n"
        "v_mfma_f32_16x16x32_bf16 v[116:119], v[36:39], v[64:67], v[116:119]\nv_mfma_f32_16x16x32_bf16 v[120:123], v[36:39], v[68:71], v[120:123]\n"
        "v_mfma_f32_16x16x32_bf16 v[104:107], v[36:39], v[52:55], v[104:107]\nv_cvt_pk_bf16_f32 v96, v96, v97\nv_cvt_pk_bf16_f32 v97, v98, v99\nv_cvt_pk_bf16_f32 v98, v100, v101\n"
        "v_cvt_pk_bf16_f32 v99, v102, v103\nv_mfma_f32_16x16x32_bf16 v[108:111], v[36:39], v[56:59], v[108:111]\nv_permlane16_swap_b32 v96, v98\nv_permlane16_swap_b32 v97, v99\n"
        "ds_write_b128 v51, v[96:99] offset:24032\nv_mfma_f32_16x16x32_bf16 v[112:115], v[36:39], v[60:63], v[112:115]\nv_cvt_pk_bf16_f32 v116, v116, v117\nv_cvt_pk_bf16_f32 v117, v118, v119\n"
        "v_cvt_pk_bf16_f32 v118, v120, v121\nv_cvt_pk_bf16_f32 v119, v122, v123\nv_mfma_f32_16x16x32_bf16 v[136:139], v[40:43], v[64:67], v[136:139]\nv_permlane16_swap_b32 v116, v118\n"
        "v_permlane16_swap_b32 v117, v119\nds_write_b128 v51, v[116:119] offset:23936\nv_cvt_pk_bf16_f32 v104, v104, v105\nv_mfma_f32_16x16x32_bf16 v[140:143], v[40:43], v[68:71], v[140:143]\n"
        "v_cvt_pk_bf16_f32 v105, v106, v107\nds_write_b64 v50, v[104:105] offset:128\nv_mfma_f32_16x16x32_bf16 v[156:159], v[44:47], v[64:67], v[156:159]\nv_cvt_pk_bf16_f32 v108, v108, v109\n"
        "v_cvt_pk_bf16_f32 v109, v110, v111\nv_cvt_pk_bf16_f32 v110, v112, v113\nv_cvt_pk_bf16_f32 v111, v114, v115\nv_mfma_f32_16x16x32_bf16 v[160:163], v[44:47], v[68:71], v[160:163]\n"
        "v_permlane16_swap_b32 v108, v110\nv_permlane16_swap_b32 v109, v111\nds_write_b128 v51, v[108:111] offset:8064\nds_write_b32 %[pg], %[pv]\n"
        "v_mfma_f32_16x16x32_bf16 v[124:127], v[40:43], v[52:55], v[124:127]\nv_add_u32 %[sy], 12, %[sy]\nv_cvt_pk_bf16_f32 v136, v136, v137\nv_cvt_pk_bf16_f32 v137, v138, v139\n"
        "v_cvt_pk_bf16_f32 v138, v140, v141\nv_mfma_f32_16x16x32_bf16 v[144:147], v[44:47], v[52:55], v[144:147]\nv_cvt_pk_bf16_f32 v139, v142, v143\ns_nop 0\nv_permlane16_swap_b32 v136, v138\n"
        "v_permlane16_swap_b32 v137, v139\nv_mfma_f32_16x16x32_bf16 v[128:131], v[40:43], v[56:59], v[128:131]\nds_write_b128 v51, v[136:139] offset:24224\nv_cvt_pk_bf16_f32 v156, v156, v157\n"
        "v_cvt_pk_bf16_f32 v157, v158, v159\nv_cvt_pk_bf16_f32 v158, v160, v161\nv_mfma_f32_16x16x32_bf16 v[132:135], v[40:43], v[60:63], v[132:135]\nv_cvt_pk_bf16_f32 v159, v162, v163\ns_nop 0\n"
        "v_permlane16_swap_b32 v156, v158\nv_permlane16_swap_b32 v157, v159\nv_mfma_f32_16x16x32_bf16 v[148:151], v[44:47], v[56:59], v[148:151]\nds_write_b128 v51, v[156:159] offset:24256\n"
        "v_cvt_pk_bf16_f32 v124, v124, v125\nv_cvt_pk_bf16_f32 v125, v126, v127\nds_write_b64 v50, v[124:125] offset:416\nv_mfma_f32_16x16x32_bf16 v[152:155], v[44:47], v[60:63], v[152:155]\n"
        "v_cvt_pk_bf16_f32 v144, v144, v145\nv_cvt_pk_bf16_f32 v145, v146, v147\nds_write_b64 v50, v[144:145] offset:448\ns_nop 3\nv_cvt_pk_bf16_f32 v128, v128, v129\n"
        "v_cvt_pk_bf16_f32 v129, v130, v131\nv_cvt_pk_bf16_f32 v130, v132, v133\nv_cvt_pk_bf16_f32 v131, v134, v135\ns_nop 0\nv_permlane16_swap_b32 v128, v130\nv_permlane16_swap_b32 v129, v131\n"
        "ds_write_b128 v51, v[128:131] offset:8352\nv_cvt_pk_bf16_f32 v148, v148, v149\nv_cvt_pk_bf16_f32 v149, v150, v151\nv_cvt_pk_bf16_f32 v150, v152, v153\nv_cvt_pk_bf16_f32 v151, v154, v155\n"
        "s_nop 0\nv_permlane16_swap_b32 v148, v150\nv_permlane16_swap_b32 v149, v151\nds_write_b128 v51, v[148:151] offset:8384\nds_write_b32 %[pg], %[sy]\ns_branch .LE%=\n.LB1s0_%=:\n"
        "s_mov_b32 %[s1], 0x100000\n.LB1p0_%=:\nds_read_b32 %[pv], %[sy] offset:4\ns_waitcnt lgkmcnt(0)\nv_readfirstlane_b32 %[s0], %[pv]\ns_nop 0\ns_cmp_ge_i32 %[s0], 6\ns_cbranch_scc1 .LB1r0_%=\n"
        "s_sleep 1\ns_sub_u32 %[s1], %[s1], 1\ns_cbranch_scc0 .LB1p0_%=\ns_branch .LB1r0_%=\n.LB1s1_%=:\ns_mov_b32 %[s1], 0x100000\n.LB1p1_%=:\nds_read_b32 %[pv], %[sy] offset:4\ns_waitcnt lgkmcnt(0)\n"
        "v_readfirstlane_b32 %[s0], %[pv]\ns_nop 0\ns_cmp_ge_i32 %[s0], 6\ns_cbranch_scc1 .LB1r1_%=\ns_sleep 1\ns_sub_u32 %[s1], %[s1], 1\ns_cbranch_scc0 .LB1p1_%=\ns_branch .LB1r1_%=\n.LB1s2_%=:\n"
        "s_mov_b32 %[s1], 0x100000\n.LB1p2_%=:\nds_read_b32 %[pv], %[sy] offset:4\ns_waitcnt lgkmcnt(0)\nv_readfirstlane_b32 %[s0], %[pv]\ns_nop 0\ns_cmp_ge_i32 %[s0], 6\ns_cbranch_scc1 .LB1r2_%=\n"
        "s_sleep 1\ns_sub_u32 %[s1], %[s1], 1\ns_cbranch_scc0 .LB1p2_%=\ns_branch .LB1r2_%=\n.LB1t%=:\ns_sub_u32 %[s1], %[s1], 1\ns_cbranch_scc1 .LB1u%=\ns_sleep 1\nds_read_b32 %[pv], %[sy] offset:4\n"
        "ds_read_b64 v[32:33], v48 offset:896\nds_read_b64 v[36:37], v48 offset:512\nds_read_b64 v[40:41], v48 offset:1664\nds_read_b64 v[44:45], v48 offset:1792\nds_read_b128 v[52:55], v49 offset:0\n"
        "ds_read_b128 v[56:59], v49 offset:256\nds_read_b128 v[60:63], v49 offset:512\nds_read_b128 v[64:67], v49 offset:768\nds_read_b128 v[68:71], v49 offset:1024\ns_branch .LB1q%=\n.LC%=:\n"
        "s_mov_b32 %[so], 0\nbuffer_load_dwordx4 v[32:35], %[wa], %[rs], %[so] offen offset:3072 sc1\nbuffer_load_dwordx4 v[36:39], %[wa], %[rs], %[so] offen offset:0 sc1\n"
        "buffer_load_dwordx4 v[40:43], %[wa], %[rs], %[so] offen offset:1024 sc1\nbuffer_load_dwordx4 v[44:47], %[wa], %[rs], %[so] offen offset:2048 sc1\ns_mov_b32 %[so], 15360\n"
        "buffer_load_dwordx4 v[48:51], %[wa], %[rs], %[so] offen offset:3072 sc1\nbuffer_load_dwordx4 v[52:55], %[wa], %[rs], %[so] offen offset:0 sc1\n"
        "buffer_load_dwordx4 v[56:59], %[wa], %[rs], %[so] offen offset:1024 sc1\nbuffer_load_dwordx4 v[60:63], %[wa], %[rs], %[so] offen offset:2048 sc1\ns_mov_b32 %[so], 30720\n"
        "buffer_load_dwordx4 v[64:67], %[wa], %[rs], %[so] offen offset:3072 sc1\nbuffer_load_dwordx4 v[68:71], %[wa], %[rs], %[so] offen offset:0 sc1\n"
        "buffer_load_dwordx4 v[72:75], %[wa], %[rs], %[so] offen offset:1024 sc1\nbuffer_load_dwordx4 v[76:79], %[wa], %[rs], %[so] offen offset:2048 sc1\ns_mov_b32 %[so], 46080\n"
        "buffer_load_dwordx4 v[80:83], %[wa], %[rs], %[so] offen offset:3072 sc1\nbuffer_load_dwordx4 v[84:87], %[wa], %[rs], %[so] offen offset:0 sc1\n"
        "buffer_load_dwordx4 v[88:91], %[wa], %[rs], %[so] offen offset:1024 sc1\nbuffer_load_dwordx4 v[92:95], %[wa], %[rs], %[so] offen offset:2048 sc1\ns_mov_b32 %[s1], 0x100000\n.LC1w%=:\n"
        "ds_read_b32 %[pv], %[sy] offset:0\ns_waitcnt lgkmcnt(0)\nv_readfirstlane_b32 %[s0], %[pv]\ns_nop 0\ns_cmp_ge_i32 %[s0], 6\ns_cbranch_scc1 .LC1g%=\ns_sleep 1\ns_sub_u32 %[s1], %[s1], 1\n"
        "s_cbranch_scc0 .LC1w%=\n.LC1g%=:\nds_read_b64_tr_b16 v[16:17], %[xa] offset:1056\nds_read_b64_tr_b16 v[18:19], %[xa] offset:1568\nds_read_b64_tr_b16 v[20:21], %[xa] offset:2112\n"
        "ds_read_b64_tr_b16 v[22:23], %[xa] offset:2624\nds_read_b64_tr_b16 v[12:13], %[xa] offset:0\nds_read_b64_tr_b16 v[14:15], %[xa] offset:512\nds_read_b64_tr_b16 v[24:25], %[xa] offset:3168\n"
        "ds_read_b64_tr_b16 v[26:27], %[xa] offset:3680\ns_waitcnt vmcnt(12) lgkmcnt(6)\nv_mfma_f32_16x16x32_bf16 v[96:99], v[32:35], v[16:19], 0\nds_read_b64_tr_b16 v[28:29], %[xa] offset:4224\n"
        "v_mfma_f32_16x16x32_bf16 v[108:111], v[36:39], v[16:19], 0\nds_read_b64_tr_b16 v[30:31], %[xa] offset:4736\nv_mfma_f32_16x16x32_bf16 v[128:131], v[40:43], v[16:19], 0\n"
        "ds_read_b32 %[pv], %[sy] offset:4\nv_mfma_f32_16x16x32_bf16 v[148:151], v[44:47], v[16:19], 0\nds_read_b64_tr_b16 v[16:17], %[xa] offset:6336\ns_waitcnt lgkmcnt(8)\n"
        "v_mfma_f32_16x16x32_bf16 v[100:103], v[32:35], v[20:23], 0\nds_read_b64_tr_b16 v[18:19], %[xa] offset:6848\nv_mfma_f32_16x16x32_bf16 v[112:115], v[36:39], v[20:23], 0\n"
        "v_mfma_f32_16x16x32_bf16 v[132:135], v[40:43], v[20:23], 0\nv_mfma_f32_16x16x32_bf16 v[152:155], v[44:47], v[20:23], 0\nds_read_b64_tr_b16 v[20:21], %[xa] offset:7392\ns_waitcnt lgkmcnt(8)\n"
        "v_mfma_f32_16x16x32_bf16 v[104:107], v[36:39], v[12:15], 0\nds_read_b64_tr_b16 v[22:23], %[xa] offset:7904\nv_mfma_f32_16x16x32_bf16 v[124:127], v[40:43], v[12:15], 0\n"
        "v_mfma_f32_16x16x32_bf16 v[144:147], v[44:47], v[12:15], 0\nds_read_b64_tr_b16 v[12:13], %[xa] offset:5280\ns_waitcnt lgkmcnt(8)\nv_mfma_f32_16x16x32_bf16 v[116:119], v[36:39], v[24:27], 0\n"
        "ds_read_b64_tr_b16 v[14:15], %[xa] offset:5792\nv_mfma_f32_16x16x32_bf16 v[136:139], v[40:43], v[24:27], 0\nv_mfma_f32_16x16x32_bf16 v[156:159], v[44:47], v[24:27], 0\n"
        "ds_read_b64_tr_b16 v[24:25], %[xa] offset:8448\ns_waitcnt lgkmcnt(8)\nv_mfma_f32_16x16x32_bf16 v[120:123], v[36:39], v[28:31], 0\nds_read_b64_tr_b16 v[26:27], %[xa] offset:8960\n"
        "v_mfma_f32_16x16x32_bf16 v[140:143], v[40:43], v[28:31], 0\nv_mfma_f32_16x16x32_bf16 v[160:163], v[44:47], v[28:31], 0\n.LC1l%=:\ns_waitcnt vmcnt(8) lgkmcnt(6)\n"
        "v_mfma_f32_16x16x32_bf16 v[96:99], v[48:51], v[16:19], v[96:99]\ns_addk_i32 %[so], 15360\nv_readfirstlane_b32 %[s0], %[pv]\n"
        "v_mfma_f32_16x16x32_bf16 v[108:111], v[52:55], v[16:19], v[108:111]\ns_cmp_ge_i32 %[s0], 6\nds_read_b64_tr_b16 v[28:29], %[xa] offset:9504\n"
        "v_mfma_f32_16x16x32_bf16 v[128:131], v[56:59], v[16:19], v[128:131]\ns_cbranch_scc0 .LC1s0_%=\n.LC1r0_%=:\nds_read_b64_tr_b16 v[30:31], %[xa] offset:10016\n"
        "v_mfma_f32_16x16x32_bf16 v[148:151], v[60:63], v[16:19], v[148:151]\nds_read_b64_tr_b16 v[16:17], %[xa] offset:11616\ns_waitcnt lgkmcnt(7)\n"
        "v_mfma_f32_16x16x32_bf16 v[100:103], v[48:51], v[20:23], v[100:103]\nds_read_b64_tr_b16 v[18:19], %[xa] offset:12128\nbuffer_load_dwordx4 v[32:35], %[wa], %[rs], %[so] offen offset:3072 sc1\n"
        "v_mfma_f32_16x16x32_bf16 v[112:115], v[52:55], v[20:23], v[112:115]\nbuffer_load_dwordx4 v[36:39], %[wa], %[rs], %[so] offen offset:0 sc1\n"
        "v_mfma_f32_16x16x32_bf16 v[132:135], v[56:59], v[20:23], v[132:135]\nv_mfma_f32_16x16x32_bf16 v[152:155], v[60:63], v[20:23], v[152:155]\nds_read_b64_tr_b16 v[20:21], %[xa] offset:12672\n"
        "s_waitcnt lgkmcnt(7)\nv_mfma_f32_16x16x32_bf16 v[104:107], v[52:55], v[12:15], v[104:107]\nds_read_b64_tr_b16 v[22:23], %[xa] offset:13184\n"
        "v_mfma_f32_16x16x32_bf16 v[124:127], v[56:59], v[12:15], v[124:127]\nbuffer_load_dwordx4 v[40:43], %[wa], %[rs], %[so] offen offset:1024 sc1\n"
        "v_mfma_f32_16x16x32_bf16 v[144:147], v[60:63], v[12:15], v[144:147]\nds_read_b64_tr_b16 v[12:13], %[xa] offset:10560\ns_waitcnt lgkmcnt(7)\n"
        "v_mfma_f32_16x16x32_bf16 v[116:119], v[52:55], v[24:27], v[116:119]\nds_read_b64_tr_b16 v[14:15], %[xa] offset:11072\nv_mfma_f32_16x16x32_bf16 v[136:139], v[56:59], v[24:27], v[136:139]\n"
        "buffer_load_dwordx4 v[44:47], %[wa], %[rs], %[so] offen offset:2048 sc1\nv_mfma_f32_16x16x32_bf16 v[156:159], v[60:63], v[24:27], v[156:159]\nds_read_b64_tr_b16 v[24:25], %[xa] offset:13728\n"
        "s_waitcnt lgkmcnt(7)\nv_mfma_f32_16x16x32_bf16 v[120:123], v[52:55], v[28:31], v[120:123]\nv_add_u32 %[sy], 4, %[sy]\nds_read_b64_tr_b16 v[26:27], %[xa] offset:14240\n"
        "v_mfma_f32_16x16x32_bf16 v[140:143], v[56:59], v[28:31], v[140:143]\nds_write_b32 %[pg], %[sy]\nv_mfma_f32_16x16x32_bf16 v[160:163], v[60:63], v[28:31], v[160:163]\n"
        "s_waitcnt vmcnt(8) lgkmcnt(7)\nv_mfma_f32_16x16x32_bf16 v[96:99], v[64:67], v[16:19], v[96:99]\ns_addk_i32 %[so], 15360\nds_read_b64_tr_b16 v[28:29], %[xa] offset:14784\n"
        "v_mfma_f32_16x16x32_bf16 v[108:111], v[68:71], v[16:19], v[108:111]\nds_read_b64_tr_b16 v[30:31], %[xa] offset:15296\nv_mfma_f32_16x16x32_bf16 v[128:131], v[72:75], v[16:19], v[128:131]\n"
        "ds_read_b32 %[pv], %[sy] offset:4\nbuffer_load_dwordx4 v[48:51], %[wa], %[rs], %[so] offen offset:3072 sc1\nv_mfma_f32_16x16x32_bf16 v[148:151], v[76:79], v[16:19], v[148:151]\n"
        "ds_read_b64_tr_b16 v[16:17], %[xa] offset:16896\ns_waitcnt lgkmcnt(9)\nv_mfma_f32_16x16x32_bf16 v[100:103], v[64:67], v[20:23], v[100:103]\nds_read_b64_tr_b16 v[18:19], %[xa] offset:17408\n"
        "v_mfma_f32_16x16x32_bf16 v[112:115], v[68:71], v[20:23], v[112:115]\nbuffer_load_dwordx4 v[52:55], %[wa], %[rs], %[so] offen offset:0 sc1\n"
        "v_mfma_f32_16x16x32_bf16 v[132:135], v[72:75], v[20:23], v[132:135]\nv_mfma_f32_16x16x32_bf16 v[152:155], v[76:79], v[20:23], v[152:155]\nds_read_b64_tr_b16 v[20:21], %[xa] offset:17952\n"
        "s_waitcnt lgkmcnt(9)\nv_mfma_f32_16x16x32_bf16 v[104:107], v[68:71], v[12:15], v[104:107]\nds_read_b64_tr_b16 v[22:23], %[xa] offset:18464\n"
        "v_mfma_f32_16x16x32_bf16 v[124:127], v[72:75], v[12:15], v[124:127]\nbuffer_load_dwordx4 v[56:59], %[wa], %[rs], %[so] offen offset:1024 sc1\n"
        "v_mfma_f32_16x16x32_bf16 v[144:147], v[76:79], v[12:15], v[144:147]\nds_read_b64_tr_b16 v[12:13], %[xa] offset:15840\ns_waitcnt lgkmcnt(9)\n"
        "v_mfma_f32_16x16x32_bf16 v[116:119], v[68:71], v[24:27], v[116:119]\nds_read_b64_tr_b16 v[14:15], %[xa] offset:16352\nv_mfma_f32_16x16x32_bf16 v[136:139], v[72:75], v[24:27], v[136:139]\n"
        "buffer_load_dwordx4 v[60:63], %[wa], %[rs], %[so] offen offset:2048 sc1\nv_mfma_f32_16x16x32_bf16 v[156:159], v[76:79], v[24:27], v[156:159]\nds_read_b64_tr_b16 v[24:25], %[xa] offset:19008\n"
        "s_waitcnt lgkmcnt(8)\nv_mfma_f32_16x16x32_bf16 v[120:123], v[68:71], v[28:31], v[120:123]\nds_read_b64_tr_b16 v[26:27], %[xa] offset:19520\n"
        "v_mfma_f32_16x16x32_bf16 v[140:143], v[72:75], v[28:31], v[140:143]\nv_mfma_f32_16x16x32_bf16 v[160:163], v[76:79], v[28:31], v[160:163]\ns_waitcnt vmcnt(8) lgkmcnt(6)\n"
        "v_mfma_f32_16x16x32_bf16 v[96:99], v[80:83], v[16:19], v[96:99]\ns_addk_i32 %[so], 15360\nv_readfirstlane_b32 %[s0], %[pv]\n"
        "v_mfma_f32_16x16x32_bf16 v[108:111], v[84:87], v[16:19], v[108:111]\ns_cmp_ge_i32 %[s0], 6\nds_read_b64_tr_b16 v[28:29], %[xa] offset:20064\n"
        "v_mfma_f32_16x16x32_bf16 v[128:131], v[88:91], v[16:19], v[128:131]\ns_cbranch_scc0 .LC1s1_%=\n.LC1r1_%=:\nds_read_b64_tr_b16 v[30:31], %[xa] offset:20576\n"
        "v_mfma_f32_16x16x32_bf16 v[148:151], v[92:95], v[16:19], v[148:151]\nds_read_b64_tr_b16 v[16:17], %[xa] offset:22176\ns_waitcnt lgkmcnt(7)\n"
        "v_mfma_f32_16x16x32_bf16 v[100:103], v[80:83], v[20:23], v[100:103]\nds_read_b64_tr_b16 v[18:19], %[xa] offset:22688\nbuffer_load_dwordx4 v[64:67], %[wa], %[rs], %[so] offen offset:3072 sc1\n"
        "v_mfma_f32_16x16x32_bf16 v[112:115], v[84:87], v[20:23], v[112:115]\nbuffer_load_dwordx4 v[68:71], %[wa], %[rs], %[so] offen offset:0 sc1\n"
        "v_mfma_f32_16x16x32_bf16 v[132:135], v[88:91], v[20:23], v[132:135]\nv_mfma_f32_16x16x32_bf16 v[152:155], v[92:95], v[20:23], v[152:155]\nds_read_b64_tr_b16 v[20:21], %[xa] offset:23232\n"
        "s_waitcnt lgkmcnt(7)\nv_mfma_f32_16x16x32_bf16 v[104:107], v[84:87], v[12:15], v[104:107]\nds_read_b64_tr_b16 v[22:23], %[xa] offset:23744\n"
        "v_mfma_f32_16x16x32_bf16 v[124:127], v[88:91], v[12:15], v[124:127]\nbuffer_load_dwordx4 v[72:75], %[wa], %[rs], %[so] offen offset:1024 sc1\n"
        "v_mfma_f32_16x16x32_bf16 v[144:147], v[92:95], v[12:15], v[144:147]\nds_read_b64_tr_b16 v[12:13], %[xa] offset:21120\ns_waitcnt lgkmcnt(7)\n"
        "v_mfma_f32_16x16x32_bf16 v[116:119], v[84:87], v[24:27], v[116:119]\nds_read_b64_tr_b16 v[14:15], %[xa] offset:21632\nv_mfma_f32_16x16x32_bf16 v[136:139], v[88:91], v[24:27], v[136:139]\n"
        "buffer_load_dwordx4 v[76:79], %[wa], %[rs], %[so] offen offset:2048 sc1\nv_mfma_f32_16x16x32_bf16 v[156:159], v[92:95], v[24:27], v[156:159]\nds_read_b64_tr_b16 v[24:25], %[xa] offset:24288\n"
        "s_waitcnt lgkmcnt(7)\nv_mfma_f32_16x16x32_bf16 v[120:123], v[84:87], v[28:31], v[120:123]\nv_add_u32 %[sy], 4, %[sy]\nds_read_b64_tr_b16 v[26:27], %[xa] offset:24800\n"
        "v_mfma_f32_16x16x32_bf16 v[140:143], v[88:91], v[28:31], v[140:143]\nds_write_b32 %[pg], %[sy]\nv_mfma_f32_16x16x32_bf16 v[160:163], v[92:95], v[28:31], v[160:163]\n"
        "s_waitcnt vmcnt(8) lgkmcnt(7)\nv_mfma_f32_16x16x32_bf16 v[96:99], v[32:35], v[16:19], v[96:99]\ns_addk_i32 %[so], 15360\nds_read_b64_tr_b16 v[28:29], %[xa] offset:25344\n"
        "v_mfma_f32_16x16x32_bf16 v[108:111], v[36:39], v[16:19], v[108:111]\nds_read_b64_tr_b16 v[30:31], %[xa] offset:25856\nv_mfma_f32_16x16x32_bf16 v[128:131], v[40:43], v[16:19], v[128:131]\n"
        "ds_read_b32 %[pv], %[sy] offset:4\nbuffer_load_dwordx4 v[80:83], %[wa], %[rs], %[so] offen offset:3072 sc1\nv_mfma_f32_16x16x32_bf16 v[148:151], v[44:47], v[16:19], v[148:151]\n"
        "ds_read_b64_tr_b16 v[16:17], %[xa] offset:27456\ns_waitcnt lgkmcnt(9)\nv_mfma_f32_16x16x32_bf16 v[100:103], v[32:35], v[20:23], v[100:103]\nds_read_b64_tr_b16 v[18:19], %[xa] offset:27968\n"
        "v_mfma_f32_16x16x32_bf16 v[112:115], v[36:39], v[20:23], v[112:115]\nbuffer_load_dwordx4 v[84:87], %[wa], %[rs], %[so] offen offset:0 sc1\n"
        "v_mfma_f32_16x16x32_bf16 v[132:135], v[40:43], v[20:23], v[132:135]\nv_mfma_f32_16x16x32_bf16 v[152:155], v[44:47], v[20:23], v[152:155]\nds_read_b64_tr_b16 v[20:21], %[xa] offset:28512\n"
        "s_waitcnt lgkmcnt(9)\nv_mfma_f32_16x16x32_bf16 v[104:107], v[36:39], v[12:15], v[104:107]\nds_read_b64_tr_b16 v[22:23], %[xa] offset:29024\n"
        "v_mfma_f32_16x16x32_bf16 v[124:127], v[40:43], v[12:15], v[124:127]\nbuffer_load_dwordx4 v[88:91], %[wa], %[rs], %[so] offen offset:1024 sc1\n"
        "v_mfma_f32_16x16x32_bf16 v[144:147], v[44:47], v[12:15], v[144:147]\nds_read_b64_tr_b16 v[12:13], %[xa] offset:26400\ns_waitcnt lgkmcnt(9)\n"
        "v_mfma_f32_16x16x32_bf16 v[116:119], v[36:39], v[24:27], v[116:119]\nds_read_b64_tr_b16 v[14:15], %[xa] offset:26912\nv_mfma_f32_16x16x32_bf16 v[136:139], v[40:43], v[24:27], v[136:139]\n"
        "buffer_load_dwordx4 v[92:95], %[wa], %[rs], %[so] offen offset:2048 sc1\nv_mfma_f32_16x16x32_bf16 v[156:159], v[44:47], v[24:27], v[156:159]\nds_read_b64_tr_b16 v[24:25], %[xa] offset:29568\n"
        "s_waitcnt lgkmcnt(8)\nv_mfma_f32_16x16x32_bf16 v[120:123], v[36:39], v[28:31], v[120:123]\nds_read_b64_tr_b16 v[26:27], %[xa] offset:30080\n"
        "v_mfma_f32_16x16x32_bf16 v[140:143], v[40:43], v[28:31], v[140:143]\ns_cmp_lg_u32 %[so], 291840\nv_mfma_f32_16x16x32_bf16 v[160:163], v[44:47], v[28:31], v[160:163]\n"
        "v_add_u32 %[xa], 21120, %[xa]\ns_cbranch_scc1 .LC1l%=\ns_waitcnt vmcnt(8) lgkmcnt(6)\nv_mfma_f32_16x16x32_bf16 v[96:99], v[48:51], v[16:19], v[96:99]\nv_readfirstlane_b32 %[s0], %[pv]\n"
        "ds_read_b64_tr_b16 v[28:29], %[xa] offset:9504\nv_mfma_f32_16x16x32_bf16 v[108:111], v[52:55], v[16:19], v[108:111]\ns_cmp_ge_i32 %[s0], 6\nds_read_b64_tr_b16 v[30:31], %[xa] offset:10016\n"
        "v_mfma_f32_16x16x32_bf16 v[128:131], v[56:59], v[16:19], v[128:131]\ns_cbranch_scc0 .LC1s2_%=\n.LC1r2_%=:\nv_mfma_f32_16x16x32_bf16 v[148:151], v[60:63], v[16:19], v[148:151]\n"
        "ds_read_b64_tr_b16 v[16:17], %[xa] offset:11616\ns_waitcnt lgkmcnt(7)\nv_mfma_f32_16x16x32_bf16 v[100:103], v[48:51], v[20:23], v[100:103]\nds_read_b64_tr_b16 v[18:19], %[xa] offset:12128\n"
        "v_mfma_f32_16x16x32_bf16 v[112:115], v[52:55], v[20:23], v[112:115]\nv_mfma_f32_16x16x32_bf16 v[132:135], v[56:59], v[20:23], v[132:135]\n"
        "v_mfma_f32_16x16x32_bf16 v[152:155], v[60:63], v[20:23], v[152:155]\nds_read_b64_tr_b16 v[20:21], %[xa] offset:12672\ns_waitcnt lgkmcnt(7)\n"
        "v_mfma_f32_16x16x32_bf16 v[104:107], v[52:55], v[12:15], v[104:107]\nds_read_b64_tr_b16 v[22:23], %[xa] offset:13184\nv_mfma_f32_16x16x32_bf16 v[124:127], v[56:59], v[12:15], v[124:127]\n"
        "v_mfma_f32_16x16x32_bf16 v[144:147], v[60:63], v[12:15], v[144:147]\nds_read_b64_tr_b16 v[12:13], %[xa] offset:10560\ns_waitcnt lgkmcnt(7)\n"
        "v_mfma_f32_16x16x32_bf16 v[116:119], v[52:55], v[24:27], v[116:119]\nds_read_b64_tr_b16 v[14:15], %[xa] offset:11072\nv_mfma_f32_16x16x32_bf16 v[136:139], v[56:59], v[24:27], v[136:139]\n"
        "v_mfma_f32_16x16x32_bf16 v[156:159], v[60:63], v[24:27], v[156:159]\nds_read_b64_tr_b16 v[24:25], %[xa] offset:13728\ns_waitcnt lgkmcnt(7)\n"
        "v_mfma_f32_16x16x32_bf16 v[120:123], v[52:55], v[28:31], v[120:123]\nv_add_u32 %[sy], 4, %[sy]\nds_read_b64_tr_b16 v[26:27], %[xa] offset:14240\n"
        "v_mfma_f32_16x16x32_bf16 v[140:143], v[56:59], v[28:31], v[140:143]\nv_mfma_f32_16x16x32_bf16 v[160:163], v[60:63], v[28:31], v[160:163]\ns_waitcnt vmcnt(4) lgkmcnt(6)\n"
        "v_mfma_f32_16x16x32_bf16 v[96:99], v[64:67], v[16:19], v[96:99]\nds_read_b64_tr_b16 v[28:29], %[xa] offset:14784\nv_mfma_f32_16x16x32_bf16 v[108:111], v[68:71], v[16:19], v[108:111]\n"
        "ds_read_b64_tr_b16 v[30:31], %[xa] offset:15296\nv_and_b32 v49, 0xf0, %[wa]\nv_mfma_f32_16x16x32_bf16 v[128:131], v[72:75], v[16:19], v[128:131]\nv_lshrrev_b32 v48, 1, v49\n"
        "v_mfma_f32_16x16x32_bf16 v[148:151], v[76:79], v[16:19], v[148:151]\nds_read_b64_tr_b16 v[16:17], %[xa] offset:16896\nv_mad_u32_u24 v50, v49, 31, %[c2]\ns_waitcnt lgkmcnt(7)\n"
        "v_mfma_f32_16x16x32_bf16 v[100:103], v[64:67], v[20:23], v[100:103]\nds_read_b64_tr_b16 v[18:19], %[xa] offset:17408\nv_add_u32 v48, %[c0], v48\n"
        "v_mfma_f32_16x16x32_bf16 v[112:115], v[68:71], v[20:23], v[112:115]\nv_add_u32 v49, %[c1], v49\nv_mfma_f32_16x16x32_bf16 v[132:135], v[72:75], v[20:23], v[132:135]\n"
        "v_bfe_u32 %[pv], %[wa], 8, 1\nv_mfma_f32_16x16x32_bf16 v[152:155], v[76:79], v[20:23], v[152:155]\nds_read_b64_tr_b16 v[20:21], %[xa] offset:17952\ns_waitcnt lgkmcnt(7)\n"
        "v_mfma_f32_16x16x32_bf16 v[104:107], v[68:71], v[12:15], v[104:107]\nds_read_b64_tr_b16 v[22:23], %[xa] offset:18464\nv_mul_u32_u24 %[pv], 7936, %[pv]\n"
        "v_mfma_f32_16x16x32_bf16 v[124:127], v[72:75], v[12:15], v[124:127]\nv_bfe_u32 v51, %[wa], 9, 1\nv_mfma_f32_16x16x32_bf16 v[144:147], v[76:79], v[12:15], v[144:147]\n"
        "ds_read_b64_tr_b16 v[12:13], %[xa] offset:15840\nv_lshl_add_u32 v51, v51, 4, v50\ns_waitcnt lgkmcnt(7)\nv_mfma_f32_16x16x32_bf16 v[116:119], v[68:71], v[24:27], v[116:119]\n"
        "ds_read_b64_tr_b16 v[14:15], %[xa] offset:16352\nv_add_u32 v51, v51, %[pv]\nv_mfma_f32_16x16x32_bf16 v[136:139], v[72:75], v[24:27], v[136:139]\nv_bfe_u32 %[pv], %[wa], 8, 2\n"
        "v_mfma_f32_16x16x32_bf16 v[156:159], v[76:79], v[24:27], v[156:159]\nds_read_b64_tr_b16 v[24:25], %[xa] offset:19008\nv_lshl_add_u32 v50, %[pv], 3, v50\ns_waitcnt lgkmcnt(7)\n"
        "v_mfma_f32_16x16x32_bf16 v[120:123], v[68:71], v[28:31], v[120:123]\nds_read_b64_tr_b16 v[26:27], %[xa] offset:19520\nv_mfma_f32_16x16x32_bf16 v[140:143], v[72:75], v[28:31], v[140:143]\n"
        "v_mfma_f32_16x16x32_bf16 v[160:163], v[76:79], v[28:31], v[160:163]\ns_waitcnt vmcnt(0) lgkmcnt(6)\nv_mfma_f32_16x16x32_bf16 v[96:99], v[80:83], v[16:19], v[96:99]\n"
        "ds_read_b64_tr_b16 v[28:29], %[xa] offset:20064\nv_mfma_f32_16x16x32_bf16 v[108:111], v[84:87], v[16:19], v[108:111]\nds_read_b64_tr_b16 v[30:31], %[xa] offset:20576\n"
        "v_mfma_f32_16x16x32_bf16 v[128:131], v[88:91], v[16:19], v[128:131]\nv_mfma_f32_16x16x32_bf16 v[148:151], v[92:95], v[16:19], v[148:151]\ns_waitcnt lgkmcnt(6)\n"
        "v_mfma_f32_16x16x32_bf16 v[100:103], v[80:83], v[20:23], v[100:103]\nv_mfma_f32_16x16x32_bf16 v[112:115], v[84:87], v[20:23], v[112:115]\n"
        "v_mfma_f32_16x16x32_bf16 v[132:135], v[88:91], v[20:23], v[132:135]\nds_read_b32 %[pv], %[sy] offset:4\nds_read_b64 v[32:33], v48 offset:896\n"
        "v_mfma_f32_16x16x32_bf16 v[152:155], v[92:95], v[20:23], v[152:155]\nds_read_b64 v[36:37], v48 offset:256\ns_waitcnt lgkmcnt(7)\n"
        "v_mfma_f32_16x16x32_bf16 v[104:107], v[84:87], v[12:15], v[104:107]\nds_read_b64 v[40:41], v48 offset:384\nds_read_b64 v[44:45], v48 offset:1536\n"
        "v_mfma_f32_16x16x32_bf16 v[124:127], v[88:91], v[12:15], v[124:127]\nds_read_b128 v[52:55], v49 offset:0\nv_mfma_f32_16x16x32_bf16 v[144:147], v[92:95], v[12:15], v[144:147]\n"
        "ds_read_b128 v[56:59], v49 offset:256\nds_read_b128 v[60:63], v49 offset:512\ns_waitcnt lgkmcnt(10)\nv_mfma_f32_16x16x32_bf16 v[116:119], v[84:87], v[24:27], v[116:119]\n"
        "ds_read_b128 v[64:67], v49 offset:768\nv_mfma_f32_16x16x32_bf16 v[136:139], v[88:91], v[24:27], v[136:139]\nds_read_b128 v[68:71], v49 offset:1024\n"
        "v_mfma_f32_16x16x32_bf16 v[156:159], v[92:95], v[24:27], v[156:159]\ns_waitcnt lgkmcnt(10)\nv_mfma_f32_16x16x32_bf16 v[120:123], v[84:87], v[28:31], v[120:123]\ns_setprio %[pfs]\n"
        "v_mfma_f32_16x16x32_bf16 v[140:143], v[88:91], v[28:31], v[140:143]\nv_mfma_f32_16x16x32_bf16 v[160:163], v[92:95], v[28:31], v[160:163]\ns_mov_b32 %[s1], 0x100000\n.LC1q%=:\n"
        "s_waitcnt lgkmcnt(0)\nv_readfirstlane_b32 %[s0], %[pv]\nv_add_u32 %[pv], 8, %[sy]\ns_cmp_ge_i32 %[s0], %[nst]\ns_cbranch_scc0 .LC1t%=\n.LC1u%=:\n"
        "v_mfma_f32_16x16x32_bf16 v[96:99], v[32:35], v[56:59], v[96:99]\nv_mfma_f32_16x16x32_bf16 v[100:103], v[32:35], v[60:63], v[100:103]\n"
        "v_mfma_f32_16x16x32_bf16 v[108:111], v[36:39], v[56:59], v[108:111]\nv_mfma_f32_16x16x32_bf16 v[112:115], v[36:39], v[60:63], v[112:115]\n"
        "v_mfma_f32_16x16x32_bf16 v[128:131], v[40:43], v[56:59], v[128:131]\nv_cvt_pk_bf16_f32 v96, v96, v97\nv_cvt_pk_bf16_f32 v97, v98, v99\nv_cvt_pk_bf16_f32 v98, v100, v101\n"
        "v_cvt_pk_bf16_f32 v99, v102, v103\nv_mfma_f32_16x16x32_bf16 v[132:135], v[40:43], v[60:63], v[132:135]\nv_permlane16_swap_b32 v96, v98\nv_permlane16_swap_b32 v97, v99\n"
        "ds_write_b128 v51, v[96:99] offset:8160\nv_mfma_f32_16x16x32_bf16 v[104:107], v[36:39], v[52:55], v[104:107]\nv_cvt_pk_bf16_f32 v108, v108, v109\nv_cvt_pk_bf16_f32 v109, v110, v111\n"
        "v_cvt_pk_bf16_f32 v110, v112, v113\nv_cvt_pk_bf16_f32 v111, v114, v115\nv_mfma_f32_16x16x32_bf16 v[124:127], v[40:43], v[52:55], v[124:127]\nv_permlane16_swap_b32 v108, v110\n"
        "v_permlane16_swap_b32 v109, v111\nds_write_b128 v51, v[108:111] offset:8000\nv_mfma_f32_16x16x32_bf16 v[116:119], v[36:39], v[64:67], v[116:119]\nv_cvt_pk_bf16_f32 v128, v128, v129\n"
        "v_cvt_pk_bf16_f32 v129, v130, v131\nv_cvt_pk_bf16_f32 v130, v132, v133\nv_cvt_pk_bf16_f32 v131, v134, v135\nv_mfma_f32_16x16x32_bf16 v[120:123], v[36:39], v[68:71], v[120:123]\n"
        "v_permlane16_swap_b32 v128, v130\nv_permlane16_swap_b32 v129, v131\nds_write_b128 v51, v[128:131] offset:8032\nv_cvt_pk_bf16_f32 v104, v104, v105\n"
        "v_mfma_f32_16x16x32_bf16 v[136:139], v[40:43], v[64:67], v[136:139]\nv_cvt_pk_bf16_f32 v105, v106, v107\nds_write_b64 v50, v[104:105] offset:64\nv_cvt_pk_bf16_f32 v124, v124, v125\n"
        "v_cvt_pk_bf16_f32 v125, v126, v127\nv_mfma_f32_16x16x32_bf16 v[140:143], v[40:43], v[68:71], v[140:143]\nds_write_b64 v50, v[124:125] offset:96\n"
        "v_mfma_f32_16x16x32_bf16 v[148:151], v[44:47], v[56:59], v[148:151]\nv_cvt_pk_bf16_f32 v116, v116, v117\nv_cvt_pk_bf16_f32 v117, v118, v119\nv_cvt_pk_bf16_f32 v118, v120, v121\n"
        "v_cvt_pk_bf16_f32 v119, v122, v123\nv_mfma_f32_16x16x32_bf16 v[152:155], v[44:47], v[60:63], v[152:155]\nv_permlane16_swap_b32 v116, v118\nv_permlane16_swap_b32 v117, v119\n"
        "ds_write_b128 v51, v[116:119] offset:23872\nv_mfma_f32_16x16x32_bf16 v[144:147], v[44:47], v[52:55], v[144:147]\nv_cvt_pk_bf16_f32 v136, v136, v137\nv_cvt_pk_bf16_f32 v137, v138, v139\n"
        "v_cvt_pk_bf16_f32 v138, v140, v141\nv_cvt_pk_bf16_f32 v139, v142, v143\nv_mfma_f32_16x16x32_bf16 v[156:159], v[44:47], v[64:67], v[156:159]\nv_permlane16_swap_b32 v136, v138\n"
        "v_permlane16_swap_b32 v137, v139\nds_write_b128 v51, v[136:139] offset:23904\nds_write_b32 %[pg], %[pv]\nv_mfma_f32_16x16x32_bf16 v[160:163], v[44:47], v[68:71], v[160:163]\n"
        "v_add_u32 %[sy], 12, %[sy]\nv_cvt_pk_bf16_f32 v148, v148, v149\nv_cvt_pk_bf16_f32 v149, v150, v151\nv_cvt_pk_bf16_f32 v150, v152, v153\ns_nop 3\nv_cvt_pk_bf16_f32 v151, v154, v155\ns_nop 0\n"
        "v_permlane16_swap_b32 v148, v150\nv_permlane16_swap_b32 v149, v151\nds_write_b128 v51, v[148:151] offset:8320\nv_cvt_pk_bf16_f32 v144, v144, v145\nv_cvt_pk_bf16_f32 v145, v146, v147\n"
        "ds_write_b64 v50, v[144:145] offset:384\nv_cvt_pk_bf16_f32 v156, v156, v157\nv_cvt_pk_bf16_f32 v157, v158, v159\nv_cvt_pk_bf16_f32 v158, v160, v161\nv_cvt_pk_bf16_f32 v159, v162, v163\n"
        "s_nop 0\nv_permlane16_swap_b32 v156, v158\nv_permlane16_swap_b32 v157, v159\nds_write_b128 v51, v[156:159] offset:24192\nds_write_b32 %[pg], %[sy]\ns_branch .LE%=\n.LC1s0_%=:\n"
        "s_mov_b32 %[s1], 0x100000\n.LC1p0_%=:\nds_read_b32 %[pv], %[sy] offset:4\ns_waitcnt lgkmcnt(0)\nv_readfirstlane_b32 %[s0], %[pv]\ns_nop 0\ns_cmp_ge_i32 %[s0], 6\ns_cbranch_scc1 .LC1r0_%=\n"
        "s_sleep 1\ns_sub_u32 %[s1], %[s1], 1\ns_cbranch_scc0 .LC1p0_%=\ns_branch .LC1r0_%=\n.LC1s1_%=:\ns_mov_b32 %[s1], 0x100000\n.LC1p1_%=:\nds_read_b32 %[pv], %[sy] offset:4\ns_waitcnt lgkmcnt(0)\n"
        "v_readfirstlane_b32 %[s0], %[pv]\ns_nop 0\ns_cmp_ge_i32 %[s0], 6\ns_cbranch_scc1 .LC1r1_%=\ns_sleep 1\ns_sub_u32 %[s1], %[s1], 1\ns_cbranch_scc0 .LC1p1_%=\ns_branch .LC1r1_%=\n.LC1s2_%=:\n"
        "s_mov_b32 %[s1], 0x100000\n.LC1p2_%=:\nds_read_b32 %[pv], %[sy] offset:4\ns_waitcnt lgkmcnt(0)\nv_readfirstlane_b32 %[s0], %[pv]\ns_nop 0\ns_cmp_ge_i32 %[s0], 6\ns_cbranch_scc1 .LC1r2_%=\n"
        "s_sleep 1\ns_sub_u32 %[s1], %[s1], 1\ns_cbranch_scc0 .LC1p2_%=\ns_branch .LC1r2_%=\n.LC1t%=:\ns_sub_u32 %[s1], %[s1], 1\ns_cbranch_scc1 .LC1u%=\ns_sleep 1\nds_read_b32 %[pv], %[sy] offset:4\n"
        "ds_read_b64 v[32:33], v48 offset:896\nds_read_b64 v[36:37], v48 offset:256\nds_read_b64 v[40:41], v48 offset:384\nds_read_b64 v[44:45], v48 offset:1536\nds_read_b128 v[52:55], v49 offset:0\n"
        "ds_read_b128 v[56:59], v49 offset:256\nds_read_b128 v[60:63], v49 offset:512\nds_read_b128 v[64:67], v49 offset:768\nds_read_b128 v[68:71], v49 offset:1024\ns_branch .LC1q%=\n.LE%=:\n"
        : [pv] "=&v"(pv), [xa] "+v"(xa), [sy] "+v"(sy), [so] "=&s"(so), [s0] "=&s"(s0_), [s1] "=&s"(s1_)
        : [wa] "v"(wa), [rs] "s"(wrsrc), [pg] "v"(pga), [c0] "s"(LDSA(smem) + CW_OFF + tb * 128), [c1] "s"(LDSA(smem) + A_R2), [c2] "s"(LDSA(St) + tb * 32), [w] "s"(w), [ow] "i"(SY_W - SY_X), [pfs] "i"(PFS), [pf] "i"(PF), [nst] "i"(NST1)
        : "memory", "scc", "v8", "v9", "v10", "v11", "v12", "v13", "v14", "v15", "v16", "v17", "v18", "v19", "v20", "v21", "v22", "v23", "v24", "v25", "v26", "v27", "v28", "v29", "v30", "v31", "v32", "v33", "v34", "v35", "v36", "v37", "v38", "v39", "v40", "v41", "v42", "v43", "v44", "v45", "v46", "v47", "v48", "v49", "v50", "v51", "v52", "v53", "v54", "v55", "v56", "v57", "v58", "v59", "v60", "v61", "v62", "v63", "v64", "v65", "v66", "v67", "v68", "v69", "v70", "v71", "v72", "v73", "v74", "v75", "v76", "v77", "v78", "v79", "v80", "v81", "v82", "v83", "v84", "v85", "v86", "v87", "v88", "v89", "v90", "v91", "v92", "v93", "v94", "v95", "v96", "v97", "v98", "v99", "v100", "v101", "v102", "v103", "v104", "v105", "v106", "v107", "v108", "v109", "v110", "v111", "v112", "v113", "v114", "v115", "v116", "v117", "v118", "v119", "v120", "v121", "v122", "v123", "v124", "v125", "v126", "v127", "v128", "v129", "v130", "v131", "v132", "v133", "v134", "v135", "v136", "v137", "v138", "v139", "v140", "v141", "v142", "v143", "v144", "v145", "v146", "v147", "v148", "v149", "v150", "v151", "v152", "v153", "v154", "v155", "v156", "v157", "v158", "v159", "v160", "v161", "v162", "v163", "v164", "v165", "v166", "v167");
    }
    }
    ST(5);
    LANE(lane);
    const int t = 64 * w + lane;
    if (!layer && w == 3) {
      int spins = 0;
      _Pragma("unroll 1")
      while (true) {
        asm volatile("" ::: "memory");
        u32 d = 0;
#pragma unroll
        for (int i = 0; i < 2; ++i) {
          const u32x4 f = __builtin_amdgcn_raw_buffer_load_b128(frs, lane * 16, FL_M2 + i * 1024, 16);
          d |= (f[0] ^ tg0) | (f[1] ^ tg1) | (f[2] ^ tg0) | (f[3] ^ tg1);
        }
        if (__builtin_amdgcn_ballot_w64(d != 0) == 0 || ++spins > GCAP) break;
        __builtin_amdgcn_s_sleep(8);
      }
      VMCNT(0);
      ST(8);
    }
    if (layer && b == 0 && w == 1) {
      if (lane == 0) __builtin_amdgcn_raw_buffer_store_b64((u32x2){tg0, tg1}, frs, 0, 0, 16);
    }
    ST_FLUSH(kid);
    return;
  }
  const int j = w - 4, grp = j >> 2, mt2 = j & 3, kh = grp, qt = mt2, tt = t - 256;
  u32 l16 = lane * 16;
  int xso = g == (l15 >> 2) ? (rb * 8 + hg) * 1024 + mt2 * 256 + l15 * 16 : -1;
  const u32 tq1 = tg0 + tg1;
  if (layer) {
    const __amdgpu_buffer_rsrc_t xrs = __builtin_amdgcn_make_buffer_rsrc((void*)(xt + (size_t)rb * 51200), 0, 102400, 0x00020000);
    int gp[5], gh[3];
#pragma unroll
    for (int i = 0; i < 5; ++i) { const int p = i < 4 ? 4 * (i >> 1) + (i & 1) + 2 * grp : 8 + grp; gp[i] = PAIR(p, rot) * 10240; }
    const int ho = (mt2 & 1) * 5120 + (rb ? 4096 : 0);
    gh[0] = (mt2 & 2 ? gp[1] : gp[0]) + ho; gh[1] = (mt2 & 2 ? gp[3] : gp[2]) + ho; gh[2] = mt2 & 2 ? 102400 : gp[4] + ho;
    const int ck = (lane & 7) ^ ((lane >> 3) & 6);
    u32 vm0 = l16 + mt2 * 1024, vm1 = vm0 + 5120;
    u32 lm0 = LDSA(At) + grp * 21120 + (1 + (ck >> 1)) * 1056 + (8 * mt2 + (lane >> 3)) * 32 + (ck & 1) * 16, lm1 = lm0 + 84480 - grp * 10560;
    u32 lh0 = LDSA(At) + j * 5280 + l16, lh1 = lh0 + 84480 - grp * 10560, sy0 = sya + 8 * grp, sy1 = sya + 32 + 4 * grp, syh = sy0 + 2 * (mt2 & 2), tm;
    u32 xsb = LDSA(At) + (mt2 + 1) * 1056 + (4 * g + ((lane >> 2) & 3)) * 32 + 8 * (lane & 3), xsa = xsb + (12 + (hg & 1)) * 5280;
    xsb += xs0 * 5280;
    const int ns = hg;
    const u64 m1 = 0xaaaaaaaaaaaaaaaaull, m2 = 0xccccccccccccccccull;
    int cap, s0_, s1_;
    __builtin_amdgcn_s_setprio(PTX);
    if (grp) __builtin_amdgcn_s_sleep(XSLEEP1); else __builtin_amdgcn_s_sleep(XSLEEP);
    u32x4 xq[5];
      asm volatile(
        "buffer_load_dwordx4 v[44:47], %[vm0], %[xrs], %[gp0] offen sc1\nbuffer_load_dwordx4 v[48:51], %[vm1], %[xrs], %[gp0] offen sc1\nbuffer_load_dwordx4 v[52:55], %[l16], %[xrs], %[gh0] offen sc1\n"
        "buffer_load_dwordx4 v[56:59], %[vm0], %[xrs], %[gp1] offen sc1\n"
        : "=&{v[44:47]}"(xq[0]), "=&{v[48:51]}"(xq[1]), "=&{v[52:55]}"(xq[2]), "=&{v[56:59]}"(xq[3])
        : [l16] "v"(l16), [vm0] "v"(vm0), [vm1] "v"(vm1), [xrs] "s"(xrs), [gp0] "s"(gp[0]), [gp1] "s"(gp[1]), [gh0] "s"(gh[0])
        : "memory", "scc", "vcc");
      asm volatile(
        "s_mov_b32 %[cap], 0x10000\ns_mov_b32 %[s1], 0x100000\n.LB%=:\nds_read_b32 %[tm], %[sy] offset:%[obd]\ns_waitcnt lgkmcnt(0)\nv_readfirstlane_b32 %[s0], %[tm]\ns_nop 0\ns_cmp_ge_i32 %[s0], 12\n"
        "s_cbranch_scc1 .LP0_%=\ns_sleep 1\ns_sub_u32 %[s1], %[s1], 1\ns_cbranch_scc0 .LB%=\n.LP0_%=:\ns_waitcnt vmcnt(3)\nv_max3_u32 %[tm], v44, v45, v46\nv_max_u32 %[tm], %[tm], v47\n"
        "v_cmp_eq_u32 vcc, -1, %[tm]\ns_cbranch_vccnz .LR0_%=\n.LK0_%=:\nds_write_b128 %[lm0], v[44:47] offset:0\nbuffer_load_dwordx4 v[60:63], %[vm1], %[xrs], %[gp1] offen sc1\n.LP1_%=:\n"
        "s_waitcnt vmcnt(3)\nv_max3_u32 %[tm], v48, v49, v50\nv_max_u32 %[tm], %[tm], v51\nv_cmp_eq_u32 vcc, -1, %[tm]\ns_cbranch_vccnz .LR1_%=\n.LK1_%=:\nds_write_b128 %[lm0], v[48:51] offset:5280\n"
        "s_mov_b64 exec, 1\nds_add_u32 %[sy0], %[one] offset:%[ox]+0\ns_mov_b64 exec, -1\nbuffer_load_dwordx4 v[44:47], %[vm0], %[xrs], %[gp2] offen sc1\n.LP2_%=:\ns_waitcnt vmcnt(3)\n"
        "v_max3_u32 %[tm], v52, v53, v54\nv_max_u32 %[tm], %[tm], v55\nv_cmp_eq_u32 vcc, -1, %[tm]\ns_cbranch_vccnz .LR2_%=\n.LK2_%=:\nds_write_b128 %[lh0], v[52:55] offset:0\ns_mov_b64 exec, 1\n"
        "ds_add_u32 %[syh], %[one] offset:%[ox]+0\ns_mov_b64 exec, -1\nbuffer_load_dwordx4 v[48:51], %[vm1], %[xrs], %[gp2] offen sc1\n.LP3_%=:\ns_waitcnt vmcnt(3)\nv_max3_u32 %[tm], v56, v57, v58\n"
        "v_max_u32 %[tm], %[tm], v59\nv_cmp_eq_u32 vcc, -1, %[tm]\ns_cbranch_vccnz .LR3_%=\n.LK3_%=:\nds_write_b128 %[lm0], v[56:59] offset:10560\n"
        "buffer_load_dwordx4 v[52:55], %[l16], %[xrs], %[gh1] offen sc1\n.LP4_%=:\ns_waitcnt vmcnt(3)\nv_max3_u32 %[tm], v60, v61, v62\nv_max_u32 %[tm], %[tm], v63\nv_cmp_eq_u32 vcc, -1, %[tm]\n"
        "s_cbranch_vccnz .LR4_%=\n.LK4_%=:\nds_write_b128 %[lm0], v[60:63] offset:15840\ns_mov_b64 exec, 1\nds_add_u32 %[sy0], %[one] offset:%[ox]+4\ns_mov_b64 exec, -1\n"
        "buffer_load_dwordx4 v[56:59], %[vm0], %[xrs], %[gp3] offen sc1\n.LP5_%=:\ns_waitcnt vmcnt(3)\nv_max3_u32 %[tm], v44, v45, v46\nv_max_u32 %[tm], %[tm], v47\nv_cmp_eq_u32 vcc, -1, %[tm]\n"
        "s_cbranch_vccnz .LR5_%=\n.LK5_%=:\nds_write_b128 %[lm0], v[44:47] offset:42240\nbuffer_load_dwordx4 v[60:63], %[vm1], %[xrs], %[gp3] offen sc1\n.LP6_%=:\ns_waitcnt vmcnt(3)\n"
        "v_max3_u32 %[tm], v48, v49, v50\nv_max_u32 %[tm], %[tm], v51\nv_cmp_eq_u32 vcc, -1, %[tm]\ns_cbranch_vccnz .LR6_%=\n.LK6_%=:\nds_write_b128 %[lm0], v[48:51] offset:47520\ns_mov_b64 exec, 1\n"
        "ds_add_u32 %[sy0], %[one] offset:%[ox]+16\ns_mov_b64 exec, -1\nbuffer_load_dwordx4 v[44:47], %[vm0], %[xrs], %[gp4] offen sc1\n.LP7_%=:\ns_waitcnt vmcnt(3)\nv_max3_u32 %[tm], v52, v53, v54\n"
        "v_max_u32 %[tm], %[tm], v55\nv_cmp_eq_u32 vcc, -1, %[tm]\ns_cbranch_vccnz .LR7_%=\n.LK7_%=:\nds_write_b128 %[lh0], v[52:55] offset:42240\ns_mov_b64 exec, 1\n"
        "ds_add_u32 %[syh], %[one] offset:%[ox]+16\ns_mov_b64 exec, -1\nbuffer_load_dwordx4 v[48:51], %[vm1], %[xrs], %[gp4] offen sc1\n.LP8_%=:\ns_waitcnt vmcnt(3)\nv_max3_u32 %[tm], v56, v57, v58\n"
        "v_max_u32 %[tm], %[tm], v59\nv_cmp_eq_u32 vcc, -1, %[tm]\ns_cbranch_vccnz .LR8_%=\n.LK8_%=:\nds_write_b128 %[lm0], v[56:59] offset:52800\n"
        "buffer_load_dwordx4 v[52:55], %[l16], %[xrs], %[gh2] offen sc1\ns_cmp_lt_u32 %[j], 4\ns_cbranch_scc1 .LT%=\ns_mov_b32 %[s1], 0x100000\n.LU%=:\nds_read_b128 v[36:39], %[sy0] offset:%[ox]-8\n"
        "ds_read_b128 v[40:43], %[sy0] offset:%[ox]+8\nds_read_b64_tr_b16 v[28:29], %[xsa]\nds_read_b64_tr_b16 v[30:31], %[xsa] offset:512\nds_read_b64_tr_b16 v[32:33], %[xsb]\n"
        "ds_read_b64_tr_b16 v[34:35], %[xsb] offset:512\ns_waitcnt lgkmcnt(4)\nv_min3_u32 v36, v36, v37, v38\nv_min3_u32 v40, v40, v41, v42\nv_min3_u32 v36, v36, v39, v40\ns_nop 0\n"
        "v_readfirstlane_b32 %[s0], v36\ns_waitcnt lgkmcnt(0)\ns_cmp_ge_i32 %[s0], 6\ns_cbranch_scc1 .LV%=\ns_sleep 1\ns_sub_u32 %[s1], %[s1], 1\ns_cbranch_scc0 .LU%=\n.LV%=:\n"
        "v_mfma_f32_16x16x32_bf16 v[36:39], %[ones], v[28:31], 0\nv_mfma_f32_16x16x32_bf16 v[40:43], v[28:31], v[28:31], 0\nds_read_b64_tr_b16 v[28:29], %[xsb] offset:5280\n"
        "ds_read_b64_tr_b16 v[30:31], %[xsb] offset:5792\ns_bitcmp1_b32 %[ns], 0\nv_mfma_f32_16x16x32_bf16 v[36:39], %[ones], v[32:35], v[36:39]\n"
        "v_mfma_f32_16x16x32_bf16 v[40:43], v[32:35], v[32:35], v[40:43]\ns_waitcnt lgkmcnt(0)\ns_cbranch_scc1 .LW%=\nv_mfma_f32_16x16x32_bf16 v[36:39], %[ones], v[28:31], v[36:39]\n"
        "v_mfma_f32_16x16x32_bf16 v[40:43], v[28:31], v[28:31], v[40:43]\n.LW%=:\ns_nop 7\nv_cndmask_b32 v40, v40, v41, %[m1]\nv_cndmask_b32 v42, v42, v43, %[m1]\nv_mov_b32 v37, %[tg0]\n"
        "v_mov_b32 v39, %[tq1]\nv_cndmask_b32 v38, v40, v42, %[m2]\nbuffer_store_dwordx4 v[36:39], %[xso], %[srs], 0 offen sc1\n.LT%=:\n.LP9_%=:\ns_waitcnt vmcnt(3)\nv_max3_u32 %[tm], v60, v61, v62\n"
        "v_max_u32 %[tm], %[tm], v63\nv_cmp_eq_u32 vcc, -1, %[tm]\ns_cbranch_vccnz .LR9_%=\n.LK9_%=:\nds_write_b128 %[lm0], v[60:63] offset:58080\ns_mov_b64 exec, 1\n"
        "ds_add_u32 %[sy0], %[one] offset:%[ox]+20\ns_mov_b64 exec, -1\n.LP10_%=:\ns_waitcnt vmcnt(2)\nv_max3_u32 %[tm], v44, v45, v46\nv_max_u32 %[tm], %[tm], v47\nv_cmp_eq_u32 vcc, -1, %[tm]\n"
        "s_cbranch_vccnz .LR10_%=\n.LK10_%=:\nds_write_b128 %[lm1], v[44:47] offset:0\n.LP11_%=:\ns_waitcnt vmcnt(1)\nv_max3_u32 %[tm], v48, v49, v50\nv_max_u32 %[tm], %[tm], v51\n"
        "v_cmp_eq_u32 vcc, -1, %[tm]\ns_cbranch_vccnz .LR11_%=\n.LK11_%=:\nds_write_b128 %[lm1], v[48:51] offset:5280\ns_mov_b64 exec, 1\nds_add_u32 %[sy1], %[one] offset:%[ox]+0\ns_mov_b64 exec, -1\n"
        ".LP12_%=:\ns_waitcnt vmcnt(0)\nv_max3_u32 %[tm], v52, v53, v54\nv_max_u32 %[tm], %[tm], v55\nv_cmp_eq_u32 vcc, -1, %[tm]\ns_cbranch_vccnz .LR12_%=\n.LK12_%=:\ns_bitcmp1_b32 %[j], 1\n"
        "s_cbranch_scc1 .LE%=\nds_write_b128 %[lh1], v[52:55] offset:0\ns_mov_b64 exec, 1\nds_add_u32 %[sy1], %[one] offset:%[ox]\ns_mov_b64 exec, -1\ns_branch .LE%=\n.LR0_%=:\n"
        "s_sub_u32 %[cap], %[cap], 1\ns_cbranch_scc0 .LQ0_%=\ns_mov_b32 %[cap], 0\ns_branch .LK0_%=\n.LQ0_%=:\ns_sleep %[xp]\nbuffer_load_dwordx4 v[44:47], %[vm0], %[xrs], %[gp0] offen sc1\n"
        "buffer_load_dwordx4 v[48:51], %[vm1], %[xrs], %[gp0] offen sc1\nbuffer_load_dwordx4 v[52:55], %[l16], %[xrs], %[gh0] offen sc1\nbuffer_load_dwordx4 v[56:59], %[vm0], %[xrs], %[gp1] offen sc1\n"
        "s_branch .LP0_%=\n.LR1_%=:\ns_sub_u32 %[cap], %[cap], 1\ns_cbranch_scc0 .LQ1_%=\ns_mov_b32 %[cap], 0\ns_branch .LK1_%=\n.LQ1_%=:\ns_sleep %[xp]\n"
        "buffer_load_dwordx4 v[48:51], %[vm1], %[xrs], %[gp0] offen sc1\nbuffer_load_dwordx4 v[52:55], %[l16], %[xrs], %[gh0] offen sc1\nbuffer_load_dwordx4 v[56:59], %[vm0], %[xrs], %[gp1] offen sc1\n"
        "buffer_load_dwordx4 v[60:63], %[vm1], %[xrs], %[gp1] offen sc1\ns_branch .LP1_%=\n.LR2_%=:\ns_sub_u32 %[cap], %[cap], 1\ns_cbranch_scc0 .LQ2_%=\ns_mov_b32 %[cap], 0\ns_branch .LK2_%=\n"
        ".LQ2_%=:\ns_sleep %[xp]\nbuffer_load_dwordx4 v[52:55], %[l16], %[xrs], %[gh0] offen sc1\nbuffer_load_dwordx4 v[56:59], %[vm0], %[xrs], %[gp1] offen sc1\n"
        "buffer_load_dwordx4 v[60:63], %[vm1], %[xrs], %[gp1] offen sc1\nbuffer_load_dwordx4 v[44:47], %[vm0], %[xrs], %[gp2] offen sc1\ns_branch .LP2_%=\n.LR3_%=:\ns_sub_u32 %[cap], %[cap], 1\n"
        "s_cbranch_scc0 .LQ3_%=\ns_mov_b32 %[cap], 0\ns_branch .LK3_%=\n.LQ3_%=:\ns_sleep %[xp]\nbuffer_load_dwordx4 v[56:59], %[vm0], %[xrs], %[gp1] offen sc1\n"
        "buffer_load_dwordx4 v[60:63], %[vm1], %[xrs], %[gp1] offen sc1\nbuffer_load_dwordx4 v[44:47], %[vm0], %[xrs], %[gp2] offen sc1\nbuffer_load_dwordx4 v[48:51], %[vm1], %[xrs], %[gp2] offen sc1\n"
        "s_branch .LP3_%=\n.LR4_%=:\ns_sub_u32 %[cap], %[cap], 1\ns_cbranch_scc0 .LQ4_%=\ns_mov_b32 %[cap], 0\ns_branch .LK4_%=\n.LQ4_%=:\ns_sleep %[xp]\n"
        "buffer_load_dwordx4 v[60:63], %[vm1], %[xrs], %[gp1] offen sc1\nbuffer_load_dwordx4 v[44:47], %[vm0], %[xrs], %[gp2] offen sc1\nbuffer_load_dwordx4 v[48:51], %[vm1], %[xrs], %[gp2] offen sc1\n"
        "buffer_load_dwordx4 v[52:55], %[l16], %[xrs], %[gh1] offen sc1\ns_branch .LP4_%=\n.LR5_%=:\ns_sub_u32 %[cap], %[cap], 1\ns_cbranch_scc0 .LQ5_%=\ns_mov_b32 %[cap], 0\ns_branch .LK5_%=\n"
        ".LQ5_%=:\ns_sleep %[xp]\nbuffer_load_dwordx4 v[44:47], %[vm0], %[xrs], %[gp2] offen sc1\nbuffer_load_dwordx4 v[48:51], %[vm1], %[xrs], %[gp2] offen sc1\n"
        "buffer_load_dwordx4 v[52:55], %[l16], %[xrs], %[gh1] offen sc1\nbuffer_load_dwordx4 v[56:59], %[vm0], %[xrs], %[gp3] offen sc1\ns_branch .LP5_%=\n.LR6_%=:\ns_sub_u32 %[cap], %[cap], 1\n"
        "s_cbranch_scc0 .LQ6_%=\ns_mov_b32 %[cap], 0\ns_branch .LK6_%=\n.LQ6_%=:\ns_sleep %[xp]\nbuffer_load_dwordx4 v[48:51], %[vm1], %[xrs], %[gp2] offen sc1\n"
        "buffer_load_dwordx4 v[52:55], %[l16], %[xrs], %[gh1] offen sc1\nbuffer_load_dwordx4 v[56:59], %[vm0], %[xrs], %[gp3] offen sc1\nbuffer_load_dwordx4 v[60:63], %[vm1], %[xrs], %[gp3] offen sc1\n"
        "s_branch .LP6_%=\n.LR7_%=:\ns_sub_u32 %[cap], %[cap], 1\ns_cbranch_scc0 .LQ7_%=\ns_mov_b32 %[cap], 0\ns_branch .LK7_%=\n.LQ7_%=:\ns_sleep %[xp]\n"
        "buffer_load_dwordx4 v[52:55], %[l16], %[xrs], %[gh1] offen sc1\nbuffer_load_dwordx4 v[56:59], %[vm0], %[xrs], %[gp3] offen sc1\nbuffer_load_dwordx4 v[60:63], %[vm1], %[xrs], %[gp3] offen sc1\n"
        "buffer_load_dwordx4 v[44:47], %[vm0], %[xrs], %[gp4] offen sc1\ns_branch .LP7_%=\n.LR8_%=:\ns_sub_u32 %[cap], %[cap], 1\ns_cbranch_scc0 .LQ8_%=\ns_mov_b32 %[cap], 0\ns_branch .LK8_%=\n"
        ".LQ8_%=:\ns_sleep %[xp]\nbuffer_load_dwordx4 v[56:59], %[vm0], %[xrs], %[gp3] offen sc1\nbuffer_load_dwordx4 v[60:63], %[vm1], %[xrs], %[gp3] offen sc1\n"
        "buffer_load_dwordx4 v[44:47], %[vm0], %[xrs], %[gp4] offen sc1\nbuffer_load_dwordx4 v[48:51], %[vm1], %[xrs], %[gp4] offen sc1\ns_branch .LP8_%=\n.LR9_%=:\ns_sub_u32 %[cap], %[cap], 1\n"
        "s_cbranch_scc0 .LQ9_%=\ns_mov_b32 %[cap], 0\ns_branch .LK9_%=\n.LQ9_%=:\ns_sleep %[xp]\nbuffer_load_dwordx4 v[60:63], %[vm1], %[xrs], %[gp3] offen sc1\n"
        "buffer_load_dwordx4 v[44:47], %[vm0], %[xrs], %[gp4] offen sc1\nbuffer_load_dwordx4 v[48:51], %[vm1], %[xrs], %[gp4] offen sc1\nbuffer_load_dwordx4 v[52:55], %[l16], %[xrs], %[gh2] offen sc1\n"
        "s_branch .LP9_%=\n.LR10_%=:\ns_sub_u32 %[cap], %[cap], 1\ns_cbranch_scc0 .LQ10_%=\ns_mov_b32 %[cap], 0\ns_branch .LK10_%=\n.LQ10_%=:\ns_sleep %[xp]\n"
        "buffer_load_dwordx4 v[44:47], %[vm0], %[xrs], %[gp4] offen sc1\nbuffer_load_dwordx4 v[48:51], %[vm1], %[xrs], %[gp4] offen sc1\nbuffer_load_dwordx4 v[52:55], %[l16], %[xrs], %[gh2] offen sc1\n"
        "s_branch .LP10_%=\n.LR11_%=:\ns_sub_u32 %[cap], %[cap], 1\ns_cbranch_scc0 .LQ11_%=\ns_mov_b32 %[cap], 0\ns_branch .LK11_%=\n.LQ11_%=:\ns_sleep %[xp]\n"
        "buffer_load_dwordx4 v[48:51], %[vm1], %[xrs], %[gp4] offen sc1\nbuffer_load_dwordx4 v[52:55], %[l16], %[xrs], %[gh2] offen sc1\ns_branch .LP11_%=\n.LR12_%=:\ns_sub_u32 %[cap], %[cap], 1\n"
        "s_cbranch_scc0 .LQ12_%=\ns_mov_b32 %[cap], 0\ns_branch .LK12_%=\n.LQ12_%=:\ns_sleep %[xp]\nbuffer_load_dwordx4 v[52:55], %[l16], %[xrs], %[gh2] offen sc1\ns_branch .LP12_%=\n.LE%=:\n"
        : [tm] "=&v"(tm), [cap] "=&s"(cap), [s0] "=&s"(s0_), [s1] "=&s"(s1_), "+{v[44:47]}"(xq[0]), "+{v[48:51]}"(xq[1]), "+{v[52:55]}"(xq[2]), "+{v[56:59]}"(xq[3])
        : [l16] "v"(l16), [vm0] "v"(vm0), [vm1] "v"(vm1), [xrs] "s"(xrs), [gp0] "s"(gp[0]), [gp1] "s"(gp[1]), [gp2] "s"(gp[2]), [gp3] "s"(gp[3]), [gp4] "s"(gp[4]), [gh0] "s"(gh[0]), [gh1] "s"(gh[1]), [gh2] "s"(gh[2]), [lm0] "v"(lm0), [lm1] "v"(lm1), [lh0] "v"(lh0), [lh1] "v"(lh1), [sy0] "v"(sy0), [sy1] "v"(sy1), [syh] "v"(syh), [xsb] "v"(xsb), [one] "v"(one), [j] "s"(j), [xsa] "v"(xsa), [ns] "s"(ns), [ones] "v"(onesf), [m1] "s"(m1), [m2] "s"(m2), [xso] "v"(xso), [srs] "s"(xsrs), [tg0] "s"(tg0), [tq1] "s"(tq1), [ox] "i"(SY_X), [xp] "i"(XPOLL), [sy] "v"(sya), [obd] "i"(SY_TI)
        : "memory", "scc", "vcc", "v28", "v29", "v30", "v31", "v32", "v33", "v34", "v35", "v36", "v37", "v38", "v39", "v40", "v41", "v42", "v43", "v60", "v61", "v62", "v63");
    RETRY(8, 0x10000 - cap);
    ST(3);
  } else {

#define MARKS \
    if (w == MKW) { \
      const __amdgpu_buffer_rsrc_t mrs = __builtin_amdgcn_make_buffer_rsrc(obuf, 0, HO_BYTES, 0x00020000); \
      const __amdgpu_buffer_rsrc_t irs = __builtin_amdgcn_make_buffer_rsrc(ws + WS_IM1 + b * 12800, 0, 12800, 0x00020000); \
      __builtin_amdgcn_raw_buffer_store_b128(MARK16, mrs, lane < 16 ? (lane >> 3) * 4096 + (lane & 7) * 16 : -1, HO_FS + hg * 8192 + s0 * 2, 17); \
      _Pragma("unroll 1") \
      for (int i = 0; i < 10; ++i) { \
        __builtin_amdgcn_raw_buffer_store_b128(MARK16, mrs, (lane >> 3) * (XP * 2) + (lane & 7) * 16, HO_X1 + (hg * 80 + 8 * i) * (XP * 2) + s0 * 2, 17); \
        __builtin_amdgcn_s_sleep(MSL1); \
      } \
      _Pragma("unroll 1") \
      for (int i = 0; i < 13; ++i) { \
        __builtin_amdgcn_raw_buffer_store_b128(MARK16, irs, lane * 16, i * 1024, 17); \
        __builtin_amdgcn_s_sleep(MSL2); \
      } \
      VMCNT(0); \
      if (lane == 0) __builtin_amdgcn_raw_buffer_store_b64((u32x2){tg0, tg1}, frs, 0, FL_M2 + b * 8, 16); \
       \
    } \

    lds_poll<SY_X, 2>(sya, 3);
    if (j < 4) {
      const u32 xa = LDSA(At) + (2 * mt2 + 2 + (l15 >> 3)) * 1024 + (l15 & 7) * 128 + ((g ^ (l15 & 6)) << 4);
      bf16x8 fv[3];
      asm volatile("ds_read_b128 %0, %3 offset:61440\n\tds_read_b128 %1, %4\n\tds_read_b128 %2, %5\n\ts_waitcnt lgkmcnt(0)" : "=&v"(fv[0]), "=&v"(fv[1]), "=&v"(fv[2])
                   : "v"(xa ^ (64 * (hg & 1))), "v"((xa ^ (64 * (xs0 & 1))) + (xs0 >> 1) * 10240), "v"((xa ^ (64 * (~xs0 & 1))) + ((xs0 + 1) >> 1) * 10240) : "memory");
      f32x4 s1 = {0.f, 0.f, 0.f, 0.f}, s2 = s1;
#pragma unroll
      for (int q = 0; q < 3; ++q) {
        if (q < 2 || !(hg & 1)) {
          s1 = __builtin_amdgcn_mfma_f32_16x16x32_bf16(onesf, fv[q], s1, 0, 0, 0);
          s2 = __builtin_amdgcn_mfma_f32_16x16x32_bf16(fv[q], fv[q], s2, 0, 0, 0);
        }
      }
      const float d2 = (l15 & 2) ? ((l15 & 1) ? s2[3] : s2[2]) : ((l15 & 1) ? s2[1] : s2[0]);
      __builtin_amdgcn_raw_buffer_store_b128((u32x4){__float_as_uint(s1[0]), tg0, __float_as_uint(d2), tq1}, xsrs, xso, 0, 16);
    }
  }
  __builtin_amdgcn_s_setprio(PTW);
  if (layer) lds_inc<SY_PART>(sya, one);
  u32 ra[3];
  {
    const int rr_ = 4 * (g & 1) + ((lane >> 2) & 3), pr_ = 4 * (l15 >> 3) + (l15 & 3) + 4 * (l15 & 4);
    const u32 ro = LDSA(At) + (layer ? (mt2 + 1) * 1056 + pr_ * 32 + 8 * g : (2 * mt2 + 2 + (g >> 1)) * 1024 + rr_ * 128 + ((rr_ & 6) << 4) + (lane & 3) * 8);
#pragma unroll
    for (int i = 0; i < 3; ++i) {
      const int kc = hg * 80 + 16 * (2 * kh + i), kr = 64 * PAIR(kc >> 6, -rot) + (kc & 63);
      ra[i] = layer ? ro + (kr >> 5) * 5280 + (kr & 16) * 16 : (ro ^ ((kr & 48) * 2)) + (kr >> 6) * 10240;
      if (i == 0 && kh) ra[i] = sya + SY_Z;
      PIN(ra[i]);
    }
  }
  u32x2 rv[3];
#define RVR { lds_poll<SY_PART, 1>(sya, 8); \
    asm volatile("ds_read_b64 %0, %3\n\tds_read_b64 %1, %4\n\tds_read_b64 %2, %5\n\ts_waitcnt lgkmcnt(0)" : "=&v"(rv[0]), "=&v"(rv[1]), "=&v"(rv[2]) : "v"(ra[0]), "v"(ra[1]), "v"(ra[2]) : "memory"); \
    lds_inc<SY_RV>(sya, one); lds_inc<SY_STAT>(sya, one); }
  STX(8);
  if (!layer) {
    asm volatile("ds_read_b64_tr_b16 %0, %3\n\tds_read_b64_tr_b16 %1, %4\n\tds_read_b64_tr_b16 %2, %5\n\ts_waitcnt lgkmcnt(0)" : "=&v"(rv[0]), "=&v"(rv[1]), "=&v"(rv[2]) : "v"(ra[0]), "v"(ra[1]), "v"(ra[2]) : "memory");
    lds_inc<SY_RV>(sya, one); lds_inc<SY_STAT>(sya, one);
    MARKS
  } else if (grp) RVR
  STX(10);
  __amdgpu_buffer_rsrc_t orsrc = __builtin_amdgcn_make_buffer_rsrc(obuf + (size_t)rb * 40960, 0, 81920, 0x00020000);
  u32 qka = LDSA(St) + ((16 * qt + l15) * X1_P + 160 * grp + 8 * g) * 2;
  u32 vta = LDSA(St) + ((16 * qt + 4 * g + ((lane >> 2) & 3)) * X1_P + 160 * grp + 40 + 4 * (lane & 3)) * 2;
  const bool par = l15 & 1;
  const int kk0 = 4 * g + (l15 & 1), kk1 = kk0 + 2;
  const bool c0 = kk0 >= l15 + 2, c1 = kk1 >= l15 + 2;
  const u32 e0 = par ? 0x01000c0cu : 0x0c0c0100u, e1 = par ? 0x03020c0cu : 0x0c0c0302u;
  u32 sA0 = c0 ? e0 : 0x0c0c0c0cu, sB0 = c0 ? 0x0c0c0c0cu : e0;
  u32 sA1 = c1 ? e1 : 0x0c0c0c0cu, sB1 = c1 ? 0x0c0c0c0cu : e1;
  u32 oha = g * 10240 + l15 * 16, qa[4];
#pragma unroll
  for (int i = 0; i < 4; ++i) { qa[i] = g >= 3 - (i & 1) ? sya + SY_Z : qka + (32 * X1_P + 160 - 240 * grp) + 40 * (i >> 1) + 8 * (i & 1); PIN(qa[i]); }
  const float nb = s0 + qt ? 0.f : -1e38f;
  f32x4 bias = {nb, nb, nb, nb};
  int ohs = g ? -1 : l16;
  u64 c0m = __builtin_amdgcn_ballot_w64(c0), c1m = __builtin_amdgcn_ballot_w64(c1);
  int so0 = (hg * 4 + qt) * 2560 + grp * 1024, so1 = (hg * 4 + qt) * 2560 + 2048 + grp * 256;
  float nc = 0.22360679774997896f * 1.4426950408889634f;
  u32 rq = LDSA(smem) + CW_OFF + 1920 + 64 * qt, rk0 = rq + 4 * (kk0 + (c0 ? 0 : 16)), rk1 = rq + 4 * (kk1 + (c1 ? 0 : 16));
  rq += 64 + 4 * l15;
  PIN(rq); PIN(rk0); PIN(rk1);
  asm volatile("" : "+s"(c0m), "+s"(c1m), "+s"(so0), "+s"(so1), "+s"(nc));
  PIN(qka); PIN(vta); PIN(sA0); PIN(sA1); PIN(sB0); PIN(sB1); PIN(l16); PIN(oha); PIN(sya); PIN(one); PIN(bias); PIN(ohs);
  u32 rw = LDSA(smem) + EX_OFF + j * 3072 + l16, fw = sya + SY_RED + 4 * j, f0 = 1 - kh;
  u32 xo_ = LDSA(smem + X1T_OFF) + (32 * kh + l15) * 144 + 32 * mt2 + 8 * g;
  u32 xi_ = LDSA(smem + X1T_OFF) + (tt >> 3) * 144 + (tt & 7) * 16;
  __amdgpu_buffer_rsrc_t x1rs = __builtin_amdgcn_make_buffer_rsrc(xtout + (size_t)hg * 80 * XP + s0, 0, 80 * XP * 2, 0x00020000);
  int xg_ = (tt >> 3) * (XP * 2) + (tt & 7) * 16;
  __amdgpu_buffer_rsrc_t srsrc = __builtin_amdgcn_make_buffer_rsrc(stats + (size_t)s0 * 16, 0, 8192, 0x00020000);
  u32 rr = LDSA(smem) + EX_OFF + (j ^ 4) * 3072 + l16 - 1024 * kh;
  int so_ = (l15 & 3) ? -1 : (lane >> 2) * 8;
  const int wf = layer ? 8 * ((j + 6) & 7) : j;
  u32 bb = LDSA(smem) + WO_OFF + kh * 6144 + l16, fr = fw ^ 16, wd = LDSA(smem) + WO_OFF + wf * 1024 + l16;
  PIN(rw); PIN(fw); PIN(f0); PIN(xo_); PIN(xi_); PIN(xg_); PIN(rr); PIN(so_); PIN(bb); PIN(fr); PIN(wd);
  __amdgpu_buffer_rsrc_t wos = __builtin_amdgcn_make_buffer_rsrc((void*)(wot + (size_t)hg * 51200 + wf * 512), 0, 102400 - wf * 1024, 0x00020000);
  int wg = (j < 4 ? 8 : 58) * 1024;
  u32 wd0 = wd + wg;
  if (!layer) { PIN(wd0); asm volatile("" : "+s"(wg)); }
  int pgt = LDSA(smem) + SY_OFF + 256 * layer + SY_X + 44 + 4 * grp, ob[6];
#pragma unroll
  for (int i = 0; i < 6; ++i) { ob[i] = i == 5 ? mt2 * 128 + (2 * hg + kh) * 512 : (16 * kh + mt2) * 2560 + (i < 4 ? 10240 * i : 2048); asm volatile("" : "+s"(ob[i])); }
  asm volatile("" : "+s"(wos), "+s"(pgt), "+s"(srsrc), "+s"(x1rs), "+s"(orsrc));
  STX(11);
    if (!layer) {
      if (w == CWW) {
        const __amdgpu_buffer_rsrc_t crs = __builtin_amdgcn_make_buffer_rsrc(ws + WS_CW + hg * 23040, 0, 23040, 0x00020000);
        const int cp = min(lane, 59) / 20, cc = min(lane, 59) - 20 * cp;
        pg_poll<4>(sya, LDSA(smem) + SY_OFF + SY_X + CWAT);
        __builtin_amdgcn_s_setprio(PCW);
        f32x4 cv[20];
#pragma unroll
        for (int i = 0; i < 20; ++i) cv[i] = __builtin_bit_cast(f32x4, __builtin_amdgcn_raw_buffer_load_b128(crs, cp * 384 + cc * 16, i * 1152, 16));
#pragma unroll
        for (int i = 0; i < 10; ++i) cv[i] += cv[i + 10];
        const f32x4 cs = ((cv[0] + cv[1]) + (cv[2] + cv[3])) + ((cv[4] + cv[5]) + (cv[6] + cv[7])) + (cv[8] + cv[9]);
        u32 cq[8];
#pragma unroll
        for (int r = 0; r < 4; ++r) {
          const float h0 = rbf(cs[r]), h1 = rbf(cs[r] - h0);
          cq[2 * r] = pack2(h0, h1); cq[2 * r + 1] = pack2(cs[r] - h0 - h1, 0.f);
        }
        asm volatile("ds_write_b128 %0, %1\n\tds_write_b128 %0, %2 offset:16" :: "v"(LDSA(smem) + CW_OFF + (cc + (cp ? 10 * cp - 10 + (cc >= 10 ? 30 : 0) : 20)) * 32),
                     "v"((u32x4){cq[0], cq[1], cq[2], cq[3]}), "v"((u32x4){cq[4], cq[5], cq[6], cq[7]}) : "memory");
        lds_inc<SY_STAT>(sya, one);
        __builtin_amdgcn_s_setprio(PTW);

      }
      if (w == MPW) {
        const int fb = lane < 8 ? (b & 62) + (lane & 1) + 64 * (lane >> 1) : 256 + 16 * ((lane >> 1) & 7) + 2 * hg + (lane & 1);
        pg_poll<8>(sya, LDSA(smem) + SY_OFF + SY_X + MKAT);
        int spins = 0;
        _Pragma("unroll 1")
        while (true) {
          asm volatile("" ::: "memory");
          const u32x2 f = __builtin_amdgcn_raw_buffer_load_b64(frs, lane < 24 ? fb * 128 : b * 128, FL_M, 16);
          if (__builtin_amdgcn_ballot_w64(f[0] != tg0 || f[1] != tg1) == 0 || ++spins > GCAP) break;
          __builtin_amdgcn_s_sleep(8);
        }
        RETRY(15, spins);
        lds_inc<SY_STAT>(sya, one);
        lds_inc<SY_MK>(sya, one);

      }
    }
    if (j == XJ0 || j == XJ1) {
      const int xo = j == XJ0 ? rb * 8192 + l16 : (rb ? rb - 1 : 0) * 8192 + 768 + l15 * 16;
      pg_poll<4>(sya, LDSA(smem) + SY_OFF + 256 * layer + SY_X + (layer ? XSAT : XSAT0));
      __builtin_amdgcn_s_sleep(layer ? XSSL : 0);
      __builtin_amdgcn_s_setprio(3);
      u32x4 pv[8];
      int spins = 0;
      _Pragma("unroll 1")
      while (true) {
        asm volatile("" ::: "memory");
        u32 m = 0;
#pragma unroll
        for (int i = 0; i < 8; ++i) pv[i] = __builtin_amdgcn_raw_buffer_load_b128(xsrs, xo, i * 1024, 16);
#pragma unroll
        for (int i = 0; i < 8; ++i) m |= (pv[i][1] ^ tg0) | (pv[i][3] ^ tq1);
        if (__builtin_amdgcn_ballot_w64(m != 0) == 0 || ++spins > GCAP) break;
        __builtin_amdgcn_s_sleep(XSRT);
      }
      RETRY(15, spins);
      float sv[2];
#pragma unroll
      for (int i = 0; i < 2; ++i)
        sv[i] = ((__uint_as_float(pv[0][2 * i]) + __uint_as_float(pv[1][2 * i])) + (__uint_as_float(pv[2][2 * i]) + __uint_as_float(pv[3][2 * i]))) +
                ((__uint_as_float(pv[4][2 * i]) + __uint_as_float(pv[5][2 * i])) + (__uint_as_float(pv[6][2 * i]) + __uint_as_float(pv[7][2 * i])));
      const float mean = sv[0] * (1.0f / 640.0f), var = fmaxf(sv[1] * (1.0f / 640.0f) - mean * mean, 0.f);
      const u32 mq = pack2(-0.25f * mean, -0.25f * mean);
      const int row = j == XJ0 ? 16 + lane : l15;
      asm volatile("ds_write_b128 %0, %1\n\tds_write_b32 %2, %3" :: "v"(LDSA(smem) + A_R2 + row * 16), "v"((u32x4){mq, mq & 0xffffu, 0u, 0u}),
                   "v"(LDSA(smem) + CW_OFF + 1920 + row * 4), "v"(__builtin_amdgcn_rcpf(rbf(__builtin_amdgcn_sqrtf(rbf(rbf(var) + 1e-5f))))) : "memory");
      lds_inc<SY_STAT>(sya, one);
      __builtin_amdgcn_s_setprio(PTW);

    }
  if (layer && !grp) RVR
  STX(7);
  if (!layer) lds_poll<SY_MK, 2>(sya, 1);
  STX(2);
  pg_poll<2>(sya, LDSA(smem) + SY_OFF + 256 * layer + SY_X + (layer ? WOAT : WOAT0));
  STX(12);
  __builtin_amdgcn_s_setprio(PTO);
  {
    int s0_, s1_;
    if (layer) {
      asm volatile(
        "s_bitcmp1_b32 0x3c, %[j]\ns_cbranch_scc0 .LA%=\ns_mov_b32 %[s1], 0x100000\n.LC%=:\nds_read_b32 v30, %[sy] offset:%[orv]\ns_waitcnt lgkmcnt(0)\nv_readfirstlane_b32 %[s0], v30\ns_nop 0\n"
        "s_cmp_ge_u32 %[s0], 8\ns_cbranch_scc1 .LCx%=\ns_sleep 1\ns_sub_u32 %[s1], %[s1], 1\ns_cbranch_scc0 .LC%=\n.LCx%=:\nds_write_b128 %[wd], v[128:131] offset:0\n"
        "ds_write_b128 %[wd], v[132:135] offset:1024\nds_write_b128 %[wd], v[144:147] offset:2048\nds_write_b128 %[wd], v[148:151] offset:3072\nds_write_b128 %[wd], v[152:155] offset:4096\n"
        "ds_write_b128 %[wd], v[156:159] offset:5120\nds_write_b128 %[wd], v[160:163] offset:6144\nds_write_b128 %[wd], v[164:167] offset:7168\ns_branch .LD%=\n.LA%=:\ns_movk_i32 %[s0], 4096\n"
        "buffer_load_dwordx4 v[128:131], %[l16], %[wos], 0 offen offset:0 sc1\nbuffer_load_dwordx4 v[132:135], %[l16], %[wos], 0 offen offset:1024 sc1\n"
        "buffer_load_dwordx4 v[144:147], %[l16], %[wos], 0 offen offset:2048 sc1\nbuffer_load_dwordx4 v[148:151], %[l16], %[wos], 0 offen offset:3072 sc1\n"
        "buffer_load_dwordx4 v[152:155], %[l16], %[wos], %[s0] offen offset:0 sc1\nbuffer_load_dwordx4 v[156:159], %[l16], %[wos], %[s0] offen offset:1024 sc1\ns_cmp_eq_u32 %[j], 1\n"
        "s_cbranch_scc1 .LD%=\nbuffer_load_dwordx4 v[160:163], %[l16], %[wos], %[s0] offen offset:2048 sc1\nbuffer_load_dwordx4 v[164:167], %[l16], %[wos], %[s0] offen offset:3072 sc1\n.LD%=:\n"
        "s_mov_b64 exec, 1\nds_add_u32 %[sy], %[one] offset:%[owo]\ns_mov_b64 exec, -1\ns_setprio %[pta]\ns_mov_b32 %[s1], 0x100000\n.LE%=:\nds_read_b128 v[30:33], %[sy] offset:%[opg]\n"
        "s_waitcnt lgkmcnt(0)\nv_min3_u32 v30, v30, v31, v32\nv_min_u32 v30, v30, v33\ns_nop 0\nv_readfirstlane_b32 %[s0], v30\ns_nop 0\ns_cmp_ge_u32 %[s0], %[pgt]\ns_cbranch_scc1 .LEx%=\n"
        "s_sleep %[aps]\ns_sub_u32 %[s1], %[s1], 1\ns_cbranch_scc0 .LE%=\n.LEx%=:\nds_read_b32 v86, %[rq]\nds_read_b32 v28, %[rk0]\nds_read_b32 v29, %[rk1]\nds_read_b64 v[30:31], %[q0]\n"
        "ds_read_b64 v[32:33], %[q1]\nds_read_b64 v[38:39], %[ka] offset:0\nds_read_b64 v[40:41], %[ka] offset:8\nds_read_b64 v[42:43], %[ka] offset:7936\nds_read_b64 v[44:45], %[ka] offset:7944\n"
        "ds_read_b64 v[34:35], %[q2]\nds_read_b64 v[36:37], %[q3]\nds_read_b64 v[46:47], %[ka] offset:40\nds_read_b64 v[48:49], %[ka] offset:48\nds_read_b64 v[50:51], %[ka] offset:7976\n"
        "ds_read_b64 v[52:53], %[ka] offset:7984\nds_read_b64_tr_b16 v[54:55], %[va] offset:0\nds_read_b64_tr_b16 v[56:57], %[va] offset:7936\nds_read_b64_tr_b16 v[58:59], %[va] offset:32\n"
        "ds_read_b64_tr_b16 v[60:61], %[va] offset:7968\nds_read_b64_tr_b16 v[62:63], %[va] offset:40\nds_read_b64_tr_b16 v[64:65], %[va] offset:7976\nds_read_b64_tr_b16 v[66:67], %[va] offset:72\n"
        "ds_read_b64_tr_b16 v[68:69], %[va] offset:8008\ns_waitcnt lgkmcnt(14)\nv_mfma_f32_16x16x32_bf16 v[70:73], v[38:41], v[30:33], %[bias]\n"
        "v_mfma_f32_16x16x32_bf16 v[74:77], v[42:45], v[30:33], 0\ns_waitcnt lgkmcnt(8)\nv_mfma_f32_16x16x32_bf16 v[78:81], v[46:49], v[34:37], %[bias]\n"
        "v_mfma_f32_16x16x32_bf16 v[82:85], v[50:53], v[34:37], 0\nv_mul_f32 v86, %[nc], v86\ns_nop 0\nv_mul_f32 v87, v86, v29\nv_mul_f32 v86, v86, v28\ns_nop 0\nv_cndmask_b32 v70, v70, v71, %[par]\n"
        "v_cndmask_b32 v72, v72, v73, %[par]\nv_cndmask_b32 v74, v74, v75, %[par]\nv_cndmask_b32 v76, v76, v77, %[par]\nv_cndmask_b32 v30, v74, v70, %[c0]\nv_cndmask_b32 v31, v76, v72, %[c1]\n"
        "v_mul_f32 v30, v30, v86\nv_mul_f32 v31, v31, v87\nv_max_f32 v34, v30, v31\nv_cndmask_b32 v78, v78, v79, %[par]\nv_cndmask_b32 v80, v80, v81, %[par]\nv_cndmask_b32 v82, v82, v83, %[par]\n"
        "v_cndmask_b32 v84, v84, v85, %[par]\nv_cndmask_b32 v32, v82, v78, %[c0]\nv_cndmask_b32 v33, v84, v80, %[c1]\nv_mul_f32 v32, v32, v86\nv_mul_f32 v33, v33, v87\nv_max_f32 v35, v32, v33\n"
        "s_nop 1\nv_permlane16_swap_b32 v34, v35\nv_max_f32 v34, v34, v35\nv_mov_b32 v35, v34\ns_nop 1\nv_permlane32_swap_b32 v34, v35\nv_max_f32 v34, v34, v35\nv_mov_b32 v35, v34\ns_nop 1\n"
        "v_permlane16_swap_b32 v34, v35\nv_sub_f32 v30, v30, v34\nv_sub_f32 v31, v31, v34\nv_sub_f32 v32, v32, v35\nv_sub_f32 v33, v33, v35\nv_exp_f32 v30, v30\nv_exp_f32 v31, v31\nv_exp_f32 v32, v32\n"
        "v_exp_f32 v33, v33\nv_add_f32 v34, v30, v31\nv_add_f32 v35, v32, v33\nv_mul_f32 v30, v30, v28\nv_mul_f32 v31, v31, v29\nv_permlane16_swap_b32 v34, v35\nv_add_f32 v34, v34, v35\n"
        "v_mov_b32 v35, v34\nv_mul_f32 v32, v32, v28\nv_mul_f32 v33, v33, v29\nv_permlane32_swap_b32 v34, v35\nv_add_f32 v34, v34, v35\nv_rcp_f32 v34, v34\ns_nop 0\nv_mov_b32 v35, v34\ns_nop 1\n"
        "v_permlane16_swap_b32 v34, v35\nv_mul_f32 v30, v30, v34\nv_mul_f32 v31, v31, v34\nv_mul_f32 v32, v32, v35\nv_mul_f32 v33, v33, v35\nv_cvt_pk_bf16_f32 v30, v30, v31\n"
        "v_cvt_pk_bf16_f32 v32, v32, v33\nv_perm_b32 v38, v30, v30, %[sa0]\nv_perm_b32 v39, v30, v30, %[sa1]\nv_perm_b32 v40, v30, v30, %[sb0]\nv_perm_b32 v41, v30, v30, %[sb1]\n"
        "v_perm_b32 v42, v32, v32, %[sa0]\nv_perm_b32 v43, v32, v32, %[sa1]\nv_perm_b32 v44, v32, v32, %[sb0]\nv_perm_b32 v45, v32, v32, %[sb1]\ns_waitcnt lgkmcnt(0)\n"
        "v_mfma_f32_16x16x32_bf16 v[70:73], v[54:57], v[38:41], 0\nv_mfma_f32_16x16x32_bf16 v[78:81], v[62:65], v[42:45], 0\nv_mfma_f32_16x16x32_bf16 v[74:77], v[58:61], v[38:41], 0\n"
        "v_mfma_f32_16x16x32_bf16 v[82:85], v[66:69], v[42:45], 0\ns_nop 4\nv_cvt_pk_bf16_f32 v46, v70, v71\nv_cvt_pk_bf16_f32 v47, v72, v73\nv_cvt_pk_bf16_f32 v48, v78, v79\n"
        "v_cvt_pk_bf16_f32 v49, v80, v81\nv_min_u32 v46, -2, v46\nv_min_u32 v47, -2, v47\nv_min_u32 v48, -2, v48\nv_min_u32 v49, -2, v49\n"
        "buffer_store_dwordx4 v[46:49], %[l16], %[ors], %[so0] offen sc1\nv_cvt_pk_bf16_f32 v50, v74, v75\nv_cvt_pk_bf16_f32 v51, v76, v77\nv_cvt_pk_bf16_f32 v52, v82, v83\n"
        "v_cvt_pk_bf16_f32 v53, v84, v85\nv_min_u32 v50, -2, v50\nv_min_u32 v51, -2, v51\nv_min_u32 v52, -2, v52\nv_min_u32 v53, -2, v53\n"
        "buffer_store_dwordx4 v[50:53], %[oh], %[ors], %[so1] offen sc1\ns_bitcmp1_b32 0x3c, %[j]\ns_cbranch_scc1 .LF%=\ns_waitcnt vmcnt(2)\nds_write_b128 %[wd], v[128:131] offset:0\n"
        "ds_write_b128 %[wd], v[132:135] offset:1024\nds_write_b128 %[wd], v[144:147] offset:2048\nds_write_b128 %[wd], v[148:151] offset:3072\nds_write_b128 %[wd], v[152:155] offset:4096\n"
        "ds_write_b128 %[wd], v[156:159] offset:5120\ns_cmp_eq_u32 %[j], 1\ns_cbranch_scc1 .LF%=\nds_write_b128 %[wd], v[160:163] offset:6144\nds_write_b128 %[wd], v[164:167] offset:7168\n.LF%=:\n"
        "s_mov_b64 exec, 1\nds_add_u32 %[sy], %[one] offset:%[owo]+4\ns_mov_b64 exec, -1\n"
        : [wd] "+{v107}"(wd), [s0] "=&s"(s0_), [s1] "=&s"(s1_), "+{v[128:131]}"(wst[0]), "+{v[132:135]}"(wst[1]), "+{v[144:147]}"(wst[2]), "+{v[148:151]}"(wst[3]), "+{v[152:155]}"(wst[4]), "+{v[156:159]}"(wst[5]), "+{v[160:163]}"(wst[6]), "+{v[164:167]}"(wst[7])
        : [q0] "{v92}"(qa[0]), [q1] "{v93}"(qa[1]), [q2] "{v94}"(qa[2]), [q3] "{v95}"(qa[3]), [ka] "{v96}"(qka), [va] "{v97}"(vta), [bias] "{v[88:91]}"(bias), [par] "s"(0xaaaaaaaaaaaaaaaaull), [c0] "s"(c0m), [c1] "s"(c1m), [nc] "s"(nc), [rq] "{v103}"(rq), [rk0] "{v104}"(rk0), [rk1] "{v105}"(rk1), [sa0] "{v98}"(sA0), [sa1] "{v99}"(sA1), [sb0] "{v100}"(sB0), [sb1] "{v101}"(sB1), [l16] "{v8}"(l16), [oh] "{v102}"(ohs), [ors] "s"(orsrc), [so0] "s"(so0), [so1] "s"(so1), [wos] "s"(wos), [j] "s"(j), [sy] "{v10}"(sya), [one] "{v11}"(one), [pgt] "s"(pgt), [orv] "i"(SY_RV), [owo] "i"(SY_WO), [opg] "i"(SY_PG), [pta] "i"(PTA), [aps] "i"(APS)
        : "memory", "scc", "v28", "v29", "v30", "v31", "v32", "v33", "v34", "v35", "v36", "v37", "v38", "v39", "v40", "v41", "v42", "v43", "v44", "v45", "v46", "v47", "v48", "v49", "v50", "v51", "v52", "v53", "v54", "v55", "v56", "v57", "v58", "v59", "v60", "v61", "v62", "v63", "v64", "v65", "v66", "v67", "v68", "v69", "v70", "v71", "v72", "v73", "v74", "v75", "v76", "v77", "v78", "v79", "v80", "v81", "v82", "v83", "v84", "v85", "v86", "v87");
    } else {
      asm volatile(
        "s_cmp_lt_u32 %[j], 2\ns_cbranch_scc0 .LB%=\ns_mov_b32 %[s0], 100352\nbuffer_load_dwordx4 v[164:167], %[l16], %[wos], %[s0] offen sc1\ns_mov_b32 %[s0], 61440\n"
        "buffer_load_dwordx4 v[136:139], %[l16], %[wos], %[s0] offen sc1\n.LB%=:\ns_mov_b32 %[s0], 0\nbuffer_load_dwordx4 v[140:143], %[l16], %[wos], %[s0] offen sc1\n"
        "buffer_load_dwordx4 v[144:147], %[l16], %[wos], %[g0] offen sc1\ns_mov_b32 %[s0], 67584\nbuffer_load_dwordx4 v[148:151], %[l16], %[wos], %[s0] offen sc1\ns_mov_b32 %[s0], 75776\n"
        "buffer_load_dwordx4 v[152:155], %[l16], %[wos], %[s0] offen sc1\ns_mov_b32 %[s0], 83968\nbuffer_load_dwordx4 v[156:159], %[l16], %[wos], %[s0] offen sc1\ns_mov_b32 %[s0], 92160\n"
        "buffer_load_dwordx4 v[160:163], %[l16], %[wos], %[s0] offen sc1\ns_mov_b32 %[s0], 12288\nbuffer_load_dwordx4 v[112:115], %[l16], %[wos], %[s0] offen sc1\ns_mov_b32 %[s0], 20480\n"
        "buffer_load_dwordx4 v[116:119], %[l16], %[wos], %[s0] offen sc1\ns_mov_b32 %[s0], 28672\nbuffer_load_dwordx4 v[120:123], %[l16], %[wos], %[s0] offen sc1\ns_mov_b32 %[s0], 36864\n"
        "buffer_load_dwordx4 v[124:127], %[l16], %[wos], %[s0] offen sc1\ns_mov_b32 %[s0], 45056\nbuffer_load_dwordx4 v[128:131], %[l16], %[wos], %[s0] offen sc1\ns_mov_b32 %[s0], 53248\n"
        "buffer_load_dwordx4 v[132:135], %[l16], %[wos], %[s0] offen sc1\ns_mov_b32 %[s1], 0x100000\n.LC%=:\nds_read_b32 v30, %[sy] offset:%[orv]\ns_waitcnt lgkmcnt(0)\nv_readfirstlane_b32 %[s0], v30\n"
        "s_nop 0\ns_cmp_ge_u32 %[s0], 8\ns_cbranch_scc1 .LCx%=\ns_sleep 1\ns_sub_u32 %[s1], %[s1], 1\ns_cbranch_scc0 .LC%=\n.LCx%=:\ns_waitcnt vmcnt(11)\nds_write_b128 %[wd], v[140:143] offset:0\n"
        "v_add_u32 %[wd], 67584, %[wd]\ns_waitcnt vmcnt(10)\nds_write_b128 %[wd0], v[144:147] offset:0\ns_waitcnt vmcnt(9)\nds_write_b128 %[wd], v[148:151] offset:0\ns_waitcnt vmcnt(8)\n"
        "ds_write_b128 %[wd], v[152:155] offset:8192\ns_waitcnt vmcnt(7)\nds_write_b128 %[wd], v[156:159] offset:16384\ns_waitcnt vmcnt(6)\nds_write_b128 %[wd], v[160:163] offset:24576\n"
        "s_cmp_lt_u32 %[j], 2\ns_cbranch_scc0 .LD%=\nds_write_b128 %[wd], v[164:167] offset:32768\n.LD%=:\nv_subrev_u32 %[wd], 55296, %[wd]\ns_mov_b64 exec, 1\nds_add_u32 %[sy], %[one] offset:%[owo]\n"
        "s_mov_b64 exec, -1\ns_setprio %[pta]\ns_mov_b32 %[s1], 0x100000\n.LE%=:\nds_read_b128 v[30:33], %[sy] offset:%[opg]\ns_waitcnt lgkmcnt(0)\nv_min3_u32 v30, v30, v31, v32\n"
        "v_min_u32 v30, v30, v33\ns_nop 0\nv_readfirstlane_b32 %[s0], v30\ns_nop 0\ns_cmp_ge_u32 %[s0], %[pgt]\ns_cbranch_scc1 .LEx%=\ns_sleep %[aps]\ns_sub_u32 %[s1], %[s1], 1\ns_cbranch_scc0 .LE%=\n"
        ".LEx%=:\nds_read_b32 v86, %[rq]\nds_read_b32 v28, %[rk0]\nds_read_b32 v29, %[rk1]\nds_read_b64 v[30:31], %[q0]\nds_read_b64 v[32:33], %[q1]\nds_read_b64 v[38:39], %[ka] offset:0\n"
        "ds_read_b64 v[40:41], %[ka] offset:8\nds_read_b64 v[42:43], %[ka] offset:7936\nds_read_b64 v[44:45], %[ka] offset:7944\nds_read_b64 v[34:35], %[q2]\nds_read_b64 v[36:37], %[q3]\n"
        "ds_read_b64 v[46:47], %[ka] offset:40\nds_read_b64 v[48:49], %[ka] offset:48\nds_read_b64 v[50:51], %[ka] offset:7976\nds_read_b64 v[52:53], %[ka] offset:7984\n"
        "ds_read_b64_tr_b16 v[54:55], %[va] offset:0\nds_read_b64_tr_b16 v[56:57], %[va] offset:7936\nds_read_b64_tr_b16 v[58:59], %[va] offset:32\nds_read_b64_tr_b16 v[60:61], %[va] offset:7968\n"
        "ds_read_b64_tr_b16 v[62:63], %[va] offset:40\nds_read_b64_tr_b16 v[64:65], %[va] offset:7976\nds_read_b64_tr_b16 v[66:67], %[va] offset:72\nds_read_b64_tr_b16 v[68:69], %[va] offset:8008\n"
        "s_waitcnt lgkmcnt(14)\nv_mfma_f32_16x16x32_bf16 v[70:73], v[38:41], v[30:33], %[bias]\nv_mfma_f32_16x16x32_bf16 v[74:77], v[42:45], v[30:33], 0\ns_waitcnt lgkmcnt(8)\n"
        "v_mfma_f32_16x16x32_bf16 v[78:81], v[46:49], v[34:37], %[bias]\nv_mfma_f32_16x16x32_bf16 v[82:85], v[50:53], v[34:37], 0\nv_mul_f32 v86, %[nc], v86\ns_nop 0\nv_mul_f32 v87, v86, v29\n"
        "v_mul_f32 v86, v86, v28\ns_nop 0\nv_cndmask_b32 v70, v70, v71, %[par]\nv_cndmask_b32 v72, v72, v73, %[par]\nv_cndmask_b32 v74, v74, v75, %[par]\nv_cndmask_b32 v76, v76, v77, %[par]\n"
        "v_cndmask_b32 v30, v74, v70, %[c0]\nv_cndmask_b32 v31, v76, v72, %[c1]\nv_mul_f32 v30, v30, v86\nv_mul_f32 v31, v31, v87\nv_max_f32 v34, v30, v31\nv_cndmask_b32 v78, v78, v79, %[par]\n"
        "v_cndmask_b32 v80, v80, v81, %[par]\nv_cndmask_b32 v82, v82, v83, %[par]\nv_cndmask_b32 v84, v84, v85, %[par]\nv_cndmask_b32 v32, v82, v78, %[c0]\nv_cndmask_b32 v33, v84, v80, %[c1]\n"
        "v_mul_f32 v32, v32, v86\nv_mul_f32 v33, v33, v87\nv_max_f32 v35, v32, v33\ns_nop 1\nv_permlane16_swap_b32 v34, v35\nv_max_f32 v34, v34, v35\nv_mov_b32 v35, v34\ns_nop 1\n"
        "v_permlane32_swap_b32 v34, v35\nv_max_f32 v34, v34, v35\nv_mov_b32 v35, v34\ns_nop 1\nv_permlane16_swap_b32 v34, v35\nv_sub_f32 v30, v30, v34\nv_sub_f32 v31, v31, v34\n"
        "v_sub_f32 v32, v32, v35\nv_sub_f32 v33, v33, v35\nv_exp_f32 v30, v30\nv_exp_f32 v31, v31\nv_exp_f32 v32, v32\nv_exp_f32 v33, v33\nv_add_f32 v34, v30, v31\nv_add_f32 v35, v32, v33\n"
        "v_mul_f32 v30, v30, v28\nv_mul_f32 v31, v31, v29\nv_permlane16_swap_b32 v34, v35\nv_add_f32 v34, v34, v35\nv_mov_b32 v35, v34\nv_mul_f32 v32, v32, v28\nv_mul_f32 v33, v33, v29\n"
        "v_permlane32_swap_b32 v34, v35\nv_add_f32 v34, v34, v35\nv_rcp_f32 v34, v34\ns_nop 0\nv_mov_b32 v35, v34\ns_nop 1\nv_permlane16_swap_b32 v34, v35\nv_mul_f32 v30, v30, v34\n"
        "v_mul_f32 v31, v31, v34\nv_mul_f32 v32, v32, v35\nv_mul_f32 v33, v33, v35\nv_cvt_pk_bf16_f32 v30, v30, v31\nv_cvt_pk_bf16_f32 v32, v32, v33\nv_perm_b32 v38, v30, v30, %[sa0]\n"
        "v_perm_b32 v39, v30, v30, %[sa1]\nv_perm_b32 v40, v30, v30, %[sb0]\nv_perm_b32 v41, v30, v30, %[sb1]\nv_perm_b32 v42, v32, v32, %[sa0]\nv_perm_b32 v43, v32, v32, %[sa1]\n"
        "v_perm_b32 v44, v32, v32, %[sb0]\nv_perm_b32 v45, v32, v32, %[sb1]\ns_waitcnt lgkmcnt(0)\nv_mfma_f32_16x16x32_bf16 v[70:73], v[54:57], v[38:41], 0\n"
        "v_mfma_f32_16x16x32_bf16 v[78:81], v[62:65], v[42:45], 0\nv_mfma_f32_16x16x32_bf16 v[74:77], v[58:61], v[38:41], 0\nv_mfma_f32_16x16x32_bf16 v[82:85], v[66:69], v[42:45], 0\ns_nop 4\n"
        "v_cvt_pk_bf16_f32 v46, v70, v71\nv_cvt_pk_bf16_f32 v47, v72, v73\nv_cvt_pk_bf16_f32 v48, v78, v79\nv_cvt_pk_bf16_f32 v49, v80, v81\nv_min_u32 v46, -2, v46\nv_min_u32 v47, -2, v47\n"
        "v_min_u32 v48, -2, v48\nv_min_u32 v49, -2, v49\nbuffer_store_dwordx4 v[46:49], %[l16], %[ors], %[so0] offen sc1\nv_cvt_pk_bf16_f32 v50, v74, v75\nv_cvt_pk_bf16_f32 v51, v76, v77\n"
        "v_cvt_pk_bf16_f32 v52, v82, v83\nv_cvt_pk_bf16_f32 v53, v84, v85\nv_min_u32 v50, -2, v50\nv_min_u32 v51, -2, v51\nv_min_u32 v52, -2, v52\nv_min_u32 v53, -2, v53\n"
        "buffer_store_dwordx4 v[50:53], %[oh], %[ors], %[so1] offen sc1\ns_waitcnt vmcnt(2)\nds_write_b128 %[wd], v[112:115] offset:0\nds_write_b128 %[wd], v[116:119] offset:8192\n"
        "ds_write_b128 %[wd], v[120:123] offset:16384\nds_write_b128 %[wd], v[124:127] offset:24576\nds_write_b128 %[wd], v[128:131] offset:32768\nds_write_b128 %[wd], v[132:135] offset:40960\n"
        "s_cmp_lt_u32 %[j], 2\ns_cbranch_scc0 .LF%=\nds_write_b128 %[wd], v[136:139] offset:49152\n.LF%=:\ns_mov_b64 exec, 1\nds_add_u32 %[sy], %[one] offset:%[owo]+4\ns_mov_b64 exec, -1\n"
        : [wd] "+{v107}"(wd), [s0] "=&s"(s0_), [s1] "=&s"(s1_)
        : [q0] "{v92}"(qa[0]), [q1] "{v93}"(qa[1]), [q2] "{v94}"(qa[2]), [q3] "{v95}"(qa[3]), [ka] "{v96}"(qka), [va] "{v97}"(vta), [bias] "{v[88:91]}"(bias), [par] "s"(0xaaaaaaaaaaaaaaaaull), [c0] "s"(c0m), [c1] "s"(c1m), [nc] "s"(nc), [rq] "{v103}"(rq), [rk0] "{v104}"(rk0), [rk1] "{v105}"(rk1), [sa0] "{v98}"(sA0), [sa1] "{v99}"(sA1), [sb0] "{v100}"(sB0), [sb1] "{v101}"(sB1), [l16] "{v8}"(l16), [oh] "{v102}"(ohs), [ors] "s"(orsrc), [so0] "s"(so0), [so1] "s"(so1), [wos] "s"(wos), [j] "s"(j), [sy] "{v10}"(sya), [one] "{v11}"(one), [pgt] "s"(pgt), [orv] "i"(SY_RV), [owo] "i"(SY_WO), [opg] "i"(SY_PG), [pta] "i"(PTA), [aps] "i"(APS), [wd0] "{v106}"(wd0), [g0] "s"(wg)
        : "memory", "scc", "v28", "v29", "v30", "v31", "v32", "v33", "v34", "v35", "v36", "v37", "v38", "v39", "v40", "v41", "v42", "v43", "v44", "v45", "v46", "v47", "v48", "v49", "v50", "v51", "v52", "v53", "v54", "v55", "v56", "v57", "v58", "v59", "v60", "v61", "v62", "v63", "v64", "v65", "v66", "v67", "v68", "v69", "v70", "v71", "v72", "v73", "v74", "v75", "v76", "v77", "v78", "v79", "v80", "v81", "v82", "v83", "v84", "v85", "v86", "v87", "v112", "v113", "v114", "v115", "v116", "v117", "v118", "v119", "v120", "v121", "v122", "v123", "v124", "v125", "v126", "v127", "v128", "v129", "v130", "v131", "v132", "v133", "v134", "v135", "v136", "v137", "v138", "v139", "v140", "v141", "v142", "v143", "v144", "v145", "v146", "v147", "v148", "v149", "v150", "v151", "v152", "v153", "v154", "v155", "v156", "v157", "v158", "v159", "v160", "v161", "v162", "v163", "v164", "v165", "v166", "v167");
    }
  }
  {
    u32 tm;
    constexpr int NFR = 8;
    int cap, s0_, s1_, rt = 0;
      asm volatile(
        "s_mov_b32 %[cap], 0x10000\ns_cmp_eq_u32 %[kh], 0\ns_cbranch_scc1 .LS0_%=\ns_sleep %[sl1]\nbuffer_load_dwordx4 v[28:31], %[l16], %[ors], %[o0] offen offset:0 sc1\n"
        "buffer_load_dwordx4 v[32:35], %[l16], %[ors], %[o1] offen offset:0 sc1\nbuffer_load_dwordx4 v[36:39], %[l16], %[ors], %[o2] offen offset:0 sc1\n"
        "buffer_load_dwordx4 v[40:43], %[l16], %[ors], %[o3] offen offset:0 sc1\nbuffer_load_dwordx4 v[44:47], %[oha], %[ors], %[o4] offen offset:0 sc1\n.LS0_%=:\ns_mov_b32 %[s1], 0x100000\n.LWa%=:\n"
        "ds_read_b32 %[tm], %[sy] offset:%[ow1]-4\ns_waitcnt lgkmcnt(0)\nv_readfirstlane_b32 %[s0], %[tm]\ns_nop 0\ns_cmp_ge_i32 %[s0], %[nwo]\ns_cbranch_scc1 .LXa%=\ns_sleep 1\n"
        "s_sub_u32 %[s1], %[s1], 1\ns_cbranch_scc0 .LWa%=\n.LXa%=:\nds_read_b128 v[68:71], %[bb] offset:0\nds_read_b128 v[72:75], %[bb] offset:1024\nds_read_b128 v[76:79], %[bb] offset:2048\n"
        "ds_read_b128 v[80:83], %[bb] offset:3072\nds_read_b128 v[84:87], %[bb] offset:4096\nds_read_b128 v[88:91], %[bb] offset:5120\nv_add_u32 %[bb], %[d1], %[bb]\n"
        "ds_read_b128 v[92:95], %[bb] offset:6144\nds_read_b128 v[96:99], %[bb] offset:7168\nds_read_b128 v[100:103], %[bb] offset:8192\nds_read_b128 v[104:107], %[bb] offset:9216\n"
        "ds_read_b128 v[108:111], %[bb] offset:10240\nds_read_b128 v[112:115], %[bb] offset:11264\nds_read_b128 v[116:119], %[bb] offset:12288\nds_read_b128 v[120:123], %[bb] offset:13312\n"
        "ds_read_b128 v[124:127], %[bb] offset:14336\nds_read_b128 v[128:131], %[bb] offset:15360\nds_read_b128 v[132:135], %[bb] offset:16384\nds_read_b128 v[136:139], %[bb] offset:17408\n"
        "ds_read_b128 v[140:143], %[bb] offset:18432\nds_read_b128 v[144:147], %[bb] offset:19456\nds_read_b128 v[148:151], %[bb] offset:20480\nds_read_b128 v[152:155], %[bb] offset:21504\n"
        "ds_read_b128 v[156:159], %[bb] offset:22528\nds_read_b128 v[160:163], %[bb] offset:23552\nds_read_b128 v[164:167], %[bb] offset:24576\ns_mov_b64 exec, 1\n"
        "ds_add_u32 %[sy], %[one] offset:%[ofr]\ns_mov_b64 exec, -1\nv_add_u32 %[bb], %[d2], %[bb]\nv_lshlrev_b32 v48, 16, v22\nv_and_b32 v49, 0xffff0000, v22\nv_lshlrev_b32 v50, 16, v23\n"
        "v_and_b32 v51, 0xffff0000, v23\nv_lshlrev_b32 v52, 16, v24\nv_and_b32 v53, 0xffff0000, v24\nv_lshlrev_b32 v54, 16, v25\nv_and_b32 v55, 0xffff0000, v25\nv_lshlrev_b32 v56, 16, v26\n"
        "v_and_b32 v57, 0xffff0000, v26\nv_lshlrev_b32 v58, 16, v27\nv_and_b32 v59, 0xffff0000, v27\ns_cmp_eq_u32 %[kh], 0\ns_cbranch_scc0 .LS1_%=\ns_sleep %[sl0]\n"
        "buffer_load_dwordx4 v[28:31], %[l16], %[ors], %[o0] offen offset:0 sc1\nbuffer_load_dwordx4 v[32:35], %[l16], %[ors], %[o1] offen offset:0 sc1\n"
        "buffer_load_dwordx4 v[36:39], %[l16], %[ors], %[o2] offen offset:0 sc1\nbuffer_load_dwordx4 v[40:43], %[l16], %[ors], %[o3] offen offset:0 sc1\n"
        "buffer_load_dwordx4 v[44:47], %[oha], %[ors], %[o4] offen offset:0 sc1\n.LS1_%=:\ns_mov_b32 %[s1], 0x100000\n.LWb%=:\nds_read_b32 %[tm], %[sy] offset:%[ow1]\ns_waitcnt lgkmcnt(0)\n"
        "v_readfirstlane_b32 %[s0], %[tm]\ns_nop 0\ns_cmp_ge_i32 %[s0], %[nwo]\ns_cbranch_scc1 .LXb%=\ns_sleep 1\ns_sub_u32 %[s1], %[s1], 1\ns_cbranch_scc0 .LWb%=\n.LXb%=:\n.LC0_%=:\n"
        "s_waitcnt vmcnt(4)\nv_max_u32 %[tm], v28, v29\nv_max3_u32 %[tm], %[tm], v30, v31\ns_waitcnt vmcnt(3)\nv_max3_u32 %[tm], %[tm], v32, v33\nv_max3_u32 %[tm], %[tm], v34, v35\ns_waitcnt vmcnt(2)\n"
        "v_max3_u32 %[tm], %[tm], v36, v37\nv_max3_u32 %[tm], %[tm], v38, v39\ns_waitcnt vmcnt(1)\nv_max3_u32 %[tm], %[tm], v40, v41\nv_max3_u32 %[tm], %[tm], v42, v43\ns_waitcnt vmcnt(0)\n"
        "v_max3_u32 %[tm], %[tm], v44, v45\nv_max3_u32 %[tm], %[tm], v46, v47\nv_cmp_eq_u32 vcc, -1, %[tm]\ns_cbranch_vccnz .LR0_%=\n.LK0_%=:\nv_mfma_f32_16x16x32_bf16 v[68:71], v[28:31], v[68:71], 0\n"
        "v_mfma_f32_16x16x32_bf16 v[72:75], v[28:31], v[72:75], 0\nv_mfma_f32_16x16x32_bf16 v[76:79], v[28:31], v[76:79], v[48:51]\nv_mfma_f32_16x16x32_bf16 v[80:83], v[28:31], v[80:83], v[52:55]\n"
        "v_mfma_f32_16x16x32_bf16 v[84:87], v[28:31], v[84:87], v[56:59]\nbuffer_load_dwordx4 v[48:51], %[l16], %[ors], %[o0] offen offset:1024 sc1\n"
        "buffer_load_dwordx4 v[52:55], %[l16], %[ors], %[o1] offen offset:1024 sc1\nbuffer_load_dwordx4 v[56:59], %[l16], %[ors], %[o2] offen offset:1024 sc1\n"
        "buffer_load_dwordx4 v[60:63], %[l16], %[ors], %[o3] offen offset:1024 sc1\nbuffer_load_dwordx4 v[64:67], %[oha], %[ors], %[o4] offen offset:256 sc1\n"
        "v_mfma_f32_16x16x32_bf16 v[68:71], v[32:35], v[88:91], v[68:71]\nds_read_b128 v[88:91], %[bb] offset:0\nv_mfma_f32_16x16x32_bf16 v[72:75], v[32:35], v[92:95], v[72:75]\n"
        "ds_read_b128 v[92:95], %[bb] offset:5120\nv_mfma_f32_16x16x32_bf16 v[76:79], v[32:35], v[96:99], v[76:79]\nds_read_b128 v[96:99], %[bb] offset:10240\n"
        "v_mfma_f32_16x16x32_bf16 v[80:83], v[32:35], v[100:103], v[80:83]\nds_read_b128 v[100:103], %[bb] offset:15360\nv_mfma_f32_16x16x32_bf16 v[84:87], v[32:35], v[104:107], v[84:87]\n"
        "ds_read_b128 v[104:107], %[bb] offset:20480\nv_mfma_f32_16x16x32_bf16 v[68:71], v[36:39], v[108:111], v[68:71]\nds_read_b128 v[108:111], %[bb] offset:1024\n"
        "v_mfma_f32_16x16x32_bf16 v[72:75], v[36:39], v[112:115], v[72:75]\nds_read_b128 v[112:115], %[bb] offset:6144\nv_mfma_f32_16x16x32_bf16 v[76:79], v[36:39], v[116:119], v[76:79]\n"
        "ds_read_b128 v[116:119], %[bb] offset:11264\nv_mfma_f32_16x16x32_bf16 v[80:83], v[36:39], v[120:123], v[80:83]\nds_read_b128 v[120:123], %[bb] offset:16384\n"
        "v_mfma_f32_16x16x32_bf16 v[84:87], v[36:39], v[124:127], v[84:87]\nds_read_b128 v[124:127], %[bb] offset:21504\nv_mfma_f32_16x16x32_bf16 v[68:71], v[40:43], v[128:131], v[68:71]\n"
        "ds_read_b128 v[128:131], %[bb] offset:2048\nv_mfma_f32_16x16x32_bf16 v[72:75], v[40:43], v[132:135], v[72:75]\nds_read_b128 v[132:135], %[bb] offset:7168\n"
        "v_mfma_f32_16x16x32_bf16 v[76:79], v[40:43], v[136:139], v[76:79]\nds_read_b128 v[136:139], %[bb] offset:12288\nv_mfma_f32_16x16x32_bf16 v[80:83], v[40:43], v[140:143], v[80:83]\n"
        "ds_read_b128 v[140:143], %[bb] offset:17408\nv_mfma_f32_16x16x32_bf16 v[84:87], v[40:43], v[144:147], v[84:87]\nds_read_b128 v[144:147], %[bb] offset:22528\n"
        "v_mfma_f32_16x16x32_bf16 v[68:71], v[44:47], v[148:151], v[68:71]\nds_read_b128 v[148:151], %[bb] offset:3072\nv_mfma_f32_16x16x32_bf16 v[72:75], v[44:47], v[152:155], v[72:75]\n"
        "ds_read_b128 v[152:155], %[bb] offset:8192\nv_mfma_f32_16x16x32_bf16 v[76:79], v[44:47], v[156:159], v[76:79]\nds_read_b128 v[156:159], %[bb] offset:13312\n"
        "v_mfma_f32_16x16x32_bf16 v[80:83], v[44:47], v[160:163], v[80:83]\nds_read_b128 v[160:163], %[bb] offset:18432\nv_mfma_f32_16x16x32_bf16 v[84:87], v[44:47], v[164:167], v[84:87]\n"
        "ds_read_b128 v[164:167], %[bb] offset:23552\nds_read_b128 v[28:31], %[bb] offset:4096\nds_read_b128 v[32:35], %[bb] offset:9216\nds_read_b128 v[36:39], %[bb] offset:14336\n"
        "ds_read_b128 v[40:43], %[bb] offset:19456\nds_read_b128 v[44:47], %[bb] offset:24576\ns_mov_b32 %[s1], 0x100000\n.LWc%=:\nds_read_b32 %[tm], %[sy] offset:%[ofr]\ns_waitcnt lgkmcnt(0)\n"
        "v_readfirstlane_b32 %[s0], %[tm]\ns_nop 0\ns_cmp_ge_i32 %[s0], %[nfr]\ns_cbranch_scc1 .LXc%=\ns_sleep 1\ns_sub_u32 %[s1], %[s1], 1\ns_cbranch_scc0 .LWc%=\n.LXc%=:\n.LC1_%=:\n"
        "s_waitcnt vmcnt(4)\nv_max_u32 %[tm], v48, v49\nv_max3_u32 %[tm], %[tm], v50, v51\ns_waitcnt vmcnt(3)\nv_max3_u32 %[tm], %[tm], v52, v53\nv_max3_u32 %[tm], %[tm], v54, v55\ns_waitcnt vmcnt(2)\n"
        "v_max3_u32 %[tm], %[tm], v56, v57\nv_max3_u32 %[tm], %[tm], v58, v59\ns_waitcnt vmcnt(1)\nv_max3_u32 %[tm], %[tm], v60, v61\nv_max3_u32 %[tm], %[tm], v62, v63\ns_waitcnt vmcnt(0)\n"
        "v_max3_u32 %[tm], %[tm], v64, v65\nv_max3_u32 %[tm], %[tm], v66, v67\nv_cmp_eq_u32 vcc, -1, %[tm]\ns_cbranch_vccnz .LR1_%=\n.LK1_%=:\n"
        "v_mfma_f32_16x16x32_bf16 v[68:71], v[48:51], v[88:91], v[68:71]\nv_mfma_f32_16x16x32_bf16 v[68:71], v[52:55], v[92:95], v[68:71]\n"
        "v_mfma_f32_16x16x32_bf16 v[68:71], v[56:59], v[96:99], v[68:71]\nv_mfma_f32_16x16x32_bf16 v[68:71], v[60:63], v[100:103], v[68:71]\n"
        "v_mfma_f32_16x16x32_bf16 v[68:71], v[64:67], v[104:107], v[68:71]\nv_mfma_f32_16x16x32_bf16 v[72:75], v[48:51], v[108:111], v[72:75]\n"
        "v_mfma_f32_16x16x32_bf16 v[72:75], v[52:55], v[112:115], v[72:75]\nv_mfma_f32_16x16x32_bf16 v[72:75], v[56:59], v[116:119], v[72:75]\nds_write_b128 %[rw], v[68:71]\n"
        "v_mfma_f32_16x16x32_bf16 v[72:75], v[60:63], v[120:123], v[72:75]\nv_mfma_f32_16x16x32_bf16 v[72:75], v[64:67], v[124:127], v[72:75]\n"
        "v_mfma_f32_16x16x32_bf16 v[76:79], v[48:51], v[128:131], v[76:79]\nv_mfma_f32_16x16x32_bf16 v[76:79], v[52:55], v[132:135], v[76:79]\n"
        "v_mfma_f32_16x16x32_bf16 v[76:79], v[56:59], v[136:139], v[76:79]\nds_write_b128 %[rw], v[72:75] offset:1024\nv_mfma_f32_16x16x32_bf16 v[76:79], v[60:63], v[140:143], v[76:79]\n"
        "ds_write_b32 %[fw], %[f0]\nv_mfma_f32_16x16x32_bf16 v[76:79], v[64:67], v[144:147], v[76:79]\nv_mfma_f32_16x16x32_bf16 v[80:83], v[48:51], v[148:151], v[80:83]\n"
        "v_mfma_f32_16x16x32_bf16 v[80:83], v[52:55], v[152:155], v[80:83]\nv_mfma_f32_16x16x32_bf16 v[80:83], v[56:59], v[156:159], v[80:83]\nds_write_b128 %[rw], v[76:79] offset:2048\n"
        "v_mfma_f32_16x16x32_bf16 v[80:83], v[60:63], v[160:163], v[80:83]\nds_write_b32 %[fw], %[one]\nv_mfma_f32_16x16x32_bf16 v[80:83], v[64:67], v[164:167], v[80:83]\n"
        "v_mfma_f32_16x16x32_bf16 v[84:87], v[48:51], v[28:31], v[84:87]\nv_mfma_f32_16x16x32_bf16 v[84:87], v[52:55], v[32:35], v[84:87]\n"
        "v_mfma_f32_16x16x32_bf16 v[84:87], v[56:59], v[36:39], v[84:87]\nv_mfma_f32_16x16x32_bf16 v[84:87], v[60:63], v[40:43], v[84:87]\n"
        "v_mfma_f32_16x16x32_bf16 v[84:87], v[64:67], v[44:47], v[84:87]\ns_mov_b32 %[s1], 0x100000\n.LY%=:\nds_read_b32 %[tm], %[fr]\nds_read_b128 v[96:99], %[rr] offset:2048\n"
        "ds_read_b128 v[92:95], %[rr] offset:1024\nds_read_b128 v[88:91], %[rr] offset:0\ns_waitcnt lgkmcnt(3)\nv_readfirstlane_b32 %[s0], %[tm]\ns_waitcnt lgkmcnt(0)\ns_cmp_lg_u32 %[s0], 0\n"
        "s_cbranch_scc1 .LZ%=\ns_sub_u32 %[s1], %[s1], 1\ns_cbranch_scc0 .LY%=\n.LZ%=:\nv_add_f32 v80, v80, v92\nv_add_f32 v84, v84, v96\nv_add_f32 v81, v81, v93\nv_add_f32 v85, v85, v97\n"
        "v_add_f32 v82, v82, v94\nv_add_f32 v86, v86, v98\nv_add_f32 v83, v83, v95\nv_add_f32 v87, v87, v99\nv_cvt_pk_bf16_f32 v92, v80, v81\nv_cvt_pk_bf16_f32 v96, v84, v85\n"
        "v_cvt_pk_bf16_f32 v93, v82, v83\nv_cvt_pk_bf16_f32 v97, v86, v87\nv_min_u32 v92, -2, v92\nv_min_u32 v96, -2, v96\nv_min_u32 v93, -2, v93\nv_min_u32 v97, -2, v97\n"
        "ds_write_b64 %[xo], v[92:93] offset:2304\nds_write_b64 %[xo], v[96:97] offset:4608\ns_cmp_eq_u32 %[kh], 0\nv_add_f32 v100, v80, v84\nv_add_f32 v101, v81, v85\nv_add_f32 v102, v82, v86\n"
        "v_add_f32 v103, v83, v87\nv_mul_f32 v104, v84, v84\nv_mul_f32 v105, v85, v85\nv_mul_f32 v106, v86, v86\nv_mul_f32 v107, v87, v87\nv_fmac_f32 v104, v80, v80\nv_fmac_f32 v105, v81, v81\n"
        "v_fmac_f32 v106, v82, v82\nv_fmac_f32 v107, v83, v83\ns_cbranch_scc0 .LV%=\nv_add_f32 v76, v76, v88\nv_add_f32 v77, v77, v89\nv_add_f32 v78, v78, v90\nv_add_f32 v79, v79, v91\n"
        "v_cvt_pk_bf16_f32 v88, v76, v77\nv_cvt_pk_bf16_f32 v89, v78, v79\nv_min_u32 v88, -2, v88\nv_min_u32 v89, -2, v89\nds_write_b64 %[xo], v[88:89] offset:0\nv_add_f32 v100, v100, v76\n"
        "v_add_f32 v101, v101, v77\nv_add_f32 v102, v102, v78\nv_add_f32 v103, v103, v79\nv_fmac_f32 v104, v76, v76\nv_fmac_f32 v105, v77, v77\nv_fmac_f32 v106, v78, v78\nv_fmac_f32 v107, v79, v79\n"
        ".LV%=:\ns_mov_b64 exec, 1\nds_add_u32 %[sy], %[one] offset:%[ox1]\ns_mov_b64 exec, -1\nv_add_f32_dpp v100, v100, v100 row_ror:8 row_mask:0xf bank_mask:0x3\n"
        "v_add_f32_dpp v101, v101, v101 row_ror:8 row_mask:0xf bank_mask:0x3\nv_add_f32_dpp v104, v104, v104 row_ror:8 row_mask:0xf bank_mask:0x3\n"
        "v_add_f32_dpp v105, v105, v105 row_ror:8 row_mask:0xf bank_mask:0x3\nv_add_f32_dpp v100, v102, v102 row_ror:8 row_mask:0xf bank_mask:0xc\n"
        "v_add_f32_dpp v101, v103, v103 row_ror:8 row_mask:0xf bank_mask:0xc\nv_add_f32_dpp v104, v106, v106 row_ror:8 row_mask:0xf bank_mask:0xc\n"
        "v_add_f32_dpp v105, v107, v107 row_ror:8 row_mask:0xf bank_mask:0xc\nv_add_f32_dpp v102, v100, v100 row_half_mirror row_mask:0xf bank_mask:0x5\n"
        "v_add_f32_dpp v103, v104, v104 row_half_mirror row_mask:0xf bank_mask:0x5\ns_nop 0\nv_add_f32_dpp v102, v101, v101 row_half_mirror row_mask:0xf bank_mask:0xa\n"
        "v_add_f32_dpp v103, v105, v105 row_half_mirror row_mask:0xf bank_mask:0xa\ns_nop 0\nv_add_f32_dpp v102, v102, v102 quad_perm:[1,0,3,2] row_mask:0xf bank_mask:0xf\n"
        "v_add_f32_dpp v103, v103, v103 quad_perm:[1,0,3,2] row_mask:0xf bank_mask:0xf\ns_nop 0\nv_add_f32_dpp v102, v102, v102 quad_perm:[2,3,0,1] row_mask:0xf bank_mask:0xf\n"
        "v_add_f32_dpp v103, v103, v103 quad_perm:[2,3,0,1] row_mask:0xf bank_mask:0xf\nv_min_u32 v102, -2, v102\nv_min_u32 v103, -2, v103\n"
        "buffer_store_dwordx2 v[102:103], %[sv], %[srs], %[ss] offen sc1\ns_branch .LE%=\n.LR0_%=:\ns_sub_u32 %[cap], %[cap], 1\ns_cbranch_scc0 .LQ0_%=\ns_mov_b32 %[cap], 0\ns_branch .LK0_%=\n"
        ".LQ0_%=:\ns_sleep 2\nbuffer_load_dwordx4 v[28:31], %[l16], %[ors], %[o0] offen offset:0 sc1\nbuffer_load_dwordx4 v[32:35], %[l16], %[ors], %[o1] offen offset:0 sc1\n"
        "buffer_load_dwordx4 v[36:39], %[l16], %[ors], %[o2] offen offset:0 sc1\nbuffer_load_dwordx4 v[40:43], %[l16], %[ors], %[o3] offen offset:0 sc1\n"
        "buffer_load_dwordx4 v[44:47], %[oha], %[ors], %[o4] offen offset:0 sc1\ns_branch .LC0_%=\n.LR1_%=:\ns_sub_u32 %[cap], %[cap], 1\ns_cbranch_scc0 .LQ1_%=\ns_mov_b32 %[cap], 0\ns_branch .LK1_%=\n"
        ".LQ1_%=:\ns_sleep 2\nbuffer_load_dwordx4 v[48:51], %[l16], %[ors], %[o0] offen offset:1024 sc1\nbuffer_load_dwordx4 v[52:55], %[l16], %[ors], %[o1] offen offset:1024 sc1\n"
        "buffer_load_dwordx4 v[56:59], %[l16], %[ors], %[o2] offen offset:1024 sc1\nbuffer_load_dwordx4 v[60:63], %[l16], %[ors], %[o3] offen offset:1024 sc1\n"
        "buffer_load_dwordx4 v[64:67], %[oha], %[ors], %[o4] offen offset:256 sc1\ns_branch .LC1_%=\n.LE%=:\n"
        : [tm] "=&v"(tm), [bb] "+{v19}"(bb), [cap] "=&s"(cap), [s0] "=&s"(s0_), [s1] "=&s"(s1_), "+{v20}"(xi_), "+{v21}"(xg_)
        : "{v[22:23]}"(rv[0]), "{v[24:25]}"(rv[1]), "{v[26:27]}"(rv[2]), [l16] "{v8}"(l16), [oha] "{v9}"(oha), [ors] "s"(orsrc), [o0] "s"(ob[0]), [o1] "s"(ob[1]), [o2] "s"(ob[2]), [o3] "s"(ob[3]), [o4] "s"(ob[4]), [sy] "{v10}"(sya), [one] "{v11}"(one), [rw] "{v12}"(rw), [fw] "{v13}"(fw), [f0] "{v14}"(f0), [rr] "{v15}"(rr), [fr] "{v18}"(fr), [xo] "{v16}"(xo_), [sv] "{v17}"(so_), [srs] "s"(srsrc), [ss] "s"(ob[5]), [kh] "s"(kh), [d1] "s"((56 + 13 * kh) * 1024), [d2] "s"((6 * kh - 44) * 1024), [ofr] "i"(SY_FREE), [nfr] "i"(NFR), [ox1] "i"(SY_X1), [ow1] "i"(SY_WO + 4), [nwo] "i"(8), [sl0] "i"(layer ? OSLEEPB : OSLEEP), [sl1] "i"(OSLEEP1)
        : "memory", "scc", "vcc", "v28", "v29", "v30", "v31", "v32", "v33", "v34", "v35", "v36", "v37", "v38", "v39", "v40", "v41", "v42", "v43", "v44", "v45", "v46", "v47", "v48", "v49", "v50", "v51", "v52", "v53", "v54", "v55", "v56", "v57", "v58", "v59", "v60", "v61", "v62", "v63", "v64", "v65", "v66", "v67", "v68", "v69", "v70", "v71", "v72", "v73", "v74", "v75", "v76", "v77", "v78", "v79", "v80", "v81", "v82", "v83", "v84", "v85", "v86", "v87", "v88", "v89", "v90", "v91", "v92", "v93", "v94", "v95", "v96", "v97", "v98", "v99", "v100", "v101", "v102", "v103", "v104", "v105", "v106", "v107", "v108", "v109", "v110", "v111", "v112", "v113", "v114", "v115", "v116", "v117", "v118", "v119", "v120", "v121", "v122", "v123", "v124", "v125", "v126", "v127", "v128", "v129", "v130", "v131", "v132", "v133", "v134", "v135", "v136", "v137", "v138", "v139", "v140", "v141", "v142", "v143", "v144", "v145", "v146", "v147", "v148", "v149", "v150", "v151", "v152", "v153", "v154", "v155", "v156", "v157", "v158", "v159", "v160", "v161", "v162", "v163", "v164", "v165", "v166", "v167");
    RETRY(8 * layer + 1 + grp, 0x10000 - cap);
    RETRY(8 * layer + 3 + grp, rt >> 16);
  }
  lds_inc<SY_FIN>(sya, one);
  lds_poll<SY_X1, 0>(sya, 8);
  {
    typedef __attribute__((address_space(3))) const u32x4* lp4;
    __builtin_amdgcn_raw_buffer_store_b128(*(lp4)(size_t)xi_, x1rs, xg_, 0, 17);
    if (j < 2) __builtin_amdgcn_raw_buffer_store_b128(*(lp4)(size_t)(xi_ + 64 * 144), x1rs, xg_, 64 * XP * 2, 17);
  }
  lds_inc<SY_WI>(sya, one);
  STY(13);
  {
    LANE(lane);
    const int t = 64 * w + lane;
    ST_FLUSH(kid);
  }
}

#define BAUX 17
#define UPAD (5 * 144)
#define UF_N (UPAD + 16 * 144)
#define KY0 1058
#define KC_N 1120
#define KZ0 130
#define K8_N 192
#define CH_EL (UF_N + 4 * KC_N + 2 * K8_N)
#define AUX_B 18944

__device__ __forceinline__ void kc_store(u16* kd, const u32* d) {
  u32 sw[5], mx[5];
#pragma unroll
  for (int i = 0; i < 5; ++i) { sw[i] = __builtin_amdgcn_alignbit(d[i], d[i], 16); mx[i] = __builtin_amdgcn_perm(d[i], d[i + 1], 0x07060100); }
  *(uint4*)kd = make_uint4(mx[4], mx[3], mx[2], mx[1]);
  *(uint4*)(kd + KC_N) = make_uint4(sw[4], sw[3], sw[2], sw[1]);
  *(uint4*)(kd + 2 * KC_N) = make_uint4(mx[3], mx[2], mx[1], mx[0]);
  *(uint4*)(kd + 3 * KC_N) = make_uint4(sw[3], sw[2], sw[1], sw[0]);
}
#define DUPLO_(x) __builtin_amdgcn_perm(x, x, 0x01000100)

#define TSLEEP 18
#define TPS 5
#define FPOLL 1
__device__ __forceinline__ void b_tables(unsigned char* smem, const int ch, const int lane) {
  const u16* kc = (const u16*)(smem + KQ_OFF) + ch * 1536;
  u16* Uf = (u16*)smem + ch * CH_EL;
  u16* KC = Uf + UF_N;
  const u16* p = kc + (lane < 32 ? 8 * lane : 256 + 4 * (lane - 32));
  const uint2 a0 = *(const uint2*)p, a1 = *(const uint2*)(p + 4), a2 = *(const uint2*)(p + 8);
  const u32 b0 = *(const u32*)&kc[384 + 2 * lane], b1 = *(const u32*)&kc[386 + 2 * lane];
  const uint2 eh = *(const uint2*)(kc + (lane < 3 ? 0 : lane == 3 ? 384 : 512));
  const u32 c0 = *(const u32*)&kc[638 - 2 * lane], c1 = *(const u32*)&kc[640 - 2 * lane];
  u32 dh[6];
  {
    const bool r1 = lane < 32;
    dh[0] = r1 ? a0.x : DUPLO_(a0.x);
    dh[1] = r1 ? a0.y : __builtin_amdgcn_perm(a0.y, a0.x, 0x05040302);
    dh[2] = r1 ? a1.x : DUPLO_(a0.y);
    dh[3] = r1 ? a1.y : __builtin_amdgcn_perm(a1.x, a0.y, 0x05040302);
    const u32 nx = r1 ? a2.x : a1.x;
    dh[4] = lane < 31 ? nx : DUPLO_(nx);
    dh[5] = lane < 31 ? a2.y : (lane == 63 ? nx : nx >> 16);
    kc_store(KC + 8 * (131 - lane), dh);
  }
  {
    u16* R = KC + 128;
#pragma unroll
    for (int c = 0; c < 4; ++c) if (lane < 36) *(uint4*)&R[c * KC_N + 8 * lane] = make_uint4(0, 0, 0, 0);
    asm volatile("" ::: "memory");
#pragma unroll
    for (int c = 0; c < 4; ++c) { R[c * (KC_N - 1) + 95 - lane] = (u16)b1; R[c * (KC_N - 1) + 239 - lane] = (u16)(b0 >> 16); }
  }
  {
    u32* K8 = (u32*)(KC + 4 * KC_N);
    const u32 wh = __builtin_amdgcn_perm(c0, c1, 0x07060100);
    K8[lane + 1] = wh;
    K8[K8_N / 2 + lane] = wh;
    K8[lane < 32 ? (lane ? 64 + lane : 0) : 128 + lane] = 0u;
  }
  *(uint4*)&Uf[8 * lane] = make_uint4(0, 0, 0, 0);
  *(uint4*)&Uf[8 * lane + UPAD - 512] = make_uint4(0, 0, 0, 0);
  if (lane < 23) {
    const bool e0 = lane == 2, e1 = lane == 3, e2 = lane == 19;
    dh[0] = e1 ? DUPLO_(eh.x) : 0u; dh[1] = e1 ? eh.x & 0xffffu : e2 ? eh.x << 16 : 0u; dh[2] = e2 ? eh.x & 0xffffu : 0u; dh[3] = 0u; dh[4] = e0 ? eh.x : 0u; dh[5] = e0 ? eh.y : 0u;
    kc_store(KC + 8 * (131 - (lane < 3 ? lane - 3 : lane < 11 ? 61 + lane : 109 + lane)), dh);
  }
}
#define FSH 10
#define FSX 1
#define FSH0 13
#define PTB 2
#define PBU 1
#define PBU2 1
#define PBUF 1
#define PBL 2
#define PBH 3
#define PBE 3
template <bool FULL, int layer>
__device__ __forceinline__ void b_conv(unsigned char* smem, const int b, const int w, const int gq, const int wq, const int cG, const u16* xtin, float2* stats, u32* fstat,
                                       u16* xtout, u32x4* mark, const u16* wot, u32x4 (&wst)[8]
                                       ) {
  LANE(lane);
  const int t = 64 * w + lane, n = lane & 15, g = lane >> 4, ck = 64 * wq + lane;
  const int v = FULL ? (wq + 2 * gq) & 3 : (0x1023 >> (4 * wq)) & 3;
  const int p = FULL ? (v & 1) : (b & 1);
  const u32 Uf = LDSA(smem) + gq * (CH_EL * 2), Kb = Uf + 2 * UF_N;
  const u32 aux = LDSA(smem) + 47360 + gq * AUX_B;
  const u32 sya = LDSA(smem) + SY_OFF + 256 * layer;
  u32 cna = sya + 80 + 16 * gq, one = 1u;
  u32 ra, rb, wa, ka, kb = Kb;
  if (v < 2) {
    const int r1 = FULL ? 4 : 2 + 2 * v, y0 = KY0 - 16 - n + 8 * g - 128 * r1;
    ra = Uf + 2 * (UPAD + 144 * (n - r1) + 8 * g + 16 * p - 32);
    rb = ra + ((g >= 2 || p) ? 32 : 0);
    wa = aux + (FULL ? (g & 1) * 4096 + n * 256 + (((2 * p + (g >> 1)) ^ (n & 7)) << 4) : v * 4096 + (g & 1) * 2048 + n * 128 + (((g >> 1) ^ (n & 7)) << 4));
    ka = Kb + 2 * ((y0 & 3) * KC_N + (y0 & ~3));
  } else if (v == 2) {
    const int h = n + (g & 1) - 1, zs = 31 - (FULL ? n : 4 * (n >> 1) + 2 * p + (n & 1)) + 4 * g;
    ra = Uf + 2 * (UPAD + 864 * (g >> 1) - 576 + 8 * h - (h < 0 ? 16 : 0));
    rb = ra - (h == 0 ? 40 : 8);
    wa = aux + 12800 + 3072 * (g & 1) + 16 * ((FULL ? 96 : 48) * (g >> 1) + n);
    ka = Kb + 2 * ((zs & 3) * KC_N + 128 + (zs & ~3));
  } else {
    const int h = n + g - 3, tx = (FULL ? 16 * n : 32 * n + 16 * p) - 8 + 8 * g;
    const int z0 = KZ0 - 2 - (FULL ? 2 * n + 96 : 4 * n + 2 * p + 64) + 4 * g, yx = KY0 - 1032 - n + 8 * g;
    ra = Uf + 2 * (UPAD + 8 * h - (h < 0 ? 16 : 0));
    rb = Uf + 2 * (UPAD + tx + 16 * (tx >> 7));
    wa = aux + 8192 + (FULL ? (g & 1) * 2304 + (g >> 1) * 1152 : (g & 1) * 1152 + (g >> 1) * 640) + n * 16;
    ka = Kb + 2 * (4 * KC_N + ((z0 >> 1) & 1) * K8_N + (z0 & ~3));
    kb = Kb + 2 * ((yx & 3) * KC_N + (yx & ~3));
  }
  u32 uw_ = Uf + 2 * (UPAD + 8 * ck + 16 * (ck >> 4));
  const int eck = FULL ? ck : (4 * (ck >> 1) + 2 * p + (ck & 1)) & 255;
  u32 ue = Uf + 2 * (UPAD + 8 * eck + 16 * (eck >> 4));
  u32 exr = aux + (FULL ? (ck ^ ((ck >> 4) & 7)) : ((ck & 127) ^ ((ck >> 3) & 7))) * 16;
  u32 frr = aux + 8192 + (FULL ? (ck & 1) * 1152 + (ck & 126) * 8 : (ck & 1) * 640 + (ck & 62) * 8);
  u32 mr_ = aux + 12800 + 16 * (FULL ? ck - 64 : ck >= 32 ? ck - 32 : ck);
  int od, oh;
  if (layer) { od = (((eck >> 3) * 640 + cG) * 64 + 8 * (eck & 7)) * 2; oh = -1; }
  else {
    const int dst = (eck >> 3) * 51200 + (cG >> 5) * 2560;
    const int pr = 4 * ((cG >> 3) & 3) + (cG & 3) + 4 * (cG & 4);
    od = (dst + pr * 64 + 8 * ((eck & 7) ^ (pr & 6))) * 2;
    oh = ((eck & 7) >= 6 && eck < 248) ? (dst + 51200 + 2048 + pr * 16 + 8 * (eck & 1)) * 2 : -1;
  }
  u32 bna = LDSA(smem) + BND_OFF + (v == 2 ? 2048 : 0) + 16 * lane, kfa = LDSA(smem) + KQ_OFF + gq * 3072 + 1536;
  int c16 = ck * 16;
  const __amdgpu_buffer_rsrc_t xrs = __builtin_amdgcn_make_buffer_rsrc((void*)(xtin + (size_t)cG * XP), 0, XP * 2, 0x00020000);
  const __amdgpu_buffer_rsrc_t frs = __builtin_amdgcn_make_buffer_rsrc((void*)(fstat + (b & 7) * 2048), 0, 8192, 0x00020000);
  const __amdgpu_buffer_rsrc_t ors = __builtin_amdgcn_make_buffer_rsrc(xtout, 0, 32 * 102400, 0x00020000);
  PIN(ra); PIN(rb); PIN(wa); PIN(uw_); PIN(exr); PIN(frr); PIN(mr_); PIN(od);
  int xo = eck * 16;
  if (!FULL) { PIN(ue); PIN(xo); }
  const __amdgpu_buffer_rsrc_t mrs = __builtin_amdgcn_make_buffer_rsrc(mark + b * 2032, 0, 2032 * 16, 0x00020000);
  int mo = ck * 16 + (gq ? 0 : 8 * 4096);
  if (!layer) { PIN(oh); PIN(mo); }
  PIN(cna); PIN(one); PIN(ka); PIN(kb); PIN(bna); PIN(kfa); PIN(c16);
  ST(6);
  if (!FULL && wq < 3) {
    lds_poll<SY_WI, 1>(sya, 8);
    __builtin_amdgcn_s_setprio(PTB);
    b_tables(smem, wq, lane);
    lds_inc<80 + 12>(sya + 16 * wq, 1u);
    ST(9);
  }
  if (!FULL && wq == 3) {
    const int xcd = b & 7, idx = b >> 3;
    const int s0 = 64 * (8 * (xcd >> 1) + (idx & 7)), hg = 4 * (xcd & 1) + (idx >> 3);
    const __amdgpu_buffer_rsrc_t srsrc = __builtin_amdgcn_make_buffer_rsrc(stats + (size_t)s0 * 16, 0, 8192, 0x00020000);
    const __amdgpu_buffer_rsrc_t wrs = __builtin_amdgcn_make_buffer_rsrc(fstat + hg * 2048 + s0 / 2, 0, 4096 + 128, 0x00020000);
    int so = lane * 16;
    int wo_ = (lane >> 5) * 4096 + (lane & 31) * 4;
    PIN(so); PIN(wo_);
    lds_poll<SY_FIN, 2>(sya, 8);
    __builtin_amdgcn_s_sleep(SSLEEP);
    f32x4 sm;
    int spins = 0;
    _Pragma("unroll 1")
    while (true) {
      asm volatile("" ::: "memory");
      u32x4 pv[8];
#pragma unroll
      for (int i = 0; i < 8; ++i) pv[i] = __builtin_amdgcn_raw_buffer_load_b128(srsrc, so, i * 1024, 16);
      u32 m = 0;
      sm = __builtin_bit_cast(f32x4, pv[0]);
#pragma unroll
      for (int i = 0; i < 8; ++i) {
        m = umax3(umax3(m, pv[i][0], pv[i][1]), pv[i][2], pv[i][3]);
        if (i) sm += __builtin_bit_cast(f32x4, pv[i]);
      }
      if (__builtin_amdgcn_ballot_w64(m == 0xffffffffu) == 0 || ++spins > GCAP) break;
      __builtin_amdgcn_s_sleep(4);
    }
    RETRY(8 * layer + 5, spins);
    ST(7);
    const auto q0_ = __builtin_amdgcn_permlane32_swap(__float_as_uint(sm[0]), __float_as_uint(sm[2]), false, false);
    const auto q1_ = __builtin_amdgcn_permlane32_swap(__float_as_uint(sm[1]), __float_as_uint(sm[3]), false, false);
    const float mean = (__uint_as_float(q0_[0]) + __uint_as_float(q0_[1])) * (1.0f / 640.0f);
    const float var = fmaxf(fmaf(-mean, mean, (__uint_as_float(q1_[0]) + __uint_as_float(q1_[1])) * (1.0f / 640.0f)), 0.f);
    const float r = 256.0f * __builtin_amdgcn_rcpf(rbf(__builtin_amdgcn_sqrtf(rbf(rbf(var) + 1e-5f))));
    const u32 d = pack2(-mean * r, 0.f);
    const u32 o = ((max(__float_as_uint(r) + 0x8000u, d) - d) & 0xffff0000u) | d;
    __builtin_amdgcn_raw_buffer_store_b32(min(o, 0xfffffffeu), wrs, wo_, 0, 17);
    lds_inc<SY_FS>(sya, one);
    ST(8);
    __builtin_amdgcn_s_setprio(PBU);
  }
  f32x2 kd;
  u32x4 kt[17];
  {
    u32 tm; int s0_, s1_;
    if (FULL && !layer) {
      const __amdgpu_buffer_rsrc_t wos = __builtin_amdgcn_make_buffer_rsrc((void*)(wot + (size_t)(4 * (b & 1) + (b >> 6)) * 51200 + ((w - 6) & 3) * 4096), 0, 8192, 0x00020000);
      asm volatile(
        "s_cmp_lt_u32 %[v], 2\ns_cbranch_scc1 .LW%=\ns_movk_i32 %[s0], 4096\nbuffer_load_dwordx4 v[128:131], %[l16], %[wos], 0 offen offset:0 sc1\n"
        "buffer_load_dwordx4 v[132:135], %[l16], %[wos], 0 offen offset:1024 sc1\nbuffer_load_dwordx4 v[144:147], %[l16], %[wos], 0 offen offset:2048 sc1\n"
        "buffer_load_dwordx4 v[148:151], %[l16], %[wos], 0 offen offset:3072 sc1\nbuffer_load_dwordx4 v[152:155], %[l16], %[wos], %[s0] offen offset:0 sc1\n"
        "buffer_load_dwordx4 v[156:159], %[l16], %[wos], %[s0] offen offset:1024 sc1\nbuffer_load_dwordx4 v[160:163], %[l16], %[wos], %[s0] offen offset:2048 sc1\n"
        "buffer_load_dwordx4 v[164:167], %[l16], %[wos], %[s0] offen offset:3072 sc1\n.LW%=:\ns_mov_b32 %[s1], 0x100000\n.LP%=:\nds_read_b32 %[tm], %[cna] offset:12\ns_waitcnt lgkmcnt(0)\n"
        "v_readfirstlane_b32 %[s0], %[tm]\ns_nop 0\ns_cmp_ge_i32 %[s0], 1\ns_cbranch_scc1 .LQ%=\ns_sleep 1\ns_sub_u32 %[s1], %[s1], 1\ns_cbranch_scc0 .LP%=\n.LQ%=:\nds_read_b64 %[kd], %[kfa]\n"
        "s_cmp_lt_u32 %[v], 2\ns_cbranch_scc0 .LA%=\nds_read_b64 v[100:101], %[ka] offset:1024\nds_read_b64 v[102:103], %[ka] offset:1032\nds_read_b64 v[104:105], %[ka] offset:960\n"
        "ds_read_b64 v[106:107], %[ka] offset:968\nds_read_b64 v[108:109], %[ka] offset:896\nds_read_b64 v[110:111], %[ka] offset:904\nds_read_b64 v[112:113], %[ka] offset:832\n"
        "ds_read_b64 v[114:115], %[ka] offset:840\nds_read_b64 v[116:117], %[ka] offset:768\nds_read_b64 v[118:119], %[ka] offset:776\nds_read_b64 v[120:121], %[ka] offset:704\n"
        "ds_read_b64 v[122:123], %[ka] offset:712\nds_read_b64 v[124:125], %[ka] offset:640\nds_read_b64 v[126:127], %[ka] offset:648\nds_read_b64 v[128:129], %[ka] offset:576\n"
        "ds_read_b64 v[130:131], %[ka] offset:584\nds_read_b64 v[132:133], %[ka] offset:512\nds_read_b64 v[134:135], %[ka] offset:520\nds_read_b64 v[136:137], %[ka] offset:448\n"
        "ds_read_b64 v[138:139], %[ka] offset:456\nds_read_b64 v[140:141], %[ka] offset:384\nds_read_b64 v[142:143], %[ka] offset:392\nds_read_b64 v[144:145], %[ka] offset:320\n"
        "ds_read_b64 v[146:147], %[ka] offset:328\nds_read_b64 v[148:149], %[ka] offset:256\nds_read_b64 v[150:151], %[ka] offset:264\nds_read_b64 v[152:153], %[ka] offset:192\n"
        "ds_read_b64 v[154:155], %[ka] offset:200\nds_read_b64 v[156:157], %[ka] offset:128\nds_read_b64 v[158:159], %[ka] offset:136\nds_read_b64 v[160:161], %[ka] offset:64\n"
        "ds_read_b64 v[162:163], %[ka] offset:72\nds_read_b64 v[164:165], %[ka] offset:0\nds_read_b64 v[166:167], %[ka] offset:8\ns_branch .LE%=\n.LA%=:\nds_read_b128 v[100:103], %[bna]\n"
        "ds_read_b128 v[104:107], %[bna] offset:1024\ns_cmp_eq_u32 %[v], 2\ns_cbranch_scc0 .LB%=\nds_read_b64 v[108:109], %[ka] offset:0\nds_read_b64 v[110:111], %[ka] offset:288\n"
        "ds_read_b64 v[112:113], %[ka] offset:32\nds_read_b64 v[114:115], %[ka] offset:320\nds_read_b64 v[116:117], %[ka] offset:64\nds_read_b64 v[118:119], %[ka] offset:352\n"
        "ds_read_b64 v[120:121], %[ka] offset:96\nds_read_b64 v[122:123], %[ka] offset:384\nds_read_b64 v[124:125], %[ka] offset:128\nds_read_b64 v[126:127], %[ka] offset:416\ns_branch .LE%=\n.LB%=:\n"
        "ds_read_b64 v[108:109], %[ka] offset:192\nds_read_b64 v[110:111], %[ka] offset:224\nds_read_b64 v[112:113], %[ka] offset:128\nds_read_b64 v[114:115], %[ka] offset:160\n"
        "ds_read_b64 v[116:117], %[ka] offset:64\nds_read_b64 v[118:119], %[ka] offset:96\nds_read_b64 v[120:121], %[ka] offset:0\nds_read_b64 v[122:123], %[ka] offset:32\n"
        "ds_read_b64 v[124:125], %[kb] offset:0\nds_read_b64 v[126:127], %[kb] offset:8\n.LE%=:\ns_waitcnt lgkmcnt(0)\n"
        : "=&{v[100:103]}"(kt[0]), "=&{v[104:107]}"(kt[1]), "=&{v[108:111]}"(kt[2]), "=&{v[112:115]}"(kt[3]), "=&{v[116:119]}"(kt[4]), "=&{v[120:123]}"(kt[5]), "=&{v[124:127]}"(kt[6]), "=&{v[128:131]}"(kt[7]), "=&{v[132:135]}"(kt[8]), "=&{v[136:139]}"(kt[9]), "=&{v[140:143]}"(kt[10]), "=&{v[144:147]}"(kt[11]), "=&{v[148:151]}"(kt[12]), "=&{v[152:155]}"(kt[13]), "=&{v[156:159]}"(kt[14]), "=&{v[160:163]}"(kt[15]), "=&{v[164:167]}"(kt[16]), [kd] "=&v"(kd), [tm] "=&v"(tm), [s0] "=&s"(s0_), [s1] "=&s"(s1_)
        : [ka] "v"(ka), [kb] "v"(kb), [bna] "v"(bna), [kfa] "v"(kfa), [v] "s"(v), [cna] "v"(cna), [l16] "v"(lane * 16), [wos] "s"(wos)
        : "memory", "scc", "vcc");
    } else if (FULL) {
      asm volatile(
        "s_mov_b32 %[s1], 0x100000\n.LP%=:\nds_read_b32 %[tm], %[cna] offset:12\ns_waitcnt lgkmcnt(0)\nv_readfirstlane_b32 %[s0], %[tm]\ns_nop 0\ns_cmp_ge_i32 %[s0], 1\ns_cbranch_scc1 .LQ%=\n"
        "s_sleep 1\ns_sub_u32 %[s1], %[s1], 1\ns_cbranch_scc0 .LP%=\n.LQ%=:\nds_read_b64 %[kd], %[kfa]\ns_cmp_lt_u32 %[v], 2\ns_cbranch_scc0 .LA%=\nds_read_b64 v[100:101], %[ka] offset:1024\n"
        "ds_read_b64 v[102:103], %[ka] offset:1032\nds_read_b64 v[104:105], %[ka] offset:960\nds_read_b64 v[106:107], %[ka] offset:968\nds_read_b64 v[108:109], %[ka] offset:896\n"
        "ds_read_b64 v[110:111], %[ka] offset:904\nds_read_b64 v[112:113], %[ka] offset:832\nds_read_b64 v[114:115], %[ka] offset:840\nds_read_b64 v[116:117], %[ka] offset:768\n"
        "ds_read_b64 v[118:119], %[ka] offset:776\nds_read_b64 v[120:121], %[ka] offset:704\nds_read_b64 v[122:123], %[ka] offset:712\nds_read_b64 v[124:125], %[ka] offset:640\n"
        "ds_read_b64 v[126:127], %[ka] offset:648\nds_read_b64 v[128:129], %[ka] offset:576\nds_read_b64 v[130:131], %[ka] offset:584\nds_read_b64 v[132:133], %[ka] offset:512\n"
        "ds_read_b64 v[134:135], %[ka] offset:520\nds_read_b64 v[136:137], %[ka] offset:448\nds_read_b64 v[138:139], %[ka] offset:456\nds_read_b64 v[140:141], %[ka] offset:384\n"
        "ds_read_b64 v[142:143], %[ka] offset:392\nds_read_b64 v[144:145], %[ka] offset:320\nds_read_b64 v[146:147], %[ka] offset:328\nds_read_b64 v[148:149], %[ka] offset:256\n"
        "ds_read_b64 v[150:151], %[ka] offset:264\nds_read_b64 v[152:153], %[ka] offset:192\nds_read_b64 v[154:155], %[ka] offset:200\nds_read_b64 v[156:157], %[ka] offset:128\n"
        "ds_read_b64 v[158:159], %[ka] offset:136\nds_read_b64 v[160:161], %[ka] offset:64\nds_read_b64 v[162:163], %[ka] offset:72\nds_read_b64 v[164:165], %[ka] offset:0\n"
        "ds_read_b64 v[166:167], %[ka] offset:8\ns_branch .LE%=\n.LA%=:\nds_read_b128 v[100:103], %[bna]\nds_read_b128 v[104:107], %[bna] offset:1024\ns_cmp_eq_u32 %[v], 2\ns_cbranch_scc0 .LB%=\n"
        "ds_read_b64 v[108:109], %[ka] offset:0\nds_read_b64 v[110:111], %[ka] offset:288\nds_read_b64 v[112:113], %[ka] offset:32\nds_read_b64 v[114:115], %[ka] offset:320\n"
        "ds_read_b64 v[116:117], %[ka] offset:64\nds_read_b64 v[118:119], %[ka] offset:352\nds_read_b64 v[120:121], %[ka] offset:96\nds_read_b64 v[122:123], %[ka] offset:384\n"
        "ds_read_b64 v[124:125], %[ka] offset:128\nds_read_b64 v[126:127], %[ka] offset:416\ns_branch .LE%=\n.LB%=:\nds_read_b64 v[108:109], %[ka] offset:192\nds_read_b64 v[110:111], %[ka] offset:224\n"
        "ds_read_b64 v[112:113], %[ka] offset:128\nds_read_b64 v[114:115], %[ka] offset:160\nds_read_b64 v[116:117], %[ka] offset:64\nds_read_b64 v[118:119], %[ka] offset:96\n"
        "ds_read_b64 v[120:121], %[ka] offset:0\nds_read_b64 v[122:123], %[ka] offset:32\nds_read_b64 v[124:125], %[kb] offset:0\nds_read_b64 v[126:127], %[kb] offset:8\n.LE%=:\ns_waitcnt lgkmcnt(0)\n"
        : "=&{v[100:103]}"(kt[0]), "=&{v[104:107]}"(kt[1]), "=&{v[108:111]}"(kt[2]), "=&{v[112:115]}"(kt[3]), "=&{v[116:119]}"(kt[4]), "=&{v[120:123]}"(kt[5]), "=&{v[124:127]}"(kt[6]), "=&{v[128:131]}"(kt[7]), "=&{v[132:135]}"(kt[8]), "=&{v[136:139]}"(kt[9]), "=&{v[140:143]}"(kt[10]), "=&{v[144:147]}"(kt[11]), "=&{v[148:151]}"(kt[12]), "=&{v[152:155]}"(kt[13]), "=&{v[156:159]}"(kt[14]), "=&{v[160:163]}"(kt[15]), "=&{v[164:167]}"(kt[16]), [kd] "=&v"(kd), [tm] "=&v"(tm), [s0] "=&s"(s0_), [s1] "=&s"(s1_)
        : [ka] "v"(ka), [kb] "v"(kb), [bna] "v"(bna), [kfa] "v"(kfa), [v] "s"(v), [cna] "v"(cna)
        : "memory", "scc", "vcc");
    } else {
      asm volatile(
        "s_mov_b32 %[s1], 0x100000\n.LP%=:\nds_read_b32 %[tm], %[cna] offset:12\ns_waitcnt lgkmcnt(0)\nv_readfirstlane_b32 %[s0], %[tm]\ns_nop 0\ns_cmp_ge_i32 %[s0], 1\ns_cbranch_scc1 .LQ%=\n"
        "s_sleep 1\ns_sub_u32 %[s1], %[s1], 1\ns_cbranch_scc0 .LP%=\n.LQ%=:\nds_read_b64 %[kd], %[kfa]\ns_cmp_lt_u32 %[v], 2\ns_cbranch_scc0 .LA%=\nds_read_b64 v[100:101], %[ka] offset:512\n"
        "ds_read_b64 v[102:103], %[ka] offset:520\nds_read_b64 v[104:105], %[ka] offset:448\nds_read_b64 v[106:107], %[ka] offset:456\nds_read_b64 v[108:109], %[ka] offset:384\n"
        "ds_read_b64 v[110:111], %[ka] offset:392\nds_read_b64 v[112:113], %[ka] offset:320\nds_read_b64 v[114:115], %[ka] offset:328\nds_read_b64 v[116:117], %[ka] offset:256\n"
        "ds_read_b64 v[118:119], %[ka] offset:264\nds_read_b64 v[120:121], %[ka] offset:192\nds_read_b64 v[122:123], %[ka] offset:200\nds_read_b64 v[124:125], %[ka] offset:128\n"
        "ds_read_b64 v[126:127], %[ka] offset:136\nds_read_b64 v[128:129], %[ka] offset:64\nds_read_b64 v[130:131], %[ka] offset:72\nds_read_b64 v[132:133], %[ka] offset:0\n"
        "ds_read_b64 v[134:135], %[ka] offset:8\ns_branch .LE%=\n.LA%=:\nds_read_b128 v[100:103], %[bna]\nds_read_b128 v[104:107], %[bna] offset:1024\ns_cmp_eq_u32 %[v], 2\ns_cbranch_scc0 .LB%=\n"
        "ds_read_b64 v[108:109], %[ka] offset:0\nds_read_b64 v[110:111], %[ka] offset:288\nds_read_b64 v[112:113], %[ka] offset:32\nds_read_b64 v[114:115], %[ka] offset:320\n"
        "ds_read_b64 v[116:117], %[ka] offset:64\nds_read_b64 v[118:119], %[ka] offset:352\nds_read_b64 v[120:121], %[ka] offset:96\nds_read_b64 v[122:123], %[ka] offset:384\n"
        "ds_read_b64 v[124:125], %[ka] offset:128\nds_read_b64 v[126:127], %[ka] offset:416\nds_read_b64 v[128:129], %[ka] offset:160\nds_read_b64 v[130:131], %[ka] offset:448\ns_branch .LE%=\n.LB%=:\n"
        "ds_read_b64 v[108:109], %[ka] offset:192\nds_read_b64 v[110:111], %[ka] offset:224\nds_read_b64 v[112:113], %[ka] offset:128\nds_read_b64 v[114:115], %[ka] offset:160\n"
        "ds_read_b64 v[116:117], %[ka] offset:64\nds_read_b64 v[118:119], %[ka] offset:96\nds_read_b64 v[120:121], %[ka] offset:0\nds_read_b64 v[122:123], %[ka] offset:32\n"
        "ds_read_b64 v[124:125], %[kb] offset:0\nds_read_b64 v[126:127], %[kb] offset:8\n.LE%=:\ns_waitcnt lgkmcnt(0)\n"
        : "=&{v[100:103]}"(kt[0]), "=&{v[104:107]}"(kt[1]), "=&{v[108:111]}"(kt[2]), "=&{v[112:115]}"(kt[3]), "=&{v[116:119]}"(kt[4]), "=&{v[120:123]}"(kt[5]), "=&{v[124:127]}"(kt[6]), "=&{v[128:131]}"(kt[7]), "=&{v[132:135]}"(kt[8]), [kd] "=&v"(kd), [tm] "=&v"(tm), [s0] "=&s"(s0_), [s1] "=&s"(s1_)
        : [ka] "v"(ka), [kb] "v"(kb), [bna] "v"(bna), [kfa] "v"(kfa), [v] "s"(v), [cna] "v"(cna)
        : "memory", "scc", "vcc");
    }
  }
  float kf4 = ck >= 32 ? kd[0] : 0.f;
  if (!FULL) PIN(kf4);
  ST(1);
  lds_poll<SY_FS, 2>(sya, 1);
  __builtin_amdgcn_s_setprio(FULL ? PBUF : PBU2);
  {
    u32 pv; int s0_, s1_, cap;
    if (FULL && layer) {
      asm volatile(
        "s_mov_b32 %[cap], 0x100000\ns_movk_i32 %[s1], 4096\ns_sleep %[fsx]\nbuffer_load_dwordx4 v[36:39], %[c16], %[xrs], 0 offen sc1\n.LA%=:\nds_read_b32 %[pv], %[sy] offset:%[ofs]\n"
        "s_waitcnt lgkmcnt(0)\nv_readfirstlane_b32 %[s0], %[pv]\ns_nop 0\ns_cmp_ge_i32 %[s0], 2\ns_cbranch_scc1 .LX%=\ns_sub_u32 %[cap], %[cap], 1\ns_cbranch_scc0 .LA%=\n.LX%=:\n"
        "s_mov_b32 %[cap], 0x10000\nbuffer_load_dwordx4 v[40:43], %[c16], %[frs], 0 offen sc1\nbuffer_load_dwordx4 v[44:47], %[c16], %[frs], %[s1] offen sc1\ns_waitcnt vmcnt(2)\n.LB%=:\n"
        "v_max3_u32 %[pv], v36, v37, v38\nv_max_u32 %[pv], %[pv], v39\nv_cmp_eq_u32 vcc, -1, %[pv]\ns_cbranch_vccnz .LC%=\n.LD%=:\nv_lshlrev_b32 v26, 16, v36\nv_and_b32 v27, 0xffff0000, v36\n"
        "v_lshlrev_b32 v28, 16, v37\nv_and_b32 v29, 0xffff0000, v37\nv_lshlrev_b32 v30, 16, v38\nv_and_b32 v31, 0xffff0000, v38\nv_lshlrev_b32 v32, 16, v39\nv_and_b32 v33, 0xffff0000, v39\n.LF%=:\n"
        "s_waitcnt vmcnt(1)\nv_max3_u32 %[pv], v40, v41, v42\nv_lshlrev_b32 v48, 16, v40\nv_lshlrev_b32 v50, 16, v41\nv_lshlrev_b32 v52, 16, v42\nv_lshlrev_b32 v54, 16, v43\nv_fmac_f32 v48, v26, v40\n"
        "v_fmac_f32 v50, v28, v41\nv_fmac_f32 v52, v30, v42\nv_fmac_f32 v54, v32, v43\ns_waitcnt vmcnt(0)\nv_max3_u32 %[pv], %[pv], v43, v44\nv_max3_u32 %[pv], %[pv], v45, v46\n"
        "v_max_u32 %[pv], %[pv], v47\nv_cmp_eq_u32 vcc, -1, %[pv]\nv_lshlrev_b32 v49, 16, v44\nv_lshlrev_b32 v51, 16, v45\nv_lshlrev_b32 v53, 16, v46\nv_lshlrev_b32 v55, 16, v47\n"
        "s_cbranch_vccnz .LI%=\n.LM%=:\nv_fmac_f32 v49, v27, v44\nv_fmac_f32 v51, v29, v45\nv_fmac_f32 v53, v31, v46\nv_fmac_f32 v55, v33, v47\nv_cvt_pk_f16_f32 v36, v48, v49\n"
        "v_cvt_pk_f16_f32 v37, v50, v51\nv_cvt_pk_f16_f32 v38, v52, v53\nv_cvt_pk_f16_f32 v39, v54, v55\nds_write_b128 %[uw], v[36:39]\ns_mov_b64 exec, 1\nds_add_u32 %[cna], %[one] offset:0\n"
        "s_mov_b64 exec, -1\nv_fmac_f32 v26, v48, %[kd1]\nv_fmac_f32 v27, v49, %[kd1]\nv_fmac_f32 v28, v50, %[kd1]\nv_fmac_f32 v29, v51, %[kd1]\nv_fmac_f32 v30, v52, %[kd1]\n"
        "v_fmac_f32 v31, v53, %[kd1]\nv_fmac_f32 v32, v54, %[kd1]\nv_fmac_f32 v33, v55, %[kd1]\ns_setprio %[pbl]\ns_mov_b32 %[s1], 0x100000\n.LN%=:\nds_read_b32 %[pv], %[cna]\ns_waitcnt lgkmcnt(0)\n"
        "v_readfirstlane_b32 %[s0], %[pv]\ns_nop 0\ns_cmp_ge_i32 %[s0], 4\ns_cbranch_scc1 .LO%=\ns_sub_u32 %[s1], %[s1], 1\ns_cbranch_scc0 .LN%=\n.LO%=:\ns_cmp_lt_u32 %[v], 2\ns_cbranch_scc0 .LT%=\n"
        "ds_read_b128 v[52:55], %[ra] offset:1376\nds_read_b128 v[56:59], %[ra] offset:1312\nds_read_b128 v[60:63], %[ra] offset:1248\nds_read_b128 v[64:67], %[rb] offset:1152\n"
        "ds_read_b128 v[68:71], %[ra] offset:1088\nds_read_b128 v[72:75], %[ra] offset:1024\nds_read_b128 v[76:79], %[ra] offset:960\nds_read_b128 v[80:83], %[rb] offset:864\nv_xor_b32 v14, 64, %[wa]\n"
        "s_waitcnt lgkmcnt(7)\nv_mfma_f32_16x16x32_f16 v[48:51], v[100:103], v[52:55], 0\nds_read_b128 v[52:55], %[ra] offset:800\ns_waitcnt lgkmcnt(7)\n"
        "v_mfma_f32_16x16x32_f16 v[44:47], v[100:103], v[56:59], 0\ns_waitcnt lgkmcnt(6)\nv_mfma_f32_16x16x32_f16 v[40:43], v[100:103], v[60:63], 0\ns_waitcnt lgkmcnt(5)\n"
        "v_mfma_f32_16x16x32_f16 v[36:39], v[100:103], v[64:67], 0\nv_mfma_f32_16x16x32_f16 v[48:51], v[104:107], v[56:59], v[48:51]\nds_read_b128 v[56:59], %[ra] offset:736\n"
        "v_mfma_f32_16x16x32_f16 v[44:47], v[104:107], v[60:63], v[44:47]\nv_mfma_f32_16x16x32_f16 v[40:43], v[104:107], v[64:67], v[40:43]\ns_waitcnt lgkmcnt(5)\n"
        "v_mfma_f32_16x16x32_f16 v[36:39], v[104:107], v[68:71], v[36:39]\nv_mfma_f32_16x16x32_f16 v[48:51], v[108:111], v[60:63], v[48:51]\nds_read_b128 v[60:63], %[ra] offset:672\n"
        "v_mfma_f32_16x16x32_f16 v[44:47], v[108:111], v[64:67], v[44:47]\nv_mfma_f32_16x16x32_f16 v[40:43], v[108:111], v[68:71], v[40:43]\ns_waitcnt lgkmcnt(5)\n"
        "v_mfma_f32_16x16x32_f16 v[36:39], v[108:111], v[72:75], v[36:39]\nv_mfma_f32_16x16x32_f16 v[48:51], v[112:115], v[64:67], v[48:51]\nds_read_b128 v[64:67], %[rb] offset:576\n"
        "v_mfma_f32_16x16x32_f16 v[44:47], v[112:115], v[68:71], v[44:47]\nv_mfma_f32_16x16x32_f16 v[40:43], v[112:115], v[72:75], v[40:43]\ns_waitcnt lgkmcnt(5)\n"
        "v_mfma_f32_16x16x32_f16 v[36:39], v[112:115], v[76:79], v[36:39]\nv_mfma_f32_16x16x32_f16 v[48:51], v[116:119], v[68:71], v[48:51]\nds_read_b128 v[68:71], %[ra] offset:512\n"
        "v_mfma_f32_16x16x32_f16 v[44:47], v[116:119], v[72:75], v[44:47]\nv_mfma_f32_16x16x32_f16 v[40:43], v[116:119], v[76:79], v[40:43]\ns_waitcnt lgkmcnt(5)\n"
        "v_mfma_f32_16x16x32_f16 v[36:39], v[116:119], v[80:83], v[36:39]\nv_mfma_f32_16x16x32_f16 v[48:51], v[120:123], v[72:75], v[48:51]\nds_read_b128 v[72:75], %[ra] offset:448\n"
        "v_mfma_f32_16x16x32_f16 v[44:47], v[120:123], v[76:79], v[44:47]\nv_mfma_f32_16x16x32_f16 v[40:43], v[120:123], v[80:83], v[40:43]\ns_waitcnt lgkmcnt(5)\n"
        "v_mfma_f32_16x16x32_f16 v[36:39], v[120:123], v[52:55], v[36:39]\nv_mfma_f32_16x16x32_f16 v[48:51], v[124:127], v[76:79], v[48:51]\nds_read_b128 v[76:79], %[ra] offset:384\n"
        "v_mfma_f32_16x16x32_f16 v[44:47], v[124:127], v[80:83], v[44:47]\nv_mfma_f32_16x16x32_f16 v[40:43], v[124:127], v[52:55], v[40:43]\ns_waitcnt lgkmcnt(5)\n"
        "v_mfma_f32_16x16x32_f16 v[36:39], v[124:127], v[56:59], v[36:39]\nv_mfma_f32_16x16x32_f16 v[48:51], v[128:131], v[80:83], v[48:51]\nds_read_b128 v[80:83], %[rb] offset:288\n"
        "v_mfma_f32_16x16x32_f16 v[44:47], v[128:131], v[52:55], v[44:47]\nv_mfma_f32_16x16x32_f16 v[40:43], v[128:131], v[56:59], v[40:43]\ns_waitcnt lgkmcnt(5)\n"
        "v_mfma_f32_16x16x32_f16 v[36:39], v[128:131], v[60:63], v[36:39]\nv_mfma_f32_16x16x32_f16 v[48:51], v[132:135], v[52:55], v[48:51]\nds_read_b128 v[52:55], %[ra] offset:224\n"
        "v_mfma_f32_16x16x32_f16 v[44:47], v[132:135], v[56:59], v[44:47]\nv_mfma_f32_16x16x32_f16 v[40:43], v[132:135], v[60:63], v[40:43]\ns_waitcnt lgkmcnt(5)\n"
        "v_mfma_f32_16x16x32_f16 v[36:39], v[132:135], v[64:67], v[36:39]\nv_mfma_f32_16x16x32_f16 v[48:51], v[136:139], v[56:59], v[48:51]\nds_read_b128 v[56:59], %[ra] offset:160\n"
        "v_mfma_f32_16x16x32_f16 v[44:47], v[136:139], v[60:63], v[44:47]\nv_mfma_f32_16x16x32_f16 v[40:43], v[136:139], v[64:67], v[40:43]\ns_waitcnt lgkmcnt(5)\n"
        "v_mfma_f32_16x16x32_f16 v[36:39], v[136:139], v[68:71], v[36:39]\nv_mfma_f32_16x16x32_f16 v[48:51], v[140:143], v[60:63], v[48:51]\nds_read_b128 v[60:63], %[ra] offset:96\n"
        "v_mfma_f32_16x16x32_f16 v[44:47], v[140:143], v[64:67], v[44:47]\nv_mfma_f32_16x16x32_f16 v[40:43], v[140:143], v[68:71], v[40:43]\ns_waitcnt lgkmcnt(5)\n"
        "v_mfma_f32_16x16x32_f16 v[36:39], v[140:143], v[72:75], v[36:39]\nv_mfma_f32_16x16x32_f16 v[48:51], v[144:147], v[64:67], v[48:51]\nds_read_b128 v[64:67], %[rb] offset:0\n"
        "v_mfma_f32_16x16x32_f16 v[44:47], v[144:147], v[68:71], v[44:47]\nv_mfma_f32_16x16x32_f16 v[40:43], v[144:147], v[72:75], v[40:43]\ns_waitcnt lgkmcnt(5)\n"
        "v_mfma_f32_16x16x32_f16 v[36:39], v[144:147], v[76:79], v[36:39]\nv_mfma_f32_16x16x32_f16 v[48:51], v[148:151], v[68:71], v[48:51]\n"
        "v_mfma_f32_16x16x32_f16 v[44:47], v[148:151], v[72:75], v[44:47]\nv_mfma_f32_16x16x32_f16 v[40:43], v[148:151], v[76:79], v[40:43]\ns_waitcnt lgkmcnt(4)\n"
        "v_mfma_f32_16x16x32_f16 v[36:39], v[148:151], v[80:83], v[36:39]\nv_mfma_f32_16x16x32_f16 v[48:51], v[152:155], v[72:75], v[48:51]\n"
        "v_mfma_f32_16x16x32_f16 v[44:47], v[152:155], v[76:79], v[44:47]\nv_mfma_f32_16x16x32_f16 v[40:43], v[152:155], v[80:83], v[40:43]\ns_waitcnt lgkmcnt(3)\n"
        "v_mfma_f32_16x16x32_f16 v[36:39], v[152:155], v[52:55], v[36:39]\nv_mfma_f32_16x16x32_f16 v[48:51], v[156:159], v[76:79], v[48:51]\n"
        "v_mfma_f32_16x16x32_f16 v[44:47], v[156:159], v[80:83], v[44:47]\nv_mfma_f32_16x16x32_f16 v[40:43], v[156:159], v[52:55], v[40:43]\ns_waitcnt lgkmcnt(2)\n"
        "v_mfma_f32_16x16x32_f16 v[36:39], v[156:159], v[56:59], v[36:39]\nv_mfma_f32_16x16x32_f16 v[48:51], v[160:163], v[80:83], v[48:51]\n"
        "v_mfma_f32_16x16x32_f16 v[44:47], v[160:163], v[52:55], v[44:47]\nv_mfma_f32_16x16x32_f16 v[40:43], v[160:163], v[56:59], v[40:43]\ns_waitcnt lgkmcnt(1)\n"
        "v_mfma_f32_16x16x32_f16 v[36:39], v[160:163], v[60:63], v[36:39]\nv_mfma_f32_16x16x32_f16 v[48:51], v[164:167], v[52:55], v[48:51]\n"
        "v_mfma_f32_16x16x32_f16 v[44:47], v[164:167], v[56:59], v[44:47]\nv_mfma_f32_16x16x32_f16 v[40:43], v[164:167], v[60:63], v[40:43]\ns_waitcnt lgkmcnt(0)\n"
        "v_mfma_f32_16x16x32_f16 v[36:39], v[164:167], v[64:67], v[36:39]\ns_nop 4\nds_write_b128 v14, v[48:51] offset:128\nds_write_b128 %[wa], v[44:47] offset:128\n"
        "ds_write_b128 v14, v[40:43] offset:0\nds_write_b128 %[wa], v[36:39] offset:0\ns_branch .LE%=\n.LT%=:\ns_cmp_eq_u32 %[v], 2\ns_cbranch_scc0 .LV%=\nds_read_b64 v[60:61], %[rb] offset:0\n"
        "ds_read_b128 v[62:65], %[ra] offset:0\nds_read_b64 v[66:67], %[rb] offset:288\nds_read_b128 v[68:71], %[ra] offset:288\nds_read_b64 v[72:73], %[rb] offset:576\n"
        "ds_read_b128 v[74:77], %[ra] offset:576\nds_read_b64 v[78:79], %[rb] offset:864\nds_read_b128 v[80:83], %[ra] offset:864\ns_waitcnt lgkmcnt(6)\n"
        "v_mfma_f32_16x16x32_f16 v[84:87], v[60:63], v[100:103], 0\nv_mfma_f32_16x16x32_f16 v[88:91], v[62:65], v[104:107], 0\nds_read_b64 v[60:61], %[rb] offset:1152\n"
        "ds_read_b128 v[62:65], %[ra] offset:1152\ns_waitcnt lgkmcnt(6)\nv_mfma_f32_16x16x32_f16 v[92:95], v[66:69], v[100:103], 0\nv_mfma_f32_16x16x32_f16 v[96:99], v[68:71], v[104:107], 0\n"
        "ds_read_b64 v[66:67], %[rb] offset:1440\nds_read_b128 v[68:71], %[ra] offset:1440\ns_nop 7\nv_cvt_pk_f16_f32 v136, v84, v85\nv_cvt_pk_f16_f32 v137, v86, v87\nv_cvt_pk_f16_f32 v138, v88, v89\n"
        "v_cvt_pk_f16_f32 v139, v90, v91\ns_nop 1\nv_mfma_f32_16x16x32_f16 v[36:39], v[136:139], v[108:111], 0\ns_waitcnt lgkmcnt(6)\nv_mfma_f32_16x16x32_f16 v[84:87], v[72:75], v[100:103], 0\n"
        "v_mfma_f32_16x16x32_f16 v[88:91], v[74:77], v[104:107], 0\nds_read_b64 v[72:73], %[rb] offset:1728\nds_read_b128 v[74:77], %[ra] offset:1728\nv_cvt_pk_f16_f32 v140, v92, v93\n"
        "v_cvt_pk_f16_f32 v141, v94, v95\nv_cvt_pk_f16_f32 v142, v96, v97\nv_cvt_pk_f16_f32 v143, v98, v99\ns_nop 1\nv_mfma_f32_16x16x32_f16 v[36:39], v[140:143], v[112:115], v[36:39]\n"
        "v_mfma_f32_16x16x32_f16 v[40:43], v[140:143], v[108:111], 0\ns_waitcnt lgkmcnt(6)\nv_mfma_f32_16x16x32_f16 v[92:95], v[78:81], v[100:103], 0\nv_cvt_pk_f16_f32 v136, v84, v85\n"
        "v_cvt_pk_f16_f32 v137, v86, v87\nv_mfma_f32_16x16x32_f16 v[96:99], v[80:83], v[104:107], 0\nv_cvt_pk_f16_f32 v138, v88, v89\nv_cvt_pk_f16_f32 v139, v90, v91\ns_nop 1\n"
        "v_mfma_f32_16x16x32_f16 v[36:39], v[136:139], v[116:119], v[36:39]\nds_read_b64 v[78:79], %[rb] offset:2016\nds_read_b128 v[80:83], %[ra] offset:2016\n"
        "v_mfma_f32_16x16x32_f16 v[40:43], v[136:139], v[112:115], v[40:43]\nv_mfma_f32_16x16x32_f16 v[44:47], v[136:139], v[108:111], 0\nv_cvt_pk_f16_f32 v140, v92, v93\n"
        "v_cvt_pk_f16_f32 v141, v94, v95\ns_waitcnt lgkmcnt(6)\nv_mfma_f32_16x16x32_f16 v[84:87], v[60:63], v[100:103], 0\nv_cvt_pk_f16_f32 v142, v96, v97\nv_cvt_pk_f16_f32 v143, v98, v99\n"
        "v_mfma_f32_16x16x32_f16 v[88:91], v[62:65], v[104:107], 0\nds_read_b64 v[60:61], %[rb] offset:2304\nds_read_b128 v[62:65], %[ra] offset:2304\n"
        "v_mfma_f32_16x16x32_f16 v[36:39], v[140:143], v[120:123], v[36:39]\nv_mfma_f32_16x16x32_f16 v[40:43], v[140:143], v[116:119], v[40:43]\n"
        "v_mfma_f32_16x16x32_f16 v[44:47], v[140:143], v[112:115], v[44:47]\nv_mfma_f32_16x16x32_f16 v[48:51], v[140:143], v[108:111], 0\nv_cvt_pk_f16_f32 v136, v84, v85\n"
        "v_cvt_pk_f16_f32 v137, v86, v87\ns_waitcnt lgkmcnt(6)\nv_mfma_f32_16x16x32_f16 v[92:95], v[66:69], v[100:103], 0\nv_cvt_pk_f16_f32 v138, v88, v89\nv_cvt_pk_f16_f32 v139, v90, v91\n"
        "v_mfma_f32_16x16x32_f16 v[96:99], v[68:71], v[104:107], 0\nds_read_b64 v[66:67], %[rb] offset:2592\nds_read_b128 v[68:71], %[ra] offset:2592\n"
        "v_mfma_f32_16x16x32_f16 v[36:39], v[136:139], v[124:127], v[36:39]\nv_mfma_f32_16x16x32_f16 v[40:43], v[136:139], v[120:123], v[40:43]\n"
        "v_mfma_f32_16x16x32_f16 v[44:47], v[136:139], v[116:119], v[44:47]\nv_mfma_f32_16x16x32_f16 v[48:51], v[136:139], v[112:115], v[48:51]\nds_write_b128 %[wa], v[36:39] offset:0\n"
        "v_mfma_f32_16x16x32_f16 v[52:55], v[136:139], v[108:111], 0\nv_cvt_pk_f16_f32 v140, v92, v93\nv_cvt_pk_f16_f32 v141, v94, v95\ns_waitcnt lgkmcnt(7)\n"
        "v_mfma_f32_16x16x32_f16 v[84:87], v[72:75], v[100:103], 0\nv_cvt_pk_f16_f32 v142, v96, v97\nv_cvt_pk_f16_f32 v143, v98, v99\nv_mfma_f32_16x16x32_f16 v[88:91], v[74:77], v[104:107], 0\n"
        "s_nop 0\nv_mfma_f32_16x16x32_f16 v[40:43], v[140:143], v[124:127], v[40:43]\nv_mfma_f32_16x16x32_f16 v[44:47], v[140:143], v[120:123], v[44:47]\n"
        "v_mfma_f32_16x16x32_f16 v[48:51], v[140:143], v[116:119], v[48:51]\nv_mfma_f32_16x16x32_f16 v[52:55], v[140:143], v[112:115], v[52:55]\nds_write_b128 %[wa], v[40:43] offset:256\n"
        "v_mfma_f32_16x16x32_f16 v[56:59], v[140:143], v[108:111], 0\nv_cvt_pk_f16_f32 v136, v84, v85\nv_cvt_pk_f16_f32 v137, v86, v87\ns_waitcnt lgkmcnt(6)\n"
        "v_mfma_f32_16x16x32_f16 v[92:95], v[78:81], v[100:103], 0\nv_cvt_pk_f16_f32 v138, v88, v89\nv_cvt_pk_f16_f32 v139, v90, v91\nv_mfma_f32_16x16x32_f16 v[96:99], v[80:83], v[104:107], 0\n"
        "s_nop 0\nv_mfma_f32_16x16x32_f16 v[44:47], v[136:139], v[124:127], v[44:47]\nv_mfma_f32_16x16x32_f16 v[48:51], v[136:139], v[120:123], v[48:51]\n"
        "v_mfma_f32_16x16x32_f16 v[52:55], v[136:139], v[116:119], v[52:55]\nv_mfma_f32_16x16x32_f16 v[56:59], v[136:139], v[112:115], v[56:59]\nds_write_b128 %[wa], v[44:47] offset:512\n"
        "v_cvt_pk_f16_f32 v140, v92, v93\ns_waitcnt lgkmcnt(5)\nv_mfma_f32_16x16x32_f16 v[84:87], v[60:63], v[100:103], 0\nv_cvt_pk_f16_f32 v141, v94, v95\nv_cvt_pk_f16_f32 v142, v96, v97\n"
        "v_mfma_f32_16x16x32_f16 v[88:91], v[62:65], v[104:107], 0\nv_cvt_pk_f16_f32 v143, v98, v99\ns_nop 1\nv_mfma_f32_16x16x32_f16 v[48:51], v[140:143], v[124:127], v[48:51]\n"
        "v_mfma_f32_16x16x32_f16 v[52:55], v[140:143], v[120:123], v[52:55]\nv_mfma_f32_16x16x32_f16 v[56:59], v[140:143], v[116:119], v[56:59]\nv_cvt_pk_f16_f32 v136, v84, v85\n"
        "v_cvt_pk_f16_f32 v137, v86, v87\ns_waitcnt lgkmcnt(3)\nv_mfma_f32_16x16x32_f16 v[92:95], v[66:69], v[100:103], 0\nv_cvt_pk_f16_f32 v138, v88, v89\nv_cvt_pk_f16_f32 v139, v90, v91\n"
        "v_mfma_f32_16x16x32_f16 v[96:99], v[68:71], v[104:107], 0\nds_write_b128 %[wa], v[48:51] offset:768\nv_mfma_f32_16x16x32_f16 v[52:55], v[136:139], v[124:127], v[52:55]\n"
        "v_mfma_f32_16x16x32_f16 v[56:59], v[136:139], v[120:123], v[56:59]\ns_nop 7\nv_cvt_pk_f16_f32 v140, v92, v93\nv_cvt_pk_f16_f32 v141, v94, v95\nv_cvt_pk_f16_f32 v142, v96, v97\n"
        "v_cvt_pk_f16_f32 v143, v98, v99\ns_nop 1\nv_mfma_f32_16x16x32_f16 v[56:59], v[140:143], v[124:127], v[56:59]\ns_nop 7\nds_write_b128 %[wa], v[52:55] offset:1024\n"
        "ds_write_b128 %[wa], v[56:59] offset:1280\ns_branch .LE%=\n.LV%=:\nds_read_b128 v[52:55], %[ra] offset:0\nds_read_b128 v[56:59], %[ra] offset:288\nds_read_b128 v[60:63], %[ra] offset:576\n"
        "ds_read_b128 v[64:67], %[ra] offset:864\nds_read_b128 v[84:87], %[rb] offset:0\nds_read_b128 v[88:91], %[rb] offset:576\nds_read_b128 v[92:95], %[rb] offset:1152\n"
        "ds_read_b128 v[96:99], %[rb] offset:1728\ns_waitcnt lgkmcnt(7)\nv_mfma_f32_16x16x32_f16 v[68:71], v[52:55], v[100:103], 0\nv_mfma_f32_16x16x32_f16 v[72:75], v[52:55], v[104:107], 0\n"
        "ds_read_b128 v[52:55], %[ra] offset:1152\ns_waitcnt lgkmcnt(7)\nv_mfma_f32_16x16x32_f16 v[76:79], v[56:59], v[100:103], 0\nv_mfma_f32_16x16x32_f16 v[80:83], v[56:59], v[104:107], 0\n"
        "ds_read_b128 v[56:59], %[ra] offset:1440\ns_waitcnt lgkmcnt(5)\nv_mfma_f32_16x16x32_f16 v[36:39], v[124:127], v[84:87], 0\nv_cvt_pk_f16_f32 v136, v68, v73\nv_cvt_pk_f16_f32 v137, v70, v75\n"
        "s_waitcnt lgkmcnt(4)\nv_mfma_f32_16x16x32_f16 v[40:43], v[124:127], v[88:91], 0\ns_waitcnt lgkmcnt(3)\nv_mfma_f32_16x16x32_f16 v[44:47], v[124:127], v[92:95], 0\n"
        "v_cvt_pk_f16_f32 v138, v76, v81\nv_cvt_pk_f16_f32 v139, v78, v83\ns_waitcnt lgkmcnt(2)\nv_mfma_f32_16x16x32_f16 v[48:51], v[124:127], v[96:99], 0\n"
        "v_mfma_f32_16x16x32_f16 v[68:71], v[60:63], v[100:103], 0\nv_mfma_f32_16x16x32_f16 v[72:75], v[60:63], v[104:107], 0\nds_read_b128 v[60:63], %[ra] offset:1728\n"
        "v_mfma_f32_16x16x32_f16 v[36:39], v[136:139], v[108:111], v[36:39]\nv_mfma_f32_16x16x32_f16 v[40:43], v[136:139], v[112:115], v[40:43]\n"
        "v_mfma_f32_16x16x32_f16 v[44:47], v[136:139], v[116:119], v[44:47]\nv_cvt_pk_f16_f32 v140, v68, v73\nv_cvt_pk_f16_f32 v141, v70, v75\n"
        "v_mfma_f32_16x16x32_f16 v[48:51], v[136:139], v[120:123], v[48:51]\nds_write_b128 %[wa], v[36:39] offset:0\nv_mfma_f32_16x16x32_f16 v[76:79], v[64:67], v[100:103], 0\n"
        "v_mfma_f32_16x16x32_f16 v[80:83], v[64:67], v[104:107], 0\nds_read_b128 v[64:67], %[ra] offset:2016\ns_waitcnt lgkmcnt(4)\nv_mfma_f32_16x16x32_f16 v[68:71], v[52:55], v[100:103], 0\n"
        "v_mfma_f32_16x16x32_f16 v[72:75], v[52:55], v[104:107], 0\ns_nop 7\nv_cvt_pk_f16_f32 v142, v76, v81\nv_cvt_pk_f16_f32 v143, v78, v83\ns_nop 1\n"
        "v_mfma_f32_16x16x32_f16 v[40:43], v[140:143], v[108:111], v[40:43]\nv_mfma_f32_16x16x32_f16 v[44:47], v[140:143], v[112:115], v[44:47]\n"
        "v_mfma_f32_16x16x32_f16 v[48:51], v[140:143], v[116:119], v[48:51]\nv_cvt_pk_f16_f32 v136, v68, v73\nv_cvt_pk_f16_f32 v137, v70, v75\ns_waitcnt lgkmcnt(3)\n"
        "v_mfma_f32_16x16x32_f16 v[76:79], v[56:59], v[100:103], 0\nds_write_b128 %[wa], v[40:43] offset:256\nv_mfma_f32_16x16x32_f16 v[80:83], v[56:59], v[104:107], 0\ns_waitcnt lgkmcnt(3)\n"
        "v_mfma_f32_16x16x32_f16 v[68:71], v[60:63], v[100:103], 0\nv_mfma_f32_16x16x32_f16 v[72:75], v[60:63], v[104:107], 0\ns_nop 7\nv_cvt_pk_f16_f32 v138, v76, v81\n"
        "v_cvt_pk_f16_f32 v139, v78, v83\ns_nop 1\nv_mfma_f32_16x16x32_f16 v[44:47], v[136:139], v[108:111], v[44:47]\nv_mfma_f32_16x16x32_f16 v[48:51], v[136:139], v[112:115], v[48:51]\n"
        "s_waitcnt lgkmcnt(1)\nv_mfma_f32_16x16x32_f16 v[76:79], v[64:67], v[100:103], 0\nv_cvt_pk_f16_f32 v140, v68, v73\nv_cvt_pk_f16_f32 v141, v70, v75\n"
        "v_mfma_f32_16x16x32_f16 v[80:83], v[64:67], v[104:107], 0\nds_write_b128 %[wa], v[44:47] offset:512\ns_nop 7\nv_cvt_pk_f16_f32 v142, v76, v81\nv_cvt_pk_f16_f32 v143, v78, v83\ns_nop 1\n"
        "v_mfma_f32_16x16x32_f16 v[48:51], v[140:143], v[108:111], v[48:51]\ns_nop 7\nds_write_b128 %[wa], v[48:51] offset:768\ns_branch .LE%=\n.LC%=:\ns_sub_u32 %[cap], %[cap], 1\n"
        "s_cbranch_scc0 .LCy%=\ns_mov_b32 %[cap], 0\ns_branch .LD%=\n.LCy%=:\nbuffer_load_dwordx4 v[36:39], %[c16], %[xrs], 0 offen sc1\ns_waitcnt vmcnt(0)\ns_branch .LB%=\n.LI%=:\n"
        "s_sub_u32 %[cap], %[cap], 1\ns_cbranch_scc0 .LIy%=\ns_mov_b32 %[cap], 0\ns_branch .LM%=\n.LIy%=:\ns_sleep %[fp]\nbuffer_load_dwordx4 v[40:43], %[c16], %[frs], 0 offen sc1\n"
        "buffer_load_dwordx4 v[44:47], %[c16], %[frs], %[s1] offen sc1\ns_branch .LF%=\n.LE%=:\ns_mov_b64 exec, 1\nds_add_u32 %[cna], %[one] offset:8\ns_mov_b64 exec, -1\ns_setprio %[pbe]\n"
        "s_mov_b32 %[s1], 0x100000\n.LP%=:\nds_read_b32 %[pv], %[cna] offset:8\ns_waitcnt lgkmcnt(0)\nv_readfirstlane_b32 %[s0], %[pv]\ns_nop 0\ns_cmp_ge_i32 %[s0], 4\ns_cbranch_scc1 .LR%=\n"
        "s_sub_u32 %[s1], %[s1], 1\ns_cbranch_scc0 .LP%=\n.LR%=:\ns_cmp_lt_u32 %[wq], 2\ns_cbranch_scc1 .LS3_%=\nds_read_b128 v[36:39], %[exr] offset:0\nds_read_b128 v[40:43], %[exr] offset:4096\n"
        "ds_read_b128 v[44:47], %[mr] offset:0\nds_read_b128 v[48:51], %[mr] offset:3072\nds_read_b128 v[52:55], %[frr] offset:0\nds_read_b128 v[56:59], %[frr] offset:2304\ns_waitcnt lgkmcnt(4)\n"
        "v_fmac_f32 v26, v36, %[kf]\nv_fmac_f32 v27, v37, %[kf]\nv_fmac_f32 v28, v38, %[kf]\nv_fmac_f32 v29, v39, %[kf]\nv_fmac_f32 v30, v40, %[kf]\nv_fmac_f32 v31, v41, %[kf]\n"
        "v_fmac_f32 v32, v42, %[kf]\nv_fmac_f32 v33, v43, %[kf]\ns_waitcnt lgkmcnt(2)\nv_fmac_f32 v26, v44, %[kf]\nv_fmac_f32 v27, v45, %[kf]\nv_fmac_f32 v28, v46, %[kf]\nv_fmac_f32 v29, v47, %[kf]\n"
        "v_fmac_f32 v30, v48, %[kf]\nv_fmac_f32 v31, v49, %[kf]\nv_fmac_f32 v32, v50, %[kf]\nv_fmac_f32 v33, v51, %[kf]\ns_waitcnt lgkmcnt(0)\nv_fmac_f32 v26, v52, %[kf]\nv_fmac_f32 v27, v53, %[kf]\n"
        "v_fmac_f32 v28, v54, %[kf]\nv_fmac_f32 v29, v55, %[kf]\nv_fmac_f32 v30, v56, %[kf]\nv_fmac_f32 v31, v57, %[kf]\nv_fmac_f32 v32, v58, %[kf]\nv_fmac_f32 v33, v59, %[kf]\ns_branch .LS%=\n"
        ".LS3_%=:\ns_cmp_lt_u32 %[wq], 1\ns_cbranch_scc1 .LS2_%=\nds_read_b128 v[36:39], %[exr] offset:0\nds_read_b128 v[40:43], %[exr] offset:4096\nds_read_b128 v[44:47], %[mr] offset:0\n"
        "ds_read_b128 v[48:51], %[mr] offset:3072\ns_waitcnt lgkmcnt(2)\nv_fmac_f32 v26, v36, %[kf]\nv_fmac_f32 v27, v37, %[kf]\nv_fmac_f32 v28, v38, %[kf]\nv_fmac_f32 v29, v39, %[kf]\n"
        "v_fmac_f32 v30, v40, %[kf]\nv_fmac_f32 v31, v41, %[kf]\nv_fmac_f32 v32, v42, %[kf]\nv_fmac_f32 v33, v43, %[kf]\ns_waitcnt lgkmcnt(0)\nv_fmac_f32 v26, v44, %[kf]\nv_fmac_f32 v27, v45, %[kf]\n"
        "v_fmac_f32 v28, v46, %[kf]\nv_fmac_f32 v29, v47, %[kf]\nv_fmac_f32 v30, v48, %[kf]\nv_fmac_f32 v31, v49, %[kf]\nv_fmac_f32 v32, v50, %[kf]\nv_fmac_f32 v33, v51, %[kf]\ns_branch .LS%=\n"
        ".LS2_%=:\nds_read_b128 v[36:39], %[exr] offset:0\nds_read_b128 v[40:43], %[exr] offset:4096\ns_waitcnt lgkmcnt(0)\nv_fmac_f32 v26, v36, %[kf]\nv_fmac_f32 v27, v37, %[kf]\n"
        "v_fmac_f32 v28, v38, %[kf]\nv_fmac_f32 v29, v39, %[kf]\nv_fmac_f32 v30, v40, %[kf]\nv_fmac_f32 v31, v41, %[kf]\nv_fmac_f32 v32, v42, %[kf]\nv_fmac_f32 v33, v43, %[kf]\n.LS%=:\n"
        "v_cvt_pk_bf16_f32 v36, v26, v27\nv_cvt_pk_bf16_f32 v37, v28, v29\nv_cvt_pk_bf16_f32 v38, v30, v31\nv_cvt_pk_bf16_f32 v39, v32, v33\nv_min_u32 v36, -2, v36\nv_min_u32 v37, -2, v37\n"
        "v_min_u32 v38, -2, v38\nv_min_u32 v39, -2, v39\nbuffer_store_dwordx4 v[36:39], %[od], %[ors], 0 offen sc0 sc1\ns_nop 1\n.LZ%=:\n"
        : "+{v[100:103]}"(kt[0]), "+{v[104:107]}"(kt[1]), "+{v[108:111]}"(kt[2]), "+{v[112:115]}"(kt[3]), "+{v[116:119]}"(kt[4]), "+{v[120:123]}"(kt[5]), "+{v[124:127]}"(kt[6]), "+{v[128:131]}"(kt[7]), "+{v[132:135]}"(kt[8]), "+{v[136:139]}"(kt[9]), "+{v[140:143]}"(kt[10]), "+{v[144:147]}"(kt[11]), "+{v[148:151]}"(kt[12]), "+{v[152:155]}"(kt[13]), "+{v[156:159]}"(kt[14]), "+{v[160:163]}"(kt[15]), "+{v[164:167]}"(kt[16]), [pv] "=&{v16}"(pv), [s0] "=&s"(s0_), [s1] "=&s"(s1_), [cap] "=&s"(cap)
        : [ra] "{v1}"(ra), [rb] "{v2}"(rb), [wa] "{v3}"(wa), [cna] "{v4}"(cna), [one] "{v5}"(one), [exr] "{v6}"(exr), [frr] "{v7}"(frr), [mr] "{v8}"(mr_), [od] "{v9}"(od), [kf] "{v10}"(kd[0]), [kd1] "{v11}"(kd[1]), [c16] "{v12}"(c16), [uw] "{v17}"(uw_), [sy] "{v23}"(sya), [ofs] "i"(SY_FS), [pbl] "i"(PBL), [v] "s"(v), [wq] "s"(wq), [ors] "s"(ors), [xrs] "s"(xrs), [frs] "s"(frs), [pbe] "i"(PBE), [fsx] "i"(FSX), [fp] "i"(FPOLL)
        : "memory", "scc", "v13", "v14", "v15", "v26", "v27", "v28", "v29", "v30", "v31", "v32", "v33", "v36", "v37", "v38", "v39", "v40", "v41", "v42", "v43", "v44", "v45", "v46", "v47", "v48", "v49", "v50", "v51", "v52", "v53", "v54", "v55", "v56", "v57", "v58", "v59", "v60", "v61", "v62", "v63", "v64", "v65", "v66", "v67", "v68", "v69", "v70", "v71", "v72", "v73", "v74", "v75", "v76", "v77", "v78", "v79", "v80", "v81", "v82", "v83", "v84", "v85", "v86", "v87", "v88", "v89", "v90", "v91", "v92", "v93", "v94", "v95", "v96", "v97", "v98", "v99", "vcc");
    } else if (FULL) {
      asm volatile(
        "s_mov_b32 %[cap], 0x100000\ns_movk_i32 %[s1], 4096\nv_mov_b32 v56, -1\nv_mov_b32 v57, -1\nv_mov_b32 v58, -1\nv_mov_b32 v59, -1\ns_sleep %[fsx]\n"
        "buffer_load_dwordx4 v[36:39], %[c16], %[xrs], 0 offen sc1\n.LA%=:\nds_read_b32 %[pv], %[sy] offset:%[ofs]\ns_waitcnt lgkmcnt(0)\nv_readfirstlane_b32 %[s0], %[pv]\ns_nop 0\n"
        "s_cmp_ge_i32 %[s0], 2\ns_cbranch_scc1 .LX%=\ns_sub_u32 %[cap], %[cap], 1\ns_cbranch_scc0 .LA%=\n.LX%=:\ns_mov_b32 %[cap], 0x10000\nbuffer_load_dwordx4 v[40:43], %[c16], %[frs], 0 offen sc1\n"
        "buffer_load_dwordx4 v[44:47], %[c16], %[frs], %[s1] offen sc1\ns_waitcnt vmcnt(2)\n.LB%=:\nv_max3_u32 %[pv], v36, v37, v38\nv_max_u32 %[pv], %[pv], v39\nv_cmp_eq_u32 vcc, -1, %[pv]\n"
        "s_cbranch_vccnz .LC%=\n.LD%=:\nv_lshlrev_b32 v26, 16, v36\nv_and_b32 v27, 0xffff0000, v36\nv_lshlrev_b32 v28, 16, v37\nv_and_b32 v29, 0xffff0000, v37\nv_lshlrev_b32 v30, 16, v38\n"
        "v_and_b32 v31, 0xffff0000, v38\nv_lshlrev_b32 v32, 16, v39\nv_and_b32 v33, 0xffff0000, v39\n.LF%=:\ns_waitcnt vmcnt(1)\nv_max3_u32 %[pv], v40, v41, v42\nv_lshlrev_b32 v48, 16, v40\n"
        "v_lshlrev_b32 v50, 16, v41\nv_lshlrev_b32 v52, 16, v42\nv_lshlrev_b32 v54, 16, v43\nv_fmac_f32 v48, v26, v40\nv_fmac_f32 v50, v28, v41\nv_fmac_f32 v52, v30, v42\nv_fmac_f32 v54, v32, v43\n"
        "s_waitcnt vmcnt(0)\nv_max3_u32 %[pv], %[pv], v43, v44\nv_max3_u32 %[pv], %[pv], v45, v46\nv_max_u32 %[pv], %[pv], v47\nv_cmp_eq_u32 vcc, -1, %[pv]\nv_lshlrev_b32 v49, 16, v44\n"
        "v_lshlrev_b32 v51, 16, v45\nv_lshlrev_b32 v53, 16, v46\nv_lshlrev_b32 v55, 16, v47\ns_cbranch_vccnz .LI%=\n.LM%=:\nv_fmac_f32 v49, v27, v44\nv_fmac_f32 v51, v29, v45\n"
        "v_fmac_f32 v53, v31, v46\nv_fmac_f32 v55, v33, v47\nv_cvt_pk_f16_f32 v36, v48, v49\nv_cvt_pk_f16_f32 v37, v50, v51\nv_cvt_pk_f16_f32 v38, v52, v53\nv_cvt_pk_f16_f32 v39, v54, v55\n"
        "ds_write_b128 %[uw], v[36:39]\ns_mov_b64 exec, 1\nds_add_u32 %[cna], %[one] offset:0\ns_mov_b64 exec, -1\ns_cmp_eq_u32 %[gq], 0\ns_cbranch_scc1 .LY%=\ns_movk_i32 %[s0], 0\n"
        "buffer_store_dwordx4 v[56:59], %[mo], %[mrs], %[s0] offen sc0 sc1\ns_movk_i32 %[s0], 4096\nbuffer_store_dwordx4 v[56:59], %[mo], %[mrs], %[s0] offen sc0 sc1\ns_movk_i32 %[s0], 8192\n"
        "buffer_store_dwordx4 v[56:59], %[mo], %[mrs], %[s0] offen sc0 sc1\ns_movk_i32 %[s0], 12288\nbuffer_store_dwordx4 v[56:59], %[mo], %[mrs], %[s0] offen sc0 sc1\ns_movk_i32 %[s0], 16384\n"
        "buffer_store_dwordx4 v[56:59], %[mo], %[mrs], %[s0] offen sc0 sc1\ns_movk_i32 %[s0], 20480\nbuffer_store_dwordx4 v[56:59], %[mo], %[mrs], %[s0] offen sc0 sc1\ns_movk_i32 %[s0], 24576\n"
        "buffer_store_dwordx4 v[56:59], %[mo], %[mrs], %[s0] offen sc0 sc1\ns_movk_i32 %[s0], 28672\nbuffer_store_dwordx4 v[56:59], %[mo], %[mrs], %[s0] offen sc0 sc1\n.LY%=:\n"
        "v_fmac_f32 v26, v48, %[kd1]\nv_fmac_f32 v27, v49, %[kd1]\nv_fmac_f32 v28, v50, %[kd1]\nv_fmac_f32 v29, v51, %[kd1]\nv_fmac_f32 v30, v52, %[kd1]\nv_fmac_f32 v31, v53, %[kd1]\n"
        "v_fmac_f32 v32, v54, %[kd1]\nv_fmac_f32 v33, v55, %[kd1]\ns_setprio %[pbl]\ns_mov_b32 %[s1], 0x100000\n.LN%=:\nds_read_b32 %[pv], %[cna]\ns_waitcnt lgkmcnt(0)\n"
        "v_readfirstlane_b32 %[s0], %[pv]\ns_nop 0\ns_cmp_ge_i32 %[s0], 4\ns_cbranch_scc1 .LO%=\ns_sub_u32 %[s1], %[s1], 1\ns_cbranch_scc0 .LN%=\n.LO%=:\ns_cmp_lt_u32 %[v], 2\ns_cbranch_scc0 .LT%=\n"
        "ds_read_b128 v[52:55], %[ra] offset:1376\nds_read_b128 v[56:59], %[ra] offset:1312\nds_read_b128 v[60:63], %[ra] offset:1248\nds_read_b128 v[64:67], %[rb] offset:1152\n"
        "ds_read_b128 v[68:71], %[ra] offset:1088\nds_read_b128 v[72:75], %[ra] offset:1024\nds_read_b128 v[76:79], %[ra] offset:960\nds_read_b128 v[80:83], %[rb] offset:864\nv_xor_b32 v14, 64, %[wa]\n"
        "s_waitcnt lgkmcnt(7)\nv_mfma_f32_16x16x32_f16 v[48:51], v[100:103], v[52:55], 0\nds_read_b128 v[52:55], %[ra] offset:800\ns_waitcnt lgkmcnt(7)\n"
        "v_mfma_f32_16x16x32_f16 v[44:47], v[100:103], v[56:59], 0\ns_waitcnt lgkmcnt(6)\nv_mfma_f32_16x16x32_f16 v[40:43], v[100:103], v[60:63], 0\ns_waitcnt lgkmcnt(5)\n"
        "v_mfma_f32_16x16x32_f16 v[36:39], v[100:103], v[64:67], 0\nv_mfma_f32_16x16x32_f16 v[48:51], v[104:107], v[56:59], v[48:51]\nds_read_b128 v[56:59], %[ra] offset:736\n"
        "v_mfma_f32_16x16x32_f16 v[44:47], v[104:107], v[60:63], v[44:47]\nv_mfma_f32_16x16x32_f16 v[40:43], v[104:107], v[64:67], v[40:43]\ns_waitcnt lgkmcnt(5)\n"
        "v_mfma_f32_16x16x32_f16 v[36:39], v[104:107], v[68:71], v[36:39]\nv_mfma_f32_16x16x32_f16 v[48:51], v[108:111], v[60:63], v[48:51]\nds_read_b128 v[60:63], %[ra] offset:672\n"
        "v_mfma_f32_16x16x32_f16 v[44:47], v[108:111], v[64:67], v[44:47]\nv_mfma_f32_16x16x32_f16 v[40:43], v[108:111], v[68:71], v[40:43]\ns_waitcnt lgkmcnt(5)\n"
        "v_mfma_f32_16x16x32_f16 v[36:39], v[108:111], v[72:75], v[36:39]\nv_mfma_f32_16x16x32_f16 v[48:51], v[112:115], v[64:67], v[48:51]\nds_read_b128 v[64:67], %[rb] offset:576\n"
        "v_mfma_f32_16x16x32_f16 v[44:47], v[112:115], v[68:71], v[44:47]\nv_mfma_f32_16x16x32_f16 v[40:43], v[112:115], v[72:75], v[40:43]\ns_waitcnt lgkmcnt(5)\n"
        "v_mfma_f32_16x16x32_f16 v[36:39], v[112:115], v[76:79], v[36:39]\nv_mfma_f32_16x16x32_f16 v[48:51], v[116:119], v[68:71], v[48:51]\nds_read_b128 v[68:71], %[ra] offset:512\n"
        "v_mfma_f32_16x16x32_f16 v[44:47], v[116:119], v[72:75], v[44:47]\nv_mfma_f32_16x16x32_f16 v[40:43], v[116:119], v[76:79], v[40:43]\ns_waitcnt lgkmcnt(5)\n"
        "v_mfma_f32_16x16x32_f16 v[36:39], v[116:119], v[80:83], v[36:39]\nv_mfma_f32_16x16x32_f16 v[48:51], v[120:123], v[72:75], v[48:51]\nds_read_b128 v[72:75], %[ra] offset:448\n"
        "v_mfma_f32_16x16x32_f16 v[44:47], v[120:123], v[76:79], v[44:47]\nv_mfma_f32_16x16x32_f16 v[40:43], v[120:123], v[80:83], v[40:43]\ns_waitcnt lgkmcnt(5)\n"
        "v_mfma_f32_16x16x32_f16 v[36:39], v[120:123], v[52:55], v[36:39]\nv_mfma_f32_16x16x32_f16 v[48:51], v[124:127], v[76:79], v[48:51]\nds_read_b128 v[76:79], %[ra] offset:384\n"
        "v_mfma_f32_16x16x32_f16 v[44:47], v[124:127], v[80:83], v[44:47]\nv_mfma_f32_16x16x32_f16 v[40:43], v[124:127], v[52:55], v[40:43]\ns_waitcnt lgkmcnt(5)\n"
        "v_mfma_f32_16x16x32_f16 v[36:39], v[124:127], v[56:59], v[36:39]\nv_mfma_f32_16x16x32_f16 v[48:51], v[128:131], v[80:83], v[48:51]\nds_read_b128 v[80:83], %[rb] offset:288\n"
        "v_mfma_f32_16x16x32_f16 v[44:47], v[128:131], v[52:55], v[44:47]\nv_mfma_f32_16x16x32_f16 v[40:43], v[128:131], v[56:59], v[40:43]\ns_waitcnt lgkmcnt(5)\n"
        "v_mfma_f32_16x16x32_f16 v[36:39], v[128:131], v[60:63], v[36:39]\nv_mfma_f32_16x16x32_f16 v[48:51], v[132:135], v[52:55], v[48:51]\nds_read_b128 v[52:55], %[ra] offset:224\n"
        "v_mfma_f32_16x16x32_f16 v[44:47], v[132:135], v[56:59], v[44:47]\nv_mfma_f32_16x16x32_f16 v[40:43], v[132:135], v[60:63], v[40:43]\ns_waitcnt lgkmcnt(5)\n"
        "v_mfma_f32_16x16x32_f16 v[36:39], v[132:135], v[64:67], v[36:39]\nv_mfma_f32_16x16x32_f16 v[48:51], v[136:139], v[56:59], v[48:51]\nds_read_b128 v[56:59], %[ra] offset:160\n"
        "v_mfma_f32_16x16x32_f16 v[44:47], v[136:139], v[60:63], v[44:47]\nv_mfma_f32_16x16x32_f16 v[40:43], v[136:139], v[64:67], v[40:43]\ns_waitcnt lgkmcnt(5)\n"
        "v_mfma_f32_16x16x32_f16 v[36:39], v[136:139], v[68:71], v[36:39]\nv_mfma_f32_16x16x32_f16 v[48:51], v[140:143], v[60:63], v[48:51]\nds_read_b128 v[60:63], %[ra] offset:96\n"
        "v_mfma_f32_16x16x32_f16 v[44:47], v[140:143], v[64:67], v[44:47]\nv_mfma_f32_16x16x32_f16 v[40:43], v[140:143], v[68:71], v[40:43]\ns_waitcnt lgkmcnt(5)\n"
        "v_mfma_f32_16x16x32_f16 v[36:39], v[140:143], v[72:75], v[36:39]\nv_mfma_f32_16x16x32_f16 v[48:51], v[144:147], v[64:67], v[48:51]\nds_read_b128 v[64:67], %[rb] offset:0\n"
        "v_mfma_f32_16x16x32_f16 v[44:47], v[144:147], v[68:71], v[44:47]\nv_mfma_f32_16x16x32_f16 v[40:43], v[144:147], v[72:75], v[40:43]\ns_waitcnt lgkmcnt(5)\n"
        "v_mfma_f32_16x16x32_f16 v[36:39], v[144:147], v[76:79], v[36:39]\nv_mfma_f32_16x16x32_f16 v[48:51], v[148:151], v[68:71], v[48:51]\n"
        "v_mfma_f32_16x16x32_f16 v[44:47], v[148:151], v[72:75], v[44:47]\nv_mfma_f32_16x16x32_f16 v[40:43], v[148:151], v[76:79], v[40:43]\ns_waitcnt lgkmcnt(4)\n"
        "v_mfma_f32_16x16x32_f16 v[36:39], v[148:151], v[80:83], v[36:39]\nv_mfma_f32_16x16x32_f16 v[48:51], v[152:155], v[72:75], v[48:51]\n"
        "v_mfma_f32_16x16x32_f16 v[44:47], v[152:155], v[76:79], v[44:47]\nv_mfma_f32_16x16x32_f16 v[40:43], v[152:155], v[80:83], v[40:43]\ns_waitcnt lgkmcnt(3)\n"
        "v_mfma_f32_16x16x32_f16 v[36:39], v[152:155], v[52:55], v[36:39]\nv_mfma_f32_16x16x32_f16 v[48:51], v[156:159], v[76:79], v[48:51]\n"
        "v_mfma_f32_16x16x32_f16 v[44:47], v[156:159], v[80:83], v[44:47]\nv_mfma_f32_16x16x32_f16 v[40:43], v[156:159], v[52:55], v[40:43]\ns_waitcnt lgkmcnt(2)\n"
        "v_mfma_f32_16x16x32_f16 v[36:39], v[156:159], v[56:59], v[36:39]\nv_mfma_f32_16x16x32_f16 v[48:51], v[160:163], v[80:83], v[48:51]\n"
        "v_mfma_f32_16x16x32_f16 v[44:47], v[160:163], v[52:55], v[44:47]\nv_mfma_f32_16x16x32_f16 v[40:43], v[160:163], v[56:59], v[40:43]\ns_waitcnt lgkmcnt(1)\n"
        "v_mfma_f32_16x16x32_f16 v[36:39], v[160:163], v[60:63], v[36:39]\nv_mfma_f32_16x16x32_f16 v[48:51], v[164:167], v[52:55], v[48:51]\n"
        "v_mfma_f32_16x16x32_f16 v[44:47], v[164:167], v[56:59], v[44:47]\nv_mfma_f32_16x16x32_f16 v[40:43], v[164:167], v[60:63], v[40:43]\ns_waitcnt lgkmcnt(0)\n"
        "v_mfma_f32_16x16x32_f16 v[36:39], v[164:167], v[64:67], v[36:39]\ns_nop 4\nds_write_b128 v14, v[48:51] offset:128\nds_write_b128 %[wa], v[44:47] offset:128\n"
        "ds_write_b128 v14, v[40:43] offset:0\nds_write_b128 %[wa], v[36:39] offset:0\ns_branch .LE%=\n.LT%=:\ns_cmp_eq_u32 %[v], 2\ns_cbranch_scc0 .LV%=\nds_read_b64 v[60:61], %[rb] offset:0\n"
        "ds_read_b128 v[62:65], %[ra] offset:0\nds_read_b64 v[66:67], %[rb] offset:288\nds_read_b128 v[68:71], %[ra] offset:288\nds_read_b64 v[72:73], %[rb] offset:576\n"
        "ds_read_b128 v[74:77], %[ra] offset:576\nds_read_b64 v[78:79], %[rb] offset:864\nds_read_b128 v[80:83], %[ra] offset:864\ns_waitcnt lgkmcnt(6)\n"
        "v_mfma_f32_16x16x32_f16 v[84:87], v[60:63], v[100:103], 0\nv_mfma_f32_16x16x32_f16 v[88:91], v[62:65], v[104:107], 0\nds_read_b64 v[60:61], %[rb] offset:1152\n"
        "ds_read_b128 v[62:65], %[ra] offset:1152\ns_waitcnt lgkmcnt(6)\nv_mfma_f32_16x16x32_f16 v[92:95], v[66:69], v[100:103], 0\nv_mfma_f32_16x16x32_f16 v[96:99], v[68:71], v[104:107], 0\n"
        "ds_read_b64 v[66:67], %[rb] offset:1440\nds_read_b128 v[68:71], %[ra] offset:1440\ns_nop 7\nv_cvt_pk_f16_f32 v136, v84, v85\nv_cvt_pk_f16_f32 v137, v86, v87\nv_cvt_pk_f16_f32 v138, v88, v89\n"
        "v_cvt_pk_f16_f32 v139, v90, v91\ns_nop 1\nv_mfma_f32_16x16x32_f16 v[36:39], v[136:139], v[108:111], 0\ns_waitcnt lgkmcnt(6)\nv_mfma_f32_16x16x32_f16 v[84:87], v[72:75], v[100:103], 0\n"
        "v_mfma_f32_16x16x32_f16 v[88:91], v[74:77], v[104:107], 0\nds_read_b64 v[72:73], %[rb] offset:1728\nds_read_b128 v[74:77], %[ra] offset:1728\nv_cvt_pk_f16_f32 v140, v92, v93\n"
        "v_cvt_pk_f16_f32 v141, v94, v95\nv_cvt_pk_f16_f32 v142, v96, v97\nv_cvt_pk_f16_f32 v143, v98, v99\ns_nop 1\nv_mfma_f32_16x16x32_f16 v[36:39], v[140:143], v[112:115], v[36:39]\n"
        "v_mfma_f32_16x16x32_f16 v[40:43], v[140:143], v[108:111], 0\ns_waitcnt lgkmcnt(6)\nv_mfma_f32_16x16x32_f16 v[92:95], v[78:81], v[100:103], 0\nv_cvt_pk_f16_f32 v136, v84, v85\n"
        "v_cvt_pk_f16_f32 v137, v86, v87\nv_mfma_f32_16x16x32_f16 v[96:99], v[80:83], v[104:107], 0\nv_cvt_pk_f16_f32 v138, v88, v89\nv_cvt_pk_f16_f32 v139, v90, v91\ns_nop 1\n"
        "v_mfma_f32_16x16x32_f16 v[36:39], v[136:139], v[116:119], v[36:39]\nds_read_b64 v[78:79], %[rb] offset:2016\nds_read_b128 v[80:83], %[ra] offset:2016\n"
        "v_mfma_f32_16x16x32_f16 v[40:43], v[136:139], v[112:115], v[40:43]\nv_mfma_f32_16x16x32_f16 v[44:47], v[136:139], v[108:111], 0\nv_cvt_pk_f16_f32 v140, v92, v93\n"
        "v_cvt_pk_f16_f32 v141, v94, v95\ns_waitcnt lgkmcnt(6)\nv_mfma_f32_16x16x32_f16 v[84:87], v[60:63], v[100:103], 0\nv_cvt_pk_f16_f32 v142, v96, v97\nv_cvt_pk_f16_f32 v143, v98, v99\n"
        "v_mfma_f32_16x16x32_f16 v[88:91], v[62:65], v[104:107], 0\nds_read_b64 v[60:61], %[rb] offset:2304\nds_read_b128 v[62:65], %[ra] offset:2304\n"
        "v_mfma_f32_16x16x32_f16 v[36:39], v[140:143], v[120:123], v[36:39]\nv_mfma_f32_16x16x32_f16 v[40:43], v[140:143], v[116:119], v[40:43]\n"
        "v_mfma_f32_16x16x32_f16 v[44:47], v[140:143], v[112:115], v[44:47]\nv_mfma_f32_16x16x32_f16 v[48:51], v[140:143], v[108:111], 0\nv_cvt_pk_f16_f32 v136, v84, v85\n"
        "v_cvt_pk_f16_f32 v137, v86, v87\ns_waitcnt lgkmcnt(6)\nv_mfma_f32_16x16x32_f16 v[92:95], v[66:69], v[100:103], 0\nv_cvt_pk_f16_f32 v138, v88, v89\nv_cvt_pk_f16_f32 v139, v90, v91\n"
        "v_mfma_f32_16x16x32_f16 v[96:99], v[68:71], v[104:107], 0\nds_read_b64 v[66:67], %[rb] offset:2592\nds_read_b128 v[68:71], %[ra] offset:2592\n"
        "v_mfma_f32_16x16x32_f16 v[36:39], v[136:139], v[124:127], v[36:39]\nv_mfma_f32_16x16x32_f16 v[40:43], v[136:139], v[120:123], v[40:43]\n"
        "v_mfma_f32_16x16x32_f16 v[44:47], v[136:139], v[116:119], v[44:47]\nv_mfma_f32_16x16x32_f16 v[48:51], v[136:139], v[112:115], v[48:51]\nds_write_b128 %[wa], v[36:39] offset:0\n"
        "v_mfma_f32_16x16x32_f16 v[52:55], v[136:139], v[108:111], 0\nv_cvt_pk_f16_f32 v140, v92, v93\nv_cvt_pk_f16_f32 v141, v94, v95\ns_waitcnt lgkmcnt(7)\n"
        "v_mfma_f32_16x16x32_f16 v[84:87], v[72:75], v[100:103], 0\nv_cvt_pk_f16_f32 v142, v96, v97\nv_cvt_pk_f16_f32 v143, v98, v99\nv_mfma_f32_16x16x32_f16 v[88:91], v[74:77], v[104:107], 0\n"
        "s_nop 0\nv_mfma_f32_16x16x32_f16 v[40:43], v[140:143], v[124:127], v[40:43]\nv_mfma_f32_16x16x32_f16 v[44:47], v[140:143], v[120:123], v[44:47]\n"
        "v_mfma_f32_16x16x32_f16 v[48:51], v[140:143], v[116:119], v[48:51]\nv_mfma_f32_16x16x32_f16 v[52:55], v[140:143], v[112:115], v[52:55]\nds_write_b128 %[wa], v[40:43] offset:256\n"
        "v_mfma_f32_16x16x32_f16 v[56:59], v[140:143], v[108:111], 0\nv_cvt_pk_f16_f32 v136, v84, v85\nv_cvt_pk_f16_f32 v137, v86, v87\ns_waitcnt lgkmcnt(6)\n"
        "v_mfma_f32_16x16x32_f16 v[92:95], v[78:81], v[100:103], 0\nv_cvt_pk_f16_f32 v138, v88, v89\nv_cvt_pk_f16_f32 v139, v90, v91\nv_mfma_f32_16x16x32_f16 v[96:99], v[80:83], v[104:107], 0\n"
        "s_nop 0\nv_mfma_f32_16x16x32_f16 v[44:47], v[136:139], v[124:127], v[44:47]\nv_mfma_f32_16x16x32_f16 v[48:51], v[136:139], v[120:123], v[48:51]\n"
        "v_mfma_f32_16x16x32_f16 v[52:55], v[136:139], v[116:119], v[52:55]\nv_mfma_f32_16x16x32_f16 v[56:59], v[136:139], v[112:115], v[56:59]\nds_write_b128 %[wa], v[44:47] offset:512\n"
        "v_cvt_pk_f16_f32 v140, v92, v93\ns_waitcnt lgkmcnt(5)\nv_mfma_f32_16x16x32_f16 v[84:87], v[60:63], v[100:103], 0\nv_cvt_pk_f16_f32 v141, v94, v95\nv_cvt_pk_f16_f32 v142, v96, v97\n"
        "v_mfma_f32_16x16x32_f16 v[88:91], v[62:65], v[104:107], 0\nv_cvt_pk_f16_f32 v143, v98, v99\ns_nop 1\nv_mfma_f32_16x16x32_f16 v[48:51], v[140:143], v[124:127], v[48:51]\n"
        "v_mfma_f32_16x16x32_f16 v[52:55], v[140:143], v[120:123], v[52:55]\nv_mfma_f32_16x16x32_f16 v[56:59], v[140:143], v[116:119], v[56:59]\nv_cvt_pk_f16_f32 v136, v84, v85\n"
        "v_cvt_pk_f16_f32 v137, v86, v87\ns_waitcnt lgkmcnt(3)\nv_mfma_f32_16x16x32_f16 v[92:95], v[66:69], v[100:103], 0\nv_cvt_pk_f16_f32 v138, v88, v89\nv_cvt_pk_f16_f32 v139, v90, v91\n"
        "v_mfma_f32_16x16x32_f16 v[96:99], v[68:71], v[104:107], 0\nds_write_b128 %[wa], v[48:51] offset:768\nv_mfma_f32_16x16x32_f16 v[52:55], v[136:139], v[124:127], v[52:55]\n"
        "v_mfma_f32_16x16x32_f16 v[56:59], v[136:139], v[120:123], v[56:59]\ns_nop 7\nv_cvt_pk_f16_f32 v140, v92, v93\nv_cvt_pk_f16_f32 v141, v94, v95\nv_cvt_pk_f16_f32 v142, v96, v97\n"
        "v_cvt_pk_f16_f32 v143, v98, v99\ns_nop 1\nv_mfma_f32_16x16x32_f16 v[56:59], v[140:143], v[124:127], v[56:59]\ns_nop 7\nds_write_b128 %[wa], v[52:55] offset:1024\n"
        "ds_write_b128 %[wa], v[56:59] offset:1280\ns_branch .LE%=\n.LV%=:\nds_read_b128 v[52:55], %[ra] offset:0\nds_read_b128 v[56:59], %[ra] offset:288\nds_read_b128 v[60:63], %[ra] offset:576\n"
        "ds_read_b128 v[64:67], %[ra] offset:864\nds_read_b128 v[84:87], %[rb] offset:0\nds_read_b128 v[88:91], %[rb] offset:576\nds_read_b128 v[92:95], %[rb] offset:1152\n"
        "ds_read_b128 v[96:99], %[rb] offset:1728\ns_waitcnt lgkmcnt(7)\nv_mfma_f32_16x16x32_f16 v[68:71], v[52:55], v[100:103], 0\nv_mfma_f32_16x16x32_f16 v[72:75], v[52:55], v[104:107], 0\n"
        "ds_read_b128 v[52:55], %[ra] offset:1152\ns_waitcnt lgkmcnt(7)\nv_mfma_f32_16x16x32_f16 v[76:79], v[56:59], v[100:103], 0\nv_mfma_f32_16x16x32_f16 v[80:83], v[56:59], v[104:107], 0\n"
        "ds_read_b128 v[56:59], %[ra] offset:1440\ns_waitcnt lgkmcnt(5)\nv_mfma_f32_16x16x32_f16 v[36:39], v[124:127], v[84:87], 0\nv_cvt_pk_f16_f32 v136, v68, v73\nv_cvt_pk_f16_f32 v137, v70, v75\n"
        "s_waitcnt lgkmcnt(4)\nv_mfma_f32_16x16x32_f16 v[40:43], v[124:127], v[88:91], 0\ns_waitcnt lgkmcnt(3)\nv_mfma_f32_16x16x32_f16 v[44:47], v[124:127], v[92:95], 0\n"
        "v_cvt_pk_f16_f32 v138, v76, v81\nv_cvt_pk_f16_f32 v139, v78, v83\ns_waitcnt lgkmcnt(2)\nv_mfma_f32_16x16x32_f16 v[48:51], v[124:127], v[96:99], 0\n"
        "v_mfma_f32_16x16x32_f16 v[68:71], v[60:63], v[100:103], 0\nv_mfma_f32_16x16x32_f16 v[72:75], v[60:63], v[104:107], 0\nds_read_b128 v[60:63], %[ra] offset:1728\n"
        "v_mfma_f32_16x16x32_f16 v[36:39], v[136:139], v[108:111], v[36:39]\nv_mfma_f32_16x16x32_f16 v[40:43], v[136:139], v[112:115], v[40:43]\n"
        "v_mfma_f32_16x16x32_f16 v[44:47], v[136:139], v[116:119], v[44:47]\nv_cvt_pk_f16_f32 v140, v68, v73\nv_cvt_pk_f16_f32 v141, v70, v75\n"
        "v_mfma_f32_16x16x32_f16 v[48:51], v[136:139], v[120:123], v[48:51]\nds_write_b128 %[wa], v[36:39] offset:0\nv_mfma_f32_16x16x32_f16 v[76:79], v[64:67], v[100:103], 0\n"
        "v_mfma_f32_16x16x32_f16 v[80:83], v[64:67], v[104:107], 0\nds_read_b128 v[64:67], %[ra] offset:2016\ns_waitcnt lgkmcnt(4)\nv_mfma_f32_16x16x32_f16 v[68:71], v[52:55], v[100:103], 0\n"
        "v_mfma_f32_16x16x32_f16 v[72:75], v[52:55], v[104:107], 0\ns_nop 7\nv_cvt_pk_f16_f32 v142, v76, v81\nv_cvt_pk_f16_f32 v143, v78, v83\ns_nop 1\n"
        "v_mfma_f32_16x16x32_f16 v[40:43], v[140:143], v[108:111], v[40:43]\nv_mfma_f32_16x16x32_f16 v[44:47], v[140:143], v[112:115], v[44:47]\n"
        "v_mfma_f32_16x16x32_f16 v[48:51], v[140:143], v[116:119], v[48:51]\nv_cvt_pk_f16_f32 v136, v68, v73\nv_cvt_pk_f16_f32 v137, v70, v75\ns_waitcnt lgkmcnt(3)\n"
        "v_mfma_f32_16x16x32_f16 v[76:79], v[56:59], v[100:103], 0\nds_write_b128 %[wa], v[40:43] offset:256\nv_mfma_f32_16x16x32_f16 v[80:83], v[56:59], v[104:107], 0\ns_waitcnt lgkmcnt(3)\n"
        "v_mfma_f32_16x16x32_f16 v[68:71], v[60:63], v[100:103], 0\nv_mfma_f32_16x16x32_f16 v[72:75], v[60:63], v[104:107], 0\ns_nop 7\nv_cvt_pk_f16_f32 v138, v76, v81\n"
        "v_cvt_pk_f16_f32 v139, v78, v83\ns_nop 1\nv_mfma_f32_16x16x32_f16 v[44:47], v[136:139], v[108:111], v[44:47]\nv_mfma_f32_16x16x32_f16 v[48:51], v[136:139], v[112:115], v[48:51]\n"
        "s_waitcnt lgkmcnt(1)\nv_mfma_f32_16x16x32_f16 v[76:79], v[64:67], v[100:103], 0\nv_cvt_pk_f16_f32 v140, v68, v73\nv_cvt_pk_f16_f32 v141, v70, v75\n"
        "v_mfma_f32_16x16x32_f16 v[80:83], v[64:67], v[104:107], 0\nds_write_b128 %[wa], v[44:47] offset:512\ns_nop 7\nv_cvt_pk_f16_f32 v142, v76, v81\nv_cvt_pk_f16_f32 v143, v78, v83\ns_nop 1\n"
        "v_mfma_f32_16x16x32_f16 v[48:51], v[140:143], v[108:111], v[48:51]\ns_nop 7\nds_write_b128 %[wa], v[48:51] offset:768\ns_branch .LE%=\n.LC%=:\ns_sub_u32 %[cap], %[cap], 1\n"
        "s_cbranch_scc0 .LCy%=\ns_mov_b32 %[cap], 0\ns_branch .LD%=\n.LCy%=:\nbuffer_load_dwordx4 v[36:39], %[c16], %[xrs], 0 offen sc1\ns_waitcnt vmcnt(0)\ns_branch .LB%=\n.LI%=:\n"
        "s_sub_u32 %[cap], %[cap], 1\ns_cbranch_scc0 .LIy%=\ns_mov_b32 %[cap], 0\ns_branch .LM%=\n.LIy%=:\ns_sleep %[fp]\nbuffer_load_dwordx4 v[40:43], %[c16], %[frs], 0 offen sc1\n"
        "buffer_load_dwordx4 v[44:47], %[c16], %[frs], %[s1] offen sc1\ns_branch .LF%=\n.LE%=:\ns_waitcnt vmcnt(0)\ns_mov_b64 exec, 1\nds_add_u32 %[cna], %[one] offset:8\ns_mov_b64 exec, -1\n"
        "s_setprio %[pbe]\ns_mov_b32 %[s1], 0x100000\n.LP%=:\nds_read_b32 %[pv], %[cna] offset:8\ns_waitcnt lgkmcnt(0)\nv_readfirstlane_b32 %[s0], %[pv]\ns_nop 0\ns_cmp_ge_i32 %[s0], 4\n"
        "s_cbranch_scc1 .LR%=\ns_sub_u32 %[s1], %[s1], 1\ns_cbranch_scc0 .LP%=\n.LR%=:\ns_cmp_lt_u32 %[wq], 2\ns_cbranch_scc1 .LS3_%=\nds_read_b128 v[36:39], %[exr] offset:0\n"
        "ds_read_b128 v[40:43], %[exr] offset:4096\nds_read_b128 v[44:47], %[mr] offset:0\nds_read_b128 v[48:51], %[mr] offset:3072\nds_read_b128 v[52:55], %[frr] offset:0\n"
        "ds_read_b128 v[56:59], %[frr] offset:2304\ns_waitcnt lgkmcnt(4)\nv_fmac_f32 v26, v36, %[kf]\nv_fmac_f32 v27, v37, %[kf]\nv_fmac_f32 v28, v38, %[kf]\nv_fmac_f32 v29, v39, %[kf]\n"
        "v_fmac_f32 v30, v40, %[kf]\nv_fmac_f32 v31, v41, %[kf]\nv_fmac_f32 v32, v42, %[kf]\nv_fmac_f32 v33, v43, %[kf]\ns_waitcnt lgkmcnt(2)\nv_fmac_f32 v26, v44, %[kf]\nv_fmac_f32 v27, v45, %[kf]\n"
        "v_fmac_f32 v28, v46, %[kf]\nv_fmac_f32 v29, v47, %[kf]\nv_fmac_f32 v30, v48, %[kf]\nv_fmac_f32 v31, v49, %[kf]\nv_fmac_f32 v32, v50, %[kf]\nv_fmac_f32 v33, v51, %[kf]\ns_waitcnt lgkmcnt(0)\n"
        "v_fmac_f32 v26, v52, %[kf]\nv_fmac_f32 v27, v53, %[kf]\nv_fmac_f32 v28, v54, %[kf]\nv_fmac_f32 v29, v55, %[kf]\nv_fmac_f32 v30, v56, %[kf]\nv_fmac_f32 v31, v57, %[kf]\n"
        "v_fmac_f32 v32, v58, %[kf]\nv_fmac_f32 v33, v59, %[kf]\ns_branch .LS%=\n.LS3_%=:\ns_cmp_lt_u32 %[wq], 1\ns_cbranch_scc1 .LS2_%=\nds_read_b128 v[36:39], %[exr] offset:0\n"
        "ds_read_b128 v[40:43], %[exr] offset:4096\nds_read_b128 v[44:47], %[mr] offset:0\nds_read_b128 v[48:51], %[mr] offset:3072\ns_waitcnt lgkmcnt(2)\nv_fmac_f32 v26, v36, %[kf]\n"
        "v_fmac_f32 v27, v37, %[kf]\nv_fmac_f32 v28, v38, %[kf]\nv_fmac_f32 v29, v39, %[kf]\nv_fmac_f32 v30, v40, %[kf]\nv_fmac_f32 v31, v41, %[kf]\nv_fmac_f32 v32, v42, %[kf]\n"
        "v_fmac_f32 v33, v43, %[kf]\ns_waitcnt lgkmcnt(0)\nv_fmac_f32 v26, v44, %[kf]\nv_fmac_f32 v27, v45, %[kf]\nv_fmac_f32 v28, v46, %[kf]\nv_fmac_f32 v29, v47, %[kf]\nv_fmac_f32 v30, v48, %[kf]\n"
        "v_fmac_f32 v31, v49, %[kf]\nv_fmac_f32 v32, v50, %[kf]\nv_fmac_f32 v33, v51, %[kf]\ns_branch .LS%=\n.LS2_%=:\nds_read_b128 v[36:39], %[exr] offset:0\n"
        "ds_read_b128 v[40:43], %[exr] offset:4096\ns_waitcnt lgkmcnt(0)\nv_fmac_f32 v26, v36, %[kf]\nv_fmac_f32 v27, v37, %[kf]\nv_fmac_f32 v28, v38, %[kf]\nv_fmac_f32 v29, v39, %[kf]\n"
        "v_fmac_f32 v30, v40, %[kf]\nv_fmac_f32 v31, v41, %[kf]\nv_fmac_f32 v32, v42, %[kf]\nv_fmac_f32 v33, v43, %[kf]\n.LS%=:\nv_cvt_pk_bf16_f32 v36, v26, v27\nv_cvt_pk_bf16_f32 v37, v28, v29\n"
        "v_cvt_pk_bf16_f32 v38, v30, v31\nv_cvt_pk_bf16_f32 v39, v32, v33\nv_min_u32 v36, -2, v36\nv_min_u32 v37, -2, v37\nv_min_u32 v38, -2, v38\nv_min_u32 v39, -2, v39\n"
        "buffer_store_dwordx4 v[36:39], %[od], %[ors], 0 offen sc0 sc1\nbuffer_store_dwordx4 v[36:39], %[oh], %[ors], 0 offen sc0 sc1\ns_nop 1\n.LZ%=:\n"
        : "+{v[100:103]}"(kt[0]), "+{v[104:107]}"(kt[1]), "+{v[108:111]}"(kt[2]), "+{v[112:115]}"(kt[3]), "+{v[116:119]}"(kt[4]), "+{v[120:123]}"(kt[5]), "+{v[124:127]}"(kt[6]), "+{v[128:131]}"(kt[7]), "+{v[132:135]}"(kt[8]), "+{v[136:139]}"(kt[9]), "+{v[140:143]}"(kt[10]), "+{v[144:147]}"(kt[11]), "+{v[148:151]}"(kt[12]), "+{v[152:155]}"(kt[13]), "+{v[156:159]}"(kt[14]), "+{v[160:163]}"(kt[15]), "+{v[164:167]}"(kt[16]), [pv] "=&{v16}"(pv), [s0] "=&s"(s0_), [s1] "=&s"(s1_), [cap] "=&s"(cap)
        : [ra] "{v1}"(ra), [rb] "{v2}"(rb), [wa] "{v3}"(wa), [cna] "{v4}"(cna), [one] "{v5}"(one), [exr] "{v6}"(exr), [frr] "{v7}"(frr), [mr] "{v8}"(mr_), [od] "{v9}"(od), [kf] "{v10}"(kd[0]), [kd1] "{v11}"(kd[1]), [c16] "{v12}"(c16), [uw] "{v17}"(uw_), [sy] "{v23}"(sya), [ofs] "i"(SY_FS), [oh] "{v18}"(oh), [mo] "{v19}"(mo), [mrs] "s"(mrs), [gq] "s"(gq), [pbl] "i"(PBL), [v] "s"(v), [wq] "s"(wq), [ors] "s"(ors), [xrs] "s"(xrs), [frs] "s"(frs), [pbe] "i"(PBE), [fsx] "i"(FSX), [fp] "i"(FPOLL)
        : "memory", "scc", "v13", "v14", "v15", "v26", "v27", "v28", "v29", "v30", "v31", "v32", "v33", "v36", "v37", "v38", "v39", "v40", "v41", "v42", "v43", "v44", "v45", "v46", "v47", "v48", "v49", "v50", "v51", "v52", "v53", "v54", "v55", "v56", "v57", "v58", "v59", "v60", "v61", "v62", "v63", "v64", "v65", "v66", "v67", "v68", "v69", "v70", "v71", "v72", "v73", "v74", "v75", "v76", "v77", "v78", "v79", "v80", "v81", "v82", "v83", "v84", "v85", "v86", "v87", "v88", "v89", "v90", "v91", "v92", "v93", "v94", "v95", "v96", "v97", "v98", "v99", "vcc");
    } else if (layer) {
      asm volatile(
        "s_mov_b32 %[cap], 0x100000\ns_movk_i32 %[s1], 4096\ns_sleep %[fsx]\nbuffer_load_dwordx4 v[36:39], %[c16], %[xrs], 0 offen sc1\ns_cmp_eq_u32 %[wq], 3\ns_cbranch_scc0 .LA%=\ns_sleep %[fsa]\n"
        "s_mov_b64 exec, 1\nds_add_u32 %[sy], %[one] offset:%[ofs]\ns_mov_b64 exec, -1\ns_sleep 1\ns_branch .LX%=\n.LA%=:\nds_read_b32 %[pv], %[sy] offset:%[ofs]\ns_waitcnt lgkmcnt(0)\n"
        "v_readfirstlane_b32 %[s0], %[pv]\ns_nop 0\ns_cmp_ge_i32 %[s0], 2\ns_cbranch_scc1 .LX%=\ns_sub_u32 %[cap], %[cap], 1\ns_cbranch_scc0 .LA%=\n.LX%=:\ns_mov_b32 %[cap], 0x10000\n"
        "buffer_load_dwordx4 v[40:43], %[c16], %[frs], 0 offen sc1\nbuffer_load_dwordx4 v[44:47], %[c16], %[frs], %[s1] offen sc1\ns_waitcnt vmcnt(2)\n.LB%=:\nv_max3_u32 %[pv], v36, v37, v38\n"
        "v_max_u32 %[pv], %[pv], v39\nv_cmp_eq_u32 vcc, -1, %[pv]\ns_cbranch_vccnz .LC%=\n.LD%=:\nv_lshlrev_b32 v26, 16, v36\nv_and_b32 v27, 0xffff0000, v36\nv_lshlrev_b32 v28, 16, v37\n"
        "v_and_b32 v29, 0xffff0000, v37\nv_lshlrev_b32 v30, 16, v38\nv_and_b32 v31, 0xffff0000, v38\nv_lshlrev_b32 v32, 16, v39\nv_and_b32 v33, 0xffff0000, v39\n.LF%=:\ns_waitcnt vmcnt(1)\n"
        "v_max3_u32 %[pv], v40, v41, v42\nv_lshlrev_b32 v48, 16, v40\nv_lshlrev_b32 v50, 16, v41\nv_lshlrev_b32 v52, 16, v42\nv_lshlrev_b32 v54, 16, v43\nv_fmac_f32 v48, v26, v40\n"
        "v_fmac_f32 v50, v28, v41\nv_fmac_f32 v52, v30, v42\nv_fmac_f32 v54, v32, v43\ns_waitcnt vmcnt(0)\nv_max3_u32 %[pv], %[pv], v43, v44\nv_max3_u32 %[pv], %[pv], v45, v46\n"
        "v_max_u32 %[pv], %[pv], v47\nv_cmp_eq_u32 vcc, -1, %[pv]\nv_lshlrev_b32 v49, 16, v44\nv_lshlrev_b32 v51, 16, v45\nv_lshlrev_b32 v53, 16, v46\nv_lshlrev_b32 v55, 16, v47\n"
        "s_cbranch_vccnz .LI%=\n.LM%=:\nv_fmac_f32 v49, v27, v44\nv_fmac_f32 v51, v29, v45\nv_fmac_f32 v53, v31, v46\nv_fmac_f32 v55, v33, v47\nv_cvt_pk_f16_f32 v36, v48, v49\n"
        "v_cvt_pk_f16_f32 v37, v50, v51\nv_cvt_pk_f16_f32 v38, v52, v53\nv_cvt_pk_f16_f32 v39, v54, v55\nds_write_b128 %[uw], v[36:39]\ns_mov_b64 exec, 1\nds_add_u32 %[cna], %[one] offset:0\n"
        "s_mov_b64 exec, -1\ns_setprio %[pbl]\ns_mov_b32 %[s1], 0x100000\n.LN%=:\nds_read_b32 %[pv], %[cna]\ns_waitcnt lgkmcnt(0)\nv_readfirstlane_b32 %[s0], %[pv]\ns_nop 0\ns_cmp_ge_i32 %[s0], 4\n"
        "s_cbranch_scc1 .LO%=\ns_sub_u32 %[s1], %[s1], 1\ns_cbranch_scc0 .LN%=\n.LO%=:\nbuffer_load_dwordx4 v[152:155], %[xo], %[xrs], 0 offen sc1\nds_read_b128 v[156:159], %[ue]\n"
        "s_cmp_lt_u32 %[v], 2\ns_cbranch_scc0 .LT%=\nds_read_b128 v[52:55], %[ra] offset:800\nds_read_b128 v[56:59], %[ra] offset:736\nds_read_b128 v[60:63], %[ra] offset:672\n"
        "ds_read_b128 v[64:67], %[rb] offset:576\nds_read_b128 v[68:71], %[ra] offset:512\nds_read_b128 v[72:75], %[ra] offset:448\nds_read_b128 v[76:79], %[ra] offset:384\n"
        "ds_read_b128 v[80:83], %[rb] offset:288\nv_xor_b32 v14, 32, %[wa]\nv_xor_b32 v15, 64, %[wa]\nv_xor_b32 v13, 96, %[wa]\ns_waitcnt lgkmcnt(7)\n"
        "v_mfma_f32_16x16x32_f16 v[48:51], v[100:103], v[52:55], 0\nds_read_b128 v[52:55], %[ra] offset:224\ns_waitcnt lgkmcnt(7)\nv_mfma_f32_16x16x32_f16 v[44:47], v[100:103], v[56:59], 0\n"
        "s_waitcnt lgkmcnt(6)\nv_mfma_f32_16x16x32_f16 v[40:43], v[100:103], v[60:63], 0\ns_waitcnt lgkmcnt(5)\nv_mfma_f32_16x16x32_f16 v[36:39], v[100:103], v[64:67], 0\n"
        "v_mfma_f32_16x16x32_f16 v[48:51], v[104:107], v[56:59], v[48:51]\nds_read_b128 v[56:59], %[ra] offset:160\nv_mfma_f32_16x16x32_f16 v[44:47], v[104:107], v[60:63], v[44:47]\n"
        "v_mfma_f32_16x16x32_f16 v[40:43], v[104:107], v[64:67], v[40:43]\ns_waitcnt lgkmcnt(5)\nv_mfma_f32_16x16x32_f16 v[36:39], v[104:107], v[68:71], v[36:39]\n"
        "v_mfma_f32_16x16x32_f16 v[48:51], v[108:111], v[60:63], v[48:51]\nds_read_b128 v[60:63], %[ra] offset:96\nv_mfma_f32_16x16x32_f16 v[44:47], v[108:111], v[64:67], v[44:47]\n"
        "v_mfma_f32_16x16x32_f16 v[40:43], v[108:111], v[68:71], v[40:43]\ns_waitcnt lgkmcnt(5)\nv_mfma_f32_16x16x32_f16 v[36:39], v[108:111], v[72:75], v[36:39]\n"
        "v_mfma_f32_16x16x32_f16 v[48:51], v[112:115], v[64:67], v[48:51]\nds_read_b128 v[64:67], %[rb] offset:0\nv_mfma_f32_16x16x32_f16 v[44:47], v[112:115], v[68:71], v[44:47]\n"
        "v_mfma_f32_16x16x32_f16 v[40:43], v[112:115], v[72:75], v[40:43]\ns_waitcnt lgkmcnt(5)\nv_mfma_f32_16x16x32_f16 v[36:39], v[112:115], v[76:79], v[36:39]\n"
        "v_mfma_f32_16x16x32_f16 v[48:51], v[116:119], v[68:71], v[48:51]\nv_mfma_f32_16x16x32_f16 v[44:47], v[116:119], v[72:75], v[44:47]\n"
        "v_mfma_f32_16x16x32_f16 v[40:43], v[116:119], v[76:79], v[40:43]\ns_waitcnt lgkmcnt(4)\nv_mfma_f32_16x16x32_f16 v[36:39], v[116:119], v[80:83], v[36:39]\n"
        "v_mfma_f32_16x16x32_f16 v[48:51], v[120:123], v[72:75], v[48:51]\nv_mfma_f32_16x16x32_f16 v[44:47], v[120:123], v[76:79], v[44:47]\n"
        "v_mfma_f32_16x16x32_f16 v[40:43], v[120:123], v[80:83], v[40:43]\ns_waitcnt lgkmcnt(3)\nv_mfma_f32_16x16x32_f16 v[36:39], v[120:123], v[52:55], v[36:39]\n"
        "v_mfma_f32_16x16x32_f16 v[48:51], v[124:127], v[76:79], v[48:51]\nv_mfma_f32_16x16x32_f16 v[44:47], v[124:127], v[80:83], v[44:47]\n"
        "v_mfma_f32_16x16x32_f16 v[40:43], v[124:127], v[52:55], v[40:43]\ns_waitcnt lgkmcnt(2)\nv_mfma_f32_16x16x32_f16 v[36:39], v[124:127], v[56:59], v[36:39]\n"
        "v_mfma_f32_16x16x32_f16 v[48:51], v[128:131], v[80:83], v[48:51]\nv_mfma_f32_16x16x32_f16 v[44:47], v[128:131], v[52:55], v[44:47]\n"
        "v_mfma_f32_16x16x32_f16 v[40:43], v[128:131], v[56:59], v[40:43]\ns_waitcnt lgkmcnt(1)\nv_mfma_f32_16x16x32_f16 v[36:39], v[128:131], v[60:63], v[36:39]\ns_cmp_eq_u32 %[v], 0\n"
        "s_cbranch_scc1 .LU%=\nv_mfma_f32_16x16x32_f16 v[48:51], v[132:135], v[52:55], v[48:51]\nv_mfma_f32_16x16x32_f16 v[44:47], v[132:135], v[56:59], v[44:47]\n"
        "v_mfma_f32_16x16x32_f16 v[40:43], v[132:135], v[60:63], v[40:43]\ns_waitcnt lgkmcnt(0)\nv_mfma_f32_16x16x32_f16 v[36:39], v[132:135], v[64:67], v[36:39]\n.LU%=:\ns_nop 4\n"
        "ds_write_b128 v13, v[48:51]\nds_write_b128 v15, v[44:47]\nds_write_b128 v14, v[40:43]\nds_write_b128 %[wa], v[36:39]\ns_branch .LE%=\n.LT%=:\ns_cmp_eq_u32 %[v], 2\ns_cbranch_scc0 .LV%=\n"
        "ds_read_b64 v[60:61], %[rb] offset:0\nds_read_b128 v[62:65], %[ra] offset:0\nds_read_b64 v[66:67], %[rb] offset:288\nds_read_b128 v[68:71], %[ra] offset:288\n"
        "ds_read_b64 v[72:73], %[rb] offset:576\nds_read_b128 v[74:77], %[ra] offset:576\nds_read_b64 v[78:79], %[rb] offset:864\nds_read_b128 v[80:83], %[ra] offset:864\ns_waitcnt lgkmcnt(6)\n"
        "v_mfma_f32_16x16x32_f16 v[84:87], v[60:63], v[100:103], 0\nv_mfma_f32_16x16x32_f16 v[88:91], v[62:65], v[104:107], 0\nds_read_b64 v[60:61], %[rb] offset:1152\n"
        "ds_read_b128 v[62:65], %[ra] offset:1152\ns_waitcnt lgkmcnt(6)\nv_mfma_f32_16x16x32_f16 v[92:95], v[66:69], v[100:103], 0\nv_mfma_f32_16x16x32_f16 v[96:99], v[68:71], v[104:107], 0\n"
        "ds_read_b64 v[66:67], %[rb] offset:1440\nds_read_b128 v[68:71], %[ra] offset:1440\ns_nop 7\nv_cvt_pk_f16_f32 v136, v84, v85\nv_cvt_pk_f16_f32 v137, v86, v87\nv_cvt_pk_f16_f32 v138, v88, v89\n"
        "v_cvt_pk_f16_f32 v139, v90, v91\ns_nop 1\nv_mfma_f32_16x16x32_f16 v[36:39], v[136:139], v[108:111], 0\ns_waitcnt lgkmcnt(6)\nv_mfma_f32_16x16x32_f16 v[84:87], v[72:75], v[100:103], 0\n"
        "v_mfma_f32_16x16x32_f16 v[88:91], v[74:77], v[104:107], 0\nds_read_b64 v[72:73], %[rb] offset:1728\nds_read_b128 v[74:77], %[ra] offset:1728\nv_cvt_pk_f16_f32 v140, v92, v93\n"
        "v_cvt_pk_f16_f32 v141, v94, v95\nv_cvt_pk_f16_f32 v142, v96, v97\nv_cvt_pk_f16_f32 v143, v98, v99\ns_nop 1\nv_mfma_f32_16x16x32_f16 v[36:39], v[140:143], v[112:115], v[36:39]\n"
        "s_waitcnt lgkmcnt(6)\nv_mfma_f32_16x16x32_f16 v[92:95], v[78:81], v[100:103], 0\nv_mfma_f32_16x16x32_f16 v[96:99], v[80:83], v[104:107], 0\nds_read_b64 v[78:79], %[rb] offset:2016\n"
        "ds_read_b128 v[80:83], %[ra] offset:2016\nv_cvt_pk_f16_f32 v136, v84, v85\nv_cvt_pk_f16_f32 v137, v86, v87\nv_cvt_pk_f16_f32 v138, v88, v89\nv_cvt_pk_f16_f32 v139, v90, v91\ns_nop 1\n"
        "v_mfma_f32_16x16x32_f16 v[36:39], v[136:139], v[116:119], v[36:39]\nv_mfma_f32_16x16x32_f16 v[40:43], v[136:139], v[108:111], 0\ns_waitcnt lgkmcnt(6)\n"
        "v_mfma_f32_16x16x32_f16 v[84:87], v[60:63], v[100:103], 0\nv_cvt_pk_f16_f32 v140, v92, v93\nv_cvt_pk_f16_f32 v141, v94, v95\nv_mfma_f32_16x16x32_f16 v[88:91], v[62:65], v[104:107], 0\n"
        "v_cvt_pk_f16_f32 v142, v96, v97\nv_cvt_pk_f16_f32 v143, v98, v99\ns_nop 1\nv_mfma_f32_16x16x32_f16 v[36:39], v[140:143], v[120:123], v[36:39]\nds_read_b64 v[60:61], %[rb] offset:2304\n"
        "ds_read_b128 v[62:65], %[ra] offset:2304\nv_mfma_f32_16x16x32_f16 v[40:43], v[140:143], v[112:115], v[40:43]\ns_waitcnt lgkmcnt(6)\nv_mfma_f32_16x16x32_f16 v[92:95], v[66:69], v[100:103], 0\n"
        "v_cvt_pk_f16_f32 v136, v84, v85\nv_cvt_pk_f16_f32 v137, v86, v87\nv_mfma_f32_16x16x32_f16 v[96:99], v[68:71], v[104:107], 0\nv_cvt_pk_f16_f32 v138, v88, v89\nv_cvt_pk_f16_f32 v139, v90, v91\n"
        "s_nop 1\nv_mfma_f32_16x16x32_f16 v[36:39], v[136:139], v[124:127], v[36:39]\nds_read_b64 v[66:67], %[rb] offset:2592\nds_read_b128 v[68:71], %[ra] offset:2592\n"
        "v_mfma_f32_16x16x32_f16 v[40:43], v[136:139], v[116:119], v[40:43]\nv_mfma_f32_16x16x32_f16 v[44:47], v[136:139], v[108:111], 0\nv_cvt_pk_f16_f32 v140, v92, v93\n"
        "v_cvt_pk_f16_f32 v141, v94, v95\ns_waitcnt lgkmcnt(6)\nv_mfma_f32_16x16x32_f16 v[84:87], v[72:75], v[100:103], 0\nv_cvt_pk_f16_f32 v142, v96, v97\nv_cvt_pk_f16_f32 v143, v98, v99\n"
        "v_mfma_f32_16x16x32_f16 v[88:91], v[74:77], v[104:107], 0\ns_nop 0\nv_mfma_f32_16x16x32_f16 v[36:39], v[140:143], v[128:131], v[36:39]\n"
        "v_mfma_f32_16x16x32_f16 v[40:43], v[140:143], v[120:123], v[40:43]\nv_mfma_f32_16x16x32_f16 v[44:47], v[140:143], v[112:115], v[44:47]\nv_cvt_pk_f16_f32 v136, v84, v85\n"
        "v_cvt_pk_f16_f32 v137, v86, v87\ns_waitcnt lgkmcnt(4)\nv_mfma_f32_16x16x32_f16 v[92:95], v[78:81], v[100:103], 0\nv_cvt_pk_f16_f32 v138, v88, v89\nv_cvt_pk_f16_f32 v139, v90, v91\n"
        "v_mfma_f32_16x16x32_f16 v[96:99], v[80:83], v[104:107], 0\nds_write_b128 %[wa], v[36:39] offset:0\nv_mfma_f32_16x16x32_f16 v[40:43], v[136:139], v[124:127], v[40:43]\n"
        "v_mfma_f32_16x16x32_f16 v[44:47], v[136:139], v[116:119], v[44:47]\ns_waitcnt lgkmcnt(3)\nv_mfma_f32_16x16x32_f16 v[84:87], v[60:63], v[100:103], 0\nv_cvt_pk_f16_f32 v140, v92, v93\n"
        "v_cvt_pk_f16_f32 v141, v94, v95\nv_mfma_f32_16x16x32_f16 v[88:91], v[62:65], v[104:107], 0\nv_cvt_pk_f16_f32 v142, v96, v97\nv_cvt_pk_f16_f32 v143, v98, v99\ns_nop 1\n"
        "v_mfma_f32_16x16x32_f16 v[40:43], v[140:143], v[128:131], v[40:43]\nv_mfma_f32_16x16x32_f16 v[44:47], v[140:143], v[120:123], v[44:47]\ns_waitcnt lgkmcnt(1)\n"
        "v_mfma_f32_16x16x32_f16 v[92:95], v[66:69], v[100:103], 0\nv_cvt_pk_f16_f32 v136, v84, v85\nv_cvt_pk_f16_f32 v137, v86, v87\nv_mfma_f32_16x16x32_f16 v[96:99], v[68:71], v[104:107], 0\n"
        "v_cvt_pk_f16_f32 v138, v88, v89\nv_cvt_pk_f16_f32 v139, v90, v91\ns_nop 1\nv_mfma_f32_16x16x32_f16 v[44:47], v[136:139], v[124:127], v[44:47]\nds_write_b128 %[wa], v[40:43] offset:256\n"
        "s_nop 7\nv_cvt_pk_f16_f32 v140, v92, v93\nv_cvt_pk_f16_f32 v141, v94, v95\nv_cvt_pk_f16_f32 v142, v96, v97\nv_cvt_pk_f16_f32 v143, v98, v99\ns_nop 1\n"
        "v_mfma_f32_16x16x32_f16 v[44:47], v[140:143], v[128:131], v[44:47]\ns_nop 7\nds_write_b128 %[wa], v[44:47] offset:512\ns_branch .LE%=\n.LV%=:\nds_read_b128 v[52:55], %[ra] offset:0\n"
        "ds_read_b128 v[56:59], %[ra] offset:288\nds_read_b128 v[60:63], %[ra] offset:576\nds_read_b128 v[64:67], %[ra] offset:864\nds_read_b128 v[84:87], %[rb] offset:0\n"
        "ds_read_b128 v[88:91], %[rb] offset:1152\ns_waitcnt lgkmcnt(5)\nv_mfma_f32_16x16x32_f16 v[68:71], v[52:55], v[100:103], 0\nv_mfma_f32_16x16x32_f16 v[72:75], v[52:55], v[104:107], 0\n"
        "ds_read_b128 v[52:55], %[ra] offset:1152\ns_waitcnt lgkmcnt(5)\nv_mfma_f32_16x16x32_f16 v[76:79], v[56:59], v[100:103], 0\nv_mfma_f32_16x16x32_f16 v[80:83], v[56:59], v[104:107], 0\n"
        "ds_read_b128 v[56:59], %[ra] offset:1440\ns_waitcnt lgkmcnt(3)\nv_mfma_f32_16x16x32_f16 v[36:39], v[124:127], v[84:87], 0\nv_cvt_pk_f16_f32 v136, v68, v73\nv_cvt_pk_f16_f32 v137, v70, v75\n"
        "s_waitcnt lgkmcnt(2)\nv_mfma_f32_16x16x32_f16 v[40:43], v[124:127], v[88:91], 0\nv_mfma_f32_16x16x32_f16 v[68:71], v[60:63], v[100:103], 0\nv_cvt_pk_f16_f32 v138, v76, v81\n"
        "v_cvt_pk_f16_f32 v139, v78, v83\nv_mfma_f32_16x16x32_f16 v[72:75], v[60:63], v[104:107], 0\nds_read_b128 v[60:63], %[ra] offset:1728\n"
        "v_mfma_f32_16x16x32_f16 v[36:39], v[136:139], v[112:115], v[36:39]\nv_mfma_f32_16x16x32_f16 v[40:43], v[136:139], v[120:123], v[40:43]\n"
        "v_mfma_f32_16x16x32_f16 v[76:79], v[64:67], v[100:103], 0\nv_cvt_pk_f16_f32 v140, v68, v73\nv_cvt_pk_f16_f32 v141, v70, v75\nv_mfma_f32_16x16x32_f16 v[80:83], v[64:67], v[104:107], 0\n"
        "ds_read_b128 v[64:67], %[ra] offset:2016\ns_waitcnt lgkmcnt(3)\nv_mfma_f32_16x16x32_f16 v[68:71], v[52:55], v[100:103], 0\nv_mfma_f32_16x16x32_f16 v[72:75], v[52:55], v[104:107], 0\ns_nop 7\n"
        "v_cvt_pk_f16_f32 v142, v76, v81\nv_cvt_pk_f16_f32 v143, v78, v83\ns_nop 1\nv_mfma_f32_16x16x32_f16 v[36:39], v[140:143], v[108:111], v[36:39]\n"
        "v_mfma_f32_16x16x32_f16 v[40:43], v[140:143], v[116:119], v[40:43]\ns_waitcnt lgkmcnt(2)\nv_mfma_f32_16x16x32_f16 v[76:79], v[56:59], v[100:103], 0\nv_cvt_pk_f16_f32 v136, v68, v73\n"
        "v_cvt_pk_f16_f32 v137, v70, v75\nv_mfma_f32_16x16x32_f16 v[80:83], v[56:59], v[104:107], 0\nds_write_b128 %[wa], v[36:39] offset:0\ns_waitcnt lgkmcnt(2)\n"
        "v_mfma_f32_16x16x32_f16 v[68:71], v[60:63], v[100:103], 0\nv_mfma_f32_16x16x32_f16 v[72:75], v[60:63], v[104:107], 0\ns_nop 7\nv_cvt_pk_f16_f32 v138, v76, v81\n"
        "v_cvt_pk_f16_f32 v139, v78, v83\ns_nop 1\nv_mfma_f32_16x16x32_f16 v[40:43], v[136:139], v[112:115], v[40:43]\ns_waitcnt lgkmcnt(1)\nv_mfma_f32_16x16x32_f16 v[76:79], v[64:67], v[100:103], 0\n"
        "v_mfma_f32_16x16x32_f16 v[80:83], v[64:67], v[104:107], 0\nv_cvt_pk_f16_f32 v140, v68, v73\nv_cvt_pk_f16_f32 v141, v70, v75\ns_nop 7\nv_cvt_pk_f16_f32 v142, v76, v81\n"
        "v_cvt_pk_f16_f32 v143, v78, v83\ns_nop 1\nv_mfma_f32_16x16x32_f16 v[40:43], v[140:143], v[108:111], v[40:43]\ns_nop 7\nds_write_b128 %[wa], v[40:43] offset:256\ns_branch .LE%=\n.LC%=:\n"
        "s_sub_u32 %[cap], %[cap], 1\ns_cbranch_scc0 .LCy%=\ns_mov_b32 %[cap], 0\ns_branch .LD%=\n.LCy%=:\nbuffer_load_dwordx4 v[36:39], %[c16], %[xrs], 0 offen sc1\ns_waitcnt vmcnt(0)\n"
        "s_branch .LB%=\n.LI%=:\ns_sub_u32 %[cap], %[cap], 1\ns_cbranch_scc0 .LIy%=\ns_mov_b32 %[cap], 0\ns_branch .LM%=\n.LIy%=:\ns_sleep %[fp]\n"
        "buffer_load_dwordx4 v[40:43], %[c16], %[frs], 0 offen sc1\nbuffer_load_dwordx4 v[44:47], %[c16], %[frs], %[s1] offen sc1\ns_branch .LF%=\n.LE%=:\ns_waitcnt vmcnt(0)\ns_mov_b64 exec, 1\n"
        "ds_add_u32 %[cna], %[one] offset:8\ns_mov_b64 exec, -1\ns_setprio %[pbe]\ns_cmp_lt_u32 %[wq], 2\ns_cbranch_scc0 .LZ%=\nv_lshlrev_b32 v26, 16, v152\nv_and_b32 v27, 0xffff0000, v152\n"
        "v_cvt_f32_f16 v152, v156\nv_lshrrev_b32 v156, 16, v156\nv_lshlrev_b32 v28, 16, v153\nv_and_b32 v29, 0xffff0000, v153\nv_cvt_f32_f16 v153, v157\nv_lshrrev_b32 v157, 16, v157\n"
        "v_lshlrev_b32 v30, 16, v154\nv_and_b32 v31, 0xffff0000, v154\nv_cvt_f32_f16 v154, v158\nv_lshrrev_b32 v158, 16, v158\nv_lshlrev_b32 v32, 16, v155\nv_and_b32 v33, 0xffff0000, v155\n"
        "v_cvt_f32_f16 v155, v159\nv_lshrrev_b32 v159, 16, v159\nv_cvt_f32_f16 v156, v156\nv_fmac_f32 v26, v152, %[kd1]\nv_cvt_f32_f16 v157, v157\nv_fmac_f32 v28, v153, %[kd1]\n"
        "v_cvt_f32_f16 v158, v158\nv_fmac_f32 v30, v154, %[kd1]\nv_cvt_f32_f16 v159, v159\nv_fmac_f32 v32, v155, %[kd1]\nv_fmac_f32 v27, v156, %[kd1]\nv_fmac_f32 v29, v157, %[kd1]\n"
        "v_fmac_f32 v31, v158, %[kd1]\nv_fmac_f32 v33, v159, %[kd1]\ns_mov_b32 %[s1], 0x100000\n.LP%=:\nds_read_b32 %[pv], %[cna] offset:8\ns_waitcnt lgkmcnt(0)\nv_readfirstlane_b32 %[s0], %[pv]\n"
        "s_nop 0\ns_cmp_ge_i32 %[s0], 4\ns_cbranch_scc1 .LR%=\ns_sub_u32 %[s1], %[s1], 1\ns_cbranch_scc0 .LP%=\n.LR%=:\ns_cmp_lt_u32 %[wq], 1\ns_cbranch_scc1 .LS4_%=\n"
        "ds_read_b128 v[36:39], %[exr] offset:0\nds_read_b128 v[40:43], %[exr] offset:2048\nds_read_b128 v[44:47], %[exr] offset:4096\nds_read_b128 v[48:51], %[exr] offset:6144\n"
        "ds_read_b128 v[52:55], %[mr] offset:0\nds_read_b128 v[56:59], %[mr] offset:3072\nds_read_b128 v[60:63], %[frr] offset:0\nds_read_b128 v[64:67], %[frr] offset:1152\ns_waitcnt lgkmcnt(6)\n"
        "v_fmac_f32 v26, v36, %[kf]\nv_fmac_f32 v27, v37, %[kf]\nv_fmac_f32 v28, v38, %[kf]\nv_fmac_f32 v29, v39, %[kf]\nv_fmac_f32 v30, v40, %[kf]\nv_fmac_f32 v31, v41, %[kf]\n"
        "v_fmac_f32 v32, v42, %[kf]\nv_fmac_f32 v33, v43, %[kf]\ns_waitcnt lgkmcnt(4)\nv_fmac_f32 v26, v44, %[kf]\nv_fmac_f32 v27, v45, %[kf]\nv_fmac_f32 v28, v46, %[kf]\nv_fmac_f32 v29, v47, %[kf]\n"
        "v_fmac_f32 v30, v48, %[kf]\nv_fmac_f32 v31, v49, %[kf]\nv_fmac_f32 v32, v50, %[kf]\nv_fmac_f32 v33, v51, %[kf]\ns_waitcnt lgkmcnt(2)\nv_fmac_f32 v26, v52, %[kf4]\nv_fmac_f32 v27, v53, %[kf4]\n"
        "v_fmac_f32 v28, v54, %[kf4]\nv_fmac_f32 v29, v55, %[kf4]\nv_fmac_f32 v30, v56, %[kf4]\nv_fmac_f32 v31, v57, %[kf4]\nv_fmac_f32 v32, v58, %[kf4]\nv_fmac_f32 v33, v59, %[kf4]\n"
        "s_waitcnt lgkmcnt(0)\nv_fmac_f32 v26, v60, %[kf]\nv_fmac_f32 v27, v61, %[kf]\nv_fmac_f32 v28, v62, %[kf]\nv_fmac_f32 v29, v63, %[kf]\nv_fmac_f32 v30, v64, %[kf]\nv_fmac_f32 v31, v65, %[kf]\n"
        "v_fmac_f32 v32, v66, %[kf]\nv_fmac_f32 v33, v67, %[kf]\ns_branch .LS%=\n.LS4_%=:\nds_read_b128 v[36:39], %[exr] offset:0\nds_read_b128 v[40:43], %[exr] offset:2048\n"
        "ds_read_b128 v[44:47], %[exr] offset:4096\nds_read_b128 v[48:51], %[exr] offset:6144\nds_read_b128 v[52:55], %[mr] offset:0\nds_read_b128 v[56:59], %[mr] offset:3072\ns_waitcnt lgkmcnt(4)\n"
        "v_fmac_f32 v26, v36, %[kf]\nv_fmac_f32 v27, v37, %[kf]\nv_fmac_f32 v28, v38, %[kf]\nv_fmac_f32 v29, v39, %[kf]\nv_fmac_f32 v30, v40, %[kf]\nv_fmac_f32 v31, v41, %[kf]\n"
        "v_fmac_f32 v32, v42, %[kf]\nv_fmac_f32 v33, v43, %[kf]\ns_waitcnt lgkmcnt(2)\nv_fmac_f32 v26, v44, %[kf]\nv_fmac_f32 v27, v45, %[kf]\nv_fmac_f32 v28, v46, %[kf]\nv_fmac_f32 v29, v47, %[kf]\n"
        "v_fmac_f32 v30, v48, %[kf]\nv_fmac_f32 v31, v49, %[kf]\nv_fmac_f32 v32, v50, %[kf]\nv_fmac_f32 v33, v51, %[kf]\ns_waitcnt lgkmcnt(0)\nv_fmac_f32 v26, v52, %[kf4]\nv_fmac_f32 v27, v53, %[kf4]\n"
        "v_fmac_f32 v28, v54, %[kf4]\nv_fmac_f32 v29, v55, %[kf4]\nv_fmac_f32 v30, v56, %[kf4]\nv_fmac_f32 v31, v57, %[kf4]\nv_fmac_f32 v32, v58, %[kf4]\nv_fmac_f32 v33, v59, %[kf4]\n.LS%=:\n"
        "v_cvt_pk_bf16_f32 v36, v26, v27\nv_cvt_pk_bf16_f32 v37, v28, v29\nv_cvt_pk_bf16_f32 v38, v30, v31\nv_cvt_pk_bf16_f32 v39, v32, v33\nv_min_u32 v36, -2, v36\nv_min_u32 v37, -2, v37\n"
        "v_min_u32 v38, -2, v38\nv_min_u32 v39, -2, v39\nbuffer_store_dwordx4 v[36:39], %[od], %[ors], 0 offen sc0 sc1\ns_nop 1\n.LZ%=:\n"
        : "+{v[100:103]}"(kt[0]), "+{v[104:107]}"(kt[1]), "+{v[108:111]}"(kt[2]), "+{v[112:115]}"(kt[3]), "+{v[116:119]}"(kt[4]), "+{v[120:123]}"(kt[5]), "+{v[124:127]}"(kt[6]), "+{v[128:131]}"(kt[7]), "+{v[132:135]}"(kt[8]), [pv] "=&{v16}"(pv), [s0] "=&s"(s0_), [s1] "=&s"(s1_), [cap] "=&s"(cap)
        : [ra] "{v1}"(ra), [rb] "{v2}"(rb), [wa] "{v3}"(wa), [cna] "{v4}"(cna), [one] "{v5}"(one), [exr] "{v6}"(exr), [frr] "{v7}"(frr), [mr] "{v8}"(mr_), [od] "{v9}"(od), [kf] "{v10}"(kd[0]), [kd1] "{v11}"(kd[1]), [c16] "{v12}"(c16), [uw] "{v17}"(uw_), [sy] "{v23}"(sya), [ofs] "i"(SY_FS), [ue] "{v20}"(ue), [kf4] "{v21}"(kf4), [xo] "{v22}"(xo), [fsa] "i"((layer ? FSH : FSH0) - FSX), [pbl] "i"(PBH), [v] "s"(v), [wq] "s"(wq), [ors] "s"(ors), [xrs] "s"(xrs), [frs] "s"(frs), [pbe] "i"(PBE), [fsx] "i"(FSX), [fp] "i"(FPOLL)
        : "memory", "scc", "v13", "v14", "v15", "v26", "v27", "v28", "v29", "v30", "v31", "v32", "v33", "v36", "v37", "v38", "v39", "v40", "v41", "v42", "v43", "v44", "v45", "v46", "v47", "v48", "v49", "v50", "v51", "v52", "v53", "v54", "v55", "v56", "v57", "v58", "v59", "v60", "v61", "v62", "v63", "v64", "v65", "v66", "v67", "v68", "v69", "v70", "v71", "v72", "v73", "v74", "v75", "v76", "v77", "v78", "v79", "v80", "v81", "v82", "v83", "v84", "v85", "v86", "v87", "v88", "v89", "v90", "v91", "v92", "v93", "v94", "v95", "v96", "v97", "v98", "v99", "v136", "v137", "v138", "v139", "v140", "v141", "v142", "v143", "v144", "v145", "v146", "v147", "v148", "v149", "v150", "v151", "v152", "v153", "v154", "v155", "v156", "v157", "v158", "v159", "v160", "v161", "v162", "v163", "v164", "v165", "v166", "v167", "vcc");
    } else {
      asm volatile(
        "s_mov_b32 %[cap], 0x100000\ns_movk_i32 %[s1], 4096\nv_mov_b32 v56, -1\nv_mov_b32 v57, -1\nv_mov_b32 v58, -1\nv_mov_b32 v59, -1\ns_sleep %[fsx]\n"
        "buffer_load_dwordx4 v[36:39], %[c16], %[xrs], 0 offen sc1\ns_cmp_eq_u32 %[wq], 3\ns_cbranch_scc0 .LA%=\ns_sleep %[fsa]\ns_mov_b64 exec, 1\nds_add_u32 %[sy], %[one] offset:%[ofs]\n"
        "s_mov_b64 exec, -1\ns_sleep 1\ns_branch .LX%=\n.LA%=:\nds_read_b32 %[pv], %[sy] offset:%[ofs]\ns_waitcnt lgkmcnt(0)\nv_readfirstlane_b32 %[s0], %[pv]\ns_nop 0\ns_cmp_ge_i32 %[s0], 2\n"
        "s_cbranch_scc1 .LX%=\ns_sub_u32 %[cap], %[cap], 1\ns_cbranch_scc0 .LA%=\n.LX%=:\ns_mov_b32 %[cap], 0x10000\nbuffer_load_dwordx4 v[40:43], %[c16], %[frs], 0 offen sc1\n"
        "buffer_load_dwordx4 v[44:47], %[c16], %[frs], %[s1] offen sc1\ns_waitcnt vmcnt(2)\n.LB%=:\nv_max3_u32 %[pv], v36, v37, v38\nv_max_u32 %[pv], %[pv], v39\nv_cmp_eq_u32 vcc, -1, %[pv]\n"
        "s_cbranch_vccnz .LC%=\n.LD%=:\nv_lshlrev_b32 v26, 16, v36\nv_and_b32 v27, 0xffff0000, v36\nv_lshlrev_b32 v28, 16, v37\nv_and_b32 v29, 0xffff0000, v37\nv_lshlrev_b32 v30, 16, v38\n"
        "v_and_b32 v31, 0xffff0000, v38\nv_lshlrev_b32 v32, 16, v39\nv_and_b32 v33, 0xffff0000, v39\n.LF%=:\ns_waitcnt vmcnt(1)\nv_max3_u32 %[pv], v40, v41, v42\nv_lshlrev_b32 v48, 16, v40\n"
        "v_lshlrev_b32 v50, 16, v41\nv_lshlrev_b32 v52, 16, v42\nv_lshlrev_b32 v54, 16, v43\nv_fmac_f32 v48, v26, v40\nv_fmac_f32 v50, v28, v41\nv_fmac_f32 v52, v30, v42\nv_fmac_f32 v54, v32, v43\n"
        "s_waitcnt vmcnt(0)\nv_max3_u32 %[pv], %[pv], v43, v44\nv_max3_u32 %[pv], %[pv], v45, v46\nv_max_u32 %[pv], %[pv], v47\nv_cmp_eq_u32 vcc, -1, %[pv]\nv_lshlrev_b32 v49, 16, v44\n"
        "v_lshlrev_b32 v51, 16, v45\nv_lshlrev_b32 v53, 16, v46\nv_lshlrev_b32 v55, 16, v47\ns_cbranch_vccnz .LI%=\n.LM%=:\nv_fmac_f32 v49, v27, v44\nv_fmac_f32 v51, v29, v45\n"
        "v_fmac_f32 v53, v31, v46\nv_fmac_f32 v55, v33, v47\nv_cvt_pk_f16_f32 v36, v48, v49\nv_cvt_pk_f16_f32 v37, v50, v51\nv_cvt_pk_f16_f32 v38, v52, v53\nv_cvt_pk_f16_f32 v39, v54, v55\n"
        "ds_write_b128 %[uw], v[36:39]\ns_mov_b64 exec, 1\nds_add_u32 %[cna], %[one] offset:0\ns_mov_b64 exec, -1\ns_setprio %[pbl]\ns_mov_b32 %[s1], 0x100000\n.LN%=:\nds_read_b32 %[pv], %[cna]\n"
        "s_waitcnt lgkmcnt(0)\nv_readfirstlane_b32 %[s0], %[pv]\ns_nop 0\ns_cmp_ge_i32 %[s0], 4\ns_cbranch_scc1 .LO%=\ns_sub_u32 %[s1], %[s1], 1\ns_cbranch_scc0 .LN%=\n.LO%=:\n"
        "buffer_load_dwordx4 v[152:155], %[xo], %[xrs], 0 offen sc1\nds_read_b128 v[156:159], %[ue]\ns_cmp_lt_u32 %[v], 2\ns_cbranch_scc0 .LT%=\nds_read_b128 v[52:55], %[ra] offset:800\n"
        "ds_read_b128 v[56:59], %[ra] offset:736\nds_read_b128 v[60:63], %[ra] offset:672\nds_read_b128 v[64:67], %[rb] offset:576\nds_read_b128 v[68:71], %[ra] offset:512\n"
        "ds_read_b128 v[72:75], %[ra] offset:448\nds_read_b128 v[76:79], %[ra] offset:384\nds_read_b128 v[80:83], %[rb] offset:288\nv_xor_b32 v14, 32, %[wa]\nv_xor_b32 v15, 64, %[wa]\n"
        "v_xor_b32 v13, 96, %[wa]\ns_waitcnt lgkmcnt(7)\nv_mfma_f32_16x16x32_f16 v[48:51], v[100:103], v[52:55], 0\nds_read_b128 v[52:55], %[ra] offset:224\ns_waitcnt lgkmcnt(7)\n"
        "v_mfma_f32_16x16x32_f16 v[44:47], v[100:103], v[56:59], 0\ns_waitcnt lgkmcnt(6)\nv_mfma_f32_16x16x32_f16 v[40:43], v[100:103], v[60:63], 0\ns_waitcnt lgkmcnt(5)\n"
        "v_mfma_f32_16x16x32_f16 v[36:39], v[100:103], v[64:67], 0\nv_mfma_f32_16x16x32_f16 v[48:51], v[104:107], v[56:59], v[48:51]\nds_read_b128 v[56:59], %[ra] offset:160\n"
        "v_mfma_f32_16x16x32_f16 v[44:47], v[104:107], v[60:63], v[44:47]\nv_mfma_f32_16x16x32_f16 v[40:43], v[104:107], v[64:67], v[40:43]\ns_waitcnt lgkmcnt(5)\n"
        "v_mfma_f32_16x16x32_f16 v[36:39], v[104:107], v[68:71], v[36:39]\nv_mfma_f32_16x16x32_f16 v[48:51], v[108:111], v[60:63], v[48:51]\nds_read_b128 v[60:63], %[ra] offset:96\n"
        "v_mfma_f32_16x16x32_f16 v[44:47], v[108:111], v[64:67], v[44:47]\nv_mfma_f32_16x16x32_f16 v[40:43], v[108:111], v[68:71], v[40:43]\ns_waitcnt lgkmcnt(5)\n"
        "v_mfma_f32_16x16x32_f16 v[36:39], v[108:111], v[72:75], v[36:39]\nv_mfma_f32_16x16x32_f16 v[48:51], v[112:115], v[64:67], v[48:51]\nds_read_b128 v[64:67], %[rb] offset:0\n"
        "v_mfma_f32_16x16x32_f16 v[44:47], v[112:115], v[68:71], v[44:47]\nv_mfma_f32_16x16x32_f16 v[40:43], v[112:115], v[72:75], v[40:43]\ns_waitcnt lgkmcnt(5)\n"
        "v_mfma_f32_16x16x32_f16 v[36:39], v[112:115], v[76:79], v[36:39]\nv_mfma_f32_16x16x32_f16 v[48:51], v[116:119], v[68:71], v[48:51]\n"
        "v_mfma_f32_16x16x32_f16 v[44:47], v[116:119], v[72:75], v[44:47]\nv_mfma_f32_16x16x32_f16 v[40:43], v[116:119], v[76:79], v[40:43]\ns_waitcnt lgkmcnt(4)\n"
        "v_mfma_f32_16x16x32_f16 v[36:39], v[116:119], v[80:83], v[36:39]\nv_mfma_f32_16x16x32_f16 v[48:51], v[120:123], v[72:75], v[48:51]\n"
        "v_mfma_f32_16x16x32_f16 v[44:47], v[120:123], v[76:79], v[44:47]\nv_mfma_f32_16x16x32_f16 v[40:43], v[120:123], v[80:83], v[40:43]\ns_waitcnt lgkmcnt(3)\n"
        "v_mfma_f32_16x16x32_f16 v[36:39], v[120:123], v[52:55], v[36:39]\nv_mfma_f32_16x16x32_f16 v[48:51], v[124:127], v[76:79], v[48:51]\n"
        "v_mfma_f32_16x16x32_f16 v[44:47], v[124:127], v[80:83], v[44:47]\nv_mfma_f32_16x16x32_f16 v[40:43], v[124:127], v[52:55], v[40:43]\ns_waitcnt lgkmcnt(2)\n"
        "v_mfma_f32_16x16x32_f16 v[36:39], v[124:127], v[56:59], v[36:39]\nv_mfma_f32_16x16x32_f16 v[48:51], v[128:131], v[80:83], v[48:51]\n"
        "v_mfma_f32_16x16x32_f16 v[44:47], v[128:131], v[52:55], v[44:47]\nv_mfma_f32_16x16x32_f16 v[40:43], v[128:131], v[56:59], v[40:43]\ns_waitcnt lgkmcnt(1)\n"
        "v_mfma_f32_16x16x32_f16 v[36:39], v[128:131], v[60:63], v[36:39]\ns_cmp_eq_u32 %[v], 0\ns_cbranch_scc1 .LU%=\nv_mfma_f32_16x16x32_f16 v[48:51], v[132:135], v[52:55], v[48:51]\n"
        "v_mfma_f32_16x16x32_f16 v[44:47], v[132:135], v[56:59], v[44:47]\nv_mfma_f32_16x16x32_f16 v[40:43], v[132:135], v[60:63], v[40:43]\ns_waitcnt lgkmcnt(0)\n"
        "v_mfma_f32_16x16x32_f16 v[36:39], v[132:135], v[64:67], v[36:39]\n.LU%=:\ns_nop 4\nds_write_b128 v13, v[48:51]\nds_write_b128 v15, v[44:47]\nds_write_b128 v14, v[40:43]\n"
        "ds_write_b128 %[wa], v[36:39]\ns_branch .LE%=\n.LT%=:\ns_cmp_eq_u32 %[v], 2\ns_cbranch_scc0 .LV%=\nds_read_b64 v[60:61], %[rb] offset:0\nds_read_b128 v[62:65], %[ra] offset:0\n"
        "ds_read_b64 v[66:67], %[rb] offset:288\nds_read_b128 v[68:71], %[ra] offset:288\nds_read_b64 v[72:73], %[rb] offset:576\nds_read_b128 v[74:77], %[ra] offset:576\n"
        "ds_read_b64 v[78:79], %[rb] offset:864\nds_read_b128 v[80:83], %[ra] offset:864\ns_waitcnt lgkmcnt(6)\nv_mfma_f32_16x16x32_f16 v[84:87], v[60:63], v[100:103], 0\n"
        "v_mfma_f32_16x16x32_f16 v[88:91], v[62:65], v[104:107], 0\nds_read_b64 v[60:61], %[rb] offset:1152\nds_read_b128 v[62:65], %[ra] offset:1152\ns_waitcnt lgkmcnt(6)\n"
        "v_mfma_f32_16x16x32_f16 v[92:95], v[66:69], v[100:103], 0\nv_mfma_f32_16x16x32_f16 v[96:99], v[68:71], v[104:107], 0\nds_read_b64 v[66:67], %[rb] offset:1440\n"
        "ds_read_b128 v[68:71], %[ra] offset:1440\ns_nop 7\nv_cvt_pk_f16_f32 v136, v84, v85\nv_cvt_pk_f16_f32 v137, v86, v87\nv_cvt_pk_f16_f32 v138, v88, v89\nv_cvt_pk_f16_f32 v139, v90, v91\ns_nop 1\n"
        "v_mfma_f32_16x16x32_f16 v[36:39], v[136:139], v[108:111], 0\ns_waitcnt lgkmcnt(6)\nv_mfma_f32_16x16x32_f16 v[84:87], v[72:75], v[100:103], 0\n"
        "v_mfma_f32_16x16x32_f16 v[88:91], v[74:77], v[104:107], 0\nds_read_b64 v[72:73], %[rb] offset:1728\nds_read_b128 v[74:77], %[ra] offset:1728\nv_cvt_pk_f16_f32 v140, v92, v93\n"
        "v_cvt_pk_f16_f32 v141, v94, v95\nv_cvt_pk_f16_f32 v142, v96, v97\nv_cvt_pk_f16_f32 v143, v98, v99\ns_nop 1\nv_mfma_f32_16x16x32_f16 v[36:39], v[140:143], v[112:115], v[36:39]\n"
        "s_waitcnt lgkmcnt(6)\nv_mfma_f32_16x16x32_f16 v[92:95], v[78:81], v[100:103], 0\nv_mfma_f32_16x16x32_f16 v[96:99], v[80:83], v[104:107], 0\nds_read_b64 v[78:79], %[rb] offset:2016\n"
        "ds_read_b128 v[80:83], %[ra] offset:2016\nv_cvt_pk_f16_f32 v136, v84, v85\nv_cvt_pk_f16_f32 v137, v86, v87\nv_cvt_pk_f16_f32 v138, v88, v89\nv_cvt_pk_f16_f32 v139, v90, v91\ns_nop 1\n"
        "v_mfma_f32_16x16x32_f16 v[36:39], v[136:139], v[116:119], v[36:39]\nv_mfma_f32_16x16x32_f16 v[40:43], v[136:139], v[108:111], 0\ns_waitcnt lgkmcnt(6)\n"
        "v_mfma_f32_16x16x32_f16 v[84:87], v[60:63], v[100:103], 0\nv_cvt_pk_f16_f32 v140, v92, v93\nv_cvt_pk_f16_f32 v141, v94, v95\nv_mfma_f32_16x16x32_f16 v[88:91], v[62:65], v[104:107], 0\n"
        "v_cvt_pk_f16_f32 v142, v96, v97\nv_cvt_pk_f16_f32 v143, v98, v99\ns_nop 1\nv_mfma_f32_16x16x32_f16 v[36:39], v[140:143], v[120:123], v[36:39]\nds_read_b64 v[60:61], %[rb] offset:2304\n"
        "ds_read_b128 v[62:65], %[ra] offset:2304\nv_mfma_f32_16x16x32_f16 v[40:43], v[140:143], v[112:115], v[40:43]\ns_waitcnt lgkmcnt(6)\nv_mfma_f32_16x16x32_f16 v[92:95], v[66:69], v[100:103], 0\n"
        "v_cvt_pk_f16_f32 v136, v84, v85\nv_cvt_pk_f16_f32 v137, v86, v87\nv_mfma_f32_16x16x32_f16 v[96:99], v[68:71], v[104:107], 0\nv_cvt_pk_f16_f32 v138, v88, v89\nv_cvt_pk_f16_f32 v139, v90, v91\n"
        "s_nop 1\nv_mfma_f32_16x16x32_f16 v[36:39], v[136:139], v[124:127], v[36:39]\nds_read_b64 v[66:67], %[rb] offset:2592\nds_read_b128 v[68:71], %[ra] offset:2592\n"
        "v_mfma_f32_16x16x32_f16 v[40:43], v[136:139], v[116:119], v[40:43]\nv_mfma_f32_16x16x32_f16 v[44:47], v[136:139], v[108:111], 0\nv_cvt_pk_f16_f32 v140, v92, v93\n"
        "v_cvt_pk_f16_f32 v141, v94, v95\ns_waitcnt lgkmcnt(6)\nv_mfma_f32_16x16x32_f16 v[84:87], v[72:75], v[100:103], 0\nv_cvt_pk_f16_f32 v142, v96, v97\nv_cvt_pk_f16_f32 v143, v98, v99\n"
        "v_mfma_f32_16x16x32_f16 v[88:91], v[74:77], v[104:107], 0\ns_nop 0\nv_mfma_f32_16x16x32_f16 v[36:39], v[140:143], v[128:131], v[36:39]\n"
        "v_mfma_f32_16x16x32_f16 v[40:43], v[140:143], v[120:123], v[40:43]\nv_mfma_f32_16x16x32_f16 v[44:47], v[140:143], v[112:115], v[44:47]\nv_cvt_pk_f16_f32 v136, v84, v85\n"
        "v_cvt_pk_f16_f32 v137, v86, v87\ns_waitcnt lgkmcnt(4)\nv_mfma_f32_16x16x32_f16 v[92:95], v[78:81], v[100:103], 0\nv_cvt_pk_f16_f32 v138, v88, v89\nv_cvt_pk_f16_f32 v139, v90, v91\n"
        "v_mfma_f32_16x16x32_f16 v[96:99], v[80:83], v[104:107], 0\nds_write_b128 %[wa], v[36:39] offset:0\nv_mfma_f32_16x16x32_f16 v[40:43], v[136:139], v[124:127], v[40:43]\n"
        "v_mfma_f32_16x16x32_f16 v[44:47], v[136:139], v[116:119], v[44:47]\ns_waitcnt lgkmcnt(3)\nv_mfma_f32_16x16x32_f16 v[84:87], v[60:63], v[100:103], 0\nv_cvt_pk_f16_f32 v140, v92, v93\n"
        "v_cvt_pk_f16_f32 v141, v94, v95\nv_mfma_f32_16x16x32_f16 v[88:91], v[62:65], v[104:107], 0\nv_cvt_pk_f16_f32 v142, v96, v97\nv_cvt_pk_f16_f32 v143, v98, v99\ns_nop 1\n"
        "v_mfma_f32_16x16x32_f16 v[40:43], v[140:143], v[128:131], v[40:43]\nv_mfma_f32_16x16x32_f16 v[44:47], v[140:143], v[120:123], v[44:47]\ns_waitcnt lgkmcnt(1)\n"
        "v_mfma_f32_16x16x32_f16 v[92:95], v[66:69], v[100:103], 0\nv_cvt_pk_f16_f32 v136, v84, v85\nv_cvt_pk_f16_f32 v137, v86, v87\nv_mfma_f32_16x16x32_f16 v[96:99], v[68:71], v[104:107], 0\n"
        "v_cvt_pk_f16_f32 v138, v88, v89\nv_cvt_pk_f16_f32 v139, v90, v91\ns_nop 1\nv_mfma_f32_16x16x32_f16 v[44:47], v[136:139], v[124:127], v[44:47]\nds_write_b128 %[wa], v[40:43] offset:256\n"
        "s_nop 7\nv_cvt_pk_f16_f32 v140, v92, v93\nv_cvt_pk_f16_f32 v141, v94, v95\nv_cvt_pk_f16_f32 v142, v96, v97\nv_cvt_pk_f16_f32 v143, v98, v99\ns_nop 1\n"
        "v_mfma_f32_16x16x32_f16 v[44:47], v[140:143], v[128:131], v[44:47]\ns_nop 7\nds_write_b128 %[wa], v[44:47] offset:512\ns_branch .LE%=\n.LV%=:\nds_read_b128 v[52:55], %[ra] offset:0\n"
        "ds_read_b128 v[56:59], %[ra] offset:288\nds_read_b128 v[60:63], %[ra] offset:576\nds_read_b128 v[64:67], %[ra] offset:864\nds_read_b128 v[84:87], %[rb] offset:0\n"
        "ds_read_b128 v[88:91], %[rb] offset:1152\ns_waitcnt lgkmcnt(5)\nv_mfma_f32_16x16x32_f16 v[68:71], v[52:55], v[100:103], 0\nv_mfma_f32_16x16x32_f16 v[72:75], v[52:55], v[104:107], 0\n"
        "ds_read_b128 v[52:55], %[ra] offset:1152\ns_waitcnt lgkmcnt(5)\nv_mfma_f32_16x16x32_f16 v[76:79], v[56:59], v[100:103], 0\nv_mfma_f32_16x16x32_f16 v[80:83], v[56:59], v[104:107], 0\n"
        "ds_read_b128 v[56:59], %[ra] offset:1440\ns_waitcnt lgkmcnt(3)\nv_mfma_f32_16x16x32_f16 v[36:39], v[124:127], v[84:87], 0\nv_cvt_pk_f16_f32 v136, v68, v73\nv_cvt_pk_f16_f32 v137, v70, v75\n"
        "s_waitcnt lgkmcnt(2)\nv_mfma_f32_16x16x32_f16 v[40:43], v[124:127], v[88:91], 0\nv_mfma_f32_16x16x32_f16 v[68:71], v[60:63], v[100:103], 0\nv_cvt_pk_f16_f32 v138, v76, v81\n"
        "v_cvt_pk_f16_f32 v139, v78, v83\nv_mfma_f32_16x16x32_f16 v[72:75], v[60:63], v[104:107], 0\nds_read_b128 v[60:63], %[ra] offset:1728\n"
        "v_mfma_f32_16x16x32_f16 v[36:39], v[136:139], v[112:115], v[36:39]\nv_mfma_f32_16x16x32_f16 v[40:43], v[136:139], v[120:123], v[40:43]\n"
        "v_mfma_f32_16x16x32_f16 v[76:79], v[64:67], v[100:103], 0\nv_cvt_pk_f16_f32 v140, v68, v73\nv_cvt_pk_f16_f32 v141, v70, v75\nv_mfma_f32_16x16x32_f16 v[80:83], v[64:67], v[104:107], 0\n"
        "ds_read_b128 v[64:67], %[ra] offset:2016\ns_waitcnt lgkmcnt(3)\nv_mfma_f32_16x16x32_f16 v[68:71], v[52:55], v[100:103], 0\nv_mfma_f32_16x16x32_f16 v[72:75], v[52:55], v[104:107], 0\ns_nop 7\n"
        "v_cvt_pk_f16_f32 v142, v76, v81\nv_cvt_pk_f16_f32 v143, v78, v83\ns_nop 1\nv_mfma_f32_16x16x32_f16 v[36:39], v[140:143], v[108:111], v[36:39]\n"
        "v_mfma_f32_16x16x32_f16 v[40:43], v[140:143], v[116:119], v[40:43]\ns_waitcnt lgkmcnt(2)\nv_mfma_f32_16x16x32_f16 v[76:79], v[56:59], v[100:103], 0\nv_cvt_pk_f16_f32 v136, v68, v73\n"
        "v_cvt_pk_f16_f32 v137, v70, v75\nv_mfma_f32_16x16x32_f16 v[80:83], v[56:59], v[104:107], 0\nds_write_b128 %[wa], v[36:39] offset:0\ns_waitcnt lgkmcnt(2)\n"
        "v_mfma_f32_16x16x32_f16 v[68:71], v[60:63], v[100:103], 0\nv_mfma_f32_16x16x32_f16 v[72:75], v[60:63], v[104:107], 0\ns_nop 7\nv_cvt_pk_f16_f32 v138, v76, v81\n"
        "v_cvt_pk_f16_f32 v139, v78, v83\ns_nop 1\nv_mfma_f32_16x16x32_f16 v[40:43], v[136:139], v[112:115], v[40:43]\ns_waitcnt lgkmcnt(1)\nv_mfma_f32_16x16x32_f16 v[76:79], v[64:67], v[100:103], 0\n"
        "v_mfma_f32_16x16x32_f16 v[80:83], v[64:67], v[104:107], 0\nv_cvt_pk_f16_f32 v140, v68, v73\nv_cvt_pk_f16_f32 v141, v70, v75\ns_nop 7\nv_cvt_pk_f16_f32 v142, v76, v81\n"
        "v_cvt_pk_f16_f32 v143, v78, v83\ns_nop 1\nv_mfma_f32_16x16x32_f16 v[40:43], v[140:143], v[108:111], v[40:43]\ns_nop 7\nds_write_b128 %[wa], v[40:43] offset:256\ns_branch .LE%=\n.LC%=:\n"
        "s_sub_u32 %[cap], %[cap], 1\ns_cbranch_scc0 .LCy%=\ns_mov_b32 %[cap], 0\ns_branch .LD%=\n.LCy%=:\nbuffer_load_dwordx4 v[36:39], %[c16], %[xrs], 0 offen sc1\ns_waitcnt vmcnt(0)\n"
        "s_branch .LB%=\n.LI%=:\ns_sub_u32 %[cap], %[cap], 1\ns_cbranch_scc0 .LIy%=\ns_mov_b32 %[cap], 0\ns_branch .LM%=\n.LIy%=:\ns_sleep %[fp]\n"
        "buffer_load_dwordx4 v[40:43], %[c16], %[frs], 0 offen sc1\nbuffer_load_dwordx4 v[44:47], %[c16], %[frs], %[s1] offen sc1\ns_branch .LF%=\n.LE%=:\ns_waitcnt vmcnt(0)\ns_mov_b64 exec, 1\n"
        "ds_add_u32 %[cna], %[one] offset:8\ns_mov_b64 exec, -1\ns_setprio %[pbe]\ns_cmp_lt_u32 %[wq], 2\ns_cbranch_scc0 .LZ%=\nv_lshlrev_b32 v26, 16, v152\nv_and_b32 v27, 0xffff0000, v152\n"
        "v_cvt_f32_f16 v152, v156\nv_lshrrev_b32 v156, 16, v156\nv_lshlrev_b32 v28, 16, v153\nv_and_b32 v29, 0xffff0000, v153\nv_cvt_f32_f16 v153, v157\nv_lshrrev_b32 v157, 16, v157\n"
        "v_lshlrev_b32 v30, 16, v154\nv_and_b32 v31, 0xffff0000, v154\nv_cvt_f32_f16 v154, v158\nv_lshrrev_b32 v158, 16, v158\nv_lshlrev_b32 v32, 16, v155\nv_and_b32 v33, 0xffff0000, v155\n"
        "v_cvt_f32_f16 v155, v159\nv_lshrrev_b32 v159, 16, v159\nv_cvt_f32_f16 v156, v156\nv_fmac_f32 v26, v152, %[kd1]\nv_cvt_f32_f16 v157, v157\nv_fmac_f32 v28, v153, %[kd1]\n"
        "v_cvt_f32_f16 v158, v158\nv_fmac_f32 v30, v154, %[kd1]\nv_cvt_f32_f16 v159, v159\nv_fmac_f32 v32, v155, %[kd1]\nv_fmac_f32 v27, v156, %[kd1]\nv_fmac_f32 v29, v157, %[kd1]\n"
        "v_fmac_f32 v31, v158, %[kd1]\nv_fmac_f32 v33, v159, %[kd1]\ns_mov_b32 %[s1], 0x100000\n.LP%=:\nds_read_b32 %[pv], %[cna] offset:8\ns_waitcnt lgkmcnt(0)\nv_readfirstlane_b32 %[s0], %[pv]\n"
        "s_nop 0\ns_cmp_ge_i32 %[s0], 4\ns_cbranch_scc1 .LR%=\ns_sub_u32 %[s1], %[s1], 1\ns_cbranch_scc0 .LP%=\n.LR%=:\ns_cmp_lt_u32 %[wq], 1\ns_cbranch_scc1 .LS4_%=\n"
        "ds_read_b128 v[36:39], %[exr] offset:0\nds_read_b128 v[40:43], %[exr] offset:2048\nds_read_b128 v[44:47], %[exr] offset:4096\nds_read_b128 v[48:51], %[exr] offset:6144\n"
        "ds_read_b128 v[52:55], %[mr] offset:0\nds_read_b128 v[56:59], %[mr] offset:3072\nds_read_b128 v[60:63], %[frr] offset:0\nds_read_b128 v[64:67], %[frr] offset:1152\ns_waitcnt lgkmcnt(6)\n"
        "v_fmac_f32 v26, v36, %[kf]\nv_fmac_f32 v27, v37, %[kf]\nv_fmac_f32 v28, v38, %[kf]\nv_fmac_f32 v29, v39, %[kf]\nv_fmac_f32 v30, v40, %[kf]\nv_fmac_f32 v31, v41, %[kf]\n"
        "v_fmac_f32 v32, v42, %[kf]\nv_fmac_f32 v33, v43, %[kf]\ns_waitcnt lgkmcnt(4)\nv_fmac_f32 v26, v44, %[kf]\nv_fmac_f32 v27, v45, %[kf]\nv_fmac_f32 v28, v46, %[kf]\nv_fmac_f32 v29, v47, %[kf]\n"
        "v_fmac_f32 v30, v48, %[kf]\nv_fmac_f32 v31, v49, %[kf]\nv_fmac_f32 v32, v50, %[kf]\nv_fmac_f32 v33, v51, %[kf]\ns_waitcnt lgkmcnt(2)\nv_fmac_f32 v26, v52, %[kf4]\nv_fmac_f32 v27, v53, %[kf4]\n"
        "v_fmac_f32 v28, v54, %[kf4]\nv_fmac_f32 v29, v55, %[kf4]\nv_fmac_f32 v30, v56, %[kf4]\nv_fmac_f32 v31, v57, %[kf4]\nv_fmac_f32 v32, v58, %[kf4]\nv_fmac_f32 v33, v59, %[kf4]\n"
        "s_waitcnt lgkmcnt(0)\nv_fmac_f32 v26, v60, %[kf]\nv_fmac_f32 v27, v61, %[kf]\nv_fmac_f32 v28, v62, %[kf]\nv_fmac_f32 v29, v63, %[kf]\nv_fmac_f32 v30, v64, %[kf]\nv_fmac_f32 v31, v65, %[kf]\n"
        "v_fmac_f32 v32, v66, %[kf]\nv_fmac_f32 v33, v67, %[kf]\ns_branch .LS%=\n.LS4_%=:\nds_read_b128 v[36:39], %[exr] offset:0\nds_read_b128 v[40:43], %[exr] offset:2048\n"
        "ds_read_b128 v[44:47], %[exr] offset:4096\nds_read_b128 v[48:51], %[exr] offset:6144\nds_read_b128 v[52:55], %[mr] offset:0\nds_read_b128 v[56:59], %[mr] offset:3072\ns_waitcnt lgkmcnt(4)\n"
        "v_fmac_f32 v26, v36, %[kf]\nv_fmac_f32 v27, v37, %[kf]\nv_fmac_f32 v28, v38, %[kf]\nv_fmac_f32 v29, v39, %[kf]\nv_fmac_f32 v30, v40, %[kf]\nv_fmac_f32 v31, v41, %[kf]\n"
        "v_fmac_f32 v32, v42, %[kf]\nv_fmac_f32 v33, v43, %[kf]\ns_waitcnt lgkmcnt(2)\nv_fmac_f32 v26, v44, %[kf]\nv_fmac_f32 v27, v45, %[kf]\nv_fmac_f32 v28, v46, %[kf]\nv_fmac_f32 v29, v47, %[kf]\n"
        "v_fmac_f32 v30, v48, %[kf]\nv_fmac_f32 v31, v49, %[kf]\nv_fmac_f32 v32, v50, %[kf]\nv_fmac_f32 v33, v51, %[kf]\ns_waitcnt lgkmcnt(0)\nv_fmac_f32 v26, v52, %[kf4]\nv_fmac_f32 v27, v53, %[kf4]\n"
        "v_fmac_f32 v28, v54, %[kf4]\nv_fmac_f32 v29, v55, %[kf4]\nv_fmac_f32 v30, v56, %[kf4]\nv_fmac_f32 v31, v57, %[kf4]\nv_fmac_f32 v32, v58, %[kf4]\nv_fmac_f32 v33, v59, %[kf4]\n.LS%=:\n"
        "v_cvt_pk_bf16_f32 v36, v26, v27\nv_cvt_pk_bf16_f32 v37, v28, v29\nv_cvt_pk_bf16_f32 v38, v30, v31\nv_cvt_pk_bf16_f32 v39, v32, v33\nv_min_u32 v36, -2, v36\nv_min_u32 v37, -2, v37\n"
        "v_min_u32 v38, -2, v38\nv_min_u32 v39, -2, v39\nbuffer_store_dwordx4 v[36:39], %[od], %[ors], 0 offen sc0 sc1\nbuffer_store_dwordx4 v[36:39], %[oh], %[ors], 0 offen sc0 sc1\ns_nop 1\n.LZ%=:\n"
        : "+{v[100:103]}"(kt[0]), "+{v[104:107]}"(kt[1]), "+{v[108:111]}"(kt[2]), "+{v[112:115]}"(kt[3]), "+{v[116:119]}"(kt[4]), "+{v[120:123]}"(kt[5]), "+{v[124:127]}"(kt[6]), "+{v[128:131]}"(kt[7]), "+{v[132:135]}"(kt[8]), [pv] "=&{v16}"(pv), [s0] "=&s"(s0_), [s1] "=&s"(s1_), [cap] "=&s"(cap)
        : [ra] "{v1}"(ra), [rb] "{v2}"(rb), [wa] "{v3}"(wa), [cna] "{v4}"(cna), [one] "{v5}"(one), [exr] "{v6}"(exr), [frr] "{v7}"(frr), [mr] "{v8}"(mr_), [od] "{v9}"(od), [kf] "{v10}"(kd[0]), [kd1] "{v11}"(kd[1]), [c16] "{v12}"(c16), [uw] "{v17}"(uw_), [sy] "{v23}"(sya), [ofs] "i"(SY_FS), [oh] "{v18}"(oh), [mo] "{v19}"(mo), [mrs] "s"(mrs), [gq] "s"(gq), [ue] "{v20}"(ue), [kf4] "{v21}"(kf4), [xo] "{v22}"(xo), [fsa] "i"((layer ? FSH : FSH0) - FSX), [pbl] "i"(PBH), [v] "s"(v), [wq] "s"(wq), [ors] "s"(ors), [xrs] "s"(xrs), [frs] "s"(frs), [pbe] "i"(PBE), [fsx] "i"(FSX), [fp] "i"(FPOLL)
        : "memory", "scc", "v13", "v14", "v15", "v26", "v27", "v28", "v29", "v30", "v31", "v32", "v33", "v36", "v37", "v38", "v39", "v40", "v41", "v42", "v43", "v44", "v45", "v46", "v47", "v48", "v49", "v50", "v51", "v52", "v53", "v54", "v55", "v56", "v57", "v58", "v59", "v60", "v61", "v62", "v63", "v64", "v65", "v66", "v67", "v68", "v69", "v70", "v71", "v72", "v73", "v74", "v75", "v76", "v77", "v78", "v79", "v80", "v81", "v82", "v83", "v84", "v85", "v86", "v87", "v88", "v89", "v90", "v91", "v92", "v93", "v94", "v95", "v96", "v97", "v98", "v99", "v136", "v137", "v138", "v139", "v140", "v141", "v142", "v143", "v144", "v145", "v146", "v147", "v148", "v149", "v150", "v151", "v152", "v153", "v154", "v155", "v156", "v157", "v158", "v159", "v160", "v161", "v162", "v163", "v164", "v165", "v166", "v167", "vcc");
    }
    RETRY(8 * layer + 6, 0x10000 - cap);
    if (FULL && !layer) { wst[0] = kt[7]; wst[1] = kt[8]; for (int i = 2; i < 8; ++i) wst[i] = kt[9 + i]; }
  }
}

template <int layer>
__device__ __forceinline__ void b_phase(unsigned char* smem, const int b, const int t, const u16* xtin, float2* stats, u32* fstat,
                                        u16* xtout, u32x4* mark, const u16* wot, u32x4 (&wst)[8]) {
  const int kid = layer ? 3 : 5;
  const int lane = t & 63, w = __builtin_amdgcn_readfirstlane(t >> 6);
  const int gq = w < 4 ? 2 : (w >> 2) - 1, wq = w & 3;
  const int cG = CHAN(b, gq);
  ST_DECL
  if (w != 3) __builtin_amdgcn_s_setprio(PBU); else __builtin_amdgcn_s_setprio(3);
#define TSARG
  if (gq < 2) b_conv<true, layer>(smem, b, w, gq, wq, cG, xtin, stats, fstat, xtout, mark, wot, wst TSARG);
  else b_conv<false, layer>(smem, b, w, gq, wq, cG, xtin, stats, fstat, xtout, mark, wot, wst TSARG);
  ST(5);
  {
    LANE(lane);
    const int t = 64 * w + lane;
    ST_FLUSH(kid);
  }
}

struct MainArgs { const u16 *x, *wqkv, *wo; unsigned char* ws; u16* out; };
__global__ __launch_bounds__(768, 3) void k_main(MainArgs ma, KgenArgs ka) {
  __shared__ __attribute__((aligned(16))) unsigned char smem[A_LDS + 64];
  const int b_ = BIDX;
  u32x4 sink;
  int sb = b_, t0 = TIDX;
  asm volatile(
      "s_cmp_lt_u32 %1, 16\n\t"
      "s_cbranch_scc0 1f\n\t"
      "v_cmp_gt_u32 vcc, 0x80, %2\n\t"
      "s_and_b64 exec, exec, vcc\n\t"
      "s_getpc_b64 s[40:41]\n\t"
      "s_add_u32 s40, s40, _Z6k_main8MainArgs8KgenArgs@rel32@lo+4\n\t"
      "s_addc_u32 s41, s41, _Z6k_main8MainArgs8KgenArgs@rel32@hi+12\n\t"
      "s_lshl_b32 s42, %1, 11\n\t"
      "s_and_b32 s42, s42, 0x4000\n\t"
      "v_lshl_add_u32 %3, %2, 7, s42\n\t"
      "s_mov_b32 s42, kmain_tail-_Z6k_main8MainArgs8KgenArgs\n\t"
      "global_load_dword %0, %3, s[40:41]\n\t"
      "v_add_u32 %3, 0x8000, %3\n\t"
      "v_min_u32 %4, s42, %3\n\t"
      "global_load_dword %4, %4, s[40:41]\n\t"
      "v_add_u32 %3, 0x8000, %3\n\t"
      "v_min_u32 %3, s42, %3\n\t"
      "global_load_dword %3, %3, s[40:41]\n\t"
      "s_mov_b64 exec, -1\n\t"
      "1:"
      : "=&v"(sink[0]), "+s"(sb), "+v"(t0), "=&v"(sink[1]), "=&v"(sink[2]) :: "scc", "vcc", "s40", "s41", "s42", "memory");
  unsigned char* const ws_ = ma.ws;
  const int w64 = __builtin_amdgcn_readfirstlane(t0) & ~63;
#define GETT(t) asm volatile("v_mbcnt_lo_u32_b32 %0, -1, %1\n\tv_mbcnt_hi_u32_b32 %0, -1, %0" : "=v"(t) : "s"(w64))
#define FRESH int t, b = b_; unsigned char* ws = ws_; GETT(t); asm volatile("" : "+s"(b), "+s"(ws));
#define LAYER(L) { \
    { FRESH unsigned char* ho = ws + WS_O + L * HO_BYTES; \
      a_phase<L>(smem, b, t, L ? (const u16*)(ws + WS_IM1) : ma.x, ma.wqkv, ma.wo, ws, (u16*)ho, (u16*)(ho + HO_X1), (float2*)(ho + HO_ST), ka, sink, tg0, tg1, wst); } \
    { FRESH unsigned char* ho = ws + WS_O + L * HO_BYTES; \
      b_phase<L>(smem, b, t, (u16*)(ho + HO_X1), (float2*)(ho + HO_ST), (u32*)(ho + HO_FS), (u16*)(ws + (L ? WS_FIN : WS_IM1)), (u32x4*)(ws + WS_O + HO_BYTES), (const u16*)(ws + WS_WOT), wst); } }
  u32x4 wst[8];
  u32 tg0, tg1;
  LAYER(0)
  LAYER(1)
  {
    FRESH
    const int lane = t & 63, w = __builtin_amdgcn_readfirstlane(t >> 6);
    ST_DECL
    const int i7 = (b >> 3) & 7;
    if (w == 2 || w == 3 || (w == 1 && i7 < 4)) {
      const int rb = 4 * (b & 7) + (b >> 6), cg = (i7 >> 1) + (w == 2 ? 6 : w == 3 ? 2 : 0), hf = i7 & 1;
      const __amdgpu_buffer_rsrc_t rs = __builtin_amdgcn_make_buffer_rsrc(ws + WS_FIN + (size_t)(rb * 640 + cg * 64) * 128 + hf * 64, 0, 8192 - 64, 0x00020000);
      const __amdgpu_buffer_rsrc_t os = __builtin_amdgcn_make_buffer_rsrc(ma.out + (size_t)(rb * 64 + hf * 32) * D_ + cg * 64, 0, 31 * D_ * 2 + 128, 0x00020000);
      u32 sw = LDSA(smem) + A_R1 + (w - 1) * 5120, sr = sw + (32 * ((lane >> 4) & 1) + 8 * (lane & 3) + ((lane >> 2) & 3)) * 80 + 8 * (lane >> 5);
      sw += (lane >> 2) * 80 + (lane & 3) * 16;
      int l16 = (lane >> 2) * 128 + (lane & 3) * 16, oo = (4 * (lane >> 5) + (lane & 3)) * (D_ * 2) + ((lane >> 2) & 7) * 16;
      PIN(sw); PIN(sr); PIN(l16); PIN(oo);
      __builtin_amdgcn_s_setprio(3);
      if (w != 1) lds_poll<88, 2>(LDSA(smem) + SY_OFF + 256 + (w == 2 ? 16 : 0), 4);
      if (w == 2) __builtin_amdgcn_s_sleep(TSLEEP); else if (w == 3) __builtin_amdgcn_s_sleep(24); else __builtin_amdgcn_s_sleep(16);
      int s0_, cap;
      asm volatile(
        "s_movk_i32 %[s0], 4096\ns_mov_b32 %[cap], 0x40000\nbuffer_load_dwordx4 v[16:19], %[l16], %[rs], 0 offen offset:0 sc1\nbuffer_load_dwordx4 v[20:23], %[l16], %[rs], 0 offen offset:2048 sc1\n"
        "buffer_load_dwordx4 v[24:27], %[l16], %[rs], %[s0] offen offset:0 sc1\nbuffer_load_dwordx4 v[28:31], %[l16], %[rs], %[s0] offen offset:2048 sc1\ns_sleep %[tp]\n.LA%=:\n"
        "buffer_load_dwordx4 v[16:19], %[l16], %[rs], 0 offen offset:0 sc1\nbuffer_load_dwordx4 v[20:23], %[l16], %[rs], 0 offen offset:2048 sc1\n"
        "buffer_load_dwordx4 v[24:27], %[l16], %[rs], %[s0] offen offset:0 sc1\nbuffer_load_dwordx4 v[28:31], %[l16], %[rs], %[s0] offen offset:2048 sc1\ns_waitcnt vmcnt(4)\n"
        "v_max3_u32 v32, v16, v17, v18\nv_max3_u32 v32, v32, v19, v20\nv_max3_u32 v32, v32, v21, v22\nv_max3_u32 v32, v32, v23, v24\nv_max3_u32 v32, v32, v25, v26\nv_max3_u32 v32, v32, v27, v28\n"
        "v_max3_u32 v32, v32, v29, v30\nv_max_u32 v32, v32, v31\nv_cmp_eq_u32 vcc, -1, v32\ns_cbranch_vccz .LB%=\ns_sub_u32 %[cap], %[cap], 1\ns_cbranch_scc0 .LA%=\n.LB%=:\n"
        "ds_write_b128 %[sw], v[16:19] offset:0\nds_write_b128 %[sw], v[20:23] offset:1280\nds_write_b128 %[sw], v[24:27] offset:2560\nds_write_b128 %[sw], v[28:31] offset:3840\n"
        "ds_read_b64_tr_b16 v[36:37], %[sr] offset:0\nds_read_b64_tr_b16 v[38:39], %[sr] offset:320\nds_read_b64_tr_b16 v[40:41], %[sr] offset:16\nds_read_b64_tr_b16 v[42:43], %[sr] offset:336\n"
        "ds_read_b64_tr_b16 v[44:45], %[sr] offset:32\nds_read_b64_tr_b16 v[46:47], %[sr] offset:352\nds_read_b64_tr_b16 v[48:49], %[sr] offset:48\nds_read_b64_tr_b16 v[50:51], %[sr] offset:368\n"
        "s_movk_i32 %[s0], 0\ns_waitcnt lgkmcnt(6)\nbuffer_store_dwordx4 v[36:39], %[oo], %[os], %[s0] offen sc1\ns_movk_i32 %[s0], 10240\ns_waitcnt lgkmcnt(4)\n"
        "buffer_store_dwordx4 v[40:43], %[oo], %[os], %[s0] offen sc1\ns_movk_i32 %[s0], 20480\ns_waitcnt lgkmcnt(2)\nbuffer_store_dwordx4 v[44:47], %[oo], %[os], %[s0] offen sc1\n"
        "s_movk_i32 %[s0], 30720\ns_waitcnt lgkmcnt(0)\nbuffer_store_dwordx4 v[48:51], %[oo], %[os], %[s0] offen sc1\ns_waitcnt vmcnt(4)\n"
        : [s0] "=&s"(s0_), [cap] "=&s"(cap)
        : [l16] "v"(l16), [rs] "s"(rs), [sw] "v"(sw), [sr] "v"(sr), [oo] "v"(oo), [os] "s"(os), [tp] "i"(TPS)
        : "memory", "scc", "vcc", "v16", "v17", "v18", "v19", "v20", "v21", "v22", "v23", "v24", "v25", "v26", "v27", "v28", "v29", "v30", "v31", "v32", "v33", "v34", "v35", "v36", "v37", "v38", "v39", "v40", "v41", "v42", "v43", "v44", "v45", "v46", "v47", "v48", "v49", "v50", "v51");
      RETRY(7, 0x40000 - cap);
    }
    ST(2);
    ST_FLUSH(4);
  }
}
extern "C" __device__ __attribute__((used, noinline)) void kmain_tail() {}
extern "C" void kernel_launch(void* const* d_in, const int* in_sizes, int n_in, void* d_out, int out_size,
                              void* d_ws, size_t ws_size, hipStream_t stream) {
  unsigned char* ws = (unsigned char*)d_ws;
  KgenArgs ka;
  ka.logA = (const u16*)d_in[4]; ka.Aim = (const u16*)d_in[5]; ka.Cre = (const u16*)d_in[6]; ka.Cim = (const u16*)d_in[7];
  ka.logdt = (const u16*)d_in[8]; ka.Dskip = (const u16*)d_in[9];
  MainArgs ma;
  ma.x = (const u16*)d_in[0];
  ma.wqkv = (const u16*)d_in[1]; ma.wo = (const u16*)d_in[2];
  ma.ws = ws; ma.out = (u16*)d_out;
  k_main<<<256, 768, 0, stream>>>(ma, ka);
}
#if defined(__HIP_DEVICE_COMPILE__) && !defined(ALLOW_PK)
#pragma clang attribute pop
#endif
```
